```python
import torch

def _softmax(x, axis=-1):
    return torch.softmax(x, dim=axis)
def _gelu(x):
    return torch.nn.functional.gelu(x, approximate="tanh")
def _silu(x):
    return torch.nn.functional.silu(x)
def _sigmoid(x):
    return torch.sigmoid(x)
def _layernorm(x, axis=-1, eps=1e-5):
    m = x.mean(dim=axis, keepdim=True)
    v = x.var(dim=axis, keepdim=True, unbiased=False)
    return (x - m) / torch.sqrt(v + eps)
def _scatter_add(tgt, idx, src, axis=0):
    return tgt.clone().index_add_(axis, idx, src)
def _segment_sum(src, seg, n):
    out = torch.zeros((n,) + src.shape[1:], dtype=src.dtype)
    return out.index_add_(0, seg, src)
def _pad(x, pw):
    flat = tuple(v for pair in reversed(pw) for v in pair)
    return torch.nn.functional.pad(x, flat)

kernel_name = 'parallel_sum__ssm_conv_s4d__moe_arch_mod__prompt'

def setup_inputs():

    _w = (lambda a: (lambda t: t.to(torch.bfloat16) if t.is_floating_point() else t)(torch.as_tensor(a)))
    def _A_setup():
        Bsz, L, D, N = 1, 2048, 1024, 128
        return {
            "u": _w((lambda *s: torch.randn(*s, dtype=torch.bfloat16) * 0.05)(Bsz, L, D)),
            "log_A_real": _w((lambda *s: torch.randn(*s, dtype=torch.bfloat16) * 3.2)(D, N) * 0.5),
            "A_imag": _w((lambda *s: torch.randn(*s, dtype=torch.bfloat16) * 0.2)(D, N) * 3.14),
            "C_real": _w((lambda *s: torch.randn(*s, dtype=torch.bfloat16) * 3.2)(D, N) / N ** 0.5),
            "C_imag": _w((lambda *s: torch.randn(*s, dtype=torch.bfloat16) * 3.2)(D, N) / N ** 0.5),
            "log_dt": _w((lambda *s: torch.randn(*s, dtype=torch.bfloat16) * 0.2)(D) * 0.5 - 4.0),
            "D_skip": _w((lambda *s: torch.randn(*s, dtype=torch.bfloat16) * 0.4)(D)),
        }
    _A_in = _A_setup()

    def _B_setup():
        B, S, D, F, C = 1, 2048, 1024, 3584, 8
        return {
            "x": _w((lambda *s: torch.randn(*s, dtype=torch.bfloat16) * 0.05)(B, S, D)),
            "w_r": _w((lambda *s: torch.randn(*s, dtype=torch.bfloat16) * 0.05)(D)),
            "w_gate": _w((lambda *s: torch.randn(*s, dtype=torch.bfloat16) * 0.05)(D, F)),
            "w_up": _w((lambda *s: torch.randn(*s, dtype=torch.bfloat16) * 0.05)(D, F)),
            "w_down": _w((lambda *s: torch.randn(*s, dtype=torch.bfloat16) * 0.05)(F, D)),
        }
    _B_in = _B_setup()

    return {
        "A_u": _A_in["u"],
        "A_log_A_real": _A_in["log_A_real"],
        "A_A_imag": _A_in["A_imag"],
        "A_C_real": _A_in["C_real"],
        "A_C_imag": _A_in["C_imag"],
        "A_log_dt": _A_in["log_dt"],
        "A_D_skip": _A_in["D_skip"],
        "B_x": _B_in["x"],
        "B_w_r": _B_in["w_r"],
        "B_w_gate": _B_in["w_gate"],
        "B_w_up": _B_in["w_up"],
        "B_w_down": _B_in["w_down"],
    }

def reference(A_u, A_log_A_real, A_A_imag, A_C_real, A_C_imag, A_log_dt, A_D_skip, B_x, B_w_r, B_w_gate, B_w_up, B_w_down):
    def _A_ref(u, log_A_real, A_imag, C_real, C_imag, log_dt, D_skip):
        L = u.shape[1]
        dt = torch.exp(log_dt)
        A = -torch.exp(log_A_real) + 1j * A_imag
        C = (C_real + 1j * C_imag) * dt[:, None]
        pos = (lambda x, dt: x.to(dt))(torch.arange(L), log_dt.dtype)
        P = torch.exp((A * dt[:, None])[:, :, None] * pos[None, None, :])
        K = 2.0 * torch.einsum("dn,dnl->dl", C, P).real
        L2 = 2 * L
        u_t = (lambda x, *d: x.permute(*d))(u, 0, 2, 1)
        Kf = (lambda x, *a, **k: torch.fft.rfft(x.float(), *a, **k))(K, L2)
        Uf = (lambda x, *a, **k: torch.fft.rfft(x.float(), *a, **k))(u_t, L2)
        y = (lambda x, *a, **k: torch.fft.irfft(x, *a, **k).to(torch.bfloat16))(Uf * Kf[None, :, :], L2)[:, :, :L]
        y = (lambda x, *d: x.permute(*d))(y, 0, 2, 1)
        return y + u * D_skip[None, None, :]

    def _B_ref(x, w_r, w_gate, w_up, w_down):
        B, S, D = x.shape
        C = 8
        r = x @ w_r
        idx = torch.argsort(-r, axis=-1)[:, :C]
        idx_e = (lambda x, n, axis: torch.repeat_interleave(x, n, dim=axis))(idx.reshape(B, C, 1), D, axis=2)
        x_sel = (lambda x, i, axis: torch.take_along_dim(x, i, dim=axis))(x, idx_e, axis=1)
        g = _silu(x_sel @ w_gate)
        u = x_sel @ w_up
        y = (g * u) @ w_down
        r_sel = _sigmoid((lambda x, i, axis: torch.take_along_dim(x, i, dim=axis))(r, idx, axis=1))
        y = y * r_sel.reshape(B, C, 1)
        b_off = (torch.arange(B) * S).reshape(B, 1)
        flat_idx = (idx + b_off).reshape(B * C)
        zero = (x * 0.0).reshape(B * S, D)
        upd = _scatter_add(zero, flat_idx, y.reshape(B * C, D), axis=0)
        return x + upd.reshape(B, S, D)

    x = A_u
    xn = _layernorm(x, axis=-1)
    return x + _A_ref(xn, A_log_A_real, A_A_imag, A_C_real, A_C_imag, A_log_dt, A_D_skip) + _B_ref(xn, B_w_r, B_w_gate, B_w_up, B_w_down)
```

```cpp
#include <hip/hip_runtime.h>
#include <stdint.h>
#define WLD(p) __builtin_nontemporal_load((const u32x4*)(p))

#define STR2(x) #x
#define STR(x) STR2(x)
#define WDELAY 0
#define DPOS 2
#define DDELAY 0
#define GPOS 0
#define PFO 2
#define SDELAY 20
#define EPRIO 3
#define OP1 3
#define OP2 2
#define OP3 1
#define OP4 0
#define EPRIO1 2
#define PF2 4
#define YNAP "s_sleep 2"
#define EPRIO2 3
#define CODE_KB 24
typedef unsigned short u16;
typedef unsigned int u32;
typedef __attribute__((ext_vector_type(8))) __bf16 bf16x8;
typedef __attribute__((ext_vector_type(2))) __bf16 bf16x2;
typedef __attribute__((ext_vector_type(8))) _Float16 f16x8;
typedef __attribute__((ext_vector_type(2))) _Float16 f16x2;
typedef __attribute__((ext_vector_type(4))) short v4i16;
typedef __attribute__((ext_vector_type(4))) float f32x4;
typedef __attribute__((ext_vector_type(2))) float f32x2;
typedef __attribute__((ext_vector_type(4))) u32 u32x4;
typedef __attribute__((ext_vector_type(2))) u32 u32x2;
typedef __attribute__((ext_vector_type(2), aligned(4))) u32 u32x2_a4;
typedef __amdgpu_buffer_rsrc_t rsrc_t;
#define LDS __attribute__((address_space(3)))

#define STAMP(I) do { if ((I) == 0) { SB(); pst = (u32)__builtin_amdgcn_s_memrealtime(); SB(); } } while (0)
#define FLUSH() do { if ((b == 0) && pst) { while ((u32)__builtin_amdgcn_s_memrealtime() - pst < 3000u) asm volatile("s_sleep 1"); } } while (0)

constexpr int L = 2048, D = 1024, NM = 128, F = 3584;
constexpr u32 O_ARR = 0x1000, O_RG = 0x4000, O_SG = 0x8000, O_IDX = 0x10000, O_HG = 0x20000,
              O_PG = 0x60000, WS_END = 0xD0000;
constexpr int SPIN = 1 << 16;

__device__ __forceinline__ float lo2f(u32 w) { return __uint_as_float(w << 16); }
__device__ __forceinline__ float hi2f(u32 w) { return __uint_as_float(w & 0xffff0000u); }
__device__ __forceinline__ u32 pack2(float a, float b) {
  bf16x2 r = {(__bf16)a, (__bf16)b};
  return __builtin_bit_cast(u32, r);
}
__device__ __forceinline__ u32 packh(float a, float b) {
  f16x2 r = {(_Float16)a, (_Float16)b};
  return __builtin_bit_cast(u32, r);
}
__device__ __forceinline__ float rb(float f) { return __uint_as_float(pack2(0.f, f)); }
__device__ __forceinline__ u32 hadd(u32 a, u32 b) {
  return __builtin_bit_cast(u32, __builtin_bit_cast(f16x2, a) + __builtin_bit_cast(f16x2, b));
}
__device__ __forceinline__ float hlo(u32 a) { return (float)__builtin_bit_cast(f16x2, a).x; }
__device__ __forceinline__ float hhi(u32 a) { return (float)__builtin_bit_cast(f16x2, a).y; }

template <int CTRL, int RM>
__device__ __forceinline__ float dppf(float v) {
  return __builtin_bit_cast(float, __builtin_amdgcn_update_dpp(0, __builtin_bit_cast(int, v), CTRL, RM, 0xf, true));
}
template <int CTRL, int RM>
__device__ __forceinline__ u32 dppu(u32 v) {
  return (u32)__builtin_amdgcn_update_dpp(0, (int)v, CTRL, RM, 0xf, true);
}
__device__ __forceinline__ float rdl(float v, int i) {
  return __builtin_bit_cast(float, __builtin_amdgcn_readlane(__builtin_bit_cast(int, v), i));
}
__device__ __forceinline__ u32 wmaxu(u32 v) {
  v = max(v, dppu<0x111, 0xf>(v)); v = max(v, dppu<0x112, 0xf>(v)); v = max(v, dppu<0x114, 0xf>(v));
  v = max(v, dppu<0x118, 0xf>(v)); v = max(v, dppu<0x142, 0xa>(v)); v = max(v, dppu<0x143, 0xc>(v));
  return (u32)__builtin_amdgcn_readlane((int)v, 63);
}
__device__ __forceinline__ void swap32(float& a, float& b) {
  auto r = __builtin_amdgcn_permlane32_swap(__float_as_uint(a), __float_as_uint(b), false, false);
  a = __uint_as_float(r[0]); b = __uint_as_float(r[1]);
}
__device__ __forceinline__ void swap16(float& a, float& b) {
  auto r = __builtin_amdgcn_permlane16_swap(__float_as_uint(a), __float_as_uint(b), false, false);
  a = __uint_as_float(r[0]); b = __uint_as_float(r[1]);
}
__device__ __forceinline__ u32x2 trread(const char* p) {
  v4i16 r = __builtin_amdgcn_ds_read_tr16_b64_v4i16((LDS v4i16*)p);
  return __builtin_bit_cast(u32x2, r);
}
__device__ __forceinline__ f16x8 mkh(u32x2 a, u32x2 b) {
  u32x4 v = {a.x, a.y, b.x, b.y};
  return __builtin_bit_cast(f16x8, v);
}
__device__ __forceinline__ bf16x8 asb(u32x4 v) { return __builtin_bit_cast(bf16x8, v); }
__device__ __forceinline__ f16x8 ash(u32x4 v) { return __builtin_bit_cast(f16x8, v); }
#define MFMAB(a, b, c) __builtin_amdgcn_mfma_f32_16x16x32_bf16(a, b, c, 0, 0, 0)
#define MFMAH(a, b, c) __builtin_amdgcn_mfma_f32_16x16x32_f16(a, b, c, 0, 0, 0)

__device__ __forceinline__ u32x4 ld16(rsrc_t r, u32 off) { return __builtin_amdgcn_raw_buffer_load_b128(r, off, 0, 16); }
__device__ __forceinline__ u32x2 ld8(rsrc_t r, u32 off) { return __builtin_amdgcn_raw_buffer_load_b64(r, off, 0, 16); }
__device__ __forceinline__ void st16(rsrc_t r, u32 off, u32x4 v) { __builtin_amdgcn_raw_buffer_store_b128(v, r, off, 0, 16); }
__device__ __forceinline__ void st8(rsrc_t r, u32 off, u32x2 v) { __builtin_amdgcn_raw_buffer_store_b64(v, r, off, 0, 16); }
#define NAP() asm volatile("s_sleep 1" ::: "memory")
#define SB() __builtin_amdgcn_sched_barrier(0)
#define LBAR() asm volatile("s_waitcnt lgkmcnt(0)\n\ts_barrier" ::: "memory")

__device__ __forceinline__ void cexp2(float arl, float aif, float e, float& o_r, float& o_i) {
  float E = __builtin_amdgcn_exp2f(arl * e);
  float r = __builtin_amdgcn_fractf(aif * e);
  o_r = E * __builtin_amdgcn_cosf(r);
  o_i = E * __builtin_amdgcn_sinf(r);
}
__device__ __forceinline__ float fexp(float v) { return __builtin_amdgcn_exp2f(v * 1.4426950408889634f); }
__device__ __forceinline__ float dot2b(u32 a, u32 b, float c) {
  return __builtin_amdgcn_fdot2_f32_bf16(__builtin_bit_cast(bf16x2, a), __builtin_bit_cast(bf16x2, b), c, false);
}

__device__ __forceinline__ void tr88(const u32x4 (&w)[8], u32x4 (&f)[8]) {
#pragma unroll
  for (int m = 0; m < 4; m++)
#pragma unroll
    for (int c = 0; c < 8; c++) {
      u32 hi = w[2 * m + 1][c >> 1], lo = w[2 * m][c >> 1];
      f[c][m] = __builtin_amdgcn_perm(hi, lo, (c & 1) ? 0x07060302u : 0x05040100u);
    }
}
__device__ __forceinline__ void presum_store(f32x4 (&acc)[8], float* red, int l) {
  const int n = l & 15, q = l >> 4;
  const bool lo = n < 8;
  float* p = red + (4 * (q & 1) + 2 * (n >> 3) + (q >> 1)) * 68 + (n & 7) * 8;
#pragma unroll
  for (int i = 0; i < 2; i++) {
    f32x4 o;
#pragma unroll
    for (int c = 0; c < 4; c++) {
      float a1 = acc[4 * i + c][0], b1 = acc[4 * i + c][1], a2 = acc[4 * i + c][2], b2 = acc[4 * i + c][3];
      swap32(a1, b1); swap32(a2, b2);
      const float u = lo ? a1 : b2, tt = lo ? a2 : b1;
      o[c] = u + dppf<0x128, 0xf>(tt);
    }
    *(f32x4*)(p + 4 * i) = o;
  }
}
__device__ __forceinline__ f32x2 redsum(const float* p) {
  f32x4 v[4];
#pragma unroll
  for (int i = 0; i < 4; i++) v[i] = *(const f32x4*)(p + i * 544);
  float o[4];
#pragma unroll
  for (int c = 0; c < 4; c++) o[c] = (v[0][c] + v[1][c]) + (v[2][c] + v[3][c]);
  swap32(o[0], o[2]); swap32(o[1], o[3]);
  return f32x2{o[0] + o[2], o[1] + o[3]};
}

constexpr int RS = 288;
constexpr int SCR = 7488;
constexpr int WREG = 10880, OW3 = 0, OW5 = 3632, OW7 = 6720, OW9 = 8784;
constexpr int OPE = 8 * WREG;
constexpr int OX = OPE + 8 * 4096, XREG = 4640, XO = 266;
constexpr int OMISC = OX + 8 * XREG;
constexpr int SMEM = OMISC + 512;
constexpr int OEP = OMISC + 256, OYP = OMISC + 400, OSC = OMISC + 448;
constexpr int TB = 2048, CPE = 704, CPO = 1344;
constexpr int T2B = 256, T3B = T2B + 208, TRASH = T3B + 164;
static_assert(OX == 16 * SCR, "lds map");

struct Tab { u16 v[2][64][8]; };
constexpr Tab mktab() {
  Tab T = {};
  for (int l = 0; l < 64; l++) {
    const int n16 = l & 15, kg = l >> 4;
    const int rowE = n16 < 8 ? n16 ^ 3 : (n16 ^ 3) - 7, rowO = n16 <= 12 ? 9 + n16 : n16 == 13 ? 21 : n16 == 14 ? 13 : 17;
    u16* e = T.v[0][l];
    u16* o = T.v[1][l];
    e[6] = o[7] = rowE * RS + 16 * kg; e[7] = o[6] = rowO * RS + 16 * kg;
    const int odd = kg >> 1, e0 = 278 - 128 - 8 * n16 - 4 * (kg & 1) - odd;
    e[0] = TB + (odd ? CPE + 2 * (e0 - 1) : 2 * CPE + 2 * (e0 - 2));
    e[1] = TB + (odd ? 3 * CPE + 2 * (e0 - 3) : 2 * e0);
    const int s = l & 3, x = (l & 4) ? 281 - 257 : (s & 1) ? 281 : 281 - 256;
    e[2] = TB + s * CPE + 2 * (x - s);
    e[3] = TB + ((l >> 3) & 3) * CPE + ((l & 4) ? 544 - 64 : 0) + 16 * (l & 7);
    e[5] = TB + ((15 - n16) & 3) * CPE + 2 * ((15 - n16) & 12) + 16 * kg;
    const int cs = kg == 0 ? 32 : kg == 1 ? 48 : 80, top = kg == 0 ? 31 : kg == 1 ? T2B + 47 : T3B + 63;
    const int k2 = kg > 2 ? 2 : kg;
    for (int r = 0; r < 4; r++) {
      const int off = kg == 0 ? r : kg == 1 ? (r & 1) + 64 * (r >> 1) : 32 * r;
      const int q = ((n16 + 16) << (2 - k2)) + off - 1;
      int p = top + cs * (q & (7 >> k2)) - (q >> (3 - k2));
      if (kg == 3) p = (n16 ? TRASH : r == 0 ? T3B + 80 : r == 1 ? TRASH : r == 2 ? 15 : T2B + 15) - 8;
      o[r] = TB + 2 * p;
    }
    o[4] = n16 || kg == 3 ? o[0] : TB + 2 * ((kg == 0 ? TRASH : kg == 1 ? 32 * 7 + 16 : T2B + 48 * 3 + 16) - 8);
    o[5] = TB + ((15 - n16) & 3) * CPO + 2 * ((15 - n16) & 12) + 8 * kg;
  }
  return T;
}
__device__ static const __attribute__((aligned(16))) Tab TAB = mktab();

#define CMUL(dr, di, ar, ai, br, bi, t0, t1) \
  "v_mul_f32 " t0 ", " ai ", " bi "\n\tv_mul_f32 " t1 ", " ai ", " br "\n\t" \
  "v_fma_f32 " dr ", " ar ", " br ", -" t0 "\n\tv_fma_f32 " di ", " ar ", " bi ", " t1 "\n\t"
#define HMUL(d, a, b) \
  "v_pk_mul_f16 " d ", " a ", " b " op_sel:[1,1] op_sel_hi:[1,0] neg_lo:[1,0]\n\t" \
  "v_pk_fma_f16 " d ", " a ", " b ", " d " op_sel:[0,0,0] op_sel_hi:[0,1,1]\n\t"
#define CVT(d, r, i) "v_cvt_pk_f16_f32 " d ", " r ", " i "\n\t"
#define CVJ(d, r, i) "v_cvt_pk_f16_f32 " d ", " r ", -" i "\n\t"
#define WR(v, row) "ds_write_addtid_b32 " v " offset:" STR(row) "*288\n\t"

__device__ __forceinline__ float vmul(float a, float b) {
  float r;
  asm("v_mul_f32 %0, %1, %2" : "=v"(r) : "v"(a), "v"(b));
  return r;
}
__device__ __forceinline__ float vadd(float a, float b) {
  float r;
  asm("v_add_f32 %0, %1, %2" : "=v"(r) : "v"(a), "v"(b));
  return r;
}
__device__ __forceinline__ f32x4 dot4(u32x4 a, u32 k, float c0, float c1, float c2, float c3) {
  float r0, r1, r2, r3;
  const u32 a0 = a.x, a1 = a.y, a2 = a.z, a3 = a.w;
  asm("v_dot2_f32_bf16 %0, %4, %8, %9\n\tv_dot2_f32_bf16 %1, %5, %8, %10\n\tv_dot2_f32_bf16 %2, %6, %8, %11\n\t"
      "v_dot2_f32_bf16 %3, %7, %8, %12\n\ts_nop 2"
      : "=&v"(r0), "=&v"(r1), "=&v"(r2), "=&v"(r3) : "v"(a0), "v"(a1), "v"(a2), "v"(a3), "s"(k), "v"(c0), "v"(c1), "v"(c2), "v"(c3));
  return f32x4{r0, r1, r2, r3};
}
__device__ __forceinline__ f32x4 dot4z(u32 a0, u32 a1, u32 a2, u32 a3, u32 k) {
  float r0, r1, r2, r3;
  asm("v_dot2_f32_bf16 %0, %4, %8, 0\n\tv_dot2_f32_bf16 %1, %5, %8, 0\n\tv_dot2_f32_bf16 %2, %6, %8, 0\n\t"
      "v_dot2_f32_bf16 %3, %7, %8, 0\n\ts_nop 2"
      : "=&v"(r0), "=&v"(r1), "=&v"(r2), "=&v"(r3) : "v"(a0), "v"(a1), "v"(a2), "v"(a3), "s"(k));
  return f32x4{r0, r1, r2, r3};
}

__device__ __forceinline__ float rbv(float a) {
  float r;
  asm("v_cvt_pk_bf16_f32 %0, 0, %1" : "=v"(r) : "v"(a));
  return r;
}
template <int HI>
__device__ __forceinline__ float mixs(u32 h, float f) {
  float r;
  if (HI) asm("v_fma_mix_f32 %0, %1, %2, 0 op_sel:[1,0,0] op_sel_hi:[1,0,0]" : "=v"(r) : "v"(h), "s"(f));
  else asm("v_fma_mix_f32 %0, %1, %2, 0 op_sel_hi:[1,0,0]" : "=v"(r) : "v"(h), "s"(f));
  return r;
}
template <int HI>
__device__ __forceinline__ float mixa(u32 h, float c) {
  float r;
  if (HI) asm("v_fma_mix_f32 %0, %1, 1.0, %2 op_sel:[1,0,0] op_sel_hi:[1,0,0]" : "=v"(r) : "v"(h), "v"(c));
  else asm("v_fma_mix_f32 %0, %1, 1.0, %2 op_sel_hi:[1,0,0]" : "=v"(r) : "v"(h), "v"(c));
  return r;
}
template <int HI>
__device__ __forceinline__ float mixf(u32 h, float f, float c) {
  float r;
  if (HI) asm("v_fma_mix_f32 %0, %1, %2, %3 op_sel:[1,0,0] op_sel_hi:[1,0,0]" : "=v"(r) : "v"(h), "v"(f), "v"(c));
  else asm("v_fma_mix_f32 %0, %1, %2, %3 op_sel_hi:[1,0,0]" : "=v"(r) : "v"(h), "v"(f), "v"(c));
  return r;
}
__device__ __forceinline__ u32x4 norm8(u32x4 v, float nm, float rsd) {
  const f32x4 lo = dot4(v, 0x3f80u, nm, nm, nm, nm);
#pragma unroll
  for (int k = 0; k < 4; k++) { const u32 a = v[k]; v[k] = pack2(vmul(rbv(lo[k]), rsd), vmul(rbv(vadd(hi2f(a), nm)), rsd)); }
  return v;
}

__device__ __forceinline__ void ln_row(const u32x4 (&xv)[2], const u32x4 (&wv)[2], rsrc_t rs, u32 tag, int g, int w, int l, char* st) {
  float s1 = 0.f, s2 = 0.f, s2b = 0.f, s3 = 0.f, sw = 0.f;
  float c = -lo2f((u32)__builtin_amdgcn_readfirstlane((int)xv[0][0]));
  asm volatile("" : "+v"(c));
#pragma unroll
  for (int i = 0; i < 2; i++) {
    const f32x4 lo = dot4(xv[i], 0x3f80u, c, c, c, c);
#pragma unroll
    for (int k = 0; k < 4; k++) {
      const u32 a = xv[i][k], b = wv[i][k];
      const float d = vadd(hi2f(a), c);
      s2 = __builtin_fmaf(lo[k], lo[k], s2); s2b = __builtin_fmaf(d, d, s2b);
      s1 = dot2b(a, 0x3f803f80u, s1); s3 = dot2b(a, b, s3); sw = dot2b(b, 0x3f803f80u, sw);
    }
  }
  s2 += s2b;
  swap32(s1, s2); swap32(s3, sw);
  float a = s1 + s2, e = s3 + sw;
  swap16(a, e);
  a += e;
  a += dppf<0x121, 0xf>(a); a += dppf<0x122, 0xf>(a); a += dppf<0x124, 0xf>(a); a += dppf<0x128, 0xf>(a);
  const float mu = rdl(a, 0) * (1.f / D), dv = mu + c;
  const float var = fmaxf(__builtin_fmaf(-dv, dv, rdl(a, 32) * (1.f / D)), 0.f);
  const float m = rbv(mu), sd = rb(__builtin_amdgcn_sqrtf(rbv(rbv(var) + 1e-5f)));
  const float lg = __builtin_fmaf(-m, rdl(a, 48), rdl(a, 16)) * __builtin_amdgcn_rcpf(sd);
  u32 rv = __float_as_uint(rbv(lg));
  rv = (rv ^ (((int)rv < 0) ? 0xffff0000u : 0x80000000u)) >> 16;
  u32 old = 0u;
  if (l == 0) {
    ((u32*)st)[w] = pack2(m, sd); ((u16*)(st + 64))[w] = (u16)rv;
    asm volatile("ds_add_rtn_u32 %0, %1, %2\n\ts_waitcnt lgkmcnt(0)" : "=v"(old) : "v"((u32)(size_t)(LDS char*)(st + 96)), "v"(1u) : "memory");
  }
  if (__builtin_amdgcn_readfirstlane((int)old) == 15) {
    if (l < 24) st8(rs, (l < 16 ? O_SG + 128 * g : O_RG + 64 * g - 128) + 8 * l, u32x2{((const u32*)st)[l], tag});
  }
}

constexpr int oseg(int s) { return s < 4 ? 2 : s < 16 ? 1 : (s - 16) % 7 == 0 ? 3 : (s - 16) % 7 < 3 ? 2 : 1; }
constexpr int opar(int s) { return s < 4 ? s & 1 : s < 16 ? s & 3 : (s - 16) % 7 == 0 ? 0 : (s - 16) % 7 < 3 ? (s - 16) % 7 - 1 : (s - 16) % 7 - 3; }
constexpr int otil(int s) { return s < 4 ? s >> 1 : s < 16 ? (s >> 2) - 1 : (s - 16) / 7 + (oseg(s) == 3 ? 0 : oseg(s) == 2 ? 2 : 3); }
template <int V> struct IC { static constexpr int value = V; };
template <int A, int B, class F> __device__ __forceinline__ void sfor(F&& f) {
  if constexpr (A < B) { f(IC<A>{}); sfor<A + 1, B>(f); }
}
__global__ __launch_bounds__(1024, 4) void fused(const u16* __restrict__ x, const u16* __restrict__ lar,
    const u16* __restrict__ aim, const u16* __restrict__ cre, const u16* __restrict__ cim,
    const u16* __restrict__ ldt, const u16* __restrict__ dsk, const u16* __restrict__ wr,
    const u16* __restrict__ wg, const u16* __restrict__ wu, const u16* __restrict__ wd,
    u16* __restrict__ out, char* ws) {
  __shared__ __attribute__((aligned(16))) char smem[SMEM];
  const int t = threadIdx.x, w = __builtin_amdgcn_readfirstlane(t >> 6), l = t & 63;
  const int b = blockIdx.x, xk = b & 7, j = (b >> 3) & 15;
  u32 pst = 0;
  STAMP(0);
  asm volatile("" ::"s"(x), "s"(lar), "s"(aim), "s"(cre), "s"(cim), "s"(ldt), "s"(dsk), "s"(wr), "s"(wg), "s"(wu), "s"(wd), "s"(out), "s"(ws));
  u32 tag = *(const __attribute__((address_space(4))) u32*)ws;
  const rsrc_t rs = __builtin_amdgcn_make_buffer_rsrc(ws, 0, WS_END, 0x00020000);
#define PIN_TAG() do { asm volatile("" : "+s"(tag)); tag += 1u; } while (0)
#define BAR() asm volatile("s_barrier" ::: "memory")
  const int n16 = l & 15, kg = l >> 4;
  const char* pc0 = (const char*)(__builtin_amdgcn_s_getpc() & ~255ull);
  asm volatile("" : "+s"(pc0));

  u32 code;
#define CODELD() do { int pi_ = w + 16 * (j & 1); \
    pi_ = pi_ < CODE_KB ? pi_ : CODE_KB - 1; code = *(const u32*)(pc0 + 1024 * pi_ + 16 * l); SB(); } while (0)
#define CODEUSE() asm volatile("" ::"v"(code))

  if (b >= 128) {
    const int chn = n16 & 7, kh = n16 >> 3, w8 = w & 7;
    if (j >= 14) {
      const int sb = 2 * xk + (j - 14);
      u32* gm = (u32*)smem;
      u32* ncand = gm + 32;
      u32* cand = gm + 64;
      u32* sel = gm + 640;
      CODELD();
      if (w >= 8 && w < 15) return;
      PIN_TAG();
      if (w < 8) {
        cand[t] = 0u;
        if (t < 16) cand[512 + t] = 0u;
        if (t == 0) *ncand = 0u;
        asm volatile("s_sleep " STR(SDELAY) ::: "memory");
        u32x4 g;
#pragma unroll 1
        for (int spin = 0; spin < SPIN; spin++) {
          g = ld16(rs, O_RG + 16 * t);
          if (__all(g.y == tag && g.w == tag)) break;
          NAP();
        }
        STAMP(2);
        const u32 ib = 2047 - 4 * t;
        const u32 key[4] = {(g.x << 16) | ib, (g.x & 0xffff0000u) | (ib - 1), (g.z << 16) | (ib - 2), (g.z & 0xffff0000u) | (ib - 3)};
        u32 m = max(max(key[0], key[1]), max(key[2], key[3]));
        const u32 m4 = m;
        m = max(m, dppu<0x121, 0xf>(m)); m = max(m, dppu<0x122, 0xf>(m));
        m = max(m, dppu<0x124, 0xf>(m)); m = max(m, dppu<0x128, 0xf>(m));
        if (n16 == 0) gm[t >> 4] = m;
        LBAR();
        STAMP(3);
        u32 thr;
        {
          const u32 mine = gm[l & 31];
          int cnt = 0;
#pragma unroll
          for (int i = 0; i < 4; i++) {
            u32x4 q = *(const u32x4*)(gm + 16 * (l >> 5) + 4 * i);
            cnt += (q.x > mine) + (q.y > mine) + (q.z > mine) + (q.w > mine);
          }
          auto sw = __builtin_amdgcn_permlane32_swap((u32)cnt, (u32)cnt, false, false);
          cnt = (int)(sw[0] + sw[1]);
          const unsigned long long bal = __ballot(cnt == 7);
          thr = (u32)__builtin_amdgcn_readlane((int)mine, __builtin_ctzll(bal));
        }
        if (m4 >= thr) {
          const u32 c = (key[0] >= thr) + (key[1] >= thr) + (key[2] >= thr) + (key[3] >= thr);
          u32 slot;
          asm volatile("ds_add_rtn_u32 %0, %1, %2\n\ts_waitcnt lgkmcnt(0)" : "=v"(slot) : "v"((u32)(size_t)(LDS u32*)ncand), "v"(c) : "memory");
#pragma unroll
          for (int i = 0; i < 4; i++)
            if (key[i] >= thr) cand[slot++] = key[i];
        }
        LBAR();
        STAMP(4);
        if (w == 0) {
          const int nv = (int)*ncand;
          u32 mine = cand[l];
          int cnt = 0;
#pragma unroll
          for (int i = 0; i < 4; i++) {
            const u32x4 q = *(const u32x4*)(cand + 4 * i);
            cnt += (q.x > mine) + (q.y > mine) + (q.z > mine) + (q.w > mine);
          }
          const int n = __builtin_amdgcn_readfirstlane(nv);
          int i0 = 0, i = 16;
#pragma unroll 1
          for (;;) {
#pragma unroll 1
            for (; i < n; i += 4) {
              const u32x4 q = *(const u32x4*)(cand + i);
              cnt += (q.x > mine) + (q.y > mine) + (q.z > mine) + (q.w > mine);
            }
            if (i0 + l < n && cnt < 8)
              st8(rs, O_IDX + sb * 128 + cnt * 8, u32x2{(2047u - (mine & 0x7ffu)) | ((mine ^ (((int)mine < 0) ? 0x80000000u : 0xffff0000u)) & 0xffff0000u), tag});
            i0 += 64;
            if (i0 >= n) break;
            mine = cand[i0 + l]; cnt = 0; i = 0;
          }
          STAMP(5);
          if (l == 0) st8(rs, O_ARR + b * 8, u32x2{0u, tag});
        }
      } else { BAR(); BAR(); }
      CODEUSE();
      if (b == 255 && w == 15) {
#pragma unroll 1
        for (int spin = 0; spin < SPIN; spin++) {
          const u32x4 a = ld16(rs, O_ARR + 16 * l), c = ld16(rs, O_ARR + 1024 + 16 * l);
          if (__all(a.y == tag && a.w == tag && c.y == tag && c.w == tag)) break;
          asm volatile("s_sleep 8" ::: "memory");
        }
        if (l == 0) __builtin_amdgcn_raw_buffer_store_b32(tag, rs, 0, 0, 16);
      }
      FLUSH();
      return;
    }
    float* red = (float*)(smem + (w >> 3) * 17408);
    if (w < 8) {
      const int slab = 14 * xk + j, mat = slab & 1, c64 = slab >> 1;
      const u16* gp = (mat ? wu : wg) + (size_t)(128 * w + 32 * kh + 8 * kg) * F + c64 * 64 + 8 * chn;
      u32x4 gw[2][8], gf[2][8];
#pragma unroll
      for (int i = 0; i < 16; i++) gw[i >> 3][i & 7] = WLD(gp + (size_t)(64 * (i >> 3) + (i & 7)) * F);
      SB();
      PIN_TAG();
      tr88(gw[0], gf[0]);
#pragma unroll
      for (int c = 0; c < 8; c++) asm volatile("" : "+v"(gf[0][c]));
      SB();
      tr88(gw[1], gf[1]);
#pragma unroll
      for (int c = 0; c < 8; c++) asm volatile("" : "+v"(gf[1][c]));
      SB();
      STAMP(1);
      u32* rows = (u32*)(smem + 2 * 17408);
      if (w == 0) {
        u32x2 ix;
#pragma unroll 1
        for (int spin = 0; spin < SPIN; spin++) {
          ix = ld8(rs, O_IDX + (2 * xk + (j & 1)) * 128 + (l & 7) * 8);
          if (__all(ix.y == tag)) break;
          asm volatile("s_sleep 2" ::: "memory");
        }
        rows[l] = ix.x;
      }
      LBAR();
      STAMP(3);
      const int row = (int)(rows[l] & 2047u);
      const u16* xp = x + (size_t)row * D + 128 * w + 32 * kh + 8 * kg;
      const u32x4 xr[2] = {*(const u32x4*)xp, *(const u32x4*)(xp + 64)};
      u32x2 sg;
#pragma unroll 1
      for (int spin = 0; spin < SPIN; spin++) {
        sg = ld8(rs, O_SG + row * 8);
        if (__all(sg.y == tag)) break;
        NAP();
      }
      STAMP(4);
      {
        const float nm = -lo2f(sg.x), rsd = __builtin_amdgcn_rcpf(hi2f(sg.x)) + 0.f;
        const bf16x8 a0 = asb(norm8(xr[0], nm, rsd)), a1 = asb(norm8(xr[1], nm, rsd));
        f32x4 acc[8];
#pragma unroll
        for (int c = 0; c < 8; c++) {
          f32x4 z = {};
          acc[c] = MFMAB(a0, asb(gf[0][c]), z);
        }
#pragma unroll
        for (int c = 0; c < 8; c++) acc[c] = MFMAB(a1, asb(gf[1][c]), acc[c]);
        presum_store(acc, red + w * 544, l);
      }
      LBAR();
      STAMP(5);
      if (w < 4) {
        const f32x2 v = redsum(red + (l >> 5) * 2176 + (l & 7) * 68 + 4 * (l & 8) + ((l >> 1) & 8) + 16 * (w & 1) + 4 * (w >> 1));
        float g0 = v.x, g1 = v.y;
        if (mat == 0) {
          g0 = rb(g0); g1 = rb(g1);
          g0 = g0 * __builtin_amdgcn_rcpf(1.f + __builtin_amdgcn_exp2f(-1.4426950408889634f * g0));
          g1 = g1 * __builtin_amdgcn_rcpf(1.f + __builtin_amdgcn_exp2f(-1.4426950408889634f * g1));
        }
        st8(rs, O_HG + (c64 * 4 + 2 * mat) * 1024 + 512 * w + 16 * (l & 31) + 8 * (l >> 5), u32x2{pack2(g0, g1), tag});
      }
      if (t == 448) st8(rs, O_ARR + b * 8, u32x2{0u, tag});
      STAMP(6);
      FLUSH();
      return;
    }
    const int cg = 2 * xk + j / 7, fc = j % 7;
    const u16* dp = wd + (size_t)(512 * fc + 64 * w8 + 32 * kh + 8 * kg) * D + 64 * cg + 8 * chn;
    u32x4 dw[8], df[8];
    PIN_TAG();
    BAR();
    STAMP(1);
#pragma unroll
    for (int i = 0; i < 8; i++) dw[i] = WLD(dp + (size_t)i * D);
    SB();
    BAR();
    STAMP(5);
    tr88(dw, df);
#pragma unroll
    for (int c = 0; c < 8; c++) asm volatile("" : "+v"(df[c]));
    SB();
    STAMP(9);
    u32x4 ha[4];
    {
      const u32 off = O_HG + (8 * fc + w8) * 4096 + 16 * l;
#pragma unroll 1
      for (int spin = 0; spin < SPIN; spin++) {
        ha[0] = ld16(rs, off); ha[1] = ld16(rs, off + 1024); ha[2] = ld16(rs, off + 2048); ha[3] = ld16(rs, off + 3072);
        bool ok = true;
#pragma unroll
        for (int i = 0; i < 4; i++) ok &= ha[i].y == tag && ha[i].w == tag;
        if (__all(ok)) break;
        NAP();
      }
    }
    STAMP(6);
    {
      u32x4 hv;
#pragma unroll
      for (int i = 0; i < 4; i++) {
        const u32 gg = ha[i >> 1][2 * (i & 1)], uu = ha[2 + (i >> 1)][2 * (i & 1)];
        hv[i] = pack2(lo2f(gg) * lo2f(uu), hi2f(gg) * hi2f(uu));
      }
      const bf16x8 a0 = asb(hv);
      f32x4 acc[8];
#pragma unroll
      for (int c = 0; c < 8; c++) {
        f32x4 z = {};
        acc[c] = MFMAB(a0, asb(df[c]), z);
      }
      presum_store(acc, red + w8 * 544, l);
    }
    LBAR();
    STAMP(7);
    if (w8 < 4) {
      const f32x2 v = redsum(red + (l >> 5) * 2176 + (l & 7) * 68 + 16 * w8 + ((l >> 1) & 12));
      st16(rs, O_PG + (fc * 128 + 8 * cg + 2 * w8) * 512 + 32 * (l & 31) + 16 * (l >> 5), u32x4{__float_as_uint(v.x), tag, __float_as_uint(v.y), tag});
    }
    STAMP(8);
    FLUSH();
    return;
  }

#define GMAP 1
  const int g = GMAP == 0 ? 16 * xk + j : GMAP == 1 ? 8 * xk + (j & 7) + 64 * (j >> 3) : GMAP == 2 ? 8 * j + xk : 4 * xk + (j & 3) + 32 * (j >> 2), ch = w & 7, hf = w >> 3, d = 8 * g + ch;
  u32x4 xs[4];
  const u16* xsp = x + (size_t)(2 * t) * D + 8 * g;
#define XSLD(i) do { SB(); xs[i] = *(const u32x4*)(xsp + (((i) & 1) + 512 * ((i) & 2)) * D); SB(); } while (0)
  char* scr = smem + w * SCR;
  char* X = smem + OX + ch * XREG;
  u32x4 xv[2], wv[2];
  xv[0] = *(const u32x4*)(x + (size_t)(16 * g + w) * D + l * 8); xv[1] = *(const u32x4*)(x + (size_t)(16 * g + w) * D + 512 + l * 8);
  wv[0] = *(const u32x4*)(wr + l * 8); wv[1] = *(const u32x4*)(wr + 512 + l * 8);
  SB();
  u32 pl = ((const u32*)lar)[d * 64 + l], pa = ((const u32*)aim)[d * 64 + l];
  u32 pr = ((const u32*)cre)[d * 64 + l], pi = ((const u32*)cim)[d * 64 + l], ldtv = ldt[d], dskv = dsk[d];
  SB();
  const u32x4 tv = *(const u32x4*)TAB.v[hf][l];
  SB();
  CODELD();
  if (w == 0) *(u32x2*)(smem + OMISC + 8 * l) = u32x2{0u, 0u};
  PIN_TAG();
  LBAR();
  if (hf == 0) {
    if (l < 33) *(u32x4*)(X + 16 * l) = u32x4{0u, 0u, 0u, 0u};
    if (l == 33) *(u32*)(X + 528) = 0u;
    if (l >= 34 && l < 37) *(u32*)(X + 2 * (XO + 2048) + 4 * (l - 34)) = 0u;
  } else {
    *(u32x4*)(scr + SCR - 1024 + 16 * l) = u32x4{0u, 0u, 0u, 0u};
    if (l < 8) *(u32x4*)(scr + 22 * RS + 16 * l) = u32x4{0u, 0u, 0u, 0u};
  }
  SB();
  __builtin_amdgcn_s_setprio(3);
  ln_row(xv, wv, rs, tag, g, w, l, smem + OMISC + 288);
  __builtin_amdgcn_s_setprio(0);
  STAMP(10);
  {
    const u32 base = (u32)(size_t)(LDS char*)scr;
    asm volatile("s_mov_b32 m0, %0" ::"s"(base));
  }
  asm volatile("" : "+v"(pl), "+v"(pa), "+v"(pr), "+v"(pi), "+v"(ldtv), "+v"(dskv));

  const float dt = rb(fexp(lo2f(ldtv)));
  float csc, ysc;
  {
    u32 mx = max(max(pr & 0x7fffu, (pr >> 16) & 0x7fffu), max(pi & 0x7fffu, (pi >> 16) & 0x7fffu));
    int e = (int)(wmaxu(mx) >> 7);
    e = e < 8 ? 8 : e;
    csc = __uint_as_float((u32)(260 - e) << 23);
    ysc = rdl(__uint_as_float((u32)(e - 5) << 23) * dt, 0);
  }
  *(float*)(smem + OSC + 32 * hf + 4 * ch) = hf ? 1.f + lo2f(dskv) : ysc * 65536.f;
  f32x4 km[2] = {}, kt[2] = {};
  {
    const int sh = hf ? 0 : 16;
    const float av = hi2f(pl << sh), iv = hi2f(pa << sh);
    const float arl = -rb(fexp(av)) * dt * 1.4426950408889634f, aif = iv * dt * 0.15915494309189535f;
    float cr = hi2f(pr << sh) * csc, ci = hi2f(pi << sh) * csc;
    float z1r, z1i, z8r, z8i, zBr, zBi;
    cexp2(arl, aif, 1.f, z1r, z1i); cexp2(arl, aif, 8.f, z8r, z8i); cexp2(arl, aif, 256.f, zBr, zBi);
    u32 q2, q4, q6, one = 0x3c00u;
    {
      float a, bb, c, dd, t0, t1; u32 q1, q3;
      asm volatile(
        WR("%16", 0)
        CMUL("%0", "%1", "%12", "%13", "%12", "%13", "%4", "%5")
        CVJ("%6", "%12", "%13") CVJ("%8", "%0", "%1") WR("%6", 1) WR("%8", 2)
        CMUL("%2", "%3", "%0", "%1", "%12", "%13", "%4", "%5")
        CVJ("%7", "%2", "%3") WR("%7", 3)
        CMUL("%2", "%3", "%0", "%1", "%0", "%1", "%4", "%5")
        CVJ("%9", "%2", "%3") WR("%9", 4)
        CMUL("%4", "%5", "%2", "%3", "%0", "%1", "%10", "%11")
        CVJ("%10", "%4", "%5") WR("%10", 6)
        HMUL("%0", "%9", "%6") WR("%0", 5) HMUL("%1", "%9", "%7") WR("%1", 7)
        CVJ("%2", "%14", "%15") WR("%2", 8)
        : "=&v"(a), "=&v"(bb), "=&v"(c), "=&v"(dd), "=&v"(t0), "=&v"(t1), "=&v"(q1), "=&v"(q3), "=&v"(q2), "=&v"(q4), "=&v"(q6), "=&v"(one)
        : "v"(z1r), "v"(z1i), "v"(z8r), "v"(z8i), "v"(0x3c00u) : "memory");
    }
    {
      float a, bb, c, dd, t0, t1; u32 qB, qC, qD, qE;
      asm volatile(
        CMUL("%0", "%1", "%10", "%11", "%10", "%11", "%4", "%5")
        CVJ("%6", "%10", "%11") CVJ("%7", "%0", "%1") WR("%6", 9) WR("%7", 13)
        CMUL("%2", "%3", "%0", "%1", "%10", "%11", "%4", "%5")
        CVJ("%8", "%2", "%3") WR("%8", 15)
        CMUL("%2", "%3", "%0", "%1", "%0", "%1", "%4", "%5")
        CVJ("%9", "%2", "%3") WR("%9", 17)
        HMUL("%0", "%6", "%12") WR("%0", 10) HMUL("%1", "%6", "%13") WR("%1", 11) HMUL("%2", "%6", "%14") WR("%2", 12)
        HMUL("%3", "%7", "%13") WR("%3", 14) HMUL("%4", "%8", "%13") WR("%4", 16)
        HMUL("%5", "%9", "%6") WR("%5", 18) HMUL("%0", "%9", "%7") WR("%0", 19) HMUL("%1", "%9", "%8") WR("%1", 20)
        HMUL("%2", "%9", "%9") WR("%2", 21)
        : "=&v"(a), "=&v"(bb), "=&v"(c), "=&v"(dd), "=&v"(t0), "=&v"(t1), "=&v"(qB), "=&v"(qC), "=&v"(qD), "=&v"(qE)
        : "v"(zBr), "v"(zBi), "v"(q2), "v"(q4), "v"(q6) : "memory");
    }
    u32x4 qm[4], qt[4];
    const u32 tw = tv.w, qam = tw & 0xffffu, qat = tw >> 16;
#pragma unroll
    for (int s = 0; s < 4; s++) { qm[s] = *(const u32x4*)(scr + qam + 64 * s); qt[s] = *(const u32x4*)(scr + qat + 64 * s); }
    XSLD(0);
    u32 p8, p16, p24;
    float zr, zi;
    {
      float a, bb, t0, t1;
      asm volatile(
        CMUL("%0", "%1", "%9", "%10", "%9", "%10", "%2", "%3")
        CVT("%4", "%9", "%10") CVT("%5", "%0", "%1")
        CMUL("%7", "%8", "%0", "%1", "%9", "%10", "%2", "%3")
        CVT("%6", "%7", "%8")
        CMUL("%7", "%8", "%0", "%1", "%0", "%1", "%2", "%3")
        : "=&v"(a), "=&v"(bb), "=&v"(t0), "=&v"(t1), "=&v"(p8), "=&v"(p16), "=&v"(p24), "=&v"(zr), "=&v"(zi)
        : "v"(z8r), "v"(z8i));
    }
    const char* pfr = scr + n16 * RS + 16 * kg;
#pragma unroll
    for (int h = 0; h < 2; h++) {
      float a, bb, t0, t1; u32 pb, h0, h1, h2;
#define PSTEP(cr_, ci_, nr, ni, i) \
        CVT("%6", cr_, ci_) WR("%6", 4 * i) CMUL(nr, ni, cr_, ci_, "%10", "%11", "%4", "%5") \
        HMUL("%7", "%6", "%12") WR("%7", (4 * i + 1)) HMUL("%8", "%6", "%13") WR("%8", (4 * i + 2)) HMUL("%9", "%6", "%14") WR("%9", (4 * i + 3))
      asm volatile(
        PSTEP("%0", "%1", "%2", "%3", 0) PSTEP("%2", "%3", "%0", "%1", 1) PSTEP("%0", "%1", "%2", "%3", 2) PSTEP("%2", "%3", "%0", "%1", 3)
        : "+v"(cr), "+v"(ci), "=&v"(a), "=&v"(bb), "=&v"(t0), "=&v"(t1), "=&v"(pb), "=&v"(h0), "=&v"(h1), "=&v"(h2)
        : "v"(zr), "v"(zi), "v"(p8), "v"(p16), "v"(p24) : "memory");
      u32x4 pf[4];
#pragma unroll
      for (int s = 0; s < 4; s++) pf[s] = *(const u32x4*)(pfr + 64 * s);
      if (h == 0) XSLD(1);
#pragma unroll
      for (int s = 0; s < 4; s++) {
        kt[h] = MFMAH(ash(qt[s]), ash(pf[s]), kt[h]);
        km[h] = MFMAH(ash(qm[s]), ash(pf[s]), km[h]);
      }
    }
  }
  const u32 tx = tv.x, ty = tv.y, tz = tv.z;
  u32 tp[6] = {tx & 0xffffu, tx >> 16, ty & 0xffffu, ty >> 16, tz & 0xffffu, tz >> 16};
  *(f32x4*)(scr + 16 * l) = kt[0]; *(f32x4*)(scr + 1024 + 16 * l) = kt[1];
  if (hf) {
#pragma unroll
    for (int i = 0; i < 5; i++)
      if (i < 4 || l < 12) *(u32x4*)(scr + TB + 1024 * i + 16 * l) = u32x4{0u, 0u, 0u, 0u};
  } else if (l < 32) *(u32x4*)(scr + tp[3]) = u32x4{0u, 0u, 0u, 0u};
  STAMP(1);
  LBAR();
  if (hf) __builtin_amdgcn_s_setprio(2);
  if (t == 0) st8(rs, O_ARR + b * 8, u32x2{0u, tag});
  u32x4 sg = ld16(rs, O_SG + 16 * t);
  u32x4 kv[2];
  {
    const char* ps = smem + (w ^ 8) * SCR + 16 * l;
    const f32x4 a = *(const f32x4*)ps, c = *(const f32x4*)(ps + 1024);
    kv[0].x = packh(km[0][0] + a[0], km[0][1] + a[1]); kv[0].z = packh(km[0][2] + a[2], km[0][3] + a[3]);
    kv[1].x = packh(km[1][0] + c[0], km[1][1] + c[1]); kv[1].z = packh(km[1][2] + c[2], km[1][3] + c[3]);
  }
  auto normalise = [&]() __attribute__((always_inline)) {
    SB();
    STAMP(2);
#pragma unroll 1
    for (int spin = 0; spin < SPIN; spin++) {
      if (__all(sg.y == tag && sg.w == tag)) break;
      NAP();
      sg = ld16(rs, O_SG + 16 * t);
    }
    STAMP(3);
    CODEUSE();
    const u32 sa = sg.x, sb = sg.z;
    float r0 = __builtin_amdgcn_rcpf(hi2f(sa)), r1 = __builtin_amdgcn_rcpf(hi2f(sb));
    r0 = r0 + 0.f; r1 = r1 + 0.f;
    const float m0 = -lo2f(sa) * r0, m1 = -lo2f(sb) * r1;
    {
      const u32 base = (u32)(size_t)(LDS char*)(smem + OX + 2 * XO + 256 * w);
      asm volatile("s_mov_b32 m0, %0" ::"s"(base));
    }
#pragma unroll
    for (int c = 0; c < 4; c++) {
      const u32 a = xs[0][c], bb = xs[1][c];
      u32 o0, o1;
      asm volatile("v_fma_mixlo_f16 %0, %2, %6, %7\n\tv_fma_mixlo_f16 %1, %3, %6, %7\n\t"
                   "v_fma_mixhi_f16 %0, %4, %8, %9\n\tv_fma_mixhi_f16 %1, %5, %8, %9\n\t"
                   "ds_write_addtid_b32 %0 offset:%10\n\tds_write_addtid_b32 %1 offset:%11"
                   : "=&v"(o0), "=&v"(o1) : "v"(lo2f(a)), "v"(hi2f(a)), "v"(lo2f(bb)), "v"(hi2f(bb)), "v"(r0), "v"(m0), "v"(r1), "v"(m1),
                     "n"(2 * c * XREG), "n"((2 * c + 1) * XREG) : "memory");
    }
    STAMP(4);
    LBAR();
    STAMP(5);
  };
  char* W = smem + ch * WREG;
#define PW(l_) (32 * ((l_) & 15) + 8 * (((l_) >> 4) ^ (((l_) >> 2) & 3)))
#define PSTORE(base, I, a, pad) do { u32 r0_, r1_; \
    asm volatile(pad "v_fma_mixlo_f16 %0, %2, %6, 0\n\tv_fma_mixlo_f16 %1, %4, %6, 0\n\tv_fma_mixhi_f16 %0, %3, %6, 0\n\tv_fma_mixhi_f16 %1, %5, %6, 0" \
        : "=&v"(r0_), "=&v"(r1_) : "v"((a)[0]), "v"((a)[1]), "v"((a)[2]), "v"((a)[3]), "s"(1.f / 65536)); \
    *(u32x2*)((base) + 512 * (I) + pw) = u32x2{r0_, r1_}; } while (0)
#define FLAG(o, v) do { asm volatile("" ::: "memory"); SB(); *(volatile LDS u32*)(LDS char*)(smem + (o)) = (v); SB(); } while (0)
#define YWAIT(n) do { _Pragma("unroll 1") for (int spin = 0; spin < SPIN; spin++) { \
      if (__all(*(const volatile LDS u32*)(LDS char*)(smem + OYP + 4 * min(l, 8)) >= (u32)(n))) break; \
      asm volatile(YNAP ::: "memory"); } SB(); asm volatile("" ::: "memory"); } while (0)
#define FRAG(o0, o1) mkh(*(const volatile LDS u32x2*)(tb + (o0)), *(const volatile LDS u32x2*)(tb + (o1)))
  int hf2 = hf;
  asm volatile("" : "+s"(hf2));
  if (hf == 0) {
    f16x8 FR[9];
    {
      u32x2 v[2];
#pragma unroll
      for (int h = 0; h < 2; h++) {
        v[h] = u32x2{kv[h].x, kv[h].z};
        *(u32x2_a4*)(scr + tp[0] + 256 * (1 - h)) = v[h];
        *(u32x2_a4*)(scr + tp[1] + 256 * (1 - h)) = v[h];
      }
      const u32 vy = v[0].y, vx = v[1].x;
      const u32 k0 = (u32)__builtin_amdgcn_readlane((int)vy, 0) >> 16, k256 = (u32)__builtin_amdgcn_readlane((int)vx, 63);
      if (l < 8) *(u16*)(scr + tp[2]) = (u16)((l & 5) == 1 ? k0 : k256);
      const LDS char* tb = (const LDS char*)scr + tp[5];
#pragma unroll
      for (int c = 0; c < 9; c++) FR[c] = FRAG(64 * c, 64 * c + 8);
    }
    normalise();
    u32x2 ix = {0u, 0u};
    if (w == 0) ix = ld8(rs, O_IDX + (2 * xk + (j & 1)) * 128 + (l & 7) * 8);
    const char* xb = X + 32 * n16 + 16 * kg;
    char* PE = smem + OPE + 4096 * ch;
    const int pw = PW(l);
    f32x4 acc[8] = {};
    u32x4 B[2][34];
    auto estep = [&](auto S_) __attribute__((always_inline)) {
      constexpr int s = decltype(S_)::value;
      if (s < 34) {
        constexpr int o = s < 17 ? 64 * s : 64 * (s - 17) + 2048;
        B[0][s % 34] = *(const u32x4*)(xb + o); B[1][s % 34] = *(const u32x4*)(xb + o + 1024);
      }
      SB();
      if (s >= 4 && s < 38) {
        constexpr int q = s >= 4 && s < 38 ? s - 4 : 0, hb = q < 17 ? 0 : 4, r = q < 17 ? q : q - 17, I = r >> 3, c = r & 7;
#pragma unroll
        for (int h = 0; h < 2; h++) {
          if (c == 0 && I > 0) acc[(hb + 2 * h + I - 1) & 7] = MFMAH(FR[8], ash(B[h][q]), acc[(hb + 2 * h + I - 1) & 7]);
          if (I < 2) acc[(hb + 2 * h + I) & 7] = MFMAH(FR[c], ash(B[h][q]), acc[(hb + 2 * h + I) & 7]);
          if (r == 11 + h) PSTORE(PE, hb + 2 * h, acc[hb + 2 * h], "");
          if (q == 19 + h) PSTORE(PE, 2 * h + 1, acc[2 * h + 1], "");
        }
        if (q == 20) { FLAG(OEP + 4 * ch, 4u); __builtin_amdgcn_s_setprio(EPRIO1); }
      }
      SB();
    };
    __builtin_amdgcn_s_setprio(EPRIO);
    sfor<0, 38>(estep);
    PSTORE(PE, 5, acc[5], "s_nop 7\n\t"); PSTORE(PE, 7, acc[7], "");
    SB();
    FLAG(OEP + 4 * ch, 8u);
    __builtin_amdgcn_s_setprio(EPRIO2);
    STAMP(6);
    XSLD(2); XSLD(3);
    u32x2 pv[7];
    u32 px = 0u;
#define PGLD() do { _Pragma("unroll") for (int f = 0; f < 7; f++) pv[f] = ld8(rs, O_PG + (f * 128 + g) * 512 + 8 * (l & 32) + 32 * (l & 7) + (l & 24)); } while (0)
    if (w == 0) {
#pragma unroll 1
      for (int spin = 0; spin < SPIN; spin++) {
        if (__all(ix.y == tag)) break;
        NAP();
        ix = ld8(rs, O_IDX + (2 * xk + (j & 1)) * 128 + (l & 7) * 8);
      }
      px = x[(size_t)(ix.x & 2047u) * D + 8 * g + (l >> 3)];
      if (l < 8) atomicOr((u32*)(smem + OMISC) + ((ix.x & 2047u) >> 5), 1u << (ix.x & 31u));
      FLAG(OYP + 32, 8u);
    }
    int pr_ = OPE + 4 * (t ^ ((t >> 4) & 6)), pb_ = OMISC + 4 * (t >> 4), pq_ = OX + 2 * XO + 4 * t;
    u32 op = (u32)(4 * t * D + 16 * g);
    asm volatile("" : "+v"(pr_), "+v"(pb_), "+v"(pq_), "+v"(op));
    const f32x4 sc[4] = {*(const f32x4*)(smem + OSC), *(const f32x4*)(smem + OSC + 16), *(const f32x4*)(smem + OSC + 32), *(const f32x4*)(smem + OSC + 48)};
#pragma unroll
    for (int st = 0; st < 2; st++) {
      u32 q[8], y[8];
      float xq[2][8];
#pragma unroll
      for (int c = 0; c < 8; c++) q[c] = *(const u32*)(smem + pq_ + c * XREG + 2048 * st);
#pragma unroll
      for (int c = 0; c < 8; c++) {
        const u32 a = xs[2 * st][c >> 1], bb = xs[2 * st + 1][c >> 1];
        xq[0][c] = mixf<0>(q[c], sc[2 + (c >> 2)][c & 3], (c & 1) ? hi2f(a) : lo2f(a));
        xq[1][c] = mixf<1>(q[c], sc[2 + (c >> 2)][c & 3], (c & 1) ? hi2f(bb) : lo2f(bb));
        asm volatile("" : "+v"(xq[0][c]), "+v"(xq[1][c]));
      }
      SB();
      YWAIT(st ? 5 + (w >> 1) : 4);
      if (st == 0) { STAMP(7); } else { STAMP(14); }
#pragma unroll
      for (int c = 0; c < 8; c++) y[c] = *(const u32*)(smem + pr_ + 4096 * c + 2048 * st);
      const u32 bm = *(const u32*)(smem + pb_ + 128 * st) >> (2 * n16);
      SB();
      if (st == 1 && w == 0) PGLD();
      SB();
#pragma unroll
      for (int r = 0; r < 2; r++) {
        u32x4 v;
#pragma unroll
        for (int i = 0; i < 4; i++) {
          const float s0 = sc[i >> 1][(2 * i) & 3], s1 = sc[i >> 1][(2 * i + 1) & 3];
          v[i] = r ? pack2(mixf<1>(y[2 * i], s0, xq[1][2 * i]), mixf<1>(y[2 * i + 1], s1, xq[1][2 * i + 1]))
                   : pack2(mixf<0>(y[2 * i], s0, xq[0][2 * i]), mixf<0>(y[2 * i + 1], s1, xq[0][2 * i + 1]));
        }
        if (!(bm & (1u << r))) *(u32x4*)((char*)(out + (1024 * st + r) * D) + op) = v;
        SB();
      }
      if (st == 0) { STAMP(8); } else { STAMP(15); }
    }
    if (w == 0) {
      YWAIT(8);
      const int prow = (int)(ix.x & 2047u), c = l >> 3, u = prow >> 1;
      const float yl = hlo(*(const u16*)(smem + OPE + 4096 * c + 4 * (u ^ ((u >> 4) & 6)) + 2 * (prow & 1)));
      const float ql = hlo(*(const u16*)(smem + OX + 2 * XO + c * XREG + 2 * prow));
      const float e = lo2f(px) + yl * *(const float*)(smem + OSC + 4 * c) + ql * *(const float*)(smem + OSC + 32 + 4 * c);
      const float rsel = rb(__builtin_amdgcn_rcpf(1.f + __builtin_amdgcn_exp2f(-1.4426950408889634f * hi2f(ix.x))));
#pragma unroll 1
      for (int spin = 0; spin < SPIN; spin++) {
        bool ok = true;
#pragma unroll
        for (int f = 0; f < 7; f++) ok &= pv[f].y == tag;
        if (__all(ok)) break;
        NAP();
        PGLD();
      }
      STAMP(9);
      float s = 0.f;
#pragma unroll
      for (int f = 0; f < 7; f++) { const u32 v = pv[f].x; s += __uint_as_float(v); }
      const float upd = rb(rb(s) * rsel);
      out[(size_t)prow * D + 8 * g + c] = (u16)(pack2(e + upd, 0.f) & 0xffffu);
    }
    STAMP(13);
    asm volatile("" : "=v"(tp[0]), "=v"(tp[1]), "=v"(tp[2]), "=v"(tp[3]), "=v"(tp[4]), "=v"(tp[5]), "=v"(kv[0]), "=v"(kv[1]), "=v"(sg), "=v"(code));
    asm volatile("" : "=v"(xs[0]), "=v"(xs[1]));
  }
  if (hf2) {
    f16x8 F1[4][2], F2[2][3], F3[4];
    float k1024 = 0.f;
    {
#pragma unroll
      for (int h = 0; h < 2; h++) {
        const u32 p0 = kv[h].x, p1 = kv[h].z;
        if (h == 0) k1024 = -rdl(hhi(p1), 48);
        if (h == 0 || kg < 3) {
#pragma unroll
          for (int s = 0; s < 4; s++) {
            const int o = s * (CPO - 2) + 16 * (1 - h);
            *(u16*)(scr + tp[h ? 0 : 4] + o) = (u16)p0; *(u16*)(scr + tp[1] + o) = (u16)(p0 >> 16);
            *(u16*)(scr + tp[2] + o) = (u16)p1; *(u16*)(scr + tp[3] + o) = (u16)(p1 >> 16);
          }
        }
      }
      const LDS char* tb = (const LDS char*)scr + tp[5];
#pragma unroll
      for (int dl = 0; dl < 4; dl++) F3[dl] = FRAG(2 * (T3B + 48 - 16 * dl), 2 * (T3B + 80 + 48 - 16 * dl));
#pragma unroll
      for (int p = 0; p < 2; p++)
#pragma unroll
        for (int dl = 0; dl < 3; dl++) F2[p][dl] = FRAG(2 * (T2B + 96 * p + 32 - 16 * dl), 2 * (T2B + 96 * p + 48 + 32 - 16 * dl));
#pragma unroll
      for (int p = 0; p < 4; p++)
#pragma unroll
        for (int dl = 0; dl < 2; dl++) F1[p][dl] = FRAG(2 * (64 * p + 16 - 16 * dl), 2 * (64 * p + 32 + 16 - 16 * dl));
    }
    normalise();
    if (l < 4) *(u32x4*)(W + (l == 0 ? OW3 : l == 1 ? OW5 : l == 2 ? OW7 : OW9)) = u32x4{0u, 0u, 0u, 0u};
    const char* xq = X + 512 + 16 * l;
    auto windows = [&](int p0) __attribute__((always_inline)) {
      u32x4 Q1[2], Q2[2];
      u32x2 Q0[2];
#pragma unroll
      for (int p = 0; p < 2; p++) {
        Q1[p] = *(const u32x4*)(xq + 1024 * (p0 + p)); Q2[p] = *(const u32x4*)(xq + 1024 * (p0 + p) + 16);
        if (p0 == 0) Q0[p] = *(const u32x2*)(xq + 1024 * p - 8);
      }
      SB();
#pragma unroll
      for (int p = 0; p < 2; p++) {
        const int pp = p0 + p;
        const u32 Dd[8] = {p0 == 0 ? Q0[p].y : 0u, Q1[p].x, Q1[p].y, Q1[p].z, Q1[p].w, Q2[p].x, Q2[p].y, Q2[p].z};
        u32 Uo[8], P2[8], P4[8], W3[8], W5[8];
#pragma unroll
        for (int e = 1; e <= 7; e++) { Uo[e] = __builtin_amdgcn_alignbit(Dd[e], Dd[e - 1], 16); P2[e] = hadd(Dd[e], Uo[e]); }
#pragma unroll
        for (int e = 2; e <= 7; e++) P4[e] = hadd(P2[e], P2[e - 1]);
#pragma unroll
        for (int e = 2; e <= 6; e++) W3[e] = hadd(P2[e], Uo[e + 1]);
#pragma unroll
        for (int e = 1; e <= 5; e++) W5[e] = hadd(P4[e + 1], Dd[e - 1]);
        char* wo = W + 1024 * pp + 16 * l + 16;
        if (pp < 3 || l < 34) *(u32x4*)(wo + OW3) = u32x4{W3[2], W3[3], W3[4], W3[5]};
        if (pp < 3) *(u32x4*)(wo + OW5) = u32x4{W5[2], W5[3], W5[4], W5[5]};
        if (pp < 2) {
          *(u32x4*)(wo + OW7) = u32x4{hadd(W3[3], P4[2]), hadd(W3[4], P4[3]), hadd(W3[5], P4[4]), hadd(W3[6], P4[5])};
          *(u32x4*)(wo + OW9) = u32x4{hadd(P4[4], W5[1]), hadd(P4[5], W5[2]), hadd(P4[6], W5[3]), hadd(P4[7], W5[4])};
        }
        SB();
      }
    };
    __builtin_amdgcn_s_setprio(OP1);
    windows(0);
    STAMP(12);
    f32x4 acc[8] = {};
    const char* xu = X + 2 * XO + 8 * l;
    const char* wb = W + 32 + 8 * l;
    char* PE = smem + OPE + 4096 * ch + PW(l);
    u32x2 pe[8];
#define YSTORE(I, pad) do { u32 p0 = pe[I].x, p1 = pe[I].y; \
      asm volatile(pad "v_fma_mixlo_f16 %0, %2, %6, %0 op_sel_hi:[0,0,1]\n\tv_fma_mixlo_f16 %1, %4, %6, %1 op_sel_hi:[0,0,1]\n\t" \
                   "v_fma_mixhi_f16 %0, %3, %6, %0 op_sel:[0,0,1] op_sel_hi:[0,0,1]\n\tv_fma_mixhi_f16 %1, %5, %6, %1 op_sel:[0,0,1] op_sel_hi:[0,0,1]" \
          : "+v"(p0), "+v"(p1) : "v"(acc[I][0]), "v"(acc[I][1]), "v"(acc[I][2]), "v"(acc[I][3]), "s"(1.f / 65536)); \
      *(u32x2*)(PE + 512 * (I)) = u32x2{p0, p1}; } while (0)
    f16x8 A[44];
#define HI(s_) ((s_) < 0 ? 0 : (s_) < 16 ? ((s_) + PFO + 1 < 16 ? (s_) + PFO + 1 : 16) : ((s_) + PF2 + 1 < 44 ? (s_) + PF2 + 1 : 44))
#define FIX(I) do { const char* p_ = X + 2 * (256 * ((I) - 4) + 16 * n16 + 4 * kg + 260); const u32x2 a = *(const u32x2*)p_; \
      const u32 c = *(const u32*)(p_ + 8), ax = a.x, ay = a.y; \
      acc[I][0] = k1024 * hhi(ax); acc[I][1] = k1024 * hlo(ay); acc[I][2] = k1024 * hhi(ay); acc[I][3] = k1024 * hlo(c); } while (0)
#define EWAIT(n, I0, I1) do { _Pragma("unroll 1") for (int spin = 0; spin < SPIN; spin++) { \
      const u32 f = *(const volatile LDS u32*)(LDS char*)(smem + OEP + 4 * ch); \
      _Pragma("unroll") for (int I = I0; I < I1; I++) pe[I] = *(const volatile LDS u32x2*)(LDS char*)(PE + 512 * I); \
      if (__builtin_amdgcn_readfirstlane((int)f) >= (n)) break; \
      NAP(); } } while (0)
    auto oload = [&](auto LD_) __attribute__((always_inline)) {
      constexpr int ld = decltype(LD_)::value;
      constexpr int sg_ = oseg(ld), p = opar(ld), T = otil(ld);
      if (sg_ == 3) A[ld] = mkh(trread(wb + OW7 + 512 * T - 16), trread(wb + OW9 + 512 * T - 32));
      else if (sg_ == 2) A[ld] = mkh(trread(wb + OW3 + 512 * T - 2 * (4 + 8 * p)), trread(wb + OW5 + 512 * T - 2 * (8 + 8 * p)));
      else A[ld] = mkh(trread(xu + 512 * T - 2 * (2 + 4 * p)), trread(wb + OW3 + 512 * T - 2 * (4 + 4 * p)));
    };
    auto ostep = [&](auto S_) __attribute__((always_inline)) {
      constexpr int s = decltype(S_)::value;
      sfor<HI(s - 1), HI(s)>(oload);
      SB();
      constexpr int sg_ = oseg(s), p = opar(s), T = otil(s);
      if (sg_ == 3) {
#pragma unroll
        for (int dl = 0; dl < 4; dl++)
          if (T + 4 + dl < 8) acc[(T + 4 + dl) & 7] = MFMAH(A[s], F3[dl], acc[(T + 4 + dl) & 7]);
      } else if (sg_ == 2) {
#pragma unroll
        for (int dl = 0; dl < 3; dl++)
          if (T + 2 + dl < 8) acc[(T + 2 + dl) & 7] = MFMAH(A[s], F2[p & 1][dl], acc[(T + 2 + dl) & 7]);
      } else {
        acc[T + 1] = MFMAH(A[s], F1[p][0], acc[T + 1]);
        if (T < 6) acc[(T + 2) & 7] = MFMAH(A[s], F1[p][1], acc[(T + 2) & 7]);
      }
      constexpr int U = (s - 17) / 7 + 3;
      if (s == 28 || s == 35 || s == 41) pe[(U + 1) & 7] = *(const volatile LDS u32x2*)(LDS char*)(PE + 512 * (U + 1));
      if (s == 24) EWAIT(8, 4, 5);
      if (s == 24 || s == 31 || s == 38) { SB(); YSTORE(U & 7, ""); FLAG(OYP + 4 * ch, U + 1); }
      if (s == 24) __builtin_amdgcn_s_setprio(OP3);
      if (s == 31) __builtin_amdgcn_s_setprio(OP4);
      SB();
    };
    FIX(4); FIX(5);
    SB();
    sfor<0, 16>(ostep);
    STAMP(9);
    EWAIT(4, 1, 4);
    STAMP(15);
    YSTORE(1, ""); YSTORE(2, ""); YSTORE(3, "");
    FLAG(OYP + 4 * ch, 4u);
    __builtin_amdgcn_s_setprio(OP2);
    windows(2);
    FIX(6); FIX(7);
    SB();
    STAMP(7);
    sfor<16, 44>(ostep);
    YSTORE(7, "s_nop 7\n\t");
    FLAG(OYP + 4 * ch, 8u);
    STAMP(13);
  }
  FLUSH();
}

extern "C" void kernel_launch(void* const* d_in, const int* in_sizes, int n_in, void* d_out, int out_size,
                              void* d_ws, size_t ws_size, hipStream_t stream) {
  const u16* const* in = (const u16* const*)d_in;
  fused<<<256, 1024, 0, stream>>>(in[0], in[1], in[2], in[3], in[4], in[5], in[6], in[8], in[9], in[10], in[11],
                                  (u16*)d_out, (char*)d_ws);
}
```

```cpp
#include <hip/hip_runtime.h>
#include <stdint.h>

#define STR2(x) #x
#define STR(x) STR2(x)
#define LA 4
#define WL2 44
#define WL3 100
#define TOG 0
#define ABL 0
#define PPRIO 0
#define Z8D 1
#define CPRIO 1
#define GDELAY 0
#define ROT 1
#define PRE 0
#define NE 8
#define YNAP "s_sleep 2"
#define CODE_KB 24
typedef unsigned short u16;
typedef unsigned int u32;
typedef __attribute__((ext_vector_type(8))) __bf16 bf16x8;
typedef __attribute__((ext_vector_type(2))) __bf16 bf16x2;
typedef __attribute__((ext_vector_type(8))) _Float16 f16x8;
typedef __attribute__((ext_vector_type(2))) _Float16 f16x2;
typedef __attribute__((ext_vector_type(4))) short v4i16;
typedef __attribute__((ext_vector_type(4))) float f32x4;
typedef __attribute__((ext_vector_type(2))) float f32x2;
typedef __attribute__((ext_vector_type(4))) u32 u32x4;
typedef __attribute__((ext_vector_type(2))) u32 u32x2;
typedef __attribute__((ext_vector_type(3))) u32 u32x3;
typedef __attribute__((ext_vector_type(2), aligned(4))) u32 u32x2_a4;
typedef __amdgpu_buffer_rsrc_t rsrc_t;
#define LDS __attribute__((address_space(3)))

#define STAMP(I)
#define STAMPC(I)
#define FLUSH()

constexpr int L = 2048, D = 1024, NM = 128, F = 3584;
constexpr u32 O_ARR = 0x1000, O_RG = 0x4000, O_SG = 0x8000, O_IDX = 0x10000, O_HG = 0x20000,
              O_PG = 0x90000, WS_END = 0x170000;
constexpr int SPIN = 1 << 16;

__device__ __forceinline__ float lo2f(u32 w) { return __uint_as_float(w << 16); }
__device__ __forceinline__ float hi2f(u32 w) { return __uint_as_float(w & 0xffff0000u); }
__device__ __forceinline__ u32 pack2(float a, float b) {
  bf16x2 r = {(__bf16)a, (__bf16)b};
  return __builtin_bit_cast(u32, r);
}
__device__ __forceinline__ u32 packh(float a, float b) {
  f16x2 r = {(_Float16)a, (_Float16)b};
  return __builtin_bit_cast(u32, r);
}
__device__ __forceinline__ float rb(float f) { return __uint_as_float(pack2(0.f, f)); }
__device__ __forceinline__ u32 hadd(u32 a, u32 b) {
  return __builtin_bit_cast(u32, __builtin_bit_cast(f16x2, a) + __builtin_bit_cast(f16x2, b));
}
__device__ __forceinline__ float hlo(u32 a) { return (float)__builtin_bit_cast(f16x2, a).x; }
__device__ __forceinline__ float hhi(u32 a) { return (float)__builtin_bit_cast(f16x2, a).y; }

template <int CTRL, int RM>
__device__ __forceinline__ float dppf(float v) {
  return __builtin_bit_cast(float, __builtin_amdgcn_update_dpp(0, __builtin_bit_cast(int, v), CTRL, RM, 0xf, true));
}
template <int CTRL, int RM>
__device__ __forceinline__ u32 dppu(u32 v) {
  return (u32)__builtin_amdgcn_update_dpp(0, (int)v, CTRL, RM, 0xf, true);
}
__device__ __forceinline__ float rdl(float v, int i) {
  return __builtin_bit_cast(float, __builtin_amdgcn_readlane(__builtin_bit_cast(int, v), i));
}
__device__ __forceinline__ u32 wmaxu(u32 v) {
  v = max(v, dppu<0x111, 0xf>(v)); v = max(v, dppu<0x112, 0xf>(v)); v = max(v, dppu<0x114, 0xf>(v));
  v = max(v, dppu<0x118, 0xf>(v)); v = max(v, dppu<0x142, 0xa>(v)); v = max(v, dppu<0x143, 0xc>(v));
  return (u32)__builtin_amdgcn_readlane((int)v, 63);
}
__device__ __forceinline__ void swap32(float& a, float& b) {
  auto r = __builtin_amdgcn_permlane32_swap(__float_as_uint(a), __float_as_uint(b), false, false);
  a = __uint_as_float(r[0]); b = __uint_as_float(r[1]);
}
__device__ __forceinline__ void swap16(float& a, float& b) {
  auto r = __builtin_amdgcn_permlane16_swap(__float_as_uint(a), __float_as_uint(b), false, false);
  a = __uint_as_float(r[0]); b = __uint_as_float(r[1]);
}
__device__ __forceinline__ u32x2 trread(const char* p) {
  v4i16 r = __builtin_amdgcn_ds_read_tr16_b64_v4i16((LDS v4i16*)p);
  return __builtin_bit_cast(u32x2, r);
}
__device__ __forceinline__ f16x8 mkh(u32x2 a, u32x2 b) {
  u32x4 v = {a.x, a.y, b.x, b.y};
  return __builtin_bit_cast(f16x8, v);
}
__device__ __forceinline__ bf16x8 mkb(u32x2 a, u32x2 b) {
  u32x4 v = {a.x, a.y, b.x, b.y};
  return __builtin_bit_cast(bf16x8, v);
}
__device__ __forceinline__ bf16x8 asb(u32x4 v) { return __builtin_bit_cast(bf16x8, v); }
__device__ __forceinline__ f16x8 ash(u32x4 v) { return __builtin_bit_cast(f16x8, v); }
#define MFMAB(a, b, c) __builtin_amdgcn_mfma_f32_16x16x32_bf16(a, b, c, 0, 0, 0)
#define MFMAH(a, b, c) __builtin_amdgcn_mfma_f32_16x16x32_f16(a, b, c, 0, 0, 0)

__device__ __forceinline__ u32x4 ld16(rsrc_t r, u32 off) { return __builtin_amdgcn_raw_buffer_load_b128(r, off, 0, 16); }
__device__ __forceinline__ u32x2 ld8(rsrc_t r, u32 off) { return __builtin_amdgcn_raw_buffer_load_b64(r, off, 0, 16); }
__device__ __forceinline__ void st16(rsrc_t r, u32 off, u32x4 v) { __builtin_amdgcn_raw_buffer_store_b128(v, r, off, 0, 16); }
__device__ __forceinline__ void st8(rsrc_t r, u32 off, u32x2 v) { __builtin_amdgcn_raw_buffer_store_b64(v, r, off, 0, 16); }
#define NAP() asm volatile("s_sleep 1" ::: "memory")
#define SB() __builtin_amdgcn_sched_barrier(0)
#define LBAR() asm volatile("s_waitcnt lgkmcnt(0)\n\ts_barrier" ::: "memory")

__device__ __forceinline__ void cexp2(float arl, float aif, float e, float& o_r, float& o_i) {
  float E = __builtin_amdgcn_exp2f(arl * e);
  float r = __builtin_amdgcn_fractf(aif * e);
  o_r = E * __builtin_amdgcn_cosf(r);
  o_i = E * __builtin_amdgcn_sinf(r);
}
__device__ __forceinline__ float fexp(float v) { return __builtin_amdgcn_exp2f(v * 1.4426950408889634f); }
__device__ __forceinline__ float dot2b(u32 a, u32 b, float c) {
  return __builtin_amdgcn_fdot2_f32_bf16(__builtin_bit_cast(bf16x2, a), __builtin_bit_cast(bf16x2, b), c, false);
}
__device__ __forceinline__ float sigm(float v) { return __builtin_amdgcn_rcpf(1.f + __builtin_amdgcn_exp2f(-1.4426950408889634f * v)); }

constexpr int RS = 288, QB = 22 * RS, PB = 16 * RS;
constexpr int OW3 = 0, OW5 = 3632, OW7 = 6720, OW9 = 8784, OY = 11616, CHR = OY + 4096;
constexpr int OX = 8 * CHR, XREG = 4640, XO = 266;
constexpr int OMISC = OX + 8 * XREG;
constexpr int OYP = OMISC + 256, OXF = OMISC + 320, OCN = OMISC + 336, OSC = OMISC + 352, OLN = OMISC + 384;
constexpr int SMEM = OMISC + 512;
constexpr int CPE = 704, TB = 2 * PB, CPO = 576;
constexpr int T2B = 2 * CPO, T3B = (OY + 2048 - TB) / 2, TRASH = 280;
constexpr int FA = 131072, AP = 1056, FH = FA + 8 * AP, HP = 544, FC = FH + 8 * HP, FL = FC + 128;
static_assert(FL + 640 <= SMEM && SMEM <= 163840, "lds map");

struct Tab { u16 v[2][64][8]; };
constexpr Tab mktab() {
  Tab T = {};
  for (int l = 0; l < 64; l++) {
    const int n16 = l & 15, kg = l >> 4;
    const int rowE = n16 < 8 ? n16 ^ 3 : (n16 ^ 3) - 7, rowO = n16 <= 12 ? 9 + n16 : n16 == 13 ? 21 : n16 == 14 ? 13 : 17;
    u16* e = T.v[0][l];
    u16* o = T.v[1][l];
    e[6] = rowE * RS + 16 * kg; e[7] = rowO * RS + 16 * kg;
    const int odd = kg >> 1, e0 = 278 - 128 - 8 * n16 - 4 * (kg & 1) - odd;
    e[0] = odd ? CPE + 2 * (e0 - 1) : 2 * CPE + 2 * (e0 - 2);
    e[1] = odd ? 3 * CPE + 2 * (e0 - 3) : 2 * e0;
    const int s = l & 3, x = (l & 4) ? 281 - 257 : (s & 1) ? 281 : 281 - 256;
    e[2] = s * CPE + 2 * (x - s);
    e[3] = ((l >> 3) & 3) * CPE + ((l & 4) ? 544 - 64 : 0) + 16 * (l & 7);
    e[5] = ((15 - n16) & 3) * CPE + 2 * ((15 - n16) & 12) + 16 * kg;
    const int cs = kg == 0 ? 32 : kg == 1 ? 48 : 80, top = kg == 0 ? 31 : kg == 1 ? T2B + 47 : T3B + 63;
    const int k2 = kg > 2 ? 2 : kg;
    for (int r = 0; r < 4; r++) {
      const int off = kg == 0 ? r : kg == 1 ? (r & 1) + 64 * (r >> 1) : 32 * r;
      const int q = ((n16 + 16) << (2 - k2)) + off - 1;
      int p = top + cs * (q & (7 >> k2)) - (q >> (3 - k2));
      if (kg == 3) p = (n16 ? TRASH : r == 0 ? T3B + 80 : r == 1 ? TRASH : r == 2 ? 15 : T2B + 15) - 8;
      o[r] = TB + 2 * p;
    }
    o[4] = n16 || kg == 3 ? o[0] : TB + 2 * ((kg == 0 ? TRASH : kg == 1 ? 32 * 7 + 16 : T2B + 48 * 3 + 16) - 8);
    o[5] = TB + ((15 - n16) & 3) * CPO + 2 * ((15 - n16) & 12) + 8 * kg;
  }
  return T;
}
__device__ static const __attribute__((aligned(16))) Tab TAB = mktab();

#define PKM(t) " op_sel:[1,1] op_sel_hi:[1,0] neg_lo:[1,0]\n\t"
#define PKF(t) " op_sel:[0,0,0] op_sel_hi:[0,1,1]\n\t"
__device__ __forceinline__ void cm2(f32x2 (&d)[2], const f32x2 (&a)[2], const f32x2 (&b)[2]) {
  asm volatile("v_pk_mul_f32 %0, %2, %3" PKM() "v_pk_mul_f32 %1, %4, %5" PKM() "v_pk_fma_f32 %0, %2, %3, %0" PKF() "v_pk_fma_f32 %1, %4, %5, %1" PKF()
               : "=&v"(d[0]), "=&v"(d[1]) : "v"(a[0]), "v"(b[0]), "v"(a[1]), "v"(b[1]));
}
template <int ROW, int ST>
__device__ __forceinline__ void hw2(const u32 (&a)[2], const u32 (&b)[2]) {
  u32 d0, d1;
  asm volatile("v_pk_mul_f16 %0, %2, %3" PKM() "v_pk_mul_f16 %1, %4, %5" PKM() "v_pk_fma_f16 %0, %2, %3, %0" PKF() "v_pk_fma_f16 %1, %4, %5, %1" PKF()
               "ds_write_addtid_b32 %0 offset:%6\n\tds_write_addtid_b32 %1 offset:%7"
               : "=&v"(d0), "=&v"(d1) : "v"(a[0]), "v"(b[0]), "v"(a[1]), "v"(b[1]), "n"(ROW * RS), "n"(ROW * RS + ST) : "memory");
}
template <int ROW>
__device__ __forceinline__ void cj2(u32 (&d)[2], const f32x2 (&a)[2]) {
  asm volatile("v_cvt_pk_f16_f32 %0, %2, -%3\n\tv_cvt_pk_f16_f32 %1, %4, -%5\n\tds_write_addtid_b32 %0 offset:%6\n\tds_write_addtid_b32 %1 offset:%7"
               : "=&v"(d[0]), "=&v"(d[1]) : "v"(a[0].x), "v"(a[0].y), "v"(a[1].x), "v"(a[1].y), "n"(ROW * RS), "n"(ROW * RS + QB) : "memory");
}
template <int ROW>
__device__ __forceinline__ void cw2(u32 (&d)[2], const f32x2 (&a)[2]) {
  if (ROW < 0)
    asm volatile("v_cvt_pk_f16_f32 %0, %2, %3\n\tv_cvt_pk_f16_f32 %1, %4, %5"
                 : "=&v"(d[0]), "=&v"(d[1]) : "v"(a[0].x), "v"(a[0].y), "v"(a[1].x), "v"(a[1].y));
  else
    asm volatile("v_cvt_pk_f16_f32 %0, %2, %3\n\tv_cvt_pk_f16_f32 %1, %4, %5\n\tds_write_addtid_b32 %0 offset:%6\n\tds_write_addtid_b32 %1 offset:%7"
                 : "=&v"(d[0]), "=&v"(d[1]) : "v"(a[0].x), "v"(a[0].y), "v"(a[1].x), "v"(a[1].y), "n"(ROW * RS), "n"(ROW * RS + PB) : "memory");
}
template <int HI>
__device__ __forceinline__ float mixs(u32 h, float f) {
  float r;
  if (HI) asm("v_fma_mix_f32 %0, %1, %2, 0 op_sel:[1,0,0] op_sel_hi:[1,0,0]" : "=v"(r) : "v"(h), "s"(f));
  else asm("v_fma_mix_f32 %0, %1, %2, 0 op_sel_hi:[1,0,0]" : "=v"(r) : "v"(h), "s"(f));
  return r;
}
__device__ __forceinline__ f32x2 pkfma(f32x2 a, f32x2 b, f32x2 c) {
  f32x2 r;
  asm("v_pk_fma_f32 %0, %1, %2, %3" : "=v"(r) : "v"(a), "v"(b), "v"(c));
  return r;
}
template <int HI>
__device__ __forceinline__ f32x2 pkfmas(f32x2 a, f32x2 s, f32x2 c) {
  f32x2 r;
  if (HI) asm("v_pk_fma_f32 %0, %2, %1, %3 op_sel:[1,0,0] op_sel_hi:[1,1,1]" : "=v"(r) : "v"(a), "v"(s), "v"(c));
  else asm("v_pk_fma_f32 %0, %2, %1, %3 op_sel:[0,0,0] op_sel_hi:[0,1,1]" : "=v"(r) : "v"(a), "v"(s), "v"(c));
  return r;
}
__device__ __forceinline__ float red4(float (&q)[4]) {
  swap32(q[0], q[2]); swap32(q[1], q[3]);
  float a = q[0] + q[2], e = q[1] + q[3];
  swap16(a, e);
  a += e;
  a += dppf<0x121, 0xf>(a); a += dppf<0x122, 0xf>(a); a += dppf<0x124, 0xf>(a); a += dppf<0x128, 0xf>(a);
  return a;
}

__device__ __forceinline__ void ln_rows(const u32x4 (&xv)[4], const u32x4 (&wv)[2], rsrc_t rs, u32 tag, int b, int w, int l, char* st) {
  float sw = 0.f;
#pragma unroll
  for (int i = 0; i < 8; i++) sw = dot2b(wv[i >> 2][i & 3], 0x3f803f80u, sw);
  SB();
#pragma unroll
  for (int r = 0; r < 2; r++) {
    float s[4] = {0.f, 0.f, 0.f, sw};
#pragma unroll
    for (int i = 0; i < 8; i++) {
      const u32 a = xv[2 * r + (i >> 2)][i & 3];
      s[0] = dot2b(a, 0x3f803f80u, s[0]); s[1] = dot2b(a, a, s[1]); s[2] = dot2b(a, wv[i >> 2][i & 3], s[2]);
    }
    const float ml = s[0] * 0.0625f, c = rdl(ml, 0), dl = ml - c;
    s[1] = __builtin_fmaf(16.f * dl, dl, __builtin_fmaf(-s[0], ml, s[1]));
    const float a = red4(s);
    if ((l & 15) == 0 || l == 1) *(float*)(st + 64 * w + 32 * r + (l == 1 ? 16 : l >> 2)) = l == 1 ? c : a;
    SB();
  }
  u32 old = 0u;
  if (l == 0) asm volatile("ds_add_rtn_u32 %0, %1, %2\n\ts_waitcnt lgkmcnt(0)" : "=v"(old) : "v"((u32)(size_t)(LDS char*)(st + 512)), "v"(1u) : "memory");
  if (__builtin_amdgcn_readfirstlane((int)old) == 7) {
    const f32x4 q = *(const f32x4*)(st + 32 * (l & 15));
    const float c = *(const float*)(st + 32 * (l & 15) + 16);
    const float mu = q[0] * (1.f / D), dv = mu - c;
    const float var = fmaxf(__builtin_fmaf(-dv, dv, q[1] * (1.f / D)), 0.f);
    const float m = rb(mu), sd = rb(__builtin_amdgcn_sqrtf(rb(rb(var) + 1e-5f)));
    const float lg = __builtin_fmaf(-m, q[3], q[2]) * __builtin_amdgcn_rcpf(sd);
    u32 rv = __float_as_uint(rb(lg));
    rv = (rv ^ (((int)rv < 0) ? 0xffff0000u : 0x80000000u)) & 0xffff0000u;
    const u32 pr = (rv >> 16) | dppu<0xb1, 0xf>(rv);
    u32 off = l < 16 ? O_SG + 128 * (b - 128) + 8 * l : l < 32 ? O_RG + 64 * (b - 128) + 4 * (l - 16) : O_ARR + 8 * b;
    if (l > 32 || (l & 17) == 17) off = 0x80000000u;
    st8(rs, off, u32x2{l < 16 ? pack2(m, sd) : pr, tag});
    SB();
    *(volatile LDS u32*)(LDS char*)(st + 516) = 1u;
    SB();
  }
}

constexpr int NOPS = 109, NSLOT = 164;
struct Ops {
  signed char k[NOPS], a[NOPS], b[NOPS];
  short first[NOPS + 1];
  unsigned char op[NSLOT];
  short ld[NOPS][2];
  short end[8];
};
constexpr int nmf(int k, int T, int p) { return k == 0 ? (p == 0 && T > 0) + (T < 8) : k == 1 ? 1 + (T < 6) : k == 2 ? (T < 4 ? 3 : 6 - T) : 4 - T; }
constexpr Ops mkops() {
  Ops O = {};
  int n = 0, last[8] = {};
  O.k[n] = 0; O.a[n] = 0; O.b[n] = 0; n++;
  for (int I = 0; I < 8; I++) {
    int fk[7] = {}, fa[7] = {}, fb[7] = {}, m = 0;
    if (I >= 4) { fk[m] = 3; fa[m] = I - 4; fb[m] = 0; m++; }
    if (I >= 2) for (int p = 0; p < 2; p++) { fk[m] = 2; fa[m] = I - 2; fb[m] = p; m++; }
    if (I >= 1) for (int p = 0; p < 4; p++) { fk[m] = 1; fa[m] = I - 1; fb[m] = p; m++; }
    int f = 0;
    for (int c = 1; c <= 8; c++) {
      O.k[n] = 0; O.a[n] = c == 8 ? I + 1 : I; O.b[n] = c & 7; n++;
      if (I >= 2 && f < m) { O.k[n] = fk[f]; O.a[n] = fa[f]; O.b[n] = fb[f]; n++; f++; }
    }
    for (; f < m; f++) { O.k[n] = fk[f]; O.a[n] = fa[f]; O.b[n] = fb[f]; n++; }
    last[I] = n - 1;
  }
  int q = 0;
  for (int i = 0; i < NOPS; i++) {
    O.first[i] = q;
    for (int u = nmf(O.k[i], O.a[i], O.b[i]); u > 0; u--) O.op[q++] = i;
  }
  O.first[NOPS] = q;
  for (int I = 0; I < 8; I++) O.end[I] = O.first[last[I]];
  for (int i = 0; i < NOPS; i++) {
    O.ld[i][1] = O.first[i] - LA;
    O.ld[i][0] = O.ld[i][1] - (O.k[i] && O.first[i] - O.first[i - 1] > 1);
  }
  return O;
}
constexpr Ops OPS = mkops();
static_assert(OPS.first[NOPS] == NSLOT, "schedule");
constexpr int WL[4] = {-1, 17, WL2, WL3}, WN[4] = {38, 38, 22, 13};
constexpr int wslot(int pp, int k) { return pp < 2 ? (pp ? 22 : 2) + k / 2 : WL[pp] + 4 + k; }
constexpr int opat(int k, int T, int p) { for (int i = 0; i < NOPS; i++) if (OPS.k[i] == k && OPS.a[i] == T && OPS.b[i] == p) return i; return -1; }
static_assert(wslot(0, 12) < OPS.ld[opat(1, 0, 0)][0] && wslot(0, 21) < OPS.ld[opat(2, 0, 0)][0] && wslot(1, 12) < OPS.ld[opat(1, 1, 0)][0] &&
              wslot(1, 21) < OPS.ld[opat(2, 1, 0)][0] && wslot(1, 37) < OPS.ld[opat(3, 0, 0)][0] && wslot(2, 12) < OPS.ld[opat(1, 3, 0)][0] &&
              wslot(2, 21) < OPS.ld[opat(2, 3, 0)][0] && wslot(3, 12) < OPS.ld[opat(1, 5, 0)][0] && wslot(0, 29) < WL[1] && wslot(1, 37) < WL[2] &&
              wslot(2, 21) < WL[3], "window order");
constexpr int XC[3] = {OPS.ld[opat(0, 2, 0)][1], WL2 < OPS.ld[opat(0, 4, 0)][1] ? WL2 : OPS.ld[opat(0, 4, 0)][1], WL3};
static_assert(XC[0] <= WL[1] && WL3 <= OPS.ld[opat(0, 6, 0)][1], "chunk order");
template <int V> struct IC { static constexpr int value = V; };
template <int A, int B, class F> __device__ __forceinline__ void sfor(F&& f) {
  if constexpr (A < B) { f(IC<A>{}); sfor<A + 1, B>(f); }
}
__device__ __forceinline__ void dma(const void* base, u32 v, u32 dst) {
  asm volatile("s_mov_b32 m0, %0\n\ts_nop 0\n\tglobal_load_lds_dwordx4 %1, %2 nt" ::"s"(dst), "v"(v), "s"(base) : "memory");
}

__global__ __launch_bounds__(768, 3) void fused(const u16* __restrict__ x, const u16* __restrict__ lar,
    const u16* __restrict__ aim, const u16* __restrict__ cre, const u16* __restrict__ cim,
    const u16* __restrict__ ldt, const u16* __restrict__ dsk, const u16* __restrict__ wr,
    const u16* __restrict__ wg, const u16* __restrict__ wu, const u16* __restrict__ wd,
    u16* __restrict__ out, char* ws) {
  __shared__ __attribute__((aligned(256))) char smem[SMEM];
  const int t = threadIdx.x, w = __builtin_amdgcn_readfirstlane(t >> 6), l = t & 63;
  const int b = blockIdx.x, xk = b & 7, j = (b >> 3) & 15;
  STAMP(0);
  asm volatile("" ::"s"(x), "s"(lar), "s"(aim), "s"(cre), "s"(cim), "s"(ldt), "s"(dsk), "s"(wr), "s"(wg), "s"(wu), "s"(wd), "s"(out), "s"(ws));
  u32 tag = *(const __attribute__((address_space(4))) u32*)ws;
  const rsrc_t rs = __builtin_amdgcn_make_buffer_rsrc(ws, 0, WS_END, 0x00020000);
#define PIN_TAG() do { asm volatile("" : "+s"(tag)); tag += 1u; } while (0)
#define BAR() asm volatile("s_barrier" ::: "memory")
  const int n16 = l & 15, kg = l >> 4;
  const char* pc0 = (const char*)(__builtin_amdgcn_s_getpc() & ~255ull);
  asm volatile("" : "+s"(pc0));
  const u32 lds0 = (u32)(size_t)(LDS char*)smem;
#define FLAG(o, v) do { asm volatile("" ::: "memory"); SB(); *(volatile LDS u32*)(LDS char*)(smem + (o)) = (v); SB(); } while (0)
#define ARRIVE(o) do { SB(); if (l == 0) asm volatile("ds_add_u32 %0, %1" ::"v"(lds0 + (o)), "v"(1u) : "memory"); SB(); } while (0)
#define CWAIT(o, n, nap) do { _Pragma("unroll 1") for (int spin = 0; spin < SPIN; spin++) { \
      if (__builtin_amdgcn_readfirstlane((int)*(const volatile LDS u32*)(LDS char*)(smem + (o))) >= (int)(n)) break; \
      asm volatile(nap ::: "memory"); } SB(); asm volatile("" ::: "memory"); } while (0)
#define AWAIT(o, n, nap) do { _Pragma("unroll 1") for (int spin = 0; spin < SPIN; spin++) { \
      if (__all(*(const volatile LDS u32*)(LDS char*)(smem + (o)) >= (u32)(n))) break; \
      asm volatile(nap ::: "memory"); } SB(); asm volatile("" ::: "memory"); } while (0)
  u32 code;
#define CODELD() do { int pi_ = (w & 7) + 8 * (j & 3); \
    pi_ = pi_ < CODE_KB ? pi_ : CODE_KB - 1; code = *(const __attribute__((address_space(1))) u32*)(pc0 + 1024 * pi_ + 16 * l); SB(); } while (0)
#define CODEUSE() asm volatile("" ::"v"(code))
  const u32 ixo = O_IDX + (2 * xk + (j & 1)) * 128 + (l & 7) * 8;

  if (b >= 128) {
    if (w < 8) {
      u32x4 xv[4], wv[2];
      const int wl = ROT == 0 ? w : ROT == 1 ? (w + b) & 7 : (w + j) & 7;
      wv[0] = *(const u32x4*)(wr + l * 8); wv[1] = *(const u32x4*)(wr + 512 + l * 8);
#pragma unroll
      for (int r = 0; r < 4; r++) xv[r] = *(const u32x4*)(x + (size_t)(16 * (b - 128) + 2 * wl) * D + 512 * r + l * 8);
      SB();
      STAMP(11);
      if (t < 32) *(u32*)(smem + FC + 4 * t) = 0u;
      if (t == 32) *(u32x2*)(smem + FL + 512) = u32x2{0u, 0u};
      PIN_TAG();
      LBAR();
      STAMP(12);
      ln_rows(xv, wv, rs, tag, b, wl, l, smem + FL);
      STAMP(10);
    } else {
      PIN_TAG();
      BAR();
    }
    if (j >= 14) {
      const int sb = 2 * xk + (j - 14);
      u32* gm = (u32*)smem;
      u32* ncand = gm + 32;
      u32* cand = gm + 64;
      CODELD();
      if (w >= 8 && w < 11) return;
      if (w < 8) {
        cand[t] = 0u;
        if (t < 16) cand[512 + t] = 0u;
        if (t == 0) *ncand = 0u;
        u32x4 g;
#pragma unroll 1
        for (int spin = 0; spin < SPIN; spin++) {
          g = ld16(rs, O_RG + 16 * t);
          if (__all(g.y == tag && g.w == tag)) break;
          NAP();
        }
        STAMP(2);
        const u32 ib = 2047 - 4 * t;
        const u32 key[4] = {(g.x << 16) | ib, (g.x & 0xffff0000u) | (ib - 1), (g.z << 16) | (ib - 2), (g.z & 0xffff0000u) | (ib - 3)};
        u32 m = max(max(key[0], key[1]), max(key[2], key[3]));
        const u32 m4 = m;
        m = max(m, dppu<0x121, 0xf>(m)); m = max(m, dppu<0x122, 0xf>(m));
        m = max(m, dppu<0x124, 0xf>(m)); m = max(m, dppu<0x128, 0xf>(m));
        if (n16 == 0) gm[t >> 4] = m;
        LBAR();
        STAMP(3);
        u32 thr;
        {
          const u32 mine = gm[l & 31];
          int cnt = 0;
#pragma unroll
          for (int i = 0; i < 4; i++) {
            u32x4 q = *(const u32x4*)(gm + 16 * (l >> 5) + 4 * i);
            cnt += (q.x > mine) + (q.y > mine) + (q.z > mine) + (q.w > mine);
          }
          auto sw = __builtin_amdgcn_permlane32_swap((u32)cnt, (u32)cnt, false, false);
          cnt = (int)(sw[0] + sw[1]);
          const unsigned long long bal = __ballot(cnt == 7);
          thr = (u32)__builtin_amdgcn_readlane((int)mine, __builtin_ctzll(bal));
        }
        if (m4 >= thr) {
          const u32 c = (key[0] >= thr) + (key[1] >= thr) + (key[2] >= thr) + (key[3] >= thr);
          u32 slot;
          asm volatile("ds_add_rtn_u32 %0, %1, %2\n\ts_waitcnt lgkmcnt(0)" : "=v"(slot) : "v"((u32)(size_t)(LDS u32*)ncand), "v"(c) : "memory");
#pragma unroll
          for (int i = 0; i < 4; i++)
            if (key[i] >= thr) cand[slot++] = key[i];
        }
        LBAR();
        STAMP(4);
        if (w == 0) {
          const int nv = (int)*ncand;
          u32 mine = cand[l];
          int cnt = 0;
#pragma unroll
          for (int i = 0; i < 4; i++) {
            const u32x4 q = *(const u32x4*)(cand + 4 * i);
            cnt += (q.x > mine) + (q.y > mine) + (q.z > mine) + (q.w > mine);
          }
          const int n = __builtin_amdgcn_readfirstlane(nv);
          int i0 = 0, i = 16;
#pragma unroll 1
          for (;;) {
#pragma unroll 1
            for (; i < n; i += 4) {
              const u32x4 q = *(const u32x4*)(cand + i);
              cnt += (q.x > mine) + (q.y > mine) + (q.z > mine) + (q.w > mine);
            }
            if (i0 + l < n && cnt < 8)
              st8(rs, O_IDX + sb * 128 + cnt * 8, u32x2{(2047u - (mine & 0x7ffu)) | ((mine ^ (((int)mine < 0) ? 0x80000000u : 0xffff0000u)) & 0xffff0000u), tag});
            i0 += 64;
            if (i0 >= n) break;
            mine = cand[i0 + l]; cnt = 0; i = 0;
          }
          STAMP(5);
        }
      } else { BAR(); BAR(); }
      CODEUSE();
      if (b == 255 && w == 11) {
#pragma unroll 1
        for (int spin = 0; spin < SPIN; spin++) {
          const u32x4 a = ld16(rs, O_ARR + 16 * l), c = ld16(rs, O_ARR + 1024 + 16 * l);
          if (__all(a.y == tag && a.w == tag && c.y == tag && c.w == tag)) break;
          asm volatile("s_sleep 8" ::: "memory");
        }
        if (l == 0) __builtin_amdgcn_raw_buffer_store_b32(tag, rs, 0, 0, 16);
      }
      FLUSH();
      return;
    }
    const int id = 14 * xk + j;
    if (w >= 8) {
      const int dw = w - 8;
      const u32 sw = (((n16 >> 1) ^ (kg | ((dw >> 1) << 2))) << 5) + ((l & 1) << 4);
      {
        const u16* base = ((id & 2) ? wu : wg) + (size_t)(512 * (id & 1) + 4 * dw) * F + (id >> 2) * 128;
        const u32 v = kg * (2 * F) + sw;
#pragma unroll
        for (int i = 0; i < 32; i++) {
          if (i == NE) { CWAIT(FL + 516, 1, "s_sleep 1"); STAMP(5); }
          dma(base + (size_t)(16 * i) * F, v, lds0 + 4096 * i + 1024 * dw);
        }
      }
      STAMP(1);
      asm volatile("s_waitcnt vmcnt(0)" ::: "memory");
      ARRIVE(FC);
      STAMP(2);
      CWAIT(FC + 4, 8, "s_sleep 1");
      STAMP(3);
      {
        const u16* base = wd + (size_t)(256 * (id >> 3) + 4 * dw) * D + (id & 7) * 128;
        const u32 v = kg * (2 * D) + sw;
#pragma unroll
        for (int i = 0; i < 16; i++) dma(base + (size_t)(16 * i) * D, v, lds0 + 4096 * i + 1024 * dw);
      }
      asm volatile("s_waitcnt vmcnt(0)" ::: "memory");
      ARRIVE(FC + 8);
      STAMP(4);
      FLUSH();
      return;
    }
    u32x2 sg = {0u, 0u};
    u32 so = 0u;
    if (w == 0) {
      u32x2 ix;
#pragma unroll 1
      for (int spin = 0; spin < SPIN; spin++) {
        ix = ld8(rs, ixo);
        if (__all(ix.y == tag)) break;
        asm volatile("s_sleep 2" ::: "memory");
      }
      STAMP(1);
#pragma unroll
      for (int i = 0; i < 8; i++) {
        const u32 row = (u32)__builtin_amdgcn_readlane((int)ix.x, i) & 2047u;
        asm volatile("s_mov_b32 m0, %0\n\ts_nop 0\n\tglobal_load_lds_dwordx4 %1, %2" ::"s"(lds0 + FA + AP * i), "v"(16 * l),
                     "s"(x + (size_t)row * D + 512 * (id & 1)) : "memory");
      }
      so = O_SG + (ix.x & 2047u) * 8;
      sg = ld8(rs, so);
      SB();
    }
    const char* wp = smem + (8 * kg + (n16 >> 2)) * 256 + ((w ^ ((n16 >> 2) | ((kg & 1) << 2))) << 5) + 8 * (l & 3);
    const char* wq = wp + 65536;
    {
      CWAIT(FC, 4, "s_sleep 1");
      STAMP(2);
      bf16x8 wf[16];
#pragma unroll
      for (int k = 0; k < 8; k++) {
        wf[k] = mkb(trread(wp + 8192 * k), trread(wp + 8192 * k + 1024));
        wf[8 + k] = mkb(trread(wq + 8192 * k), trread(wq + 8192 * k + 1024));
      }
      ARRIVE(FC + 4);
      f32x4 cs = {};
      {
        const bf16x8 one = asb(u32x4{0x3f803f80u, 0x3f803f80u, 0x3f803f80u, 0x3f803f80u});
#pragma unroll
        for (int k = 0; k < 16; k++) cs = MFMAB(wf[(k >> 1) + 8 * (k & 1)], one, cs);
      }
      SB();
      STAMP(3);
      if (w == 0) {
#pragma unroll 1
        for (int spin = 0; spin < SPIN; spin++) {
          if (__all(sg.y == tag)) break;
          NAP();
          sg = ld8(rs, so);
        }
        asm volatile("s_waitcnt vmcnt(0)" ::: "memory");
        *(u32*)(smem + FC + 32 + 4 * (l & 7)) = sg.x;
        FLAG(FC + 12, 1u);
      } else CWAIT(FC + 12, 1, "s_sleep 1");
      STAMP(4);
      u32x4 xa[8];
      const char* ap = smem + FA + (l & 7) * AP + 512 * (n16 >> 3) + 16 * kg;
#pragma unroll
      for (int i = 0; i < 8; i++) xa[i] = *(const u32x4*)(ap + 64 * i);
      const u32 st = *(const u32*)(smem + FC + 32 + 4 * (l & 7));
      SB();
      f32x4 a1 = {}, a2 = {};
#pragma unroll
      for (int i = 0; i < 8; i++) { a1 = MFMAB(wf[i], asb(xa[i]), a1); a2 = MFMAB(wf[8 + i], asb(xa[i]), a2); }
      const float rsd = __builtin_amdgcn_rcpf(hi2f(st)), nm = -lo2f(st);
      float o[4];
#pragma unroll
      for (int i = 0; i < 4; i++) o[i] = __builtin_fmaf(nm, cs[i], a1[i] + dppf<0x128, 0xf>(a2[i])) * rsd;
      if (n16 < 8) st16(rs, O_HG + id * 4096 + 512 * w + 128 * kg + 16 * n16, u32x4{pack2(o[0], o[1]), tag, pack2(o[2], o[3]), tag});
      SB();
      STAMP(5);
    }
    {
      const u32 off = O_HG + (2 * (id >> 3) + (w >> 2)) * 16384 + (w & 3) * 1024 + 16 * l;
      CWAIT(FC + 8, 4, "s_sleep 1");
      STAMP(6);
      u32x4 ha[4];
#pragma unroll
      for (int i = 0; i < 4; i++) ha[i] = ld16(rs, off + 4096 * i);
      SB();
      bf16x8 df[8];
#pragma unroll
      for (int k = 0; k < 8; k++) df[k] = mkb(trread(wp + 8192 * k), trread(wp + 8192 * k + 1024));
      SB();
#pragma unroll 1
      for (int spin = 0; spin < SPIN; spin++) {
        bool ok = true;
#pragma unroll
        for (int i = 0; i < 4; i++) ok &= ha[i].y == tag && ha[i].w == tag;
        if (__all(ok)) break;
        NAP();
#pragma unroll
        for (int i = 0; i < 4; i++) ha[i] = ld16(rs, off + 4096 * i);
      }
      STAMP(7);
      u32x2 hv;
#pragma unroll
      for (int i = 0; i < 2; i++) {
        const u32 g0 = ha[0][2 * i], g1 = ha[1][2 * i], u0 = ha[2][2 * i], u1 = ha[3][2 * i];
        const u32 gg = pack2(lo2f(g0) + lo2f(g1), hi2f(g0) + hi2f(g1)), uu = pack2(lo2f(u0) + lo2f(u1), hi2f(u0) + hi2f(u1));
        const u32 sl = pack2(lo2f(gg) * sigm(lo2f(gg)), hi2f(gg) * sigm(hi2f(gg)));
        hv[i] = pack2(lo2f(sl) * lo2f(uu), hi2f(sl) * hi2f(uu));
      }
      *(u32x2*)(smem + FH + (l & 7) * HP + 64 * w + (l & 56)) = hv;
      LBAR();
      STAMP(8);
      const char* hp = smem + FH + (l & 7) * HP + 256 * (n16 >> 3) + 16 * kg;
      f32x4 a1 = {}, a2 = {};
#pragma unroll
      for (int i = 0; i < 4; i++) {
        const bf16x8 h = asb(*(const u32x4*)(hp + 64 * i));
        a1 = MFMAB(df[i], h, a1); a2 = MFMAB(df[4 + i], h, a2);
      }
      u32 o[4];
#pragma unroll
      for (int i = 0; i < 4; i++) o[i] = __float_as_uint(a1[i] + dppf<0x128, 0xf>(a2[i]));
      const u32 po = O_PG + ((id >> 3) * 128 + 16 * (id & 7) + 2 * w + (kg >> 1)) * 512 + 64 * n16 + 32 * (kg & 1);
      if (n16 < 8) { st16(rs, po, u32x4{o[0], tag, o[1], tag}); st16(rs, po + 16, u32x4{o[2], tag, o[3], tag}); }
      STAMP(9);
    }
    FLUSH();
    return;
  }

  const int g = 8 * xk + (j & 7) + 64 * (j >> 3);
  if (w >= 8) {
    const int p = w - 8;
    u32x4 xs[4][2];
    f32x2 sc[4];
    const u32x4 dv = *(const __attribute__((address_space(4))) u32x4*)(dsk + 8 * g);
    PIN_TAG();
    BAR();
    {
      const u16* xsp = x + (size_t)(128 * p + 2 * l) * D + 8 * g;
#pragma unroll
      for (int i = 0; i < 8; i++) xs[i >> 1][i & 1] = *(const u32x4*)(xsp + (512 * (i >> 1) + (i & 1)) * D);
      SB();
    }
    if (t == 512) st8(rs, O_ARR + b * 8, u32x2{0u, tag});
#pragma unroll
    for (int i = 0; i < 4; i++) { sc[i] = f32x2{1.f + lo2f(dv[i]), 1.f + hi2f(dv[i])}; asm volatile("" : "+v"(sc[i])); }
    STAMP(9);
    __builtin_amdgcn_s_setprio(3);
    f32x2 xq[4][8];
    {
      u32x4 sg[4];
#pragma unroll 1
      for (int spin = 0; spin < SPIN; spin++) {
        bool ok = true;
#pragma unroll
        for (int i = 0; i < 4; i++) sg[i] = ld16(rs, O_SG + 4096 * i + 1024 * p + 16 * l);
#pragma unroll
        for (int i = 0; i < 4; i++) ok &= sg[i].y == tag && sg[i].w == tag;
        if (__all(ok)) break;
        NAP();
      }
      STAMP(3);
      asm volatile("s_mov_b32 m0, %0\n\ts_nop 0" ::"s"(lds0 + OX + 2 * XO + 256 * p));
#pragma unroll
      for (int i = 0; i < 4; i++) {
        const u32 sa = sg[i].x, sb = sg[i].z;
        const f32x2 r2 = {__builtin_amdgcn_rcpf(hi2f(sa)), __builtin_amdgcn_rcpf(hi2f(sb))};
        const f32x2 m2 = {-lo2f(sa) * r2.x, -lo2f(sb) * r2.y};
        f32x2 x2[8], n2[8];
        u32 o[8], fv = i + 1;
#pragma unroll
        for (int c = 0; c < 8; c++) {
          const u32 a = xs[i][0][c >> 1], bb = xs[i][1][c >> 1];
          x2[c] = (c & 1) ? f32x2{hi2f(a), hi2f(bb)} : f32x2{lo2f(a), lo2f(bb)};
        }
        asm volatile("" : "+v"(fv));
        SB();
#pragma unroll
        for (int c = 0; c < 8; c++) n2[c] = pkfma(x2[c], r2, m2);
        SB();
#pragma unroll
        for (int c = 0; c < 8; c++) o[c] = packh(n2[c].x, n2[c].y);
        SB();
#pragma unroll
        for (int c = 0; c < 8; c++) asm volatile("ds_write_addtid_b32 %0 offset:%1" ::"v"(o[c]), "n"(c * XREG + 1024 * i) : "memory");
        FLAG(OXF + 4 * p, fv);
#pragma unroll
        for (int c = 0; c < 8; c++) xq[i][c] = (c & 1) ? pkfmas<1>(n2[c], sc[c >> 1], x2[c]) : pkfmas<0>(n2[c], sc[c >> 1], x2[c]);
        SB();
        if (i == 3) __builtin_amdgcn_s_setprio(PPRIO);
      }
    }
    STAMP(4);
    const rsrc_t ro = __builtin_amdgcn_make_buffer_rsrc(out, 0, 2 * L * D, 0x00020000);
    const int u = 64 * (p & 1) + l;
    const char* yp = smem + OY + 512 * (p >> 1) + 4 * (u ^ ((u >> 4) & 6));
    const char* yq = yp + 4 * CHR;
    const u32 op = (u32)((128 * p + 2 * l) * (2 * D) + 16 * g);
    u32 ys[8];
    u32x2 ix = {0u, 0u};
    u32 px = 0u, pd = 0u;
    u32x4 pv[7];
#define PGLD() do { _Pragma("unroll") for (int q = 0; q < 7; q++) pv[q] = ld16(rs, O_PG + ((2 * q + (l >> 5)) * 128 + g) * 512 + 16 * (l & 31)); } while (0)
#pragma unroll
    for (int i = 0; i < 4; i++) {
      if (i == 2 && p == 0) {
        const u32 io = ixo - 8 * (l & 7) + 2 * (l & 28);
#pragma unroll 1
        for (int spin = 0; spin < SPIN; spin++) {
          ix = ld8(rs, io);
          if (__all(ix.y == tag)) break;
          NAP();
        }
        px = x[(size_t)(ix.x & 2047u) * D + 8 * g + 2 * (l & 3) + (l >> 5)];
        pd = dsk[8 * g + 2 * (l & 3) + (l >> 5)];
        if (!(l & 35)) atomicOr((u32*)(smem + OMISC) + ((ix.x & 2047u) >> 5), 1u << (ix.x & 31u));
        FLAG(OYP + 32, 8u);
        STAMP(11);
      }
      if (i == 3) {
        asm volatile("s_waitcnt vmcnt(2)" ::: "memory");
        FLAG(OYP + 36 + 4 * p, 8u);
      }
      AWAIT(OYP + 4 * min(l, i < 2 ? 7 : 8), 2 * i + 1 + (p >> 1), YNAP);
      if (i == 0) STAMP(5); else if (i == 1) STAMP(6); else if (i == 2) STAMP(7); else STAMP(8);
      u32 y[8], bm = 0u;
#pragma unroll
      for (int c = 0; c < 8; c++) y[c] = *(const u32*)((c < 4 ? yp : yq) + (c & 3) * CHR + 1024 * i);
      if (i >= 2) bm = *(const u32*)(smem + OMISC + 64 * i + 16 * p + 4 * kg) >> (2 * n16);
      if (i == 0) {
        const u32x4 a = *(const u32x4*)(smem + OSC), c = *(const u32x4*)(smem + OSC + 16);
#pragma unroll
        for (int k = 0; k < 4; k++) { ys[k] = (u32)__builtin_amdgcn_readfirstlane((int)a[k]); ys[4 + k] = (u32)__builtin_amdgcn_readfirstlane((int)c[k]); }
      }
      SB();
      if (i == 3 && p == 0) PGLD();
      SB();
#pragma unroll
      for (int r = 0; r < 2; r++) {
        u32x4 v;
#pragma unroll
        for (int k = 0; k < 4; k++)
          v[k] = pack2(dot2b(y[2 * k], ys[2 * k] << (16 * r), xq[i][2 * k][r]), dot2b(y[2 * k + 1], ys[2 * k + 1] << (16 * r), xq[i][2 * k + 1][r]));
        __builtin_amdgcn_raw_buffer_store_b128(v, ro, op | (bm << (31 - r) & 0x80000000u), (512 * i + r) * (2 * D), 0);
        SB();
      }
    }
    STAMP(12);
    if (p == 0) {
      AWAIT(OYP + 4 * min(l, 12), 8, YNAP);
      const int prow = (int)(ix.x & 2047u), c = 2 * (l & 3) + (l >> 5), v = prow >> 1;
      const float yl = lo2f(*(const u16*)(smem + OY + CHR * c + 4 * (v ^ ((v >> 4) & 6)) + 2 * (prow & 1)));
      const float ql = hlo(*(const u16*)(smem + OX + 2 * XO + c * XREG + 2 * prow));
      const float e = lo2f(px) + yl * lo2f(*(const u32*)(smem + OSC + 4 * c)) + ql * (1.f + lo2f(pd));
      const float rsel = rb(sigm(hi2f(ix.x)));
#pragma unroll 1
      for (int spin = 0; spin < SPIN; spin++) {
        bool ok = true;
#pragma unroll
        for (int q = 0; q < 7; q++) ok &= pv[q].y == tag && pv[q].w == tag;
        if (__all(ok)) break;
        NAP();
        PGLD();
      }
      STAMP(14);
      float s0 = 0.f, s1 = 0.f;
#pragma unroll
      for (int q = 0; q < 7; q++) { const u32 a = pv[q].x, c2 = pv[q].z; s0 += __uint_as_float(a); s1 += __uint_as_float(c2); }
      swap32(s0, s1);
      const float upd = rb(rb(s0 + s1) * rsel);
      out[(size_t)prow * D + 8 * g + c] = (u16)(pack2(e + upd, 0.f) & 0xffffu);
    }
    STAMP(13);
    FLUSH();
    return;
  }

  const int d = 8 * g + w;
  char* W = smem + w * CHR;
  char* X = smem + OX + w * XREG;
  u32 pl = ((const u32*)lar)[d * 64 + l], pa = ((const u32*)aim)[d * 64 + l], ldtv = ldt[d];
  u32 pr = ((const u32*)cre)[d * 64 + l], pi = ((const u32*)cim)[d * 64 + l];
  SB();
  const u32x4 te = *(const u32x4*)TAB.v[0][l];
  const u32x3 to = *(const u32x3*)TAB.v[1][l];
  SB();
  CODELD();
  STAMP(3);
  const u32x4 zero4 = {0u, 0u, 0u, 0u};
  if (w == 0) *(u32x2*)(smem + OMISC + 8 * l) = u32x2{0u, 0u};
  PIN_TAG();
  LBAR();
  STAMP(4);
  if (l < 33) *(u32x4*)(X + 16 * l) = zero4;
  if (l == 33) *(u32*)(X + 528) = 0u;
  if (l >= 34 && l < 37) *(u32*)(X + 2 * (XO + 2048) + 4 * (l - 34)) = 0u;
  *(u32x4*)(W + 2 * QB + 16 * l) = zero4; *(u32x4*)(W + 2 * QB + 1024 + 16 * l) = zero4; *(u32x4*)(W + CHR - 1024 + 16 * l) = zero4;
  SB();
  asm volatile("s_mov_b32 m0, %0\n\ts_nop 0\n\tds_write_addtid_b32 %1\n\tds_write_addtid_b32 %1 offset:%2" ::"s"(lds0 + w * CHR), "v"(0x3c00u), "n"(QB) : "memory");
  asm volatile("" : "+v"(pl), "+v"(pa), "+v"(ldtv));
  STAMP(10);
  STAMPC(11);
  f32x4 ke[2] = {}, ko[2] = {};
  {
    const float dt = rb(fexp(lo2f(ldtv)));
    f32x2 z1[2], z8[2], zB[2], cc[2];
#pragma unroll
    for (int i = 0; i < 2; i++) {
      const float av = i ? hi2f(pl) : lo2f(pl), iv = i ? hi2f(pa) : lo2f(pa);
      const float arl = -rb(fexp(av)) * dt * 1.4426950408889634f, aif = iv * dt * 0.15915494309189535f;
      float a, c;
      cexp2(arl, aif, 1.f, a, c); z1[i] = f32x2{a, c};
      cexp2(arl, aif, 256.f, a, c); zB[i] = f32x2{a, c};
    }
    u32 q2[2], q4[2], q6[2];
    {
      f32x2 z2[2], z3[2], z4[2], z6[2];
      u32 q1[2], q3[2], q8[2];
      cm2(z2, z1, z1); cj2<1>(q1, z1);
      cm2(z3, z2, z1); cj2<2>(q2, z2);
      cm2(z4, z2, z2); cj2<3>(q3, z3);
      cm2(z6, z4, z2); cj2<4>(q4, z4);
      cm2(z8, z4, z4);
      cj2<6>(q6, z6);
      hw2<5, QB>(q4, q1); hw2<7, QB>(q4, q3);
      cj2<8>(q8, z8);
    }
    {
      f32x2 zC[2], zD[2], zE[2];
      u32 qB[2], qC[2], qD[2], qE[2];
      cm2(zC, zB, zB); cj2<9>(qB, zB);
      cm2(zD, zC, zB); cj2<13>(qC, zC);
      cm2(zE, zC, zC); cj2<15>(qD, zD);
      hw2<10, QB>(qB, q2); cj2<17>(qE, zE);
      hw2<11, QB>(qB, q4); hw2<12, QB>(qB, q6); hw2<14, QB>(qC, q4); hw2<16, QB>(qD, q4);
      hw2<18, QB>(qE, qB); hw2<19, QB>(qE, qC); hw2<20, QB>(qE, qD); hw2<21, QB>(qE, qE);
    }
    u32x4 qe[8], qo[8];
    {
      const u32 tw = te.w;
      const char* pe = W + (tw & 0xffffu);
      const char* po = W + (tw >> 16);
#pragma unroll
      for (int s = 0; s < 8; s++) { qe[s] = *(const u32x4*)(pe + 64 * (s & 3) + QB * (s >> 2)); qo[s] = *(const u32x4*)(po + 64 * (s & 3) + QB * (s >> 2)); }
#pragma unroll
      for (int i = 0; i < 4; i++) *(u32x4*)(W + (i < 3 ? TB + 1024 * i : 2 * QB - 1024) + 16 * l) = zero4;
    }
    SB();
    {
      const u32 mx = max(max(pr & 0x7fffu, (pr >> 16) & 0x7fffu), max(pi & 0x7fffu, (pi >> 16) & 0x7fffu));
      int e = (int)(wmaxu(mx) >> 7);
      e = e < 8 ? 8 : e;
      const float csc = __uint_as_float((u32)(260 - e) << 23);
      *(u32*)(smem + OSC + 4 * w) = __float_as_uint(__uint_as_float((u32)(e - 5) << 23) * dt) >> 16;
      cc[0] = f32x2{lo2f(pr) * csc, lo2f(pi) * csc}; cc[1] = f32x2{hi2f(pr) * csc, hi2f(pi) * csc};
    }
    u32 p8[2], p16[2], p24[2];
    f32x2 zs[2];
    {
      f32x2 z16[2], z24[2];
      cm2(z16, z8, z8); cw2<-1>(p8, z8);
      cm2(z24, z16, z8); cw2<-1>(p16, z16);
      cm2(zs, z16, z16); cw2<-1>(p24, z24);
    }
    const char* pfr = W + n16 * RS + 16 * kg;
    sfor<0, 2>([&](auto H_) __attribute__((always_inline)) {
      constexpr int h = decltype(H_)::value;
      sfor<0, 4>([&](auto I_) __attribute__((always_inline)) {
        constexpr int i = decltype(I_)::value;
        u32 pb[2];
        cw2<4 * i>(pb, cc);
        if (4 * h + i < 7) { f32x2 nx[2]; cm2(nx, cc, zs); cc[0] = nx[0]; cc[1] = nx[1]; }
        hw2<4 * i + 1, PB>(pb, p8); hw2<4 * i + 2, PB>(pb, p16); hw2<4 * i + 3, PB>(pb, p24);
      });
      u32x4 pf[8];
#pragma unroll
      for (int s = 0; s < 8; s++) pf[s] = *(const u32x4*)(pfr + 64 * (s & 3) + PB * (s >> 2));
#pragma unroll
      for (int s = 0; s < 8; s++) {
        ke[h] = MFMAH(ash(qe[s]), ash(pf[s]), ke[h]);
        ko[h] = MFMAH(ash(qo[s]), ash(pf[s]), ko[h]);
      }
    });
  }
  STAMP(1);
  STAMPC(12);
#define FRAG(o0, o1) mkh(*(const volatile LDS u32x2*)(tb + (o0)), *(const volatile LDS u32x2*)(tb + (o1)))
  f16x8 FR[9], F1[4][2], F2[2][3], F3[4];
  float k1024;
  const LDS char* tb;
  {
    const u32 tx = te.x, ty = te.y, tz = te.z;
    if (l < 32) *(u32x4*)(W + (ty >> 16)) = zero4;
    u32x2 v[2];
#pragma unroll
    for (int h = 0; h < 2; h++) {
      v[h] = u32x2{packh(ke[h][0], ke[h][1]), packh(ke[h][2], ke[h][3])};
      *(u32x2_a4*)(W + (tx & 0xffffu) + 256 * (1 - h)) = v[h];
      *(u32x2_a4*)(W + (tx >> 16) + 256 * (1 - h)) = v[h];
    }
    const u32 vy = v[0].y, vx = v[1].x;
    const u32 k0 = (u32)__builtin_amdgcn_readlane((int)vy, 0) >> 16, k256 = (u32)__builtin_amdgcn_readlane((int)vx, 63);
    if (l < 8) *(u16*)(W + (ty & 0xffffu)) = (u16)((l & 5) == 1 ? k0 : k256);
    tb = (const LDS char*)W + (tz >> 16);
#pragma unroll
    for (int c = 0; c < 9; c++) FR[c] = FRAG(64 * c, 64 * c + 8);
  }
  {
    const u32 tx = to.x, ty = to.y, tz = to.z;
    const u32 tp[5] = {tx & 0xffffu, tx >> 16, ty & 0xffffu, ty >> 16, tz & 0xffffu};
#pragma unroll
    for (int h = 0; h < 2; h++) {
      const u32 p0 = packh(ko[h][0], ko[h][1]), p1 = packh(ko[h][2], ko[h][3]);
      if (h == 0) k1024 = -rdl(hhi(p1), 48);
      if (h == 0 || kg < 3) {
#pragma unroll
        for (int s = 0; s < 4; s++) {
          const int o = s * (CPO - 2) + 16 * (1 - h);
          *(u16*)(W + tp[h ? 0 : 4] + o) = (u16)p0; *(u16*)(W + tp[1] + o) = (u16)(p0 >> 16);
          *(u16*)(W + tp[2] + o) = (u16)p1; *(u16*)(W + tp[3] + o) = (u16)(p1 >> 16);
        }
      }
    }
    tb = (const LDS char*)W + (tz >> 16);
#pragma unroll
    for (int p = 0; p < 4; p++)
#pragma unroll
      for (int dl = 0; dl < 2; dl++) F1[p][dl] = FRAG(2 * (64 * p + 16 - 16 * dl), 2 * (64 * p + 32 + 16 - 16 * dl));
#pragma unroll
    for (int p = 0; p < 2; p++)
#pragma unroll
      for (int dl = 0; dl < 3; dl++) F2[p][dl] = FRAG(2 * (T2B + 96 * p + 32 - 16 * dl), 2 * (T2B + 96 * p + 48 + 32 - 16 * dl));
  }
  if (l < 4) *(u32x4*)(W + (l == 0 ? OW3 : l == 1 ? OW5 : l == 2 ? OW7 : OW9)) = zero4;
  CODEUSE();
  STAMP(2);
  AWAIT(OXF + 4 * (l & 3), 1, "s_sleep 1");
  STAMP(5);
  STAMPC(14);
  __builtin_amdgcn_s_setprio(CPRIO);
  {
    const char* xb = X + 32 * n16 + 16 * kg;
    const char* xu = X + 2 * XO + 8 * l;
    const char* wb = W + 32 + 8 * l;
    const char* xq = X + 512 + 16 * l;
    char* wo = W + 16 + 16 * l;
    char* Y = W + OY + 32 * n16 + 8 * (kg ^ ((l >> 2) & 3));
    f32x4 acc[8];
    u32x4 A[NOPS];
    u32x4 Q1, Q2, W7, W9;
    u32 Q0 = 0u, Uo[8], P2[8], P4[8], W3[8], W5[8], xf = 0u, fl = 0u, fad = lds0 + OYP + 4 * w;
    u32x2 fa[2], yv;
    u32 fc[2];
    asm volatile("" : "+v"(fad));
    auto winl = [&](auto P_) __attribute__((always_inline)) {
      constexpr int pp = decltype(P_)::value;
      Q1 = *(const u32x4*)(xq + 1024 * pp);
      if (pp < 2) { Q2 = *(const u32x4*)(xq + 1024 * pp + 16); Q0 = *(const u32*)(xq + 1024 * pp - 4); }
      else { const u32x2 v = *(const u32x2*)(xq + 1024 * pp + 16); Q2.x = v.x; Q2.y = v.y; }
    };
    auto win = [&](auto K_) __attribute__((always_inline)) {
      constexpr int k = decltype(K_)::value;
      const u32 Dd[8] = {Q0, Q1.x, Q1.y, Q1.z, Q1.w, Q2.x, Q2.y, Q2.z};
      if constexpr (k < 5) Uo[2 + k] = __builtin_amdgcn_alignbit(Dd[2 + k], Dd[1 + k], 16);
      else if constexpr (k < 9) P2[k - 3] = hadd(Dd[k - 3], Uo[k - 3]);
      else if constexpr (k < 13) W3[k - 7] = hadd(P2[k - 7], Uo[k - 6]);
      else if constexpr (k == 13) P2[6] = hadd(Dd[6], Uo[6]);
      else if constexpr (k < 18) P4[k - 11] = hadd(P2[k - 11], P2[k - 12]);
      else if constexpr (k < 22) W5[k - 16] = hadd(P4[k - 15], Dd[k - 17]);
      else if constexpr (k == 22) Uo[1] = __builtin_amdgcn_alignbit(Dd[1], Dd[0], 16);
      else if constexpr (k == 23) Uo[7] = __builtin_amdgcn_alignbit(Dd[7], Dd[6], 16);
      else if constexpr (k == 24) P2[1] = hadd(Dd[1], Uo[1]);
      else if constexpr (k == 25) P2[7] = hadd(Dd[7], Uo[7]);
      else if constexpr (k == 26) P4[2] = hadd(P2[2], P2[1]);
      else if constexpr (k == 27) P4[7] = hadd(P2[7], P2[6]);
      else if constexpr (k == 28) W3[6] = hadd(P2[6], Uo[7]);
      else if constexpr (k == 29) W5[1] = hadd(P4[2], Dd[0]);
      else if constexpr (k < 34) W7[k - 30] = hadd(W3[k - 27], P4[k - 28]);
      else W9[k - 34] = hadd(P4[k - 30], W5[k - 33]);
      if constexpr (k == 13) asm volatile("" : "+v"(P2[6]));
      if constexpr (k > 13 && k < 18) asm volatile("" : "+v"(P4[k - 11]));
      if constexpr (k >= 18 && k < 22) asm volatile("" : "+v"(W5[k - 16]));
    };
    const char* xfx = xb - 8 * kg + 2 * 260;
#define FIXL(I) do { fa[(I) & 1] = *(const u32x2*)(xfx + 512 * ((I) - 4)); fc[(I) & 1] = *(const u32*)(xfx + 512 * ((I) - 4) + 8); } while (0)
#define FIXM(I, h) do { const u32 ax = fa[(I) & 1].x, ay = fa[(I) & 1].y; \
      if (h) { acc[I][2] = mixs<1>(ay, k1024); acc[I][3] = mixs<0>(fc[(I) & 1], k1024); } \
      else { acc[I][0] = mixs<1>(ax, k1024); acc[I][1] = mixs<0>(ay, k1024); } } while (0)
    auto loads = [&](auto M_) __attribute__((always_inline)) {
      constexpr int m = decltype(M_)::value;
      sfor<0, 2>([&](auto N_) __attribute__((always_inline)) {
        constexpr int mm = m + LA + decltype(N_)::value;
        if constexpr (mm < NSLOT) {
          constexpr int s = OPS.op[mm], k = OPS.k[s], T = OPS.a[s], p = OPS.b[s];
          if constexpr (OPS.first[s] == mm) {
            if constexpr (k == 0) { if (OPS.ld[s][1] == m) A[s] = *(const u32x4*)(xb + 512 * T + 64 * p); }
            else {
              if (OPS.ld[s][0] == m) {
                const u32x2 a = trread(k == 3 ? wb + OW7 + 512 * T - 16 : k == 2 ? wb + OW3 + 512 * T - 2 * (4 + 8 * p) : xu + 512 * T - 2 * (2 + 4 * p));
                A[s].x = a.x; A[s].y = a.y;
              }
              if (OPS.ld[s][1] == m) {
                const u32x2 c = trread(k == 3 ? wb + OW9 + 512 * T - 32 : k == 2 ? wb + OW5 + 512 * T - 2 * (8 + 8 * p) : wb + OW3 + 512 * T - 2 * (4 + 4 * p));
                A[s].z = c.x; A[s].w = c.y;
              }
            }
          }
        }
      });
    };
    constexpr int FX[4] = {OPS.first[opat(2, 0, 0)], OPS.first[opat(2, 1, 0)], OPS.first[opat(3, 0, 0)] - 2, OPS.first[opat(3, 0, 0)]};
    constexpr int FT[8] = {0, OPS.first[opat(0, 1, 0)] + 1, OPS.first[opat(0, 2, 0)] + 1, OPS.first[opat(2, 0, 0)] + 1, -1, -1, -1, -1};
    loads(IC<-LA>{});
    winl(IC<0>{});
    sfor<1 - LA, 0>(loads);
    auto step = [&](auto M_) __attribute__((always_inline)) {
      constexpr int m = decltype(M_)::value, s = OPS.op[m], u = m - OPS.first[s], k = OPS.k[s], T = OPS.a[s], p = OPS.b[s];
      SB();
      const f16x8 a = ash(A[s]);
#define MM(I, x_, y_) do { if constexpr (m == FT[I]) asm volatile("v_mfma_f32_16x16x32_f16 %0, %1, %2, 0" : "=&v"(acc[I]) : "v"(x_), "v"(y_)); \
      else asm volatile("v_mfma_f32_16x16x32_f16 %0, %1, %2, %0" : "+v"(acc[I]) : "v"(x_), "v"(y_)); } while (0)
      if constexpr (k == 0) {
        if constexpr (p == 0 && T > 0 && u == 0) MM(T - 1, FR[8], a);
        else MM(T, FR[p], a);
      }
      else if constexpr (k == 3) MM(T + 4 + u, a, F3[u]);
      else if constexpr (k == 2) MM(T + 2 + u, a, F2[p][u]);
      else MM(T + 1 + u, a, F1[p][u]);
      SB();
      sfor<0, 3>([&](auto N_) __attribute__((always_inline)) {
        constexpr int n = decltype(N_)::value;
        if (m == XC[n]) { if (!__all(xf >= n + 2u)) AWAIT(OXF + 4 * (l & 3), n + 2, "s_sleep 1"); }
        if (m == XC[n] - 4) xf = *(const volatile LDS u32*)(LDS char*)(smem + OXF + 4 * (l & 3));
      });
      sfor<0, 7>([&](auto I_) __attribute__((always_inline)) {
        constexpr int I = decltype(I_)::value;
        if (m == OPS.end[I] + 4) {
          *(u32x2*)(Y + 512 * I) = yv;
          asm volatile("ds_write_b32 %0, %1" ::"v"(fad), "v"(fl) : "memory");
          if (I == 0) STAMP(6);
          if (I == 3) STAMP(7);
        }
      });
      sfor<0, 4>([&](auto P_) __attribute__((always_inline)) {
        constexpr int pp = decltype(P_)::value;
#if !(ABL & 4)
        if (m == wslot(pp, 12) + 1) *(u32x4*)(wo + 1024 * pp + OW3) = u32x4{W3[2], W3[3], W3[4], W3[5]};
        if (pp < 3 && m == wslot(pp, 21) + 1) *(u32x4*)(wo + 1024 * pp + OW5) = u32x4{W5[2], W5[3], W5[4], W5[5]};
        if (pp < 2 && m == wslot(pp, 33) + 1) *(u32x4*)(wo + 1024 * pp + OW7) = W7;
        if (pp < 2 && m == wslot(pp, 37) + 1) *(u32x4*)(wo + 1024 * pp + OW9) = W9;
#endif
        if (m == WL[pp]) winl(P_);
        if (m == FX[pp] - 6) FIXL(4 + pp);
        if (m == FX[3] - 8 + pp) F3[pp] = FRAG(2 * (T3B + 48 - 16 * pp), 2 * (T3B + 80 + 48 - 16 * pp));
      });
      loads(M_);
      SB();
      sfor<0, 4>([&](auto P_) __attribute__((always_inline)) {
        constexpr int pp = decltype(P_)::value;
        if (pp < 2 && m == wslot(pp, 3)) asm volatile("" : "+v"(Q2));
        if (!(ABL & 6)) sfor<0, WN[pp]>([&](auto K_) __attribute__((always_inline)) { if constexpr (wslot(pp, decltype(K_)::value) == m) win(K_); });
        if (m == FX[pp] - 1) FIXM(4 + pp, 0);
        if (m == FX[pp]) FIXM(4 + pp, 1);
      });
      sfor<0, 7>([&](auto I_) __attribute__((always_inline)) {
        constexpr int I = decltype(I_)::value;
        if (m == OPS.end[I] + 3) {
          yv = u32x2{pack2(acc[I][0], acc[I][1]), pack2(acc[I][2], acc[I][3])};
          fl = I + 1;
          asm volatile("" : "+v"(fl), "+v"(yv));
        }
      });
    };
    sfor<0, NSLOT>(step);
    SB();
    asm volatile("s_nop 7\n\ts_nop 1" : "+v"(acc[7]));
    *(u32x2*)(Y + 512 * 7) = u32x2{pack2(acc[7][0], acc[7][1]), pack2(acc[7][2], acc[7][3])};
    FLAG(OYP + 4 * w, 8u);
  }
  STAMP(13);
  STAMPC(15);
  FLUSH();
}

extern "C" void kernel_launch(void* const* d_in, const int* in_sizes, int n_in, void* d_out, int out_size,
                              void* d_ws, size_t ws_size, hipStream_t stream) {
  const u16* const* in = (const u16* const*)d_in;
  fused<<<256, 768, 0, stream>>>(in[0], in[1], in[2], in[3], in[4], in[5], in[6], in[8], in[9], in[10], in[11],
                                 (u16*)d_out, (char*)d_ws);
}
```

```cpp
#include <hip/hip_runtime.h>
#include <stdint.h>

#define STR2(x) #x
#define STR(x) STR2(x)
#define LA 4
#define WL2 44
#define WL3 100
#define TOG 0
#define ABL 0
#define PPRIO 0
#define CPRIO 1
#define GDELAY 0
#define ROT 1
#define PRE 0
#define NE 8
#define YNAP "s_sleep 2"
#define CODE_KB 24
typedef unsigned short u16;
typedef unsigned int u32;
typedef __attribute__((ext_vector_type(8))) __bf16 bf16x8;
typedef __attribute__((ext_vector_type(2))) __bf16 bf16x2;
typedef __attribute__((ext_vector_type(8))) _Float16 f16x8;
typedef __attribute__((ext_vector_type(2))) _Float16 f16x2;
typedef __attribute__((ext_vector_type(4))) short v4i16;
typedef __attribute__((ext_vector_type(4))) float f32x4;
typedef __attribute__((ext_vector_type(2))) float f32x2;
typedef __attribute__((ext_vector_type(4))) u32 u32x4;
typedef __attribute__((ext_vector_type(2))) u32 u32x2;
typedef __attribute__((ext_vector_type(8))) u32 u32x8;
typedef __attribute__((ext_vector_type(2), aligned(4))) u32 u32x2_a4;
typedef __amdgpu_buffer_rsrc_t rsrc_t;
#define LDS __attribute__((address_space(3)))

#define STAMP(I)
#define STAMPC(I)
#define FLUSH()

constexpr int L = 2048, D = 1024, NM = 128, F = 3584;
constexpr u32 O_ARR = 0x1000, O_RG = 0x4000, O_SG = 0x8000, O_IDX = 0x10000, O_HG = 0x20000,
              O_PG = 0x90000, WS_END = 0x170000;
constexpr int SPIN = 1 << 16;

__device__ __forceinline__ float lo2f(u32 w) { return __uint_as_float(w << 16); }
__device__ __forceinline__ float hi2f(u32 w) { return __uint_as_float(w & 0xffff0000u); }
__device__ __forceinline__ u32 pack2(float a, float b) {
  bf16x2 r = {(__bf16)a, (__bf16)b};
  return __builtin_bit_cast(u32, r);
}
__device__ __forceinline__ u32 packh(float a, float b) {
  f16x2 r = {(_Float16)a, (_Float16)b};
  return __builtin_bit_cast(u32, r);
}
__device__ __forceinline__ float rb(float f) { return __uint_as_float(pack2(0.f, f)); }
__device__ __forceinline__ u32 hadd(u32 a, u32 b) {
  return __builtin_bit_cast(u32, __builtin_bit_cast(f16x2, a) + __builtin_bit_cast(f16x2, b));
}
__device__ __forceinline__ float hlo(u32 a) { return (float)__builtin_bit_cast(f16x2, a).x; }
__device__ __forceinline__ float hhi(u32 a) { return (float)__builtin_bit_cast(f16x2, a).y; }

template <int CTRL, int RM>
__device__ __forceinline__ float dppf(float v) {
  return __builtin_bit_cast(float, __builtin_amdgcn_update_dpp(0, __builtin_bit_cast(int, v), CTRL, RM, 0xf, true));
}
template <int CTRL, int RM>
__device__ __forceinline__ u32 dppu(u32 v) {
  return (u32)__builtin_amdgcn_update_dpp(0, (int)v, CTRL, RM, 0xf, true);
}
__device__ __forceinline__ float rdl(float v, int i) {
  return __builtin_bit_cast(float, __builtin_amdgcn_readlane(__builtin_bit_cast(int, v), i));
}
__device__ __forceinline__ u32 wmaxu(u32 v) {
  v = max(v, dppu<0x111, 0xf>(v)); v = max(v, dppu<0x112, 0xf>(v)); v = max(v, dppu<0x114, 0xf>(v));
  v = max(v, dppu<0x118, 0xf>(v)); v = max(v, dppu<0x142, 0xa>(v)); v = max(v, dppu<0x143, 0xc>(v));
  return (u32)__builtin_amdgcn_readlane((int)v, 63);
}
__device__ __forceinline__ void swap32(float& a, float& b) {
  auto r = __builtin_amdgcn_permlane32_swap(__float_as_uint(a), __float_as_uint(b), false, false);
  a = __uint_as_float(r[0]); b = __uint_as_float(r[1]);
}
__device__ __forceinline__ void swap16(float& a, float& b) {
  auto r = __builtin_amdgcn_permlane16_swap(__float_as_uint(a), __float_as_uint(b), false, false);
  a = __uint_as_float(r[0]); b = __uint_as_float(r[1]);
}
__device__ __forceinline__ u32x2 trread(const char* p) {
  v4i16 r = __builtin_amdgcn_ds_read_tr16_b64_v4i16((LDS v4i16*)p);
  return __builtin_bit_cast(u32x2, r);
}
__device__ __forceinline__ u32x2 trl(LDS char* p) { return __builtin_bit_cast(u32x2, __builtin_amdgcn_ds_read_tr16_b64_v4i16((LDS v4i16*)p)); }
__device__ __forceinline__ f16x8 mkh(u32x2 a, u32x2 b) {
  u32x4 v = {a.x, a.y, b.x, b.y};
  return __builtin_bit_cast(f16x8, v);
}
__device__ __forceinline__ bf16x8 mkb(u32x2 a, u32x2 b) {
  u32x4 v = {a.x, a.y, b.x, b.y};
  return __builtin_bit_cast(bf16x8, v);
}
__device__ __forceinline__ bf16x8 asb(u32x4 v) { return __builtin_bit_cast(bf16x8, v); }
__device__ __forceinline__ f16x8 ash(u32x4 v) { return __builtin_bit_cast(f16x8, v); }
#define MFMAB(a, b, c) __builtin_amdgcn_mfma_f32_16x16x32_bf16(a, b, c, 0, 0, 0)
#define MFMAH(a, b, c) __builtin_amdgcn_mfma_f32_16x16x32_f16(a, b, c, 0, 0, 0)

__device__ __forceinline__ u32x4 ld16(rsrc_t r, u32 off) { return __builtin_amdgcn_raw_buffer_load_b128(r, off, 0, 16); }
__device__ __forceinline__ u32x2 ld8(rsrc_t r, u32 off) { return __builtin_amdgcn_raw_buffer_load_b64(r, off, 0, 16); }
__device__ __forceinline__ void st16(rsrc_t r, u32 off, u32x4 v) { __builtin_amdgcn_raw_buffer_store_b128(v, r, off, 0, 16); }
__device__ __forceinline__ void st8(rsrc_t r, u32 off, u32x2 v) { __builtin_amdgcn_raw_buffer_store_b64(v, r, off, 0, 16); }
#define NAP() asm volatile("s_sleep 1" ::: "memory")
#define SB() __builtin_amdgcn_sched_barrier(0)
#define LBAR() asm volatile("s_waitcnt lgkmcnt(0)\n\ts_barrier" ::: "memory")

__device__ __forceinline__ float fexp(float v) { return __builtin_amdgcn_exp2f(v * 1.4426950408889634f); }
__device__ __forceinline__ float dot2b(u32 a, u32 b, float c) {
  return __builtin_amdgcn_fdot2_f32_bf16(__builtin_bit_cast(bf16x2, a), __builtin_bit_cast(bf16x2, b), c, false);
}
__device__ __forceinline__ float sigm(float v) { return __builtin_amdgcn_rcpf(1.f + __builtin_amdgcn_exp2f(-1.4426950408889634f * v)); }

constexpr int RS = 288, PB = 16 * RS;
constexpr int OW3 = 0, OW5 = 3632, OW7 = 6720, OW9 = 8784, OY = 11648, CHR = OY + 4096;
constexpr int OX = 8 * CHR, XREG = 4640, XO = 266;
constexpr int OMISC = OX + 8 * XREG;
constexpr int OYP = OMISC + 256, OXF = OMISC + 320, OCN = OMISC + 336, OSC = OMISC + 352, OLN = OMISC + 384;
constexpr int SMEM = OMISC + 512;
constexpr int CPE = 704, TB = 2 * PB, CPO = 576;
constexpr int T2B = 2 * CPO + 4, T3B = (OY + 2048 - TB) / 2, C1 = 36, C2 = 52, C3 = 84;
constexpr int FA = 131072, AP = 1056, FH = FA + 8 * AP, HP = 544, FC = FH + 8 * HP, FL = FC + 128;
static_assert(FL + 640 <= SMEM && SMEM <= 163840, "lds map");

struct Tab { u32 v[2][64][4]; };
constexpr Tab mktab() {
  Tab T = {};
  for (int l = 0; l < 64; l++) {
    const int n16 = l & 15, kg = l >> 4, pc = ((n16 >> 2) & 1) + (n16 & 8) + 2 * (3 - (n16 & 3)), odd = kg >> 1, s = l & 3;
    const int e0 = 278 - 128 - 8 * pc - 4 * (kg & 1) - odd;
    const int ez = ((l >> 3) & 3) * CPE + ((l & 4) ? 544 - 64 : 0) + 16 * (l & 7);
    const int b1 = 28 + C1 * (n16 + 4 * (kg & 1)), b2 = T2B + 44 + C2 * ((n16 & 1) + 2 * (kg & 1)) - 8 * (n16 & 2);
    const int b3 = T3B + 60 + C3 * (kg & 1) - 16 * ((n16 < 11 ? n16 : 11) - 8);
    const int to = TB + 2 * ((n16 < 4 ? b1 : n16 < 8 ? b2 : b3) - 4 * odd - 8);
    const int sp = n16 < 8 ? s * (CPE - 2) + 2 * ((l & 4) ? 281 - 257 : (s & 1) ? 281 : 281 - 256) : TB + s * (CPO - 2) + 2 * ((l & 4) ? T2B + 15 : 15);
    T.v[0][l][0] = (2 * e0 + (2 - odd) * (CPE - 2)) | (2 * e0 + 3 * odd * (CPE - 2)) << 16;
    T.v[0][l][1] = ez | to << 16;
    T.v[0][l][2] = sp | (s == 0 ? OW3 : s == 1 ? OW5 : s == 2 ? OW7 : OW9) << 16;
    T.v[0][l][3] = n16 < 8 ? ((l & 5) == 1 ? 0x0100u : 0x0302u) : (l & 4) ? 0x0706u : 0x0504u;
    const int jx = n16 + 5 - 8 * kg;
    for (int r = 0; r < 4; r++) T.v[1][l][r] = jx == 2 * r ? 0xffffu : jx == 2 * r + 1 ? 0xffff0000u : 0u;
  }
  return T;
}
__device__ static const __attribute__((aligned(16))) Tab TAB = mktab();

#define GEN_ASM \
  "s_waitcnt vmcnt(4)\n\tv_lshlrev_b32 v56, 16, v96\n\t" \
  "v_and_b32 v57, 0xffff0000, v96\n\tv_pk_mul_f32 v[56:57], %[k], v[56:57] op_sel_hi:[0,1]\n\t" \
  "v_lshlrev_b32 v49, 16, v97\n\tv_exp_f32 v56, v56\n\t" \
  "v_exp_f32 v57, v57\n\tv_and_b32 v51, 0xffff0000, v97\n\t" \
  "v_cvt_pk_bf16_f32 v56, v56, v57\n\tv_lshlrev_b32 v48, 16, v56\n\t" \
  "v_and_b32 v50, 0xffff0000, v56\n\tv_pk_mul_f32 v[48:49], %[c], v[48:49]\n\t" \
  "v_pk_mul_f32 v[50:51], %[c], v[50:51]\n\tv_pk_mul_f32 v[98:99], %[k], v[48:49] op_sel:[1,0]\n\t" \
  "v_pk_mul_f32 v[100:101], %[k], v[50:51] op_sel:[1,0]\n\tv_fract_f32 v49, v49\n\t" \
  "v_fract_f32 v51, v51\n\tv_exp_f32 v48, v48\n\t" \
  "v_exp_f32 v50, v50\n\tv_cos_f32 v40, v49\n\t" \
  "v_sin_f32 v41, v49\n\tv_cos_f32 v42, v51\n\t" \
  "v_sin_f32 v43, v51\n\tv_pk_mul_f32 v[40:41], v[40:41], v[48:49] op_sel_hi:[1,0]\n\t" \
  "v_pk_mul_f32 v[42:43], v[42:43], v[50:51] op_sel_hi:[1,0]\n\tv_pk_mul_f32 v[48:49], v[40:41], v[40:41] op_sel:[1,1] op_sel_hi:[1,0] neg_lo:[1,0]\n\t" \
  "v_pk_mul_f32 v[50:51], v[42:43], v[42:43] op_sel:[1,1] op_sel_hi:[1,0] neg_lo:[1,0]\n\tv_pk_fma_f32 v[48:49], v[40:41], v[40:41], v[48:49] op_sel:[0,0,0] op_sel_hi:[0,1,1]\n\t" \
  "v_pk_fma_f32 v[50:51], v[42:43], v[42:43], v[50:51] op_sel:[0,0,0] op_sel_hi:[0,1,1]\n\tv_cvt_pk_f16_f32 v72, v40, -v41\n\t" \
  "v_cvt_pk_f16_f32 v73, v42, -v43\n\tv_pk_mul_f32 v[52:53], v[48:49], v[40:41] op_sel:[1,1] op_sel_hi:[1,0] neg_lo:[1,0]\n\t" \
  "v_pk_mul_f32 v[54:55], v[50:51], v[42:43] op_sel:[1,1] op_sel_hi:[1,0] neg_lo:[1,0]\n\tv_pk_fma_f32 v[52:53], v[48:49], v[40:41], v[52:53] op_sel:[0,0,0] op_sel_hi:[0,1,1]\n\t" \
  "v_pk_fma_f32 v[54:55], v[50:51], v[42:43], v[54:55] op_sel:[0,0,0] op_sel_hi:[0,1,1]\n\tv_pk_mul_f32 v[56:57], v[48:49], v[48:49] op_sel:[1,1] op_sel_hi:[1,0] neg_lo:[1,0]\n\t" \
  "v_pk_mul_f32 v[58:59], v[50:51], v[50:51] op_sel:[1,1] op_sel_hi:[1,0] neg_lo:[1,0]\n\tv_pk_fma_f32 v[56:57], v[48:49], v[48:49], v[56:57] op_sel:[0,0,0] op_sel_hi:[0,1,1]\n\t" \
  "v_pk_fma_f32 v[58:59], v[50:51], v[50:51], v[58:59] op_sel:[0,0,0] op_sel_hi:[0,1,1]\n\tv_cvt_pk_f16_f32 v74, v48, -v49\n\t" \
  "v_cvt_pk_f16_f32 v75, v50, -v51\n\tv_cvt_pk_f16_f32 v76, v52, -v53\n\t" \
  "v_cvt_pk_f16_f32 v77, v54, -v55\n\tv_pk_mul_f32 v[52:53], v[56:57], v[48:49] op_sel:[1,1] op_sel_hi:[1,0] neg_lo:[1,0]\n\t" \
  "v_pk_mul_f32 v[54:55], v[58:59], v[50:51] op_sel:[1,1] op_sel_hi:[1,0] neg_lo:[1,0]\n\tv_pk_fma_f32 v[52:53], v[56:57], v[48:49], v[52:53] op_sel:[0,0,0] op_sel_hi:[0,1,1]\n\t" \
  "v_pk_fma_f32 v[54:55], v[58:59], v[50:51], v[54:55] op_sel:[0,0,0] op_sel_hi:[0,1,1]\n\tv_pk_mul_f32 v[60:61], v[56:57], v[56:57] op_sel:[1,1] op_sel_hi:[1,0] neg_lo:[1,0]\n\t" \
  "v_pk_mul_f32 v[62:63], v[58:59], v[58:59] op_sel:[1,1] op_sel_hi:[1,0] neg_lo:[1,0]\n\tv_pk_fma_f32 v[60:61], v[56:57], v[56:57], v[60:61] op_sel:[0,0,0] op_sel_hi:[0,1,1]\n\t" \
  "v_pk_fma_f32 v[62:63], v[58:59], v[58:59], v[62:63] op_sel:[0,0,0] op_sel_hi:[0,1,1]\n\tv_cvt_pk_f16_f32 v78, v56, -v57\n\t" \
  "v_cvt_pk_f16_f32 v79, v58, -v59\n\tv_cvt_pk_f16_f32 v82, v52, -v53\n\t" \
  "v_cvt_pk_f16_f32 v83, v54, -v55\n\tv_pk_mul_f16 v80, v78, v72 op_sel:[1,1] op_sel_hi:[1,0] neg_lo:[1,0]\n\t" \
  "v_pk_mul_f16 v81, v79, v73 op_sel:[1,1] op_sel_hi:[1,0] neg_lo:[1,0]\n\tv_pk_fma_f16 v80, v78, v72, v80 op_sel:[0,0,0] op_sel_hi:[0,1,1]\n\t" \
  "v_pk_fma_f16 v81, v79, v73, v81 op_sel:[0,0,0] op_sel_hi:[0,1,1]\n\tv_pk_mul_f16 v84, v78, v76 op_sel:[1,1] op_sel_hi:[1,0] neg_lo:[1,0]\n\t" \
  "v_pk_mul_f16 v85, v79, v77 op_sel:[1,1] op_sel_hi:[1,0] neg_lo:[1,0]\n\tv_pk_fma_f16 v84, v78, v76, v84 op_sel:[0,0,0] op_sel_hi:[0,1,1]\n\t" \
  "v_pk_fma_f16 v85, v79, v77, v85 op_sel:[0,0,0] op_sel_hi:[0,1,1]\n\tv_cvt_pk_f16_f32 v86, v60, -v61\n\t" \
  "v_cvt_pk_f16_f32 v87, v62, -v63\n\tds_write_addtid_b32 v72 offset:288\n\t" \
  "ds_write_addtid_b32 v73 offset:4896\n\tds_write_addtid_b32 v74 offset:576\n\t" \
  "ds_write_addtid_b32 v75 offset:5184\n\tds_write_addtid_b32 v76 offset:864\n\t" \
  "ds_write_addtid_b32 v77 offset:5472\n\tds_write_addtid_b32 v78 offset:1152\n\t" \
  "ds_write_addtid_b32 v79 offset:5760\n\tds_write_addtid_b32 v80 offset:1440\n\t" \
  "ds_write_addtid_b32 v81 offset:6048\n\tds_write_addtid_b32 v82 offset:1728\n\t" \
  "ds_write_addtid_b32 v83 offset:6336\n\tds_write_addtid_b32 v84 offset:2016\n\t" \
  "ds_write_addtid_b32 v85 offset:6624\n\tds_write_addtid_b32 v86 offset:2304\n\t" \
  "ds_write_addtid_b32 v87 offset:6912\n\tds_read_b128 v[104:107], v70 offset:0\n\t" \
  "ds_read_b128 v[108:111], v70 offset:64\n\tds_read_b128 v[112:115], v70 offset:128\n\t" \
  "ds_read_b128 v[116:119], v70 offset:192\n\tds_read_b128 v[120:123], v70 offset:4608\n\t" \
  "ds_read_b128 v[124:127], v70 offset:4672\n\tds_read_b128 v[128:131], v70 offset:4736\n\t" \
  "ds_read_b128 v[132:135], v70 offset:4800\n\tv_fract_f32 v99, v99\n\t" \
  "v_fract_f32 v101, v101\n\tv_exp_f32 v98, v98\n\t" \
  "v_exp_f32 v100, v100\n\tv_cos_f32 v44, v99\n\t" \
  "v_sin_f32 v45, v99\n\tv_cos_f32 v46, v101\n\t" \
  "v_sin_f32 v47, v101\n\tv_pk_mul_f32 v[44:45], v[44:45], v[98:99] op_sel_hi:[1,0]\n\t" \
  "v_pk_mul_f32 v[46:47], v[46:47], v[100:101] op_sel_hi:[1,0]\n\ts_waitcnt vmcnt(2)\n\t" \
  "v_pk_mul_f32 v[48:49], v[44:45], v[44:45] op_sel:[1,1] op_sel_hi:[1,0] neg_lo:[1,0]\n\tv_pk_mul_f32 v[50:51], v[46:47], v[46:47] op_sel:[1,1] op_sel_hi:[1,0] neg_lo:[1,0]\n\t" \
  "v_pk_fma_f32 v[48:49], v[44:45], v[44:45], v[48:49] op_sel:[0,0,0] op_sel_hi:[0,1,1]\n\tv_pk_fma_f32 v[50:51], v[46:47], v[46:47], v[50:51] op_sel:[0,0,0] op_sel_hi:[0,1,1]\n\t" \
  "v_and_b32 v102, 0x7fff7fff, v68\n\tv_cvt_pk_f16_f32 v88, v44, -v45\n\t" \
  "v_cvt_pk_f16_f32 v89, v46, -v47\n\tv_pk_mul_f32 v[52:53], v[48:49], v[44:45] op_sel:[1,1] op_sel_hi:[1,0] neg_lo:[1,0]\n\t" \
  "v_pk_mul_f32 v[54:55], v[50:51], v[46:47] op_sel:[1,1] op_sel_hi:[1,0] neg_lo:[1,0]\n\tv_and_b32 v103, 0x7fff7fff, v69\n\t" \
  "v_pk_fma_f32 v[52:53], v[48:49], v[44:45], v[52:53] op_sel:[0,0,0] op_sel_hi:[0,1,1]\n\tv_pk_fma_f32 v[54:55], v[50:51], v[46:47], v[54:55] op_sel:[0,0,0] op_sel_hi:[0,1,1]\n\t" \
  "v_pk_mul_f32 v[56:57], v[48:49], v[48:49] op_sel:[1,1] op_sel_hi:[1,0] neg_lo:[1,0]\n\tv_pk_mul_f32 v[58:59], v[50:51], v[50:51] op_sel:[1,1] op_sel_hi:[1,0] neg_lo:[1,0]\n\t" \
  "v_pk_max_u16 v102, v102, v103\n\tv_pk_fma_f32 v[56:57], v[48:49], v[48:49], v[56:57] op_sel:[0,0,0] op_sel_hi:[0,1,1]\n\t" \
  "v_pk_fma_f32 v[58:59], v[50:51], v[50:51], v[58:59] op_sel:[0,0,0] op_sel_hi:[0,1,1]\n\tv_cvt_pk_f16_f32 v90, v48, -v49\n\t" \
  "v_cvt_pk_f16_f32 v91, v50, -v51\n\tv_pk_max_u16 v102, v102, v102 op_sel:[1,0] op_sel_hi:[0,1]\n\t" \
  "v_pk_mul_f32 v[64:65], v[56:57], v[60:61] op_sel:[1,1] op_sel_hi:[1,0] neg_lo:[1,0]\n\tv_pk_mul_f32 v[66:67], v[58:59], v[62:63] op_sel:[1,1] op_sel_hi:[1,0] neg_lo:[1,0]\n\t" \
  "v_pk_fma_f32 v[64:65], v[56:57], v[60:61], v[64:65] op_sel:[0,0,0] op_sel_hi:[0,1,1]\n\tv_pk_fma_f32 v[66:67], v[58:59], v[62:63], v[66:67] op_sel:[0,0,0] op_sel_hi:[0,1,1]\n\t" \
  "v_max_u32_dpp v102, v102, v102 row_shr:1 row_mask:0xf bank_mask:0xf\n\tv_cvt_pk_f16_f32 v92, v52, -v53\n\t" \
  "v_cvt_pk_f16_f32 v93, v54, -v55\n\tv_pk_mul_f16 v136, v88, v74 op_sel:[1,1] op_sel_hi:[1,0] neg_lo:[1,0]\n\t" \
  "v_pk_mul_f16 v137, v89, v75 op_sel:[1,1] op_sel_hi:[1,0] neg_lo:[1,0]\n\tv_max_u32_dpp v102, v102, v102 row_shr:2 row_mask:0xf bank_mask:0xf\n\t" \
  "v_pk_fma_f16 v136, v88, v74, v136 op_sel:[0,0,0] op_sel_hi:[0,1,1]\n\tv_pk_fma_f16 v137, v89, v75, v137 op_sel:[0,0,0] op_sel_hi:[0,1,1]\n\t" \
  "v_pk_mul_f16 v138, v88, v78 op_sel:[1,1] op_sel_hi:[1,0] neg_lo:[1,0]\n\tv_pk_mul_f16 v139, v89, v79 op_sel:[1,1] op_sel_hi:[1,0] neg_lo:[1,0]\n\t" \
  "v_max_u32_dpp v102, v102, v102 row_shr:4 row_mask:0xf bank_mask:0xf\n\tv_pk_fma_f16 v138, v88, v78, v138 op_sel:[0,0,0] op_sel_hi:[0,1,1]\n\t" \
  "v_pk_fma_f16 v139, v89, v79, v139 op_sel:[0,0,0] op_sel_hi:[0,1,1]\n\tv_cvt_pk_f16_f32 v94, v64, -v65\n\t" \
  "v_cvt_pk_f16_f32 v95, v66, -v67\n\tv_max_u32_dpp v102, v102, v102 row_shr:8 row_mask:0xf bank_mask:0xf\n\t" \
  "v_pk_mul_f16 v140, v88, v82 op_sel:[1,1] op_sel_hi:[1,0] neg_lo:[1,0]\n\tv_pk_mul_f16 v141, v89, v83 op_sel:[1,1] op_sel_hi:[1,0] neg_lo:[1,0]\n\t" \
  "v_pk_fma_f16 v140, v88, v82, v140 op_sel:[0,0,0] op_sel_hi:[0,1,1]\n\tv_pk_fma_f16 v141, v89, v83, v141 op_sel:[0,0,0] op_sel_hi:[0,1,1]\n\t" \
  "v_max_u32_dpp v102, v102, v102 row_bcast:15 row_mask:0xa bank_mask:0xf\n\tv_pk_mul_f16 v142, v88, v86 op_sel:[1,1] op_sel_hi:[1,0] neg_lo:[1,0]\n\t" \
  "v_pk_mul_f16 v143, v89, v87 op_sel:[1,1] op_sel_hi:[1,0] neg_lo:[1,0]\n\tv_pk_fma_f16 v142, v88, v86, v142 op_sel:[0,0,0] op_sel_hi:[0,1,1]\n\t" \
  "v_pk_fma_f16 v143, v89, v87, v143 op_sel:[0,0,0] op_sel_hi:[0,1,1]\n\tv_max_u32_dpp v102, v102, v102 row_bcast:31 row_mask:0xc bank_mask:0xf\n\t" \
  "v_pk_mul_f16 v144, v90, v78 op_sel:[1,1] op_sel_hi:[1,0] neg_lo:[1,0]\n\tv_pk_mul_f16 v145, v91, v79 op_sel:[1,1] op_sel_hi:[1,0] neg_lo:[1,0]\n\t" \
  "v_pk_fma_f16 v144, v90, v78, v144 op_sel:[0,0,0] op_sel_hi:[0,1,1]\n\tv_pk_fma_f16 v145, v91, v79, v145 op_sel:[0,0,0] op_sel_hi:[0,1,1]\n\t" \
  "v_readlane_b32 %[e], v102, 63\n\tv_pk_mul_f16 v146, v90, v86 op_sel:[1,1] op_sel_hi:[1,0] neg_lo:[1,0]\n\t" \
  "v_pk_mul_f16 v147, v91, v87 op_sel:[1,1] op_sel_hi:[1,0] neg_lo:[1,0]\n\tv_pk_fma_f16 v146, v90, v86, v146 op_sel:[0,0,0] op_sel_hi:[0,1,1]\n\t" \
  "v_pk_fma_f16 v147, v91, v87, v147 op_sel:[0,0,0] op_sel_hi:[0,1,1]\n\tv_pk_mul_f16 v148, v92, v78 op_sel:[1,1] op_sel_hi:[1,0] neg_lo:[1,0]\n\t" \
  "v_pk_mul_f16 v149, v93, v79 op_sel:[1,1] op_sel_hi:[1,0] neg_lo:[1,0]\n\tv_pk_fma_f16 v148, v92, v78, v148 op_sel:[0,0,0] op_sel_hi:[0,1,1]\n\t" \
  "v_pk_fma_f16 v149, v93, v79, v149 op_sel:[0,0,0] op_sel_hi:[0,1,1]\n\tv_pk_mul_f16 v150, v92, v86 op_sel:[1,1] op_sel_hi:[1,0] neg_lo:[1,0]\n\t" \
  "v_pk_mul_f16 v151, v93, v87 op_sel:[1,1] op_sel_hi:[1,0] neg_lo:[1,0]\n\tv_pk_fma_f16 v150, v92, v86, v150 op_sel:[0,0,0] op_sel_hi:[0,1,1]\n\t" \
  "v_pk_fma_f16 v151, v93, v87, v151 op_sel:[0,0,0] op_sel_hi:[0,1,1]\n\tv_pk_mul_f16 v96, v94, v88 op_sel:[1,1] op_sel_hi:[1,0] neg_lo:[1,0]\n\t" \
  "v_pk_mul_f16 v97, v95, v89 op_sel:[1,1] op_sel_hi:[1,0] neg_lo:[1,0]\n\tv_pk_fma_f16 v96, v94, v88, v96 op_sel:[0,0,0] op_sel_hi:[0,1,1]\n\t" \
  "v_pk_fma_f16 v97, v95, v89, v97 op_sel:[0,0,0] op_sel_hi:[0,1,1]\n\tv_pk_mul_f16 v98, v94, v90 op_sel:[1,1] op_sel_hi:[1,0] neg_lo:[1,0]\n\t" \
  "v_pk_mul_f16 v99, v95, v91 op_sel:[1,1] op_sel_hi:[1,0] neg_lo:[1,0]\n\tv_pk_fma_f16 v98, v94, v90, v98 op_sel:[0,0,0] op_sel_hi:[0,1,1]\n\t" \
  "v_pk_fma_f16 v99, v95, v91, v99 op_sel:[0,0,0] op_sel_hi:[0,1,1]\n\tv_pk_mul_f16 v100, v94, v92 op_sel:[1,1] op_sel_hi:[1,0] neg_lo:[1,0]\n\t" \
  "v_pk_mul_f16 v101, v95, v93 op_sel:[1,1] op_sel_hi:[1,0] neg_lo:[1,0]\n\tv_pk_fma_f16 v100, v94, v92, v100 op_sel:[0,0,0] op_sel_hi:[0,1,1]\n\t" \
  "v_pk_fma_f16 v101, v95, v93, v101 op_sel:[0,0,0] op_sel_hi:[0,1,1]\n\ts_bfe_u32 %[e], %[e], 0x80007\n\t" \
  "s_max_u32 %[e], %[e], 8\n\ts_sub_u32 %[t], 260, %[e]\n\t" \
  "s_lshl_b32 %[t], %[t], 23\n\tds_write_addtid_b32 v136 offset:0\n\t" \
  "ds_write_addtid_b32 v137 offset:4608\n\tds_write_addtid_b32 v138 offset:288\n\t" \
  "ds_write_addtid_b32 v139 offset:4896\n\tds_write_addtid_b32 v140 offset:576\n\t" \
  "ds_write_addtid_b32 v141 offset:5184\n\tds_write_addtid_b32 v142 offset:864\n\t" \
  "ds_write_addtid_b32 v143 offset:5472\n\tds_write_addtid_b32 v144 offset:1152\n\t" \
  "ds_write_addtid_b32 v145 offset:5760\n\tds_write_addtid_b32 v146 offset:1440\n\t" \
  "ds_write_addtid_b32 v147 offset:6048\n\tds_write_addtid_b32 v148 offset:1728\n\t" \
  "ds_write_addtid_b32 v149 offset:6336\n\tds_write_addtid_b32 v150 offset:2016\n\t" \
  "ds_write_addtid_b32 v151 offset:6624\n\tds_write_addtid_b32 v94 offset:2304\n\t" \
  "ds_write_addtid_b32 v95 offset:6912\n\tds_write_addtid_b32 v96 offset:2592\n\t" \
  "ds_write_addtid_b32 v97 offset:7200\n\tds_write_addtid_b32 v98 offset:2880\n\t" \
  "ds_write_addtid_b32 v99 offset:7488\n\tds_write_addtid_b32 v100 offset:3168\n\t" \
  "ds_write_addtid_b32 v101 offset:7776\n\tds_read_b128 v[136:139], v71 offset:0\n\t" \
  "ds_read_b128 v[140:143], v71 offset:64\n\tds_read_b128 v[144:147], v71 offset:128\n\t" \
  "ds_read_b128 v[148:151], v71 offset:192\n\tds_read_b128 v[152:155], v71 offset:4608\n\t" \
  "ds_read_b128 v[156:159], v71 offset:4672\n\tds_read_b128 v[160:163], v71 offset:4736\n\t" \
  "ds_read_b128 v[164:167], v71 offset:4800\n\tv_pk_mul_f32 v[48:49], v[60:61], v[60:61] op_sel:[1,1] op_sel_hi:[1,0] neg_lo:[1,0]\n\t" \
  "v_pk_mul_f32 v[50:51], v[62:63], v[62:63] op_sel:[1,1] op_sel_hi:[1,0] neg_lo:[1,0]\n\tv_pk_fma_f32 v[48:49], v[60:61], v[60:61], v[48:49] op_sel:[0,0,0] op_sel_hi:[0,1,1]\n\t" \
  "v_pk_fma_f32 v[50:51], v[62:63], v[62:63], v[50:51] op_sel:[0,0,0] op_sel_hi:[0,1,1]\n\tv_cvt_pk_f16_f32 v44, v60, v61\n\t" \
  "v_cvt_pk_f16_f32 v45, v62, v63\n\tv_pk_mul_f32 v[52:53], v[48:49], v[60:61] op_sel:[1,1] op_sel_hi:[1,0] neg_lo:[1,0]\n\t" \
  "v_pk_mul_f32 v[54:55], v[50:51], v[62:63] op_sel:[1,1] op_sel_hi:[1,0] neg_lo:[1,0]\n\tv_pk_fma_f32 v[52:53], v[48:49], v[60:61], v[52:53] op_sel:[0,0,0] op_sel_hi:[0,1,1]\n\t" \
  "v_pk_fma_f32 v[54:55], v[50:51], v[62:63], v[54:55] op_sel:[0,0,0] op_sel_hi:[0,1,1]\n\tv_pk_mul_f32 v[40:41], v[48:49], v[48:49] op_sel:[1,1] op_sel_hi:[1,0] neg_lo:[1,0]\n\t" \
  "v_pk_mul_f32 v[42:43], v[50:51], v[50:51] op_sel:[1,1] op_sel_hi:[1,0] neg_lo:[1,0]\n\tv_pk_fma_f32 v[40:41], v[48:49], v[48:49], v[40:41] op_sel:[0,0,0] op_sel_hi:[0,1,1]\n\t" \
  "v_pk_fma_f32 v[42:43], v[50:51], v[50:51], v[42:43] op_sel:[0,0,0] op_sel_hi:[0,1,1]\n\tv_cvt_pk_f16_f32 v46, v48, v49\n\t" \
  "v_cvt_pk_f16_f32 v47, v50, v51\n\tv_cvt_pk_f16_f32 v56, v52, v53\n\t" \
  "v_cvt_pk_f16_f32 v57, v54, v55\n\tv_lshlrev_b32 v64, 16, v68\n\t" \
  "v_lshlrev_b32 v65, 16, v69\n\tv_and_b32 v66, 0xffff0000, v68\n\t" \
  "v_and_b32 v67, 0xffff0000, v69\n\tv_mul_f32 v64, %[t], v64\n\t" \
  "v_mul_f32 v65, %[t], v65\n\tv_mul_f32 v66, %[t], v66\n\t" \
  "v_mul_f32 v67, %[t], v67\n\tv_cvt_pk_f16_f32 v48, v64, v65\n\t" \
  "v_cvt_pk_f16_f32 v49, v66, v67\n\tv_pk_mul_f32 v[68:69], v[64:65], v[40:41] op_sel:[1,1] op_sel_hi:[1,0] neg_lo:[1,0]\n\t" \
  "v_pk_mul_f32 v[70:71], v[66:67], v[42:43] op_sel:[1,1] op_sel_hi:[1,0] neg_lo:[1,0]\n\tv_pk_fma_f32 v[68:69], v[64:65], v[40:41], v[68:69] op_sel:[0,0,0] op_sel_hi:[0,1,1]\n\t" \
  "v_pk_fma_f32 v[70:71], v[66:67], v[42:43], v[70:71] op_sel:[0,0,0] op_sel_hi:[0,1,1]\n\tv_pk_mul_f16 v50, v48, v44 op_sel:[1,1] op_sel_hi:[1,0] neg_lo:[1,0]\n\t" \
  "v_pk_mul_f16 v51, v49, v45 op_sel:[1,1] op_sel_hi:[1,0] neg_lo:[1,0]\n\tv_pk_fma_f16 v50, v48, v44, v50 op_sel:[0,0,0] op_sel_hi:[0,1,1]\n\t" \
  "v_pk_fma_f16 v51, v49, v45, v51 op_sel:[0,0,0] op_sel_hi:[0,1,1]\n\tv_pk_mul_f16 v52, v48, v46 op_sel:[1,1] op_sel_hi:[1,0] neg_lo:[1,0]\n\t" \
  "v_pk_mul_f16 v53, v49, v47 op_sel:[1,1] op_sel_hi:[1,0] neg_lo:[1,0]\n\tv_pk_fma_f16 v52, v48, v46, v52 op_sel:[0,0,0] op_sel_hi:[0,1,1]\n\t" \
  "v_pk_fma_f16 v53, v49, v47, v53 op_sel:[0,0,0] op_sel_hi:[0,1,1]\n\tv_pk_mul_f16 v54, v48, v56 op_sel:[1,1] op_sel_hi:[1,0] neg_lo:[1,0]\n\t" \
  "v_pk_mul_f16 v55, v49, v57 op_sel:[1,1] op_sel_hi:[1,0] neg_lo:[1,0]\n\tv_pk_fma_f16 v54, v48, v56, v54 op_sel:[0,0,0] op_sel_hi:[0,1,1]\n\t" \
  "v_pk_fma_f16 v55, v49, v57, v55 op_sel:[0,0,0] op_sel_hi:[0,1,1]\n\tds_write_addtid_b32 v48 offset:0\n\t" \
  "ds_write_addtid_b32 v49 offset:4608\n\tds_write_addtid_b32 v50 offset:288\n\t" \
  "ds_write_addtid_b32 v51 offset:4896\n\tds_write_addtid_b32 v52 offset:576\n\t" \
  "ds_write_addtid_b32 v53 offset:5184\n\tds_write_addtid_b32 v54 offset:864\n\t" \
  "ds_write_addtid_b32 v55 offset:5472\n\tv_cvt_pk_f16_f32 v48, v68, v69\n\t" \
  "v_cvt_pk_f16_f32 v49, v70, v71\n\tv_pk_mul_f32 v[64:65], v[68:69], v[40:41] op_sel:[1,1] op_sel_hi:[1,0] neg_lo:[1,0]\n\t" \
  "v_pk_mul_f32 v[66:67], v[70:71], v[42:43] op_sel:[1,1] op_sel_hi:[1,0] neg_lo:[1,0]\n\tv_pk_fma_f32 v[64:65], v[68:69], v[40:41], v[64:65] op_sel:[0,0,0] op_sel_hi:[0,1,1]\n\t" \
  "v_pk_fma_f32 v[66:67], v[70:71], v[42:43], v[66:67] op_sel:[0,0,0] op_sel_hi:[0,1,1]\n\tv_pk_mul_f16 v50, v48, v44 op_sel:[1,1] op_sel_hi:[1,0] neg_lo:[1,0]\n\t" \
  "v_pk_mul_f16 v51, v49, v45 op_sel:[1,1] op_sel_hi:[1,0] neg_lo:[1,0]\n\tv_pk_fma_f16 v50, v48, v44, v50 op_sel:[0,0,0] op_sel_hi:[0,1,1]\n\t" \
  "v_pk_fma_f16 v51, v49, v45, v51 op_sel:[0,0,0] op_sel_hi:[0,1,1]\n\tv_pk_mul_f16 v52, v48, v46 op_sel:[1,1] op_sel_hi:[1,0] neg_lo:[1,0]\n\t" \
  "v_pk_mul_f16 v53, v49, v47 op_sel:[1,1] op_sel_hi:[1,0] neg_lo:[1,0]\n\tv_pk_fma_f16 v52, v48, v46, v52 op_sel:[0,0,0] op_sel_hi:[0,1,1]\n\t" \
  "v_pk_fma_f16 v53, v49, v47, v53 op_sel:[0,0,0] op_sel_hi:[0,1,1]\n\tv_pk_mul_f16 v54, v48, v56 op_sel:[1,1] op_sel_hi:[1,0] neg_lo:[1,0]\n\t" \
  "v_pk_mul_f16 v55, v49, v57 op_sel:[1,1] op_sel_hi:[1,0] neg_lo:[1,0]\n\tv_pk_fma_f16 v54, v48, v56, v54 op_sel:[0,0,0] op_sel_hi:[0,1,1]\n\t" \
  "v_pk_fma_f16 v55, v49, v57, v55 op_sel:[0,0,0] op_sel_hi:[0,1,1]\n\tds_write_addtid_b32 v48 offset:1152\n\t" \
  "ds_write_addtid_b32 v49 offset:5760\n\tds_write_addtid_b32 v50 offset:1440\n\t" \
  "ds_write_addtid_b32 v51 offset:6048\n\tds_write_addtid_b32 v52 offset:1728\n\t" \
  "ds_write_addtid_b32 v53 offset:6336\n\tds_write_addtid_b32 v54 offset:2016\n\t" \
  "ds_write_addtid_b32 v55 offset:6624\n\tv_cvt_pk_f16_f32 v48, v64, v65\n\t" \
  "v_cvt_pk_f16_f32 v49, v66, v67\n\tv_pk_mul_f32 v[68:69], v[64:65], v[40:41] op_sel:[1,1] op_sel_hi:[1,0] neg_lo:[1,0]\n\t" \
  "v_pk_mul_f32 v[70:71], v[66:67], v[42:43] op_sel:[1,1] op_sel_hi:[1,0] neg_lo:[1,0]\n\tv_pk_fma_f32 v[68:69], v[64:65], v[40:41], v[68:69] op_sel:[0,0,0] op_sel_hi:[0,1,1]\n\t" \
  "v_pk_fma_f32 v[70:71], v[66:67], v[42:43], v[70:71] op_sel:[0,0,0] op_sel_hi:[0,1,1]\n\tv_pk_mul_f16 v50, v48, v44 op_sel:[1,1] op_sel_hi:[1,0] neg_lo:[1,0]\n\t" \
  "v_pk_mul_f16 v51, v49, v45 op_sel:[1,1] op_sel_hi:[1,0] neg_lo:[1,0]\n\tv_pk_fma_f16 v50, v48, v44, v50 op_sel:[0,0,0] op_sel_hi:[0,1,1]\n\t" \
  "v_pk_fma_f16 v51, v49, v45, v51 op_sel:[0,0,0] op_sel_hi:[0,1,1]\n\tv_pk_mul_f16 v52, v48, v46 op_sel:[1,1] op_sel_hi:[1,0] neg_lo:[1,0]\n\t" \
  "v_pk_mul_f16 v53, v49, v47 op_sel:[1,1] op_sel_hi:[1,0] neg_lo:[1,0]\n\tv_pk_fma_f16 v52, v48, v46, v52 op_sel:[0,0,0] op_sel_hi:[0,1,1]\n\t" \
  "v_pk_fma_f16 v53, v49, v47, v53 op_sel:[0,0,0] op_sel_hi:[0,1,1]\n\tv_pk_mul_f16 v54, v48, v56 op_sel:[1,1] op_sel_hi:[1,0] neg_lo:[1,0]\n\t" \
  "v_pk_mul_f16 v55, v49, v57 op_sel:[1,1] op_sel_hi:[1,0] neg_lo:[1,0]\n\tv_pk_fma_f16 v54, v48, v56, v54 op_sel:[0,0,0] op_sel_hi:[0,1,1]\n\t" \
  "v_pk_fma_f16 v55, v49, v57, v55 op_sel:[0,0,0] op_sel_hi:[0,1,1]\n\tds_write_addtid_b32 v48 offset:2304\n\t" \
  "ds_write_addtid_b32 v49 offset:6912\n\tds_write_addtid_b32 v50 offset:2592\n\t" \
  "ds_write_addtid_b32 v51 offset:7200\n\tds_write_addtid_b32 v52 offset:2880\n\t" \
  "ds_write_addtid_b32 v53 offset:7488\n\tds_write_addtid_b32 v54 offset:3168\n\t" \
  "ds_write_addtid_b32 v55 offset:7776\n\tv_cvt_pk_f16_f32 v48, v68, v69\n\t" \
  "v_cvt_pk_f16_f32 v49, v70, v71\n\tv_pk_mul_f32 v[64:65], v[68:69], v[40:41] op_sel:[1,1] op_sel_hi:[1,0] neg_lo:[1,0]\n\t" \
  "v_pk_mul_f32 v[66:67], v[70:71], v[42:43] op_sel:[1,1] op_sel_hi:[1,0] neg_lo:[1,0]\n\tv_pk_fma_f32 v[64:65], v[68:69], v[40:41], v[64:65] op_sel:[0,0,0] op_sel_hi:[0,1,1]\n\t" \
  "v_pk_fma_f32 v[66:67], v[70:71], v[42:43], v[66:67] op_sel:[0,0,0] op_sel_hi:[0,1,1]\n\tv_pk_mul_f16 v50, v48, v44 op_sel:[1,1] op_sel_hi:[1,0] neg_lo:[1,0]\n\t" \
  "v_pk_mul_f16 v51, v49, v45 op_sel:[1,1] op_sel_hi:[1,0] neg_lo:[1,0]\n\tv_pk_fma_f16 v50, v48, v44, v50 op_sel:[0,0,0] op_sel_hi:[0,1,1]\n\t" \
  "v_pk_fma_f16 v51, v49, v45, v51 op_sel:[0,0,0] op_sel_hi:[0,1,1]\n\tv_pk_mul_f16 v52, v48, v46 op_sel:[1,1] op_sel_hi:[1,0] neg_lo:[1,0]\n\t" \
  "v_pk_mul_f16 v53, v49, v47 op_sel:[1,1] op_sel_hi:[1,0] neg_lo:[1,0]\n\tv_pk_fma_f16 v52, v48, v46, v52 op_sel:[0,0,0] op_sel_hi:[0,1,1]\n\t" \
  "v_pk_fma_f16 v53, v49, v47, v53 op_sel:[0,0,0] op_sel_hi:[0,1,1]\n\tv_pk_mul_f16 v54, v48, v56 op_sel:[1,1] op_sel_hi:[1,0] neg_lo:[1,0]\n\t" \
  "v_pk_mul_f16 v55, v49, v57 op_sel:[1,1] op_sel_hi:[1,0] neg_lo:[1,0]\n\tv_pk_fma_f16 v54, v48, v56, v54 op_sel:[0,0,0] op_sel_hi:[0,1,1]\n\t" \
  "v_pk_fma_f16 v55, v49, v57, v55 op_sel:[0,0,0] op_sel_hi:[0,1,1]\n\tds_write_addtid_b32 v48 offset:3456\n\t" \
  "ds_write_addtid_b32 v49 offset:8064\n\tds_write_addtid_b32 v50 offset:3744\n\t" \
  "ds_write_addtid_b32 v51 offset:8352\n\tds_write_addtid_b32 v52 offset:4032\n\t" \
  "ds_write_addtid_b32 v53 offset:8640\n\tds_write_addtid_b32 v54 offset:4320\n\t" \
  "ds_write_addtid_b32 v55 offset:8928\n\tds_read_b128 v[72:75], v27 offset:0\n\t" \
  "ds_read_b128 v[76:79], v27 offset:64\n\tds_read_b128 v[80:83], v27 offset:128\n\t" \
  "ds_read_b128 v[84:87], v27 offset:192\n\tds_read_b128 v[88:91], v27 offset:4608\n\t" \
  "ds_read_b128 v[92:95], v27 offset:4672\n\tds_read_b128 v[96:99], v27 offset:4736\n\t" \
  "ds_read_b128 v[100:103], v27 offset:4800\n\tv_cvt_pk_f16_f32 v48, v64, v65\n\t" \
  "v_cvt_pk_f16_f32 v49, v66, v67\n\tv_pk_mul_f32 v[68:69], v[64:65], v[40:41] op_sel:[1,1] op_sel_hi:[1,0] neg_lo:[1,0]\n\t" \
  "v_pk_mul_f32 v[70:71], v[66:67], v[42:43] op_sel:[1,1] op_sel_hi:[1,0] neg_lo:[1,0]\n\tv_pk_fma_f32 v[68:69], v[64:65], v[40:41], v[68:69] op_sel:[0,0,0] op_sel_hi:[0,1,1]\n\t" \
  "v_pk_fma_f32 v[70:71], v[66:67], v[42:43], v[70:71] op_sel:[0,0,0] op_sel_hi:[0,1,1]\n\tv_pk_mul_f16 v50, v48, v44 op_sel:[1,1] op_sel_hi:[1,0] neg_lo:[1,0]\n\t" \
  "v_pk_mul_f16 v51, v49, v45 op_sel:[1,1] op_sel_hi:[1,0] neg_lo:[1,0]\n\tv_pk_fma_f16 v50, v48, v44, v50 op_sel:[0,0,0] op_sel_hi:[0,1,1]\n\t" \
  "v_pk_fma_f16 v51, v49, v45, v51 op_sel:[0,0,0] op_sel_hi:[0,1,1]\n\tv_pk_mul_f16 v52, v48, v46 op_sel:[1,1] op_sel_hi:[1,0] neg_lo:[1,0]\n\t" \
  "v_pk_mul_f16 v53, v49, v47 op_sel:[1,1] op_sel_hi:[1,0] neg_lo:[1,0]\n\tv_pk_fma_f16 v52, v48, v46, v52 op_sel:[0,0,0] op_sel_hi:[0,1,1]\n\t" \
  "v_pk_fma_f16 v53, v49, v47, v53 op_sel:[0,0,0] op_sel_hi:[0,1,1]\n\tv_pk_mul_f16 v54, v48, v56 op_sel:[1,1] op_sel_hi:[1,0] neg_lo:[1,0]\n\t" \
  "v_pk_mul_f16 v55, v49, v57 op_sel:[1,1] op_sel_hi:[1,0] neg_lo:[1,0]\n\tv_pk_fma_f16 v54, v48, v56, v54 op_sel:[0,0,0] op_sel_hi:[0,1,1]\n\t" \
  "v_pk_fma_f16 v55, v49, v57, v55 op_sel:[0,0,0] op_sel_hi:[0,1,1]\n\ts_waitcnt lgkmcnt(0)\n\t" \
  "v_mfma_f32_16x16x32_f16 v[60:63], v[104:107], v[72:75], 0\n\tv_mfma_f32_16x16x32_f16 v[36:39], v[72:75], v[136:139], 0\n\t" \
  "ds_write_addtid_b32 v48 offset:0\n\tds_write_addtid_b32 v49 offset:4608\n\t" \
  "ds_write_addtid_b32 v50 offset:288\n\tds_write_addtid_b32 v51 offset:4896\n\t" \
  "v_mfma_f32_16x16x32_f16 v[60:63], v[108:111], v[76:79], v[60:63]\n\tds_write_addtid_b32 v52 offset:576\n\t" \
  "ds_write_addtid_b32 v53 offset:5184\n\tds_write_addtid_b32 v54 offset:864\n\t" \
  "ds_write_addtid_b32 v55 offset:5472\n\tv_mfma_f32_16x16x32_f16 v[36:39], v[76:79], v[140:143], v[36:39]\n\t" \
  "v_cvt_pk_f16_f32 v48, v68, v69\n\tv_cvt_pk_f16_f32 v49, v70, v71\n\t" \
  "v_pk_mul_f32 v[64:65], v[68:69], v[40:41] op_sel:[1,1] op_sel_hi:[1,0] neg_lo:[1,0]\n\tv_pk_mul_f32 v[66:67], v[70:71], v[42:43] op_sel:[1,1] op_sel_hi:[1,0] neg_lo:[1,0]\n\t" \
  "v_pk_fma_f32 v[64:65], v[68:69], v[40:41], v[64:65] op_sel:[0,0,0] op_sel_hi:[0,1,1]\n\tv_pk_fma_f32 v[66:67], v[70:71], v[42:43], v[66:67] op_sel:[0,0,0] op_sel_hi:[0,1,1]\n\t" \
  "v_pk_mul_f16 v50, v48, v44 op_sel:[1,1] op_sel_hi:[1,0] neg_lo:[1,0]\n\tv_pk_mul_f16 v51, v49, v45 op_sel:[1,1] op_sel_hi:[1,0] neg_lo:[1,0]\n\t" \
  "v_mfma_f32_16x16x32_f16 v[60:63], v[112:115], v[80:83], v[60:63]\n\tv_pk_fma_f16 v50, v48, v44, v50 op_sel:[0,0,0] op_sel_hi:[0,1,1]\n\t" \
  "v_pk_fma_f16 v51, v49, v45, v51 op_sel:[0,0,0] op_sel_hi:[0,1,1]\n\tv_pk_mul_f16 v52, v48, v46 op_sel:[1,1] op_sel_hi:[1,0] neg_lo:[1,0]\n\t" \
  "v_pk_mul_f16 v53, v49, v47 op_sel:[1,1] op_sel_hi:[1,0] neg_lo:[1,0]\n\tv_pk_fma_f16 v52, v48, v46, v52 op_sel:[0,0,0] op_sel_hi:[0,1,1]\n\t" \
  "v_pk_fma_f16 v53, v49, v47, v53 op_sel:[0,0,0] op_sel_hi:[0,1,1]\n\tv_pk_mul_f16 v54, v48, v56 op_sel:[1,1] op_sel_hi:[1,0] neg_lo:[1,0]\n\t" \
  "v_pk_mul_f16 v55, v49, v57 op_sel:[1,1] op_sel_hi:[1,0] neg_lo:[1,0]\n\tv_pk_fma_f16 v54, v48, v56, v54 op_sel:[0,0,0] op_sel_hi:[0,1,1]\n\t" \
  "v_pk_fma_f16 v55, v49, v57, v55 op_sel:[0,0,0] op_sel_hi:[0,1,1]\n\tv_mfma_f32_16x16x32_f16 v[36:39], v[80:83], v[144:147], v[36:39]\n\t" \
  "ds_write_addtid_b32 v48 offset:1152\n\tds_write_addtid_b32 v49 offset:5760\n\t" \
  "ds_write_addtid_b32 v50 offset:1440\n\tds_write_addtid_b32 v51 offset:6048\n\t" \
  "v_mfma_f32_16x16x32_f16 v[60:63], v[116:119], v[84:87], v[60:63]\n\tds_write_addtid_b32 v52 offset:1728\n\t" \
  "ds_write_addtid_b32 v53 offset:6336\n\tds_write_addtid_b32 v54 offset:2016\n\t" \
  "ds_write_addtid_b32 v55 offset:6624\n\tv_mfma_f32_16x16x32_f16 v[36:39], v[84:87], v[148:151], v[36:39]\n\t" \
  "v_cvt_pk_f16_f32 v48, v64, v65\n\tv_cvt_pk_f16_f32 v49, v66, v67\n\t" \
  "v_pk_mul_f32 v[68:69], v[64:65], v[40:41] op_sel:[1,1] op_sel_hi:[1,0] neg_lo:[1,0]\n\tv_pk_mul_f32 v[70:71], v[66:67], v[42:43] op_sel:[1,1] op_sel_hi:[1,0] neg_lo:[1,0]\n\t" \
  "v_pk_fma_f32 v[68:69], v[64:65], v[40:41], v[68:69] op_sel:[0,0,0] op_sel_hi:[0,1,1]\n\tv_pk_fma_f32 v[70:71], v[66:67], v[42:43], v[70:71] op_sel:[0,0,0] op_sel_hi:[0,1,1]\n\t" \
  "v_pk_mul_f16 v50, v48, v44 op_sel:[1,1] op_sel_hi:[1,0] neg_lo:[1,0]\n\tv_pk_mul_f16 v51, v49, v45 op_sel:[1,1] op_sel_hi:[1,0] neg_lo:[1,0]\n\t" \
  "v_mfma_f32_16x16x32_f16 v[60:63], v[120:123], v[88:91], v[60:63]\n\tv_pk_fma_f16 v50, v48, v44, v50 op_sel:[0,0,0] op_sel_hi:[0,1,1]\n\t" \
  "v_pk_fma_f16 v51, v49, v45, v51 op_sel:[0,0,0] op_sel_hi:[0,1,1]\n\tv_pk_mul_f16 v52, v48, v46 op_sel:[1,1] op_sel_hi:[1,0] neg_lo:[1,0]\n\t" \
  "v_pk_mul_f16 v53, v49, v47 op_sel:[1,1] op_sel_hi:[1,0] neg_lo:[1,0]\n\tv_pk_fma_f16 v52, v48, v46, v52 op_sel:[0,0,0] op_sel_hi:[0,1,1]\n\t" \
  "v_pk_fma_f16 v53, v49, v47, v53 op_sel:[0,0,0] op_sel_hi:[0,1,1]\n\tv_pk_mul_f16 v54, v48, v56 op_sel:[1,1] op_sel_hi:[1,0] neg_lo:[1,0]\n\t" \
  "v_pk_mul_f16 v55, v49, v57 op_sel:[1,1] op_sel_hi:[1,0] neg_lo:[1,0]\n\tv_pk_fma_f16 v54, v48, v56, v54 op_sel:[0,0,0] op_sel_hi:[0,1,1]\n\t" \
  "v_pk_fma_f16 v55, v49, v57, v55 op_sel:[0,0,0] op_sel_hi:[0,1,1]\n\tv_mfma_f32_16x16x32_f16 v[36:39], v[88:91], v[152:155], v[36:39]\n\t" \
  "ds_write_addtid_b32 v48 offset:2304\n\tds_write_addtid_b32 v49 offset:6912\n\t" \
  "ds_write_addtid_b32 v50 offset:2592\n\tds_write_addtid_b32 v51 offset:7200\n\t" \
  "v_mfma_f32_16x16x32_f16 v[60:63], v[124:127], v[92:95], v[60:63]\n\tds_write_addtid_b32 v52 offset:2880\n\t" \
  "ds_write_addtid_b32 v53 offset:7488\n\tds_write_addtid_b32 v54 offset:3168\n\t" \
  "ds_write_addtid_b32 v55 offset:7776\n\tv_mfma_f32_16x16x32_f16 v[36:39], v[92:95], v[156:159], v[36:39]\n\t" \
  "v_cvt_pk_f16_f32 v48, v68, v69\n\tv_cvt_pk_f16_f32 v49, v70, v71\n\t" \
  "v_pk_mul_f16 v50, v48, v44 op_sel:[1,1] op_sel_hi:[1,0] neg_lo:[1,0]\n\tv_pk_mul_f16 v51, v49, v45 op_sel:[1,1] op_sel_hi:[1,0] neg_lo:[1,0]\n\t" \
  "v_pk_fma_f16 v50, v48, v44, v50 op_sel:[0,0,0] op_sel_hi:[0,1,1]\n\tv_pk_fma_f16 v51, v49, v45, v51 op_sel:[0,0,0] op_sel_hi:[0,1,1]\n\t" \
  "v_pk_mul_f16 v52, v48, v46 op_sel:[1,1] op_sel_hi:[1,0] neg_lo:[1,0]\n\tv_pk_mul_f16 v53, v49, v47 op_sel:[1,1] op_sel_hi:[1,0] neg_lo:[1,0]\n\t" \
  "v_mfma_f32_16x16x32_f16 v[60:63], v[128:131], v[96:99], v[60:63]\n\tv_pk_fma_f16 v52, v48, v46, v52 op_sel:[0,0,0] op_sel_hi:[0,1,1]\n\t" \
  "v_pk_fma_f16 v53, v49, v47, v53 op_sel:[0,0,0] op_sel_hi:[0,1,1]\n\tv_pk_mul_f16 v54, v48, v56 op_sel:[1,1] op_sel_hi:[1,0] neg_lo:[1,0]\n\t" \
  "v_pk_mul_f16 v55, v49, v57 op_sel:[1,1] op_sel_hi:[1,0] neg_lo:[1,0]\n\tv_pk_fma_f16 v54, v48, v56, v54 op_sel:[0,0,0] op_sel_hi:[0,1,1]\n\t" \
  "v_pk_fma_f16 v55, v49, v57, v55 op_sel:[0,0,0] op_sel_hi:[0,1,1]\n\tv_mfma_f32_16x16x32_f16 v[36:39], v[96:99], v[160:163], v[36:39]\n\t" \
  "ds_write_addtid_b32 v48 offset:3456\n\tds_write_addtid_b32 v49 offset:8064\n\t" \
  "ds_write_addtid_b32 v50 offset:3744\n\tds_write_addtid_b32 v51 offset:8352\n\t" \
  "v_mfma_f32_16x16x32_f16 v[60:63], v[132:135], v[100:103], v[60:63]\n\tds_write_addtid_b32 v52 offset:4032\n\t" \
  "ds_write_addtid_b32 v53 offset:8640\n\tds_write_addtid_b32 v54 offset:4320\n\t" \
  "ds_write_addtid_b32 v55 offset:8928\n\tv_mfma_f32_16x16x32_f16 v[36:39], v[100:103], v[164:167], v[36:39]\n\t" \
  "ds_read_b128 v[72:75], v27 offset:0\n\tds_read_b128 v[76:79], v27 offset:64\n\t" \
  "ds_read_b128 v[80:83], v27 offset:128\n\tds_read_b128 v[84:87], v27 offset:192\n\t" \
  "ds_read_b128 v[88:91], v27 offset:4608\n\tds_read_b128 v[92:95], v27 offset:4672\n\t" \
  "ds_read_b128 v[96:99], v27 offset:4736\n\tds_read_b128 v[100:103], v27 offset:4800\n\t" \
  "s_waitcnt vmcnt(0)\n\tv_add_u32_sdwa v56, %[w], v16 dst_sel:DWORD dst_unused:UNUSED_PAD src0_sel:DWORD src1_sel:WORD_0\n\t" \
  "v_add_u32_sdwa v57, %[w], v16 dst_sel:DWORD dst_unused:UNUSED_PAD src0_sel:DWORD src1_sel:WORD_1\n\tv_add_u32_sdwa v58, %[w], v17 dst_sel:DWORD dst_unused:UNUSED_PAD src0_sel:DWORD src1_sel:WORD_0\n\t" \
  "v_add_u32_sdwa v59, %[w], v17 dst_sel:DWORD dst_unused:UNUSED_PAD src0_sel:DWORD src1_sel:WORD_1\n\tv_mov_b32 v52, 0\n\t" \
  "v_mov_b32 v53, 0\n\tv_mov_b32 v54, 0\n\t" \
  "v_mov_b32 v55, 0\n\tv_cvt_pk_f16_f32 v42, v36, v37\n\t" \
  "v_cvt_pk_f16_f32 v43, v38, v39\n\tv_cvt_pk_f16_f32 v40, v60, v61\n\t" \
  "v_cvt_pk_f16_f32 v41, v62, v63\n\tv_alignbit_b32 v44, v43, v42, 16\n\t" \
  "s_waitcnt lgkmcnt(7)\n\tv_mfma_f32_16x16x32_f16 v[64:67], v[104:107], v[72:75], 0\n\t" \
  "v_mfma_f32_16x16x32_f16 v[68:71], v[72:75], v[136:139], 0\n\ts_waitcnt lgkmcnt(6)\n\t" \
  "v_mfma_f32_16x16x32_f16 v[64:67], v[108:111], v[76:79], v[64:67]\n\tds_write_b128 v58, v[52:55]\n\t" \
  "v_mfma_f32_16x16x32_f16 v[68:71], v[76:79], v[140:143], v[68:71]\n\tds_write2_b32 v56, v40, v41 offset0:64 offset1:65\n\t" \
  "s_waitcnt lgkmcnt(7)\n\tv_mfma_f32_16x16x32_f16 v[64:67], v[112:115], v[80:83], v[64:67]\n\t" \
  "ds_write2_b32 v57, v40, v41 offset0:64 offset1:65\n\tv_mfma_f32_16x16x32_f16 v[68:71], v[80:83], v[144:147], v[68:71]\n\t" \
  "ds_write_b64 v59, v[42:43] offset:16\n\ts_waitcnt lgkmcnt(8)\n\t" \
  "v_mfma_f32_16x16x32_f16 v[64:67], v[116:119], v[84:87], v[64:67]\n\tds_write_b32 v59, v42 offset:1164\n\t" \
  "v_mfma_f32_16x16x32_f16 v[68:71], v[84:87], v[148:151], v[68:71]\n\tds_write_b32 v59, v43 offset:1168\n\t" \
  "s_waitcnt lgkmcnt(9)\n\tv_mfma_f32_16x16x32_f16 v[64:67], v[120:123], v[88:91], v[64:67]\n\t" \
  "ds_write_b16 v59, v42 offset:590\n\tv_mfma_f32_16x16x32_f16 v[68:71], v[88:91], v[152:155], v[68:71]\n\t" \
  "ds_write_b32 v59, v44 offset:592\n\ts_waitcnt lgkmcnt(10)\n\t" \
  "v_mfma_f32_16x16x32_f16 v[64:67], v[124:127], v[92:95], v[64:67]\n\tds_write_b16_d16_hi v59, v43 offset:596\n\t" \
  "v_mfma_f32_16x16x32_f16 v[68:71], v[92:95], v[156:159], v[68:71]\n\tds_write_b16 v59, v42 offset:1738\n\t" \
  "s_waitcnt lgkmcnt(11)\n\tv_mfma_f32_16x16x32_f16 v[64:67], v[128:131], v[96:99], v[64:67]\n\t" \
  "ds_write_b32 v59, v44 offset:1740\n\tv_mfma_f32_16x16x32_f16 v[68:71], v[96:99], v[160:163], v[68:71]\n\t" \
  "ds_write_b16_d16_hi v59, v43 offset:1744\n\ts_waitcnt lgkmcnt(12)\n\t" \
  "v_mfma_f32_16x16x32_f16 v[68:71], v[100:103], v[164:167], v[68:71]\n\tv_mfma_f32_16x16x32_f16 v[64:67], v[132:135], v[100:103], v[64:67]\n\t" \
  "s_nop 4\n\tv_cvt_pk_f16_f32 v48, v68, v69\n\t" \
  "v_cvt_pk_f16_f32 v49, v70, v71\n\ts_nop 1\n\t" \
  "v_cvt_pk_f16_f32 v46, v64, v65\n\tv_cvt_pk_f16_f32 v47, v66, v67\n\t" \
  "v_alignbit_b32 v50, v49, v48, 16\n\tds_write2_b32 v56, v46, v47 offset0:0 offset1:1\n\t" \
  "ds_write2_b32 v57, v46, v47 offset0:0 offset1:1\n\tds_write_b64 v59, v[48:49] offset:0\n\t" \
  "ds_write_b32 v59, v48 offset:1148\n\tds_write_b32 v59, v49 offset:1152\n\t" \
  "ds_write_b16 v59, v48 offset:574\n\tds_write_b32 v59, v50 offset:576\n\t" \
  "ds_write_b16_d16_hi v59, v49 offset:580\n\tds_write_b16 v59, v48 offset:1722\n\t" \
  "ds_write_b32 v59, v50 offset:1724\n\tds_write_b16_d16_hi v59, v49 offset:1728\n\t"
#define GEN_CLOBBER "v36", "v37", "v38", "v39", "v40", "v42", "v43", "v44", "v45", "v47", "v49", "v50", "v51", "v52", "v53", "v54", "v55", "v56", "v57", "v58", "v59", "v60", "v61", "v62", "v63", "v64", "v65", "v66", "v67", "v72", "v73", "v74", "v75", "v76", "v77", "v78", "v79", "v80", "v81", "v82", "v83", "v84", "v85", "v86", "v87", "v88", "v89", "v90", "v91", "v92", "v93", "v94", "v95", "v98", "v99", "v100", "v101", "v102", "v103", "v104", "v105", "v106", "v107", "v108", "v109", "v110", "v111", "v112", "v113", "v114", "v115", "v116", "v117", "v118", "v119", "v120", "v121", "v122", "v123", "v124", "v125", "v126", "v127", "v128", "v129", "v130", "v131", "v132", "v133", "v134", "v135", "v136", "v137", "v138", "v139", "v140", "v141", "v142", "v143", "v144", "v145", "v146", "v147", "v148", "v149", "v150", "v151", "v152", "v153", "v154", "v155", "v156", "v157", "v158", "v159", "v160", "v161", "v162", "v163", "v164", "v165", "v166", "v167"
__device__ __forceinline__ f32x2 pkfma(f32x2 a, f32x2 b, f32x2 c) {
  f32x2 r;
  asm("v_pk_fma_f32 %0, %1, %2, %3" : "=v"(r) : "v"(a), "v"(b), "v"(c));
  return r;
}
template <int HI>
__device__ __forceinline__ f32x2 pkfmas(f32x2 a, f32x2 s, f32x2 c) {
  f32x2 r;
  if (HI) asm("v_pk_fma_f32 %0, %2, %1, %3 op_sel:[1,0,0] op_sel_hi:[1,1,1]" : "=v"(r) : "v"(a), "v"(s), "v"(c));
  else asm("v_pk_fma_f32 %0, %2, %1, %3 op_sel:[0,0,0] op_sel_hi:[0,1,1]" : "=v"(r) : "v"(a), "v"(s), "v"(c));
  return r;
}
__device__ __forceinline__ float red4(float (&q)[4]) {
  swap32(q[0], q[2]); swap32(q[1], q[3]);
  float a = q[0] + q[2], e = q[1] + q[3];
  swap16(a, e);
  a += e;
  a += dppf<0x121, 0xf>(a); a += dppf<0x122, 0xf>(a); a += dppf<0x124, 0xf>(a); a += dppf<0x128, 0xf>(a);
  return a;
}

__device__ __forceinline__ void ln_rows(const u32x4 (&xv)[4], const u32x4 (&wv)[2], rsrc_t rs, u32 tag, int b, int w, int l, char* st) {
  float sw = 0.f;
#pragma unroll
  for (int i = 0; i < 8; i++) sw = dot2b(wv[i >> 2][i & 3], 0x3f803f80u, sw);
  SB();
#pragma unroll
  for (int r = 0; r < 2; r++) {
    float s[4] = {0.f, 0.f, 0.f, sw};
#pragma unroll
    for (int i = 0; i < 8; i++) {
      const u32 a = xv[2 * r + (i >> 2)][i & 3];
      s[0] = dot2b(a, 0x3f803f80u, s[0]); s[1] = dot2b(a, a, s[1]); s[2] = dot2b(a, wv[i >> 2][i & 3], s[2]);
    }
    const float ml = s[0] * 0.0625f, c = rdl(ml, 0), dl = ml - c;
    s[1] = __builtin_fmaf(16.f * dl, dl, __builtin_fmaf(-s[0], ml, s[1]));
    const float a = red4(s);
    if ((l & 15) == 0 || l == 1) *(float*)(st + 64 * w + 32 * r + (l == 1 ? 16 : l >> 2)) = l == 1 ? c : a;
    SB();
  }
  u32 old = 0u;
  if (l == 0) asm volatile("ds_add_rtn_u32 %0, %1, %2\n\ts_waitcnt lgkmcnt(0)" : "=v"(old) : "v"((u32)(size_t)(LDS char*)(st + 512)), "v"(1u) : "memory");
  if (__builtin_amdgcn_readfirstlane((int)old) == 7) {
    const f32x4 q = *(const f32x4*)(st + 32 * (l & 15));
    const float c = *(const float*)(st + 32 * (l & 15) + 16);
    const float mu = q[0] * (1.f / D), dv = mu - c;
    const float var = fmaxf(__builtin_fmaf(-dv, dv, q[1] * (1.f / D)), 0.f);
    const float m = rb(mu), sd = rb(__builtin_amdgcn_sqrtf(rb(rb(var) + 1e-5f)));
    const float lg = __builtin_fmaf(-m, q[3], q[2]) * __builtin_amdgcn_rcpf(sd);
    u32 rv = __float_as_uint(rb(lg));
    rv = (rv ^ (((int)rv < 0) ? 0xffff0000u : 0x80000000u)) & 0xffff0000u;
    const u32 pr = (rv >> 16) | dppu<0xb1, 0xf>(rv);
    u32 off = l < 16 ? O_SG + 128 * (b - 128) + 8 * l : l < 32 ? O_RG + 64 * (b - 128) + 4 * (l - 16) : O_ARR + 8 * b;
    if (l > 32 || (l & 17) == 17) off = 0x80000000u;
    st8(rs, off, u32x2{l < 16 ? pack2(m, sd) : pr, tag});
    SB();
    *(volatile LDS u32*)(LDS char*)(st + 516) = 1u;
    SB();
  }
}

constexpr int NOPS = 109, NSLOT = 168;
struct Ops {
  signed char k[NOPS], a[NOPS], b[NOPS];
  short first[NOPS + 1];
  unsigned char op[NSLOT];
  short ld[NOPS][2];
  short end[8];
};
constexpr int nmf(int k, int T, int p) { return k == 0 ? (p == 0 && T > 0) + (T < 8) + (p == 0 && T > 0 && T < 5) : k == 1 ? 1 + (T < 6) : k == 2 ? (T < 4 ? 3 : 6 - T) : 4 - T; }
constexpr Ops mkops() {
  Ops O = {};
  int n = 0, last[8] = {};
  O.k[n] = 0; O.a[n] = 0; O.b[n] = 0; n++;
  for (int I = 0; I < 8; I++) {
    int fk[7] = {}, fa[7] = {}, fb[7] = {}, m = 0;
    if (I >= 4) { fk[m] = 3; fa[m] = I - 4; fb[m] = 0; m++; }
    if (I >= 2) for (int p = 0; p < 2; p++) { fk[m] = 2; fa[m] = I - 2; fb[m] = p; m++; }
    if (I >= 1) for (int p = 0; p < 4; p++) { fk[m] = 1; fa[m] = I - 1; fb[m] = p; m++; }
    int f = 0;
    for (int c = 1; c <= 8; c++) {
      O.k[n] = 0; O.a[n] = c == 8 ? I + 1 : I; O.b[n] = c & 7; n++;
      if (I >= 2 && f < m) { O.k[n] = fk[f]; O.a[n] = fa[f]; O.b[n] = fb[f]; n++; f++; }
    }
    for (; f < m; f++) { O.k[n] = fk[f]; O.a[n] = fa[f]; O.b[n] = fb[f]; n++; }
    last[I] = n - 1;
  }
  int q = 0;
  for (int i = 0; i < NOPS; i++) {
    O.first[i] = q;
    for (int u = nmf(O.k[i], O.a[i], O.b[i]); u > 0; u--) O.op[q++] = i;
  }
  O.first[NOPS] = q;
  for (int I = 0; I < 8; I++) O.end[I] = O.first[last[I]];
  for (int i = 0; i < NOPS; i++) {
    O.ld[i][1] = O.first[i] - LA;
    O.ld[i][0] = O.ld[i][1] - (O.k[i] && O.first[i] - O.first[i - 1] > 1);
  }
  return O;
}
constexpr Ops OPS = mkops();
static_assert(OPS.first[NOPS] == NSLOT, "schedule");
constexpr int WL[4] = {-1, 17, WL2, WL3}, WN[4] = {38, 38, 22, 13};
constexpr int wslot(int pp, int k) { return pp < 2 ? (pp ? 22 : 2) + k / 2 : WL[pp] + 4 + k; }
constexpr int opat(int k, int T, int p) { for (int i = 0; i < NOPS; i++) if (OPS.k[i] == k && OPS.a[i] == T && OPS.b[i] == p) return i; return -1; }
static_assert(wslot(0, 12) < OPS.ld[opat(1, 0, 0)][0] && wslot(0, 21) < OPS.ld[opat(2, 0, 0)][0] && wslot(1, 12) < OPS.ld[opat(1, 1, 0)][0] &&
              wslot(1, 21) < OPS.ld[opat(2, 1, 0)][0] && wslot(1, 37) < OPS.ld[opat(3, 0, 0)][0] && wslot(2, 12) < OPS.ld[opat(1, 3, 0)][0] &&
              wslot(2, 21) < OPS.ld[opat(2, 3, 0)][0] && wslot(3, 12) < OPS.ld[opat(1, 5, 0)][0] && wslot(0, 29) < WL[1] && wslot(1, 37) < WL[2] &&
              wslot(2, 21) < WL[3], "window order");
constexpr int XC[3] = {OPS.ld[opat(0, 2, 0)][1], WL2 < OPS.ld[opat(0, 4, 0)][1] ? WL2 : OPS.ld[opat(0, 4, 0)][1], WL3};
static_assert(XC[0] <= WL[1] && WL3 <= OPS.ld[opat(0, 6, 0)][1], "chunk order");
template <int V> struct IC { static constexpr int value = V; };
template <int A, int B, class F> __device__ __forceinline__ void sfor(F&& f) {
  if constexpr (A < B) { f(IC<A>{}); sfor<A + 1, B>(f); }
}
__device__ __forceinline__ void dma(const void* base, u32 v, u32 dst) {
  asm volatile("s_mov_b32 m0, %0\n\ts_nop 0\n\tglobal_load_lds_dwordx4 %1, %2 nt" ::"s"(dst), "v"(v), "s"(base) : "memory");
}

__global__ __launch_bounds__(768, 3) void fused(const u16* __restrict__ x, const u16* __restrict__ lar,
    const u16* __restrict__ aim, const u16* __restrict__ cre, const u16* __restrict__ cim,
    const u16* __restrict__ ldt, const u16* __restrict__ dsk, const u16* __restrict__ wr,
    const u16* __restrict__ wg, const u16* __restrict__ wu, const u16* __restrict__ wd,
    u16* __restrict__ out, char* ws) {
  __shared__ __attribute__((aligned(256))) char smem[SMEM];
  const int t = threadIdx.x, w = __builtin_amdgcn_readfirstlane(t >> 6), l = t & 63;
  const int b = blockIdx.x, xk = b & 7, j = (b >> 3) & 15;
  STAMP(0);
  asm volatile("" ::"s"(x), "s"(lar), "s"(aim), "s"(cre), "s"(cim), "s"(ldt), "s"(dsk), "s"(wr), "s"(wg), "s"(wu), "s"(wd), "s"(out), "s"(ws));
  u32 tag = *(const __attribute__((address_space(4))) u32*)ws;
  const rsrc_t rs = __builtin_amdgcn_make_buffer_rsrc(ws, 0, WS_END, 0x00020000);
#define PIN_TAG() do { asm volatile("" : "+s"(tag)); tag += 1u; } while (0)
#define BAR() asm volatile("s_barrier" ::: "memory")
  const int n16 = l & 15, kg = l >> 4;
  const char* pc0 = (const char*)(__builtin_amdgcn_s_getpc() & ~255ull);
  asm volatile("" : "+s"(pc0));
  const u32 lds0 = (u32)(size_t)(LDS char*)smem;
#define FLAG(o, v) do { asm volatile("" ::: "memory"); SB(); *(volatile LDS u32*)(LDS char*)(smem + (o)) = (v); SB(); } while (0)
#define ARRIVE(o) do { SB(); if (l == 0) asm volatile("ds_add_u32 %0, %1" ::"v"(lds0 + (o)), "v"(1u) : "memory"); SB(); } while (0)
#define CWAIT(o, n, nap) do { _Pragma("unroll 1") for (int spin = 0; spin < SPIN; spin++) { \
      if (__builtin_amdgcn_readfirstlane((int)*(const volatile LDS u32*)(LDS char*)(smem + (o))) >= (int)(n)) break; \
      asm volatile(nap ::: "memory"); } SB(); asm volatile("" ::: "memory"); } while (0)
#define AWAIT(o, n, nap) do { _Pragma("unroll 1") for (int spin = 0; spin < SPIN; spin++) { \
      if (__all(*(const volatile LDS u32*)(LDS char*)(smem + (o)) >= (u32)(n))) break; \
      asm volatile(nap ::: "memory"); } SB(); asm volatile("" ::: "memory"); } while (0)
  u32 code;
#define CODELD(i_) do { int pi_ = i_; \
    pi_ = pi_ < CODE_KB ? pi_ : CODE_KB - 1; code = *(const __attribute__((address_space(1))) u32*)(pc0 + 1024 * pi_ + 16 * l); SB(); } while (0)
#define CODEUSE() asm volatile("" ::"v"(code))
  const u32 ixo = O_IDX + (2 * xk + (j & 1)) * 128 + (l & 7) * 8;

  if (b >= 128) {
    if (w < 8) {
      u32x4 xv[4], wv[2];
      const int wl = ROT == 0 ? w : ROT == 1 ? (w + b) & 7 : (w + j) & 7;
      wv[0] = *(const u32x4*)(wr + l * 8); wv[1] = *(const u32x4*)(wr + 512 + l * 8);
#pragma unroll
      for (int r = 0; r < 4; r++) xv[r] = *(const u32x4*)(x + (size_t)(16 * (b - 128) + 2 * wl) * D + 512 * r + l * 8);
      SB();
      STAMP(11);
      if (t < 32) *(u32*)(smem + FC + 4 * t) = 0u;
      if (t == 32) *(u32x2*)(smem + FL + 512) = u32x2{0u, 0u};
      PIN_TAG();
      LBAR();
      STAMP(12);
      ln_rows(xv, wv, rs, tag, b, wl, l, smem + FL);
      STAMP(10);
    } else {
      PIN_TAG();
      BAR();
    }
    if (j >= 14) {
      const int sb = 2 * xk + (j - 14);
      u32* gm = (u32*)smem;
      u32* ncand = gm + 32;
      u32* cand = gm + 64;
      CODELD(w + 12 * (j & 1));
      if (w >= 8 && w < 11) return;
      if (w < 8) {
        cand[t] = 0u;
        if (t < 16) cand[512 + t] = 0u;
        if (t == 0) *ncand = 0u;
        u32x4 g;
#pragma unroll 1
        for (int spin = 0; spin < SPIN; spin++) {
          g = ld16(rs, O_RG + 16 * t);
          if (__all(g.y == tag && g.w == tag)) break;
          NAP();
        }
        STAMP(2);
        const u32 ib = 2047 - 4 * t;
        const u32 key[4] = {(g.x << 16) | ib, (g.x & 0xffff0000u) | (ib - 1), (g.z << 16) | (ib - 2), (g.z & 0xffff0000u) | (ib - 3)};
        u32 m = max(max(key[0], key[1]), max(key[2], key[3]));
        const u32 m4 = m;
        m = max(m, dppu<0x121, 0xf>(m)); m = max(m, dppu<0x122, 0xf>(m));
        m = max(m, dppu<0x124, 0xf>(m)); m = max(m, dppu<0x128, 0xf>(m));
        if (n16 == 0) gm[t >> 4] = m;
        LBAR();
        STAMP(3);
        u32 thr;
        {
          const u32 mine = gm[l & 31];
          int cnt = 0;
#pragma unroll
          for (int i = 0; i < 4; i++) {
            u32x4 q = *(const u32x4*)(gm + 16 * (l >> 5) + 4 * i);
            cnt += (q.x > mine) + (q.y > mine) + (q.z > mine) + (q.w > mine);
          }
          auto sw = __builtin_amdgcn_permlane32_swap((u32)cnt, (u32)cnt, false, false);
          cnt = (int)(sw[0] + sw[1]);
          const unsigned long long bal = __ballot(cnt == 7);
          thr = (u32)__builtin_amdgcn_readlane((int)mine, __builtin_ctzll(bal));
        }
        if (m4 >= thr) {
          const u32 c = (key[0] >= thr) + (key[1] >= thr) + (key[2] >= thr) + (key[3] >= thr);
          u32 slot;
          asm volatile("ds_add_rtn_u32 %0, %1, %2\n\ts_waitcnt lgkmcnt(0)" : "=v"(slot) : "v"((u32)(size_t)(LDS u32*)ncand), "v"(c) : "memory");
#pragma unroll
          for (int i = 0; i < 4; i++)
            if (key[i] >= thr) cand[slot++] = key[i];
        }
        LBAR();
        STAMP(4);
        if (w == 0) {
          const int nv = (int)*ncand;
          u32 mine = cand[l];
          int cnt = 0;
#pragma unroll
          for (int i = 0; i < 4; i++) {
            const u32x4 q = *(const u32x4*)(cand + 4 * i);
            cnt += (q.x > mine) + (q.y > mine) + (q.z > mine) + (q.w > mine);
          }
          const int n = __builtin_amdgcn_readfirstlane(nv);
          int i0 = 0, i = 16;
#pragma unroll 1
          for (;;) {
#pragma unroll 1
            for (; i < n; i += 4) {
              const u32x4 q = *(const u32x4*)(cand + i);
              cnt += (q.x > mine) + (q.y > mine) + (q.z > mine) + (q.w > mine);
            }
            if (i0 + l < n && cnt < 8)
              st8(rs, O_IDX + sb * 128 + cnt * 8, u32x2{(2047u - (mine & 0x7ffu)) | ((mine ^ (((int)mine < 0) ? 0x80000000u : 0xffff0000u)) & 0xffff0000u), tag});
            i0 += 64;
            if (i0 >= n) break;
            mine = cand[i0 + l]; cnt = 0; i = 0;
          }
          STAMP(5);
        }
      } else { BAR(); BAR(); }
      CODEUSE();
      if (b == 255 && w == 11) {
#pragma unroll 1
        for (int spin = 0; spin < SPIN; spin++) {
          const u32x4 a = ld16(rs, O_ARR + 16 * l), c = ld16(rs, O_ARR + 1024 + 16 * l);
          if (__all(a.y == tag && a.w == tag && c.y == tag && c.w == tag)) break;
          asm volatile("s_sleep 8" ::: "memory");
        }
        if (l == 0) __builtin_amdgcn_raw_buffer_store_b32(tag, rs, 0, 0, 16);
      }
      FLUSH();
      __builtin_amdgcn_endpgm();
    }
    const int id = 14 * xk + j;
    if (w >= 8) {
      const int dw = w - 8;
      const u32 sw = (((n16 >> 1) ^ (kg | ((dw >> 1) << 2))) << 5) + ((l & 1) << 4);
      {
        const u16* base = ((id & 2) ? wu : wg) + (size_t)(512 * (id & 1) + 4 * dw) * F + (id >> 2) * 128;
        const u32 v = kg * (2 * F) + sw;
#pragma unroll
        for (int i = 0; i < 32; i++) {
          if (i == NE) { CWAIT(FL + 516, 1, "s_sleep 1"); STAMP(5); }
          dma(base + (size_t)(16 * i) * F, v, lds0 + 4096 * i + 1024 * dw);
        }
      }
      STAMP(1);
      asm volatile("s_waitcnt vmcnt(0)" ::: "memory");
      ARRIVE(FC);
      STAMP(2);
      CWAIT(FC + 4, 8, "s_sleep 1");
      STAMP(3);
      {
        const u16* base = wd + (size_t)(256 * (id >> 3) + 4 * dw) * D + (id & 7) * 128;
        const u32 v = kg * (2 * D) + sw;
#pragma unroll
        for (int i = 0; i < 16; i++) dma(base + (size_t)(16 * i) * D, v, lds0 + 4096 * i + 1024 * dw);
      }
      asm volatile("s_waitcnt vmcnt(0)" ::: "memory");
      ARRIVE(FC + 8);
      STAMP(4);
      FLUSH();
      __builtin_amdgcn_endpgm();
    }
    u32x2 sg = {0u, 0u};
    u32 so = 0u;
    if (w == 0) {
      u32x2 ix;
#pragma unroll 1
      for (int spin = 0; spin < SPIN; spin++) {
        ix = ld8(rs, ixo);
        if (__all(ix.y == tag)) break;
        asm volatile("s_sleep 2" ::: "memory");
      }
      STAMP(1);
#pragma unroll
      for (int i = 0; i < 8; i++) {
        const u32 row = (u32)__builtin_amdgcn_readlane((int)ix.x, i) & 2047u;
        asm volatile("s_mov_b32 m0, %0\n\ts_nop 0\n\tglobal_load_lds_dwordx4 %1, %2" ::"s"(lds0 + FA + AP * i), "v"(16 * l),
                     "s"(x + (size_t)row * D + 512 * (id & 1)) : "memory");
      }
      so = O_SG + (ix.x & 2047u) * 8;
      sg = ld8(rs, so);
      SB();
    }
    const char* wp = smem + (8 * kg + (n16 >> 2)) * 256 + ((w ^ ((n16 >> 2) | ((kg & 1) << 2))) << 5) + 8 * (l & 3);
    const char* wq = wp + 65536;
    {
      CWAIT(FC, 4, "s_sleep 1");
      STAMP(2);
      bf16x8 wf[16];
#pragma unroll
      for (int k = 0; k < 8; k++) {
        wf[k] = mkb(trread(wp + 8192 * k), trread(wp + 8192 * k + 1024));
        wf[8 + k] = mkb(trread(wq + 8192 * k), trread(wq + 8192 * k + 1024));
      }
      ARRIVE(FC + 4);
      f32x4 cs = {};
      {
        const bf16x8 one = asb(u32x4{0x3f803f80u, 0x3f803f80u, 0x3f803f80u, 0x3f803f80u});
#pragma unroll
        for (int k = 0; k < 16; k++) cs = MFMAB(wf[(k >> 1) + 8 * (k & 1)], one, cs);
      }
      SB();
      STAMP(3);
      if (w == 0) {
#pragma unroll 1
        for (int spin = 0; spin < SPIN; spin++) {
          if (__all(sg.y == tag)) break;
          NAP();
          sg = ld8(rs, so);
        }
        asm volatile("s_waitcnt vmcnt(0)" ::: "memory");
        *(u32*)(smem + FC + 32 + 4 * (l & 7)) = sg.x;
        FLAG(FC + 12, 1u);
      } else CWAIT(FC + 12, 1, "s_sleep 1");
      STAMP(4);
      u32x4 xa[8];
      const char* ap = smem + FA + (l & 7) * AP + 512 * (n16 >> 3) + 16 * kg;
#pragma unroll
      for (int i = 0; i < 8; i++) xa[i] = *(const u32x4*)(ap + 64 * i);
      const u32 st = *(const u32*)(smem + FC + 32 + 4 * (l & 7));
      SB();
      f32x4 a1 = {}, a2 = {};
#pragma unroll
      for (int i = 0; i < 8; i++) { a1 = MFMAB(wf[i], asb(xa[i]), a1); a2 = MFMAB(wf[8 + i], asb(xa[i]), a2); }
      const float rsd = __builtin_amdgcn_rcpf(hi2f(st)), nm = -lo2f(st);
      float o[4];
#pragma unroll
      for (int i = 0; i < 4; i++) o[i] = __builtin_fmaf(nm, cs[i], a1[i] + dppf<0x128, 0xf>(a2[i])) * rsd;
      if (n16 < 8) st16(rs, O_HG + id * 4096 + 512 * w + 128 * kg + 16 * n16, u32x4{pack2(o[0], o[1]), tag, pack2(o[2], o[3]), tag});
      SB();
      STAMP(5);
    }
    {
      const u32 off = O_HG + (2 * (id >> 3) + (w >> 2)) * 16384 + (w & 3) * 1024 + 16 * l;
      CWAIT(FC + 8, 4, "s_sleep 1");
      STAMP(6);
      u32x4 ha[4];
#pragma unroll
      for (int i = 0; i < 4; i++) ha[i] = ld16(rs, off + 4096 * i);
      SB();
      bf16x8 df[8];
#pragma unroll
      for (int k = 0; k < 8; k++) df[k] = mkb(trread(wp + 8192 * k), trread(wp + 8192 * k + 1024));
      SB();
#pragma unroll 1
      for (int spin = 0; spin < SPIN; spin++) {
        bool ok = true;
#pragma unroll
        for (int i = 0; i < 4; i++) ok &= ha[i].y == tag && ha[i].w == tag;
        if (__all(ok)) break;
        NAP();
#pragma unroll
        for (int i = 0; i < 4; i++) ha[i] = ld16(rs, off + 4096 * i);
      }
      STAMP(7);
      u32x2 hv;
#pragma unroll
      for (int i = 0; i < 2; i++) {
        const u32 g0 = ha[0][2 * i], g1 = ha[1][2 * i], u0 = ha[2][2 * i], u1 = ha[3][2 * i];
        const u32 gg = pack2(lo2f(g0) + lo2f(g1), hi2f(g0) + hi2f(g1)), uu = pack2(lo2f(u0) + lo2f(u1), hi2f(u0) + hi2f(u1));
        const u32 sl = pack2(lo2f(gg) * sigm(lo2f(gg)), hi2f(gg) * sigm(hi2f(gg)));
        hv[i] = pack2(lo2f(sl) * lo2f(uu), hi2f(sl) * hi2f(uu));
      }
      *(u32x2*)(smem + FH + (l & 7) * HP + 64 * w + (l & 56)) = hv;
      LBAR();
      STAMP(8);
      const char* hp = smem + FH + (l & 7) * HP + 256 * (n16 >> 3) + 16 * kg;
      f32x4 a1 = {}, a2 = {};
#pragma unroll
      for (int i = 0; i < 4; i++) {
        const bf16x8 h = asb(*(const u32x4*)(hp + 64 * i));
        a1 = MFMAB(df[i], h, a1); a2 = MFMAB(df[4 + i], h, a2);
      }
      u32 o[4];
#pragma unroll
      for (int i = 0; i < 4; i++) o[i] = __float_as_uint(a1[i] + dppf<0x128, 0xf>(a2[i]));
      const u32 po = O_PG + ((id >> 3) * 128 + 16 * (id & 7) + 2 * w + (kg >> 1)) * 512 + 64 * n16 + 32 * (kg & 1);
      if (n16 < 8) { st16(rs, po, u32x4{o[0], tag, o[1], tag}); st16(rs, po + 16, u32x4{o[2], tag, o[3], tag}); }
      STAMP(9);
    }
    FLUSH();
    __builtin_amdgcn_endpgm();
  }

  const int g = 8 * xk + (j & 7) + 64 * (j >> 3);
  if (w >= 8) {
    const int p = w - 8;
    u32x4 xs[4][2];
    f32x2 sc[4];
    const u32x4 dv = *(const __attribute__((address_space(4))) u32x4*)(dsk + 8 * g);
    CODELD(p + 4 * (j & 7));
    if (p == 0) *(u32x2*)(smem + OMISC + 8 * l) = u32x2{0u, 0u};
    PIN_TAG();
    LBAR();
    {
      const u16* xsp = x + (size_t)(128 * p + 2 * l) * D + 8 * g;
#pragma unroll
      for (int i = 0; i < 8; i++) xs[i >> 1][i & 1] = *(const u32x4*)(xsp + (512 * (i >> 1) + (i & 1)) * D);
      SB();
    }
    if (t == 512) st8(rs, O_ARR + b * 8, u32x2{0u, tag});
#pragma unroll
    for (int i = 0; i < 4; i++) { sc[i] = f32x2{1.f + lo2f(dv[i]), 1.f + hi2f(dv[i])}; asm volatile("" : "+v"(sc[i])); }
    STAMP(9);
    __builtin_amdgcn_s_setprio(3);
    f32x2 xq[4][8];
    {
      u32x4 sg[4];
#pragma unroll 1
      for (int spin = 0; spin < SPIN; spin++) {
        bool ok = true;
#pragma unroll
        for (int i = 0; i < 4; i++) sg[i] = ld16(rs, O_SG + 4096 * i + 1024 * p + 16 * l);
#pragma unroll
        for (int i = 0; i < 4; i++) ok &= sg[i].y == tag && sg[i].w == tag;
        if (__all(ok)) break;
        NAP();
      }
      STAMP(3);
      CODEUSE();
      asm volatile("s_mov_b32 m0, %0\n\ts_nop 0" ::"s"(lds0 + OX + 2 * XO + 256 * p));
#pragma unroll
      for (int i = 0; i < 4; i++) {
        const u32 sa = sg[i].x, sb = sg[i].z;
        const f32x2 r2 = {__builtin_amdgcn_rcpf(hi2f(sa)), __builtin_amdgcn_rcpf(hi2f(sb))};
        const f32x2 m2 = {-lo2f(sa) * r2.x, -lo2f(sb) * r2.y};
        f32x2 x2[8], n2[8];
        u32 o[8], fv = i + 1;
#pragma unroll
        for (int c = 0; c < 8; c++) {
          const u32 a = xs[i][0][c >> 1], bb = xs[i][1][c >> 1];
          x2[c] = (c & 1) ? f32x2{hi2f(a), hi2f(bb)} : f32x2{lo2f(a), lo2f(bb)};
        }
        asm volatile("" : "+v"(fv));
        SB();
#pragma unroll
        for (int c = 0; c < 8; c++) n2[c] = pkfma(x2[c], r2, m2);
        SB();
#pragma unroll
        for (int c = 0; c < 8; c++) o[c] = packh(n2[c].x, n2[c].y);
        SB();
#pragma unroll
        for (int c = 0; c < 8; c++) asm volatile("ds_write_addtid_b32 %0 offset:%1" ::"v"(o[c]), "n"(c * XREG + 1024 * i) : "memory");
        FLAG(OXF + 4 * p, fv);
#pragma unroll
        for (int c = 0; c < 8; c++) xq[i][c] = (c & 1) ? pkfmas<1>(n2[c], sc[c >> 1], x2[c]) : pkfmas<0>(n2[c], sc[c >> 1], x2[c]);
        SB();
        if (i == 3) __builtin_amdgcn_s_setprio(PPRIO);
      }
    }
    STAMP(4);
    const rsrc_t ro = __builtin_amdgcn_make_buffer_rsrc(out, 0, 2 * L * D, 0x00020000);
    const int u = 64 * (p & 1) + l;
    const char* yp = smem + OY + 512 * (p >> 1) + 4 * (u ^ ((u >> 4) & 6));
    const char* yq = yp + 4 * CHR;
    const u32 op = (u32)((128 * p + 2 * l) * (2 * D) + 16 * g);
    u32 ys[8];
    u32x2 ix = {0u, 0u};
    u32 px = 0u, pd = 0u;
    u32x4 pv[7];
#define PGLD() do { _Pragma("unroll") for (int q = 0; q < 7; q++) pv[q] = ld16(rs, O_PG + ((2 * q + (l >> 5)) * 128 + g) * 512 + 16 * (l & 31)); } while (0)
#pragma unroll
    for (int i = 0; i < 4; i++) {
      if (i == 2 && p == 0) {
        const u32 io = ixo - 8 * (l & 7) + 2 * (l & 28);
#pragma unroll 1
        for (int spin = 0; spin < SPIN; spin++) {
          ix = ld8(rs, io);
          if (__all(ix.y == tag)) break;
          NAP();
        }
        px = x[(size_t)(ix.x & 2047u) * D + 8 * g + 2 * (l & 3) + (l >> 5)];
        pd = dsk[8 * g + 2 * (l & 3) + (l >> 5)];
        if (!(l & 35)) atomicOr((u32*)(smem + OMISC) + ((ix.x & 2047u) >> 5), 1u << (ix.x & 31u));
        FLAG(OYP + 32, 8u);
        STAMP(11);
      }
      if (i == 3) {
        asm volatile("s_waitcnt vmcnt(2)" ::: "memory");
        FLAG(OYP + 36 + 4 * p, 8u);
      }
      AWAIT(OYP + 4 * min(l, i < 2 ? 7 : 8), 2 * i + 1 + (p >> 1), YNAP);
      if (i == 0) STAMP(5); else if (i == 1) STAMP(6); else if (i == 2) STAMP(7); else STAMP(8);
      u32 y[8], bm = 0u;
#pragma unroll
      for (int c = 0; c < 8; c++) y[c] = *(const u32*)((c < 4 ? yp : yq) + (c & 3) * CHR + 1024 * i);
      if (i >= 2) bm = *(const u32*)(smem + OMISC + 64 * i + 16 * p + 4 * kg) >> (2 * n16);
      if (i == 0) {
        const u32x4 a = *(const u32x4*)(smem + OSC), c = *(const u32x4*)(smem + OSC + 16);
#pragma unroll
        for (int k = 0; k < 4; k++) { ys[k] = (u32)__builtin_amdgcn_readfirstlane((int)a[k]); ys[4 + k] = (u32)__builtin_amdgcn_readfirstlane((int)c[k]); }
      }
      SB();
      if (i == 3 && p == 0) PGLD();
      SB();
#pragma unroll
      for (int r = 0; r < 2; r++) {
        u32x4 v;
#pragma unroll
        for (int k = 0; k < 4; k++)
          v[k] = pack2(dot2b(y[2 * k], ys[2 * k] << (16 * r), xq[i][2 * k][r]), dot2b(y[2 * k + 1], ys[2 * k + 1] << (16 * r), xq[i][2 * k + 1][r]));
        __builtin_amdgcn_raw_buffer_store_b128(v, ro, op | (bm << (31 - r) & 0x80000000u), (512 * i + r) * (2 * D), 0);
        SB();
        if (r) asm volatile("s_nop 1");
        SB();
      }
    }
    STAMP(12);
    if (p == 0) {
      AWAIT(OYP + 4 * min(l, 12), 8, YNAP);
      const int prow = (int)(ix.x & 2047u), c = 2 * (l & 3) + (l >> 5), v = prow >> 1;
      const float yl = lo2f(*(const u16*)(smem + OY + CHR * c + 4 * (v ^ ((v >> 4) & 6)) + 2 * (prow & 1)));
      const float ql = hlo(*(const u16*)(smem + OX + 2 * XO + c * XREG + 2 * prow));
      const float e = lo2f(px) + yl * lo2f(*(const u32*)(smem + OSC + 4 * c)) + ql * (1.f + lo2f(pd));
      const float rsel = rb(sigm(hi2f(ix.x)));
#pragma unroll 1
      for (int spin = 0; spin < SPIN; spin++) {
        bool ok = true;
#pragma unroll
        for (int q = 0; q < 7; q++) ok &= pv[q].y == tag && pv[q].w == tag;
        if (__all(ok)) break;
        NAP();
        PGLD();
      }
      STAMP(14);
      float s0 = 0.f, s1 = 0.f;
#pragma unroll
      for (int q = 0; q < 7; q++) { const u32 a = pv[q].x, c2 = pv[q].z; s0 += __uint_as_float(a); s1 += __uint_as_float(c2); }
      swap32(s0, s1);
      const float upd = rb(rb(s0 + s1) * rsel);
      out[(size_t)prow * D + 8 * g + c] = (u16)(pack2(e + upd, 0.f) & 0xffffu);
    }
    STAMP(13);
    FLUSH();
    __builtin_amdgcn_endpgm();
  }

  const int d = 8 * g + w;
  char* W = smem + w * CHR;
  char* X = smem + OX + w * XREG;
  u32 pl, pa, pr, pi;
  u32x4 tv, fx;
  asm volatile("global_load_dword v96, %6, %8\n\tglobal_load_dword v97, %6, %9\n\tglobal_load_dword v68, %6, %10\n\tglobal_load_dword v69, %6, %11\n\t"
               "global_load_dwordx4 v[16:19], %7, %12\n\tglobal_load_dwordx4 v[20:23], %7, %12 offset:1024"
               : "=&{v96}"(pl), "=&{v97}"(pa), "=&{v68}"(pr), "=&{v69}"(pi), "=&{v[16:19]}"(tv), "=&{v[20:23]}"(fx)
               : "v"(256 * d + 4 * l), "v"(16 * l), "s"(lar), "s"(aim), "s"(cre), "s"(cim), "s"(&TAB) : "memory");
  const u32 ldtv = ((const __attribute__((address_space(4))) u32*)ldt)[d >> 1];
  STAMP(3);
  const u32x4 zero4 = {0u, 0u, 0u, 0u};
  *(u32x4*)(X + (l < 34 ? 16 * l : XREG - 16)) = zero4;
#pragma unroll
  for (int i = 0; i < 7; i++) *(u32x4*)(W + (i < 6 ? TB + 1024 * i : CHR - 1024) + 16 * l) = zero4;
  SB();
  const u32 wb0 = lds0 + w * CHR, xb0 = lds0 + OX + w * XREG;
  asm volatile("s_mov_b32 m0, %0\n\ts_nop 0\n\tds_write_addtid_b32 %1\n\tds_write_addtid_b32 %1 offset:%2" ::"s"(wb0), "v"(0x3c00u), "n"(PB) : "memory");
  typedef LDS char* lp;
  u32 ape = wb0 + ((n16 ^ 3) - 7 * (n16 >> 3)) * RS + 16 * kg, apo = wb0 + min(n16, 11) * RS + 16 * kg;
  u32x2 kb = {wb0 + ((15 - n16) & 3) * CPE + 2 * ((15 - n16) & 12) + 16 * kg, wb0 + (((n16 >> 2) & 1) + (n16 & 8) + 2 * (3 - (n16 & 3))) * RS + 16 * kg};
  u32x8 ka = {xb0 + 32 * n16 + 16 * kg, xb0 + 8 * l, wb0 + 8 * l, xb0 + 16 * l, wb0 + 16 * l,
              wb0 + 32 * n16 + 8 * (kg ^ ((l >> 2) & 3)),
              lds0 + OXF + 4 * (l & 3), wb0 + ((15 - n16) & 3) * CPO + 2 * ((15 - n16) & 12) + 8 * kg};
  asm volatile("" : "+{v70}"(ape), "+{v71}"(apo), "+{v[28:35]}"(ka), "+{v[26:27]}"(kb));
  LBAR();
  STAMP(4);
  float dt = rb(fexp((w & 1) ? hi2f(ldtv) : lo2f(ldtv)));
  asm volatile("" : "+v"(dt));
  const f32x2 cdt = {rdl(dt * -1.4426950408889634f, 0), rdl(dt * 0.15915494309189535f, 0)};
  dt = rdl(dt, 0);
  SB();
  STAMP(10);
  STAMPC(11);
  {
    u32 e, tmp, o0, o1, of;
    asm volatile(GEN_ASM
                 : [e] "=&s"(e), [t] "=&s"(tmp), "=&{v41}"(o0), "=&{v46}"(o1), "=&{v48}"(of), "+{v68}"(pr), "+{v69}"(pi), "+{v70}"(ape),
                   "+{v71}"(apo), "+{v96}"(pl), "+{v97}"(pa), "+{v[28:35]}"(ka), "+{v[26:27]}"(kb), "+{v[16:19]}"(tv), "+{v[20:23]}"(fx)
                 : [w] "s"(wb0), [c] "s"(cdt), [k] "s"(f32x2{1.4426950408889634f, 256.f})
                 : "memory", "scc", GEN_CLOBBER);
    STAMP(1);
    STAMPC(12);
    const u32 k0 = (u32)__builtin_amdgcn_readlane((int)o0, 3), k256 = (u32)__builtin_amdgcn_readlane((int)o1, 60);
    const u32 k512 = (u32)__builtin_amdgcn_readlane((int)of, 51), k1024 = (u32)__builtin_amdgcn_readlane((int)of, 55);
    *(LDS u16*)(size_t)(wb0 + (tv.z & 0xffffu)) = (u16)__builtin_amdgcn_perm((k512 & 0xffffu) | (k1024 << 16), (k0 >> 16) | (k256 << 16), tv.w);
    *(u32*)(smem + OSC + 4 * w) = __float_as_uint(__uint_as_float((e - 5) << 23) * dt) >> 16;
    fx &= ((k1024 & 0xffffu) | (k1024 << 16)) ^ 0x80008000u;
  }
  const u32 axb = ka[0], axu = ka[1], awb = ka[2], axq = ka[3], awo = ka[4], ay = ka[5], axf = ka[6], atf = ka[7], ate = kb[0];
#define FRAG(o0, o1) mkh(*(const volatile LDS u32x2*)(tb + (o0)), *(const volatile LDS u32x2*)(tb + (o1)))
  f16x8 FR[9], F1[4][2], F2[2][3], F3[4];
  const LDS char* tb = (lp)(size_t)ate;
#pragma unroll
  for (int c = 0; c < 9; c++) FR[c] = FRAG(64 * c, 64 * c + 8);
  tb = (lp)(size_t)atf + TB;
#pragma unroll
  for (int p = 0; p < 4; p++)
#pragma unroll
    for (int dl = 0; dl < 2; dl++) F1[p][dl] = FRAG(2 * (2 * C1 * p + 16 - 16 * dl), 2 * (2 * C1 * p + C1 + 16 - 16 * dl));
#pragma unroll
  for (int p = 0; p < 2; p++)
#pragma unroll
    for (int dl = 0; dl < 3; dl++) F2[p][dl] = FRAG(2 * (T2B + 2 * C2 * p + 32 - 16 * dl), 2 * (T2B + 2 * C2 * p + C2 + 32 - 16 * dl));
  *(LDS u32x4*)(size_t)(wb0 + (tv.z >> 16)) = zero4;
  STAMP(2);
#define XWAIT(n) do { _Pragma("unroll 1") for (int spin = 0; spin < SPIN; spin++) { \
      if (__all(*(const volatile LDS u32*)(size_t)axf >= (u32)(n))) break; \
      NAP(); } SB(); asm volatile("" ::: "memory"); } while (0)
  XWAIT(1);
  STAMP(5);
  STAMPC(14);
  __builtin_amdgcn_s_setprio(CPRIO);
  {
    const lp xb = (lp)(size_t)axb, xu = (lp)(size_t)axu + 2 * XO, wb = (lp)(size_t)awb + 32, xq = (lp)(size_t)axq + 512;
    const lp wo = (lp)(size_t)awo + 16, Y = (lp)(size_t)ay + OY;
    f32x4 acc[8];
    u32x4 A[NOPS];
    u32x4 Q1, Q2, W7, W9;
    u32 Q0 = 0u, Uo[8], P2[8], P4[8], W3[8], W5[8], xf = 0u, fl = 0u, fad = lds0 + OYP + 4 * w;
    u32x2 yv;
    asm volatile("" : "+v"(fad));
    auto winl = [&](auto P_) __attribute__((always_inline)) {
      constexpr int pp = decltype(P_)::value;
      Q1 = *(const LDS u32x4*)(xq + 1024 * pp);
      if (pp < 2) { Q2 = *(const LDS u32x4*)(xq + 1024 * pp + 16); Q0 = *(const LDS u32*)(xq + 1024 * pp - 4); }
      else { const u32x2 v = *(const LDS u32x2*)(xq + 1024 * pp + 16); Q2.x = v.x; Q2.y = v.y; }
    };
    auto win = [&](auto K_) __attribute__((always_inline)) {
      constexpr int k = decltype(K_)::value;
      const u32 Dd[8] = {Q0, Q1.x, Q1.y, Q1.z, Q1.w, Q2.x, Q2.y, Q2.z};
      if constexpr (k < 5) Uo[2 + k] = __builtin_amdgcn_alignbit(Dd[2 + k], Dd[1 + k], 16);
      else if constexpr (k < 9) P2[k - 3] = hadd(Dd[k - 3], Uo[k - 3]);
      else if constexpr (k < 13) W3[k - 7] = hadd(P2[k - 7], Uo[k - 6]);
      else if constexpr (k == 13) P2[6] = hadd(Dd[6], Uo[6]);
      else if constexpr (k < 18) P4[k - 11] = hadd(P2[k - 11], P2[k - 12]);
      else if constexpr (k < 22) W5[k - 16] = hadd(P4[k - 15], Dd[k - 17]);
      else if constexpr (k == 22) Uo[1] = __builtin_amdgcn_alignbit(Dd[1], Dd[0], 16);
      else if constexpr (k == 23) Uo[7] = __builtin_amdgcn_alignbit(Dd[7], Dd[6], 16);
      else if constexpr (k == 24) P2[1] = hadd(Dd[1], Uo[1]);
      else if constexpr (k == 25) P2[7] = hadd(Dd[7], Uo[7]);
      else if constexpr (k == 26) P4[2] = hadd(P2[2], P2[1]);
      else if constexpr (k == 27) P4[7] = hadd(P2[7], P2[6]);
      else if constexpr (k == 28) W3[6] = hadd(P2[6], Uo[7]);
      else if constexpr (k == 29) W5[1] = hadd(P4[2], Dd[0]);
      else if constexpr (k < 34) W7[k - 30] = hadd(W3[k - 27], P4[k - 28]);
      else W9[k - 34] = hadd(P4[k - 30], W5[k - 33]);
      if constexpr (k == 13) asm volatile("" : "+v"(P2[6]));
      if constexpr (k > 13 && k < 18) asm volatile("" : "+v"(P4[k - 11]));
      if constexpr (k >= 18 && k < 22) asm volatile("" : "+v"(W5[k - 16]));
    };
    auto loads = [&](auto M_) __attribute__((always_inline)) {
      constexpr int m = decltype(M_)::value;
      sfor<0, 2>([&](auto N_) __attribute__((always_inline)) {
        constexpr int mm = m + LA + decltype(N_)::value;
        if constexpr (mm < NSLOT) {
          constexpr int s = OPS.op[mm], k = OPS.k[s], T = OPS.a[s], p = OPS.b[s];
          if constexpr (OPS.first[s] == mm) {
            if constexpr (k == 0) { if (OPS.ld[s][1] == m) A[s] = *(const LDS u32x4*)(xb + 512 * T + 64 * p); }
            else {
              if (OPS.ld[s][0] == m) {
                const u32x2 a = trl(k == 3 ? wb + OW7 + 512 * T - 16 : k == 2 ? wb + OW3 + 512 * T - 2 * (4 + 8 * p) : xu + 512 * T - 2 * (2 + 4 * p));
                A[s].x = a.x; A[s].y = a.y;
              }
              if (OPS.ld[s][1] == m) {
                const u32x2 c = trl(k == 3 ? wb + OW9 + 512 * T - 32 : k == 2 ? wb + OW5 + 512 * T - 2 * (8 + 8 * p) : wb + OW3 + 512 * T - 2 * (4 + 4 * p));
                A[s].z = c.x; A[s].w = c.y;
              }
            }
          }
        }
      });
    };
    constexpr int FT[8] = {0, OPS.first[opat(0, 1, 0)] + 1, OPS.first[opat(0, 2, 0)] + 1, OPS.first[opat(2, 0, 0)] + 1, OPS.first[opat(0, 1, 0)] + 2,
                           OPS.first[opat(0, 2, 0)] + 2, OPS.first[opat(0, 3, 0)] + 2, OPS.first[opat(0, 4, 0)] + 2};
    loads(IC<-LA>{});
    winl(IC<0>{});
    sfor<1 - LA, 0>(loads);
    auto step = [&](auto M_) __attribute__((always_inline)) {
      constexpr int m = decltype(M_)::value, s = OPS.op[m], u = m - OPS.first[s], k = OPS.k[s], T = OPS.a[s], p = OPS.b[s];
      SB();
      const f16x8 a = ash(A[s]);
#define MM(I, x_, y_) do { if constexpr (m == FT[I]) asm volatile("v_mfma_f32_16x16x32_f16 %0, %1, %2, 0" : "=&v"(acc[I]) : "v"(x_), "v"(y_)); \
      else asm volatile("v_mfma_f32_16x16x32_f16 %0, %1, %2, %0" : "+v"(acc[I]) : "v"(x_), "v"(y_)); } while (0)
      if constexpr (k == 0) {
        if constexpr (p == 0 && T > 0 && u == 0) MM(T - 1, FR[8], a);
        else if constexpr (u == 2) MM(T + 3, fx, a);
        else MM(T, FR[p], a);
      }
      else if constexpr (k == 3) MM(T + 4 + u, a, F3[u]);
      else if constexpr (k == 2) MM(T + 2 + u, a, F2[p][u]);
      else MM(T + 1 + u, a, F1[p][u]);
      SB();
      sfor<0, 3>([&](auto N_) __attribute__((always_inline)) {
        constexpr int n = decltype(N_)::value;
        if (m == XC[n]) { if (!__all(xf >= n + 2u)) XWAIT(n + 2); }
        if (m == XC[n] - 4) xf = *(const volatile LDS u32*)(size_t)axf;
      });
      sfor<0, 7>([&](auto I_) __attribute__((always_inline)) {
        constexpr int I = decltype(I_)::value;
        if (m == OPS.end[I] + 4) {
          *(LDS u32x2*)(Y + 512 * I) = yv;
          asm volatile("ds_write_b32 %0, %1" ::"v"(fad), "v"(fl) : "memory");
          if (I == 0) STAMP(6);
          if (I == 3) STAMP(7);
        }
      });
      sfor<0, 4>([&](auto P_) __attribute__((always_inline)) {
        constexpr int pp = decltype(P_)::value;
#if !(ABL & 4)
        if (m == wslot(pp, 12) + 1) *(LDS u32x4*)(wo + 1024 * pp + OW3) = u32x4{W3[2], W3[3], W3[4], W3[5]};
        if (pp < 3 && m == wslot(pp, 21) + 1) *(LDS u32x4*)(wo + 1024 * pp + OW5) = u32x4{W5[2], W5[3], W5[4], W5[5]};
        if (pp < 2 && m == wslot(pp, 33) + 1) *(LDS u32x4*)(wo + 1024 * pp + OW7) = W7;
        if (pp < 2 && m == wslot(pp, 37) + 1) *(LDS u32x4*)(wo + 1024 * pp + OW9) = W9;
#endif
        if (m == WL[pp]) winl(P_);
        if (m == OPS.first[opat(3, 0, 0)] - 8 + pp) F3[pp] = FRAG(2 * (T3B + 48 - 16 * pp), 2 * (T3B + C3 + 48 - 16 * pp));
      });
      loads(M_);
      SB();
      sfor<0, 4>([&](auto P_) __attribute__((always_inline)) {
        constexpr int pp = decltype(P_)::value;
        if (pp < 2 && m == wslot(pp, 3)) asm volatile("" : "+v"(Q2));
        if (!(ABL & 6)) sfor<0, WN[pp]>([&](auto K_) __attribute__((always_inline)) { if constexpr (wslot(pp, decltype(K_)::value) == m) win(K_); });
      });
      sfor<0, 7>([&](auto I_) __attribute__((always_inline)) {
        constexpr int I = decltype(I_)::value;
        if (m == OPS.end[I] + 3) {
          yv = u32x2{pack2(acc[I][0], acc[I][1]), pack2(acc[I][2], acc[I][3])};
          fl = I + 1;
          asm volatile("" : "+v"(fl), "+v"(yv));
        }
      });
    };
    sfor<0, NSLOT>(step);
    SB();
    asm volatile("s_nop 7\n\ts_nop 1" : "+v"(acc[7]));
    *(LDS u32x2*)(Y + 512 * 7) = u32x2{pack2(acc[7][0], acc[7][1]), pack2(acc[7][2], acc[7][3])};
    FLAG(OYP + 4 * w, 8u);
  }
  STAMP(13);
  STAMPC(15);
  FLUSH();
  __builtin_amdgcn_endpgm();
}

extern "C" void kernel_launch(void* const* d_in, const int* in_sizes, int n_in, void* d_out, int out_size,
                              void* d_ws, size_t ws_size, hipStream_t stream) {
  const u16* const* in = (const u16* const*)d_in;
  fused<<<256, 768, 0, stream>>>(in[0], in[1], in[2], in[3], in[4], in[5], in[6], in[8], in[9], in[10], in[11],
                                 (u16*)d_out, (char*)d_ws);
}
```

```cpp
#include <hip/hip_runtime.h>
#include <stdint.h>

#define STR2(x) #x
#define STR(x) STR2(x)
#define LA 4
#define WL2 44
#define WL3 100
#define PH 1246
#define ABL 0
#define PPRIO 3
#define CPRIO 1
#define GDELAY 0
#define ROT 1
#define PRE 2
#define GNT 0
#define OAUX 0
#define LNT 1
#define PNT ""
#define LNNT ""
#define PRENT ""
#define PFO 512
#define DBAR 2
#define THR 1
#define QD 8
#define HNAP0 4
#define HNAP 2
#define KR 3
#define KI 2
#define QE 8
#define QA 5
#define NA 0
#define HOLD 1
#define RSL 5
#define DPF 0
#define HELP 0
#define WARM 0
#define HPI 0
#define Q0 6
#define NP 10
#define HW 28
#define NE 0
#define YNAP "s_sleep 2"
#define CODE_KB 24
typedef unsigned short u16;
typedef unsigned int u32;
typedef __attribute__((ext_vector_type(8))) __bf16 bf16x8;
typedef __attribute__((ext_vector_type(2))) __bf16 bf16x2;
typedef __attribute__((ext_vector_type(8))) _Float16 f16x8;
typedef __attribute__((ext_vector_type(2))) _Float16 f16x2;
typedef __attribute__((ext_vector_type(4))) short v4i16;
typedef __attribute__((ext_vector_type(4))) float f32x4;
typedef __attribute__((ext_vector_type(2))) float f32x2;
typedef __attribute__((ext_vector_type(4))) u32 u32x4;
typedef __attribute__((ext_vector_type(2))) u32 u32x2;
typedef __attribute__((ext_vector_type(8))) u32 u32x8;
typedef __attribute__((ext_vector_type(2), aligned(4))) u32 u32x2_a4;
typedef __amdgpu_buffer_rsrc_t rsrc_t;
#define LDS __attribute__((address_space(3)))

#define TLARG
#define TLVAL
#define STAMP(I)
#define STAMPC(I)
#define FLUSH()

constexpr int L = 2048, D = 1024, NM = 128, F = 3584;
constexpr u32 O_ARR = 0x1000, O_RG = 0x4000, O_SG = 0x8000, O_IDX = 0x10000, O_HG = 0x20000,
              O_PG = 0x90000, WS_END = 0x100000;
constexpr int SPIN = 1 << 16;

__device__ __forceinline__ float lo2f(u32 w) { return __uint_as_float(w << 16); }
__device__ __forceinline__ float hi2f(u32 w) { return __uint_as_float(w & 0xffff0000u); }
__device__ __forceinline__ u32 pack2(float a, float b) {
  bf16x2 r = {(__bf16)a, (__bf16)b};
  return __builtin_bit_cast(u32, r);
}
__device__ __forceinline__ u32 packh(float a, float b) {
  f16x2 r = {(_Float16)a, (_Float16)b};
  return __builtin_bit_cast(u32, r);
}
__device__ __forceinline__ float rbt(float f) { return __uint_as_float(pack2(0.f, f)); }
__device__ __forceinline__ float rb(float f) {
  float r;
  asm("v_cvt_pk_bf16_f32 %0, 0, %1" : "=v"(r) : "v"(f));
  return r;
}
__device__ __forceinline__ u32 hadd(u32 a, u32 b) {
  return __builtin_bit_cast(u32, __builtin_bit_cast(f16x2, a) + __builtin_bit_cast(f16x2, b));
}
__device__ __forceinline__ float hlo(u32 a) { return (float)__builtin_bit_cast(f16x2, a).x; }
__device__ __forceinline__ float hhi(u32 a) { return (float)__builtin_bit_cast(f16x2, a).y; }

template <int CTRL, int RM>
__device__ __forceinline__ float dppf(float v) {
  return __builtin_bit_cast(float, __builtin_amdgcn_update_dpp(0, __builtin_bit_cast(int, v), CTRL, RM, 0xf, true));
}
template <int CTRL, int RM>
__device__ __forceinline__ u32 dppu(u32 v) {
  return (u32)__builtin_amdgcn_update_dpp(0, (int)v, CTRL, RM, 0xf, true);
}
__device__ __forceinline__ float rdl(float v, int i) {
  return __builtin_bit_cast(float, __builtin_amdgcn_readlane(__builtin_bit_cast(int, v), i));
}
__device__ __forceinline__ u32 wmaxu(u32 v) {
  v = max(v, dppu<0x111, 0xf>(v)); v = max(v, dppu<0x112, 0xf>(v)); v = max(v, dppu<0x114, 0xf>(v));
  v = max(v, dppu<0x118, 0xf>(v)); v = max(v, dppu<0x142, 0xa>(v)); v = max(v, dppu<0x143, 0xc>(v));
  return (u32)__builtin_amdgcn_readlane((int)v, 63);
}
__device__ __forceinline__ void swap32(float& a, float& b) {
  auto r = __builtin_amdgcn_permlane32_swap(__float_as_uint(a), __float_as_uint(b), false, false);
  a = __uint_as_float(r[0]); b = __uint_as_float(r[1]);
}
__device__ __forceinline__ void swap16(float& a, float& b) {
  auto r = __builtin_amdgcn_permlane16_swap(__float_as_uint(a), __float_as_uint(b), false, false);
  a = __uint_as_float(r[0]); b = __uint_as_float(r[1]);
}
__device__ __forceinline__ u32x2 trread(const char* p) {
  v4i16 r = __builtin_amdgcn_ds_read_tr16_b64_v4i16((LDS v4i16*)p);
  return __builtin_bit_cast(u32x2, r);
}
__device__ __forceinline__ u32x2 trl(LDS char* p) { return __builtin_bit_cast(u32x2, __builtin_amdgcn_ds_read_tr16_b64_v4i16((LDS v4i16*)p)); }
__device__ __forceinline__ f16x8 mkh(u32x2 a, u32x2 b) {
  u32x4 v = {a.x, a.y, b.x, b.y};
  return __builtin_bit_cast(f16x8, v);
}
__device__ __forceinline__ bf16x8 mkb(u32x2 a, u32x2 b) {
  u32x4 v = {a.x, a.y, b.x, b.y};
  return __builtin_bit_cast(bf16x8, v);
}
__device__ __forceinline__ bf16x8 asb(u32x4 v) { return __builtin_bit_cast(bf16x8, v); }
__device__ __forceinline__ f16x8 ash(u32x4 v) { return __builtin_bit_cast(f16x8, v); }
#define MFMAB(a, b, c) __builtin_amdgcn_mfma_f32_16x16x32_bf16(a, b, c, 0, 0, 0)
#define MFMAH(a, b, c) __builtin_amdgcn_mfma_f32_16x16x32_f16(a, b, c, 0, 0, 0)

__device__ __forceinline__ u32x4 ld16(rsrc_t r, u32 off) { return __builtin_amdgcn_raw_buffer_load_b128(r, off, 0, 16); }
__device__ __forceinline__ u32x2 ld8(rsrc_t r, u32 off) { return __builtin_amdgcn_raw_buffer_load_b64(r, off, 0, 16); }
__device__ __forceinline__ void st16(rsrc_t r, u32 off, u32x4 v) { __builtin_amdgcn_raw_buffer_store_b128(v, r, off, 0, 16); }
__device__ __forceinline__ void st8(rsrc_t r, u32 off, u32x2 v) { __builtin_amdgcn_raw_buffer_store_b64(v, r, off, 0, 16); }
#define NAP() asm volatile("s_sleep 1" ::: "memory")
#define SB() __builtin_amdgcn_sched_barrier(0)
#define LBAR() asm volatile("s_waitcnt lgkmcnt(0)\n\ts_barrier" ::: "memory")

__device__ __forceinline__ float fexp(float v) { return __builtin_amdgcn_exp2f(v * 1.4426950408889634f); }
__device__ __forceinline__ float dot2b(u32 a, u32 b, float c) {
  return __builtin_amdgcn_fdot2_f32_bf16(__builtin_bit_cast(bf16x2, a), __builtin_bit_cast(bf16x2, b), c, false);
}
__device__ __forceinline__ float sigm(float v) { return __builtin_amdgcn_rcpf(1.f + __builtin_amdgcn_exp2f(-1.4426950408889634f * v)); }

constexpr int RS = 288, PB = 16 * RS;
constexpr int OW3 = 0, OW5 = 3632, OW7 = 6720, OW9 = 8784, OY = 11648, CHR = OY + 4096;
constexpr int OX = 8 * CHR, XREG = 4640, XO = 266;
constexpr int OMISC = OX + 8 * XREG;
constexpr int OYP = OMISC + 256, OXF = OMISC + 320, OCN = OMISC + 336, OSC = OMISC + 352, OIX = OMISC + 384;
constexpr int SMEM = OMISC + 512;
constexpr int CPE = 704, TB = 2 * PB, CPO = 576;
constexpr int T2B = 2 * CPO + 4, T3B = (OY + 2048 - TB) / 2, C1 = 36, C2 = 52, C3 = 84;
constexpr int FA = 131072, AP = 1056, FH = FA + 8 * AP, HP = 544, FC = FH + 8 * HP;
static_assert(FC + 4480 <= SMEM && SMEM <= 163840, "lds map");

struct Tab { u32 v[2][64][4]; };
constexpr Tab mktab() {
  Tab T = {};
  for (int l = 0; l < 64; l++) {
    const int n16 = l & 15, kg = l >> 4, pc = ((n16 >> 2) & 1) + (n16 & 8) + 2 * (3 - (n16 & 3)), odd = kg >> 1, s = l & 3;
    const int e0 = 278 - 128 - 8 * pc - 4 * (kg & 1) - odd;
    const int ez = ((l >> 3) & 3) * CPE + ((l & 4) ? 544 - 64 : 0) + 16 * (l & 7);
    const int b1 = 28 + C1 * (n16 + 4 * (kg & 1)), b2 = T2B + 44 + C2 * ((n16 & 1) + 2 * (kg & 1)) - 8 * (n16 & 2);
    const int b3 = T3B + 60 + C3 * (kg & 1) - 16 * ((n16 < 11 ? n16 : 11) - 8);
    const int to = TB + 2 * ((n16 < 4 ? b1 : n16 < 8 ? b2 : b3) - 4 * odd - 8);
    const int sp = n16 < 8 ? s * (CPE - 2) + 2 * ((l & 4) ? 281 - 257 : (s & 1) ? 281 : 281 - 256) : TB + s * (CPO - 2) + 2 * ((l & 4) ? T2B + 15 : 15);
    T.v[0][l][0] = (2 * e0 + (2 - odd) * (CPE - 2)) | (2 * e0 + 3 * odd * (CPE - 2)) << 16;
    T.v[0][l][1] = ez | to << 16;
    T.v[0][l][2] = sp | (s == 0 ? OW3 : s == 1 ? OW5 : s == 2 ? OW7 : OW9) << 16;
    T.v[0][l][3] = n16 < 8 ? ((l & 5) == 1 ? 0x0100u : 0x0302u) : (l & 4) ? 0x0706u : 0x0504u;
    const int jx = n16 + 5 - 8 * kg;
    for (int r = 0; r < 4; r++) T.v[1][l][r] = jx == 2 * r ? 0xffffu : jx == 2 * r + 1 ? 0xffff0000u : 0u;
  }
  return T;
}
extern "C" __device__ const __attribute__((aligned(128), used, visibility("hidden"))) Tab lane_tab = mktab();

#define GEN_ASM \
  "s_waitcnt vmcnt(2)\n\tv_lshlrev_b32 v56, 16, v96\n\t" \
  "v_and_b32 v57, 0xffff0000, v96\n\tv_pk_mul_f32 v[56:57], %[k], v[56:57] op_sel_hi:[0,1]\n\t" \
  "v_lshlrev_b32 v49, 16, v97\n\tv_exp_f32 v56, v56\n\t" \
  "v_exp_f32 v57, v57\n\tv_and_b32 v51, 0xffff0000, v97\n\t" \
  "v_cvt_pk_bf16_f32 v56, v56, v57\n\tv_lshlrev_b32 v48, 16, v56\n\t" \
  "v_and_b32 v50, 0xffff0000, v56\n\tv_pk_mul_f32 v[48:49], %[c], v[48:49]\n\t" \
  "v_pk_mul_f32 v[50:51], %[c], v[50:51]\n\tv_pk_mul_f32 v[98:99], %[k], v[48:49] op_sel:[1,0]\n\t" \
  "v_pk_mul_f32 v[100:101], %[k], v[50:51] op_sel:[1,0]\n\tv_fract_f32 v49, v49\n\t" \
  "v_fract_f32 v51, v51\n\tv_exp_f32 v48, v48\n\t" \
  "v_exp_f32 v50, v50\n\tv_cos_f32 v40, v49\n\t" \
  "v_sin_f32 v41, v49\n\tv_cos_f32 v42, v51\n\t" \
  "v_sin_f32 v43, v51\n\tv_pk_mul_f32 v[40:41], v[40:41], v[48:49] op_sel_hi:[1,0]\n\t" \
  "v_pk_mul_f32 v[42:43], v[42:43], v[50:51] op_sel_hi:[1,0]\n\tv_pk_mul_f32 v[48:49], v[40:41], v[40:41] op_sel:[1,1] op_sel_hi:[1,0] neg_lo:[1,0]\n\t" \
  "v_pk_mul_f32 v[50:51], v[42:43], v[42:43] op_sel:[1,1] op_sel_hi:[1,0] neg_lo:[1,0]\n\tv_pk_fma_f32 v[48:49], v[40:41], v[40:41], v[48:49] op_sel:[0,0,0] op_sel_hi:[0,1,1]\n\t" \
  "v_pk_fma_f32 v[50:51], v[42:43], v[42:43], v[50:51] op_sel:[0,0,0] op_sel_hi:[0,1,1]\n\tv_cvt_pk_f16_f32 v72, v40, -v41\n\t" \
  "v_cvt_pk_f16_f32 v73, v42, -v43\n\tv_pk_mul_f32 v[52:53], v[48:49], v[40:41] op_sel:[1,1] op_sel_hi:[1,0] neg_lo:[1,0]\n\t" \
  "v_pk_mul_f32 v[54:55], v[50:51], v[42:43] op_sel:[1,1] op_sel_hi:[1,0] neg_lo:[1,0]\n\tv_pk_fma_f32 v[52:53], v[48:49], v[40:41], v[52:53] op_sel:[0,0,0] op_sel_hi:[0,1,1]\n\t" \
  "v_pk_fma_f32 v[54:55], v[50:51], v[42:43], v[54:55] op_sel:[0,0,0] op_sel_hi:[0,1,1]\n\tv_pk_mul_f32 v[56:57], v[48:49], v[48:49] op_sel:[1,1] op_sel_hi:[1,0] neg_lo:[1,0]\n\t" \
  "v_pk_mul_f32 v[58:59], v[50:51], v[50:51] op_sel:[1,1] op_sel_hi:[1,0] neg_lo:[1,0]\n\tv_pk_fma_f32 v[56:57], v[48:49], v[48:49], v[56:57] op_sel:[0,0,0] op_sel_hi:[0,1,1]\n\t" \
  "v_pk_fma_f32 v[58:59], v[50:51], v[50:51], v[58:59] op_sel:[0,0,0] op_sel_hi:[0,1,1]\n\tv_cvt_pk_f16_f32 v74, v48, -v49\n\t" \
  "v_cvt_pk_f16_f32 v75, v50, -v51\n\tv_cvt_pk_f16_f32 v76, v52, -v53\n\t" \
  "v_cvt_pk_f16_f32 v77, v54, -v55\n\tv_pk_mul_f32 v[52:53], v[56:57], v[48:49] op_sel:[1,1] op_sel_hi:[1,0] neg_lo:[1,0]\n\t" \
  "v_pk_mul_f32 v[54:55], v[58:59], v[50:51] op_sel:[1,1] op_sel_hi:[1,0] neg_lo:[1,0]\n\tv_pk_fma_f32 v[52:53], v[56:57], v[48:49], v[52:53] op_sel:[0,0,0] op_sel_hi:[0,1,1]\n\t" \
  "v_pk_fma_f32 v[54:55], v[58:59], v[50:51], v[54:55] op_sel:[0,0,0] op_sel_hi:[0,1,1]\n\tv_pk_mul_f32 v[60:61], v[56:57], v[56:57] op_sel:[1,1] op_sel_hi:[1,0] neg_lo:[1,0]\n\t" \
  "v_pk_mul_f32 v[62:63], v[58:59], v[58:59] op_sel:[1,1] op_sel_hi:[1,0] neg_lo:[1,0]\n\tv_pk_fma_f32 v[60:61], v[56:57], v[56:57], v[60:61] op_sel:[0,0,0] op_sel_hi:[0,1,1]\n\t" \
  "v_pk_fma_f32 v[62:63], v[58:59], v[58:59], v[62:63] op_sel:[0,0,0] op_sel_hi:[0,1,1]\n\tv_cvt_pk_f16_f32 v78, v56, -v57\n\t" \
  "v_cvt_pk_f16_f32 v79, v58, -v59\n\tv_cvt_pk_f16_f32 v82, v52, -v53\n\t" \
  "v_cvt_pk_f16_f32 v83, v54, -v55\n\tv_pk_mul_f16 v80, v78, v72 op_sel:[1,1] op_sel_hi:[1,0] neg_lo:[1,0]\n\t" \
  "v_pk_mul_f16 v81, v79, v73 op_sel:[1,1] op_sel_hi:[1,0] neg_lo:[1,0]\n\tv_pk_fma_f16 v80, v78, v72, v80 op_sel:[0,0,0] op_sel_hi:[0,1,1]\n\t" \
  "v_pk_fma_f16 v81, v79, v73, v81 op_sel:[0,0,0] op_sel_hi:[0,1,1]\n\tv_pk_mul_f16 v84, v78, v76 op_sel:[1,1] op_sel_hi:[1,0] neg_lo:[1,0]\n\t" \
  "v_pk_mul_f16 v85, v79, v77 op_sel:[1,1] op_sel_hi:[1,0] neg_lo:[1,0]\n\tv_pk_fma_f16 v84, v78, v76, v84 op_sel:[0,0,0] op_sel_hi:[0,1,1]\n\t" \
  "v_pk_fma_f16 v85, v79, v77, v85 op_sel:[0,0,0] op_sel_hi:[0,1,1]\n\tv_cvt_pk_f16_f32 v86, v60, -v61\n\t" \
  "v_cvt_pk_f16_f32 v87, v62, -v63\n\tds_write_addtid_b32 v72 offset:288\n\t" \
  "ds_write_addtid_b32 v73 offset:4896\n\tds_write_addtid_b32 v74 offset:576\n\t" \
  "ds_write_addtid_b32 v75 offset:5184\n\tds_write_addtid_b32 v76 offset:864\n\t" \
  "ds_write_addtid_b32 v77 offset:5472\n\tds_write_addtid_b32 v78 offset:1152\n\t" \
  "ds_write_addtid_b32 v79 offset:5760\n\tds_write_addtid_b32 v80 offset:1440\n\t" \
  "ds_write_addtid_b32 v81 offset:6048\n\tds_write_addtid_b32 v82 offset:1728\n\t" \
  "ds_write_addtid_b32 v83 offset:6336\n\tds_write_addtid_b32 v84 offset:2016\n\t" \
  "ds_write_addtid_b32 v85 offset:6624\n\tds_write_addtid_b32 v86 offset:2304\n\t" \
  "ds_write_addtid_b32 v87 offset:6912\n\tds_read_b128 v[104:107], v70 offset:0\n\t" \
  "ds_read_b128 v[108:111], v70 offset:64\n\tds_read_b128 v[112:115], v70 offset:128\n\t" \
  "ds_read_b128 v[116:119], v70 offset:192\n\tds_read_b128 v[120:123], v70 offset:4608\n\t" \
  "ds_read_b128 v[124:127], v70 offset:4672\n\tds_read_b128 v[128:131], v70 offset:4736\n\t" \
  "ds_read_b128 v[132:135], v70 offset:4800\n\tv_fract_f32 v99, v99\n\t" \
  "v_fract_f32 v101, v101\n\tv_exp_f32 v98, v98\n\t" \
  "v_exp_f32 v100, v100\n\tv_cos_f32 v44, v99\n\t" \
  "v_sin_f32 v45, v99\n\tv_cos_f32 v46, v101\n\t" \
  "v_sin_f32 v47, v101\n\tv_pk_mul_f32 v[44:45], v[44:45], v[98:99] op_sel_hi:[1,0]\n\t" \
  "v_pk_mul_f32 v[46:47], v[46:47], v[100:101] op_sel_hi:[1,0]\n\ts_waitcnt vmcnt(0)\n\t" \
  "v_pk_mul_f32 v[48:49], v[44:45], v[44:45] op_sel:[1,1] op_sel_hi:[1,0] neg_lo:[1,0]\n\tv_pk_mul_f32 v[50:51], v[46:47], v[46:47] op_sel:[1,1] op_sel_hi:[1,0] neg_lo:[1,0]\n\t" \
  "v_pk_fma_f32 v[48:49], v[44:45], v[44:45], v[48:49] op_sel:[0,0,0] op_sel_hi:[0,1,1]\n\tv_pk_fma_f32 v[50:51], v[46:47], v[46:47], v[50:51] op_sel:[0,0,0] op_sel_hi:[0,1,1]\n\t" \
  "v_and_b32 v102, 0x7fff7fff, v68\n\tv_cvt_pk_f16_f32 v88, v44, -v45\n\t" \
  "v_cvt_pk_f16_f32 v89, v46, -v47\n\tv_pk_mul_f32 v[52:53], v[48:49], v[44:45] op_sel:[1,1] op_sel_hi:[1,0] neg_lo:[1,0]\n\t" \
  "v_pk_mul_f32 v[54:55], v[50:51], v[46:47] op_sel:[1,1] op_sel_hi:[1,0] neg_lo:[1,0]\n\tv_and_b32 v103, 0x7fff7fff, v69\n\t" \
  "v_pk_fma_f32 v[52:53], v[48:49], v[44:45], v[52:53] op_sel:[0,0,0] op_sel_hi:[0,1,1]\n\tv_pk_fma_f32 v[54:55], v[50:51], v[46:47], v[54:55] op_sel:[0,0,0] op_sel_hi:[0,1,1]\n\t" \
  "v_pk_mul_f32 v[56:57], v[48:49], v[48:49] op_sel:[1,1] op_sel_hi:[1,0] neg_lo:[1,0]\n\tv_pk_mul_f32 v[58:59], v[50:51], v[50:51] op_sel:[1,1] op_sel_hi:[1,0] neg_lo:[1,0]\n\t" \
  "v_pk_max_u16 v102, v102, v103\n\tv_pk_fma_f32 v[56:57], v[48:49], v[48:49], v[56:57] op_sel:[0,0,0] op_sel_hi:[0,1,1]\n\t" \
  "v_pk_fma_f32 v[58:59], v[50:51], v[50:51], v[58:59] op_sel:[0,0,0] op_sel_hi:[0,1,1]\n\tv_cvt_pk_f16_f32 v90, v48, -v49\n\t" \
  "v_cvt_pk_f16_f32 v91, v50, -v51\n\tv_pk_max_u16 v102, v102, v102 op_sel:[1,0] op_sel_hi:[0,1]\n\t" \
  "v_pk_mul_f32 v[64:65], v[56:57], v[60:61] op_sel:[1,1] op_sel_hi:[1,0] neg_lo:[1,0]\n\tv_pk_mul_f32 v[66:67], v[58:59], v[62:63] op_sel:[1,1] op_sel_hi:[1,0] neg_lo:[1,0]\n\t" \
  "v_pk_fma_f32 v[64:65], v[56:57], v[60:61], v[64:65] op_sel:[0,0,0] op_sel_hi:[0,1,1]\n\tv_pk_fma_f32 v[66:67], v[58:59], v[62:63], v[66:67] op_sel:[0,0,0] op_sel_hi:[0,1,1]\n\t" \
  "v_max_u32_dpp v102, v102, v102 row_shr:1 row_mask:0xf bank_mask:0xf\n\tv_cvt_pk_f16_f32 v92, v52, -v53\n\t" \
  "v_cvt_pk_f16_f32 v93, v54, -v55\n\tv_pk_mul_f16 v136, v88, v74 op_sel:[1,1] op_sel_hi:[1,0] neg_lo:[1,0]\n\t" \
  "v_pk_mul_f16 v137, v89, v75 op_sel:[1,1] op_sel_hi:[1,0] neg_lo:[1,0]\n\tv_max_u32_dpp v102, v102, v102 row_shr:2 row_mask:0xf bank_mask:0xf\n\t" \
  "v_pk_fma_f16 v136, v88, v74, v136 op_sel:[0,0,0] op_sel_hi:[0,1,1]\n\tv_pk_fma_f16 v137, v89, v75, v137 op_sel:[0,0,0] op_sel_hi:[0,1,1]\n\t" \
  "v_pk_mul_f16 v138, v88, v78 op_sel:[1,1] op_sel_hi:[1,0] neg_lo:[1,0]\n\tv_pk_mul_f16 v139, v89, v79 op_sel:[1,1] op_sel_hi:[1,0] neg_lo:[1,0]\n\t" \
  "v_max_u32_dpp v102, v102, v102 row_shr:4 row_mask:0xf bank_mask:0xf\n\tv_pk_fma_f16 v138, v88, v78, v138 op_sel:[0,0,0] op_sel_hi:[0,1,1]\n\t" \
  "v_pk_fma_f16 v139, v89, v79, v139 op_sel:[0,0,0] op_sel_hi:[0,1,1]\n\tv_cvt_pk_f16_f32 v94, v64, -v65\n\t" \
  "v_cvt_pk_f16_f32 v95, v66, -v67\n\tv_max_u32_dpp v102, v102, v102 row_shr:8 row_mask:0xf bank_mask:0xf\n\t" \
  "v_pk_mul_f16 v140, v88, v82 op_sel:[1,1] op_sel_hi:[1,0] neg_lo:[1,0]\n\tv_pk_mul_f16 v141, v89, v83 op_sel:[1,1] op_sel_hi:[1,0] neg_lo:[1,0]\n\t" \
  "v_pk_fma_f16 v140, v88, v82, v140 op_sel:[0,0,0] op_sel_hi:[0,1,1]\n\tv_pk_fma_f16 v141, v89, v83, v141 op_sel:[0,0,0] op_sel_hi:[0,1,1]\n\t" \
  "v_max_u32_dpp v102, v102, v102 row_bcast:15 row_mask:0xa bank_mask:0xf\n\tv_pk_mul_f16 v142, v88, v86 op_sel:[1,1] op_sel_hi:[1,0] neg_lo:[1,0]\n\t" \
  "v_pk_mul_f16 v143, v89, v87 op_sel:[1,1] op_sel_hi:[1,0] neg_lo:[1,0]\n\tv_pk_fma_f16 v142, v88, v86, v142 op_sel:[0,0,0] op_sel_hi:[0,1,1]\n\t" \
  "v_pk_fma_f16 v143, v89, v87, v143 op_sel:[0,0,0] op_sel_hi:[0,1,1]\n\tv_max_u32_dpp v102, v102, v102 row_bcast:31 row_mask:0xc bank_mask:0xf\n\t" \
  "v_pk_mul_f16 v144, v90, v78 op_sel:[1,1] op_sel_hi:[1,0] neg_lo:[1,0]\n\tv_pk_mul_f16 v145, v91, v79 op_sel:[1,1] op_sel_hi:[1,0] neg_lo:[1,0]\n\t" \
  "v_pk_fma_f16 v144, v90, v78, v144 op_sel:[0,0,0] op_sel_hi:[0,1,1]\n\tv_pk_fma_f16 v145, v91, v79, v145 op_sel:[0,0,0] op_sel_hi:[0,1,1]\n\t" \
  "v_readlane_b32 %[e], v102, 63\n\tv_pk_mul_f16 v146, v90, v86 op_sel:[1,1] op_sel_hi:[1,0] neg_lo:[1,0]\n\t" \
  "v_pk_mul_f16 v147, v91, v87 op_sel:[1,1] op_sel_hi:[1,0] neg_lo:[1,0]\n\tv_pk_fma_f16 v146, v90, v86, v146 op_sel:[0,0,0] op_sel_hi:[0,1,1]\n\t" \
  "v_pk_fma_f16 v147, v91, v87, v147 op_sel:[0,0,0] op_sel_hi:[0,1,1]\n\tv_pk_mul_f16 v148, v92, v78 op_sel:[1,1] op_sel_hi:[1,0] neg_lo:[1,0]\n\t" \
  "v_pk_mul_f16 v149, v93, v79 op_sel:[1,1] op_sel_hi:[1,0] neg_lo:[1,0]\n\tv_pk_fma_f16 v148, v92, v78, v148 op_sel:[0,0,0] op_sel_hi:[0,1,1]\n\t" \
  "v_pk_fma_f16 v149, v93, v79, v149 op_sel:[0,0,0] op_sel_hi:[0,1,1]\n\tv_pk_mul_f16 v150, v92, v86 op_sel:[1,1] op_sel_hi:[1,0] neg_lo:[1,0]\n\t" \
  "v_pk_mul_f16 v151, v93, v87 op_sel:[1,1] op_sel_hi:[1,0] neg_lo:[1,0]\n\tv_pk_fma_f16 v150, v92, v86, v150 op_sel:[0,0,0] op_sel_hi:[0,1,1]\n\t" \
  "v_pk_fma_f16 v151, v93, v87, v151 op_sel:[0,0,0] op_sel_hi:[0,1,1]\n\tv_pk_mul_f16 v96, v94, v88 op_sel:[1,1] op_sel_hi:[1,0] neg_lo:[1,0]\n\t" \
  "v_pk_mul_f16 v97, v95, v89 op_sel:[1,1] op_sel_hi:[1,0] neg_lo:[1,0]\n\tv_pk_fma_f16 v96, v94, v88, v96 op_sel:[0,0,0] op_sel_hi:[0,1,1]\n\t" \
  "v_pk_fma_f16 v97, v95, v89, v97 op_sel:[0,0,0] op_sel_hi:[0,1,1]\n\tv_pk_mul_f16 v98, v94, v90 op_sel:[1,1] op_sel_hi:[1,0] neg_lo:[1,0]\n\t" \
  "v_pk_mul_f16 v99, v95, v91 op_sel:[1,1] op_sel_hi:[1,0] neg_lo:[1,0]\n\tv_pk_fma_f16 v98, v94, v90, v98 op_sel:[0,0,0] op_sel_hi:[0,1,1]\n\t" \
  "v_pk_fma_f16 v99, v95, v91, v99 op_sel:[0,0,0] op_sel_hi:[0,1,1]\n\tv_pk_mul_f16 v100, v94, v92 op_sel:[1,1] op_sel_hi:[1,0] neg_lo:[1,0]\n\t" \
  "v_pk_mul_f16 v101, v95, v93 op_sel:[1,1] op_sel_hi:[1,0] neg_lo:[1,0]\n\tv_pk_fma_f16 v100, v94, v92, v100 op_sel:[0,0,0] op_sel_hi:[0,1,1]\n\t" \
  "v_pk_fma_f16 v101, v95, v93, v101 op_sel:[0,0,0] op_sel_hi:[0,1,1]\n\ts_bfe_u32 %[e], %[e], 0x80007\n\t" \
  "s_max_u32 %[e], %[e], 8\n\ts_sub_u32 %[t], 260, %[e]\n\t" \
  "s_lshl_b32 %[t], %[t], 23\n\tds_write_addtid_b32 v136 offset:0\n\t" \
  "ds_write_addtid_b32 v137 offset:4608\n\tds_write_addtid_b32 v138 offset:288\n\t" \
  "ds_write_addtid_b32 v139 offset:4896\n\tds_write_addtid_b32 v140 offset:576\n\t" \
  "ds_write_addtid_b32 v141 offset:5184\n\tds_write_addtid_b32 v142 offset:864\n\t" \
  "ds_write_addtid_b32 v143 offset:5472\n\tds_write_addtid_b32 v144 offset:1152\n\t" \
  "ds_write_addtid_b32 v145 offset:5760\n\tds_write_addtid_b32 v146 offset:1440\n\t" \
  "ds_write_addtid_b32 v147 offset:6048\n\tds_write_addtid_b32 v148 offset:1728\n\t" \
  "ds_write_addtid_b32 v149 offset:6336\n\tds_write_addtid_b32 v150 offset:2016\n\t" \
  "ds_write_addtid_b32 v151 offset:6624\n\tds_write_addtid_b32 v94 offset:2304\n\t" \
  "ds_write_addtid_b32 v95 offset:6912\n\tds_write_addtid_b32 v96 offset:2592\n\t" \
  "ds_write_addtid_b32 v97 offset:7200\n\tds_write_addtid_b32 v98 offset:2880\n\t" \
  "ds_write_addtid_b32 v99 offset:7488\n\tds_write_addtid_b32 v100 offset:3168\n\t" \
  "ds_write_addtid_b32 v101 offset:7776\n\tds_read_b128 v[136:139], v71 offset:0\n\t" \
  "ds_read_b128 v[140:143], v71 offset:64\n\tds_read_b128 v[144:147], v71 offset:128\n\t" \
  "ds_read_b128 v[148:151], v71 offset:192\n\tds_read_b128 v[152:155], v71 offset:4608\n\t" \
  "ds_read_b128 v[156:159], v71 offset:4672\n\tds_read_b128 v[160:163], v71 offset:4736\n\t" \
  "ds_read_b128 v[164:167], v71 offset:4800\n\tv_pk_mul_f32 v[48:49], v[60:61], v[60:61] op_sel:[1,1] op_sel_hi:[1,0] neg_lo:[1,0]\n\t" \
  "v_pk_mul_f32 v[50:51], v[62:63], v[62:63] op_sel:[1,1] op_sel_hi:[1,0] neg_lo:[1,0]\n\tv_pk_fma_f32 v[48:49], v[60:61], v[60:61], v[48:49] op_sel:[0,0,0] op_sel_hi:[0,1,1]\n\t" \
  "v_pk_fma_f32 v[50:51], v[62:63], v[62:63], v[50:51] op_sel:[0,0,0] op_sel_hi:[0,1,1]\n\tv_cvt_pk_f16_f32 v44, v60, v61\n\t" \
  "v_cvt_pk_f16_f32 v45, v62, v63\n\tv_pk_mul_f32 v[52:53], v[48:49], v[60:61] op_sel:[1,1] op_sel_hi:[1,0] neg_lo:[1,0]\n\t" \
  "v_pk_mul_f32 v[54:55], v[50:51], v[62:63] op_sel:[1,1] op_sel_hi:[1,0] neg_lo:[1,0]\n\tv_pk_fma_f32 v[52:53], v[48:49], v[60:61], v[52:53] op_sel:[0,0,0] op_sel_hi:[0,1,1]\n\t" \
  "v_pk_fma_f32 v[54:55], v[50:51], v[62:63], v[54:55] op_sel:[0,0,0] op_sel_hi:[0,1,1]\n\tv_pk_mul_f32 v[40:41], v[48:49], v[48:49] op_sel:[1,1] op_sel_hi:[1,0] neg_lo:[1,0]\n\t" \
  "v_pk_mul_f32 v[42:43], v[50:51], v[50:51] op_sel:[1,1] op_sel_hi:[1,0] neg_lo:[1,0]\n\tv_pk_fma_f32 v[40:41], v[48:49], v[48:49], v[40:41] op_sel:[0,0,0] op_sel_hi:[0,1,1]\n\t" \
  "v_pk_fma_f32 v[42:43], v[50:51], v[50:51], v[42:43] op_sel:[0,0,0] op_sel_hi:[0,1,1]\n\tv_cvt_pk_f16_f32 v46, v48, v49\n\t" \
  "v_cvt_pk_f16_f32 v47, v50, v51\n\tv_cvt_pk_f16_f32 v56, v52, v53\n\t" \
  "v_cvt_pk_f16_f32 v57, v54, v55\n\tv_lshlrev_b32 v64, 16, v68\n\t" \
  "v_lshlrev_b32 v65, 16, v69\n\tv_and_b32 v66, 0xffff0000, v68\n\t" \
  "v_and_b32 v67, 0xffff0000, v69\n\tv_mul_f32 v64, %[t], v64\n\t" \
  "v_mul_f32 v65, %[t], v65\n\tv_mul_f32 v66, %[t], v66\n\t" \
  "v_mul_f32 v67, %[t], v67\n\tv_cvt_pk_f16_f32 v48, v64, v65\n\t" \
  "v_cvt_pk_f16_f32 v49, v66, v67\n\tv_pk_mul_f32 v[68:69], v[64:65], v[40:41] op_sel:[1,1] op_sel_hi:[1,0] neg_lo:[1,0]\n\t" \
  "v_pk_mul_f32 v[70:71], v[66:67], v[42:43] op_sel:[1,1] op_sel_hi:[1,0] neg_lo:[1,0]\n\tv_pk_fma_f32 v[68:69], v[64:65], v[40:41], v[68:69] op_sel:[0,0,0] op_sel_hi:[0,1,1]\n\t" \
  "v_pk_fma_f32 v[70:71], v[66:67], v[42:43], v[70:71] op_sel:[0,0,0] op_sel_hi:[0,1,1]\n\tv_pk_mul_f16 v50, v48, v44 op_sel:[1,1] op_sel_hi:[1,0] neg_lo:[1,0]\n\t" \
  "v_pk_mul_f16 v51, v49, v45 op_sel:[1,1] op_sel_hi:[1,0] neg_lo:[1,0]\n\tv_pk_fma_f16 v50, v48, v44, v50 op_sel:[0,0,0] op_sel_hi:[0,1,1]\n\t" \
  "v_pk_fma_f16 v51, v49, v45, v51 op_sel:[0,0,0] op_sel_hi:[0,1,1]\n\tv_pk_mul_f16 v52, v48, v46 op_sel:[1,1] op_sel_hi:[1,0] neg_lo:[1,0]\n\t" \
  "v_pk_mul_f16 v53, v49, v47 op_sel:[1,1] op_sel_hi:[1,0] neg_lo:[1,0]\n\tv_pk_fma_f16 v52, v48, v46, v52 op_sel:[0,0,0] op_sel_hi:[0,1,1]\n\t" \
  "v_pk_fma_f16 v53, v49, v47, v53 op_sel:[0,0,0] op_sel_hi:[0,1,1]\n\tv_pk_mul_f16 v54, v48, v56 op_sel:[1,1] op_sel_hi:[1,0] neg_lo:[1,0]\n\t" \
  "v_pk_mul_f16 v55, v49, v57 op_sel:[1,1] op_sel_hi:[1,0] neg_lo:[1,0]\n\tv_pk_fma_f16 v54, v48, v56, v54 op_sel:[0,0,0] op_sel_hi:[0,1,1]\n\t" \
  "v_pk_fma_f16 v55, v49, v57, v55 op_sel:[0,0,0] op_sel_hi:[0,1,1]\n\tds_write_addtid_b32 v48 offset:0\n\t" \
  "ds_write_addtid_b32 v49 offset:4608\n\tds_write_addtid_b32 v50 offset:288\n\t" \
  "ds_write_addtid_b32 v51 offset:4896\n\tds_write_addtid_b32 v52 offset:576\n\t" \
  "ds_write_addtid_b32 v53 offset:5184\n\tds_write_addtid_b32 v54 offset:864\n\t" \
  "ds_write_addtid_b32 v55 offset:5472\n\tv_cvt_pk_f16_f32 v48, v68, v69\n\t" \
  "v_cvt_pk_f16_f32 v49, v70, v71\n\tv_pk_mul_f32 v[64:65], v[68:69], v[40:41] op_sel:[1,1] op_sel_hi:[1,0] neg_lo:[1,0]\n\t" \
  "v_pk_mul_f32 v[66:67], v[70:71], v[42:43] op_sel:[1,1] op_sel_hi:[1,0] neg_lo:[1,0]\n\tv_pk_fma_f32 v[64:65], v[68:69], v[40:41], v[64:65] op_sel:[0,0,0] op_sel_hi:[0,1,1]\n\t" \
  "v_pk_fma_f32 v[66:67], v[70:71], v[42:43], v[66:67] op_sel:[0,0,0] op_sel_hi:[0,1,1]\n\tv_pk_mul_f16 v50, v48, v44 op_sel:[1,1] op_sel_hi:[1,0] neg_lo:[1,0]\n\t" \
  "v_pk_mul_f16 v51, v49, v45 op_sel:[1,1] op_sel_hi:[1,0] neg_lo:[1,0]\n\tv_pk_fma_f16 v50, v48, v44, v50 op_sel:[0,0,0] op_sel_hi:[0,1,1]\n\t" \
  "v_pk_fma_f16 v51, v49, v45, v51 op_sel:[0,0,0] op_sel_hi:[0,1,1]\n\tv_pk_mul_f16 v52, v48, v46 op_sel:[1,1] op_sel_hi:[1,0] neg_lo:[1,0]\n\t" \
  "v_pk_mul_f16 v53, v49, v47 op_sel:[1,1] op_sel_hi:[1,0] neg_lo:[1,0]\n\tv_pk_fma_f16 v52, v48, v46, v52 op_sel:[0,0,0] op_sel_hi:[0,1,1]\n\t" \
  "v_pk_fma_f16 v53, v49, v47, v53 op_sel:[0,0,0] op_sel_hi:[0,1,1]\n\tv_pk_mul_f16 v54, v48, v56 op_sel:[1,1] op_sel_hi:[1,0] neg_lo:[1,0]\n\t" \
  "v_pk_mul_f16 v55, v49, v57 op_sel:[1,1] op_sel_hi:[1,0] neg_lo:[1,0]\n\tv_pk_fma_f16 v54, v48, v56, v54 op_sel:[0,0,0] op_sel_hi:[0,1,1]\n\t" \
  "v_pk_fma_f16 v55, v49, v57, v55 op_sel:[0,0,0] op_sel_hi:[0,1,1]\n\tds_write_addtid_b32 v48 offset:1152\n\t" \
  "ds_write_addtid_b32 v49 offset:5760\n\tds_write_addtid_b32 v50 offset:1440\n\t" \
  "ds_write_addtid_b32 v51 offset:6048\n\tds_write_addtid_b32 v52 offset:1728\n\t" \
  "ds_write_addtid_b32 v53 offset:6336\n\tds_write_addtid_b32 v54 offset:2016\n\t" \
  "ds_write_addtid_b32 v55 offset:6624\n\tv_cvt_pk_f16_f32 v48, v64, v65\n\t" \
  "v_cvt_pk_f16_f32 v49, v66, v67\n\tv_pk_mul_f32 v[68:69], v[64:65], v[40:41] op_sel:[1,1] op_sel_hi:[1,0] neg_lo:[1,0]\n\t" \
  "v_pk_mul_f32 v[70:71], v[66:67], v[42:43] op_sel:[1,1] op_sel_hi:[1,0] neg_lo:[1,0]\n\tv_pk_fma_f32 v[68:69], v[64:65], v[40:41], v[68:69] op_sel:[0,0,0] op_sel_hi:[0,1,1]\n\t" \
  "v_pk_fma_f32 v[70:71], v[66:67], v[42:43], v[70:71] op_sel:[0,0,0] op_sel_hi:[0,1,1]\n\tv_pk_mul_f16 v50, v48, v44 op_sel:[1,1] op_sel_hi:[1,0] neg_lo:[1,0]\n\t" \
  "v_pk_mul_f16 v51, v49, v45 op_sel:[1,1] op_sel_hi:[1,0] neg_lo:[1,0]\n\tv_pk_fma_f16 v50, v48, v44, v50 op_sel:[0,0,0] op_sel_hi:[0,1,1]\n\t" \
  "v_pk_fma_f16 v51, v49, v45, v51 op_sel:[0,0,0] op_sel_hi:[0,1,1]\n\tv_pk_mul_f16 v52, v48, v46 op_sel:[1,1] op_sel_hi:[1,0] neg_lo:[1,0]\n\t" \
  "v_pk_mul_f16 v53, v49, v47 op_sel:[1,1] op_sel_hi:[1,0] neg_lo:[1,0]\n\tv_pk_fma_f16 v52, v48, v46, v52 op_sel:[0,0,0] op_sel_hi:[0,1,1]\n\t" \
  "v_pk_fma_f16 v53, v49, v47, v53 op_sel:[0,0,0] op_sel_hi:[0,1,1]\n\tv_pk_mul_f16 v54, v48, v56 op_sel:[1,1] op_sel_hi:[1,0] neg_lo:[1,0]\n\t" \
  "v_pk_mul_f16 v55, v49, v57 op_sel:[1,1] op_sel_hi:[1,0] neg_lo:[1,0]\n\tv_pk_fma_f16 v54, v48, v56, v54 op_sel:[0,0,0] op_sel_hi:[0,1,1]\n\t" \
  "v_pk_fma_f16 v55, v49, v57, v55 op_sel:[0,0,0] op_sel_hi:[0,1,1]\n\tds_write_addtid_b32 v48 offset:2304\n\t" \
  "ds_write_addtid_b32 v49 offset:6912\n\tds_write_addtid_b32 v50 offset:2592\n\t" \
  "ds_write_addtid_b32 v51 offset:7200\n\tds_write_addtid_b32 v52 offset:2880\n\t" \
  "ds_write_addtid_b32 v53 offset:7488\n\tds_write_addtid_b32 v54 offset:3168\n\t" \
  "ds_write_addtid_b32 v55 offset:7776\n\tv_cvt_pk_f16_f32 v48, v68, v69\n\t" \
  "v_cvt_pk_f16_f32 v49, v70, v71\n\tv_pk_mul_f32 v[64:65], v[68:69], v[40:41] op_sel:[1,1] op_sel_hi:[1,0] neg_lo:[1,0]\n\t" \
  "v_pk_mul_f32 v[66:67], v[70:71], v[42:43] op_sel:[1,1] op_sel_hi:[1,0] neg_lo:[1,0]\n\tv_pk_fma_f32 v[64:65], v[68:69], v[40:41], v[64:65] op_sel:[0,0,0] op_sel_hi:[0,1,1]\n\t" \
  "v_pk_fma_f32 v[66:67], v[70:71], v[42:43], v[66:67] op_sel:[0,0,0] op_sel_hi:[0,1,1]\n\tv_pk_mul_f16 v50, v48, v44 op_sel:[1,1] op_sel_hi:[1,0] neg_lo:[1,0]\n\t" \
  "v_pk_mul_f16 v51, v49, v45 op_sel:[1,1] op_sel_hi:[1,0] neg_lo:[1,0]\n\tv_pk_fma_f16 v50, v48, v44, v50 op_sel:[0,0,0] op_sel_hi:[0,1,1]\n\t" \
  "v_pk_fma_f16 v51, v49, v45, v51 op_sel:[0,0,0] op_sel_hi:[0,1,1]\n\tv_pk_mul_f16 v52, v48, v46 op_sel:[1,1] op_sel_hi:[1,0] neg_lo:[1,0]\n\t" \
  "v_pk_mul_f16 v53, v49, v47 op_sel:[1,1] op_sel_hi:[1,0] neg_lo:[1,0]\n\tv_pk_fma_f16 v52, v48, v46, v52 op_sel:[0,0,0] op_sel_hi:[0,1,1]\n\t" \
  "v_pk_fma_f16 v53, v49, v47, v53 op_sel:[0,0,0] op_sel_hi:[0,1,1]\n\tv_pk_mul_f16 v54, v48, v56 op_sel:[1,1] op_sel_hi:[1,0] neg_lo:[1,0]\n\t" \
  "v_pk_mul_f16 v55, v49, v57 op_sel:[1,1] op_sel_hi:[1,0] neg_lo:[1,0]\n\tv_pk_fma_f16 v54, v48, v56, v54 op_sel:[0,0,0] op_sel_hi:[0,1,1]\n\t" \
  "v_pk_fma_f16 v55, v49, v57, v55 op_sel:[0,0,0] op_sel_hi:[0,1,1]\n\tds_write_addtid_b32 v48 offset:3456\n\t" \
  "ds_write_addtid_b32 v49 offset:8064\n\tds_write_addtid_b32 v50 offset:3744\n\t" \
  "ds_write_addtid_b32 v51 offset:8352\n\tds_write_addtid_b32 v52 offset:4032\n\t" \
  "ds_write_addtid_b32 v53 offset:8640\n\tds_write_addtid_b32 v54 offset:4320\n\t" \
  "ds_write_addtid_b32 v55 offset:8928\n\tds_read_b128 v[72:75], v27 offset:0\n\t" \
  "ds_read_b128 v[76:79], v27 offset:64\n\tds_read_b128 v[80:83], v27 offset:128\n\t" \
  "ds_read_b128 v[84:87], v27 offset:192\n\tds_read_b128 v[88:91], v27 offset:4608\n\t" \
  "ds_read_b128 v[92:95], v27 offset:4672\n\tds_read_b128 v[96:99], v27 offset:4736\n\t" \
  "ds_read_b128 v[100:103], v27 offset:4800\n\tv_cvt_pk_f16_f32 v48, v64, v65\n\t" \
  "v_cvt_pk_f16_f32 v49, v66, v67\n\tv_pk_mul_f32 v[68:69], v[64:65], v[40:41] op_sel:[1,1] op_sel_hi:[1,0] neg_lo:[1,0]\n\t" \
  "v_pk_mul_f32 v[70:71], v[66:67], v[42:43] op_sel:[1,1] op_sel_hi:[1,0] neg_lo:[1,0]\n\tv_pk_fma_f32 v[68:69], v[64:65], v[40:41], v[68:69] op_sel:[0,0,0] op_sel_hi:[0,1,1]\n\t" \
  "v_pk_fma_f32 v[70:71], v[66:67], v[42:43], v[70:71] op_sel:[0,0,0] op_sel_hi:[0,1,1]\n\tv_pk_mul_f16 v50, v48, v44 op_sel:[1,1] op_sel_hi:[1,0] neg_lo:[1,0]\n\t" \
  "v_pk_mul_f16 v51, v49, v45 op_sel:[1,1] op_sel_hi:[1,0] neg_lo:[1,0]\n\tv_pk_fma_f16 v50, v48, v44, v50 op_sel:[0,0,0] op_sel_hi:[0,1,1]\n\t" \
  "v_pk_fma_f16 v51, v49, v45, v51 op_sel:[0,0,0] op_sel_hi:[0,1,1]\n\tv_pk_mul_f16 v52, v48, v46 op_sel:[1,1] op_sel_hi:[1,0] neg_lo:[1,0]\n\t" \
  "v_pk_mul_f16 v53, v49, v47 op_sel:[1,1] op_sel_hi:[1,0] neg_lo:[1,0]\n\tv_pk_fma_f16 v52, v48, v46, v52 op_sel:[0,0,0] op_sel_hi:[0,1,1]\n\t" \
  "v_pk_fma_f16 v53, v49, v47, v53 op_sel:[0,0,0] op_sel_hi:[0,1,1]\n\tv_pk_mul_f16 v54, v48, v56 op_sel:[1,1] op_sel_hi:[1,0] neg_lo:[1,0]\n\t" \
  "v_pk_mul_f16 v55, v49, v57 op_sel:[1,1] op_sel_hi:[1,0] neg_lo:[1,0]\n\tv_pk_fma_f16 v54, v48, v56, v54 op_sel:[0,0,0] op_sel_hi:[0,1,1]\n\t" \
  "v_pk_fma_f16 v55, v49, v57, v55 op_sel:[0,0,0] op_sel_hi:[0,1,1]\n\ts_waitcnt lgkmcnt(0)\n\t" \
  "v_mfma_f32_16x16x32_f16 v[60:63], v[104:107], v[72:75], 0\n\tv_mfma_f32_16x16x32_f16 v[36:39], v[72:75], v[136:139], 0\n\t" \
  "ds_write_addtid_b32 v48 offset:0\n\tds_write_addtid_b32 v49 offset:4608\n\t" \
  "ds_write_addtid_b32 v50 offset:288\n\tds_write_addtid_b32 v51 offset:4896\n\t" \
  "v_mfma_f32_16x16x32_f16 v[60:63], v[108:111], v[76:79], v[60:63]\n\tds_write_addtid_b32 v52 offset:576\n\t" \
  "ds_write_addtid_b32 v53 offset:5184\n\tds_write_addtid_b32 v54 offset:864\n\t" \
  "ds_write_addtid_b32 v55 offset:5472\n\tv_mfma_f32_16x16x32_f16 v[36:39], v[76:79], v[140:143], v[36:39]\n\t" \
  "v_cvt_pk_f16_f32 v48, v68, v69\n\tv_cvt_pk_f16_f32 v49, v70, v71\n\t" \
  "v_pk_mul_f32 v[64:65], v[68:69], v[40:41] op_sel:[1,1] op_sel_hi:[1,0] neg_lo:[1,0]\n\tv_pk_mul_f32 v[66:67], v[70:71], v[42:43] op_sel:[1,1] op_sel_hi:[1,0] neg_lo:[1,0]\n\t" \
  "v_pk_fma_f32 v[64:65], v[68:69], v[40:41], v[64:65] op_sel:[0,0,0] op_sel_hi:[0,1,1]\n\tv_pk_fma_f32 v[66:67], v[70:71], v[42:43], v[66:67] op_sel:[0,0,0] op_sel_hi:[0,1,1]\n\t" \
  "v_pk_mul_f16 v50, v48, v44 op_sel:[1,1] op_sel_hi:[1,0] neg_lo:[1,0]\n\tv_pk_mul_f16 v51, v49, v45 op_sel:[1,1] op_sel_hi:[1,0] neg_lo:[1,0]\n\t" \
  "v_mfma_f32_16x16x32_f16 v[60:63], v[112:115], v[80:83], v[60:63]\n\tv_pk_fma_f16 v50, v48, v44, v50 op_sel:[0,0,0] op_sel_hi:[0,1,1]\n\t" \
  "v_pk_fma_f16 v51, v49, v45, v51 op_sel:[0,0,0] op_sel_hi:[0,1,1]\n\tv_pk_mul_f16 v52, v48, v46 op_sel:[1,1] op_sel_hi:[1,0] neg_lo:[1,0]\n\t" \
  "v_pk_mul_f16 v53, v49, v47 op_sel:[1,1] op_sel_hi:[1,0] neg_lo:[1,0]\n\tv_pk_fma_f16 v52, v48, v46, v52 op_sel:[0,0,0] op_sel_hi:[0,1,1]\n\t" \
  "v_pk_fma_f16 v53, v49, v47, v53 op_sel:[0,0,0] op_sel_hi:[0,1,1]\n\tv_pk_mul_f16 v54, v48, v56 op_sel:[1,1] op_sel_hi:[1,0] neg_lo:[1,0]\n\t" \
  "v_pk_mul_f16 v55, v49, v57 op_sel:[1,1] op_sel_hi:[1,0] neg_lo:[1,0]\n\tv_pk_fma_f16 v54, v48, v56, v54 op_sel:[0,0,0] op_sel_hi:[0,1,1]\n\t" \
  "v_pk_fma_f16 v55, v49, v57, v55 op_sel:[0,0,0] op_sel_hi:[0,1,1]\n\tv_mfma_f32_16x16x32_f16 v[36:39], v[80:83], v[144:147], v[36:39]\n\t" \
  "ds_write_addtid_b32 v48 offset:1152\n\tds_write_addtid_b32 v49 offset:5760\n\t" \
  "ds_write_addtid_b32 v50 offset:1440\n\tds_write_addtid_b32 v51 offset:6048\n\t" \
  "v_mfma_f32_16x16x32_f16 v[60:63], v[116:119], v[84:87], v[60:63]\n\tds_write_addtid_b32 v52 offset:1728\n\t" \
  "ds_write_addtid_b32 v53 offset:6336\n\tds_write_addtid_b32 v54 offset:2016\n\t" \
  "ds_write_addtid_b32 v55 offset:6624\n\tv_mfma_f32_16x16x32_f16 v[36:39], v[84:87], v[148:151], v[36:39]\n\t" \
  "v_cvt_pk_f16_f32 v48, v64, v65\n\tv_cvt_pk_f16_f32 v49, v66, v67\n\t" \
  "v_pk_mul_f32 v[68:69], v[64:65], v[40:41] op_sel:[1,1] op_sel_hi:[1,0] neg_lo:[1,0]\n\tv_pk_mul_f32 v[70:71], v[66:67], v[42:43] op_sel:[1,1] op_sel_hi:[1,0] neg_lo:[1,0]\n\t" \
  "v_pk_fma_f32 v[68:69], v[64:65], v[40:41], v[68:69] op_sel:[0,0,0] op_sel_hi:[0,1,1]\n\tv_pk_fma_f32 v[70:71], v[66:67], v[42:43], v[70:71] op_sel:[0,0,0] op_sel_hi:[0,1,1]\n\t" \
  "v_pk_mul_f16 v50, v48, v44 op_sel:[1,1] op_sel_hi:[1,0] neg_lo:[1,0]\n\tv_pk_mul_f16 v51, v49, v45 op_sel:[1,1] op_sel_hi:[1,0] neg_lo:[1,0]\n\t" \
  "v_mfma_f32_16x16x32_f16 v[60:63], v[120:123], v[88:91], v[60:63]\n\tv_pk_fma_f16 v50, v48, v44, v50 op_sel:[0,0,0] op_sel_hi:[0,1,1]\n\t" \
  "v_pk_fma_f16 v51, v49, v45, v51 op_sel:[0,0,0] op_sel_hi:[0,1,1]\n\tv_pk_mul_f16 v52, v48, v46 op_sel:[1,1] op_sel_hi:[1,0] neg_lo:[1,0]\n\t" \
  "v_pk_mul_f16 v53, v49, v47 op_sel:[1,1] op_sel_hi:[1,0] neg_lo:[1,0]\n\tv_pk_fma_f16 v52, v48, v46, v52 op_sel:[0,0,0] op_sel_hi:[0,1,1]\n\t" \
  "v_pk_fma_f16 v53, v49, v47, v53 op_sel:[0,0,0] op_sel_hi:[0,1,1]\n\tv_pk_mul_f16 v54, v48, v56 op_sel:[1,1] op_sel_hi:[1,0] neg_lo:[1,0]\n\t" \
  "v_pk_mul_f16 v55, v49, v57 op_sel:[1,1] op_sel_hi:[1,0] neg_lo:[1,0]\n\tv_pk_fma_f16 v54, v48, v56, v54 op_sel:[0,0,0] op_sel_hi:[0,1,1]\n\t" \
  "v_pk_fma_f16 v55, v49, v57, v55 op_sel:[0,0,0] op_sel_hi:[0,1,1]\n\tv_mfma_f32_16x16x32_f16 v[36:39], v[88:91], v[152:155], v[36:39]\n\t" \
  "ds_write_addtid_b32 v48 offset:2304\n\tds_write_addtid_b32 v49 offset:6912\n\t" \
  "ds_write_addtid_b32 v50 offset:2592\n\tds_write_addtid_b32 v51 offset:7200\n\t" \
  "v_mfma_f32_16x16x32_f16 v[60:63], v[124:127], v[92:95], v[60:63]\n\tds_write_addtid_b32 v52 offset:2880\n\t" \
  "ds_write_addtid_b32 v53 offset:7488\n\tds_write_addtid_b32 v54 offset:3168\n\t" \
  "ds_write_addtid_b32 v55 offset:7776\n\tv_mfma_f32_16x16x32_f16 v[36:39], v[92:95], v[156:159], v[36:39]\n\t" \
  "v_cvt_pk_f16_f32 v48, v68, v69\n\tv_cvt_pk_f16_f32 v49, v70, v71\n\t" \
  "v_pk_mul_f16 v50, v48, v44 op_sel:[1,1] op_sel_hi:[1,0] neg_lo:[1,0]\n\tv_pk_mul_f16 v51, v49, v45 op_sel:[1,1] op_sel_hi:[1,0] neg_lo:[1,0]\n\t" \
  "v_pk_fma_f16 v50, v48, v44, v50 op_sel:[0,0,0] op_sel_hi:[0,1,1]\n\tv_pk_fma_f16 v51, v49, v45, v51 op_sel:[0,0,0] op_sel_hi:[0,1,1]\n\t" \
  "v_pk_mul_f16 v52, v48, v46 op_sel:[1,1] op_sel_hi:[1,0] neg_lo:[1,0]\n\tv_pk_mul_f16 v53, v49, v47 op_sel:[1,1] op_sel_hi:[1,0] neg_lo:[1,0]\n\t" \
  "v_mfma_f32_16x16x32_f16 v[60:63], v[128:131], v[96:99], v[60:63]\n\tv_pk_fma_f16 v52, v48, v46, v52 op_sel:[0,0,0] op_sel_hi:[0,1,1]\n\t" \
  "v_pk_fma_f16 v53, v49, v47, v53 op_sel:[0,0,0] op_sel_hi:[0,1,1]\n\tv_pk_mul_f16 v54, v48, v56 op_sel:[1,1] op_sel_hi:[1,0] neg_lo:[1,0]\n\t" \
  "v_pk_mul_f16 v55, v49, v57 op_sel:[1,1] op_sel_hi:[1,0] neg_lo:[1,0]\n\tv_pk_fma_f16 v54, v48, v56, v54 op_sel:[0,0,0] op_sel_hi:[0,1,1]\n\t" \
  "v_pk_fma_f16 v55, v49, v57, v55 op_sel:[0,0,0] op_sel_hi:[0,1,1]\n\tv_mfma_f32_16x16x32_f16 v[36:39], v[96:99], v[160:163], v[36:39]\n\t" \
  "ds_write_addtid_b32 v48 offset:3456\n\tds_write_addtid_b32 v49 offset:8064\n\t" \
  "ds_write_addtid_b32 v50 offset:3744\n\tds_write_addtid_b32 v51 offset:8352\n\t" \
  "v_mfma_f32_16x16x32_f16 v[60:63], v[132:135], v[100:103], v[60:63]\n\tds_write_addtid_b32 v52 offset:4032\n\t" \
  "ds_write_addtid_b32 v53 offset:8640\n\tds_write_addtid_b32 v54 offset:4320\n\t" \
  "ds_write_addtid_b32 v55 offset:8928\n\tv_mfma_f32_16x16x32_f16 v[36:39], v[100:103], v[164:167], v[36:39]\n\t" \
  "ds_read_b128 v[72:75], v27 offset:0\n\tds_read_b128 v[76:79], v27 offset:64\n\t" \
  "ds_read_b128 v[80:83], v27 offset:128\n\tds_read_b128 v[84:87], v27 offset:192\n\t" \
  "ds_read_b128 v[88:91], v27 offset:4608\n\tds_read_b128 v[92:95], v27 offset:4672\n\t" \
  "ds_read_b128 v[96:99], v27 offset:4736\n\tds_read_b128 v[100:103], v27 offset:4800\n\t" \
  "v_add_u32_sdwa v56, %[w], v16 dst_sel:DWORD dst_unused:UNUSED_PAD src0_sel:DWORD src1_sel:WORD_0\n\t" \
  "v_add_u32_sdwa v57, %[w], v16 dst_sel:DWORD dst_unused:UNUSED_PAD src0_sel:DWORD src1_sel:WORD_1\n\tv_add_u32_sdwa v58, %[w], v17 dst_sel:DWORD dst_unused:UNUSED_PAD src0_sel:DWORD src1_sel:WORD_0\n\t" \
  "v_add_u32_sdwa v59, %[w], v17 dst_sel:DWORD dst_unused:UNUSED_PAD src0_sel:DWORD src1_sel:WORD_1\n\tv_mov_b32 v52, 0\n\t" \
  "v_mov_b32 v53, 0\n\tv_mov_b32 v54, 0\n\t" \
  "v_mov_b32 v55, 0\n\tv_cvt_pk_f16_f32 v42, v36, v37\n\t" \
  "v_cvt_pk_f16_f32 v43, v38, v39\n\tv_cvt_pk_f16_f32 v40, v60, v61\n\t" \
  "v_cvt_pk_f16_f32 v41, v62, v63\n\tv_alignbit_b32 v44, v43, v42, 16\n\t" \
  "s_waitcnt lgkmcnt(7)\n\tv_mfma_f32_16x16x32_f16 v[64:67], v[104:107], v[72:75], 0\n\t" \
  "v_mfma_f32_16x16x32_f16 v[68:71], v[72:75], v[136:139], 0\n\ts_waitcnt lgkmcnt(6)\n\t" \
  "v_mfma_f32_16x16x32_f16 v[64:67], v[108:111], v[76:79], v[64:67]\n\tds_write_b128 v58, v[52:55]\n\t" \
  "v_mfma_f32_16x16x32_f16 v[68:71], v[76:79], v[140:143], v[68:71]\n\tds_write2_b32 v56, v40, v41 offset0:64 offset1:65\n\t" \
  "s_waitcnt lgkmcnt(7)\n\tv_mfma_f32_16x16x32_f16 v[64:67], v[112:115], v[80:83], v[64:67]\n\t" \
  "ds_write2_b32 v57, v40, v41 offset0:64 offset1:65\n\tv_mfma_f32_16x16x32_f16 v[68:71], v[80:83], v[144:147], v[68:71]\n\t" \
  "ds_write_b64 v59, v[42:43] offset:16\n\ts_waitcnt lgkmcnt(8)\n\t" \
  "v_mfma_f32_16x16x32_f16 v[64:67], v[116:119], v[84:87], v[64:67]\n\tds_write_b32 v59, v42 offset:1164\n\t" \
  "v_mfma_f32_16x16x32_f16 v[68:71], v[84:87], v[148:151], v[68:71]\n\tds_write_b32 v59, v43 offset:1168\n\t" \
  "s_waitcnt lgkmcnt(9)\n\tv_mfma_f32_16x16x32_f16 v[64:67], v[120:123], v[88:91], v[64:67]\n\t" \
  "ds_write_b16 v59, v42 offset:590\n\tv_mfma_f32_16x16x32_f16 v[68:71], v[88:91], v[152:155], v[68:71]\n\t" \
  "ds_write_b32 v59, v44 offset:592\n\ts_waitcnt lgkmcnt(10)\n\t" \
  "v_mfma_f32_16x16x32_f16 v[64:67], v[124:127], v[92:95], v[64:67]\n\tds_write_b16_d16_hi v59, v43 offset:596\n\t" \
  "v_mfma_f32_16x16x32_f16 v[68:71], v[92:95], v[156:159], v[68:71]\n\tds_write_b16 v59, v42 offset:1738\n\t" \
  "s_waitcnt lgkmcnt(11)\n\tv_mfma_f32_16x16x32_f16 v[64:67], v[128:131], v[96:99], v[64:67]\n\t" \
  "ds_write_b32 v59, v44 offset:1740\n\tv_mfma_f32_16x16x32_f16 v[68:71], v[96:99], v[160:163], v[68:71]\n\t" \
  "ds_write_b16_d16_hi v59, v43 offset:1744\n\ts_waitcnt lgkmcnt(12)\n\t" \
  "v_mfma_f32_16x16x32_f16 v[68:71], v[100:103], v[164:167], v[68:71]\n\tv_mfma_f32_16x16x32_f16 v[64:67], v[132:135], v[100:103], v[64:67]\n\t" \
  "s_nop 4\n\tv_cvt_pk_f16_f32 v48, v68, v69\n\t" \
  "v_cvt_pk_f16_f32 v49, v70, v71\n\ts_nop 1\n\t" \
  "v_cvt_pk_f16_f32 v46, v64, v65\n\tv_cvt_pk_f16_f32 v47, v66, v67\n\t" \
  "v_alignbit_b32 v50, v49, v48, 16\n\tds_write2_b32 v56, v46, v47 offset0:0 offset1:1\n\t" \
  "ds_write2_b32 v57, v46, v47 offset0:0 offset1:1\n\tds_write_b64 v59, v[48:49] offset:0\n\t" \
  "ds_write_b32 v59, v48 offset:1148\n\tds_write_b32 v59, v49 offset:1152\n\t" \
  "ds_write_b16 v59, v48 offset:574\n\tds_write_b32 v59, v50 offset:576\n\t" \
  "ds_write_b16_d16_hi v59, v49 offset:580\n\tds_write_b16 v59, v48 offset:1722\n\t" \
  "ds_write_b32 v59, v50 offset:1724\n\tds_write_b16_d16_hi v59, v49 offset:1728\n\t"
#define GEN_CLOBBER "v36", "v37", "v38", "v39", "v40", "v42", "v43", "v44", "v45", "v47", "v49", "v50", "v51", "v52", "v53", "v54", "v55", "v56", "v57", "v58", "v59", "v60", "v61", "v62", "v63", "v64", "v65", "v66", "v67", "v72", "v73", "v74", "v75", "v76", "v77", "v78", "v79", "v80", "v81", "v82", "v83", "v84", "v85", "v86", "v87", "v88", "v89", "v90", "v91", "v92", "v93", "v94", "v95", "v98", "v99", "v100", "v101", "v102", "v103", "v104", "v105", "v106", "v107", "v108", "v109", "v110", "v111", "v112", "v113", "v114", "v115", "v116", "v117", "v118", "v119", "v120", "v121", "v122", "v123", "v124", "v125", "v126", "v127", "v128", "v129", "v130", "v131", "v132", "v133", "v134", "v135", "v136", "v137", "v138", "v139", "v140", "v141", "v142", "v143", "v144", "v145", "v146", "v147", "v148", "v149", "v150", "v151", "v152", "v153", "v154", "v155", "v156", "v157", "v158", "v159", "v160", "v161", "v162", "v163", "v164", "v165", "v166", "v167"
__device__ __forceinline__ f32x2 pkfma(f32x2 a, f32x2 b, f32x2 c) {
  f32x2 r;
  asm("v_pk_fma_f32 %0, %1, %2, %3" : "=v"(r) : "v"(a), "v"(b), "v"(c));
  return r;
}
template <int HI>
__device__ __forceinline__ f32x2 pkfmas(f32x2 a, f32x2 s, f32x2 c) {
  f32x2 r;
  if (HI) asm("v_pk_fma_f32 %0, %2, %1, %3 op_sel:[1,0,0] op_sel_hi:[1,1,1]" : "=v"(r) : "v"(a), "v"(s), "v"(c));
  else asm("v_pk_fma_f32 %0, %2, %1, %3 op_sel:[0,0,0] op_sel_hi:[0,1,1]" : "=v"(r) : "v"(a), "v"(s), "v"(c));
  return r;
}
__device__ __forceinline__ void ln_rows(u32x4 (&xv)[4], u32x8 wv, rsrc_t rs, u32& tag, int b, int w, int l, u32 st TLARG) {
  float s[2][3] = {}, c[2], sw = 0.f;
  u32 os = l < 24 ? (l < 16 ? O_SG : O_RG - 128) + 128 * (b - 128) + 8 * l : 0x80000000u;
  u32 aw = (l & 16) ? st + 8 * w + (l >> 5) * 4 : st + 104, ah = (l & 16) ? st + 64 + 4 * w + (l >> 5) * 2 : st + 104, ac = st + 96, ar = st + 4 * (l & 31), one = 1u;
  asm volatile("" : "+v"(os), "+v"(aw), "+v"(ah), "+v"(ac), "+v"(ar), "+v"(one));
  SB();
  asm volatile("s_barrier\n\ts_waitcnt vmcnt(4) lgkmcnt(0)" : "+{s44}"(tag)::"memory");
  asm volatile("" : "+{v[116:123]}"(wv));
  tag += 1u;
  STAMP(12);
#pragma unroll
  for (int i = 0; i < 8; i++) sw = dot2b(wv[i], 0x3f803f80u, sw);
  sw += dppf<0x121, 0xf>(sw); sw += dppf<0x122, 0xf>(sw); sw += dppf<0x124, 0xf>(sw); sw += dppf<0x128, 0xf>(sw);
  sw = (rdl(sw, 0) + rdl(sw, 16)) + (rdl(sw, 32) + rdl(sw, 48));
  asm volatile("" : "+v"(sw));
  SB();
  float cv = 0.f;
#pragma unroll
  for (int r = 0; r < 2; r++) {
#pragma unroll
    for (int i = 0; i < 8; i++) {
      if (r == 0 && i == 0) { asm volatile("s_waitcnt vmcnt(3)" : "+{v[100:103]}"(xv[0])); STAMP(13); }
      if (r == 0 && i == 4) asm volatile("s_waitcnt vmcnt(2)" : "+{v[104:107]}"(xv[1]));
      if (r == 1 && i == 0) asm volatile("s_waitcnt vmcnt(1)" : "+{v[108:111]}"(xv[2]));
      if (r == 1 && i == 4) { asm volatile("s_waitcnt vmcnt(0)" : "+{v[112:115]}"(xv[3])); STAMP(14); }
      const u32 a = xv[2 * r + (i >> 2)][i & 3];
      s[r][0] = dot2b(a, 0x3f803f80u, s[r][0]); s[r][1] = dot2b(a, a, s[r][1]); s[r][2] = dot2b(a, wv[i], s[r][2]);
      if (i == 3) {
        c[r] = rdl(s[r][0] * 0.125f, 0);
        if (r) { cv = l < 32 ? c[0] : c[1]; asm volatile("" : "+v"(cv)); }
        SB();
      }
    }
    const float ml = s[r][0] * 0.0625f, dl = __builtin_fmaf(s[r][0], 0.25f, -4.f * c[r]);
    s[r][1] = __builtin_fmaf(dl, dl, __builtin_fmaf(-s[r][0], ml, s[r][1]));
    SB();
  }
#pragma unroll
  for (int k = 0; k < 3; k++) swap32(s[0][k ? 3 - k : 0], s[1][k ? 3 - k : 0]);
  float q[3];
#pragma unroll
  for (int k = 0; k < 3; k++) q[k] = s[0][k] + s[1][k];
#define RED3(m) "v_add_f32_dpp %0, %0, %0 " m " bank_mask:0xf\n\tv_add_f32_dpp %1, %1, %1 " m " bank_mask:0xf\n\tv_add_f32_dpp %2, %2, %2 " m " bank_mask:0xf\n\t"
  asm volatile("s_nop 1\n\t" RED3("row_ror:1 row_mask:0xf") RED3("row_ror:2 row_mask:0xf") RED3("row_ror:4 row_mask:0xf") RED3("row_ror:8 row_mask:0xf")
               RED3("row_bcast:15 row_mask:0xa") : "+v"(q[0]), "+v"(q[1]), "+v"(q[2]));
  const float mu = q[0] * (1.f / D), dv = mu - cv;
  const float var = fmaxf(__builtin_fmaf(-dv, dv, q[1] * (1.f / D)), 0.f);
  const float m = rb(mu);
  float sd = __builtin_amdgcn_sqrtf(rb(rb(var) + 1e-5f)), num = __builtin_fmaf(-m, sw, q[2]);
  asm volatile("s_nop 0\n\tv_cvt_pk_bf16_f32 %0, 0, %0" : "+v"(sd), "+v"(num));
  u32 rv = __float_as_uint(rb(num * __builtin_amdgcn_rcpf(sd)));
  rv ^= (u32)((int)rv >> 15) | 0x80000000u;
  u32 old, val;
  asm volatile("ds_write_b32 %2, %3\n\tds_write_b16_d16_hi %4, %5\n\ts_mov_b64 exec, 1\n\tds_add_rtn_u32 %0, %6, %7\n\ts_mov_b64 exec, -1\n\t"
               "ds_read_b32 %1, %8\n\ts_waitcnt lgkmcnt(0)"
               : "=&v"(old), "=&v"(val) : "v"(aw), "v"(__builtin_amdgcn_perm(__float_as_uint(sd), __float_as_uint(m), 0x07060302u)), "v"(ah), "v"(rv),
                 "v"(ac), "v"(one), "v"(ar) : "memory");
  if (__builtin_amdgcn_readfirstlane((int)old) == 7) st8(rs, os, u32x2{val, tag});
  SB();
}

constexpr int NOPS = 109, NSLOT = 168;
struct Ops {
  signed char k[NOPS], a[NOPS], b[NOPS];
  short first[NOPS + 1];
  unsigned char op[NSLOT];
  short ld[NOPS][2];
  short end[8];
};
constexpr int nmf(int k, int T, int p) { return k == 0 ? (p == 0 && T > 0) + (T < 8) + (p == 0 && T > 0 && T < 5) : k == 1 ? 1 + (T < 6) : k == 2 ? (T < 4 ? 3 : 6 - T) : 4 - T; }
constexpr Ops mkops() {
  Ops O = {};
  int n = 0, last[8] = {};
  O.k[n] = 0; O.a[n] = 0; O.b[n] = 0; n++;
  for (int I = 0; I < 8; I++) {
    int fk[7] = {}, fa[7] = {}, fb[7] = {}, m = 0;
    if (I >= 4) { fk[m] = 3; fa[m] = I - 4; fb[m] = 0; m++; }
    if (I >= 2) for (int p = 0; p < 2; p++) { fk[m] = 2; fa[m] = I - 2; fb[m] = p; m++; }
    if (I >= 1) for (int p = 0; p < 4; p++) { fk[m] = 1; fa[m] = I - 1; fb[m] = p; m++; }
    int f = 0;
    for (int c = 1; c <= 8; c++) {
      O.k[n] = 0; O.a[n] = c == 8 ? I + 1 : I; O.b[n] = c & 7; n++;
      if (I >= 2 && f < m) { O.k[n] = fk[f]; O.a[n] = fa[f]; O.b[n] = fb[f]; n++; f++; }
    }
    for (; f < m; f++) { O.k[n] = fk[f]; O.a[n] = fa[f]; O.b[n] = fb[f]; n++; }
    last[I] = n - 1;
  }
  int q = 0;
  for (int i = 0; i < NOPS; i++) {
    O.first[i] = q;
    for (int u = nmf(O.k[i], O.a[i], O.b[i]); u > 0; u--) O.op[q++] = i;
  }
  O.first[NOPS] = q;
  for (int I = 0; I < 8; I++) O.end[I] = O.first[last[I]];
  for (int i = 0; i < NOPS; i++) {
    O.ld[i][1] = O.first[i] - LA;
    O.ld[i][0] = O.ld[i][1] - (O.k[i] && O.first[i] - O.first[i - 1] > 1);
  }
  return O;
}
constexpr Ops OPS = mkops();
static_assert(OPS.first[NOPS] == NSLOT, "schedule");
constexpr int WL[4] = {-1, 17, WL2, WL3}, WN[4] = {38, 38, 22, 13};
constexpr int wslot(int pp, int k) { return pp < 2 ? (pp ? 22 : 2) + k / 2 : WL[pp] + 4 + k; }
constexpr int opat(int k, int T, int p) { for (int i = 0; i < NOPS; i++) if (OPS.k[i] == k && OPS.a[i] == T && OPS.b[i] == p) return i; return -1; }
static_assert(wslot(0, 12) < OPS.ld[opat(1, 0, 0)][0] && wslot(0, 21) < OPS.ld[opat(2, 0, 0)][0] && wslot(1, 12) < OPS.ld[opat(1, 1, 0)][0] &&
              wslot(1, 21) < OPS.ld[opat(2, 1, 0)][0] && wslot(1, 37) < OPS.ld[opat(3, 0, 0)][0] && wslot(2, 12) < OPS.ld[opat(1, 3, 0)][0] &&
              wslot(2, 21) < OPS.ld[opat(2, 3, 0)][0] && wslot(3, 12) < OPS.ld[opat(1, 5, 0)][0] && wslot(0, 29) < WL[1] && wslot(1, 37) < WL[2] &&
              wslot(2, 21) < WL[3], "window order");
constexpr int XC[3] = {OPS.ld[opat(0, 2, 0)][1], WL2 < OPS.ld[opat(0, 4, 0)][1] ? WL2 : OPS.ld[opat(0, 4, 0)][1], WL3};
static_assert(XC[0] <= WL[1] && WL3 <= OPS.ld[opat(0, 6, 0)][1], "chunk order");
template <int V> struct IC { static constexpr int value = V; };
template <int A, int B, class F> __device__ __forceinline__ void sfor(F&& f) {
  if constexpr (A < B) { f(IC<A>{}); sfor<A + 1, B>(f); }
}
#define GL __attribute__((address_space(1)))
typedef const GL u16* gp;
__device__ __forceinline__ void dma(gp base, u32 v, u32 dst) {
  asm volatile("s_mov_b32 m0, %0\n\ts_nop 0\n\tglobal_load_lds_dwordx4 %1, %2 nt" ::"s"(dst), "v"(v), "s"(base) : "memory");
}

__global__ __launch_bounds__(768, 3) void fused(const u16*, const u16*, const u16*, const u16*, const u16*, const u16*, const u16*, const u16*,
    const u16*, const u16*, const u16*, u16*, char*) {
  __shared__ __attribute__((aligned(256))) char smem[SMEM];
  const u32 lds0 = (u32)(size_t)(LDS char*)smem;
  int w0;
#define GLD(r0, r1, r2, r3, o_) "global_load_dwordx4 v[" #r0 ":" #r1 "], " #o_ ", s[16:17]\n\tglobal_load_dwordx4 v[" #r2 ":" #r3 "], " #o_ ", s[16:17] offset:2048\n\t"
  asm volatile(
      "s_load_dwordx16 s[16:31], s[0:1], 0\n\ts_load_dwordx8 s[32:39], s[0:1], 64\n\ts_load_dwordx2 s[40:41], s[0:1], 96\n\t"
      "s_getpc_b64 s[4:5]\n2:\n\ts_lshr_b32 s7, s2, 3\n\ts_and_b32 s10, s4, 0xffffff00\n\ts_mov_b32 s11, s5\n\ts_mul_i32 s8, s7, 0x600\n\t"
      "v_lshl_add_u32 v3, v0, 1, s8\n\ts_cmpk_lt_u32 s2, 0x80\n\ts_cbranch_scc0 1f\n\t"
      "global_load_ushort v161, v3, s[10:11] offset:%[pf]\n"
      "1:\n\tv_readfirstlane_b32 s3, v0\n\tv_and_b32 v1, 63, v0\n\tv_mov_b64 v[4:5], 0\n\tv_mov_b64 v[6:7], 0\n\tv_lshlrev_b32 v2, 4, v1\n\tv_lshlrev_b32 v8, 2, v0\n\t"
      "s_mov_b32 s42, %[we]\n\ts_mov_b32 s43, 0x20000\n\ts_cbranch_scc0 3f\n\t"
      "s_lshr_b32 s3, s3, 6\n\ts_mul_i32 s9, s3, %[xr]\n\ts_min_u32 s9, s9, %[zm]\n\tv_add_u32 v3, s9, v2\n\tv_add_u32 v3, %[zo], v3\n\t"
      "s_and_b32 s7, s7, 7\n\ts_and_b32 s6, s2, 7\n\ts_and_b32 s8, s2, 64\n\ts_lshl3_add_u32 s8, s6, s8\n\ts_add_u32 s45, s8, s7\n\t"
      "ds_write_b128 v3, v[4:7]\n\ts_cmp_lt_u32 s3, 8\n\ts_cbranch_scc1 5f\n\t"
      "s_lshl_b32 s8, s45, 4\n\ts_lshl_b32 s9, s3, 18\n\ts_add_u32 s9, s9, s8\n\ts_bitset0_b32 s9, 21\n\tv_lshl_add_u32 v148, v1, 12, s9\n\t"
      "s_lshl3_add_u32 s9, s3, s7\n\ts_lshl_b32 s9, s9, 17\n\ts_add_u32 s9, s9, s8\n\ts_bitset0_b32 s9, 23\n\tv_lshl_add_u32 v9, v1, 11, s9\n\t"
      "v_add_u32 v11, 0x100000, v148\n\tv_add_u32 v12, 0x200000, v148\n\tv_add_u32 v13, 0x300000, v148\n\t"
      "v_lshlrev_b32 v149, 4, v0\n\tv_add_u32 v149, %[sg], v149\n\ts_movk_i32 s9, 0x1000\n\t"
      "s_waitcnt lgkmcnt(0)\n\ts_load_dword s44, s[40:41], 0\n\ts_load_dwordx4 s[48:51], s[28:29], s8\n\ts_barrier\n\t"
      "global_load_dword v160, v9, s[16:17]" PRENT "\n\t"
      GLD(100, 103, 104, 107, v148) GLD(108, 111, 112, 115, v11) GLD(116, 119, 120, 123, v12)
      "buffer_load_dwordx4 v[132:135], v149, s[40:43], 0 offen sc1\n\tbuffer_load_dwordx4 v[136:139], v149, s[40:43], s9 offen sc1\n\t"
      GLD(124, 127, 128, 131, v13) "s_branch .Lrole_p\n"
      "3:\n\ts_lshr_b32 s3, s3, 6\n\ts_cmp_lt_u32 s3, 8\n\ts_cbranch_scc0 .Lrole_d\n\t"
      "v_add_u32 v3, %[fc], v2\n\ts_add_u32 s8, s3, s2\n\ts_and_b32 s45, s8, 7\n\ts_lshl3_add_u32 s8, s2, s45\n\t"
      "s_lshl_b32 s8, s8, 12\n\ts_bitset0_b32 s8, 22\n\tds_write_b128 v3, v[4:7]\n\tv_add_u32 v3, s8, v2\n\t"
      "s_waitcnt lgkmcnt(0)\n\tglobal_load_dwordx4 v[116:119], v2, s[30:31]\n\tglobal_load_dwordx4 v[120:123], v2, s[30:31] offset:1024\n\t"
      "s_load_dword s44, s[40:41], 0\n\t"
      "global_load_dwordx4 v[100:103], v3, s[16:17]" LNNT "\n\tglobal_load_dwordx4 v[104:107], v3, s[16:17] offset:1024" LNNT "\n\t"
      "global_load_dwordx4 v[108:111], v3, s[16:17] offset:2048" LNNT "\n\tglobal_load_dwordx4 v[112:115], v3, s[16:17] offset:3072" LNNT "\n\t"
      "s_branch .Lrole_l\n"
      "5:\n\ts_add_u32 s4, s4, lane_tab@rel32@lo+(6f-2b-4)\n6:\n\ts_addc_u32 s5, s5, lane_tab@rel32@hi+(6b-2b+4)\n\t"
      "global_load_dwordx4 v[16:19], v2, s[4:5]\n\tglobal_load_dwordx4 v[20:23], v2, s[4:5] offset:1024\n\t"
      "s_lshl3_add_u32 s8, s45, s3\n\ts_lshl_b32 s9, s8, 1\n\tv_mov_b32 v9, s9\n\ts_lshl_b32 s9, s45, 11\n\tv_add_u32 v3, s9, v8\n\t"
      "s_waitcnt lgkmcnt(0)\n\tglobal_load_short_d16_hi v24, v9, s[26:27]\n\t"
      "global_load_dword v96, v3, s[18:19]" PNT "\n\tglobal_load_dword v97, v3, s[20:21]" PNT "\n\t"
      "global_load_dword v68, v3, s[22:23]" PNT "\n\tglobal_load_dword v69, v3, s[24:25]" PNT "\n\ts_barrier\n\ts_branch .Lrole_c"
      : "={s3}"(w0)
      : "{s[0:1]}"(__builtin_amdgcn_kernarg_segment_ptr()), "{s2}"(blockIdx.x), "{v0}"(threadIdx.x), [pf] "n"(PFO), [we] "n"(WS_END), [xr] "n"(XREG),
        [zm] "n"(OMISC - 512 - (OX - 16)), [zo] "n"(OX - 16), [sg] "n"(O_SG - 8192), [fc] "n"(FC)
      : "memory");
#define TLREGS()
#define ROLE(name, text, ...) \
  gp x, dsk, wg, wu, wd; GL u16* out; rsrc_t rs; int b, w, t, l, g; u32 tag, st0; u32x4 dv; \
  asm volatile(".Lrole_" #name ":" : "={v0}"(t), "={v1}"(l) __VA_ARGS__ :: "memory"); \
  asm volatile(text : "={s2}"(b), "={s3}"(w), "={s[16:17]}"(x), "={s[28:29]}"(dsk), "={s[32:33]}"(wg), "={s[34:35]}"(wu), "={s[36:37]}"(wd), \
               "={s[38:39]}"(out), "={s[40:43]}"(rs), "={s44}"(tag), "={s45}"(g), "={s46}"(st0), "={s[48:51]}"(dv) :: "memory"); \
  SB(); \
  __builtin_assume((u32)l < 64u); __builtin_assume((u32)t < 768u); __builtin_assume((u32)w < 12u); __builtin_assume((u32)b < 256u); __builtin_assume((u32)g < 128u); \
  TLREGS(); \
  const int xk = b & 7, j = (b >> 3) & 15, n16 = l & 15, kg = l >> 4
#define TAGWAIT() do { asm volatile("s_waitcnt lgkmcnt(0)" : "+{s44}"(tag)::"memory"); tag += 1u; } while (0)
#define BAR() do { SB(); asm volatile("s_barrier" ::: "memory"); SB(); } while (0)
#define FLAG(o, v) do { asm volatile("" ::: "memory"); SB(); *(volatile LDS u32*)(LDS char*)(smem + (o)) = (v); SB(); } while (0)
#define ARRIVE(o) do { SB(); if (l == 0) asm volatile("ds_add_u32 %0, %1" ::"v"(lds0 + (o)), "v"(1u) : "memory"); SB(); } while (0)
#define CWAIT(o, n, nap) do { _Pragma("unroll 1") for (int spin = 0; spin < SPIN; spin++) { \
      if (__builtin_amdgcn_readfirstlane((int)*(const volatile LDS u32*)(LDS char*)(smem + (o))) >= (int)(n)) break; \
      asm volatile(nap ::: "memory"); } SB(); asm volatile("" ::: "memory"); } while (0)
#define AWAIT(o, n, nap) do { _Pragma("unroll 1") for (int spin = 0; spin < SPIN; spin++) { \
      if (__all(*(const volatile LDS u32*)(LDS char*)(smem + (o)) >= (u32)(n))) break; \
      asm volatile(nap ::: "memory"); } SB(); asm volatile("" ::: "memory"); } while (0)
#define POLL8(res_, off_) do { u32 n_ = SPIN; \
    asm volatile("1:\n\tbuffer_load_dwordx2 v[4:5], %2, %3, 0 offen sc1\n\ts_sub_u32 %1, %1, 1\n\ts_waitcnt vmcnt(0)\n\t" \
                 "v_cmp_ne_u32 vcc, %4, v5\n\ts_cbranch_scc1 2f\n\ts_cbranch_vccnz 1b\n2:" \
                 : "=&{v[4:5]}"(res_), "+s"(n_) : "v"(off_), "s"(rs), "s"(tag) : "vcc", "scc", "memory"); } while (0)
#define LANE(v_) int v_; asm volatile("v_mbcnt_lo_u32_b32 %0, -1, 0\n\tv_mbcnt_hi_u32_b32 %0, -1, %0" : "=v"(v_))
#define IXO(l_) (O_IDX + (2 * xk + (j & 1)) * 128 + ((l_) & 7) * 8)

#define SLOW(s_) (16 * ((s_) >> 2) + 4 + ((s_) & 3))
#define IDS() const int id = 14 * xk + j, gid = id, ns = 0, s0 = 0
  if (blockIdx.x >= 128) {
    if (w0 >= 8) {
      ROLE(d, "s_cmpk_ge_u32 s2, 0xf0\n\ts_cbranch_scc1 .Lrole_end\n\ts_waitcnt lgkmcnt(0)");
      IDS();
      const int dw = w - 8;
      u32 hp = 0u;
      const u32 sw = (((n16 >> 1) ^ (kg | ((dw >> 1) << 2))) << 5) + ((l & 1) << 4);
      gp base = ((gid & 2) ? wu : wg) + (size_t)(512 * (gid & 1) + 4 * dw) * F + (gid >> 2) * 128;
      gp bd = wd + (size_t)(256 * (id >> 3) + 4 * dw) * D + (id & 7) * 128;
      u32 v = kg * (2 * F) + sw, vd = kg * (2 * D) + sw;
      const int h = j - ns, sid = SLOW(s0 + (h >> 1));
      gp hb = ((sid & 2) ? wu : wg) + (size_t)(512 * (sid & 1) + 480 - 64 * dw - 32 * (h & 1)) * F + (sid >> 2) * 128;
      u32 hv = (l >> 1) * (2 * F) + 128 * (l & 1);
      asm volatile("" : "+v"(v), "+v"(vd), "+v"(hv));
      asm volatile("" : "+s"(base), "+s"(bd), "+s"(hb));
      STAMP(8);
      BAR();
      STAMP(9);
#pragma unroll
      for (int i = 0; i < 32; i++) {
        if (i == HPI && HELP && h >= 0 && h < 2 * ns && dw < HELP)
          asm volatile("global_load_dword v100, %1, %2 nt" : "={v100}"(hp) : "v"(hv), "s"(hb) : "memory");
        if (i >= QA && i < NA) asm volatile("s_waitcnt vmcnt(%0)" ::"n"(QA - 1) : "memory");
        else if (i >= QD) asm volatile("s_waitcnt vmcnt(%0)" ::"n"(QD - 1) : "memory");
        if (i == 15 + QD) ARRIVE(FC);
        if (i == 23 + QD) ARRIVE(FC + 16);
        dma(base + (size_t)(16 * i) * F, v, lds0 + 4096 * i + 1024 * dw);
      }
      STAMP(1);
      u32 inc = 1u;
      {
        u32 n_ = SPIN, c_;
#define LOOK(c) "s_waitcnt vmcnt(" #c ")\n\tds_read_b32 %[c_], %[a] offset:4\n\ts_waitcnt lgkmcnt(0)\n\tv_cmp_lt_u32 vcc, %[k], %[c_]\n\ts_cbranch_vccnz 2f\n\t"
        asm volatile(LOOK(5) LOOK(4) LOOK(3) LOOK(2) LOOK(1) "s_waitcnt vmcnt(0)\n\ts_mov_b64 exec, 1\n\tds_add_u32 %[a], %[i] offset:20\n\ts_mov_b64 exec, -1\n\t"
                     "v_mov_b32 %[i], 0\n1:\n\t" LOOK(0) "s_sub_u32 %[n], %[n], 1\n\ts_cbranch_scc0 1b\n2:"
                     : [c_] "=&v"(c_), [i] "+v"(inc), [n] "+s"(n_) : [a] "v"(lds0 + FC), [k] "n"(7 + HOLD) : "vcc", "scc", "memory");
      }
      STAMP(3);
#pragma unroll
      for (int i = 0; i < 16; i++) {
        if (i >= QE - 5) asm volatile("s_waitcnt vmcnt(%0)" ::"n"(QE - 1) : "memory");
        if (i == QE - 1) { asm volatile("s_mov_b64 exec, 1\n\tds_add_u32 %0, %1 offset:20\n\ts_mov_b64 exec, -1" ::"v"(lds0 + FC), "v"(inc) : "memory"); STAMP(2); }
        dma(bd + (size_t)(16 * i) * D, vd, lds0 + 4096 * i + 1024 * dw);
      }
      STAMP(6);
      asm volatile("s_waitcnt vmcnt(0)" ::"{v100}"(hp) : "memory");
      ARRIVE(FC + 8);
      STAMP(4);
      FLUSH();
      __builtin_amdgcn_endpgm();
    }
    u32x4 xv[4];
    u32x8 wv;
    ROLE(l, "", , "={v[100:103]}"(xv[0]), "={v[104:107]}"(xv[1]), "={v[108:111]}"(xv[2]), "={v[112:115]}"(xv[3]), "={v[116:123]}"(wv));
    STAMP(11);
    ln_rows(xv, wv, rs, tag, b, g, l, lds0 + FC + 128 TLVAL);
    STAMP(10);
    if (j >= 14) {
      const int sb = 2 * xk + (j - 14);
      u32* gm = (u32*)(smem + FC + 256);
      u32* ncand = gm + 16;
      u32* cand = gm + 32;
      {
        struct { u32 x, z; } g;
        u32x8 ring0;
        u32x4 ring1;
        {
          u32 n_ = SPIN;
          unsigned long long tmp;
          asm volatile("buffer_load_dwordx4 v[40:43], %[o], %[r], 0 offen sc1\n\ts_sleep 3\n\tbuffer_load_dwordx4 v[44:47], %[o], %[r], 0 offen sc1\n\ts_sleep 3\n\t"
                     "buffer_load_dwordx4 v[48:51], %[o], %[r], 0 offen sc1\n1:\n\t"
                     "s_waitcnt vmcnt(2)\n\tv_cmp_ne_u32 vcc, %[t], v41\n\tv_cmp_ne_u32 %4, %[t], v43\n\tv_mov_b32 %0, v40\n\tv_mov_b32 %1, v42\n\t"
                     "s_or_b64 vcc, vcc, %4\n\ts_cbranch_scc0 2f\n\tbuffer_load_dwordx4 v[40:43], %[o], %[r], 0 offen sc1\n\t"
                     "s_waitcnt vmcnt(2)\n\tv_cmp_ne_u32 vcc, %[t], v45\n\tv_cmp_ne_u32 %4, %[t], v47\n\tv_mov_b32 %0, v44\n\tv_mov_b32 %1, v46\n\t"
                     "s_or_b64 vcc, vcc, %4\n\ts_cbranch_scc0 2f\n\tbuffer_load_dwordx4 v[44:47], %[o], %[r], 0 offen sc1\n\t"
                     "s_waitcnt vmcnt(2)\n\tv_cmp_ne_u32 vcc, %[t], v49\n\tv_cmp_ne_u32 %4, %[t], v51\n\tv_mov_b32 %0, v48\n\tv_mov_b32 %1, v50\n\t"
                     "s_or_b64 vcc, vcc, %4\n\ts_cbranch_scc0 2f\n\tbuffer_load_dwordx4 v[48:51], %[o], %[r], 0 offen sc1\n\t"
                     "s_sub_u32 %[n], %[n], 1\n\ts_cbranch_scc0 1b\n2:"
                     : "=&v"(g.x), "=&v"(g.z), "=&{v[40:47]}"(ring0), "=&{v[48:51]}"(ring1), "=&s"(tmp), [n] "+s"(n_)
                     : [o] "v"(O_RG + 128 * (t >> 2) + 16 * (t & 3)), [r] "s"(rs), [t] "s"(tag) : "vcc", "scc", "memory");
        }
        STAMP(2);
        const u32 ib = 2047 - 4 * t;
        const u32 key[4] = {(g.x << 16) | ib, (g.x & 0xffff0000u) | (ib - 1), (g.z << 16) | (ib - 2), (g.z & 0xffff0000u) | (ib - 3)};
        u32 m = max(max(key[0], key[1]), max(key[2], key[3]));
        const u32 m4 = m;
        m = max(m, dppu<0x121, 0xf>(m)); m = max(m, dppu<0x122, 0xf>(m));
        m = max(m, dppu<0x124, 0xf>(m)); m = max(m, dppu<0x128, 0xf>(m));
        {
          auto sw = __builtin_amdgcn_permlane16_swap(m, m, false, false);
          m = max(sw[0], sw[1]);
        }
        gm[t >> 5] = m;
        LBAR();
        STAMP(3);
        auto rank16 = [&](const u32* a, u32& mine) __attribute__((always_inline)) {
          mine = a[n16];
          const u32x4 q = *(const u32x4*)(a + 4 * kg);
          u32 cnt = (q.x > mine) + (q.y > mine) + (q.z > mine) + (q.w > mine);
          auto s1 = __builtin_amdgcn_permlane32_swap(cnt, cnt, false, false);
          cnt = s1[0] + s1[1];
          auto s2 = __builtin_amdgcn_permlane16_swap(cnt, cnt, false, false);
          return s2[0] + s2[1];
        };
        u32 mine;
        u32 cnt = rank16(gm, mine);
        const u32 thr = (u32)__builtin_amdgcn_readlane((int)mine, __builtin_ctzll(__ballot(cnt == 7)));
        if (m4 >= thr) {
          const u32 c = (key[0] >= thr) + (key[1] >= thr) + (key[2] >= thr) + (key[3] >= thr);
          u32 slot;
          asm volatile("ds_add_rtn_u32 %0, %1, %2\n\ts_waitcnt lgkmcnt(0)" : "=v"(slot) : "v"((u32)(size_t)(LDS u32*)ncand), "v"(c) : "memory");
#pragma unroll
          for (int i = 0; i < 4; i++) {
            const bool on = key[i] >= thr;
            gm[on ? 32 + slot : 17] = key[i];
            slot += on;
          }
        }
        LBAR();
        STAMP(4);
        const int n = __builtin_amdgcn_readfirstlane((int)*ncand);
        cnt = rank16(cand, mine);
#define IDXST(on) st8(rs, (on) ? O_IDX + sb * 128 + cnt * 8 : 0x80000000u, \
                      u32x2{(2047u - (mine & 0x7ffu)) | ((mine ^ ((u32)((int)~mine >> 15) | 0x80000000u)) & 0xffff0000u), tag})
        if (__builtin_expect(n <= 16, 1)) {
          if (w == 0) IDXST(l < n && cnt < 8);
          const u32 row = 2047u - ((u32)__builtin_amdgcn_readlane((int)mine, __builtin_ctzll(__ballot(cnt == (u32)w))) & 0x7ffu);
          const u32 pf = *(const GL u32*)(x + (size_t)row * D + 64 * n16);
          asm volatile("" ::"v"(pf));
        } else if (w == 0) {
#pragma unroll 1
          for (int i0 = 0; i0 < n; i0 += 64) {
            mine = cand[i0 + l]; cnt = 0;
#pragma clang loop unroll(disable) vectorize(disable)
            for (int i = 0; i < n; i++) cnt += cand[i] > mine;
            IDXST(i0 + l < n && cnt < 8);
          }
        }
        STAMP(5);
        asm volatile("s_waitcnt vmcnt(0)" ::"{v[40:47]}"(ring0), "{v[48:51]}"(ring1));
      }
      if (b == 255 && w == 7) {
#pragma unroll 1
        for (int spin = 0; spin < SPIN; spin++) {
          const u32x4 a = ld16(rs, O_ARR + 16 * l);
          if (__all(a.y == tag && a.w == tag)) break;
          asm volatile("s_sleep 16" ::: "memory");
        }
        if (l == 0) __builtin_amdgcn_raw_buffer_store_b32(tag, rs, 0, 0, 16);
      }
      FLUSH();
      __builtin_amdgcn_endpgm();
    }
    IDS();
    const char* wp = smem + (8 * kg + (n16 >> 2)) * 256 + ((w ^ ((n16 >> 2) | ((kg & 1) << 2))) << 5) + 8 * (l & 3);
    const char* wq = wp + 65536;
    {
      const bf16x8 one = asb(u32x4{0x3f803f80u, 0x3f803f80u, 0x3f803f80u, 0x3f803f80u});
      bf16x8 wf[16];
      f32x4 cs = {};
      CWAIT(FC, 4, "s_sleep 2");
      STAMP(1);
#pragma unroll
      for (int k = 0; k < 8; k++) wf[k] = mkb(trread(wp + 8192 * k), trread(wp + 8192 * k + 1024));
      ARRIVE(FC + 4);
#pragma unroll
      for (int k = 0; k < 8; k++) cs = MFMAB(wf[k], one, cs);
      SB();
      u32x8 ring = {};
      u32 so = 0u;
      if (w == 0) {
        u32 ix, n_ = SPIN;
        asm volatile("buffer_load_dwordx2 v[4:5], %[o], %[r], 0 offen sc1\n\ts_sleep " STR(RSL) "\n\tbuffer_load_dwordx2 v[6:7], %[o], %[r], 0 offen sc1\n\ts_sleep " STR(RSL) "\n\t"
                     "buffer_load_dwordx2 v[8:9], %[o], %[r], 0 offen sc1\n\ts_sleep " STR(RSL) "\n\tbuffer_load_dwordx2 v[10:11], %[o], %[r], 0 offen sc1\n1:\n\t"
                     "s_waitcnt vmcnt(3)\n\tv_cmp_ne_u32 vcc, %[t], v5\n\tv_mov_b32 %[x], v4\n\ts_cbranch_vccz 2f\n\tbuffer_load_dwordx2 v[4:5], %[o], %[r], 0 offen sc1\n\t"
                     "s_waitcnt vmcnt(3)\n\tv_cmp_ne_u32 vcc, %[t], v7\n\tv_mov_b32 %[x], v6\n\ts_cbranch_vccz 2f\n\tbuffer_load_dwordx2 v[6:7], %[o], %[r], 0 offen sc1\n\t"
                     "s_waitcnt vmcnt(3)\n\tv_cmp_ne_u32 vcc, %[t], v9\n\tv_mov_b32 %[x], v8\n\ts_cbranch_vccz 2f\n\tbuffer_load_dwordx2 v[8:9], %[o], %[r], 0 offen sc1\n\t"
                     "s_waitcnt vmcnt(3)\n\tv_cmp_ne_u32 vcc, %[t], v11\n\tv_mov_b32 %[x], v10\n\ts_cbranch_vccz 2f\n\tbuffer_load_dwordx2 v[10:11], %[o], %[r], 0 offen sc1\n\t"
                     "s_sub_u32 %[n], %[n], 1\n\ts_cbranch_scc0 1b\n2:"
                     : [x] "=&v"(ix), "=&{v[4:11]}"(ring), [n] "+s"(n_)
                     : [o] "v"(IXO(l)), [r] "s"(rs), [t] "s"(tag) : "vcc", "scc", "memory");
        STAMP(15);
        const rsrc_t rx = __builtin_amdgcn_make_buffer_rsrc((void*)x, 0, 2 * L * D, 0x00020000);
        const u32 vr = (ix & 2047u) << 11, vo = 1024 * (gid & 1) + 16 * l;
        u32 ro[8];
        so = O_SG + (vr >> 8);
#pragma unroll
        for (int i = 0; i < 8; i++) ro[i] = (u32)__builtin_amdgcn_readlane((int)vr, i);
        asm volatile("" : "+s"(ro[0]), "+s"(ro[1]), "+s"(ro[2]), "+s"(ro[3]), "+s"(ro[4]), "+s"(ro[5]), "+s"(ro[6]), "+s"(ro[7]));
#pragma unroll
        for (int i = 0; i < 8; i++)
          asm volatile("s_mov_b32 m0, %0\n\ts_nop 0\n\tbuffer_load_dwordx4 %1, %2, %3 offen lds" ::"s"(lds0 + FA + AP * i), "v"(vo), "s"(rx), "s"(ro[i]) : "memory");
        u32 n2 = SPIN;
        u32 inc = HOLD;
        asm volatile("1:\n\tbuffer_load_dwordx2 v[12:13], %3, %4, 0 offen sc1\n\ts_mov_b64 exec, 1\n\tds_add_u32 %6, %2\n\ts_mov_b64 exec, -1\n\t"
                     "s_sub_u32 %1, %1, 1\n\ts_waitcnt vmcnt(0)\n\tv_mov_b32 %2, 0\n\t"
                     "v_cmp_ne_u32 vcc, %5, v13\n\ts_cbranch_scc1 2f\n\ts_cbranch_vccnz 1b\n2:\n\tds_write_b32 %6, v12 offset:28"
                     : "+{v[4:11]}"(ring), "+s"(n2), "+v"(inc) : "v"(so), "s"(rs), "s"(tag), "v"(lds0 + FC + 4 + 4 * (l & 7)) : "v12", "v13", "vcc", "scc", "memory");
        FLAG(FC + 12, 4u);
      }
      CWAIT(FC + 16, 4, "s_sleep 1");
      STAMP(2);
#pragma unroll
      for (int k = 0; k < 4; k++) wf[8 + k] = mkb(trread(wq + 8192 * k), trread(wq + 8192 * k + 1024));
#pragma unroll
      for (int k = 0; k < 4; k++) cs = MFMAB(wf[8 + k], one, cs);
      SB();
      u32x4 xa[8];
      u32 st;
      f32x4 a1 = {}, a2 = {};
      const char* ap = smem + FA + (l & 7) * AP + 512 * (n16 >> 3) + 16 * kg;
      auto rows = [&]() __attribute__((always_inline)) {
        STAMP(3);
#pragma unroll
        for (int i = 0; i < 8; i++) xa[i] = *(const u32x4*)(ap + 64 * i);
        st = *(const u32*)(smem + FC + 32 + 4 * (l & 7));
        SB();
#pragma unroll
        for (int i = 0; i < 8; i++) { a1 = MFMAB(wf[i], asb(xa[i]), a1); if (i < 4) a2 = MFMAB(wf[8 + i], asb(xa[i]), a2); }
        SB();
      };
      auto last = [&]() __attribute__((always_inline)) {
        STAMP(4);
#pragma unroll
        for (int k = 4; k < 8; k++) wf[8 + k] = mkb(trread(wq + 8192 * k), trread(wq + 8192 * k + 1024));
#pragma unroll
        for (int k = 4; k < 8; k++) cs = MFMAB(wf[8 + k], one, cs);
        SB();
      };
      u32 got = 0u;
#pragma unroll 1
      for (int spin = 0; spin < SPIN; spin++) {
        got = (u32)__ballot(*(const volatile LDS u32*)(LDS char*)(smem + FC + 12 + 8 * (l & 1)) >= 4u);
        if (got) break;
        NAP();
      }
      asm volatile("" ::: "memory");
      SB();
      if (got & 2u) { last(); CWAIT(FC + 12, 4, "s_sleep 1"); rows(); }
      else { rows(); CWAIT(FC + 20, 4, "s_sleep 1"); last(); }
#pragma unroll
      for (int k = 4; k < 8; k++) a2 = MFMAB(wf[8 + k], asb(xa[k]), a2);
      const float rsd = __builtin_amdgcn_rcpf(hi2f(st)), nm = -lo2f(st);
      float o[4];
#pragma unroll
      for (int i = 0; i < 4; i++) o[i] = __builtin_fmaf(nm, cs[i], a1[i] + dppf<0x128, 0xf>(a2[i])) * rsd;
      if (n16 < 8) st16(rs, O_HG + gid * 4096 + 512 * w + 128 * kg + 16 * n16, u32x4{pack2(o[0], o[1]), tag, pack2(o[2], o[3]), tag});
      SB();
      STAMP(5);
    }
    {
      const u32 off = O_HG + (2 * (id >> 3) + (w >> 2)) * 16384 + (w & 3) * 1024 + 16 * l;
      CWAIT(FC + 8, 4, "s_sleep 1");
      STAMP(6);
      bf16x8 df[8];
#pragma unroll
      for (int k = 0; k < 8; k++) df[k] = mkb(trread(wp + 8192 * k), trread(wp + 8192 * k + 1024));
      SB();
      asm volatile("s_sleep " STR(HNAP0) ::: "memory");
      u32x4 ha[4];
#pragma unroll
      for (int i = 0; i < 4; i++) ha[i] = ld16(rs, off + 4096 * i);
      SB();
#pragma unroll 1
      for (int spin = 0; spin < SPIN; spin++) {
        bool bad[4];
#pragma unroll
        for (int i = 0; i < 4; i++) bad[i] = ha[i].y != tag || ha[i].w != tag;
        if (!__any(bad[0] || bad[1] || bad[2] || bad[3])) break;
        asm volatile("s_sleep " STR(HNAP) ::: "memory");
#pragma unroll
        for (int i = 0; i < 4; i++) if (bad[i]) ha[i] = ld16(rs, off + 4096 * i);
      }
      STAMP(7);
      u32x2 hv;
#pragma unroll
      for (int i = 0; i < 2; i++) {
        const u32 g0 = ha[0][2 * i], g1 = ha[1][2 * i], u0 = ha[2][2 * i], u1 = ha[3][2 * i];
        const u32 gg = pack2(lo2f(g0) + lo2f(g1), hi2f(g0) + hi2f(g1)), uu = pack2(lo2f(u0) + lo2f(u1), hi2f(u0) + hi2f(u1));
        const u32 sl = pack2(lo2f(gg) * sigm(lo2f(gg)), hi2f(gg) * sigm(hi2f(gg)));
        hv[i] = pack2(lo2f(sl) * lo2f(uu), hi2f(sl) * hi2f(uu));
      }
      *(u32x2*)(smem + FH + (l & 7) * HP + 64 * w + (l & 56)) = hv;
      LBAR();
      STAMP(8);
      const char* hp = smem + FH + (l & 7) * HP + 256 * (n16 >> 3) + 16 * kg;
      f32x4 a1 = {}, a2 = {};
#pragma unroll
      for (int i = 0; i < 4; i++) {
        const bf16x8 h = asb(*(const u32x4*)(hp + 64 * i));
        a1 = MFMAB(df[i], h, a1); a2 = MFMAB(df[4 + i], h, a2);
      }
      float o[4];
#pragma unroll
      for (int i = 0; i < 4; i++) o[i] = a1[i] + dppf<0x128, 0xf>(a2[i]);
      if (n16 < 8) st16(rs, O_PG + id * 4096 + 512 * w + 128 * kg + 16 * n16, u32x4{pack2(o[0], o[1]), tag, pack2(o[2], o[3]), tag});
      STAMP(9);
    }
    FLUSH();
    __builtin_amdgcn_endpgm();
  }

  if (w0 >= 8) {
    u32x8 xa0, xa1, xa2, xa3, sa0, sa1;
    u32x4 junk;
    u32 op, so;
    ROLE(p, "", , "={v[100:107]}"(xa0), "={v[108:115]}"(xa1), "={v[116:123]}"(xa2), "={v[124:131]}"(xa3), "={v[132:139]}"(sa0),
         "={v148}"(op), "={v149}"(so), "={v[160:163]}"(junk));
    const int p = w - 8;
    f32x2 sc[4];
    asm volatile("s_waitcnt lgkmcnt(0)" : "+{s44}"(tag), "+{s[48:51]}"(dv)::"memory");
    tag += 1u;
#pragma unroll
    for (int i = 0; i < 4; i++) { sc[i] = f32x2{1.f + lo2f(dv[i]), 1.f + hi2f(dv[i])}; asm volatile("" : "+v"(sc[i])); }
    STAMP(9);
    __builtin_amdgcn_s_setprio(3);
    f32x2 xq[4][8];
    {
      asm volatile("s_mov_b32 m0, %0\n\ts_nop 0" ::"s"(lds0 + OX + 2 * XO + 256 * p));
#pragma unroll
      for (int i = 0; i < 4; i++) {
#define SWAIT(c, r0, r1, r3, r4, r5, r7, ...) do { u32 n_ = SPIN, d_; \
          asm volatile("s_waitcnt vmcnt(" #c ")\n1:\n\tv_xor_b32 %[d], %[t], v" #r1 "\n\tv_bitop3_b32 %[d], %[d], %[t], v" #r3 " bitop3:0xf6\n\t" \
                       "v_bitop3_b32 %[d], %[d], %[t], v" #r5 " bitop3:0xf6\n\tv_bitop3_b32 %[d], %[d], %[t], v" #r7 " bitop3:0xf6\n\t" \
                       "s_sub_u32 %[n], %[n], 1\n\tv_cmp_ne_u32 vcc, 0, %[d]\n\ts_cbranch_scc1 2f\n\ts_cbranch_vccz 2f\n\t" \
                       "buffer_load_dwordx4 v[" #r0 ":" #r3 "], %[so], %[rs], %[k] offen sc1\n\tbuffer_load_dwordx4 v[" #r4 ":" #r7 "], %[so], %[rs], %[k1] offen sc1\n\t" \
                       "s_waitcnt vmcnt(0)\n\ts_branch 1b\n2:" \
                       : [d] "=&v"(d_), [n] "+s"(n_), __VA_ARGS__ \
                       : [t] "s"(tag), [so] "v"(so), [rs] "s"(rs), [k] "s"(4096 * i), [k1] "s"(4096 * i + 4096) : "vcc", "scc", "memory"); } while (0)
        if (i == 0) SWAIT(2, 132, 133, 135, 136, 137, 139, "+{v[132:139]}"(sa0), "+{v[100:107]}"(xa0), "+{v[108:115]}"(xa1), "+{v[160:163]}"(junk));
        if (i == 1) asm volatile("buffer_load_dwordx4 v[140:143], %[so], %[rs], %[k2] offen sc1\n\tbuffer_load_dwordx4 v[144:147], %[so], %[rs], %[k3] offen sc1"
                                 : "=&{v[140:147]}"(sa1) : [so] "v"(so), [rs] "s"(rs), [k2] "s"(8192), [k3] "s"(12288) : "memory");
        if (i == 2) {
          SWAIT(0, 140, 141, 143, 144, 145, 147, "+{v[140:147]}"(sa1), "+{v[116:123]}"(xa2), "+{v[124:131]}"(xa3));
        }
        const u32x8 xs = i == 0 ? xa0 : i == 1 ? xa1 : i == 2 ? xa2 : xa3;
        const u32 sa = i < 2 ? sa0[4 * i] : sa1[4 * i - 8], sb = i < 2 ? sa0[4 * i + 2] : sa1[4 * i - 6];
        if (i == 0) STAMP(3);
        if (i == 2) STAMP(10);
        const f32x2 r2 = {__builtin_amdgcn_rcpf(hi2f(sa)), __builtin_amdgcn_rcpf(hi2f(sb))};
        const f32x2 m2 = {-lo2f(sa) * r2.x, -lo2f(sb) * r2.y};
        f32x2 xx[8], n2[8];
        u32 o[8], fv = i + 1;
#pragma unroll
        for (int c = 0; c < 8; c++) {
          const u32 a = xs[c >> 1], bb = xs[4 + (c >> 1)];
          xx[c] = (c & 1) ? f32x2{hi2f(a), hi2f(bb)} : f32x2{lo2f(a), lo2f(bb)};
        }
        asm volatile("" : "+v"(fv));
        SB();
#pragma unroll
        for (int c = 0; c < 8; c++) n2[c] = pkfma(xx[c], r2, m2);
        SB();
#pragma unroll
        for (int c = 0; c < 8; c++) o[c] = packh(n2[c].x, n2[c].y);
        SB();
#pragma unroll
        for (int c = 0; c < 8; c++) asm volatile("ds_write_addtid_b32 %0 offset:%1" ::"v"(o[c]), "n"(c * XREG + 1024 * i) : "memory");
        FLAG(OXF + 4 * p, fv);
#pragma unroll
        for (int c = 0; c < 8; c++) xq[i][c] = (c & 1) ? pkfmas<1>(n2[c], sc[c >> 1], xx[c]) : pkfmas<0>(n2[c], sc[c >> 1], xx[c]);
        SB();
        if (i == 3) __builtin_amdgcn_s_setprio(PPRIO);
      }
    }
    STAMP(4);
    const rsrc_t ro = __builtin_amdgcn_make_buffer_rsrc((void*)out, 0, 2 * L * D, 0x00020000);
    const int u = 64 * (p & 1) + l;
    const char* yp = smem + OY + 512 * (p >> 1) + 4 * (u ^ ((u >> 4) & 6));
    const char* yq = yp + 4 * CHR;
    u32 ys[8];
    if (p == 0) {
      u32x2 ix;
      LANE(ln);
      const u32 ixo = IXO(ln);
      POLL8(ix, ixo);
      *(u32x2*)(smem + OIX + 8 * (ln & 7)) = ix;
      if (l < 8) atomicOr((u32*)(smem + OMISC) + ((ix.x & 2047u) >> 5), 1u << (ix.x & 31u));
      FLAG(OYP + 32, 8u);
      STAMP(11);
    }
#pragma unroll
    for (int i = 0; i < 4; i++) {
      AWAIT(OYP + 4 * min(l, 8), 2 * i + 1 + (p >> 1), YNAP);
      if (i == 0) STAMP(5); else if (i == 1) STAMP(6); else if (i == 2) STAMP(7); else STAMP(8);
      u32 y[8];
#pragma unroll
      for (int c = 0; c < 8; c++) y[c] = *(const u32*)((c < 4 ? yp : yq) + (c & 3) * CHR + 1024 * i);
      const u32 bm = *(const u32*)(smem + OMISC + 64 * i + 16 * p + 4 * kg) >> (2 * n16);
      if (i == 0) {
        if (t == 512) st8(rs, O_ARR + b * 8, u32x2{0u, tag});
        const u32x4 a = *(const u32x4*)(smem + OSC), c = *(const u32x4*)(smem + OSC + 16);
#pragma unroll
        for (int k = 0; k < 4; k++) { ys[k] = (u32)__builtin_amdgcn_readfirstlane((int)a[k]); ys[4 + k] = (u32)__builtin_amdgcn_readfirstlane((int)c[k]); }
      }
      SB();
#pragma unroll
      for (int r = 0; r < 2; r++) {
        u32x4 v;
#pragma unroll
        for (int k = 0; k < 4; k++)
          v[k] = pack2(dot2b(y[2 * k], ys[2 * k] << (16 * r), xq[i][2 * k][r]), dot2b(y[2 * k + 1], ys[2 * k + 1] << (16 * r), xq[i][2 * k + 1][r]));
        __builtin_amdgcn_raw_buffer_store_b128(v, ro, op | (bm << (31 - r) & 0x80000000u), (512 * i + r) * (2 * D), OAUX);
        SB();
        if (r) asm volatile("s_nop 1");
        SB();
      }
    }
    asm volatile("" ::"{v[160:163]}"(junk));
    STAMP(13);
    FLUSH();
    __builtin_amdgcn_endpgm();
  }

  u32 pl, pa, pr, pi, ldtv;
  u32x4 tv, fx, junk;
  ROLE(c, "", , "={v[16:19]}"(tv), "={v[20:23]}"(fx), "={v24}"(ldtv), "={v96}"(pl), "={v97}"(pa), "={v68}"(pr), "={v69}"(pi), "={v[160:163]}"(junk));
  char* W = smem + w * CHR;
  __builtin_amdgcn_s_setprio(2);
  STAMP(3);
  const u32x4 zero4 = {0u, 0u, 0u, 0u};
#pragma unroll
  for (int i = 0; i < 7; i++) *(u32x4*)(W + (i < 6 ? TB + 1024 * i : CHR - 1024) + 16 * l) = zero4;
  SB();
  const u32 wb0 = lds0 + w * CHR, xb0 = lds0 + OX + w * XREG;
  asm volatile("s_mov_b32 m0, %0\n\ts_nop 0\n\tds_write_addtid_b32 %1\n\tds_write_addtid_b32 %1 offset:%2" ::"s"(wb0), "v"(0x3c00u), "n"(PB) : "memory");
  typedef LDS char* lp;
  u32 ape = wb0 + ((n16 ^ 3) - 7 * (n16 >> 3)) * RS + 16 * kg, apo = wb0 + min(n16, 11) * RS + 16 * kg;
  u32x2 kb = {wb0 + ((15 - n16) & 3) * CPE + 2 * ((15 - n16) & 12) + 16 * kg, wb0 + (((n16 >> 2) & 1) + (n16 & 8) + 2 * (3 - (n16 & 3))) * RS + 16 * kg};
  u32x8 ka = {xb0 + 32 * n16 + 16 * kg, xb0 + 8 * l, wb0 + 8 * l, xb0 + 16 * l, wb0 + 16 * l,
              wb0 + 32 * n16 + 8 * (kg ^ ((l >> 2) & 3)),
              lds0 + OXF + 4 * (l & 3), wb0 + ((15 - n16) & 3) * CPO + 2 * ((15 - n16) & 12) + 8 * kg};
  asm volatile("" : "+{v70}"(ape), "+{v71}"(apo), "+{v[28:35]}"(ka), "+{v[26:27]}"(kb));
  STAMP(4);
  asm volatile("s_waitcnt vmcnt(4)" : "+{v24}"(ldtv), "+{v[160:163]}"(junk));
  float dt = rbt(fexp(hi2f(ldtv)));
  asm volatile("" : "+v"(dt));
  const f32x2 cdt = {rdl(dt * -1.4426950408889634f, 0), rdl(dt * 0.15915494309189535f, 0)};
  dt = rdl(dt, 0);
  SB();
  STAMP(10);
  STAMPC(11);
  {
    u32 e, tmp, o0, o1, of;
    asm volatile(GEN_ASM
                 : [e] "=&s"(e), [t] "=&s"(tmp), "=&{v41}"(o0), "=&{v46}"(o1), "=&{v48}"(of), "+{v68}"(pr), "+{v69}"(pi), "+{v70}"(ape),
                   "+{v71}"(apo), "+{v96}"(pl), "+{v97}"(pa), "+{v[28:35]}"(ka), "+{v[26:27]}"(kb), "+{v[16:19]}"(tv), "+{v[20:23]}"(fx)
                 : [w] "s"(wb0), [c] "s"(cdt), [k] "s"(f32x2{1.4426950408889634f, 256.f})
                 : "memory", "scc", GEN_CLOBBER);
    STAMP(1);
    STAMPC(12);
    const u32 k0 = (u32)__builtin_amdgcn_readlane((int)o0, 3), k256 = (u32)__builtin_amdgcn_readlane((int)o1, 60);
    const u32 k512 = (u32)__builtin_amdgcn_readlane((int)of, 51), k1024 = (u32)__builtin_amdgcn_readlane((int)of, 55);
    *(LDS u16*)(size_t)(wb0 + (tv.z & 0xffffu)) = (u16)__builtin_amdgcn_perm((k512 & 0xffffu) | (k1024 << 16), (k0 >> 16) | (k256 << 16), tv.w);
    *(u32*)(smem + OSC + 4 * w) = __float_as_uint(__uint_as_float((e - 5) << 23) * dt) >> 16;
    fx &= ((k1024 & 0xffffu) | (k1024 << 16)) ^ 0x80008000u;
  }
  const u32 axb = ka[0], axu = ka[1], awb = ka[2], axq = ka[3], awo = ka[4], ay = ka[5], axf = ka[6], atf = ka[7], ate = kb[0];
#define FRAG(o0, o1) mkh(*(const volatile LDS u32x2*)(tb + (o0)), *(const volatile LDS u32x2*)(tb + (o1)))
  f16x8 FR[9], F1[4][2], F2[2][3], F3[4];
  const LDS char* tb = (lp)(size_t)ate;
#pragma unroll
  for (int c = 0; c < 9; c++) FR[c] = FRAG(64 * c, 64 * c + 8);
  tb = (lp)(size_t)atf + TB;
#pragma unroll
  for (int p = 0; p < 4; p++)
#pragma unroll
    for (int dl = 0; dl < 2; dl++) F1[p][dl] = FRAG(2 * (2 * C1 * p + 16 - 16 * dl), 2 * (2 * C1 * p + C1 + 16 - 16 * dl));
#pragma unroll
  for (int p = 0; p < 2; p++)
#pragma unroll
    for (int dl = 0; dl < 3; dl++) F2[p][dl] = FRAG(2 * (T2B + 2 * C2 * p + 32 - 16 * dl), 2 * (T2B + 2 * C2 * p + C2 + 32 - 16 * dl));
  *(LDS u32x4*)(size_t)(wb0 + (tv.z >> 16)) = zero4;
  STAMP(2);
#define XWAIT(n) do { _Pragma("unroll 1") for (int spin = 0; spin < SPIN; spin++) { \
      if (__all(*(const volatile LDS u32*)(size_t)axf >= (u32)(n))) break; \
      NAP(); } SB(); asm volatile("" ::: "memory"); } while (0)
  XWAIT(1);
  STAMP(5);
  STAMPC(14);
  asm volatile("s_cmp_lt_u32 %0, 4\n\ts_cbranch_scc0 1f\n\ts_setprio 1\n1:" ::"s"(w) : "scc");
  {
    const lp xb = (lp)(size_t)axb, xu = (lp)(size_t)axu + 2 * XO, wb = (lp)(size_t)awb + 32, xq = (lp)(size_t)axq + 512;
    const lp wo = (lp)(size_t)awo + 16, Y = (lp)(size_t)ay + OY;
    f32x4 acc[8];
    u32x4 A[NOPS];
    u32x4 Q1, Q2, W7, W9;
    u32 QZ = 0u, Uo[8], P2[8], P4[8], W3[8], W5[8], xf = 0u, fl = 0u, fad = lds0 + OYP + 4 * w;
    u32x2 yv;
    asm volatile("" : "+v"(fad));
    auto winl = [&](auto P_) __attribute__((always_inline)) {
      constexpr int pp = decltype(P_)::value;
      Q1 = *(const LDS u32x4*)(xq + 1024 * pp);
      if (pp < 2) { Q2 = *(const LDS u32x4*)(xq + 1024 * pp + 16); QZ = *(const LDS u32*)(xq + 1024 * pp - 4); }
      else { const u32x2 v = *(const LDS u32x2*)(xq + 1024 * pp + 16); Q2.x = v.x; Q2.y = v.y; }
    };
    auto win = [&](auto K_) __attribute__((always_inline)) {
      constexpr int k = decltype(K_)::value;
      const u32 Dd[8] = {QZ, Q1.x, Q1.y, Q1.z, Q1.w, Q2.x, Q2.y, Q2.z};
      if constexpr (k < 5) Uo[2 + k] = __builtin_amdgcn_alignbit(Dd[2 + k], Dd[1 + k], 16);
      else if constexpr (k < 9) P2[k - 3] = hadd(Dd[k - 3], Uo[k - 3]);
      else if constexpr (k < 13) W3[k - 7] = hadd(P2[k - 7], Uo[k - 6]);
      else if constexpr (k == 13) P2[6] = hadd(Dd[6], Uo[6]);
      else if constexpr (k < 18) P4[k - 11] = hadd(P2[k - 11], P2[k - 12]);
      else if constexpr (k < 22) W5[k - 16] = hadd(P4[k - 15], Dd[k - 17]);
      else if constexpr (k == 22) Uo[1] = __builtin_amdgcn_alignbit(Dd[1], Dd[0], 16);
      else if constexpr (k == 23) Uo[7] = __builtin_amdgcn_alignbit(Dd[7], Dd[6], 16);
      else if constexpr (k == 24) P2[1] = hadd(Dd[1], Uo[1]);
      else if constexpr (k == 25) P2[7] = hadd(Dd[7], Uo[7]);
      else if constexpr (k == 26) P4[2] = hadd(P2[2], P2[1]);
      else if constexpr (k == 27) P4[7] = hadd(P2[7], P2[6]);
      else if constexpr (k == 28) W3[6] = hadd(P2[6], Uo[7]);
      else if constexpr (k == 29) W5[1] = hadd(P4[2], Dd[0]);
      else if constexpr (k < 34) W7[k - 30] = hadd(W3[k - 27], P4[k - 28]);
      else W9[k - 34] = hadd(P4[k - 30], W5[k - 33]);
      if constexpr (k == 13) asm volatile("" : "+v"(P2[6]));
      if constexpr (k > 13 && k < 18) asm volatile("" : "+v"(P4[k - 11]));
      if constexpr (k >= 18 && k < 22) asm volatile("" : "+v"(W5[k - 16]));
    };
    auto loads = [&](auto M_) __attribute__((always_inline)) {
      constexpr int m = decltype(M_)::value;
      sfor<0, 2>([&](auto N_) __attribute__((always_inline)) {
        constexpr int mm = m + LA + decltype(N_)::value;
        if constexpr (mm < NSLOT) {
          constexpr int s = OPS.op[mm], k = OPS.k[s], T = OPS.a[s], p = OPS.b[s];
          if constexpr (OPS.first[s] == mm) {
            if constexpr (k == 0) { if (OPS.ld[s][1] == m) A[s] = *(const LDS u32x4*)(xb + 512 * T + 64 * p); }
            else {
              if (OPS.ld[s][0] == m) {
                const u32x2 a = trl(k == 3 ? wb + OW7 + 512 * T - 16 : k == 2 ? wb + OW3 + 512 * T - 2 * (4 + 8 * p) : xu + 512 * T - 2 * (2 + 4 * p));
                A[s].x = a.x; A[s].y = a.y;
              }
              if (OPS.ld[s][1] == m) {
                const u32x2 c = trl(k == 3 ? wb + OW9 + 512 * T - 32 : k == 2 ? wb + OW5 + 512 * T - 2 * (8 + 8 * p) : wb + OW3 + 512 * T - 2 * (4 + 4 * p));
                A[s].z = c.x; A[s].w = c.y;
              }
            }
          }
        }
      });
    };
    constexpr int FT[8] = {0, OPS.first[opat(0, 1, 0)] + 1, OPS.first[opat(0, 2, 0)] + 1, OPS.first[opat(2, 0, 0)] + 1, OPS.first[opat(0, 1, 0)] + 2,
                           OPS.first[opat(0, 2, 0)] + 2, OPS.first[opat(0, 3, 0)] + 2, OPS.first[opat(0, 4, 0)] + 2};
    loads(IC<-LA>{});
    winl(IC<0>{});
    sfor<1 - LA, 0>(loads);
    auto step = [&](auto M_) __attribute__((always_inline)) {
      constexpr int m = decltype(M_)::value, s = OPS.op[m], u = m - OPS.first[s], k = OPS.k[s], T = OPS.a[s], p = OPS.b[s];
      SB();
      const f16x8 a = ash(A[s]);
#define MM(I, x_, y_) do { if constexpr (m == FT[I]) asm volatile("v_mfma_f32_16x16x32_f16 %0, %1, %2, 0" : "=&v"(acc[I]) : "v"(x_), "v"(y_)); \
      else asm volatile("v_mfma_f32_16x16x32_f16 %0, %1, %2, %0" : "+v"(acc[I]) : "v"(x_), "v"(y_)); } while (0)
      if constexpr (k == 0) {
        if constexpr (p == 0 && T > 0 && u == 0) MM(T - 1, FR[8], a);
        else if constexpr (u == 2) MM(T + 3, fx, a);
        else MM(T, FR[p], a);
      }
      else if constexpr (k == 3) MM(T + 4 + u, a, F3[u]);
      else if constexpr (k == 2) MM(T + 2 + u, a, F2[p][u]);
      else MM(T + 1 + u, a, F1[p][u]);
      SB();
      if constexpr (m == OPS.end[PH / 100 % 10] + 5) asm volatile("s_cmp_lt_u32 %0, 4\n\ts_cbranch_scc1 1f\n\ts_setprio 1\n1:" ::"s"(w) : "scc");
      if constexpr (m == OPS.end[PH / 10 % 10] + 5) asm volatile("s_cmp_lt_u32 %0, 4\n\ts_cbranch_scc0 1f\n\ts_setprio 0\n1:" ::"s"(w) : "scc");
      if constexpr (m == OPS.end[PH % 10] + 5) asm volatile("s_setprio 0");
      sfor<0, 3>([&](auto N_) __attribute__((always_inline)) {
        constexpr int n = decltype(N_)::value;
        if (m == XC[n]) { if (!__all(xf >= n + 2u)) XWAIT(n + 2); }
        if (m == XC[n] - 4) xf = *(const volatile LDS u32*)(size_t)axf;
      });
      sfor<0, 7>([&](auto I_) __attribute__((always_inline)) {
        constexpr int I = decltype(I_)::value;
        if (m == OPS.end[I] + 4) {
          *(LDS u32x2*)(Y + 512 * I) = yv;
          asm volatile("ds_write_b32 %0, %1" ::"v"(fad), "v"(fl) : "memory");
          if (I == 0) STAMP(6);
          if (I == 3) STAMP(7);
        }
      });
      sfor<0, 4>([&](auto P_) __attribute__((always_inline)) {
        constexpr int pp = decltype(P_)::value;
#if !(ABL & 4)
        if (m == wslot(pp, 12) + 1) *(LDS u32x4*)(wo + 1024 * pp + OW3) = u32x4{W3[2], W3[3], W3[4], W3[5]};
        if (pp < 3 && m == wslot(pp, 21) + 1) *(LDS u32x4*)(wo + 1024 * pp + OW5) = u32x4{W5[2], W5[3], W5[4], W5[5]};
        if (pp < 2 && m == wslot(pp, 33) + 1) *(LDS u32x4*)(wo + 1024 * pp + OW7) = W7;
        if (pp < 2 && m == wslot(pp, 37) + 1) *(LDS u32x4*)(wo + 1024 * pp + OW9) = W9;
#endif
        if (m == WL[pp]) winl(P_);
        if (m == OPS.first[opat(3, 0, 0)] - 8 + pp) F3[pp] = FRAG(2 * (T3B + 48 - 16 * pp), 2 * (T3B + C3 + 48 - 16 * pp));
      });
      loads(M_);
      SB();
      sfor<0, 4>([&](auto P_) __attribute__((always_inline)) {
        constexpr int pp = decltype(P_)::value;
        if (pp < 2 && m == wslot(pp, 3)) asm volatile("" : "+v"(Q2));
        if (!(ABL & 6)) sfor<0, WN[pp]>([&](auto K_) __attribute__((always_inline)) { if constexpr (wslot(pp, decltype(K_)::value) == m) win(K_); });
      });
      sfor<0, 7>([&](auto I_) __attribute__((always_inline)) {
        constexpr int I = decltype(I_)::value;
        if (m == OPS.end[I] + 3) {
          yv = u32x2{pack2(acc[I][0], acc[I][1]), pack2(acc[I][2], acc[I][3])};
          fl = I + 1;
          asm volatile("" : "+v"(fl), "+v"(yv));
        }
      });
    };
    sfor<0, NSLOT>(step);
    SB();
    asm volatile("s_nop 7\n\ts_nop 1" : "+v"(acc[7]));
    *(LDS u32x2*)(Y + 512 * 7) = u32x2{pack2(acc[7][0], acc[7][1]), pack2(acc[7][2], acc[7][3])};
    FLAG(OYP + 4 * w, 8u);
  }
  STAMP(13);
  STAMPC(15);
  if (w != 0) {
    FLUSH();
    __builtin_amdgcn_endpgm();
  }
  {
    __builtin_amdgcn_s_setprio(0);
    LANE(ln);
    CWAIT(OYP + 32, 8, "s_sleep 2");
    const u32x2 ix = *(const u32x2*)(smem + OIX + 8 * (ln & 7));
    tag = (u32)__builtin_amdgcn_readfirstlane((int)ix.y);
    const int prow = (int)(ix.x & 2047u), c = ((ln >> 1) & 4) + (ln >> 4), v = prow >> 1;
    const u32 px = x[(size_t)prow * D + 8 * g + c], pd = dsk[8 * g + c];
    float rsel = rbt(sigm(hi2f(ix.x)));
    const u32 po = O_PG + 256 * g + 16 * (ln & 15) + 32768 * (ln >> 4), p3 = ln < 32 ? po + 3 * 131072 : 0x80000000u;
    u32x4 pv[4];
    {
      const char* fp = smem + OYP + 4 * (ln & 7);
#pragma unroll 1
      for (int spin = 0; spin < SPIN; spin++) {
        if (__all(*(const volatile LDS u32*)(LDS char*)fp >= 8u)) break;
        asm volatile("s_sleep 3" ::: "memory");
      }
      asm volatile("" ::: "memory");
    }
    const float yl = lo2f(*(const u16*)(smem + OY + CHR * c + 4 * (v ^ ((v >> 4) & 6)) + 2 * (prow & 1)));
    const float ql = hlo(*(const u16*)(smem + OX + 2 * XO + c * XREG + 2 * prow));
    float e = lo2f(px) + yl * lo2f(*(const u32*)(smem + OSC + 4 * c)) + ql * (1.f + lo2f(pd));
    GL u16* op = out + (size_t)prow * D + 8 * g + c;
    asm volatile("" : "+v"(e), "+v"(op), "+v"(rsel));
    STAMP(8);
    bool ok[4] = {false, false, false, false};
#pragma unroll 1
    for (int spin = 0; spin < SPIN; spin++) {
#pragma unroll
      for (int q = 0; q < 4; q++) if (!ok[q]) pv[q] = ld16(rs, q < 3 ? po + 131072 * q : p3);
#pragma unroll
      for (int q = 0; q < 4; q++) ok[q] = __all((pv[q].y == tag && pv[q].w == tag) || (q == 3 && ln >= 32));
      if (ok[0] && ok[1] && ok[2] && ok[3]) break;
      NAP();
    }
    STAMP(9);
    float s[4] = {};
#pragma unroll
    for (int q = 0; q < 4; q++) { s[0] += lo2f(pv[q].x); s[1] += hi2f(pv[q].x); s[2] += lo2f(pv[q].z); s[3] += hi2f(pv[q].z); }
    swap32(s[0], s[2]); swap32(s[1], s[3]);
    s[0] += s[2]; s[1] += s[3];
    swap16(s[0], s[1]);
    const float upd = rb(rb(s[0] + s[1]) * rsel);
    *op = (u16)(pack2(e + upd, 0.f) & 0xffffu);
    STAMP(4);
  }
  FLUSH();
  asm volatile(".Lrole_end:");
  __builtin_amdgcn_endpgm();
}

extern "C" void kernel_launch(void* const* d_in, const int* in_sizes, int n_in, void* d_out, int out_size,
                              void* d_ws, size_t ws_size, hipStream_t stream) {
  const u16* const* in = (const u16* const*)d_in;
  fused<<<256, 768, 0, stream>>>(in[0], in[1], in[2], in[3], in[4], in[5], in[6], in[8], in[9], in[10], in[11],
                                 (u16*)d_out, (char*)d_ws);
}
```

```cpp
#include <hip/hip_runtime.h>
#include <stdint.h>

#define STR2(x) #x
#define STR(x) STR2(x)
#define LA 4
#define WL2 44
#define WL3 100
#define PH 1246
#define ABL 0
#define PPRIO 3
#define CPRIO 1
#define GDELAY 0
#define ROT 1
#define PRE 2
#define GNT 0
#define OAUX 0
#define LNT 1
#define PNT ""
#define LNNT ""
#define PRENT ""
#define PFN 15
#define PFO 1024
#define DBAR 2
#define THR 1
#define QD 8
#define HNAP0 4
#define HNAP 2
#define KR 3
#define KI 2
#define QE 8
#define QA 5
#define NA 0
#define CH 0
#define G0E 1
#define DDL 0
#define WBM 0
#define WBW 1
#define HQ 3
#define RSL 5
#define DPF 0
#define HELP 0
#define WARM 0
#define HPI 0
#define Q0 6
#define NP 10
#define HW 28
#define NE 0
#define YNAP "s_sleep 2"
#define CODE_KB 24
typedef unsigned short u16;
typedef unsigned int u32;
typedef __attribute__((ext_vector_type(8))) __bf16 bf16x8;
typedef __attribute__((ext_vector_type(2))) __bf16 bf16x2;
typedef __attribute__((ext_vector_type(8))) _Float16 f16x8;
typedef __attribute__((ext_vector_type(2))) _Float16 f16x2;
typedef __attribute__((ext_vector_type(4))) short v4i16;
typedef __attribute__((ext_vector_type(4))) float f32x4;
typedef __attribute__((ext_vector_type(2))) float f32x2;
typedef __attribute__((ext_vector_type(4))) u32 u32x4;
typedef __attribute__((ext_vector_type(2))) u32 u32x2;
typedef __attribute__((ext_vector_type(8))) u32 u32x8;
typedef __attribute__((ext_vector_type(2), aligned(4))) u32 u32x2_a4;
typedef __amdgpu_buffer_rsrc_t rsrc_t;
#define LDS __attribute__((address_space(3)))

#define TLARG
#define TLVAL
#define STAMP(I)
#define STAMPC(I)
#define FLUSH()

constexpr int L = 2048, D = 1024, NM = 128, F = 3584;
constexpr u32 O_ARR = 0x1000, O_RG = 0x4000, O_SG = 0x8000, O_IDX = 0x10000, O_HG = 0x20000,
              O_PG = 0x90000, WS_END = 0x100000;
constexpr int SPIN = 1 << 16;

__device__ __forceinline__ float lo2f(u32 w) { return __uint_as_float(w << 16); }
__device__ __forceinline__ float hi2f(u32 w) { return __uint_as_float(w & 0xffff0000u); }
__device__ __forceinline__ u32 pack2(float a, float b) {
  bf16x2 r = {(__bf16)a, (__bf16)b};
  return __builtin_bit_cast(u32, r);
}
__device__ __forceinline__ u32 packh(float a, float b) {
  f16x2 r = {(_Float16)a, (_Float16)b};
  return __builtin_bit_cast(u32, r);
}
__device__ __forceinline__ float rbt(float f) { return __uint_as_float(pack2(0.f, f)); }
__device__ __forceinline__ float rb(float f) {
  float r;
  asm("v_cvt_pk_bf16_f32 %0, 0, %1" : "=v"(r) : "v"(f));
  return r;
}
__device__ __forceinline__ u32 hadd(u32 a, u32 b) {
  return __builtin_bit_cast(u32, __builtin_bit_cast(f16x2, a) + __builtin_bit_cast(f16x2, b));
}
__device__ __forceinline__ float hlo(u32 a) { return (float)__builtin_bit_cast(f16x2, a).x; }
__device__ __forceinline__ float hhi(u32 a) { return (float)__builtin_bit_cast(f16x2, a).y; }

template <int CTRL, int RM>
__device__ __forceinline__ float dppf(float v) {
  return __builtin_bit_cast(float, __builtin_amdgcn_update_dpp(0, __builtin_bit_cast(int, v), CTRL, RM, 0xf, true));
}
template <int CTRL, int RM>
__device__ __forceinline__ u32 dppu(u32 v) {
  return (u32)__builtin_amdgcn_update_dpp(0, (int)v, CTRL, RM, 0xf, true);
}
__device__ __forceinline__ float rdl(float v, int i) {
  return __builtin_bit_cast(float, __builtin_amdgcn_readlane(__builtin_bit_cast(int, v), i));
}
__device__ __forceinline__ u32 wmaxu(u32 v) {
  v = max(v, dppu<0x111, 0xf>(v)); v = max(v, dppu<0x112, 0xf>(v)); v = max(v, dppu<0x114, 0xf>(v));
  v = max(v, dppu<0x118, 0xf>(v)); v = max(v, dppu<0x142, 0xa>(v)); v = max(v, dppu<0x143, 0xc>(v));
  return (u32)__builtin_amdgcn_readlane((int)v, 63);
}
__device__ __forceinline__ void swap32(float& a, float& b) {
  auto r = __builtin_amdgcn_permlane32_swap(__float_as_uint(a), __float_as_uint(b), false, false);
  a = __uint_as_float(r[0]); b = __uint_as_float(r[1]);
}
__device__ __forceinline__ void swap16(float& a, float& b) {
  auto r = __builtin_amdgcn_permlane16_swap(__float_as_uint(a), __float_as_uint(b), false, false);
  a = __uint_as_float(r[0]); b = __uint_as_float(r[1]);
}
__device__ __forceinline__ u32x2 trread(const char* p) {
  v4i16 r = __builtin_amdgcn_ds_read_tr16_b64_v4i16((LDS v4i16*)p);
  return __builtin_bit_cast(u32x2, r);
}
__device__ __forceinline__ u32x2 trl(LDS char* p) { return __builtin_bit_cast(u32x2, __builtin_amdgcn_ds_read_tr16_b64_v4i16((LDS v4i16*)p)); }
__device__ __forceinline__ f16x8 mkh(u32x2 a, u32x2 b) {
  u32x4 v = {a.x, a.y, b.x, b.y};
  return __builtin_bit_cast(f16x8, v);
}
__device__ __forceinline__ bf16x8 mkb(u32x2 a, u32x2 b) {
  u32x4 v = {a.x, a.y, b.x, b.y};
  return __builtin_bit_cast(bf16x8, v);
}
__device__ __forceinline__ bf16x8 asb(u32x4 v) { return __builtin_bit_cast(bf16x8, v); }
__device__ __forceinline__ f16x8 ash(u32x4 v) { return __builtin_bit_cast(f16x8, v); }
#define MFMAB(a, b, c) __builtin_amdgcn_mfma_f32_16x16x32_bf16(a, b, c, 0, 0, 0)
#define MFMAH(a, b, c) __builtin_amdgcn_mfma_f32_16x16x32_f16(a, b, c, 0, 0, 0)

__device__ __forceinline__ u32x4 ld16(rsrc_t r, u32 off) { return __builtin_amdgcn_raw_buffer_load_b128(r, off, 0, 16); }
__device__ __forceinline__ u32x2 ld8(rsrc_t r, u32 off) { return __builtin_amdgcn_raw_buffer_load_b64(r, off, 0, 16); }
__device__ __forceinline__ void st16(rsrc_t r, u32 off, u32x4 v) { __builtin_amdgcn_raw_buffer_store_b128(v, r, off, 0, 16); }
__device__ __forceinline__ void st8(rsrc_t r, u32 off, u32x2 v) { __builtin_amdgcn_raw_buffer_store_b64(v, r, off, 0, 16); }
#define NAP() asm volatile("s_sleep 1" ::: "memory")
#define SB() __builtin_amdgcn_sched_barrier(0)
#define LBAR() asm volatile("s_waitcnt lgkmcnt(0)\n\ts_barrier" ::: "memory")

__device__ __forceinline__ float fexp(float v) { return __builtin_amdgcn_exp2f(v * 1.4426950408889634f); }
__device__ __forceinline__ float dot2b(u32 a, u32 b, float c) {
  return __builtin_amdgcn_fdot2_f32_bf16(__builtin_bit_cast(bf16x2, a), __builtin_bit_cast(bf16x2, b), c, false);
}
__device__ __forceinline__ float sigm(float v) { return __builtin_amdgcn_rcpf(1.f + __builtin_amdgcn_exp2f(-1.4426950408889634f * v)); }

constexpr int RS = 288, PB = 16 * RS;
constexpr int OW3 = 0, OW5 = 3632, OW7 = 6720, OW9 = 8784, OY = 11648, CHR = OY + 4096;
constexpr int OX = 8 * CHR, XREG = 4640, XO = 266;
constexpr int OMISC = OX + 8 * XREG;
constexpr int OYP = OMISC + 256, OXF = OMISC + 320, OCN = OMISC + 336, OSC = OMISC + 352, OIX = OMISC + 384;
constexpr int OPX = OMISC + 512, SMEM = OMISC + 768;
constexpr int CPE = 704, TB = 2 * PB, CPO = 576;
constexpr int T2B = 2 * CPO + 4, T3B = (OY + 2048 - TB) / 2, C1 = 36, C2 = 52, C3 = 84;
constexpr int FA = 131072, AP = 1056, FH = FA + 8 * AP, HP = 544, FC = FH + 8 * HP;
static_assert(FC + 4480 <= SMEM && SMEM <= 163840, "lds map");

struct Tab { u32 v[2][64][4]; };
constexpr Tab mktab() {
  Tab T = {};
  for (int l = 0; l < 64; l++) {
    const int n16 = l & 15, kg = l >> 4, pc = ((n16 >> 2) & 1) + (n16 & 8) + 2 * (3 - (n16 & 3)), odd = kg >> 1, s = l & 3;
    const int e0 = 278 - 128 - 8 * pc - 4 * (kg & 1) - odd;
    const int ez = ((l >> 3) & 3) * CPE + ((l & 4) ? 544 - 64 : 0) + 16 * (l & 7);
    const int b1 = 28 + C1 * (n16 + 4 * (kg & 1)), b2 = T2B + 44 + C2 * ((n16 & 1) + 2 * (kg & 1)) - 8 * (n16 & 2);
    const int b3 = T3B + 60 + C3 * (kg & 1) - 16 * (n16 - 8);
    const int to = TB + 2 * ((n16 < 4 ? b1 : (n16 & 4) ? b2 : b3) - 4 * odd - 8);
    const int sp = n16 < 8 ? s * (CPE - 2) + 2 * ((l & 4) ? 281 - 257 : (s & 1) ? 281 : 281 - 256) : TB + s * (CPO - 2) + 2 * ((l & 4) ? T2B + 15 : 15);
    T.v[0][l][0] = (2 * e0 + (2 - odd) * (CPE - 2)) | (2 * e0 + 3 * odd * (CPE - 2)) << 16;
    T.v[0][l][1] = ez | to << 16;
    T.v[0][l][2] = sp | (s == 0 ? OW3 : s == 1 ? OW5 : s == 2 ? OW7 : OW9) << 16;
    T.v[0][l][3] = n16 < 8 ? ((l & 5) == 1 ? 0x0100u : 0x0302u) : (l & 4) ? 0x0706u : 0x0504u;
    const int jx = n16 + 5 - 8 * kg;
    for (int r = 0; r < 4; r++) T.v[1][l][r] = jx == 2 * r ? 0xffffu : jx == 2 * r + 1 ? 0xffff0000u : 0u;
  }
  return T;
}
extern "C" __device__ const __attribute__((aligned(128), used, visibility("hidden"))) Tab lane_tab = mktab();

#define GEN_ASM \
  "s_waitcnt vmcnt(2)\n\tv_lshlrev_b32 v56, 16, v96\n\t" \
  "v_and_b32 v57, 0xffff0000, v96\n\tv_pk_mul_f32 v[56:57], %[k], v[56:57] op_sel_hi:[0,1]\n\t" \
  "v_lshlrev_b32 v49, 16, v97\n\tv_exp_f32 v56, v56\n\t" \
  "v_exp_f32 v57, v57\n\tv_and_b32 v51, 0xffff0000, v97\n\t" \
  "v_cvt_pk_bf16_f32 v56, v56, v57\n\tv_lshlrev_b32 v48, 16, v56\n\t" \
  "v_and_b32 v50, 0xffff0000, v56\n\tv_pk_mul_f32 v[48:49], %[c], v[48:49]\n\t" \
  "v_pk_mul_f32 v[50:51], %[c], v[50:51]\n\tv_pk_mul_f32 v[98:99], %[k], v[48:49] op_sel:[1,0]\n\t" \
  "v_pk_mul_f32 v[100:101], %[k], v[50:51] op_sel:[1,0]\n\tv_fract_f32 v49, v49\n\t" \
  "v_fract_f32 v51, v51\n\tv_exp_f32 v48, v48\n\t" \
  "v_exp_f32 v50, v50\n\tv_cos_f32 v40, v49\n\t" \
  "v_sin_f32 v41, v49\n\tv_cos_f32 v42, v51\n\t" \
  "v_sin_f32 v43, v51\n\tv_pk_mul_f32 v[40:41], v[40:41], v[48:49] op_sel_hi:[1,0]\n\t" \
  "v_pk_mul_f32 v[42:43], v[42:43], v[50:51] op_sel_hi:[1,0]\n\tv_pk_mul_f32 v[48:49], v[40:41], v[40:41] op_sel:[1,1] op_sel_hi:[1,0] neg_lo:[1,0]\n\t" \
  "v_pk_mul_f32 v[50:51], v[42:43], v[42:43] op_sel:[1,1] op_sel_hi:[1,0] neg_lo:[1,0]\n\tv_pk_fma_f32 v[48:49], v[40:41], v[40:41], v[48:49] op_sel:[0,0,0] op_sel_hi:[0,1,1]\n\t" \
  "v_pk_fma_f32 v[50:51], v[42:43], v[42:43], v[50:51] op_sel:[0,0,0] op_sel_hi:[0,1,1]\n\tv_cvt_pk_f16_f32 v72, v40, -v41\n\t" \
  "v_cvt_pk_f16_f32 v73, v42, -v43\n\tv_pk_mul_f32 v[52:53], v[48:49], v[40:41] op_sel:[1,1] op_sel_hi:[1,0] neg_lo:[1,0]\n\t" \
  "v_pk_mul_f32 v[54:55], v[50:51], v[42:43] op_sel:[1,1] op_sel_hi:[1,0] neg_lo:[1,0]\n\tv_pk_fma_f32 v[52:53], v[48:49], v[40:41], v[52:53] op_sel:[0,0,0] op_sel_hi:[0,1,1]\n\t" \
  "v_pk_fma_f32 v[54:55], v[50:51], v[42:43], v[54:55] op_sel:[0,0,0] op_sel_hi:[0,1,1]\n\tv_pk_mul_f32 v[56:57], v[48:49], v[48:49] op_sel:[1,1] op_sel_hi:[1,0] neg_lo:[1,0]\n\t" \
  "v_pk_mul_f32 v[58:59], v[50:51], v[50:51] op_sel:[1,1] op_sel_hi:[1,0] neg_lo:[1,0]\n\tv_pk_fma_f32 v[56:57], v[48:49], v[48:49], v[56:57] op_sel:[0,0,0] op_sel_hi:[0,1,1]\n\t" \
  "v_pk_fma_f32 v[58:59], v[50:51], v[50:51], v[58:59] op_sel:[0,0,0] op_sel_hi:[0,1,1]\n\tv_cvt_pk_f16_f32 v74, v48, -v49\n\t" \
  "v_cvt_pk_f16_f32 v75, v50, -v51\n\tv_cvt_pk_f16_f32 v76, v52, -v53\n\t" \
  "v_cvt_pk_f16_f32 v77, v54, -v55\n\tv_pk_mul_f32 v[52:53], v[56:57], v[48:49] op_sel:[1,1] op_sel_hi:[1,0] neg_lo:[1,0]\n\t" \
  "v_pk_mul_f32 v[54:55], v[58:59], v[50:51] op_sel:[1,1] op_sel_hi:[1,0] neg_lo:[1,0]\n\tv_pk_fma_f32 v[52:53], v[56:57], v[48:49], v[52:53] op_sel:[0,0,0] op_sel_hi:[0,1,1]\n\t" \
  "v_pk_fma_f32 v[54:55], v[58:59], v[50:51], v[54:55] op_sel:[0,0,0] op_sel_hi:[0,1,1]\n\tv_pk_mul_f32 v[60:61], v[56:57], v[56:57] op_sel:[1,1] op_sel_hi:[1,0] neg_lo:[1,0]\n\t" \
  "v_pk_mul_f32 v[62:63], v[58:59], v[58:59] op_sel:[1,1] op_sel_hi:[1,0] neg_lo:[1,0]\n\tv_pk_fma_f32 v[60:61], v[56:57], v[56:57], v[60:61] op_sel:[0,0,0] op_sel_hi:[0,1,1]\n\t" \
  "v_pk_fma_f32 v[62:63], v[58:59], v[58:59], v[62:63] op_sel:[0,0,0] op_sel_hi:[0,1,1]\n\tv_cvt_pk_f16_f32 v78, v56, -v57\n\t" \
  "v_cvt_pk_f16_f32 v79, v58, -v59\n\tv_cvt_pk_f16_f32 v82, v52, -v53\n\t" \
  "v_cvt_pk_f16_f32 v83, v54, -v55\n\tv_pk_mul_f16 v80, v78, v72 op_sel:[1,1] op_sel_hi:[1,0] neg_lo:[1,0]\n\t" \
  "v_pk_mul_f16 v81, v79, v73 op_sel:[1,1] op_sel_hi:[1,0] neg_lo:[1,0]\n\tv_pk_fma_f16 v80, v78, v72, v80 op_sel:[0,0,0] op_sel_hi:[0,1,1]\n\t" \
  "v_pk_fma_f16 v81, v79, v73, v81 op_sel:[0,0,0] op_sel_hi:[0,1,1]\n\tv_pk_mul_f16 v84, v78, v76 op_sel:[1,1] op_sel_hi:[1,0] neg_lo:[1,0]\n\t" \
  "v_pk_mul_f16 v85, v79, v77 op_sel:[1,1] op_sel_hi:[1,0] neg_lo:[1,0]\n\tv_pk_fma_f16 v84, v78, v76, v84 op_sel:[0,0,0] op_sel_hi:[0,1,1]\n\t" \
  "v_pk_fma_f16 v85, v79, v77, v85 op_sel:[0,0,0] op_sel_hi:[0,1,1]\n\tv_cvt_pk_f16_f32 v86, v60, -v61\n\t" \
  "v_cvt_pk_f16_f32 v87, v62, -v63\n\tds_write_addtid_b32 v72 offset:288\n\t" \
  "ds_write_addtid_b32 v73 offset:4896\n\tds_write_addtid_b32 v74 offset:576\n\t" \
  "ds_write_addtid_b32 v75 offset:5184\n\tds_write_addtid_b32 v76 offset:864\n\t" \
  "ds_write_addtid_b32 v77 offset:5472\n\tds_write_addtid_b32 v78 offset:1152\n\t" \
  "ds_write_addtid_b32 v79 offset:5760\n\tds_write_addtid_b32 v80 offset:1440\n\t" \
  "ds_write_addtid_b32 v81 offset:6048\n\tds_write_addtid_b32 v82 offset:1728\n\t" \
  "ds_write_addtid_b32 v83 offset:6336\n\tds_write_addtid_b32 v84 offset:2016\n\t" \
  "ds_write_addtid_b32 v85 offset:6624\n\tds_write_addtid_b32 v86 offset:3456\n\t" \
  "ds_write_addtid_b32 v87 offset:8064\n\tds_read_b128 v[104:107], v70 offset:0\n\t" \
  "ds_read_b128 v[108:111], v70 offset:64\n\tds_read_b128 v[112:115], v70 offset:128\n\t" \
  "ds_read_b128 v[116:119], v70 offset:192\n\tds_read_b128 v[120:123], v70 offset:4608\n\t" \
  "ds_read_b128 v[124:127], v70 offset:4672\n\tds_read_b128 v[128:131], v70 offset:4736\n\t" \
  "ds_read_b128 v[132:135], v70 offset:4800\n\tv_fract_f32 v99, v99\n\t" \
  "v_fract_f32 v101, v101\n\tv_exp_f32 v98, v98\n\t" \
  "v_exp_f32 v100, v100\n\tv_cos_f32 v44, v99\n\t" \
  "v_sin_f32 v45, v99\n\tv_cos_f32 v46, v101\n\t" \
  "v_sin_f32 v47, v101\n\tv_pk_mul_f32 v[44:45], v[44:45], v[98:99] op_sel_hi:[1,0]\n\t" \
  "v_pk_mul_f32 v[46:47], v[46:47], v[100:101] op_sel_hi:[1,0]\n\ts_waitcnt vmcnt(0)\n\t" \
  "v_pk_mul_f32 v[48:49], v[44:45], v[44:45] op_sel:[1,1] op_sel_hi:[1,0] neg_lo:[1,0]\n\tv_pk_mul_f32 v[50:51], v[46:47], v[46:47] op_sel:[1,1] op_sel_hi:[1,0] neg_lo:[1,0]\n\t" \
  "v_pk_fma_f32 v[48:49], v[44:45], v[44:45], v[48:49] op_sel:[0,0,0] op_sel_hi:[0,1,1]\n\tv_pk_fma_f32 v[50:51], v[46:47], v[46:47], v[50:51] op_sel:[0,0,0] op_sel_hi:[0,1,1]\n\t" \
  "v_and_b32 v102, 0x7fff7fff, v68\n\tv_cvt_pk_f16_f32 v88, v44, -v45\n\t" \
  "v_cvt_pk_f16_f32 v89, v46, -v47\n\tv_pk_mul_f32 v[52:53], v[48:49], v[44:45] op_sel:[1,1] op_sel_hi:[1,0] neg_lo:[1,0]\n\t" \
  "v_pk_mul_f32 v[54:55], v[50:51], v[46:47] op_sel:[1,1] op_sel_hi:[1,0] neg_lo:[1,0]\n\tv_and_b32 v103, 0x7fff7fff, v69\n\t" \
  "v_pk_fma_f32 v[52:53], v[48:49], v[44:45], v[52:53] op_sel:[0,0,0] op_sel_hi:[0,1,1]\n\tv_pk_fma_f32 v[54:55], v[50:51], v[46:47], v[54:55] op_sel:[0,0,0] op_sel_hi:[0,1,1]\n\t" \
  "v_pk_mul_f32 v[56:57], v[48:49], v[48:49] op_sel:[1,1] op_sel_hi:[1,0] neg_lo:[1,0]\n\tv_pk_mul_f32 v[58:59], v[50:51], v[50:51] op_sel:[1,1] op_sel_hi:[1,0] neg_lo:[1,0]\n\t" \
  "v_pk_max_u16 v102, v102, v103\n\tv_pk_fma_f32 v[56:57], v[48:49], v[48:49], v[56:57] op_sel:[0,0,0] op_sel_hi:[0,1,1]\n\t" \
  "v_pk_fma_f32 v[58:59], v[50:51], v[50:51], v[58:59] op_sel:[0,0,0] op_sel_hi:[0,1,1]\n\tv_cvt_pk_f16_f32 v90, v48, -v49\n\t" \
  "v_cvt_pk_f16_f32 v91, v50, -v51\n\tv_pk_max_u16 v102, v102, v102 op_sel:[1,0] op_sel_hi:[0,1]\n\t" \
  "v_pk_mul_f32 v[64:65], v[56:57], v[60:61] op_sel:[1,1] op_sel_hi:[1,0] neg_lo:[1,0]\n\tv_pk_mul_f32 v[66:67], v[58:59], v[62:63] op_sel:[1,1] op_sel_hi:[1,0] neg_lo:[1,0]\n\t" \
  "v_pk_fma_f32 v[64:65], v[56:57], v[60:61], v[64:65] op_sel:[0,0,0] op_sel_hi:[0,1,1]\n\tv_pk_fma_f32 v[66:67], v[58:59], v[62:63], v[66:67] op_sel:[0,0,0] op_sel_hi:[0,1,1]\n\t" \
  "v_max_u32_dpp v102, v102, v102 row_shr:1 row_mask:0xf bank_mask:0xf\n\tv_cvt_pk_f16_f32 v92, v52, -v53\n\t" \
  "v_cvt_pk_f16_f32 v93, v54, -v55\n\tv_pk_mul_f16 v136, v88, v74 op_sel:[1,1] op_sel_hi:[1,0] neg_lo:[1,0]\n\t" \
  "v_pk_mul_f16 v137, v89, v75 op_sel:[1,1] op_sel_hi:[1,0] neg_lo:[1,0]\n\tv_max_u32_dpp v102, v102, v102 row_shr:2 row_mask:0xf bank_mask:0xf\n\t" \
  "v_pk_fma_f16 v136, v88, v74, v136 op_sel:[0,0,0] op_sel_hi:[0,1,1]\n\tv_pk_fma_f16 v137, v89, v75, v137 op_sel:[0,0,0] op_sel_hi:[0,1,1]\n\t" \
  "v_pk_mul_f16 v138, v88, v78 op_sel:[1,1] op_sel_hi:[1,0] neg_lo:[1,0]\n\tv_pk_mul_f16 v139, v89, v79 op_sel:[1,1] op_sel_hi:[1,0] neg_lo:[1,0]\n\t" \
  "v_max_u32_dpp v102, v102, v102 row_shr:4 row_mask:0xf bank_mask:0xf\n\tv_pk_fma_f16 v138, v88, v78, v138 op_sel:[0,0,0] op_sel_hi:[0,1,1]\n\t" \
  "v_pk_fma_f16 v139, v89, v79, v139 op_sel:[0,0,0] op_sel_hi:[0,1,1]\n\tv_cvt_pk_f16_f32 v94, v64, -v65\n\t" \
  "v_cvt_pk_f16_f32 v95, v66, -v67\n\tv_max_u32_dpp v102, v102, v102 row_shr:8 row_mask:0xf bank_mask:0xf\n\t" \
  "v_pk_mul_f16 v140, v88, v82 op_sel:[1,1] op_sel_hi:[1,0] neg_lo:[1,0]\n\tv_pk_mul_f16 v141, v89, v83 op_sel:[1,1] op_sel_hi:[1,0] neg_lo:[1,0]\n\t" \
  "v_pk_fma_f16 v140, v88, v82, v140 op_sel:[0,0,0] op_sel_hi:[0,1,1]\n\tv_pk_fma_f16 v141, v89, v83, v141 op_sel:[0,0,0] op_sel_hi:[0,1,1]\n\t" \
  "v_max_u32_dpp v102, v102, v102 row_bcast:15 row_mask:0xa bank_mask:0xf\n\tv_pk_mul_f16 v142, v88, v86 op_sel:[1,1] op_sel_hi:[1,0] neg_lo:[1,0]\n\t" \
  "v_pk_mul_f16 v143, v89, v87 op_sel:[1,1] op_sel_hi:[1,0] neg_lo:[1,0]\n\tv_pk_fma_f16 v142, v88, v86, v142 op_sel:[0,0,0] op_sel_hi:[0,1,1]\n\t" \
  "v_pk_fma_f16 v143, v89, v87, v143 op_sel:[0,0,0] op_sel_hi:[0,1,1]\n\tv_max_u32_dpp v102, v102, v102 row_bcast:31 row_mask:0xc bank_mask:0xf\n\t" \
  "v_pk_mul_f16 v144, v90, v78 op_sel:[1,1] op_sel_hi:[1,0] neg_lo:[1,0]\n\tv_pk_mul_f16 v145, v91, v79 op_sel:[1,1] op_sel_hi:[1,0] neg_lo:[1,0]\n\t" \
  "v_pk_fma_f16 v144, v90, v78, v144 op_sel:[0,0,0] op_sel_hi:[0,1,1]\n\tv_pk_fma_f16 v145, v91, v79, v145 op_sel:[0,0,0] op_sel_hi:[0,1,1]\n\t" \
  "v_readlane_b32 %[e], v102, 63\n\tv_pk_mul_f16 v146, v90, v86 op_sel:[1,1] op_sel_hi:[1,0] neg_lo:[1,0]\n\t" \
  "v_pk_mul_f16 v147, v91, v87 op_sel:[1,1] op_sel_hi:[1,0] neg_lo:[1,0]\n\tv_pk_fma_f16 v146, v90, v86, v146 op_sel:[0,0,0] op_sel_hi:[0,1,1]\n\t" \
  "v_pk_fma_f16 v147, v91, v87, v147 op_sel:[0,0,0] op_sel_hi:[0,1,1]\n\tv_pk_mul_f16 v148, v92, v78 op_sel:[1,1] op_sel_hi:[1,0] neg_lo:[1,0]\n\t" \
  "v_pk_mul_f16 v149, v93, v79 op_sel:[1,1] op_sel_hi:[1,0] neg_lo:[1,0]\n\tv_pk_fma_f16 v148, v92, v78, v148 op_sel:[0,0,0] op_sel_hi:[0,1,1]\n\t" \
  "v_pk_fma_f16 v149, v93, v79, v149 op_sel:[0,0,0] op_sel_hi:[0,1,1]\n\tv_pk_mul_f16 v150, v92, v86 op_sel:[1,1] op_sel_hi:[1,0] neg_lo:[1,0]\n\t" \
  "v_pk_mul_f16 v151, v93, v87 op_sel:[1,1] op_sel_hi:[1,0] neg_lo:[1,0]\n\tv_pk_fma_f16 v150, v92, v86, v150 op_sel:[0,0,0] op_sel_hi:[0,1,1]\n\t" \
  "v_pk_fma_f16 v151, v93, v87, v151 op_sel:[0,0,0] op_sel_hi:[0,1,1]\n\tv_pk_mul_f16 v96, v94, v88 op_sel:[1,1] op_sel_hi:[1,0] neg_lo:[1,0]\n\t" \
  "v_pk_mul_f16 v97, v95, v89 op_sel:[1,1] op_sel_hi:[1,0] neg_lo:[1,0]\n\tv_pk_fma_f16 v96, v94, v88, v96 op_sel:[0,0,0] op_sel_hi:[0,1,1]\n\t" \
  "v_pk_fma_f16 v97, v95, v89, v97 op_sel:[0,0,0] op_sel_hi:[0,1,1]\n\tv_pk_mul_f16 v98, v94, v90 op_sel:[1,1] op_sel_hi:[1,0] neg_lo:[1,0]\n\t" \
  "v_pk_mul_f16 v99, v95, v91 op_sel:[1,1] op_sel_hi:[1,0] neg_lo:[1,0]\n\tv_pk_fma_f16 v98, v94, v90, v98 op_sel:[0,0,0] op_sel_hi:[0,1,1]\n\t" \
  "v_pk_fma_f16 v99, v95, v91, v99 op_sel:[0,0,0] op_sel_hi:[0,1,1]\n\tv_pk_mul_f16 v100, v94, v92 op_sel:[1,1] op_sel_hi:[1,0] neg_lo:[1,0]\n\t" \
  "v_pk_mul_f16 v101, v95, v93 op_sel:[1,1] op_sel_hi:[1,0] neg_lo:[1,0]\n\tv_pk_fma_f16 v100, v94, v92, v100 op_sel:[0,0,0] op_sel_hi:[0,1,1]\n\t" \
  "v_pk_fma_f16 v101, v95, v93, v101 op_sel:[0,0,0] op_sel_hi:[0,1,1]\n\ts_bfe_u32 %[e], %[e], 0x80007\n\t" \
  "s_max_u32 %[e], %[e], 8\n\ts_sub_u32 %[t], 260, %[e]\n\t" \
  "s_lshl_b32 %[t], %[t], 23\n\tds_write_addtid_b32 v136 offset:0\n\t" \
  "ds_write_addtid_b32 v137 offset:4608\n\tds_write_addtid_b32 v138 offset:288\n\t" \
  "ds_write_addtid_b32 v139 offset:4896\n\tds_write_addtid_b32 v140 offset:576\n\t" \
  "ds_write_addtid_b32 v141 offset:5184\n\tds_write_addtid_b32 v142 offset:864\n\t" \
  "ds_write_addtid_b32 v143 offset:5472\n\tds_write_addtid_b32 v144 offset:1152\n\t" \
  "ds_write_addtid_b32 v145 offset:5760\n\tds_write_addtid_b32 v146 offset:1440\n\t" \
  "ds_write_addtid_b32 v147 offset:6048\n\tds_write_addtid_b32 v148 offset:1728\n\t" \
  "ds_write_addtid_b32 v149 offset:6336\n\tds_write_addtid_b32 v150 offset:2016\n\t" \
  "ds_write_addtid_b32 v151 offset:6624\n\tds_write_addtid_b32 v94 offset:2304\n\t" \
  "ds_write_addtid_b32 v95 offset:6912\n\tds_write_addtid_b32 v96 offset:2592\n\t" \
  "ds_write_addtid_b32 v97 offset:7200\n\tds_write_addtid_b32 v98 offset:2880\n\t" \
  "ds_write_addtid_b32 v99 offset:7488\n\tds_write_addtid_b32 v100 offset:3168\n\t" \
  "ds_write_addtid_b32 v101 offset:7776\n\tds_read_b128 v[136:139], v71 offset:0\n\t" \
  "ds_read_b128 v[140:143], v71 offset:64\n\tds_read_b128 v[144:147], v71 offset:128\n\t" \
  "ds_read_b128 v[148:151], v71 offset:192\n\tds_read_b128 v[152:155], v71 offset:4608\n\t" \
  "ds_read_b128 v[156:159], v71 offset:4672\n\tds_read_b128 v[160:163], v71 offset:4736\n\t" \
  "ds_read_b128 v[164:167], v71 offset:4800\n\tv_pk_mul_f32 v[48:49], v[60:61], v[60:61] op_sel:[1,1] op_sel_hi:[1,0] neg_lo:[1,0]\n\t" \
  "v_pk_mul_f32 v[50:51], v[62:63], v[62:63] op_sel:[1,1] op_sel_hi:[1,0] neg_lo:[1,0]\n\tv_pk_fma_f32 v[48:49], v[60:61], v[60:61], v[48:49] op_sel:[0,0,0] op_sel_hi:[0,1,1]\n\t" \
  "v_pk_fma_f32 v[50:51], v[62:63], v[62:63], v[50:51] op_sel:[0,0,0] op_sel_hi:[0,1,1]\n\tv_cvt_pk_f16_f32 v44, v60, v61\n\t" \
  "v_cvt_pk_f16_f32 v45, v62, v63\n\tv_pk_mul_f32 v[52:53], v[48:49], v[60:61] op_sel:[1,1] op_sel_hi:[1,0] neg_lo:[1,0]\n\t" \
  "v_pk_mul_f32 v[54:55], v[50:51], v[62:63] op_sel:[1,1] op_sel_hi:[1,0] neg_lo:[1,0]\n\tv_pk_fma_f32 v[52:53], v[48:49], v[60:61], v[52:53] op_sel:[0,0,0] op_sel_hi:[0,1,1]\n\t" \
  "v_pk_fma_f32 v[54:55], v[50:51], v[62:63], v[54:55] op_sel:[0,0,0] op_sel_hi:[0,1,1]\n\tv_pk_mul_f32 v[40:41], v[48:49], v[48:49] op_sel:[1,1] op_sel_hi:[1,0] neg_lo:[1,0]\n\t" \
  "v_pk_mul_f32 v[42:43], v[50:51], v[50:51] op_sel:[1,1] op_sel_hi:[1,0] neg_lo:[1,0]\n\tv_pk_fma_f32 v[40:41], v[48:49], v[48:49], v[40:41] op_sel:[0,0,0] op_sel_hi:[0,1,1]\n\t" \
  "v_pk_fma_f32 v[42:43], v[50:51], v[50:51], v[42:43] op_sel:[0,0,0] op_sel_hi:[0,1,1]\n\tv_cvt_pk_f16_f32 v46, v48, v49\n\t" \
  "v_cvt_pk_f16_f32 v47, v50, v51\n\tv_cvt_pk_f16_f32 v56, v52, v53\n\t" \
  "v_cvt_pk_f16_f32 v57, v54, v55\n\tv_lshlrev_b32 v64, 16, v68\n\t" \
  "v_lshlrev_b32 v65, 16, v69\n\tv_and_b32 v66, 0xffff0000, v68\n\t" \
  "v_and_b32 v67, 0xffff0000, v69\n\tv_mul_f32 v64, %[t], v64\n\t" \
  "v_mul_f32 v65, %[t], v65\n\tv_mul_f32 v66, %[t], v66\n\t" \
  "v_mul_f32 v67, %[t], v67\n\tv_cvt_pk_f16_f32 v48, v64, v65\n\t" \
  "v_cvt_pk_f16_f32 v49, v66, v67\n\tv_pk_mul_f32 v[68:69], v[64:65], v[40:41] op_sel:[1,1] op_sel_hi:[1,0] neg_lo:[1,0]\n\t" \
  "v_pk_mul_f32 v[70:71], v[66:67], v[42:43] op_sel:[1,1] op_sel_hi:[1,0] neg_lo:[1,0]\n\tv_pk_fma_f32 v[68:69], v[64:65], v[40:41], v[68:69] op_sel:[0,0,0] op_sel_hi:[0,1,1]\n\t" \
  "v_pk_fma_f32 v[70:71], v[66:67], v[42:43], v[70:71] op_sel:[0,0,0] op_sel_hi:[0,1,1]\n\tv_pk_mul_f16 v50, v48, v44 op_sel:[1,1] op_sel_hi:[1,0] neg_lo:[1,0]\n\t" \
  "v_pk_mul_f16 v51, v49, v45 op_sel:[1,1] op_sel_hi:[1,0] neg_lo:[1,0]\n\tv_pk_fma_f16 v50, v48, v44, v50 op_sel:[0,0,0] op_sel_hi:[0,1,1]\n\t" \
  "v_pk_fma_f16 v51, v49, v45, v51 op_sel:[0,0,0] op_sel_hi:[0,1,1]\n\tv_pk_mul_f16 v52, v48, v46 op_sel:[1,1] op_sel_hi:[1,0] neg_lo:[1,0]\n\t" \
  "v_pk_mul_f16 v53, v49, v47 op_sel:[1,1] op_sel_hi:[1,0] neg_lo:[1,0]\n\tv_pk_fma_f16 v52, v48, v46, v52 op_sel:[0,0,0] op_sel_hi:[0,1,1]\n\t" \
  "v_pk_fma_f16 v53, v49, v47, v53 op_sel:[0,0,0] op_sel_hi:[0,1,1]\n\tv_pk_mul_f16 v54, v48, v56 op_sel:[1,1] op_sel_hi:[1,0] neg_lo:[1,0]\n\t" \
  "v_pk_mul_f16 v55, v49, v57 op_sel:[1,1] op_sel_hi:[1,0] neg_lo:[1,0]\n\tv_pk_fma_f16 v54, v48, v56, v54 op_sel:[0,0,0] op_sel_hi:[0,1,1]\n\t" \
  "v_pk_fma_f16 v55, v49, v57, v55 op_sel:[0,0,0] op_sel_hi:[0,1,1]\n\tds_write_addtid_b32 v48 offset:0\n\t" \
  "ds_write_addtid_b32 v49 offset:4608\n\tds_write_addtid_b32 v50 offset:288\n\t" \
  "ds_write_addtid_b32 v51 offset:4896\n\tds_write_addtid_b32 v52 offset:576\n\t" \
  "ds_write_addtid_b32 v53 offset:5184\n\tds_write_addtid_b32 v54 offset:864\n\t" \
  "ds_write_addtid_b32 v55 offset:5472\n\tv_cvt_pk_f16_f32 v48, v68, v69\n\t" \
  "v_cvt_pk_f16_f32 v49, v70, v71\n\tv_pk_mul_f32 v[64:65], v[68:69], v[40:41] op_sel:[1,1] op_sel_hi:[1,0] neg_lo:[1,0]\n\t" \
  "v_pk_mul_f32 v[66:67], v[70:71], v[42:43] op_sel:[1,1] op_sel_hi:[1,0] neg_lo:[1,0]\n\tv_pk_fma_f32 v[64:65], v[68:69], v[40:41], v[64:65] op_sel:[0,0,0] op_sel_hi:[0,1,1]\n\t" \
  "v_pk_fma_f32 v[66:67], v[70:71], v[42:43], v[66:67] op_sel:[0,0,0] op_sel_hi:[0,1,1]\n\tv_pk_mul_f16 v50, v48, v44 op_sel:[1,1] op_sel_hi:[1,0] neg_lo:[1,0]\n\t" \
  "v_pk_mul_f16 v51, v49, v45 op_sel:[1,1] op_sel_hi:[1,0] neg_lo:[1,0]\n\tv_pk_fma_f16 v50, v48, v44, v50 op_sel:[0,0,0] op_sel_hi:[0,1,1]\n\t" \
  "v_pk_fma_f16 v51, v49, v45, v51 op_sel:[0,0,0] op_sel_hi:[0,1,1]\n\tv_pk_mul_f16 v52, v48, v46 op_sel:[1,1] op_sel_hi:[1,0] neg_lo:[1,0]\n\t" \
  "v_pk_mul_f16 v53, v49, v47 op_sel:[1,1] op_sel_hi:[1,0] neg_lo:[1,0]\n\tv_pk_fma_f16 v52, v48, v46, v52 op_sel:[0,0,0] op_sel_hi:[0,1,1]\n\t" \
  "v_pk_fma_f16 v53, v49, v47, v53 op_sel:[0,0,0] op_sel_hi:[0,1,1]\n\tv_pk_mul_f16 v54, v48, v56 op_sel:[1,1] op_sel_hi:[1,0] neg_lo:[1,0]\n\t" \
  "v_pk_mul_f16 v55, v49, v57 op_sel:[1,1] op_sel_hi:[1,0] neg_lo:[1,0]\n\tv_pk_fma_f16 v54, v48, v56, v54 op_sel:[0,0,0] op_sel_hi:[0,1,1]\n\t" \
  "v_pk_fma_f16 v55, v49, v57, v55 op_sel:[0,0,0] op_sel_hi:[0,1,1]\n\tds_write_addtid_b32 v48 offset:1152\n\t" \
  "ds_write_addtid_b32 v49 offset:5760\n\tds_write_addtid_b32 v50 offset:1440\n\t" \
  "ds_write_addtid_b32 v51 offset:6048\n\tds_write_addtid_b32 v52 offset:1728\n\t" \
  "ds_write_addtid_b32 v53 offset:6336\n\tds_write_addtid_b32 v54 offset:2016\n\t" \
  "ds_write_addtid_b32 v55 offset:6624\n\tv_cvt_pk_f16_f32 v48, v64, v65\n\t" \
  "v_cvt_pk_f16_f32 v49, v66, v67\n\tv_pk_mul_f32 v[68:69], v[64:65], v[40:41] op_sel:[1,1] op_sel_hi:[1,0] neg_lo:[1,0]\n\t" \
  "v_pk_mul_f32 v[70:71], v[66:67], v[42:43] op_sel:[1,1] op_sel_hi:[1,0] neg_lo:[1,0]\n\tv_pk_fma_f32 v[68:69], v[64:65], v[40:41], v[68:69] op_sel:[0,0,0] op_sel_hi:[0,1,1]\n\t" \
  "v_pk_fma_f32 v[70:71], v[66:67], v[42:43], v[70:71] op_sel:[0,0,0] op_sel_hi:[0,1,1]\n\tv_pk_mul_f16 v50, v48, v44 op_sel:[1,1] op_sel_hi:[1,0] neg_lo:[1,0]\n\t" \
  "v_pk_mul_f16 v51, v49, v45 op_sel:[1,1] op_sel_hi:[1,0] neg_lo:[1,0]\n\tv_pk_fma_f16 v50, v48, v44, v50 op_sel:[0,0,0] op_sel_hi:[0,1,1]\n\t" \
  "v_pk_fma_f16 v51, v49, v45, v51 op_sel:[0,0,0] op_sel_hi:[0,1,1]\n\tv_pk_mul_f16 v52, v48, v46 op_sel:[1,1] op_sel_hi:[1,0] neg_lo:[1,0]\n\t" \
  "v_pk_mul_f16 v53, v49, v47 op_sel:[1,1] op_sel_hi:[1,0] neg_lo:[1,0]\n\tv_pk_fma_f16 v52, v48, v46, v52 op_sel:[0,0,0] op_sel_hi:[0,1,1]\n\t" \
  "v_pk_fma_f16 v53, v49, v47, v53 op_sel:[0,0,0] op_sel_hi:[0,1,1]\n\tv_pk_mul_f16 v54, v48, v56 op_sel:[1,1] op_sel_hi:[1,0] neg_lo:[1,0]\n\t" \
  "v_pk_mul_f16 v55, v49, v57 op_sel:[1,1] op_sel_hi:[1,0] neg_lo:[1,0]\n\tv_pk_fma_f16 v54, v48, v56, v54 op_sel:[0,0,0] op_sel_hi:[0,1,1]\n\t" \
  "v_pk_fma_f16 v55, v49, v57, v55 op_sel:[0,0,0] op_sel_hi:[0,1,1]\n\tds_write_addtid_b32 v48 offset:2304\n\t" \
  "ds_write_addtid_b32 v49 offset:6912\n\tds_write_addtid_b32 v50 offset:2592\n\t" \
  "ds_write_addtid_b32 v51 offset:7200\n\tds_write_addtid_b32 v52 offset:2880\n\t" \
  "ds_write_addtid_b32 v53 offset:7488\n\tds_write_addtid_b32 v54 offset:3168\n\t" \
  "ds_write_addtid_b32 v55 offset:7776\n\tv_cvt_pk_f16_f32 v48, v68, v69\n\t" \
  "v_cvt_pk_f16_f32 v49, v70, v71\n\tv_pk_mul_f32 v[64:65], v[68:69], v[40:41] op_sel:[1,1] op_sel_hi:[1,0] neg_lo:[1,0]\n\t" \
  "v_pk_mul_f32 v[66:67], v[70:71], v[42:43] op_sel:[1,1] op_sel_hi:[1,0] neg_lo:[1,0]\n\tv_pk_fma_f32 v[64:65], v[68:69], v[40:41], v[64:65] op_sel:[0,0,0] op_sel_hi:[0,1,1]\n\t" \
  "v_pk_fma_f32 v[66:67], v[70:71], v[42:43], v[66:67] op_sel:[0,0,0] op_sel_hi:[0,1,1]\n\tv_pk_mul_f16 v50, v48, v44 op_sel:[1,1] op_sel_hi:[1,0] neg_lo:[1,0]\n\t" \
  "v_pk_mul_f16 v51, v49, v45 op_sel:[1,1] op_sel_hi:[1,0] neg_lo:[1,0]\n\tv_pk_fma_f16 v50, v48, v44, v50 op_sel:[0,0,0] op_sel_hi:[0,1,1]\n\t" \
  "v_pk_fma_f16 v51, v49, v45, v51 op_sel:[0,0,0] op_sel_hi:[0,1,1]\n\tv_pk_mul_f16 v52, v48, v46 op_sel:[1,1] op_sel_hi:[1,0] neg_lo:[1,0]\n\t" \
  "v_pk_mul_f16 v53, v49, v47 op_sel:[1,1] op_sel_hi:[1,0] neg_lo:[1,0]\n\tv_pk_fma_f16 v52, v48, v46, v52 op_sel:[0,0,0] op_sel_hi:[0,1,1]\n\t" \
  "v_pk_fma_f16 v53, v49, v47, v53 op_sel:[0,0,0] op_sel_hi:[0,1,1]\n\tv_pk_mul_f16 v54, v48, v56 op_sel:[1,1] op_sel_hi:[1,0] neg_lo:[1,0]\n\t" \
  "v_pk_mul_f16 v55, v49, v57 op_sel:[1,1] op_sel_hi:[1,0] neg_lo:[1,0]\n\tv_pk_fma_f16 v54, v48, v56, v54 op_sel:[0,0,0] op_sel_hi:[0,1,1]\n\t" \
  "v_pk_fma_f16 v55, v49, v57, v55 op_sel:[0,0,0] op_sel_hi:[0,1,1]\n\tds_write_addtid_b32 v48 offset:3456\n\t" \
  "ds_write_addtid_b32 v49 offset:8064\n\tds_write_addtid_b32 v50 offset:3744\n\t" \
  "ds_write_addtid_b32 v51 offset:8352\n\tds_write_addtid_b32 v52 offset:4032\n\t" \
  "ds_write_addtid_b32 v53 offset:8640\n\tds_write_addtid_b32 v54 offset:4320\n\t" \
  "ds_write_addtid_b32 v55 offset:8928\n\tds_read_b128 v[72:75], v27 offset:0\n\t" \
  "ds_read_b128 v[76:79], v27 offset:64\n\tds_read_b128 v[80:83], v27 offset:128\n\t" \
  "ds_read_b128 v[84:87], v27 offset:192\n\tds_read_b128 v[88:91], v27 offset:4608\n\t" \
  "ds_read_b128 v[92:95], v27 offset:4672\n\tds_read_b128 v[96:99], v27 offset:4736\n\t" \
  "ds_read_b128 v[100:103], v27 offset:4800\n\tv_cvt_pk_f16_f32 v48, v64, v65\n\t" \
  "v_cvt_pk_f16_f32 v49, v66, v67\n\tv_pk_mul_f32 v[68:69], v[64:65], v[40:41] op_sel:[1,1] op_sel_hi:[1,0] neg_lo:[1,0]\n\t" \
  "v_pk_mul_f32 v[70:71], v[66:67], v[42:43] op_sel:[1,1] op_sel_hi:[1,0] neg_lo:[1,0]\n\tv_pk_fma_f32 v[68:69], v[64:65], v[40:41], v[68:69] op_sel:[0,0,0] op_sel_hi:[0,1,1]\n\t" \
  "v_pk_fma_f32 v[70:71], v[66:67], v[42:43], v[70:71] op_sel:[0,0,0] op_sel_hi:[0,1,1]\n\tv_pk_mul_f16 v50, v48, v44 op_sel:[1,1] op_sel_hi:[1,0] neg_lo:[1,0]\n\t" \
  "v_pk_mul_f16 v51, v49, v45 op_sel:[1,1] op_sel_hi:[1,0] neg_lo:[1,0]\n\tv_pk_fma_f16 v50, v48, v44, v50 op_sel:[0,0,0] op_sel_hi:[0,1,1]\n\t" \
  "v_pk_fma_f16 v51, v49, v45, v51 op_sel:[0,0,0] op_sel_hi:[0,1,1]\n\tv_pk_mul_f16 v52, v48, v46 op_sel:[1,1] op_sel_hi:[1,0] neg_lo:[1,0]\n\t" \
  "v_pk_mul_f16 v53, v49, v47 op_sel:[1,1] op_sel_hi:[1,0] neg_lo:[1,0]\n\tv_pk_fma_f16 v52, v48, v46, v52 op_sel:[0,0,0] op_sel_hi:[0,1,1]\n\t" \
  "v_pk_fma_f16 v53, v49, v47, v53 op_sel:[0,0,0] op_sel_hi:[0,1,1]\n\tv_pk_mul_f16 v54, v48, v56 op_sel:[1,1] op_sel_hi:[1,0] neg_lo:[1,0]\n\t" \
  "v_pk_mul_f16 v55, v49, v57 op_sel:[1,1] op_sel_hi:[1,0] neg_lo:[1,0]\n\tv_pk_fma_f16 v54, v48, v56, v54 op_sel:[0,0,0] op_sel_hi:[0,1,1]\n\t" \
  "v_pk_fma_f16 v55, v49, v57, v55 op_sel:[0,0,0] op_sel_hi:[0,1,1]\n\ts_waitcnt lgkmcnt(0)\n\t" \
  "v_mfma_f32_16x16x32_f16 v[60:63], v[104:107], v[72:75], 0\n\tv_mfma_f32_16x16x32_f16 v[36:39], v[72:75], v[136:139], 0\n\t" \
  "ds_write_addtid_b32 v48 offset:0\n\tds_write_addtid_b32 v49 offset:4608\n\t" \
  "ds_write_addtid_b32 v50 offset:288\n\tds_write_addtid_b32 v51 offset:4896\n\t" \
  "v_mfma_f32_16x16x32_f16 v[60:63], v[108:111], v[76:79], v[60:63]\n\tds_write_addtid_b32 v52 offset:576\n\t" \
  "ds_write_addtid_b32 v53 offset:5184\n\tds_write_addtid_b32 v54 offset:864\n\t" \
  "ds_write_addtid_b32 v55 offset:5472\n\tv_mfma_f32_16x16x32_f16 v[36:39], v[76:79], v[140:143], v[36:39]\n\t" \
  "v_cvt_pk_f16_f32 v48, v68, v69\n\tv_cvt_pk_f16_f32 v49, v70, v71\n\t" \
  "v_pk_mul_f32 v[64:65], v[68:69], v[40:41] op_sel:[1,1] op_sel_hi:[1,0] neg_lo:[1,0]\n\tv_pk_mul_f32 v[66:67], v[70:71], v[42:43] op_sel:[1,1] op_sel_hi:[1,0] neg_lo:[1,0]\n\t" \
  "v_pk_fma_f32 v[64:65], v[68:69], v[40:41], v[64:65] op_sel:[0,0,0] op_sel_hi:[0,1,1]\n\tv_pk_fma_f32 v[66:67], v[70:71], v[42:43], v[66:67] op_sel:[0,0,0] op_sel_hi:[0,1,1]\n\t" \
  "v_pk_mul_f16 v50, v48, v44 op_sel:[1,1] op_sel_hi:[1,0] neg_lo:[1,0]\n\tv_pk_mul_f16 v51, v49, v45 op_sel:[1,1] op_sel_hi:[1,0] neg_lo:[1,0]\n\t" \
  "v_mfma_f32_16x16x32_f16 v[60:63], v[112:115], v[80:83], v[60:63]\n\tv_pk_fma_f16 v50, v48, v44, v50 op_sel:[0,0,0] op_sel_hi:[0,1,1]\n\t" \
  "v_pk_fma_f16 v51, v49, v45, v51 op_sel:[0,0,0] op_sel_hi:[0,1,1]\n\tv_pk_mul_f16 v52, v48, v46 op_sel:[1,1] op_sel_hi:[1,0] neg_lo:[1,0]\n\t" \
  "v_pk_mul_f16 v53, v49, v47 op_sel:[1,1] op_sel_hi:[1,0] neg_lo:[1,0]\n\tv_pk_fma_f16 v52, v48, v46, v52 op_sel:[0,0,0] op_sel_hi:[0,1,1]\n\t" \
  "v_pk_fma_f16 v53, v49, v47, v53 op_sel:[0,0,0] op_sel_hi:[0,1,1]\n\tv_pk_mul_f16 v54, v48, v56 op_sel:[1,1] op_sel_hi:[1,0] neg_lo:[1,0]\n\t" \
  "v_pk_mul_f16 v55, v49, v57 op_sel:[1,1] op_sel_hi:[1,0] neg_lo:[1,0]\n\tv_pk_fma_f16 v54, v48, v56, v54 op_sel:[0,0,0] op_sel_hi:[0,1,1]\n\t" \
  "v_pk_fma_f16 v55, v49, v57, v55 op_sel:[0,0,0] op_sel_hi:[0,1,1]\n\tv_mfma_f32_16x16x32_f16 v[36:39], v[80:83], v[144:147], v[36:39]\n\t" \
  "ds_write_addtid_b32 v48 offset:1152\n\tds_write_addtid_b32 v49 offset:5760\n\t" \
  "ds_write_addtid_b32 v50 offset:1440\n\tds_write_addtid_b32 v51 offset:6048\n\t" \
  "v_mfma_f32_16x16x32_f16 v[60:63], v[116:119], v[84:87], v[60:63]\n\tds_write_addtid_b32 v52 offset:1728\n\t" \
  "ds_write_addtid_b32 v53 offset:6336\n\tds_write_addtid_b32 v54 offset:2016\n\t" \
  "ds_write_addtid_b32 v55 offset:6624\n\tv_mfma_f32_16x16x32_f16 v[36:39], v[84:87], v[148:151], v[36:39]\n\t" \
  "v_cvt_pk_f16_f32 v48, v64, v65\n\tv_cvt_pk_f16_f32 v49, v66, v67\n\t" \
  "v_pk_mul_f32 v[68:69], v[64:65], v[40:41] op_sel:[1,1] op_sel_hi:[1,0] neg_lo:[1,0]\n\tv_pk_mul_f32 v[70:71], v[66:67], v[42:43] op_sel:[1,1] op_sel_hi:[1,0] neg_lo:[1,0]\n\t" \
  "v_pk_fma_f32 v[68:69], v[64:65], v[40:41], v[68:69] op_sel:[0,0,0] op_sel_hi:[0,1,1]\n\tv_pk_fma_f32 v[70:71], v[66:67], v[42:43], v[70:71] op_sel:[0,0,0] op_sel_hi:[0,1,1]\n\t" \
  "v_pk_mul_f16 v50, v48, v44 op_sel:[1,1] op_sel_hi:[1,0] neg_lo:[1,0]\n\tv_pk_mul_f16 v51, v49, v45 op_sel:[1,1] op_sel_hi:[1,0] neg_lo:[1,0]\n\t" \
  "v_mfma_f32_16x16x32_f16 v[60:63], v[120:123], v[88:91], v[60:63]\n\tv_pk_fma_f16 v50, v48, v44, v50 op_sel:[0,0,0] op_sel_hi:[0,1,1]\n\t" \
  "v_pk_fma_f16 v51, v49, v45, v51 op_sel:[0,0,0] op_sel_hi:[0,1,1]\n\tv_pk_mul_f16 v52, v48, v46 op_sel:[1,1] op_sel_hi:[1,0] neg_lo:[1,0]\n\t" \
  "v_pk_mul_f16 v53, v49, v47 op_sel:[1,1] op_sel_hi:[1,0] neg_lo:[1,0]\n\tv_pk_fma_f16 v52, v48, v46, v52 op_sel:[0,0,0] op_sel_hi:[0,1,1]\n\t" \
  "v_pk_fma_f16 v53, v49, v47, v53 op_sel:[0,0,0] op_sel_hi:[0,1,1]\n\tv_pk_mul_f16 v54, v48, v56 op_sel:[1,1] op_sel_hi:[1,0] neg_lo:[1,0]\n\t" \
  "v_pk_mul_f16 v55, v49, v57 op_sel:[1,1] op_sel_hi:[1,0] neg_lo:[1,0]\n\tv_pk_fma_f16 v54, v48, v56, v54 op_sel:[0,0,0] op_sel_hi:[0,1,1]\n\t" \
  "v_pk_fma_f16 v55, v49, v57, v55 op_sel:[0,0,0] op_sel_hi:[0,1,1]\n\tv_mfma_f32_16x16x32_f16 v[36:39], v[88:91], v[152:155], v[36:39]\n\t" \
  "ds_write_addtid_b32 v48 offset:2304\n\tds_write_addtid_b32 v49 offset:6912\n\t" \
  "ds_write_addtid_b32 v50 offset:2592\n\tds_write_addtid_b32 v51 offset:7200\n\t" \
  "v_mfma_f32_16x16x32_f16 v[60:63], v[124:127], v[92:95], v[60:63]\n\tds_write_addtid_b32 v52 offset:2880\n\t" \
  "ds_write_addtid_b32 v53 offset:7488\n\tds_write_addtid_b32 v54 offset:3168\n\t" \
  "ds_write_addtid_b32 v55 offset:7776\n\tv_mfma_f32_16x16x32_f16 v[36:39], v[92:95], v[156:159], v[36:39]\n\t" \
  "v_cvt_pk_f16_f32 v48, v68, v69\n\tv_cvt_pk_f16_f32 v49, v70, v71\n\t" \
  "v_pk_mul_f16 v50, v48, v44 op_sel:[1,1] op_sel_hi:[1,0] neg_lo:[1,0]\n\tv_pk_mul_f16 v51, v49, v45 op_sel:[1,1] op_sel_hi:[1,0] neg_lo:[1,0]\n\t" \
  "v_pk_fma_f16 v50, v48, v44, v50 op_sel:[0,0,0] op_sel_hi:[0,1,1]\n\tv_pk_fma_f16 v51, v49, v45, v51 op_sel:[0,0,0] op_sel_hi:[0,1,1]\n\t" \
  "v_pk_mul_f16 v52, v48, v46 op_sel:[1,1] op_sel_hi:[1,0] neg_lo:[1,0]\n\tv_pk_mul_f16 v53, v49, v47 op_sel:[1,1] op_sel_hi:[1,0] neg_lo:[1,0]\n\t" \
  "v_mfma_f32_16x16x32_f16 v[60:63], v[128:131], v[96:99], v[60:63]\n\tv_pk_fma_f16 v52, v48, v46, v52 op_sel:[0,0,0] op_sel_hi:[0,1,1]\n\t" \
  "v_pk_fma_f16 v53, v49, v47, v53 op_sel:[0,0,0] op_sel_hi:[0,1,1]\n\tv_pk_mul_f16 v54, v48, v56 op_sel:[1,1] op_sel_hi:[1,0] neg_lo:[1,0]\n\t" \
  "v_pk_mul_f16 v55, v49, v57 op_sel:[1,1] op_sel_hi:[1,0] neg_lo:[1,0]\n\tv_pk_fma_f16 v54, v48, v56, v54 op_sel:[0,0,0] op_sel_hi:[0,1,1]\n\t" \
  "v_pk_fma_f16 v55, v49, v57, v55 op_sel:[0,0,0] op_sel_hi:[0,1,1]\n\tv_mfma_f32_16x16x32_f16 v[36:39], v[96:99], v[160:163], v[36:39]\n\t" \
  "ds_write_addtid_b32 v48 offset:3456\n\tds_write_addtid_b32 v49 offset:8064\n\t" \
  "ds_write_addtid_b32 v50 offset:3744\n\tds_write_addtid_b32 v51 offset:8352\n\t" \
  "v_mfma_f32_16x16x32_f16 v[60:63], v[132:135], v[100:103], v[60:63]\n\tds_write_addtid_b32 v52 offset:4032\n\t" \
  "ds_write_addtid_b32 v53 offset:8640\n\tds_write_addtid_b32 v54 offset:4320\n\t" \
  "ds_write_addtid_b32 v55 offset:8928\n\tv_mfma_f32_16x16x32_f16 v[36:39], v[100:103], v[164:167], v[36:39]\n\t" \
  "ds_read_b128 v[72:75], v27 offset:0\n\tds_read_b128 v[76:79], v27 offset:64\n\t" \
  "ds_read_b128 v[80:83], v27 offset:128\n\tds_read_b128 v[84:87], v27 offset:192\n\t" \
  "ds_read_b128 v[88:91], v27 offset:4608\n\tds_read_b128 v[92:95], v27 offset:4672\n\t" \
  "ds_read_b128 v[96:99], v27 offset:4736\n\tds_read_b128 v[100:103], v27 offset:4800\n\t" \
  "v_add_u32_sdwa v56, %[w], v16 dst_sel:DWORD dst_unused:UNUSED_PAD src0_sel:DWORD src1_sel:WORD_0\n\t" \
  "v_add_u32_sdwa v57, %[w], v16 dst_sel:DWORD dst_unused:UNUSED_PAD src0_sel:DWORD src1_sel:WORD_1\n\tv_add_u32_sdwa v58, %[w], v17 dst_sel:DWORD dst_unused:UNUSED_PAD src0_sel:DWORD src1_sel:WORD_0\n\t" \
  "v_add_u32_sdwa v59, %[w], v17 dst_sel:DWORD dst_unused:UNUSED_PAD src0_sel:DWORD src1_sel:WORD_1\n\tv_mov_b32 v52, 0\n\t" \
  "v_mov_b32 v53, 0\n\tv_mov_b32 v54, 0\n\t" \
  "v_mov_b32 v55, 0\n\tv_cvt_pk_f16_f32 v42, v36, v37\n\t" \
  "v_cvt_pk_f16_f32 v43, v38, v39\n\tv_cvt_pk_f16_f32 v40, v60, v61\n\t" \
  "v_cvt_pk_f16_f32 v41, v62, v63\n\tv_alignbit_b32 v44, v43, v42, 16\n\t" \
  "s_waitcnt lgkmcnt(7)\n\tv_mfma_f32_16x16x32_f16 v[64:67], v[104:107], v[72:75], 0\n\t" \
  "v_mfma_f32_16x16x32_f16 v[68:71], v[72:75], v[136:139], 0\n\ts_waitcnt lgkmcnt(6)\n\t" \
  "v_mfma_f32_16x16x32_f16 v[64:67], v[108:111], v[76:79], v[64:67]\n\tds_write_b128 v58, v[52:55]\n\t" \
  "v_mfma_f32_16x16x32_f16 v[68:71], v[76:79], v[140:143], v[68:71]\n\tds_write2_b32 v56, v40, v41 offset0:64 offset1:65\n\t" \
  "s_waitcnt lgkmcnt(7)\n\tv_mfma_f32_16x16x32_f16 v[64:67], v[112:115], v[80:83], v[64:67]\n\t" \
  "ds_write2_b32 v57, v40, v41 offset0:64 offset1:65\n\tv_mfma_f32_16x16x32_f16 v[68:71], v[80:83], v[144:147], v[68:71]\n\t" \
  "ds_write_b64 v59, v[42:43] offset:16\n\ts_waitcnt lgkmcnt(8)\n\t" \
  "v_mfma_f32_16x16x32_f16 v[64:67], v[116:119], v[84:87], v[64:67]\n\tds_write_b32 v59, v42 offset:1164\n\t" \
  "v_mfma_f32_16x16x32_f16 v[68:71], v[84:87], v[148:151], v[68:71]\n\tds_write_b32 v59, v43 offset:1168\n\t" \
  "s_waitcnt lgkmcnt(9)\n\tv_mfma_f32_16x16x32_f16 v[64:67], v[120:123], v[88:91], v[64:67]\n\t" \
  "ds_write_b16 v59, v42 offset:590\n\tv_mfma_f32_16x16x32_f16 v[68:71], v[88:91], v[152:155], v[68:71]\n\t" \
  "ds_write_b32 v59, v44 offset:592\n\ts_waitcnt lgkmcnt(10)\n\t" \
  "v_mfma_f32_16x16x32_f16 v[64:67], v[124:127], v[92:95], v[64:67]\n\tds_write_b16_d16_hi v59, v43 offset:596\n\t" \
  "v_mfma_f32_16x16x32_f16 v[68:71], v[92:95], v[156:159], v[68:71]\n\tds_write_b16 v59, v42 offset:1738\n\t" \
  "s_waitcnt lgkmcnt(11)\n\tv_mfma_f32_16x16x32_f16 v[64:67], v[128:131], v[96:99], v[64:67]\n\t" \
  "ds_write_b32 v59, v44 offset:1740\n\tv_mfma_f32_16x16x32_f16 v[68:71], v[96:99], v[160:163], v[68:71]\n\t" \
  "ds_write_b16_d16_hi v59, v43 offset:1744\n\ts_waitcnt lgkmcnt(12)\n\t" \
  "v_mfma_f32_16x16x32_f16 v[68:71], v[100:103], v[164:167], v[68:71]\n\tv_mfma_f32_16x16x32_f16 v[64:67], v[132:135], v[100:103], v[64:67]\n\t" \
  "s_nop 4\n\tv_cvt_pk_f16_f32 v48, v68, v69\n\t" \
  "v_cvt_pk_f16_f32 v49, v70, v71\n\ts_nop 1\n\t" \
  "v_cvt_pk_f16_f32 v46, v64, v65\n\tv_cvt_pk_f16_f32 v47, v66, v67\n\t" \
  "v_alignbit_b32 v50, v49, v48, 16\n\tds_write2_b32 v56, v46, v47 offset0:0 offset1:1\n\t" \
  "ds_write2_b32 v57, v46, v47 offset0:0 offset1:1\n\tds_write_b64 v59, v[48:49] offset:0\n\t" \
  "ds_write_b32 v59, v48 offset:1148\n\tds_write_b32 v59, v49 offset:1152\n\t" \
  "ds_write_b16 v59, v48 offset:574\n\tds_write_b32 v59, v50 offset:576\n\t" \
  "ds_write_b16_d16_hi v59, v49 offset:580\n\tds_write_b16 v59, v48 offset:1722\n\t" \
  "ds_write_b32 v59, v50 offset:1724\n\tds_write_b16_d16_hi v59, v49 offset:1728\n\t"
#define GEN_CLOBBER "v36", "v37", "v38", "v39", "v40", "v42", "v43", "v44", "v45", "v47", "v49", "v50", "v51", "v52", "v53", "v54", "v55", "v56", "v57", "v58", "v59", "v60", "v61", "v62", "v63", "v64", "v65", "v66", "v67", "v72", "v73", "v74", "v75", "v76", "v77", "v78", "v79", "v80", "v81", "v82", "v83", "v84", "v85", "v86", "v87", "v88", "v89", "v90", "v91", "v92", "v93", "v94", "v95", "v98", "v99", "v100", "v101", "v102", "v103", "v104", "v105", "v106", "v107", "v108", "v109", "v110", "v111", "v112", "v113", "v114", "v115", "v116", "v117", "v118", "v119", "v120", "v121", "v122", "v123", "v124", "v125", "v126", "v127", "v128", "v129", "v130", "v131", "v132", "v133", "v134", "v135", "v136", "v137", "v138", "v139", "v140", "v141", "v142", "v143", "v144", "v145", "v146", "v147", "v148", "v149", "v150", "v151", "v152", "v153", "v154", "v155", "v156", "v157", "v158", "v159", "v160", "v161", "v162", "v163", "v164", "v165", "v166", "v167"
__device__ __forceinline__ f32x2 pkfma(f32x2 a, f32x2 b, f32x2 c) {
  f32x2 r;
  asm("v_pk_fma_f32 %0, %1, %2, %3" : "=v"(r) : "v"(a), "v"(b), "v"(c));
  return r;
}
template <int HI>
__device__ __forceinline__ f32x2 pkfmas(f32x2 a, f32x2 s, f32x2 c) {
  f32x2 r;
  if (HI) asm("v_pk_fma_f32 %0, %2, %1, %3 op_sel:[1,0,0] op_sel_hi:[1,1,1]" : "=v"(r) : "v"(a), "v"(s), "v"(c));
  else asm("v_pk_fma_f32 %0, %2, %1, %3 op_sel:[0,0,0] op_sel_hi:[0,1,1]" : "=v"(r) : "v"(a), "v"(s), "v"(c));
  return r;
}
__device__ __forceinline__ void ln_rows(u32x4 (&xv)[4], u32x8 wv, rsrc_t rs, u32& tag, int b, int w, int l, u32 st TLARG) {
  float s[2][3] = {}, c[2], sw = 0.f;
  u32 os = l < 24 ? (l < 16 ? O_SG : O_RG - 128) + 128 * (b - 128) + 8 * l : 0x80000000u;
  u32 aw = (l & 16) ? st + 8 * w + (l >> 5) * 4 : st + 104, ah = (l & 16) ? st + 64 + 4 * w + (l >> 5) * 2 : st + 104, ac = st + 96, ar = st + 4 * (l & 31), one = 1u;
  asm volatile("" : "+v"(os), "+v"(aw), "+v"(ah), "+v"(ac), "+v"(ar), "+v"(one));
  SB();
  asm volatile("s_waitcnt vmcnt(4) lgkmcnt(0)" : "+{s44}"(tag)::"memory");
  asm volatile("" : "+{v[116:123]}"(wv));
  tag += 1u;
  STAMP(12);
#pragma unroll
  for (int i = 0; i < 8; i++) sw = dot2b(wv[i], 0x3f803f80u, sw);
  sw += dppf<0x121, 0xf>(sw); sw += dppf<0x122, 0xf>(sw); sw += dppf<0x124, 0xf>(sw); sw += dppf<0x128, 0xf>(sw);
  sw = (rdl(sw, 0) + rdl(sw, 16)) + (rdl(sw, 32) + rdl(sw, 48));
  asm volatile("" : "+v"(sw));
  SB();
  float cv = 0.f;
#pragma unroll
  for (int r = 0; r < 2; r++) {
#pragma unroll
    for (int i = 0; i < 8; i++) {
      if (r == 0 && i == 0) { asm volatile("s_waitcnt vmcnt(3)" : "+{v[100:103]}"(xv[0])); STAMP(13); }
      if (r == 0 && i == 4) asm volatile("s_waitcnt vmcnt(2)" : "+{v[104:107]}"(xv[1]));
      if (r == 1 && i == 0) asm volatile("s_waitcnt vmcnt(1)" : "+{v[108:111]}"(xv[2]));
      if (r == 1 && i == 4) { asm volatile("s_waitcnt vmcnt(0)" : "+{v[112:115]}"(xv[3])); STAMP(14); }
      const u32 a = xv[2 * r + (i >> 2)][i & 3];
      s[r][0] = dot2b(a, 0x3f803f80u, s[r][0]); s[r][1] = dot2b(a, a, s[r][1]); s[r][2] = dot2b(a, wv[i], s[r][2]);
      if (i == 3) {
        c[r] = rdl(s[r][0] * 0.125f, 0);
        if (r) { cv = l < 32 ? c[0] : c[1]; asm volatile("" : "+v"(cv)); }
        SB();
      }
    }
    const float ml = s[r][0] * 0.0625f, dl = __builtin_fmaf(s[r][0], 0.25f, -4.f * c[r]);
    s[r][1] = __builtin_fmaf(dl, dl, __builtin_fmaf(-s[r][0], ml, s[r][1]));
    SB();
  }
#pragma unroll
  for (int k = 0; k < 3; k++) swap32(s[0][k ? 3 - k : 0], s[1][k ? 3 - k : 0]);
  float q[3];
#pragma unroll
  for (int k = 0; k < 3; k++) q[k] = s[0][k] + s[1][k];
#define RED3(m) "v_add_f32_dpp %0, %0, %0 " m " bank_mask:0xf\n\tv_add_f32_dpp %1, %1, %1 " m " bank_mask:0xf\n\tv_add_f32_dpp %2, %2, %2 " m " bank_mask:0xf\n\t"
  asm volatile("s_nop 1\n\t" RED3("row_ror:1 row_mask:0xf") RED3("row_ror:2 row_mask:0xf") RED3("row_ror:4 row_mask:0xf") RED3("row_ror:8 row_mask:0xf")
               RED3("row_bcast:15 row_mask:0xa") : "+v"(q[0]), "+v"(q[1]), "+v"(q[2]));
  const float mu = q[0] * (1.f / D), dv = mu - cv;
  const float var = fmaxf(__builtin_fmaf(-dv, dv, q[1] * (1.f / D)), 0.f);
  const float m = rb(mu);
  float sd = __builtin_amdgcn_sqrtf(rb(rb(var) + 1e-5f)), num = __builtin_fmaf(-m, sw, q[2]);
  asm volatile("s_nop 0\n\tv_cvt_pk_bf16_f32 %0, 0, %0" : "+v"(sd), "+v"(num));
  u32 rv = __float_as_uint(rb(num * __builtin_amdgcn_rcpf(sd)));
  rv ^= (u32)((int)rv >> 15) | 0x80000000u;
  u32 old, val;
  asm volatile("ds_write_b32 %2, %3\n\tds_write_b16_d16_hi %4, %5\n\ts_mov_b64 exec, 1\n\tds_add_rtn_u32 %0, %6, %7\n\ts_mov_b64 exec, -1\n\t"
               "ds_read_b32 %1, %8\n\ts_waitcnt lgkmcnt(0)"
               : "=&v"(old), "=&v"(val) : "v"(aw), "v"(__builtin_amdgcn_perm(__float_as_uint(sd), __float_as_uint(m), 0x07060302u)), "v"(ah), "v"(rv),
                 "v"(ac), "v"(one), "v"(ar) : "memory");
  if (__builtin_amdgcn_readfirstlane((int)old) == 7) st8(rs, os, u32x2{val, tag});
  SB();
}

constexpr int NOPS = 109, NSLOT = 168;
struct Ops {
  signed char k[NOPS], a[NOPS], b[NOPS];
  short first[NOPS + 1];
  unsigned char op[NSLOT];
  short ld[NOPS][2];
  short end[8];
};
constexpr int nmf(int k, int T, int p) { return k == 0 ? (p == 0 && T > 0) + (T < 8) + (p == 0 && T > 0 && T < 5) : k == 1 ? 1 + (T < 6) : k == 2 ? (T < 4 ? 3 : 6 - T) : 4 - T; }
constexpr Ops mkops() {
  Ops O = {};
  int n = 0, last[8] = {};
  O.k[n] = 0; O.a[n] = 0; O.b[n] = 0; n++;
  for (int I = 0; I < 8; I++) {
    int fk[7] = {}, fa[7] = {}, fb[7] = {}, m = 0;
    if (I >= 4) { fk[m] = 3; fa[m] = I - 4; fb[m] = 0; m++; }
    if (I >= 2) for (int p = 0; p < 2; p++) { fk[m] = 2; fa[m] = I - 2; fb[m] = p; m++; }
    if (I >= 1) for (int p = 0; p < 4; p++) { fk[m] = 1; fa[m] = I - 1; fb[m] = p; m++; }
    int f = 0;
    for (int c = 1; c <= 8; c++) {
      O.k[n] = 0; O.a[n] = c == 8 ? I + 1 : I; O.b[n] = c & 7; n++;
      if (I >= 2 && f < m) { O.k[n] = fk[f]; O.a[n] = fa[f]; O.b[n] = fb[f]; n++; f++; }
    }
    for (; f < m; f++) { O.k[n] = fk[f]; O.a[n] = fa[f]; O.b[n] = fb[f]; n++; }
    last[I] = n - 1;
  }
  int q = 0;
  for (int i = 0; i < NOPS; i++) {
    O.first[i] = q;
    for (int u = nmf(O.k[i], O.a[i], O.b[i]); u > 0; u--) O.op[q++] = i;
  }
  O.first[NOPS] = q;
  for (int I = 0; I < 8; I++) O.end[I] = O.first[last[I]];
  for (int i = 0; i < NOPS; i++) {
    O.ld[i][1] = O.first[i] - LA;
    O.ld[i][0] = O.ld[i][1] - (O.k[i] && O.first[i] - O.first[i - 1] > 1);
  }
  return O;
}
constexpr Ops OPS = mkops();
static_assert(OPS.first[NOPS] == NSLOT, "schedule");
constexpr int WL[4] = {-1, 17, WL2, WL3}, WN[4] = {38, 38, 22, 13};
constexpr int wslot(int pp, int k) { return pp < 2 ? (pp ? 22 : 2) + k / 2 : WL[pp] + 4 + k; }
constexpr int opat(int k, int T, int p) { for (int i = 0; i < NOPS; i++) if (OPS.k[i] == k && OPS.a[i] == T && OPS.b[i] == p) return i; return -1; }
static_assert(wslot(0, 12) < OPS.ld[opat(1, 0, 0)][0] && wslot(0, 21) < OPS.ld[opat(2, 0, 0)][0] && wslot(1, 12) < OPS.ld[opat(1, 1, 0)][0] &&
              wslot(1, 21) < OPS.ld[opat(2, 1, 0)][0] && wslot(1, 37) < OPS.ld[opat(3, 0, 0)][0] && wslot(2, 12) < OPS.ld[opat(1, 3, 0)][0] &&
              wslot(2, 21) < OPS.ld[opat(2, 3, 0)][0] && wslot(3, 12) < OPS.ld[opat(1, 5, 0)][0] && wslot(0, 29) < WL[1] && wslot(1, 37) < WL[2] &&
              wslot(2, 21) < WL[3], "window order");
constexpr int XC[3] = {OPS.ld[opat(0, 2, 0)][1], WL2 < OPS.ld[opat(0, 4, 0)][1] ? WL2 : OPS.ld[opat(0, 4, 0)][1], WL3};
static_assert(XC[0] <= WL[1] && WL3 <= OPS.ld[opat(0, 6, 0)][1], "chunk order");
template <int V> struct IC { static constexpr int value = V; };
template <int A, int B, class F> __device__ __forceinline__ void sfor(F&& f) {
  if constexpr (A < B) { f(IC<A>{}); sfor<A + 1, B>(f); }
}
#define GL __attribute__((address_space(1)))
typedef const GL u16* gp;
__device__ __forceinline__ void dma(gp base, u32 v, u32 dst) {
  asm volatile("s_mov_b32 m0, %0\n\ts_nop 0\n\tglobal_load_lds_dwordx4 %1, %2 nt" ::"s"(dst), "v"(v), "s"(base) : "memory");
}

__global__ __launch_bounds__(768, 3) void fused(const u16*, const u16*, const u16*, const u16*, const u16*, const u16*, const u16*, const u16*,
    const u16*, const u16*, const u16*, u16*, char*) {
  __shared__ __attribute__((aligned(256))) char smem[SMEM];
  const u32 lds0 = (u32)(size_t)(LDS char*)smem;
  int w0;
#define GLD(r0, r1, r2, r3, o_) "global_load_dwordx4 v[" #r0 ":" #r1 "], " #o_ ", s[16:17]\n\tglobal_load_dwordx4 v[" #r2 ":" #r3 "], " #o_ ", s[16:17] offset:2048\n\t"
  asm volatile(
      "s_load_dwordx16 s[16:31], s[0:1], 0\n\ts_load_dwordx8 s[32:39], s[0:1], 64\n\ts_load_dwordx2 s[40:41], s[0:1], 96\n\t"
      "s_getpc_b64 s[4:5]\n2:\n\ts_lshr_b32 s7, s2, 3\n\ts_and_b32 s10, s4, 0xffffff00\n\ts_mov_b32 s11, s5\n\ts_mul_i32 s8, s7, 0x600\n\t"
      "v_lshl_add_u32 v3, v0, 1, s8\n\ts_cmpk_lt_u32 s2, %[pn]\n\ts_cbranch_scc0 1f\n\t"
      "global_load_ushort v163, v3, s[10:11] offset:%[pf]\n"
      "1:\n\ts_cmpk_lt_u32 s2, 0x80\n\tv_readfirstlane_b32 s3, v0\n\tv_and_b32 v1, 63, v0\n\tv_mov_b64 v[4:5], 0\n\tv_mov_b64 v[6:7], 0\n\tv_lshlrev_b32 v2, 4, v1\n\tv_lshlrev_b32 v8, 2, v0\n\t"
      "s_mov_b32 s42, %[we]\n\ts_mov_b32 s43, 0x20000\n\ts_cbranch_scc0 3f\n\t"
      "s_lshr_b32 s3, s3, 6\n\ts_mul_i32 s9, s3, %[xr]\n\ts_min_u32 s9, s9, %[zm]\n\tv_add_u32 v3, s9, v2\n\tv_add_u32 v3, %[zo], v3\n\t"
      "s_and_b32 s7, s7, 7\n\ts_and_b32 s6, s2, 7\n\ts_and_b32 s8, s2, 64\n\ts_lshl3_add_u32 s8, s6, s8\n\ts_add_u32 s45, s8, s7\n\t"
      "ds_write_b128 v3, v[4:7]\n\ts_cmp_lt_u32 s3, 8\n\ts_cbranch_scc1 5f\n\t"
      "s_lshl_b32 s8, s45, 4\n\ts_lshl_b32 s9, s3, 18\n\ts_add_u32 s9, s9, s8\n\ts_bitset0_b32 s9, 21\n\tv_lshl_add_u32 v148, v1, 12, s9\n\t"
      "s_lshl3_add_u32 s9, s3, s7\n\ts_lshl_b32 s9, s9, 17\n\ts_add_u32 s9, s9, s8\n\ts_bitset0_b32 s9, 23\n\tv_lshl_add_u32 v9, v1, 11, s9\n\t"
      "v_add_u32 v11, 0x100000, v148\n\tv_add_u32 v12, 0x200000, v148\n\tv_add_u32 v13, 0x300000, v148\n\t"
      "v_lshlrev_b32 v149, 4, v0\n\tv_add_u32 v149, %[sg], v149\n\ts_movk_i32 s9, 0x1000\n\t"
      "s_waitcnt lgkmcnt(0)\n\ts_load_dword s44, s[40:41], 0\n\ts_load_dwordx4 s[48:51], s[28:29], s8\n\t"
      "s_barrier\n\t"
#define HLD "global_load_dword v162, v14, s[14:15]\n\t"
      "global_load_dword v160, v9, s[16:17]" PRENT "\n\t"
      GLD(100, 103, 104, 107, v148)
      GLD(108, 111, 112, 115, v11)
      GLD(116, 119, 120, 123, v12)
      "buffer_load_dwordx4 v[132:135], v149, s[40:43], 0 offen sc1\n\tbuffer_load_dwordx4 v[136:139], v149, s[40:43], s9 offen sc1\n\t"
      GLD(124, 127, 128, 131, v13)
      "s_branch .Lrole_p\n"
      "3:\n\ts_lshr_b32 s3, s3, 6\n\ts_cmp_lt_u32 s3, 8\n\ts_cbranch_scc1 4f\n\ts_cmpk_ge_u32 s2, 0xf0\n\ts_cbranch_scc1 .Lrole_end\n\ts_barrier\n\ts_branch .Lrole_d\n4:\n\t"
      "v_add_u32 v3, %[fc], v2\n\ts_add_u32 s8, s3, s2\n\ts_and_b32 s45, s8, 7\n\ts_lshl3_add_u32 s8, s2, s45\n\t"
      "s_lshl_b32 s8, s8, 12\n\ts_bitset0_b32 s8, 22\n\tds_write_b128 v3, v[4:7]\n\tv_add_u32 v3, s8, v2\n\t"
      "s_waitcnt lgkmcnt(0)\n\tglobal_load_dwordx4 v[116:119], v2, s[30:31]\n\tglobal_load_dwordx4 v[120:123], v2, s[30:31] offset:1024\n\t"
      "s_load_dword s44, s[40:41], 0\n\ts_barrier\n\t"
      "global_load_dwordx4 v[100:103], v3, s[16:17]" LNNT "\n\tglobal_load_dwordx4 v[104:107], v3, s[16:17] offset:1024" LNNT "\n\t"
      "global_load_dwordx4 v[108:111], v3, s[16:17] offset:2048" LNNT "\n\tglobal_load_dwordx4 v[112:115], v3, s[16:17] offset:3072" LNNT "\n\t"
      "s_branch .Lrole_l\n"
      "5:\n\ts_add_u32 s4, s4, lane_tab@rel32@lo+(6f-2b-4)\n6:\n\ts_addc_u32 s5, s5, lane_tab@rel32@hi+(6b-2b+4)\n\t"
      "global_load_dwordx4 v[16:19], v2, s[4:5]\n\tglobal_load_dwordx4 v[20:23], v2, s[4:5] offset:1024\n\t"
      "s_lshl3_add_u32 s8, s45, s3\n\ts_lshl_b32 s9, s8, 1\n\tv_mov_b32 v9, s9\n\ts_lshl_b32 s9, s45, 11\n\tv_add_u32 v3, s9, v8\n\t"
      "s_waitcnt lgkmcnt(0)\n\tglobal_load_short_d16_hi v24, v9, s[26:27]\n\t"
      "global_load_dword v96, v3, s[18:19]" PNT "\n\tglobal_load_dword v97, v3, s[20:21]" PNT "\n\t"
      "global_load_dword v68, v3, s[22:23]" PNT "\n\tglobal_load_dword v69, v3, s[24:25]" PNT "\n\ts_barrier\n\ts_branch .Lrole_c"
      : "={s3}"(w0)
      : "{s[0:1]}"(__builtin_amdgcn_kernarg_segment_ptr()), "{s2}"(blockIdx.x), "{v0}"(threadIdx.x), [pf] "n"(PFO), [pn] "n"(8 * PFN), [we] "n"(WS_END), [xr] "n"(XREG),
        [zm] "n"(OMISC - 512 - (OX - 16)), [zo] "n"(OX - 16), [sg] "n"(O_SG - 8192), [fc] "n"(FC)
      : "memory");
#define TLREGS()
#define ROLE(name, text, ...) \
  gp x, dsk, wg, wu, wd; GL u16* out; rsrc_t rs; int b, w, t, l, g; u32 tag, st0; u32x4 dv; \
  asm volatile(".Lrole_" #name ":" : "={v0}"(t), "={v1}"(l) __VA_ARGS__ :: "memory"); \
  asm volatile(text : "={s2}"(b), "={s3}"(w), "={s[16:17]}"(x), "={s[28:29]}"(dsk), "={s[32:33]}"(wg), "={s[34:35]}"(wu), "={s[36:37]}"(wd), \
               "={s[38:39]}"(out), "={s[40:43]}"(rs), "={s44}"(tag), "={s45}"(g), "={s46}"(st0), "={s[48:51]}"(dv) :: "memory"); \
  SB(); \
  __builtin_assume((u32)l < 64u); __builtin_assume((u32)t < 768u); __builtin_assume((u32)w < 12u); __builtin_assume((u32)b < 256u); __builtin_assume((u32)g < 128u); \
  TLREGS(); \
  const int xk = b & 7, j = (b >> 3) & 15, n16 = l & 15, kg = l >> 4
#define TAGWAIT() do { asm volatile("s_waitcnt lgkmcnt(0)" : "+{s44}"(tag)::"memory"); tag += 1u; } while (0)
#define BAR() do { SB(); asm volatile("s_barrier" ::: "memory"); SB(); } while (0)
#define FLAG(o, v) do { asm volatile("" ::: "memory"); SB(); *(volatile LDS u32*)(LDS char*)(smem + (o)) = (v); SB(); } while (0)
#define ARRIVE(o) do { SB(); if (l == 0) asm volatile("ds_add_u32 %0, %1" ::"v"(lds0 + (o)), "v"(1u) : "memory"); SB(); } while (0)
#define CWAIT(o, n, nap) do { _Pragma("unroll 1") for (int spin = 0; spin < SPIN; spin++) { \
      if (__builtin_amdgcn_readfirstlane((int)*(const volatile LDS u32*)(LDS char*)(smem + (o))) >= (int)(n)) break; \
      asm volatile(nap ::: "memory"); } SB(); asm volatile("" ::: "memory"); } while (0)
#define AWAIT(o, n, nap) do { _Pragma("unroll 1") for (int spin = 0; spin < SPIN; spin++) { \
      if (__all(*(const volatile LDS u32*)(LDS char*)(smem + (o)) >= (u32)(n))) break; \
      asm volatile(nap ::: "memory"); } SB(); asm volatile("" ::: "memory"); } while (0)
#define POLL8(res_, off_) do { u32 n_ = SPIN; \
    asm volatile("1:\n\tbuffer_load_dwordx2 v[4:5], %2, %3, 0 offen sc1\n\ts_sub_u32 %1, %1, 1\n\ts_waitcnt vmcnt(0)\n\t" \
                 "v_cmp_ne_u32 vcc, %4, v5\n\ts_cbranch_scc1 2f\n\ts_cbranch_vccnz 1b\n2:" \
                 : "=&{v[4:5]}"(res_), "+s"(n_) : "v"(off_), "s"(rs), "s"(tag) : "vcc", "scc", "memory"); } while (0)
#define LANE(v_) int v_; asm volatile("v_mbcnt_lo_u32_b32 %0, -1, 0\n\tv_mbcnt_hi_u32_b32 %0, -1, %0" : "=v"(v_))
#define IXO(l_) (O_IDX + (2 * xk + (j & 1)) * 128 + ((l_) & 7) * 8)

#define SLOW(s_) (16 * ((s_) >> 2) + 4 + ((s_) & 3))
#define IDS() const int id = 14 * xk + j, gid = id, ns = 0, s0 = 0
  if (blockIdx.x >= 128) {
    if (w0 >= 8) {
      ROLE(d, "s_waitcnt lgkmcnt(0)");
      IDS();
      const int dw = w - 8;
      const u32 sw = (((n16 >> 1) ^ (kg | ((dw >> 1) << 2))) << 5) + ((l & 1) << 4);
      gp base = ((gid & 2) ? wu : wg) + (size_t)(512 * (gid & 1) + 4 * dw) * F + (gid >> 2) * 128;
      gp bd = wd + (size_t)(256 * (id >> 3) + 4 * dw) * D + (id & 7) * 128;
      u32 v = kg * (2 * F) + sw, vd = kg * (2 * D) + sw, fa = lds0 + FC, inc = 1u, one = 1u;
      asm volatile("" : "+v"(v), "+v"(vd), "+v"(fa), "+v"(inc), "+v"(one));
      asm volatile("s_mov_b32 m0, %0" ::"s"(lds0 + 1024 * dw));
#define RUN(c, q, v_, b_, st_, o, i_) do { u32 n_ = (c) - 1; \
      asm volatile("1:\n\ts_waitcnt vmcnt(%3)\n\tglobal_load_lds_dwordx4 %0, %2 nt\n\ts_add_u32 m0, m0, 0x1000\n\tv_add_u32 %0, %4, %0\n\ts_sub_u32 %1, %1, 1\n\t" \
                   "s_cbranch_scc0 1b\n\ts_waitcnt vmcnt(%3)\n\ts_mov_b64 exec, 1\n\tds_add_u32 %5, %6 offset:%7\n\ts_mov_b64 exec, -1" \
                   : "+v"(v_), "+s"(n_) : "s"(b_), "n"((q) - 1), "n"(st_), "v"(fa), "v"(i_), "n"(o) : "scc", "memory"); } while (0)
      STAMP(8);
      RUN(15 + QD, QD, v, base, 32 * F, 0, one);
      RUN(8, QD, v, base, 32 * F, 16, one);
      RUN(9 - QD, QD + 1, v, base, 32 * F, 28, one);
      STAMP(1);
      {
        u32 n_ = SPIN, c_;
#define LOOK(c) "s_waitcnt vmcnt(" #c ")\n\tds_read_b32 %[c_], %[a] offset:4\n\ts_waitcnt lgkmcnt(0)\n\tv_cmp_lt_u32 vcc, %[k], %[c_]\n\ts_cbranch_vccnz 2f\n\t"
        asm volatile(LOOK(5) LOOK(3) LOOK(1) "s_waitcnt vmcnt(0)\n\ts_mov_b64 exec, 1\n\tds_add_u32 %[a], %[i] offset:20\n\ts_mov_b64 exec, -1\n\t"
                     "v_mov_b32 %[i], 0\n1:\n\t" LOOK(0) "s_sub_u32 %[n], %[n], 1\n\ts_cbranch_scc0 1b\n2:\n\ts_mov_b32 m0, %[m]"
                     : [c_] "=&v"(c_), [i] "+v"(inc), [n] "+s"(n_) : [a] "v"(fa), [k] "n"(8), [m] "s"(lds0 + 1024 * dw) : "vcc", "scc", "memory");
      }
      STAMP(3);
      RUN(QE - 1, QE, vd, bd, 32 * D, 20, inc);
      STAMP(2);
      RUN(17 - QE, QE, vd, bd, 32 * D, 28, one);
      STAMP(6);
      asm volatile("s_mov_b64 exec, 1\n\ts_waitcnt vmcnt(%2)\n\tds_add_u32 %0, %1 offset:24\n\ts_waitcnt vmcnt(0)\n\tds_add_u32 %0, %1 offset:8\n\ts_mov_b64 exec, -1"
                   ::"v"(fa), "v"(one), "n"(HQ) : "memory");
      STAMP(4);
      FLUSH();
      __builtin_amdgcn_endpgm();
    }
    u32x4 xv[4];
    u32x8 wv;
    ROLE(l, "", , "={v[100:103]}"(xv[0]), "={v[104:107]}"(xv[1]), "={v[108:111]}"(xv[2]), "={v[112:115]}"(xv[3]), "={v[116:123]}"(wv));
    STAMP(11);
    ln_rows(xv, wv, rs, tag, b, g, l, lds0 + FC + 128 TLVAL);
    STAMP(10);
    if (j >= 14) {
      const int sb = 2 * xk + (j - 14);
      u32* gm = (u32*)(smem + FC + 256);
      u32* ncand = gm + 16;
      u32* cand = gm + 32;
      {
        struct { u32 x, z; } g;
        u32x8 ring0;
        u32x4 ring1;
        {
          u32 n_ = SPIN;
          unsigned long long tmp;
          asm volatile("buffer_load_dwordx4 v[40:43], %[o], %[r], 0 offen sc1\n\ts_sleep 3\n\tbuffer_load_dwordx4 v[44:47], %[o], %[r], 0 offen sc1\n\ts_sleep 3\n\t"
                     "buffer_load_dwordx4 v[48:51], %[o], %[r], 0 offen sc1\n1:\n\t"
                     "s_waitcnt vmcnt(2)\n\tv_cmp_ne_u32 vcc, %[t], v41\n\tv_cmp_ne_u32 %4, %[t], v43\n\tv_mov_b32 %0, v40\n\tv_mov_b32 %1, v42\n\t"
                     "s_or_b64 vcc, vcc, %4\n\ts_cbranch_scc0 2f\n\tbuffer_load_dwordx4 v[40:43], %[o], %[r], 0 offen sc1\n\t"
                     "s_waitcnt vmcnt(2)\n\tv_cmp_ne_u32 vcc, %[t], v45\n\tv_cmp_ne_u32 %4, %[t], v47\n\tv_mov_b32 %0, v44\n\tv_mov_b32 %1, v46\n\t"
                     "s_or_b64 vcc, vcc, %4\n\ts_cbranch_scc0 2f\n\tbuffer_load_dwordx4 v[44:47], %[o], %[r], 0 offen sc1\n\t"
                     "s_waitcnt vmcnt(2)\n\tv_cmp_ne_u32 vcc, %[t], v49\n\tv_cmp_ne_u32 %4, %[t], v51\n\tv_mov_b32 %0, v48\n\tv_mov_b32 %1, v50\n\t"
                     "s_or_b64 vcc, vcc, %4\n\ts_cbranch_scc0 2f\n\tbuffer_load_dwordx4 v[48:51], %[o], %[r], 0 offen sc1\n\t"
                     "s_sub_u32 %[n], %[n], 1\n\ts_cbranch_scc0 1b\n2:"
                     : "=&v"(g.x), "=&v"(g.z), "=&{v[40:47]}"(ring0), "=&{v[48:51]}"(ring1), "=&s"(tmp), [n] "+s"(n_)
                     : [o] "v"(O_RG + 128 * (t >> 2) + 16 * (t & 3)), [r] "s"(rs), [t] "s"(tag) : "vcc", "scc", "memory");
        }
        STAMP(2);
        const u32 ib = 2047 - 4 * t;
        const u32 key[4] = {(g.x << 16) | ib, (g.x & 0xffff0000u) | (ib - 1), (g.z << 16) | (ib - 2), (g.z & 0xffff0000u) | (ib - 3)};
        u32 m = max(max(key[0], key[1]), max(key[2], key[3]));
        const u32 m4 = m;
        m = max(m, dppu<0x121, 0xf>(m)); m = max(m, dppu<0x122, 0xf>(m));
        m = max(m, dppu<0x124, 0xf>(m)); m = max(m, dppu<0x128, 0xf>(m));
        {
          auto sw = __builtin_amdgcn_permlane16_swap(m, m, false, false);
          m = max(sw[0], sw[1]);
        }
        gm[t >> 5] = m;
        LBAR();
        STAMP(3);
        auto rank16 = [&](const u32* a, u32& mine) __attribute__((always_inline)) {
          mine = a[n16];
          const u32x4 q = *(const u32x4*)(a + 4 * kg);
          u32 cnt = (q.x > mine) + (q.y > mine) + (q.z > mine) + (q.w > mine);
          auto s1 = __builtin_amdgcn_permlane32_swap(cnt, cnt, false, false);
          cnt = s1[0] + s1[1];
          auto s2 = __builtin_amdgcn_permlane16_swap(cnt, cnt, false, false);
          return s2[0] + s2[1];
        };
        u32 mine;
        u32 cnt = rank16(gm, mine);
        const u32 thr = (u32)__builtin_amdgcn_readlane((int)mine, __builtin_ctzll(__ballot(cnt == 7)));
        if (m4 >= thr) {
          const u32 c = (key[0] >= thr) + (key[1] >= thr) + (key[2] >= thr) + (key[3] >= thr);
          u32 slot;
          asm volatile("ds_add_rtn_u32 %0, %1, %2\n\ts_waitcnt lgkmcnt(0)" : "=v"(slot) : "v"((u32)(size_t)(LDS u32*)ncand), "v"(c) : "memory");
#pragma unroll
          for (int i = 0; i < 4; i++) {
            const bool on = key[i] >= thr;
            gm[on ? 32 + slot : 17] = key[i];
            slot += on;
          }
        }
        LBAR();
        STAMP(4);
        const int n = __builtin_amdgcn_readfirstlane((int)*ncand);
        cnt = rank16(cand, mine);
#define IDXST(on) st8(rs, (on) ? O_IDX + sb * 128 + cnt * 8 : 0x80000000u, \
                      u32x2{(2047u - (mine & 0x7ffu)) | ((mine ^ ((u32)((int)~mine >> 15) | 0x80000000u)) & 0xffff0000u), tag})
        if (__builtin_expect(n <= 16, 1)) {
          if (w == 0) IDXST(l < n && cnt < 8);
          const u32 row = 2047u - ((u32)__builtin_amdgcn_readlane((int)mine, __builtin_ctzll(__ballot(cnt == (u32)w))) & 0x7ffu);
          const u32 pf = *(const GL u32*)(x + (size_t)row * D + 64 * n16);
          asm volatile("" ::"v"(pf));
        } else if (w == 0) {
#pragma unroll 1
          for (int i0 = 0; i0 < n; i0 += 64) {
            mine = cand[i0 + l]; cnt = 0;
#pragma clang loop unroll(disable) vectorize(disable)
            for (int i = 0; i < n; i++) cnt += cand[i] > mine;
            IDXST(i0 + l < n && cnt < 8);
          }
        }
        STAMP(5);
        asm volatile("s_waitcnt vmcnt(0)" ::"{v[40:47]}"(ring0), "{v[48:51]}"(ring1));
      }
      if (b == 255 && w == 7) {
#pragma unroll 1
        for (int spin = 0; spin < SPIN; spin++) {
          const u32x4 a = ld16(rs, O_ARR + 16 * l);
          if (__all(a.y == tag && a.w == tag)) break;
          asm volatile("s_sleep 16" ::: "memory");
        }
        if (l == 0) __builtin_amdgcn_raw_buffer_store_b32(tag, rs, 0, 0, 16);
      }
      FLUSH();
      __builtin_amdgcn_endpgm();
    }
    IDS();
    const char* wp = smem + (8 * kg + (n16 >> 2)) * 256 + ((w ^ ((n16 >> 2) | ((kg & 1) << 2))) << 5) + 8 * (l & 3);
    const char* wq = wp + 65536;
    {
      const bf16x8 one = asb(u32x4{0x3f803f80u, 0x3f803f80u, 0x3f803f80u, 0x3f803f80u});
      bf16x8 wf[16];
      f32x4 cs = {};
      CWAIT(FC, 4, "s_sleep 2");
      STAMP(1);
#pragma unroll
      for (int k = 0; k < 8; k++) wf[k] = mkb(trread(wp + 8192 * k), trread(wp + 8192 * k + 1024));
      ARRIVE(FC + 4);
#pragma unroll
      for (int k = 0; k < 8; k++) cs = MFMAB(wf[k], one, cs);
      SB();
      u32 so = 0u;
      if (w == 0) {
        u32 ix, n_ = SPIN;
#define LOOKI(a, b_) "s_waitcnt vmcnt(3)\n\tv_cmp_ne_u32 vcc, %[t], v" #b_ "\n\tv_mov_b32 %[x], v" #a "\n\ts_cbranch_vccz 2f\n\tbuffer_load_dwordx2 v[" #a ":" #b_ "], %[o], %[r], 0 offen sc1\n\t"
        asm volatile("buffer_load_dwordx2 v[152:153], %[o], %[r], 0 offen sc1\n\ts_sleep " STR(RSL) "\n\tbuffer_load_dwordx2 v[154:155], %[o], %[r], 0 offen sc1\n\ts_sleep " STR(RSL) "\n\t"
                     "buffer_load_dwordx2 v[156:157], %[o], %[r], 0 offen sc1\n\ts_sleep " STR(RSL) "\n\tbuffer_load_dwordx2 v[158:159], %[o], %[r], 0 offen sc1\n1:\n\t"
                     LOOKI(152, 153) LOOKI(154, 155) LOOKI(156, 157) LOOKI(158, 159) "s_sub_u32 %[n], %[n], 1\n\ts_cbranch_scc0 1b\n2:"
                     : [x] "=&v"(ix), [n] "+s"(n_) : [o] "v"(IXO(l)), [r] "s"(rs), [t] "s"(tag) : "vcc", "scc", "memory");
        STAMP(15);
        const rsrc_t rx = __builtin_amdgcn_make_buffer_rsrc((void*)x, 0, 2 * L * D, 0x00020000);
        const u32 vr = (ix & 2047u) << 11, vo = 1024 * (gid & 1) + 16 * l;
        u32 ro[8];
        so = O_SG + (vr >> 8);
#pragma unroll
        for (int i = 0; i < 8; i++) ro[i] = (u32)__builtin_amdgcn_readlane((int)vr, i);
        asm volatile("" : "+s"(ro[0]), "+s"(ro[1]), "+s"(ro[2]), "+s"(ro[3]), "+s"(ro[4]), "+s"(ro[5]), "+s"(ro[6]), "+s"(ro[7]));
#pragma unroll
        for (int i = 0; i < 8; i++)
          asm volatile("s_mov_b32 m0, %0\n\ts_nop 0\n\tbuffer_load_dwordx4 %1, %2, %3 offen lds" ::"s"(lds0 + FA + AP * i), "v"(vo), "s"(rx), "s"(ro[i]) : "memory");
        asm volatile("buffer_load_dwordx2 v[160:161], %0, %1, 0 offen sc1" ::"v"(so), "s"(rs) : "memory");
        ARRIVE(FC + 4);
      }
      auto arrived0 = [&]() __attribute__((always_inline)) {
          u32 n_ = SPIN;
          asm volatile("1:\n\ts_waitcnt vmcnt(0)\n\tv_cmp_ne_u32 vcc, %3, v161\n\ts_sub_u32 %0, %0, 1\n\ts_cbranch_vccz 2f\n\ts_cbranch_scc1 2f\n\t"
                       "buffer_load_dwordx2 v[160:161], %1, %2, 0 offen sc1\n\ts_branch 1b\n2:\n\tds_write_b32 %4, v160\n\tds_write_b32 %5, %6"
                       : "+s"(n_) : "v"(so), "s"(rs), "s"(tag), "v"(lds0 + FC + 32 + 4 * (l & 7)), "v"(lds0 + FC + 12), "v"(4u) : "vcc", "scc", "memory");
      };
      auto arrived = [&]() __attribute__((always_inline)) {
        if (!G0E && w == 0) arrived0(); else CWAIT(FC + 12, 4, "s_nop 0");
      };
      if (G0E && w == 0) arrived0();
      CWAIT(FC + 16, 4, "s_sleep 1");
      STAMP(2);
#pragma unroll
      for (int k = 0; k < 4; k++) wf[8 + k] = mkb(trread(wq + 8192 * k), trread(wq + 8192 * k + 1024));
#pragma unroll
      for (int k = 0; k < 4; k++) cs = MFMAB(wf[8 + k], one, cs);
      SB();
      u32x4 xa[8];
      u32 st;
      f32x4 a1 = {}, a2 = {};
      const char* ap = smem + FA + (l & 7) * AP + 512 * (n16 >> 3) + 16 * kg;
      auto rows = [&]() __attribute__((always_inline)) {
        STAMP(3);
#pragma unroll
        for (int i = 0; i < 8; i++) xa[i] = *(const u32x4*)(ap + 64 * i);
        st = *(const u32*)(smem + FC + 32 + 4 * (l & 7));
        SB();
#pragma unroll
        for (int i = 0; i < 8; i++) { a1 = MFMAB(wf[i], asb(xa[i]), a1); if (i < 4) a2 = MFMAB(wf[8 + i], asb(xa[i]), a2); }
        SB();
      };
      auto last = [&]() __attribute__((always_inline)) {
        STAMP(4);
#pragma unroll
        for (int k = 4; k < 8; k++) wf[8 + k] = mkb(trread(wq + 8192 * k), trread(wq + 8192 * k + 1024));
#pragma unroll
        for (int k = 4; k < 8; k++) cs = MFMAB(wf[8 + k], one, cs);
        SB();
      };
      u32 got = 0u;
#pragma unroll 1
      for (int spin = 0; spin < SPIN; spin++) {
        got = (u32)__ballot(*(const volatile LDS u32*)(LDS char*)(smem + FC + 12 + 8 * (l & 1)) >= 4u);
        if (got || (!G0E && w == 0)) break;
        NAP();
      }
      asm volatile("" ::: "memory");
      SB();
      if (got & 2u) { last(); arrived(); rows(); }
      else { arrived(); rows(); CWAIT(FC + 20, 4, "s_nop 0"); last(); }
#pragma unroll
      for (int k = 4; k < 8; k++) a2 = MFMAB(wf[8 + k], asb(xa[k]), a2);
      const float rsd = __builtin_amdgcn_rcpf(hi2f(st)), nm = -lo2f(st);
      float o[4];
#pragma unroll
      for (int i = 0; i < 4; i++) o[i] = __builtin_fmaf(nm, cs[i], a1[i] + dppf<0x128, 0xf>(a2[i])) * rsd;
      if (n16 < 8) st16(rs, O_HG + gid * 4096 + 512 * w + 128 * kg + 16 * n16, u32x4{pack2(o[0], o[1]), tag, pack2(o[2], o[3]), tag});
      SB();
      STAMP(5);
    }
    {
      const u32 off = O_HG + (2 * (id >> 3) + (w >> 2)) * 16384 + (w & 3) * 1024 + 16 * l;
      CWAIT(FC + 24, 4, "s_sleep 1");
      u32x4 ha[4];
#pragma unroll
      for (int i = 0; i < 4; i++) ha[i] = ld16(rs, off + 4096 * i);
      SB();
      CWAIT(FC + 8, 4, "s_sleep 1");
      STAMP(6);
      bf16x8 df[8];
#pragma unroll
      for (int k = 0; k < 8; k++) df[k] = mkb(trread(wp + 8192 * k), trread(wp + 8192 * k + 1024));
      SB();
#pragma unroll 1
      for (int spin = 0; spin < SPIN; spin++) {
        bool bad[4];
#pragma unroll
        for (int i = 0; i < 4; i++) bad[i] = ha[i].y != tag || ha[i].w != tag;
        if (!__any(bad[0] || bad[1] || bad[2] || bad[3])) break;
        asm volatile("s_sleep " STR(HNAP) ::: "memory");
#pragma unroll
        for (int i = 0; i < 4; i++) if (bad[i]) ha[i] = ld16(rs, off + 4096 * i);
      }
      STAMP(7);
      u32x2 hv;
#pragma unroll
      for (int i = 0; i < 2; i++) {
        const u32 g0 = ha[0][2 * i], g1 = ha[1][2 * i], u0 = ha[2][2 * i], u1 = ha[3][2 * i];
        const u32 gg = pack2(lo2f(g0) + lo2f(g1), hi2f(g0) + hi2f(g1)), uu = pack2(lo2f(u0) + lo2f(u1), hi2f(u0) + hi2f(u1));
        const u32 sl = pack2(lo2f(gg) * sigm(lo2f(gg)), hi2f(gg) * sigm(hi2f(gg)));
        hv[i] = pack2(lo2f(sl) * lo2f(uu), hi2f(sl) * hi2f(uu));
      }
      *(u32x2*)(smem + FH + (l & 7) * HP + 64 * w + (l & 56)) = hv;
      LBAR();
      STAMP(8);
      const char* hp = smem + FH + (l & 7) * HP + 256 * (n16 >> 3) + 16 * kg;
      f32x4 a1 = {}, a2 = {};
#pragma unroll
      for (int i = 0; i < 4; i++) {
        const bf16x8 h = asb(*(const u32x4*)(hp + 64 * i));
        a1 = MFMAB(df[i], h, a1); a2 = MFMAB(df[4 + i], h, a2);
      }
      float o[4];
#pragma unroll
      for (int i = 0; i < 4; i++) o[i] = a1[i] + dppf<0x128, 0xf>(a2[i]);
      if (n16 < 8) st16(rs, O_PG + id * 4096 + 512 * w + 128 * kg + 16 * n16, u32x4{pack2(o[0], o[1]), tag, pack2(o[2], o[3]), tag});
      STAMP(9);
    }
    FLUSH();
    __builtin_amdgcn_endpgm();
  }

  if (w0 >= 8) {
    u32x8 xa0, xa1, xa2, xa3, sa0, sa1;
    u32x4 junk;
    u32 op, so;
    ROLE(p, "", , "={v[100:107]}"(xa0), "={v[108:115]}"(xa1), "={v[116:123]}"(xa2), "={v[124:131]}"(xa3), "={v[132:139]}"(sa0),
         "={v148}"(op), "={v149}"(so), "={v[160:163]}"(junk));
    const int p = w - 8;
    f32x2 sc[4];
    asm volatile("s_waitcnt lgkmcnt(0)" : "+{s44}"(tag), "+{s[48:51]}"(dv)::"memory");
    tag += 1u;
#pragma unroll
    for (int i = 0; i < 4; i++) { sc[i] = f32x2{1.f + lo2f(dv[i]), 1.f + hi2f(dv[i])}; asm volatile("" : "+v"(sc[i])); }
    STAMP(9);
    __builtin_amdgcn_s_setprio(3);
    f32x2 xq[4][8];
    {
      asm volatile("s_mov_b32 m0, %0\n\ts_nop 0" ::"s"(lds0 + OX + 2 * XO + 256 * p));
#pragma unroll
      for (int i = 0; i < 4; i++) {
#define SWAIT(c, r0, r1, r3, r4, r5, r7, ...) do { u32 n_ = SPIN, d_; \
          asm volatile("s_waitcnt vmcnt(" #c ")\n1:\n\tv_xor_b32 %[d], %[t], v" #r1 "\n\tv_bitop3_b32 %[d], %[d], %[t], v" #r3 " bitop3:0xf6\n\t" \
                       "v_bitop3_b32 %[d], %[d], %[t], v" #r5 " bitop3:0xf6\n\tv_bitop3_b32 %[d], %[d], %[t], v" #r7 " bitop3:0xf6\n\t" \
                       "s_sub_u32 %[n], %[n], 1\n\tv_cmp_ne_u32 vcc, 0, %[d]\n\ts_cbranch_scc1 2f\n\ts_cbranch_vccz 2f\n\t" \
                       "buffer_load_dwordx4 v[" #r0 ":" #r3 "], %[so], %[rs], %[k] offen sc1\n\tbuffer_load_dwordx4 v[" #r4 ":" #r7 "], %[so], %[rs], %[k1] offen sc1\n\t" \
                       "s_waitcnt vmcnt(0)\n\ts_branch 1b\n2:" \
                       : [d] "=&v"(d_), [n] "+s"(n_), __VA_ARGS__ \
                       : [t] "s"(tag), [so] "v"(so), [rs] "s"(rs), [k] "s"(4096 * i), [k1] "s"(4096 * i + 4096) : "vcc", "scc", "memory"); } while (0)
        if (i == 0) SWAIT(2, 132, 133, 135, 136, 137, 139, "+{v[132:139]}"(sa0), "+{v[100:107]}"(xa0), "+{v[108:115]}"(xa1), "+{v[160:163]}"(junk));
        if (i == 1) asm volatile("buffer_load_dwordx4 v[140:143], %[so], %[rs], %[k2] offen sc1\n\tbuffer_load_dwordx4 v[144:147], %[so], %[rs], %[k3] offen sc1"
                                 : "=&{v[140:147]}"(sa1) : [so] "v"(so), [rs] "s"(rs), [k2] "s"(8192), [k3] "s"(12288) : "memory");
        if (i == 2) {
          SWAIT(0, 140, 141, 143, 144, 145, 147, "+{v[140:147]}"(sa1), "+{v[116:123]}"(xa2), "+{v[124:131]}"(xa3));
        }
        const u32x8 xs = i == 0 ? xa0 : i == 1 ? xa1 : i == 2 ? xa2 : xa3;
        const u32 sa = i < 2 ? sa0[4 * i] : sa1[4 * i - 8], sb = i < 2 ? sa0[4 * i + 2] : sa1[4 * i - 6];
        if (i == 0) STAMP(3);
        if (i == 2) STAMP(10);
        const f32x2 r2 = {__builtin_amdgcn_rcpf(hi2f(sa)), __builtin_amdgcn_rcpf(hi2f(sb))};
        const f32x2 m2 = {-lo2f(sa) * r2.x, -lo2f(sb) * r2.y};
        f32x2 xx[8], n2[8];
        u32 o[8], fv = i + 1;
#pragma unroll
        for (int c = 0; c < 8; c++) {
          const u32 a = xs[c >> 1], bb = xs[4 + (c >> 1)];
          xx[c] = (c & 1) ? f32x2{hi2f(a), hi2f(bb)} : f32x2{lo2f(a), lo2f(bb)};
        }
        asm volatile("" : "+v"(fv));
        SB();
#pragma unroll
        for (int c = 0; c < 8; c++) n2[c] = pkfma(xx[c], r2, m2);
        SB();
#pragma unroll
        for (int c = 0; c < 8; c++) o[c] = packh(n2[c].x, n2[c].y);
        SB();
#pragma unroll
        for (int c = 0; c < 8; c++) asm volatile("ds_write_addtid_b32 %0 offset:%1" ::"v"(o[c]), "n"(c * XREG + 1024 * i) : "memory");
        FLAG(OXF + 4 * p, fv);
#pragma unroll
        for (int c = 0; c < 8; c++) xq[i][c] = (c & 1) ? pkfmas<1>(n2[c], sc[c >> 1], xx[c]) : pkfmas<0>(n2[c], sc[c >> 1], xx[c]);
        SB();
        if (i == 3) __builtin_amdgcn_s_setprio(PPRIO);
      }
    }
    STAMP(4);
    const rsrc_t ro = __builtin_amdgcn_make_buffer_rsrc((void*)out, 0, 2 * L * D, 0x00020000);
    const int u = 64 * (p & 1) + l;
    const char* yp = smem + OY + 512 * (p >> 1) + 4 * (u ^ ((u >> 4) & 6));
    const char* yq = yp + 4 * CHR;
    u32 ys[8];
    if (p == 0) {
      u32x2 ix;
      LANE(ln);
      const u32 ixo = IXO(ln);
      POLL8(ix, ixo);
      *(u32x2*)(smem + OIX + 8 * (ln & 7)) = ix;
      if (l < 8) atomicOr((u32*)(smem + OMISC) + ((ix.x & 2047u) >> 5), 1u << (ix.x & 31u));
      FLAG(OYP + 32, 8u);
      asm volatile("s_mov_b32 m0, %0\n\ts_nop 0\n\tglobal_load_lds_dword %1, %2" ::"s"(lds0 + OPX), "v"((ix.x & 2047u) * (2 * D) + 16 * g + ((ln >> 1) & 12)), "s"(x) : "memory");
      *(u32x4*)(smem + OIX + 64) = dv;
      STAMP(11);
    }
#pragma unroll
    for (int i = 0; i < 4; i++) {
      if (i == 2 && p == 0) { asm volatile("s_waitcnt vmcnt(5)" ::: "memory"); FLAG(OYP + 36, 8u); }
      AWAIT(OYP + 4 * min(l, 8), 2 * i + 1 + (p >> 1), YNAP);
      if (i == 0) STAMP(5); else if (i == 1) STAMP(6); else if (i == 2) STAMP(7); else STAMP(8);
      u32 y[8];
#pragma unroll
      for (int c = 0; c < 8; c++) y[c] = *(const u32*)((c < 4 ? yp : yq) + (c & 3) * CHR + 1024 * i);
      const u32 bm = *(const u32*)(smem + OMISC + 64 * i + 16 * p + 4 * kg) >> (2 * n16);
      if (i == 0) {
        if (t == 512) st8(rs, O_ARR + b * 8, u32x2{0u, tag});
        const u32x4 a = *(const u32x4*)(smem + OSC), c = *(const u32x4*)(smem + OSC + 16);
#pragma unroll
        for (int k = 0; k < 4; k++) { ys[k] = (u32)__builtin_amdgcn_readfirstlane((int)a[k]); ys[4 + k] = (u32)__builtin_amdgcn_readfirstlane((int)c[k]); }
      }
      SB();
#pragma unroll
      for (int r = 0; r < 2; r++) {
        u32x4 v;
#pragma unroll
        for (int k = 0; k < 4; k++)
          v[k] = pack2(dot2b(y[2 * k], ys[2 * k] << (16 * r), xq[i][2 * k][r]), dot2b(y[2 * k + 1], ys[2 * k + 1] << (16 * r), xq[i][2 * k + 1][r]));
        __builtin_amdgcn_raw_buffer_store_b128(v, ro, op | (bm << (31 - r) & 0x80000000u), (512 * i + r) * (2 * D), OAUX);
        SB();
        if (r) asm volatile("s_nop 1");
        SB();
      }
    }
    asm volatile("" ::"{v[160:163]}"(junk));
    STAMP(13);
    FLUSH();
    __builtin_amdgcn_endpgm();
  }

  u32 pl, pa, pr, pi, ldtv;
  u32x4 tv, fx, junk;
  ROLE(c, "", , "={v[16:19]}"(tv), "={v[20:23]}"(fx), "={v24}"(ldtv), "={v96}"(pl), "={v97}"(pa), "={v68}"(pr), "={v69}"(pi), "={v[160:163]}"(junk));
  char* W = smem + w * CHR;
  __builtin_amdgcn_s_setprio(2);
  STAMP(3);
  const u32x4 zero4 = {0u, 0u, 0u, 0u};
#pragma unroll
  for (int i = 0; i < 7; i++) *(u32x4*)(W + (i < 6 ? TB + 1024 * i : CHR - 1024) + 16 * l) = zero4;
  SB();
  const u32 wb0 = lds0 + w * CHR, xb0 = lds0 + OX + w * XREG;
  asm volatile("s_mov_b32 m0, %0\n\ts_nop 0\n\tds_write_addtid_b32 %1\n\tds_write_addtid_b32 %1 offset:%2" ::"s"(wb0), "v"(0x3c00u), "n"(PB) : "memory");
  typedef LDS char* lp;
  u32 ape = wb0 + (n16 == 12 ? 12 : (n16 ^ 3) - 7 * (n16 >> 3)) * RS + 16 * kg, apo = wb0 + (n16 < 12 ? n16 : n16 - 8) * RS + 16 * kg;
  u32x2 kb = {wb0 + ((15 - n16) & 3) * CPE + 2 * ((15 - n16) & 12) + 16 * kg, wb0 + (((n16 >> 2) & 1) + (n16 & 8) + 2 * (3 - (n16 & 3))) * RS + 16 * kg};
  u32x8 ka = {xb0 + 32 * n16 + 16 * kg, xb0 + 8 * l, wb0 + 8 * l, xb0 + 16 * l, wb0 + 16 * l,
              wb0 + 32 * n16 + 8 * (kg ^ ((l >> 2) & 3)),
              lds0 + OXF + 4 * (l & 3), wb0 + ((15 - n16) & 3) * CPO + 2 * ((15 - n16) & 12) + 8 * kg};
  asm volatile("" : "+{v70}"(ape), "+{v71}"(apo), "+{v[28:35]}"(ka), "+{v[26:27]}"(kb));
  STAMP(4);
  asm volatile("s_waitcnt vmcnt(4)" : "+{v24}"(ldtv), "+{v[160:163]}"(junk));
  float dt = rbt(fexp(hi2f(ldtv)));
  asm volatile("" : "+v"(dt));
  const f32x2 cdt = {rdl(dt * -1.4426950408889634f, 0), rdl(dt * 0.15915494309189535f, 0)};
  dt = rdl(dt, 0);
  SB();
  STAMP(10);
  STAMPC(11);
  {
    u32 e, tmp, o0, o1, of;
    asm volatile(GEN_ASM
                 : [e] "=&s"(e), [t] "=&s"(tmp), "=&{v41}"(o0), "=&{v46}"(o1), "=&{v48}"(of), "+{v68}"(pr), "+{v69}"(pi), "+{v70}"(ape),
                   "+{v71}"(apo), "+{v96}"(pl), "+{v97}"(pa), "+{v[28:35]}"(ka), "+{v[26:27]}"(kb), "+{v[16:19]}"(tv), "+{v[20:23]}"(fx)
                 : [w] "s"(wb0), [c] "s"(cdt), [k] "s"(f32x2{1.4426950408889634f, 256.f})
                 : "memory", "scc", GEN_CLOBBER);
    STAMP(1);
    STAMPC(12);
    const u32 k0 = (u32)__builtin_amdgcn_readlane((int)o0, 3), k256 = (u32)__builtin_amdgcn_readlane((int)o1, 60);
    const u32 k512 = (u32)__builtin_amdgcn_readlane((int)of, 51), k1024 = (u32)__builtin_amdgcn_readlane((int)of, 55);
    *(LDS u16*)(size_t)(wb0 + (tv.z & 0xffffu)) = (u16)__builtin_amdgcn_perm((k512 & 0xffffu) | (k1024 << 16), (k0 >> 16) | (k256 << 16), tv.w);
    *(u32*)(smem + OSC + 4 * w) = __float_as_uint(__uint_as_float((e - 5) << 23) * dt) >> 16;
    fx &= ((k1024 & 0xffffu) | (k1024 << 16)) ^ 0x80008000u;
  }
  const u32 axb = ka[0], axu = ka[1], awb = ka[2], axq = ka[3], awo = ka[4], ay = ka[5], axf = ka[6], atf = ka[7], ate = kb[0];
#define FRAG(o0, o1) mkh(*(const volatile LDS u32x2*)(tb + (o0)), *(const volatile LDS u32x2*)(tb + (o1)))
  f16x8 FR[9], F1[4][2], F2[2][3], F3[4];
  const LDS char* tb = (lp)(size_t)ate;
#pragma unroll
  for (int c = 0; c < 9; c++) FR[c] = FRAG(64 * c, 64 * c + 8);
  tb = (lp)(size_t)atf + TB;
#pragma unroll
  for (int p = 0; p < 4; p++)
#pragma unroll
    for (int dl = 0; dl < 2; dl++) F1[p][dl] = FRAG(2 * (2 * C1 * p + 16 - 16 * dl), 2 * (2 * C1 * p + C1 + 16 - 16 * dl));
#pragma unroll
  for (int p = 0; p < 2; p++)
#pragma unroll
    for (int dl = 0; dl < 3; dl++) F2[p][dl] = FRAG(2 * (T2B + 2 * C2 * p + 32 - 16 * dl), 2 * (T2B + 2 * C2 * p + C2 + 32 - 16 * dl));
  *(LDS u32x4*)(size_t)(wb0 + (tv.z >> 16)) = zero4;
  STAMP(2);
  const u32 wbk = (u32)(b >> 3) * 16 + w;
#define WBL2() asm volatile("s_cmp_lg_u32 %0, %1\n\ts_cbranch_scc1 1f\n\tbuffer_wbl2 sc1\n1:" ::"s"(wbk), "n"(WBW) : "scc", "memory")
#define XWAIT(n) do { _Pragma("unroll 1") for (int spin = 0; spin < SPIN; spin++) { \
      if (__all(*(const volatile LDS u32*)(size_t)axf >= (u32)(n))) break; \
      NAP(); } SB(); asm volatile("" ::: "memory"); } while (0)
  XWAIT(1);
  STAMP(5);
  STAMPC(14);
  asm volatile("s_cmp_lt_u32 %0, 4\n\ts_cbranch_scc0 1f\n\ts_setprio 1\n1:" ::"s"(w) : "scc");
  {
    const lp xb = (lp)(size_t)axb, xu = (lp)(size_t)axu + 2 * XO, wb = (lp)(size_t)awb + 32, xq = (lp)(size_t)axq + 512;
    const lp wo = (lp)(size_t)awo + 16, Y = (lp)(size_t)ay + OY;
    f32x4 acc[8];
    u32x4 A[NOPS];
    u32x4 Q1, Q2, W7, W9;
    u32 QZ = 0u, Uo[8], P2[8], P4[8], W3[8], W5[8], xf = 0u, fl = 0u, fad = lds0 + OYP + 4 * w;
    u32x2 yv;
    asm volatile("" : "+v"(fad));
    auto winl = [&](auto P_) __attribute__((always_inline)) {
      constexpr int pp = decltype(P_)::value;
      Q1 = *(const LDS u32x4*)(xq + 1024 * pp);
      if (pp < 2) { Q2 = *(const LDS u32x4*)(xq + 1024 * pp + 16); QZ = *(const LDS u32*)(xq + 1024 * pp - 4); }
      else { const u32x2 v = *(const LDS u32x2*)(xq + 1024 * pp + 16); Q2.x = v.x; Q2.y = v.y; }
    };
    auto win = [&](auto K_) __attribute__((always_inline)) {
      constexpr int k = decltype(K_)::value;
      const u32 Dd[8] = {QZ, Q1.x, Q1.y, Q1.z, Q1.w, Q2.x, Q2.y, Q2.z};
      if constexpr (k < 5) Uo[2 + k] = __builtin_amdgcn_alignbit(Dd[2 + k], Dd[1 + k], 16);
      else if constexpr (k < 9) P2[k - 3] = hadd(Dd[k - 3], Uo[k - 3]);
      else if constexpr (k < 13) W3[k - 7] = hadd(P2[k - 7], Uo[k - 6]);
      else if constexpr (k == 13) P2[6] = hadd(Dd[6], Uo[6]);
      else if constexpr (k < 18) P4[k - 11] = hadd(P2[k - 11], P2[k - 12]);
      else if constexpr (k < 22) W5[k - 16] = hadd(P4[k - 15], Dd[k - 17]);
      else if constexpr (k == 22) Uo[1] = __builtin_amdgcn_alignbit(Dd[1], Dd[0], 16);
      else if constexpr (k == 23) Uo[7] = __builtin_amdgcn_alignbit(Dd[7], Dd[6], 16);
      else if constexpr (k == 24) P2[1] = hadd(Dd[1], Uo[1]);
      else if constexpr (k == 25) P2[7] = hadd(Dd[7], Uo[7]);
      else if constexpr (k == 26) P4[2] = hadd(P2[2], P2[1]);
      else if constexpr (k == 27) P4[7] = hadd(P2[7], P2[6]);
      else if constexpr (k == 28) W3[6] = hadd(P2[6], Uo[7]);
      else if constexpr (k == 29) W5[1] = hadd(P4[2], Dd[0]);
      else if constexpr (k < 34) W7[k - 30] = hadd(W3[k - 27], P4[k - 28]);
      else W9[k - 34] = hadd(P4[k - 30], W5[k - 33]);
      if constexpr (k == 13) asm volatile("" : "+v"(P2[6]));
      if constexpr (k > 13 && k < 18) asm volatile("" : "+v"(P4[k - 11]));
      if constexpr (k >= 18 && k < 22) asm volatile("" : "+v"(W5[k - 16]));
    };
    auto loads = [&](auto M_) __attribute__((always_inline)) {
      constexpr int m = decltype(M_)::value;
      sfor<0, 2>([&](auto N_) __attribute__((always_inline)) {
        constexpr int mm = m + LA + decltype(N_)::value;
        if constexpr (mm < NSLOT) {
          constexpr int s = OPS.op[mm], k = OPS.k[s], T = OPS.a[s], p = OPS.b[s];
          if constexpr (OPS.first[s] == mm) {
            if constexpr (k == 0) { if (OPS.ld[s][1] == m) A[s] = *(const LDS u32x4*)(xb + 512 * T + 64 * p); }
            else {
              if (OPS.ld[s][0] == m) {
                const u32x2 a = trl(k == 3 ? wb + OW7 + 512 * T - 16 : k == 2 ? wb + OW3 + 512 * T - 2 * (4 + 8 * p) : xu + 512 * T - 2 * (2 + 4 * p));
                A[s].x = a.x; A[s].y = a.y;
              }
              if (OPS.ld[s][1] == m) {
                const u32x2 c = trl(k == 3 ? wb + OW9 + 512 * T - 32 : k == 2 ? wb + OW5 + 512 * T - 2 * (8 + 8 * p) : wb + OW3 + 512 * T - 2 * (4 + 4 * p));
                A[s].z = c.x; A[s].w = c.y;
              }
            }
          }
        }
      });
    };
    constexpr int FT[8] = {0, OPS.first[opat(0, 1, 0)] + 1, OPS.first[opat(0, 2, 0)] + 1, OPS.first[opat(2, 0, 0)] + 1, OPS.first[opat(0, 1, 0)] + 2,
                           OPS.first[opat(0, 2, 0)] + 2, OPS.first[opat(0, 3, 0)] + 2, OPS.first[opat(0, 4, 0)] + 2};
    loads(IC<-LA>{});
    winl(IC<0>{});
    sfor<1 - LA, 0>(loads);
    auto step = [&](auto M_) __attribute__((always_inline)) {
      constexpr int m = decltype(M_)::value, s = OPS.op[m], u = m - OPS.first[s], k = OPS.k[s], T = OPS.a[s], p = OPS.b[s];
      SB();
      const f16x8 a = ash(A[s]);
#define MM(I, x_, y_) do { if constexpr (m == FT[I]) asm volatile("v_mfma_f32_16x16x32_f16 %0, %1, %2, 0" : "=&v"(acc[I]) : "v"(x_), "v"(y_)); \
      else asm volatile("v_mfma_f32_16x16x32_f16 %0, %1, %2, %0" : "+v"(acc[I]) : "v"(x_), "v"(y_)); } while (0)
      if constexpr (k == 0) {
        if constexpr (p == 0 && T > 0 && u == 0) MM(T - 1, FR[8], a);
        else if constexpr (u == 2) MM(T + 3, fx, a);
        else MM(T, FR[p], a);
      }
      else if constexpr (k == 3) MM(T + 4 + u, a, F3[u]);
      else if constexpr (k == 2) MM(T + 2 + u, a, F2[p][u]);
      else MM(T + 1 + u, a, F1[p][u]);
      SB();
      if constexpr (m == OPS.end[PH / 100 % 10] + 5) asm volatile("s_cmp_lt_u32 %0, 4\n\ts_cbranch_scc1 1f\n\ts_setprio 1\n1:" ::"s"(w) : "scc");
      if constexpr (m == OPS.end[PH / 10 % 10] + 5) asm volatile("s_cmp_lt_u32 %0, 4\n\ts_cbranch_scc0 1f\n\ts_setprio 0\n1:" ::"s"(w) : "scc");
      if constexpr (m == OPS.end[PH % 10] + 5) asm volatile("s_setprio 0");
      sfor<0, 3>([&](auto N_) __attribute__((always_inline)) {
        constexpr int n = decltype(N_)::value;
        if (m == XC[n]) { if (!__all(xf >= n + 2u)) XWAIT(n + 2); }
        if (m == XC[n] - 4) xf = *(const volatile LDS u32*)(size_t)axf;
      });
      sfor<0, 7>([&](auto I_) __attribute__((always_inline)) {
        constexpr int I = decltype(I_)::value;
        if (m == OPS.end[I] + 4) {
          *(LDS u32x2*)(Y + 512 * I) = yv;
          asm volatile("ds_write_b32 %0, %1" ::"v"(fad), "v"(fl) : "memory");
          if (WBM >> I & 1) WBL2();
          if (I == 0) STAMP(6);
          if (I == 3) STAMP(7);
        }
      });
      sfor<0, 4>([&](auto P_) __attribute__((always_inline)) {
        constexpr int pp = decltype(P_)::value;
#if !(ABL & 4)
        if (m == wslot(pp, 12) + 1) *(LDS u32x4*)(wo + 1024 * pp + OW3) = u32x4{W3[2], W3[3], W3[4], W3[5]};
        if (pp < 3 && m == wslot(pp, 21) + 1) *(LDS u32x4*)(wo + 1024 * pp + OW5) = u32x4{W5[2], W5[3], W5[4], W5[5]};
        if (pp < 2 && m == wslot(pp, 33) + 1) *(LDS u32x4*)(wo + 1024 * pp + OW7) = W7;
        if (pp < 2 && m == wslot(pp, 37) + 1) *(LDS u32x4*)(wo + 1024 * pp + OW9) = W9;
#endif
        if (m == WL[pp]) winl(P_);
        if (m == OPS.first[opat(3, 0, 0)] - 8 + pp) F3[pp] = FRAG(2 * (T3B + 48 - 16 * pp), 2 * (T3B + C3 + 48 - 16 * pp));
      });
      loads(M_);
      SB();
      sfor<0, 4>([&](auto P_) __attribute__((always_inline)) {
        constexpr int pp = decltype(P_)::value;
        if (pp < 2 && m == wslot(pp, 3)) asm volatile("" : "+v"(Q2));
        if (!(ABL & 6)) sfor<0, WN[pp]>([&](auto K_) __attribute__((always_inline)) { if constexpr (wslot(pp, decltype(K_)::value) == m) win(K_); });
      });
      sfor<0, 7>([&](auto I_) __attribute__((always_inline)) {
        constexpr int I = decltype(I_)::value;
        if (m == OPS.end[I] + 3) {
          yv = u32x2{pack2(acc[I][0], acc[I][1]), pack2(acc[I][2], acc[I][3])};
          fl = I + 1;
          asm volatile("" : "+v"(fl), "+v"(yv));
        }
      });
    };
    sfor<0, NSLOT>(step);
    SB();
    asm volatile("s_nop 7\n\ts_nop 1" : "+v"(acc[7]));
    *(LDS u32x2*)(Y + 512 * 7) = u32x2{pack2(acc[7][0], acc[7][1]), pack2(acc[7][2], acc[7][3])};
    FLAG(OYP + 4 * w, 8u);
    if (WBM >> 7 & 1) WBL2();
  }
  STAMP(13);
  STAMPC(15);
  if (w != 0) {
    FLUSH();
    __builtin_amdgcn_endpgm();
  }
  {
    __builtin_amdgcn_s_setprio(0);
    LANE(ln);
    const u32 po = O_PG + 256 * g + 16 * (ln & 15) + 32768 * (ln >> 4), p3 = ln < 32 ? po + 3 * 131072 : 0x80000000u;
    u32x4 pv[4];
#pragma unroll
    for (int q = 0; q < 4; q++) pv[q] = ld16(rs, q < 3 ? po + 131072 * q : p3);
    SB();
    {
      const char* fp = smem + OYP + 4 * min(ln, 9);
#pragma unroll 1
      for (int spin = 0; spin < SPIN; spin++) {
        if (__all(*(const volatile LDS u32*)(LDS char*)fp >= 8u)) break;
        asm volatile("s_sleep 1" ::: "memory");
      }
      asm volatile("" ::: "memory");
    }
    const int c = ((ln >> 1) & 4) + (ln >> 4);
    const u32x2 ix = *(const u32x2*)(smem + OIX + 8 * (ln & 7));
    const u32 px = *(const u16*)(smem + OPX + 32 * (c >> 1) + 4 * (ln & 7) + 2 * (c & 1)), pd = *(const u16*)(smem + OIX + 64 + 2 * c);
    tag = (u32)__builtin_amdgcn_readfirstlane((int)ix.y);
    const int prow = (int)(ix.x & 2047u), v = prow >> 1;
    float rsel = rbt(sigm(hi2f(ix.x)));
    const float yl = lo2f(*(const u16*)(smem + OY + CHR * c + 4 * (v ^ ((v >> 4) & 6)) + 2 * (prow & 1)));
    const float ql = hlo(*(const u16*)(smem + OX + 2 * XO + c * XREG + 2 * prow));
    float e = lo2f(px) + yl * lo2f(*(const u32*)(smem + OSC + 4 * c)) + ql * (1.f + lo2f(pd));
    GL u16* op = out + (size_t)prow * D + 8 * g + c;
    asm volatile("" : "+v"(e), "+v"(op), "+v"(rsel));
    STAMP(8);
#pragma unroll 1
    for (int spin = 0; spin < SPIN; spin++) {
      bool ok[4];
#pragma unroll
      for (int q = 0; q < 4; q++) ok[q] = __all((pv[q].y == tag && pv[q].w == tag) || (q == 3 && ln >= 32));
      if (ok[0] && ok[1] && ok[2] && ok[3]) break;
#pragma unroll
      for (int q = 0; q < 4; q++) if (!ok[q]) pv[q] = ld16(rs, q < 3 ? po + 131072 * q : p3);
    }
    STAMP(9);
    float s[4] = {};
#pragma unroll
    for (int q = 0; q < 4; q++) { s[0] += lo2f(pv[q].x); s[1] += hi2f(pv[q].x); s[2] += lo2f(pv[q].z); s[3] += hi2f(pv[q].z); }
    swap32(s[0], s[2]); swap32(s[1], s[3]);
    s[0] += s[2]; s[1] += s[3];
    swap16(s[0], s[1]);
    const float upd = rb(rb(s[0] + s[1]) * rsel);
    *op = (u16)(pack2(e + upd, 0.f) & 0xffffu);
    STAMP(4);
  }
  FLUSH();
  asm volatile(".Lrole_end:");
  __builtin_amdgcn_endpgm();
}

extern "C" void kernel_launch(void* const* d_in, const int* in_sizes, int n_in, void* d_out, int out_size,
                              void* d_ws, size_t ws_size, hipStream_t stream) {
  const u16* const* in = (const u16* const*)d_in;
  fused<<<256, 768, 0, stream>>>(in[0], in[1], in[2], in[3], in[4], in[5], in[6], in[8], in[9], in[10], in[11],
                                 (u16*)d_out, (char*)d_ws);
}
```

```cpp
#include <hip/hip_runtime.h>
#include <stdint.h>

#define STR2(x) #x
#define STR(x) STR2(x)
#define LA 4
#define WL2 44
#define WL3 100
#define PH 1246
#define ABL 0
#define PPRIO 3
#define CPRIO 1
#define GDELAY 0
#define ROT 1
#define PRE 2
#define GNT 0
#define OAUX 0
#define LNT 1
#define PNT ""
#define LNNT ""
#define PRENT ""
#define PFN 15
#define PFO 1024
#define DBAR 2
#define THR 1
#define QD 11
#define HNAP0 4
#define HNAP 2
#define KR 3
#define KI 2
#define QE 11
#define QA 5
#define NA 0
#define CH 0
#define G0E 0
#define DDL 0
#define WBM 0
#define WBW 1
#define HQ 3
#define RSL 5
#define DPF 0
#define HELP 0
#define WARM 0
#define HPI 0
#define Q0 6
#define NP 10
#define HW 28
#define NE 0
#define YNAP "s_sleep 2"
#define QS 7
#define QAS 6
#define RAMP 4
#define GNAP "s_sleep 1"
#define HOLD 8
#define DPRIO 3
#define HAUX 16
#define DPO 0
#define TW 310
#define TL 4352
#define CODE_KB 24
typedef unsigned short u16;
typedef unsigned int u32;
typedef __attribute__((ext_vector_type(8))) __bf16 bf16x8;
typedef __attribute__((ext_vector_type(2))) __bf16 bf16x2;
typedef __attribute__((ext_vector_type(8))) _Float16 f16x8;
typedef __attribute__((ext_vector_type(2))) _Float16 f16x2;
typedef __attribute__((ext_vector_type(4))) short v4i16;
typedef __attribute__((ext_vector_type(4))) float f32x4;
typedef __attribute__((ext_vector_type(2))) float f32x2;
typedef __attribute__((ext_vector_type(4))) u32 u32x4;
typedef __attribute__((ext_vector_type(2))) u32 u32x2;
typedef __attribute__((ext_vector_type(8))) u32 u32x8;
typedef __attribute__((ext_vector_type(2), aligned(4))) u32 u32x2_a4;
typedef __amdgpu_buffer_rsrc_t rsrc_t;
#define LDS __attribute__((address_space(3)))

#define TLARG
#define TLVAL
#define STAMP(I)
#define STAMPC(I)
#define FLUSH()

constexpr int L = 2048, D = 1024, NM = 128, F = 3584;
constexpr u32 O_ARR = 0x1000, O_RG = 0x4000, O_SG = 0x8000, O_IDX = 0x10000, O_HG = 0x20000,
              O_PG = 0xd0000, O_TOP = 0x140000, TOPS = 8 * TL, WS_END = O_TOP + 2 * TOPS;
constexpr int SPIN = 1 << 16;

__device__ __forceinline__ float lo2f(u32 w) { return __uint_as_float(w << 16); }
__device__ __forceinline__ float hi2f(u32 w) { return __uint_as_float(w & 0xffff0000u); }
__device__ __forceinline__ u32 pack2(float a, float b) {
  bf16x2 r = {(__bf16)a, (__bf16)b};
  return __builtin_bit_cast(u32, r);
}
__device__ __forceinline__ u32 packh(float a, float b) {
  f16x2 r = {(_Float16)a, (_Float16)b};
  return __builtin_bit_cast(u32, r);
}
__device__ __forceinline__ float rbt(float f) { return __uint_as_float(pack2(0.f, f)); }
__device__ __forceinline__ float rb(float f) {
  float r;
  asm("v_cvt_pk_bf16_f32 %0, 0, %1" : "=v"(r) : "v"(f));
  return r;
}
__device__ __forceinline__ u32 hadd(u32 a, u32 b) {
  return __builtin_bit_cast(u32, __builtin_bit_cast(f16x2, a) + __builtin_bit_cast(f16x2, b));
}
__device__ __forceinline__ float hlo(u32 a) { return (float)__builtin_bit_cast(f16x2, a).x; }
__device__ __forceinline__ float hhi(u32 a) { return (float)__builtin_bit_cast(f16x2, a).y; }

template <int CTRL, int RM>
__device__ __forceinline__ float dppf(float v) {
  return __builtin_bit_cast(float, __builtin_amdgcn_update_dpp(0, __builtin_bit_cast(int, v), CTRL, RM, 0xf, true));
}
template <int CTRL, int RM>
__device__ __forceinline__ u32 dppu(u32 v) {
  return (u32)__builtin_amdgcn_update_dpp(0, (int)v, CTRL, RM, 0xf, true);
}
__device__ __forceinline__ float rdl(float v, int i) {
  return __builtin_bit_cast(float, __builtin_amdgcn_readlane(__builtin_bit_cast(int, v), i));
}
__device__ __forceinline__ u32 wmaxu(u32 v) {
  v = max(v, dppu<0x111, 0xf>(v)); v = max(v, dppu<0x112, 0xf>(v)); v = max(v, dppu<0x114, 0xf>(v));
  v = max(v, dppu<0x118, 0xf>(v)); v = max(v, dppu<0x142, 0xa>(v)); v = max(v, dppu<0x143, 0xc>(v));
  return (u32)__builtin_amdgcn_readlane((int)v, 63);
}
__device__ __forceinline__ void swap32(float& a, float& b) {
  auto r = __builtin_amdgcn_permlane32_swap(__float_as_uint(a), __float_as_uint(b), false, false);
  a = __uint_as_float(r[0]); b = __uint_as_float(r[1]);
}
__device__ __forceinline__ void swap16(float& a, float& b) {
  auto r = __builtin_amdgcn_permlane16_swap(__float_as_uint(a), __float_as_uint(b), false, false);
  a = __uint_as_float(r[0]); b = __uint_as_float(r[1]);
}
__device__ __forceinline__ u32x2 trread(const char* p) {
  v4i16 r = __builtin_amdgcn_ds_read_tr16_b64_v4i16((LDS v4i16*)p);
  return __builtin_bit_cast(u32x2, r);
}
__device__ __forceinline__ u32x2 trl(LDS char* p) { return __builtin_bit_cast(u32x2, __builtin_amdgcn_ds_read_tr16_b64_v4i16((LDS v4i16*)p)); }
__device__ __forceinline__ f16x8 mkh(u32x2 a, u32x2 b) {
  u32x4 v = {a.x, a.y, b.x, b.y};
  return __builtin_bit_cast(f16x8, v);
}
__device__ __forceinline__ bf16x8 mkb(u32x2 a, u32x2 b) {
  u32x4 v = {a.x, a.y, b.x, b.y};
  return __builtin_bit_cast(bf16x8, v);
}
__device__ __forceinline__ bf16x8 asb(u32x4 v) { return __builtin_bit_cast(bf16x8, v); }
__device__ __forceinline__ f16x8 ash(u32x4 v) { return __builtin_bit_cast(f16x8, v); }
#define MFMAB(a, b, c) __builtin_amdgcn_mfma_f32_16x16x32_bf16(a, b, c, 0, 0, 0)
#define MFMAH(a, b, c) __builtin_amdgcn_mfma_f32_16x16x32_f16(a, b, c, 0, 0, 0)

__device__ __forceinline__ u32x4 ld16(rsrc_t r, u32 off) { return __builtin_amdgcn_raw_buffer_load_b128(r, off, 0, 16); }
__device__ __forceinline__ u32x2 ld8(rsrc_t r, u32 off) { return __builtin_amdgcn_raw_buffer_load_b64(r, off, 0, 16); }
__device__ __forceinline__ void st16(rsrc_t r, u32 off, u32x4 v) { __builtin_amdgcn_raw_buffer_store_b128(v, r, off, 0, 16); }
__device__ __forceinline__ void st8(rsrc_t r, u32 off, u32x2 v) { __builtin_amdgcn_raw_buffer_store_b64(v, r, off, 0, 16); }
#define NAP() asm volatile("s_sleep 1" ::: "memory")
#define SB() __builtin_amdgcn_sched_barrier(0)
#define LBAR() asm volatile("s_waitcnt lgkmcnt(0)\n\ts_barrier" ::: "memory")

__device__ __forceinline__ float fexp(float v) { return __builtin_amdgcn_exp2f(v * 1.4426950408889634f); }
__device__ __forceinline__ float dot2b(u32 a, u32 b, float c) {
  return __builtin_amdgcn_fdot2_f32_bf16(__builtin_bit_cast(bf16x2, a), __builtin_bit_cast(bf16x2, b), c, false);
}
__device__ __forceinline__ float sigm(float v) { return __builtin_amdgcn_rcpf(1.f + __builtin_amdgcn_exp2f(-1.4426950408889634f * v)); }

constexpr int RS = 288, PB = 16 * RS;
constexpr int OW3 = 0, OW5 = 3632, OW7 = 6720, OW9 = 8784, OY = 11648, CHR = OY + 4096;
constexpr int OX = 8 * CHR, XREG = 4640, XO = 266;
constexpr int OMISC = OX + 8 * XREG;
constexpr int OYP = OMISC + 256, OXF = OMISC + 320, OCN = OMISC + 336, OSC = OMISC + 352, OIX = OMISC + 384;
constexpr int OPX = OMISC + 512, SMEM = OMISC + 768;
constexpr int CPE = 704, TB = 2 * PB, CPO = 576;
constexpr int T2B = 2 * CPO + 4, T3B = (OY + 2048 - TB) / 2, C1 = 36, C2 = 52, C3 = 84;
constexpr int FA = 131072, FH = FA + 8192, HP = 576, FC = FH + 8 * HP;
static_assert(FC + 4480 <= SMEM && SMEM <= 163840, "lds map");

struct Tab { u32 v[2][64][4]; };
constexpr Tab mktab() {
  Tab T = {};
  for (int l = 0; l < 64; l++) {
    const int n16 = l & 15, kg = l >> 4, pc = ((n16 >> 2) & 1) + (n16 & 8) + 2 * (3 - (n16 & 3)), odd = kg >> 1, s = l & 3;
    const int e0 = 278 - 128 - 8 * pc - 4 * (kg & 1) - odd;
    const int ez = ((l >> 3) & 3) * CPE + ((l & 4) ? 544 - 64 : 0) + 16 * (l & 7);
    const int b1 = 28 + C1 * (n16 + 4 * (kg & 1)), b2 = T2B + 44 + C2 * ((n16 & 1) + 2 * (kg & 1)) - 8 * (n16 & 2);
    const int b3 = T3B + 60 + C3 * (kg & 1) - 16 * (n16 - 8);
    const int to = TB + 2 * ((n16 < 4 ? b1 : (n16 & 4) ? b2 : b3) - 4 * odd - 8);
    const int sp = n16 < 8 ? s * (CPE - 2) + 2 * ((l & 4) ? 281 - 257 : (s & 1) ? 281 : 281 - 256) : TB + s * (CPO - 2) + 2 * ((l & 4) ? T2B + 15 : 15);
    T.v[0][l][0] = (2 * e0 + (2 - odd) * (CPE - 2)) | (2 * e0 + 3 * odd * (CPE - 2)) << 16;
    T.v[0][l][1] = ez | to << 16;
    T.v[0][l][2] = sp | (s == 0 ? OW3 : s == 1 ? OW5 : s == 2 ? OW7 : OW9) << 16;
    T.v[0][l][3] = n16 < 8 ? ((l & 5) == 1 ? 0x0100u : 0x0302u) : (l & 4) ? 0x0706u : 0x0504u;
    const int jx = n16 + 5 - 8 * kg;
    for (int r = 0; r < 4; r++) T.v[1][l][r] = jx == 2 * r ? 0xffffu : jx == 2 * r + 1 ? 0xffff0000u : 0u;
  }
  return T;
}
extern "C" __device__ const __attribute__((aligned(128), used, visibility("hidden"))) Tab lane_tab = mktab();

#define GEN_ASM \
  "s_waitcnt vmcnt(2)\n\tv_lshlrev_b32 v56, 16, v96\n\t" \
  "v_and_b32 v57, 0xffff0000, v96\n\tv_pk_mul_f32 v[56:57], %[k], v[56:57] op_sel_hi:[0,1]\n\t" \
  "v_lshlrev_b32 v49, 16, v97\n\tv_exp_f32 v56, v56\n\t" \
  "v_exp_f32 v57, v57\n\tv_and_b32 v51, 0xffff0000, v97\n\t" \
  "v_cvt_pk_bf16_f32 v56, v56, v57\n\tv_lshlrev_b32 v48, 16, v56\n\t" \
  "v_and_b32 v50, 0xffff0000, v56\n\tv_pk_mul_f32 v[48:49], %[c], v[48:49]\n\t" \
  "v_pk_mul_f32 v[50:51], %[c], v[50:51]\n\tv_pk_mul_f32 v[98:99], %[k], v[48:49] op_sel:[1,0]\n\t" \
  "v_pk_mul_f32 v[100:101], %[k], v[50:51] op_sel:[1,0]\n\tv_fract_f32 v49, v49\n\t" \
  "v_fract_f32 v51, v51\n\tv_exp_f32 v48, v48\n\t" \
  "v_exp_f32 v50, v50\n\tv_cos_f32 v40, v49\n\t" \
  "v_sin_f32 v41, v49\n\tv_cos_f32 v42, v51\n\t" \
  "v_sin_f32 v43, v51\n\tv_pk_mul_f32 v[40:41], v[40:41], v[48:49] op_sel_hi:[1,0]\n\t" \
  "v_pk_mul_f32 v[42:43], v[42:43], v[50:51] op_sel_hi:[1,0]\n\tv_pk_mul_f32 v[48:49], v[40:41], v[40:41] op_sel:[1,1] op_sel_hi:[1,0] neg_lo:[1,0]\n\t" \
  "v_pk_mul_f32 v[50:51], v[42:43], v[42:43] op_sel:[1,1] op_sel_hi:[1,0] neg_lo:[1,0]\n\tv_pk_fma_f32 v[48:49], v[40:41], v[40:41], v[48:49] op_sel:[0,0,0] op_sel_hi:[0,1,1]\n\t" \
  "v_pk_fma_f32 v[50:51], v[42:43], v[42:43], v[50:51] op_sel:[0,0,0] op_sel_hi:[0,1,1]\n\tv_cvt_pk_f16_f32 v72, v40, -v41\n\t" \
  "v_cvt_pk_f16_f32 v73, v42, -v43\n\tv_pk_mul_f32 v[52:53], v[48:49], v[40:41] op_sel:[1,1] op_sel_hi:[1,0] neg_lo:[1,0]\n\t" \
  "v_pk_mul_f32 v[54:55], v[50:51], v[42:43] op_sel:[1,1] op_sel_hi:[1,0] neg_lo:[1,0]\n\tv_pk_fma_f32 v[52:53], v[48:49], v[40:41], v[52:53] op_sel:[0,0,0] op_sel_hi:[0,1,1]\n\t" \
  "v_pk_fma_f32 v[54:55], v[50:51], v[42:43], v[54:55] op_sel:[0,0,0] op_sel_hi:[0,1,1]\n\tv_pk_mul_f32 v[56:57], v[48:49], v[48:49] op_sel:[1,1] op_sel_hi:[1,0] neg_lo:[1,0]\n\t" \
  "v_pk_mul_f32 v[58:59], v[50:51], v[50:51] op_sel:[1,1] op_sel_hi:[1,0] neg_lo:[1,0]\n\tv_pk_fma_f32 v[56:57], v[48:49], v[48:49], v[56:57] op_sel:[0,0,0] op_sel_hi:[0,1,1]\n\t" \
  "v_pk_fma_f32 v[58:59], v[50:51], v[50:51], v[58:59] op_sel:[0,0,0] op_sel_hi:[0,1,1]\n\tv_cvt_pk_f16_f32 v74, v48, -v49\n\t" \
  "v_cvt_pk_f16_f32 v75, v50, -v51\n\tv_cvt_pk_f16_f32 v76, v52, -v53\n\t" \
  "v_cvt_pk_f16_f32 v77, v54, -v55\n\tv_pk_mul_f32 v[52:53], v[56:57], v[48:49] op_sel:[1,1] op_sel_hi:[1,0] neg_lo:[1,0]\n\t" \
  "v_pk_mul_f32 v[54:55], v[58:59], v[50:51] op_sel:[1,1] op_sel_hi:[1,0] neg_lo:[1,0]\n\tv_pk_fma_f32 v[52:53], v[56:57], v[48:49], v[52:53] op_sel:[0,0,0] op_sel_hi:[0,1,1]\n\t" \
  "v_pk_fma_f32 v[54:55], v[58:59], v[50:51], v[54:55] op_sel:[0,0,0] op_sel_hi:[0,1,1]\n\tv_pk_mul_f32 v[60:61], v[56:57], v[56:57] op_sel:[1,1] op_sel_hi:[1,0] neg_lo:[1,0]\n\t" \
  "v_pk_mul_f32 v[62:63], v[58:59], v[58:59] op_sel:[1,1] op_sel_hi:[1,0] neg_lo:[1,0]\n\tv_pk_fma_f32 v[60:61], v[56:57], v[56:57], v[60:61] op_sel:[0,0,0] op_sel_hi:[0,1,1]\n\t" \
  "v_pk_fma_f32 v[62:63], v[58:59], v[58:59], v[62:63] op_sel:[0,0,0] op_sel_hi:[0,1,1]\n\tv_cvt_pk_f16_f32 v78, v56, -v57\n\t" \
  "v_cvt_pk_f16_f32 v79, v58, -v59\n\tv_cvt_pk_f16_f32 v82, v52, -v53\n\t" \
  "v_cvt_pk_f16_f32 v83, v54, -v55\n\tv_pk_mul_f16 v80, v78, v72 op_sel:[1,1] op_sel_hi:[1,0] neg_lo:[1,0]\n\t" \
  "v_pk_mul_f16 v81, v79, v73 op_sel:[1,1] op_sel_hi:[1,0] neg_lo:[1,0]\n\tv_pk_fma_f16 v80, v78, v72, v80 op_sel:[0,0,0] op_sel_hi:[0,1,1]\n\t" \
  "v_pk_fma_f16 v81, v79, v73, v81 op_sel:[0,0,0] op_sel_hi:[0,1,1]\n\tv_pk_mul_f16 v84, v78, v76 op_sel:[1,1] op_sel_hi:[1,0] neg_lo:[1,0]\n\t" \
  "v_pk_mul_f16 v85, v79, v77 op_sel:[1,1] op_sel_hi:[1,0] neg_lo:[1,0]\n\tv_pk_fma_f16 v84, v78, v76, v84 op_sel:[0,0,0] op_sel_hi:[0,1,1]\n\t" \
  "v_pk_fma_f16 v85, v79, v77, v85 op_sel:[0,0,0] op_sel_hi:[0,1,1]\n\tv_cvt_pk_f16_f32 v86, v60, -v61\n\t" \
  "v_cvt_pk_f16_f32 v87, v62, -v63\n\tds_write_addtid_b32 v72 offset:288\n\t" \
  "ds_write_addtid_b32 v73 offset:4896\n\tds_write_addtid_b32 v74 offset:576\n\t" \
  "ds_write_addtid_b32 v75 offset:5184\n\tds_write_addtid_b32 v76 offset:864\n\t" \
  "ds_write_addtid_b32 v77 offset:5472\n\tds_write_addtid_b32 v78 offset:1152\n\t" \
  "ds_write_addtid_b32 v79 offset:5760\n\tds_write_addtid_b32 v80 offset:1440\n\t" \
  "ds_write_addtid_b32 v81 offset:6048\n\tds_write_addtid_b32 v82 offset:1728\n\t" \
  "ds_write_addtid_b32 v83 offset:6336\n\tds_write_addtid_b32 v84 offset:2016\n\t" \
  "ds_write_addtid_b32 v85 offset:6624\n\tds_write_addtid_b32 v86 offset:3456\n\t" \
  "ds_write_addtid_b32 v87 offset:8064\n\tds_read_b128 v[104:107], v70 offset:0\n\t" \
  "ds_read_b128 v[108:111], v70 offset:64\n\tds_read_b128 v[112:115], v70 offset:128\n\t" \
  "ds_read_b128 v[116:119], v70 offset:192\n\tds_read_b128 v[120:123], v70 offset:4608\n\t" \
  "ds_read_b128 v[124:127], v70 offset:4672\n\tds_read_b128 v[128:131], v70 offset:4736\n\t" \
  "ds_read_b128 v[132:135], v70 offset:4800\n\tv_fract_f32 v99, v99\n\t" \
  "v_fract_f32 v101, v101\n\tv_exp_f32 v98, v98\n\t" \
  "v_exp_f32 v100, v100\n\tv_cos_f32 v44, v99\n\t" \
  "v_sin_f32 v45, v99\n\tv_cos_f32 v46, v101\n\t" \
  "v_sin_f32 v47, v101\n\tv_pk_mul_f32 v[44:45], v[44:45], v[98:99] op_sel_hi:[1,0]\n\t" \
  "v_pk_mul_f32 v[46:47], v[46:47], v[100:101] op_sel_hi:[1,0]\n\ts_waitcnt vmcnt(0)\n\t" \
  "v_pk_mul_f32 v[48:49], v[44:45], v[44:45] op_sel:[1,1] op_sel_hi:[1,0] neg_lo:[1,0]\n\tv_pk_mul_f32 v[50:51], v[46:47], v[46:47] op_sel:[1,1] op_sel_hi:[1,0] neg_lo:[1,0]\n\t" \
  "v_pk_fma_f32 v[48:49], v[44:45], v[44:45], v[48:49] op_sel:[0,0,0] op_sel_hi:[0,1,1]\n\tv_pk_fma_f32 v[50:51], v[46:47], v[46:47], v[50:51] op_sel:[0,0,0] op_sel_hi:[0,1,1]\n\t" \
  "v_and_b32 v102, 0x7fff7fff, v68\n\tv_cvt_pk_f16_f32 v88, v44, -v45\n\t" \
  "v_cvt_pk_f16_f32 v89, v46, -v47\n\tv_pk_mul_f32 v[52:53], v[48:49], v[44:45] op_sel:[1,1] op_sel_hi:[1,0] neg_lo:[1,0]\n\t" \
  "v_pk_mul_f32 v[54:55], v[50:51], v[46:47] op_sel:[1,1] op_sel_hi:[1,0] neg_lo:[1,0]\n\tv_and_b32 v103, 0x7fff7fff, v69\n\t" \
  "v_pk_fma_f32 v[52:53], v[48:49], v[44:45], v[52:53] op_sel:[0,0,0] op_sel_hi:[0,1,1]\n\tv_pk_fma_f32 v[54:55], v[50:51], v[46:47], v[54:55] op_sel:[0,0,0] op_sel_hi:[0,1,1]\n\t" \
  "v_pk_mul_f32 v[56:57], v[48:49], v[48:49] op_sel:[1,1] op_sel_hi:[1,0] neg_lo:[1,0]\n\tv_pk_mul_f32 v[58:59], v[50:51], v[50:51] op_sel:[1,1] op_sel_hi:[1,0] neg_lo:[1,0]\n\t" \
  "v_pk_max_u16 v102, v102, v103\n\tv_pk_fma_f32 v[56:57], v[48:49], v[48:49], v[56:57] op_sel:[0,0,0] op_sel_hi:[0,1,1]\n\t" \
  "v_pk_fma_f32 v[58:59], v[50:51], v[50:51], v[58:59] op_sel:[0,0,0] op_sel_hi:[0,1,1]\n\tv_cvt_pk_f16_f32 v90, v48, -v49\n\t" \
  "v_cvt_pk_f16_f32 v91, v50, -v51\n\tv_pk_max_u16 v102, v102, v102 op_sel:[1,0] op_sel_hi:[0,1]\n\t" \
  "v_pk_mul_f32 v[64:65], v[56:57], v[60:61] op_sel:[1,1] op_sel_hi:[1,0] neg_lo:[1,0]\n\tv_pk_mul_f32 v[66:67], v[58:59], v[62:63] op_sel:[1,1] op_sel_hi:[1,0] neg_lo:[1,0]\n\t" \
  "v_pk_fma_f32 v[64:65], v[56:57], v[60:61], v[64:65] op_sel:[0,0,0] op_sel_hi:[0,1,1]\n\tv_pk_fma_f32 v[66:67], v[58:59], v[62:63], v[66:67] op_sel:[0,0,0] op_sel_hi:[0,1,1]\n\t" \
  "v_max_u32_dpp v102, v102, v102 row_shr:1 row_mask:0xf bank_mask:0xf\n\tv_cvt_pk_f16_f32 v92, v52, -v53\n\t" \
  "v_cvt_pk_f16_f32 v93, v54, -v55\n\tv_pk_mul_f16 v136, v88, v74 op_sel:[1,1] op_sel_hi:[1,0] neg_lo:[1,0]\n\t" \
  "v_pk_mul_f16 v137, v89, v75 op_sel:[1,1] op_sel_hi:[1,0] neg_lo:[1,0]\n\tv_max_u32_dpp v102, v102, v102 row_shr:2 row_mask:0xf bank_mask:0xf\n\t" \
  "v_pk_fma_f16 v136, v88, v74, v136 op_sel:[0,0,0] op_sel_hi:[0,1,1]\n\tv_pk_fma_f16 v137, v89, v75, v137 op_sel:[0,0,0] op_sel_hi:[0,1,1]\n\t" \
  "v_pk_mul_f16 v138, v88, v78 op_sel:[1,1] op_sel_hi:[1,0] neg_lo:[1,0]\n\tv_pk_mul_f16 v139, v89, v79 op_sel:[1,1] op_sel_hi:[1,0] neg_lo:[1,0]\n\t" \
  "v_max_u32_dpp v102, v102, v102 row_shr:4 row_mask:0xf bank_mask:0xf\n\tv_pk_fma_f16 v138, v88, v78, v138 op_sel:[0,0,0] op_sel_hi:[0,1,1]\n\t" \
  "v_pk_fma_f16 v139, v89, v79, v139 op_sel:[0,0,0] op_sel_hi:[0,1,1]\n\tv_cvt_pk_f16_f32 v94, v64, -v65\n\t" \
  "v_cvt_pk_f16_f32 v95, v66, -v67\n\tv_max_u32_dpp v102, v102, v102 row_shr:8 row_mask:0xf bank_mask:0xf\n\t" \
  "v_pk_mul_f16 v140, v88, v82 op_sel:[1,1] op_sel_hi:[1,0] neg_lo:[1,0]\n\tv_pk_mul_f16 v141, v89, v83 op_sel:[1,1] op_sel_hi:[1,0] neg_lo:[1,0]\n\t" \
  "v_pk_fma_f16 v140, v88, v82, v140 op_sel:[0,0,0] op_sel_hi:[0,1,1]\n\tv_pk_fma_f16 v141, v89, v83, v141 op_sel:[0,0,0] op_sel_hi:[0,1,1]\n\t" \
  "v_max_u32_dpp v102, v102, v102 row_bcast:15 row_mask:0xa bank_mask:0xf\n\tv_pk_mul_f16 v142, v88, v86 op_sel:[1,1] op_sel_hi:[1,0] neg_lo:[1,0]\n\t" \
  "v_pk_mul_f16 v143, v89, v87 op_sel:[1,1] op_sel_hi:[1,0] neg_lo:[1,0]\n\tv_pk_fma_f16 v142, v88, v86, v142 op_sel:[0,0,0] op_sel_hi:[0,1,1]\n\t" \
  "v_pk_fma_f16 v143, v89, v87, v143 op_sel:[0,0,0] op_sel_hi:[0,1,1]\n\tv_max_u32_dpp v102, v102, v102 row_bcast:31 row_mask:0xc bank_mask:0xf\n\t" \
  "v_pk_mul_f16 v144, v90, v78 op_sel:[1,1] op_sel_hi:[1,0] neg_lo:[1,0]\n\tv_pk_mul_f16 v145, v91, v79 op_sel:[1,1] op_sel_hi:[1,0] neg_lo:[1,0]\n\t" \
  "v_pk_fma_f16 v144, v90, v78, v144 op_sel:[0,0,0] op_sel_hi:[0,1,1]\n\tv_pk_fma_f16 v145, v91, v79, v145 op_sel:[0,0,0] op_sel_hi:[0,1,1]\n\t" \
  "v_readlane_b32 %[e], v102, 63\n\tv_pk_mul_f16 v146, v90, v86 op_sel:[1,1] op_sel_hi:[1,0] neg_lo:[1,0]\n\t" \
  "v_pk_mul_f16 v147, v91, v87 op_sel:[1,1] op_sel_hi:[1,0] neg_lo:[1,0]\n\tv_pk_fma_f16 v146, v90, v86, v146 op_sel:[0,0,0] op_sel_hi:[0,1,1]\n\t" \
  "v_pk_fma_f16 v147, v91, v87, v147 op_sel:[0,0,0] op_sel_hi:[0,1,1]\n\tv_pk_mul_f16 v148, v92, v78 op_sel:[1,1] op_sel_hi:[1,0] neg_lo:[1,0]\n\t" \
  "v_pk_mul_f16 v149, v93, v79 op_sel:[1,1] op_sel_hi:[1,0] neg_lo:[1,0]\n\tv_pk_fma_f16 v148, v92, v78, v148 op_sel:[0,0,0] op_sel_hi:[0,1,1]\n\t" \
  "v_pk_fma_f16 v149, v93, v79, v149 op_sel:[0,0,0] op_sel_hi:[0,1,1]\n\tv_pk_mul_f16 v150, v92, v86 op_sel:[1,1] op_sel_hi:[1,0] neg_lo:[1,0]\n\t" \
  "v_pk_mul_f16 v151, v93, v87 op_sel:[1,1] op_sel_hi:[1,0] neg_lo:[1,0]\n\tv_pk_fma_f16 v150, v92, v86, v150 op_sel:[0,0,0] op_sel_hi:[0,1,1]\n\t" \
  "v_pk_fma_f16 v151, v93, v87, v151 op_sel:[0,0,0] op_sel_hi:[0,1,1]\n\tv_pk_mul_f16 v96, v94, v88 op_sel:[1,1] op_sel_hi:[1,0] neg_lo:[1,0]\n\t" \
  "v_pk_mul_f16 v97, v95, v89 op_sel:[1,1] op_sel_hi:[1,0] neg_lo:[1,0]\n\tv_pk_fma_f16 v96, v94, v88, v96 op_sel:[0,0,0] op_sel_hi:[0,1,1]\n\t" \
  "v_pk_fma_f16 v97, v95, v89, v97 op_sel:[0,0,0] op_sel_hi:[0,1,1]\n\tv_pk_mul_f16 v98, v94, v90 op_sel:[1,1] op_sel_hi:[1,0] neg_lo:[1,0]\n\t" \
  "v_pk_mul_f16 v99, v95, v91 op_sel:[1,1] op_sel_hi:[1,0] neg_lo:[1,0]\n\tv_pk_fma_f16 v98, v94, v90, v98 op_sel:[0,0,0] op_sel_hi:[0,1,1]\n\t" \
  "v_pk_fma_f16 v99, v95, v91, v99 op_sel:[0,0,0] op_sel_hi:[0,1,1]\n\tv_pk_mul_f16 v100, v94, v92 op_sel:[1,1] op_sel_hi:[1,0] neg_lo:[1,0]\n\t" \
  "v_pk_mul_f16 v101, v95, v93 op_sel:[1,1] op_sel_hi:[1,0] neg_lo:[1,0]\n\tv_pk_fma_f16 v100, v94, v92, v100 op_sel:[0,0,0] op_sel_hi:[0,1,1]\n\t" \
  "v_pk_fma_f16 v101, v95, v93, v101 op_sel:[0,0,0] op_sel_hi:[0,1,1]\n\ts_bfe_u32 %[e], %[e], 0x80007\n\t" \
  "s_max_u32 %[e], %[e], 8\n\ts_sub_u32 %[t], 260, %[e]\n\t" \
  "s_lshl_b32 %[t], %[t], 23\n\tds_write_addtid_b32 v136 offset:0\n\t" \
  "ds_write_addtid_b32 v137 offset:4608\n\tds_write_addtid_b32 v138 offset:288\n\t" \
  "ds_write_addtid_b32 v139 offset:4896\n\tds_write_addtid_b32 v140 offset:576\n\t" \
  "ds_write_addtid_b32 v141 offset:5184\n\tds_write_addtid_b32 v142 offset:864\n\t" \
  "ds_write_addtid_b32 v143 offset:5472\n\tds_write_addtid_b32 v144 offset:1152\n\t" \
  "ds_write_addtid_b32 v145 offset:5760\n\tds_write_addtid_b32 v146 offset:1440\n\t" \
  "ds_write_addtid_b32 v147 offset:6048\n\tds_write_addtid_b32 v148 offset:1728\n\t" \
  "ds_write_addtid_b32 v149 offset:6336\n\tds_write_addtid_b32 v150 offset:2016\n\t" \
  "ds_write_addtid_b32 v151 offset:6624\n\tds_write_addtid_b32 v94 offset:2304\n\t" \
  "ds_write_addtid_b32 v95 offset:6912\n\tds_write_addtid_b32 v96 offset:2592\n\t" \
  "ds_write_addtid_b32 v97 offset:7200\n\tds_write_addtid_b32 v98 offset:2880\n\t" \
  "ds_write_addtid_b32 v99 offset:7488\n\tds_write_addtid_b32 v100 offset:3168\n\t" \
  "ds_write_addtid_b32 v101 offset:7776\n\tds_read_b128 v[136:139], v71 offset:0\n\t" \
  "ds_read_b128 v[140:143], v71 offset:64\n\tds_read_b128 v[144:147], v71 offset:128\n\t" \
  "ds_read_b128 v[148:151], v71 offset:192\n\tds_read_b128 v[152:155], v71 offset:4608\n\t" \
  "ds_read_b128 v[156:159], v71 offset:4672\n\tds_read_b128 v[160:163], v71 offset:4736\n\t" \
  "ds_read_b128 v[164:167], v71 offset:4800\n\tv_pk_mul_f32 v[48:49], v[60:61], v[60:61] op_sel:[1,1] op_sel_hi:[1,0] neg_lo:[1,0]\n\t" \
  "v_pk_mul_f32 v[50:51], v[62:63], v[62:63] op_sel:[1,1] op_sel_hi:[1,0] neg_lo:[1,0]\n\tv_pk_fma_f32 v[48:49], v[60:61], v[60:61], v[48:49] op_sel:[0,0,0] op_sel_hi:[0,1,1]\n\t" \
  "v_pk_fma_f32 v[50:51], v[62:63], v[62:63], v[50:51] op_sel:[0,0,0] op_sel_hi:[0,1,1]\n\tv_cvt_pk_f16_f32 v44, v60, v61\n\t" \
  "v_cvt_pk_f16_f32 v45, v62, v63\n\tv_pk_mul_f32 v[52:53], v[48:49], v[60:61] op_sel:[1,1] op_sel_hi:[1,0] neg_lo:[1,0]\n\t" \
  "v_pk_mul_f32 v[54:55], v[50:51], v[62:63] op_sel:[1,1] op_sel_hi:[1,0] neg_lo:[1,0]\n\tv_pk_fma_f32 v[52:53], v[48:49], v[60:61], v[52:53] op_sel:[0,0,0] op_sel_hi:[0,1,1]\n\t" \
  "v_pk_fma_f32 v[54:55], v[50:51], v[62:63], v[54:55] op_sel:[0,0,0] op_sel_hi:[0,1,1]\n\tv_pk_mul_f32 v[40:41], v[48:49], v[48:49] op_sel:[1,1] op_sel_hi:[1,0] neg_lo:[1,0]\n\t" \
  "v_pk_mul_f32 v[42:43], v[50:51], v[50:51] op_sel:[1,1] op_sel_hi:[1,0] neg_lo:[1,0]\n\tv_pk_fma_f32 v[40:41], v[48:49], v[48:49], v[40:41] op_sel:[0,0,0] op_sel_hi:[0,1,1]\n\t" \
  "v_pk_fma_f32 v[42:43], v[50:51], v[50:51], v[42:43] op_sel:[0,0,0] op_sel_hi:[0,1,1]\n\tv_cvt_pk_f16_f32 v46, v48, v49\n\t" \
  "v_cvt_pk_f16_f32 v47, v50, v51\n\tv_cvt_pk_f16_f32 v56, v52, v53\n\t" \
  "v_cvt_pk_f16_f32 v57, v54, v55\n\tv_lshlrev_b32 v64, 16, v68\n\t" \
  "v_lshlrev_b32 v65, 16, v69\n\tv_and_b32 v66, 0xffff0000, v68\n\t" \
  "v_and_b32 v67, 0xffff0000, v69\n\tv_mul_f32 v64, %[t], v64\n\t" \
  "v_mul_f32 v65, %[t], v65\n\tv_mul_f32 v66, %[t], v66\n\t" \
  "v_mul_f32 v67, %[t], v67\n\tv_cvt_pk_f16_f32 v48, v64, v65\n\t" \
  "v_cvt_pk_f16_f32 v49, v66, v67\n\tv_pk_mul_f32 v[68:69], v[64:65], v[40:41] op_sel:[1,1] op_sel_hi:[1,0] neg_lo:[1,0]\n\t" \
  "v_pk_mul_f32 v[70:71], v[66:67], v[42:43] op_sel:[1,1] op_sel_hi:[1,0] neg_lo:[1,0]\n\tv_pk_fma_f32 v[68:69], v[64:65], v[40:41], v[68:69] op_sel:[0,0,0] op_sel_hi:[0,1,1]\n\t" \
  "v_pk_fma_f32 v[70:71], v[66:67], v[42:43], v[70:71] op_sel:[0,0,0] op_sel_hi:[0,1,1]\n\tv_pk_mul_f16 v50, v48, v44 op_sel:[1,1] op_sel_hi:[1,0] neg_lo:[1,0]\n\t" \
  "v_pk_mul_f16 v51, v49, v45 op_sel:[1,1] op_sel_hi:[1,0] neg_lo:[1,0]\n\tv_pk_fma_f16 v50, v48, v44, v50 op_sel:[0,0,0] op_sel_hi:[0,1,1]\n\t" \
  "v_pk_fma_f16 v51, v49, v45, v51 op_sel:[0,0,0] op_sel_hi:[0,1,1]\n\tv_pk_mul_f16 v52, v48, v46 op_sel:[1,1] op_sel_hi:[1,0] neg_lo:[1,0]\n\t" \
  "v_pk_mul_f16 v53, v49, v47 op_sel:[1,1] op_sel_hi:[1,0] neg_lo:[1,0]\n\tv_pk_fma_f16 v52, v48, v46, v52 op_sel:[0,0,0] op_sel_hi:[0,1,1]\n\t" \
  "v_pk_fma_f16 v53, v49, v47, v53 op_sel:[0,0,0] op_sel_hi:[0,1,1]\n\tv_pk_mul_f16 v54, v48, v56 op_sel:[1,1] op_sel_hi:[1,0] neg_lo:[1,0]\n\t" \
  "v_pk_mul_f16 v55, v49, v57 op_sel:[1,1] op_sel_hi:[1,0] neg_lo:[1,0]\n\tv_pk_fma_f16 v54, v48, v56, v54 op_sel:[0,0,0] op_sel_hi:[0,1,1]\n\t" \
  "v_pk_fma_f16 v55, v49, v57, v55 op_sel:[0,0,0] op_sel_hi:[0,1,1]\n\tds_write_addtid_b32 v48 offset:0\n\t" \
  "ds_write_addtid_b32 v49 offset:4608\n\tds_write_addtid_b32 v50 offset:288\n\t" \
  "ds_write_addtid_b32 v51 offset:4896\n\tds_write_addtid_b32 v52 offset:576\n\t" \
  "ds_write_addtid_b32 v53 offset:5184\n\tds_write_addtid_b32 v54 offset:864\n\t" \
  "ds_write_addtid_b32 v55 offset:5472\n\tv_cvt_pk_f16_f32 v48, v68, v69\n\t" \
  "v_cvt_pk_f16_f32 v49, v70, v71\n\tv_pk_mul_f32 v[64:65], v[68:69], v[40:41] op_sel:[1,1] op_sel_hi:[1,0] neg_lo:[1,0]\n\t" \
  "v_pk_mul_f32 v[66:67], v[70:71], v[42:43] op_sel:[1,1] op_sel_hi:[1,0] neg_lo:[1,0]\n\tv_pk_fma_f32 v[64:65], v[68:69], v[40:41], v[64:65] op_sel:[0,0,0] op_sel_hi:[0,1,1]\n\t" \
  "v_pk_fma_f32 v[66:67], v[70:71], v[42:43], v[66:67] op_sel:[0,0,0] op_sel_hi:[0,1,1]\n\tv_pk_mul_f16 v50, v48, v44 op_sel:[1,1] op_sel_hi:[1,0] neg_lo:[1,0]\n\t" \
  "v_pk_mul_f16 v51, v49, v45 op_sel:[1,1] op_sel_hi:[1,0] neg_lo:[1,0]\n\tv_pk_fma_f16 v50, v48, v44, v50 op_sel:[0,0,0] op_sel_hi:[0,1,1]\n\t" \
  "v_pk_fma_f16 v51, v49, v45, v51 op_sel:[0,0,0] op_sel_hi:[0,1,1]\n\tv_pk_mul_f16 v52, v48, v46 op_sel:[1,1] op_sel_hi:[1,0] neg_lo:[1,0]\n\t" \
  "v_pk_mul_f16 v53, v49, v47 op_sel:[1,1] op_sel_hi:[1,0] neg_lo:[1,0]\n\tv_pk_fma_f16 v52, v48, v46, v52 op_sel:[0,0,0] op_sel_hi:[0,1,1]\n\t" \
  "v_pk_fma_f16 v53, v49, v47, v53 op_sel:[0,0,0] op_sel_hi:[0,1,1]\n\tv_pk_mul_f16 v54, v48, v56 op_sel:[1,1] op_sel_hi:[1,0] neg_lo:[1,0]\n\t" \
  "v_pk_mul_f16 v55, v49, v57 op_sel:[1,1] op_sel_hi:[1,0] neg_lo:[1,0]\n\tv_pk_fma_f16 v54, v48, v56, v54 op_sel:[0,0,0] op_sel_hi:[0,1,1]\n\t" \
  "v_pk_fma_f16 v55, v49, v57, v55 op_sel:[0,0,0] op_sel_hi:[0,1,1]\n\tds_write_addtid_b32 v48 offset:1152\n\t" \
  "ds_write_addtid_b32 v49 offset:5760\n\tds_write_addtid_b32 v50 offset:1440\n\t" \
  "ds_write_addtid_b32 v51 offset:6048\n\tds_write_addtid_b32 v52 offset:1728\n\t" \
  "ds_write_addtid_b32 v53 offset:6336\n\tds_write_addtid_b32 v54 offset:2016\n\t" \
  "ds_write_addtid_b32 v55 offset:6624\n\tv_cvt_pk_f16_f32 v48, v64, v65\n\t" \
  "v_cvt_pk_f16_f32 v49, v66, v67\n\tv_pk_mul_f32 v[68:69], v[64:65], v[40:41] op_sel:[1,1] op_sel_hi:[1,0] neg_lo:[1,0]\n\t" \
  "v_pk_mul_f32 v[70:71], v[66:67], v[42:43] op_sel:[1,1] op_sel_hi:[1,0] neg_lo:[1,0]\n\tv_pk_fma_f32 v[68:69], v[64:65], v[40:41], v[68:69] op_sel:[0,0,0] op_sel_hi:[0,1,1]\n\t" \
  "v_pk_fma_f32 v[70:71], v[66:67], v[42:43], v[70:71] op_sel:[0,0,0] op_sel_hi:[0,1,1]\n\tv_pk_mul_f16 v50, v48, v44 op_sel:[1,1] op_sel_hi:[1,0] neg_lo:[1,0]\n\t" \
  "v_pk_mul_f16 v51, v49, v45 op_sel:[1,1] op_sel_hi:[1,0] neg_lo:[1,0]\n\tv_pk_fma_f16 v50, v48, v44, v50 op_sel:[0,0,0] op_sel_hi:[0,1,1]\n\t" \
  "v_pk_fma_f16 v51, v49, v45, v51 op_sel:[0,0,0] op_sel_hi:[0,1,1]\n\tv_pk_mul_f16 v52, v48, v46 op_sel:[1,1] op_sel_hi:[1,0] neg_lo:[1,0]\n\t" \
  "v_pk_mul_f16 v53, v49, v47 op_sel:[1,1] op_sel_hi:[1,0] neg_lo:[1,0]\n\tv_pk_fma_f16 v52, v48, v46, v52 op_sel:[0,0,0] op_sel_hi:[0,1,1]\n\t" \
  "v_pk_fma_f16 v53, v49, v47, v53 op_sel:[0,0,0] op_sel_hi:[0,1,1]\n\tv_pk_mul_f16 v54, v48, v56 op_sel:[1,1] op_sel_hi:[1,0] neg_lo:[1,0]\n\t" \
  "v_pk_mul_f16 v55, v49, v57 op_sel:[1,1] op_sel_hi:[1,0] neg_lo:[1,0]\n\tv_pk_fma_f16 v54, v48, v56, v54 op_sel:[0,0,0] op_sel_hi:[0,1,1]\n\t" \
  "v_pk_fma_f16 v55, v49, v57, v55 op_sel:[0,0,0] op_sel_hi:[0,1,1]\n\tds_write_addtid_b32 v48 offset:2304\n\t" \
  "ds_write_addtid_b32 v49 offset:6912\n\tds_write_addtid_b32 v50 offset:2592\n\t" \
  "ds_write_addtid_b32 v51 offset:7200\n\tds_write_addtid_b32 v52 offset:2880\n\t" \
  "ds_write_addtid_b32 v53 offset:7488\n\tds_write_addtid_b32 v54 offset:3168\n\t" \
  "ds_write_addtid_b32 v55 offset:7776\n\tv_cvt_pk_f16_f32 v48, v68, v69\n\t" \
  "v_cvt_pk_f16_f32 v49, v70, v71\n\tv_pk_mul_f32 v[64:65], v[68:69], v[40:41] op_sel:[1,1] op_sel_hi:[1,0] neg_lo:[1,0]\n\t" \
  "v_pk_mul_f32 v[66:67], v[70:71], v[42:43] op_sel:[1,1] op_sel_hi:[1,0] neg_lo:[1,0]\n\tv_pk_fma_f32 v[64:65], v[68:69], v[40:41], v[64:65] op_sel:[0,0,0] op_sel_hi:[0,1,1]\n\t" \
  "v_pk_fma_f32 v[66:67], v[70:71], v[42:43], v[66:67] op_sel:[0,0,0] op_sel_hi:[0,1,1]\n\tv_pk_mul_f16 v50, v48, v44 op_sel:[1,1] op_sel_hi:[1,0] neg_lo:[1,0]\n\t" \
  "v_pk_mul_f16 v51, v49, v45 op_sel:[1,1] op_sel_hi:[1,0] neg_lo:[1,0]\n\tv_pk_fma_f16 v50, v48, v44, v50 op_sel:[0,0,0] op_sel_hi:[0,1,1]\n\t" \
  "v_pk_fma_f16 v51, v49, v45, v51 op_sel:[0,0,0] op_sel_hi:[0,1,1]\n\tv_pk_mul_f16 v52, v48, v46 op_sel:[1,1] op_sel_hi:[1,0] neg_lo:[1,0]\n\t" \
  "v_pk_mul_f16 v53, v49, v47 op_sel:[1,1] op_sel_hi:[1,0] neg_lo:[1,0]\n\tv_pk_fma_f16 v52, v48, v46, v52 op_sel:[0,0,0] op_sel_hi:[0,1,1]\n\t" \
  "v_pk_fma_f16 v53, v49, v47, v53 op_sel:[0,0,0] op_sel_hi:[0,1,1]\n\tv_pk_mul_f16 v54, v48, v56 op_sel:[1,1] op_sel_hi:[1,0] neg_lo:[1,0]\n\t" \
  "v_pk_mul_f16 v55, v49, v57 op_sel:[1,1] op_sel_hi:[1,0] neg_lo:[1,0]\n\tv_pk_fma_f16 v54, v48, v56, v54 op_sel:[0,0,0] op_sel_hi:[0,1,1]\n\t" \
  "v_pk_fma_f16 v55, v49, v57, v55 op_sel:[0,0,0] op_sel_hi:[0,1,1]\n\tds_write_addtid_b32 v48 offset:3456\n\t" \
  "ds_write_addtid_b32 v49 offset:8064\n\tds_write_addtid_b32 v50 offset:3744\n\t" \
  "ds_write_addtid_b32 v51 offset:8352\n\tds_write_addtid_b32 v52 offset:4032\n\t" \
  "ds_write_addtid_b32 v53 offset:8640\n\tds_write_addtid_b32 v54 offset:4320\n\t" \
  "ds_write_addtid_b32 v55 offset:8928\n\tds_read_b128 v[72:75], v27 offset:0\n\t" \
  "ds_read_b128 v[76:79], v27 offset:64\n\tds_read_b128 v[80:83], v27 offset:128\n\t" \
  "ds_read_b128 v[84:87], v27 offset:192\n\tds_read_b128 v[88:91], v27 offset:4608\n\t" \
  "ds_read_b128 v[92:95], v27 offset:4672\n\tds_read_b128 v[96:99], v27 offset:4736\n\t" \
  "ds_read_b128 v[100:103], v27 offset:4800\n\tv_cvt_pk_f16_f32 v48, v64, v65\n\t" \
  "v_cvt_pk_f16_f32 v49, v66, v67\n\tv_pk_mul_f32 v[68:69], v[64:65], v[40:41] op_sel:[1,1] op_sel_hi:[1,0] neg_lo:[1,0]\n\t" \
  "v_pk_mul_f32 v[70:71], v[66:67], v[42:43] op_sel:[1,1] op_sel_hi:[1,0] neg_lo:[1,0]\n\tv_pk_fma_f32 v[68:69], v[64:65], v[40:41], v[68:69] op_sel:[0,0,0] op_sel_hi:[0,1,1]\n\t" \
  "v_pk_fma_f32 v[70:71], v[66:67], v[42:43], v[70:71] op_sel:[0,0,0] op_sel_hi:[0,1,1]\n\tv_pk_mul_f16 v50, v48, v44 op_sel:[1,1] op_sel_hi:[1,0] neg_lo:[1,0]\n\t" \
  "v_pk_mul_f16 v51, v49, v45 op_sel:[1,1] op_sel_hi:[1,0] neg_lo:[1,0]\n\tv_pk_fma_f16 v50, v48, v44, v50 op_sel:[0,0,0] op_sel_hi:[0,1,1]\n\t" \
  "v_pk_fma_f16 v51, v49, v45, v51 op_sel:[0,0,0] op_sel_hi:[0,1,1]\n\tv_pk_mul_f16 v52, v48, v46 op_sel:[1,1] op_sel_hi:[1,0] neg_lo:[1,0]\n\t" \
  "v_pk_mul_f16 v53, v49, v47 op_sel:[1,1] op_sel_hi:[1,0] neg_lo:[1,0]\n\tv_pk_fma_f16 v52, v48, v46, v52 op_sel:[0,0,0] op_sel_hi:[0,1,1]\n\t" \
  "v_pk_fma_f16 v53, v49, v47, v53 op_sel:[0,0,0] op_sel_hi:[0,1,1]\n\tv_pk_mul_f16 v54, v48, v56 op_sel:[1,1] op_sel_hi:[1,0] neg_lo:[1,0]\n\t" \
  "v_pk_mul_f16 v55, v49, v57 op_sel:[1,1] op_sel_hi:[1,0] neg_lo:[1,0]\n\tv_pk_fma_f16 v54, v48, v56, v54 op_sel:[0,0,0] op_sel_hi:[0,1,1]\n\t" \
  "v_pk_fma_f16 v55, v49, v57, v55 op_sel:[0,0,0] op_sel_hi:[0,1,1]\n\ts_waitcnt lgkmcnt(0)\n\t" \
  "v_mfma_f32_16x16x32_f16 v[60:63], v[104:107], v[72:75], 0\n\tv_mfma_f32_16x16x32_f16 v[36:39], v[72:75], v[136:139], 0\n\t" \
  "ds_write_addtid_b32 v48 offset:0\n\tds_write_addtid_b32 v49 offset:4608\n\t" \
  "ds_write_addtid_b32 v50 offset:288\n\tds_write_addtid_b32 v51 offset:4896\n\t" \
  "v_mfma_f32_16x16x32_f16 v[60:63], v[108:111], v[76:79], v[60:63]\n\tds_write_addtid_b32 v52 offset:576\n\t" \
  "ds_write_addtid_b32 v53 offset:5184\n\tds_write_addtid_b32 v54 offset:864\n\t" \
  "ds_write_addtid_b32 v55 offset:5472\n\tv_mfma_f32_16x16x32_f16 v[36:39], v[76:79], v[140:143], v[36:39]\n\t" \
  "v_cvt_pk_f16_f32 v48, v68, v69\n\tv_cvt_pk_f16_f32 v49, v70, v71\n\t" \
  "v_pk_mul_f32 v[64:65], v[68:69], v[40:41] op_sel:[1,1] op_sel_hi:[1,0] neg_lo:[1,0]\n\tv_pk_mul_f32 v[66:67], v[70:71], v[42:43] op_sel:[1,1] op_sel_hi:[1,0] neg_lo:[1,0]\n\t" \
  "v_pk_fma_f32 v[64:65], v[68:69], v[40:41], v[64:65] op_sel:[0,0,0] op_sel_hi:[0,1,1]\n\tv_pk_fma_f32 v[66:67], v[70:71], v[42:43], v[66:67] op_sel:[0,0,0] op_sel_hi:[0,1,1]\n\t" \
  "v_pk_mul_f16 v50, v48, v44 op_sel:[1,1] op_sel_hi:[1,0] neg_lo:[1,0]\n\tv_pk_mul_f16 v51, v49, v45 op_sel:[1,1] op_sel_hi:[1,0] neg_lo:[1,0]\n\t" \
  "v_mfma_f32_16x16x32_f16 v[60:63], v[112:115], v[80:83], v[60:63]\n\tv_pk_fma_f16 v50, v48, v44, v50 op_sel:[0,0,0] op_sel_hi:[0,1,1]\n\t" \
  "v_pk_fma_f16 v51, v49, v45, v51 op_sel:[0,0,0] op_sel_hi:[0,1,1]\n\tv_pk_mul_f16 v52, v48, v46 op_sel:[1,1] op_sel_hi:[1,0] neg_lo:[1,0]\n\t" \
  "v_pk_mul_f16 v53, v49, v47 op_sel:[1,1] op_sel_hi:[1,0] neg_lo:[1,0]\n\tv_pk_fma_f16 v52, v48, v46, v52 op_sel:[0,0,0] op_sel_hi:[0,1,1]\n\t" \
  "v_pk_fma_f16 v53, v49, v47, v53 op_sel:[0,0,0] op_sel_hi:[0,1,1]\n\tv_pk_mul_f16 v54, v48, v56 op_sel:[1,1] op_sel_hi:[1,0] neg_lo:[1,0]\n\t" \
  "v_pk_mul_f16 v55, v49, v57 op_sel:[1,1] op_sel_hi:[1,0] neg_lo:[1,0]\n\tv_pk_fma_f16 v54, v48, v56, v54 op_sel:[0,0,0] op_sel_hi:[0,1,1]\n\t" \
  "v_pk_fma_f16 v55, v49, v57, v55 op_sel:[0,0,0] op_sel_hi:[0,1,1]\n\tv_mfma_f32_16x16x32_f16 v[36:39], v[80:83], v[144:147], v[36:39]\n\t" \
  "ds_write_addtid_b32 v48 offset:1152\n\tds_write_addtid_b32 v49 offset:5760\n\t" \
  "ds_write_addtid_b32 v50 offset:1440\n\tds_write_addtid_b32 v51 offset:6048\n\t" \
  "v_mfma_f32_16x16x32_f16 v[60:63], v[116:119], v[84:87], v[60:63]\n\tds_write_addtid_b32 v52 offset:1728\n\t" \
  "ds_write_addtid_b32 v53 offset:6336\n\tds_write_addtid_b32 v54 offset:2016\n\t" \
  "ds_write_addtid_b32 v55 offset:6624\n\tv_mfma_f32_16x16x32_f16 v[36:39], v[84:87], v[148:151], v[36:39]\n\t" \
  "v_cvt_pk_f16_f32 v48, v64, v65\n\tv_cvt_pk_f16_f32 v49, v66, v67\n\t" \
  "v_pk_mul_f32 v[68:69], v[64:65], v[40:41] op_sel:[1,1] op_sel_hi:[1,0] neg_lo:[1,0]\n\tv_pk_mul_f32 v[70:71], v[66:67], v[42:43] op_sel:[1,1] op_sel_hi:[1,0] neg_lo:[1,0]\n\t" \
  "v_pk_fma_f32 v[68:69], v[64:65], v[40:41], v[68:69] op_sel:[0,0,0] op_sel_hi:[0,1,1]\n\tv_pk_fma_f32 v[70:71], v[66:67], v[42:43], v[70:71] op_sel:[0,0,0] op_sel_hi:[0,1,1]\n\t" \
  "v_pk_mul_f16 v50, v48, v44 op_sel:[1,1] op_sel_hi:[1,0] neg_lo:[1,0]\n\tv_pk_mul_f16 v51, v49, v45 op_sel:[1,1] op_sel_hi:[1,0] neg_lo:[1,0]\n\t" \
  "v_mfma_f32_16x16x32_f16 v[60:63], v[120:123], v[88:91], v[60:63]\n\tv_pk_fma_f16 v50, v48, v44, v50 op_sel:[0,0,0] op_sel_hi:[0,1,1]\n\t" \
  "v_pk_fma_f16 v51, v49, v45, v51 op_sel:[0,0,0] op_sel_hi:[0,1,1]\n\tv_pk_mul_f16 v52, v48, v46 op_sel:[1,1] op_sel_hi:[1,0] neg_lo:[1,0]\n\t" \
  "v_pk_mul_f16 v53, v49, v47 op_sel:[1,1] op_sel_hi:[1,0] neg_lo:[1,0]\n\tv_pk_fma_f16 v52, v48, v46, v52 op_sel:[0,0,0] op_sel_hi:[0,1,1]\n\t" \
  "v_pk_fma_f16 v53, v49, v47, v53 op_sel:[0,0,0] op_sel_hi:[0,1,1]\n\tv_pk_mul_f16 v54, v48, v56 op_sel:[1,1] op_sel_hi:[1,0] neg_lo:[1,0]\n\t" \
  "v_pk_mul_f16 v55, v49, v57 op_sel:[1,1] op_sel_hi:[1,0] neg_lo:[1,0]\n\tv_pk_fma_f16 v54, v48, v56, v54 op_sel:[0,0,0] op_sel_hi:[0,1,1]\n\t" \
  "v_pk_fma_f16 v55, v49, v57, v55 op_sel:[0,0,0] op_sel_hi:[0,1,1]\n\tv_mfma_f32_16x16x32_f16 v[36:39], v[88:91], v[152:155], v[36:39]\n\t" \
  "ds_write_addtid_b32 v48 offset:2304\n\tds_write_addtid_b32 v49 offset:6912\n\t" \
  "ds_write_addtid_b32 v50 offset:2592\n\tds_write_addtid_b32 v51 offset:7200\n\t" \
  "v_mfma_f32_16x16x32_f16 v[60:63], v[124:127], v[92:95], v[60:63]\n\tds_write_addtid_b32 v52 offset:2880\n\t" \
  "ds_write_addtid_b32 v53 offset:7488\n\tds_write_addtid_b32 v54 offset:3168\n\t" \
  "ds_write_addtid_b32 v55 offset:7776\n\tv_mfma_f32_16x16x32_f16 v[36:39], v[92:95], v[156:159], v[36:39]\n\t" \
  "v_cvt_pk_f16_f32 v48, v68, v69\n\tv_cvt_pk_f16_f32 v49, v70, v71\n\t" \
  "v_pk_mul_f16 v50, v48, v44 op_sel:[1,1] op_sel_hi:[1,0] neg_lo:[1,0]\n\tv_pk_mul_f16 v51, v49, v45 op_sel:[1,1] op_sel_hi:[1,0] neg_lo:[1,0]\n\t" \
  "v_pk_fma_f16 v50, v48, v44, v50 op_sel:[0,0,0] op_sel_hi:[0,1,1]\n\tv_pk_fma_f16 v51, v49, v45, v51 op_sel:[0,0,0] op_sel_hi:[0,1,1]\n\t" \
  "v_pk_mul_f16 v52, v48, v46 op_sel:[1,1] op_sel_hi:[1,0] neg_lo:[1,0]\n\tv_pk_mul_f16 v53, v49, v47 op_sel:[1,1] op_sel_hi:[1,0] neg_lo:[1,0]\n\t" \
  "v_mfma_f32_16x16x32_f16 v[60:63], v[128:131], v[96:99], v[60:63]\n\tv_pk_fma_f16 v52, v48, v46, v52 op_sel:[0,0,0] op_sel_hi:[0,1,1]\n\t" \
  "v_pk_fma_f16 v53, v49, v47, v53 op_sel:[0,0,0] op_sel_hi:[0,1,1]\n\tv_pk_mul_f16 v54, v48, v56 op_sel:[1,1] op_sel_hi:[1,0] neg_lo:[1,0]\n\t" \
  "v_pk_mul_f16 v55, v49, v57 op_sel:[1,1] op_sel_hi:[1,0] neg_lo:[1,0]\n\tv_pk_fma_f16 v54, v48, v56, v54 op_sel:[0,0,0] op_sel_hi:[0,1,1]\n\t" \
  "v_pk_fma_f16 v55, v49, v57, v55 op_sel:[0,0,0] op_sel_hi:[0,1,1]\n\tv_mfma_f32_16x16x32_f16 v[36:39], v[96:99], v[160:163], v[36:39]\n\t" \
  "ds_write_addtid_b32 v48 offset:3456\n\tds_write_addtid_b32 v49 offset:8064\n\t" \
  "ds_write_addtid_b32 v50 offset:3744\n\tds_write_addtid_b32 v51 offset:8352\n\t" \
  "v_mfma_f32_16x16x32_f16 v[60:63], v[132:135], v[100:103], v[60:63]\n\tds_write_addtid_b32 v52 offset:4032\n\t" \
  "ds_write_addtid_b32 v53 offset:8640\n\tds_write_addtid_b32 v54 offset:4320\n\t" \
  "ds_write_addtid_b32 v55 offset:8928\n\tv_mfma_f32_16x16x32_f16 v[36:39], v[100:103], v[164:167], v[36:39]\n\t" \
  "ds_read_b128 v[72:75], v27 offset:0\n\tds_read_b128 v[76:79], v27 offset:64\n\t" \
  "ds_read_b128 v[80:83], v27 offset:128\n\tds_read_b128 v[84:87], v27 offset:192\n\t" \
  "ds_read_b128 v[88:91], v27 offset:4608\n\tds_read_b128 v[92:95], v27 offset:4672\n\t" \
  "ds_read_b128 v[96:99], v27 offset:4736\n\tds_read_b128 v[100:103], v27 offset:4800\n\t" \
  "v_add_u32_sdwa v56, %[w], v16 dst_sel:DWORD dst_unused:UNUSED_PAD src0_sel:DWORD src1_sel:WORD_0\n\t" \
  "v_add_u32_sdwa v57, %[w], v16 dst_sel:DWORD dst_unused:UNUSED_PAD src0_sel:DWORD src1_sel:WORD_1\n\tv_add_u32_sdwa v58, %[w], v17 dst_sel:DWORD dst_unused:UNUSED_PAD src0_sel:DWORD src1_sel:WORD_0\n\t" \
  "v_add_u32_sdwa v59, %[w], v17 dst_sel:DWORD dst_unused:UNUSED_PAD src0_sel:DWORD src1_sel:WORD_1\n\tv_mov_b32 v52, 0\n\t" \
  "v_mov_b32 v53, 0\n\tv_mov_b32 v54, 0\n\t" \
  "v_mov_b32 v55, 0\n\tv_cvt_pk_f16_f32 v42, v36, v37\n\t" \
  "v_cvt_pk_f16_f32 v43, v38, v39\n\tv_cvt_pk_f16_f32 v40, v60, v61\n\t" \
  "v_cvt_pk_f16_f32 v41, v62, v63\n\tv_alignbit_b32 v44, v43, v42, 16\n\t" \
  "s_waitcnt lgkmcnt(7)\n\tv_mfma_f32_16x16x32_f16 v[64:67], v[104:107], v[72:75], 0\n\t" \
  "v_mfma_f32_16x16x32_f16 v[68:71], v[72:75], v[136:139], 0\n\ts_waitcnt lgkmcnt(6)\n\t" \
  "v_mfma_f32_16x16x32_f16 v[64:67], v[108:111], v[76:79], v[64:67]\n\tds_write_b128 v58, v[52:55]\n\t" \
  "v_mfma_f32_16x16x32_f16 v[68:71], v[76:79], v[140:143], v[68:71]\n\tds_write2_b32 v56, v40, v41 offset0:64 offset1:65\n\t" \
  "s_waitcnt lgkmcnt(7)\n\tv_mfma_f32_16x16x32_f16 v[64:67], v[112:115], v[80:83], v[64:67]\n\t" \
  "ds_write2_b32 v57, v40, v41 offset0:64 offset1:65\n\tv_mfma_f32_16x16x32_f16 v[68:71], v[80:83], v[144:147], v[68:71]\n\t" \
  "ds_write_b64 v59, v[42:43] offset:16\n\ts_waitcnt lgkmcnt(8)\n\t" \
  "v_mfma_f32_16x16x32_f16 v[64:67], v[116:119], v[84:87], v[64:67]\n\tds_write_b32 v59, v42 offset:1164\n\t" \
  "v_mfma_f32_16x16x32_f16 v[68:71], v[84:87], v[148:151], v[68:71]\n\tds_write_b32 v59, v43 offset:1168\n\t" \
  "s_waitcnt lgkmcnt(9)\n\tv_mfma_f32_16x16x32_f16 v[64:67], v[120:123], v[88:91], v[64:67]\n\t" \
  "ds_write_b16 v59, v42 offset:590\n\tv_mfma_f32_16x16x32_f16 v[68:71], v[88:91], v[152:155], v[68:71]\n\t" \
  "ds_write_b32 v59, v44 offset:592\n\ts_waitcnt lgkmcnt(10)\n\t" \
  "v_mfma_f32_16x16x32_f16 v[64:67], v[124:127], v[92:95], v[64:67]\n\tds_write_b16_d16_hi v59, v43 offset:596\n\t" \
  "v_mfma_f32_16x16x32_f16 v[68:71], v[92:95], v[156:159], v[68:71]\n\tds_write_b16 v59, v42 offset:1738\n\t" \
  "s_waitcnt lgkmcnt(11)\n\tv_mfma_f32_16x16x32_f16 v[64:67], v[128:131], v[96:99], v[64:67]\n\t" \
  "ds_write_b32 v59, v44 offset:1740\n\tv_mfma_f32_16x16x32_f16 v[68:71], v[96:99], v[160:163], v[68:71]\n\t" \
  "ds_write_b16_d16_hi v59, v43 offset:1744\n\ts_waitcnt lgkmcnt(12)\n\t" \
  "v_mfma_f32_16x16x32_f16 v[68:71], v[100:103], v[164:167], v[68:71]\n\tv_mfma_f32_16x16x32_f16 v[64:67], v[132:135], v[100:103], v[64:67]\n\t" \
  "s_nop 4\n\tv_cvt_pk_f16_f32 v48, v68, v69\n\t" \
  "v_cvt_pk_f16_f32 v49, v70, v71\n\ts_nop 1\n\t" \
  "v_cvt_pk_f16_f32 v46, v64, v65\n\tv_cvt_pk_f16_f32 v47, v66, v67\n\t" \
  "v_alignbit_b32 v50, v49, v48, 16\n\tds_write2_b32 v56, v46, v47 offset0:0 offset1:1\n\t" \
  "ds_write2_b32 v57, v46, v47 offset0:0 offset1:1\n\tds_write_b64 v59, v[48:49] offset:0\n\t" \
  "ds_write_b32 v59, v48 offset:1148\n\tds_write_b32 v59, v49 offset:1152\n\t" \
  "ds_write_b16 v59, v48 offset:574\n\tds_write_b32 v59, v50 offset:576\n\t" \
  "ds_write_b16_d16_hi v59, v49 offset:580\n\tds_write_b16 v59, v48 offset:1722\n\t" \
  "ds_write_b32 v59, v50 offset:1724\n\tds_write_b16_d16_hi v59, v49 offset:1728\n\t"
#define GEN_CLOBBER "v36", "v37", "v38", "v39", "v40", "v42", "v43", "v44", "v45", "v47", "v49", "v50", "v51", "v52", "v53", "v54", "v55", "v56", "v57", "v58", "v59", "v60", "v61", "v62", "v63", "v64", "v65", "v66", "v67", "v72", "v73", "v74", "v75", "v76", "v77", "v78", "v79", "v80", "v81", "v82", "v83", "v84", "v85", "v86", "v87", "v88", "v89", "v90", "v91", "v92", "v93", "v94", "v95", "v98", "v99", "v100", "v101", "v102", "v103", "v104", "v105", "v106", "v107", "v108", "v109", "v110", "v111", "v112", "v113", "v114", "v115", "v116", "v117", "v118", "v119", "v120", "v121", "v122", "v123", "v124", "v125", "v126", "v127", "v128", "v129", "v130", "v131", "v132", "v133", "v134", "v135", "v136", "v137", "v138", "v139", "v140", "v141", "v142", "v143", "v144", "v145", "v146", "v147", "v148", "v149", "v150", "v151", "v152", "v153", "v154", "v155", "v156", "v157", "v158", "v159", "v160", "v161", "v162", "v163", "v164", "v165", "v166", "v167"
#define G_CLOBBER "v8", "v9", "v10", "v11", "v12", "v13", "v14", "v15", "v16", "v17", "v18", "v19", "v20", "v21", "v22", "v23", "v24", "v25", "v26", "v27", "v28", "v29", "v30", "v32", "v33", "v34", "v35", "v36", "v37", "v38", "v39", "v40", "v41", "v42", "v43", "v44", "v45", "v46", "v47", "v48", "v49", "v50", "v51", "v52", "v53", "v54", "v55", "v56", "v57", "v58", "v59", "v60", "v61", "v62", "v63", "v64", "v65", "v66", "v67", "v68", "v69", "v70", "v71", "v72", "v73", "v74", "v75", "v76", "v77", "v78", "v79", "v80", "v81", "v82", "v83", "v84", "v85", "v86", "v87", "v88", "v89", "v90", "v91", "v92", "v93", "v94", "v95", "v96", "v97", "v98", "v99", "v100", "v101", "v102", "v103", "v104", "v105", "v106", "v107", "v108", "v109", "v110", "v111", "v112", "v113", "v114", "v115", "v116", "v117", "v118", "v119", "v120", "v121", "v122", "v123", "v124", "v125", "v126", "v127"
#define D_CLOBBER "v8", "v9", "v10", "v11", "v12", "v13", "v14", "v15", "v16", "v17", "v18", "v19", "v20", "v21", "v22", "v23", "v24", "v25", "v26", "v27", "v28", "v29", "v30", "v31"
__device__ __forceinline__ f32x2 pkfma(f32x2 a, f32x2 b, f32x2 c) {
  f32x2 r;
  asm("v_pk_fma_f32 %0, %1, %2, %3" : "=v"(r) : "v"(a), "v"(b), "v"(c));
  return r;
}
template <int HI>
__device__ __forceinline__ f32x2 pkfmas(f32x2 a, f32x2 s, f32x2 c) {
  f32x2 r;
  if (HI) asm("v_pk_fma_f32 %0, %2, %1, %3 op_sel:[1,0,0] op_sel_hi:[1,1,1]" : "=v"(r) : "v"(a), "v"(s), "v"(c));
  else asm("v_pk_fma_f32 %0, %2, %1, %3 op_sel:[0,0,0] op_sel_hi:[0,1,1]" : "=v"(r) : "v"(a), "v"(s), "v"(c));
  return r;
}
#define RANK8(d0, d1, d2, d3, c0, c1, i0, f0, f1) "v_perm_b32 " d0 ", " d0 ", " d1 ", %[sg]\n\tv_perm_b32 " d2 ", " d2 ", " d3 ", %[sg]\n\t" \
  "v_bcnt_u32_b32 " d0 ", " d0 ", " i0 "\n\tv_bcnt_u32_b32 " c0 ", " d2 ", " d0 "\n\tv_bcnt_u32_b32 " c1 ", " d2 ", " d0 "\n\t" f0 "\n\t" \
  "v_permlane16_swap_b32 " c0 ", " c1 "\n\tv_add_u32 " d0 ", " c0 ", " c1 "\n\tv_add_u32 " d1 ", " c0 ", " c1 "\n\t" f1 "\n\t" \
  "v_permlane32_swap_b32 " d0 ", " d1 "\n\tv_add_u32 " c0 ", " d0 ", " d1 "\n\t"
__device__ __forceinline__ void ln_rows(u32x4 (&xv)[4], u32x8 wv, rsrc_t rs, u32& tag, int b, int w, int l, u32 st, u32 sg TLARG) {
  float s[2][3] = {}, c[2], sw = 0.f;
  u32 os = l < 16 ? O_SG + 128 * (b - 128) + 8 * l : 0x80000000u;
  u32 ot = l < 16 ? O_TOP + 8 * (b & 15) + TL * ((b >> 4) & 7) : l < 32 ? O_RG - 128 + 128 * (b - 128) + 8 * l : 0x80000000u, om = l < 16 ? TOPS / 8 : 0u, fm = l < 16 ? 0xff0u : 0u;
  u32 aw = (l & 16) ? st + 8 * w + (l >> 5) * 4 : st + 136, ac = st + 128, ar = st + 4 * (l & 15), aq = st + (l & 48), one = 1u;
  u32 kl = (2047 - (16 * (b - 128) + 2 * w + (l >> 5))) << 1;
  asm volatile("" : "+v"(os), "+v"(aw), "+v"(ac), "+v"(ar), "+v"(aq), "+v"(one), "+v"(kl), "+v"(ot), "+v"(om), "+v"(fm));
  SB();
  asm volatile("s_waitcnt vmcnt(4) lgkmcnt(0)" : "+{s44}"(tag)::"memory");
  asm volatile("" : "+{v[116:123]}"(wv));
  tag += 1u;
  *(volatile LDS u32*)(size_t)(st - 104) = tag;
  STAMP(12);
#pragma unroll
  for (int i = 0; i < 8; i++) sw = dot2b(wv[i], 0x3f803f80u, sw);
  sw += dppf<0x121, 0xf>(sw); sw += dppf<0x122, 0xf>(sw); sw += dppf<0x124, 0xf>(sw); sw += dppf<0x128, 0xf>(sw);
  sw = (rdl(sw, 0) + rdl(sw, 16)) + (rdl(sw, 32) + rdl(sw, 48));
  asm volatile("" : "+v"(sw));
  SB();
  float cv = 0.f;
#pragma unroll
  for (int r = 0; r < 2; r++) {
#pragma unroll
    for (int i = 0; i < 8; i++) {
      if (r == 0 && i == 0) { asm volatile("s_waitcnt vmcnt(3)" : "+{v[100:103]}"(xv[0])); STAMP(13); }
      if (r == 0 && i == 4) asm volatile("s_waitcnt vmcnt(2)" : "+{v[104:107]}"(xv[1]));
      if (r == 1 && i == 0) asm volatile("s_waitcnt vmcnt(1)" : "+{v[108:111]}"(xv[2]));
      if (r == 1 && i == 4) asm volatile("s_waitcnt vmcnt(0)" : "+{v[112:115]}"(xv[3]));
      const u32 a = xv[2 * r + (i >> 2)][i & 3];
      s[r][0] = dot2b(a, 0x3f803f80u, s[r][0]); s[r][1] = dot2b(a, a, s[r][1]); s[r][2] = dot2b(a, wv[i], s[r][2]);
      if (i == 3) {
        c[r] = rdl(s[r][0] * 0.125f, 0);
        if (r) { cv = l < 32 ? c[0] : c[1]; asm volatile("" : "+v"(cv)); }
        SB();
      }
    }
    const float ml = s[r][0] * 0.0625f, dl = __builtin_fmaf(s[r][0], 0.25f, -4.f * c[r]);
    s[r][1] = __builtin_fmaf(dl, dl, __builtin_fmaf(-s[r][0], ml, s[r][1]));
    SB();
  }
#pragma unroll
  for (int k = 0; k < 3; k++) swap32(s[0][k ? 3 - k : 0], s[1][k ? 3 - k : 0]);
  float q[3];
#pragma unroll
  for (int k = 0; k < 3; k++) q[k] = s[0][k] + s[1][k];
#define RED3(m) "v_add_f32_dpp %0, %0, %0 " m " bank_mask:0xf\n\tv_add_f32_dpp %1, %1, %1 " m " bank_mask:0xf\n\tv_add_f32_dpp %2, %2, %2 " m " bank_mask:0xf\n\t"
  asm volatile("s_nop 1\n\t" RED3("row_ror:1 row_mask:0xf") RED3("row_ror:2 row_mask:0xf") RED3("row_ror:4 row_mask:0xf") RED3("row_ror:8 row_mask:0xf")
               RED3("row_bcast:15 row_mask:0xa") : "+v"(q[0]), "+v"(q[1]), "+v"(q[2]));
  const float mu = q[0] * (1.f / D), dv = mu - cv;
  const float var = fmaxf(__builtin_fmaf(-dv, dv, q[1] * (1.f / D)), 0.f);
  const float m = rb(mu);
  float sd = __builtin_amdgcn_sqrtf(rb(rb(var) + 1e-5f)), num = __builtin_fmaf(-m, sw, q[2]);
  asm volatile("s_nop 0\n\tv_cvt_pk_bf16_f32 %0, 0, %0" : "+v"(sd), "+v"(num));
  u32 rv = __float_as_uint(rb(num * __builtin_amdgcn_rcpf(sd)));
  rv ^= (u32)((int)rv >> 15) | 0x80000000u;
  rv = __builtin_amdgcn_perm(rv, kl, 0x07060100u) >> 1;
  asm volatile("ds_write2_b32 %[aw], %[sv], %[rv] offset1:16\n\ts_mov_b64 exec, 1\n\tds_add_rtn_u32 v104, %[ac], %[one]\n\ts_mov_b64 exec, -1\n\t"
               "ds_read_b32 v106, %[ar]\n\tds_read_b32 v105, %[ar] offset:64\n\tds_read_b128 v[100:103], %[aq] offset:64\n\tv_mov_b32 v107, %[t]\n\tv_mov_b32 v109, %[t]\n\t"
               "s_waitcnt lgkmcnt(3)\n\tv_cmp_eq_u32 vcc, 7, v104\n\ts_bitcmp1_b32 vcc_lo, 0\n\ts_cbranch_scc0 1f\n\t"
               "s_waitcnt lgkmcnt(2)\n\tbuffer_store_dwordx2 v[106:107], %[os], %[rs], 0 offen sc1\n\ts_waitcnt lgkmcnt(0)\n\t"
               "v_sub_u32 v100, v105, v100\n\tv_sub_u32 v101, v105, v101\n\tv_sub_u32 v102, v105, v102\n\tv_sub_u32 v103, v105, v103\n\t"
               RANK8("v100", "v101", "v102", "v103", "v104", "v103", "0", "s_nop 1", "s_nop 1") "v_cmp_eq_u32 vcc, 16, v104\n\t"
               "v_sub_u32 v108, 19, v104\n\ts_ff1_i32_b32 s6, vcc_lo\n\tv_readlane_b32 s6, v105, s6\n\t"
               "v_mad_u32_u24 v104, v104, %[om], %[ot]\n\ts_nop 0\n\tv_lshrrev_b32 v108, v108, s6\n\tv_bfi_b32 v108, %[fm], v108, v105\n\t"
               "buffer_store_dwordx2 v[108:109], v104, %[rs], 0 offen sc1\n1:\n\ts_waitcnt lgkmcnt(0)"
               :: [aw] "v"(aw), [sv] "v"(__builtin_amdgcn_perm(__float_as_uint(sd), __float_as_uint(m), 0x07060302u)), [rv] "v"(rv), [ac] "v"(ac), [one] "v"(one),
                  [ar] "v"(ar), [aq] "v"(aq), [os] "v"(os), [om] "v"(om), [ot] "v"(ot), [fm] "v"(fm), [sg] "s"(sg), [t] "s"(tag), [rs] "s"(rs)
               : "vcc", "scc", "s6", "v100", "v101", "v102", "v103", "v104", "v105", "v106", "v107", "v108", "v109", "memory");
  SB();
}

constexpr int NOPS = 109, NSLOT = 168;
struct Ops {
  signed char k[NOPS], a[NOPS], b[NOPS];
  short first[NOPS + 1];
  unsigned char op[NSLOT];
  short ld[NOPS][2];
  short end[8];
};
constexpr int nmf(int k, int T, int p) { return k == 0 ? (p == 0 && T > 0) + (T < 8) + (p == 0 && T > 0 && T < 5) : k == 1 ? 1 + (T < 6) : k == 2 ? (T < 4 ? 3 : 6 - T) : 4 - T; }
constexpr Ops mkops() {
  Ops O = {};
  int n = 0, last[8] = {};
  O.k[n] = 0; O.a[n] = 0; O.b[n] = 0; n++;
  for (int I = 0; I < 8; I++) {
    int fk[7] = {}, fa[7] = {}, fb[7] = {}, m = 0;
    if (I >= 4) { fk[m] = 3; fa[m] = I - 4; fb[m] = 0; m++; }
    if (I >= 2) for (int p = 0; p < 2; p++) { fk[m] = 2; fa[m] = I - 2; fb[m] = p; m++; }
    if (I >= 1) for (int p = 0; p < 4; p++) { fk[m] = 1; fa[m] = I - 1; fb[m] = p; m++; }
    int f = 0;
    for (int c = 1; c <= 8; c++) {
      O.k[n] = 0; O.a[n] = c == 8 ? I + 1 : I; O.b[n] = c & 7; n++;
      if (I >= 2 && f < m) { O.k[n] = fk[f]; O.a[n] = fa[f]; O.b[n] = fb[f]; n++; f++; }
    }
    for (; f < m; f++) { O.k[n] = fk[f]; O.a[n] = fa[f]; O.b[n] = fb[f]; n++; }
    last[I] = n - 1;
  }
  int q = 0;
  for (int i = 0; i < NOPS; i++) {
    O.first[i] = q;
    for (int u = nmf(O.k[i], O.a[i], O.b[i]); u > 0; u--) O.op[q++] = i;
  }
  O.first[NOPS] = q;
  for (int I = 0; I < 8; I++) O.end[I] = O.first[last[I]];
  for (int i = 0; i < NOPS; i++) {
    O.ld[i][1] = O.first[i] - LA;
    O.ld[i][0] = O.ld[i][1] - (O.k[i] && O.first[i] - O.first[i - 1] > 1);
  }
  return O;
}
constexpr Ops OPS = mkops();
static_assert(OPS.first[NOPS] == NSLOT, "schedule");
constexpr int WL[4] = {-1, 17, WL2, WL3}, WN[4] = {38, 38, 22, 13};
constexpr int wslot(int pp, int k) { return pp < 2 ? (pp ? 22 : 2) + k / 2 : WL[pp] + 4 + k; }
constexpr int opat(int k, int T, int p) { for (int i = 0; i < NOPS; i++) if (OPS.k[i] == k && OPS.a[i] == T && OPS.b[i] == p) return i; return -1; }
static_assert(wslot(0, 12) < OPS.ld[opat(1, 0, 0)][0] && wslot(0, 21) < OPS.ld[opat(2, 0, 0)][0] && wslot(1, 12) < OPS.ld[opat(1, 1, 0)][0] &&
              wslot(1, 21) < OPS.ld[opat(2, 1, 0)][0] && wslot(1, 37) < OPS.ld[opat(3, 0, 0)][0] && wslot(2, 12) < OPS.ld[opat(1, 3, 0)][0] &&
              wslot(2, 21) < OPS.ld[opat(2, 3, 0)][0] && wslot(3, 12) < OPS.ld[opat(1, 5, 0)][0] && wslot(0, 29) < WL[1] && wslot(1, 37) < WL[2] &&
              wslot(2, 21) < WL[3], "window order");
constexpr int XC[3] = {OPS.ld[opat(0, 2, 0)][1], WL2 < OPS.ld[opat(0, 4, 0)][1] ? WL2 : OPS.ld[opat(0, 4, 0)][1], WL3};
static_assert(XC[0] <= WL[1] && WL3 <= OPS.ld[opat(0, 6, 0)][1], "chunk order");
template <int V> struct IC { static constexpr int value = V; };
template <int A, int B, class F> __device__ __forceinline__ void sfor(F&& f) {
  if constexpr (A < B) { f(IC<A>{}); sfor<A + 1, B>(f); }
}
#define GL __attribute__((address_space(1)))
typedef const GL u16* gp;
__device__ __forceinline__ void dma(gp base, u32 v, u32 dst) {
  asm volatile("s_mov_b32 m0, %0\n\ts_nop 0\n\tglobal_load_lds_dwordx4 %1, %2 nt" ::"s"(dst), "v"(v), "s"(base) : "memory");
}

__global__ __launch_bounds__(768, 3) void fused(const u16*, const u16*, const u16*, const u16*, const u16*, const u16*, const u16*, const u16*,
    const u16*, const u16*, const u16*, u16*, char*) {
  __shared__ __attribute__((aligned(256))) char smem[SMEM];
  const u32 lds0 = (u32)(size_t)(LDS char*)smem;
  int w0;
#define GLD(r0, r1, r2, r3, o_) "global_load_dwordx4 v[" #r0 ":" #r1 "], " #o_ ", s[16:17]\n\tglobal_load_dwordx4 v[" #r2 ":" #r3 "], " #o_ ", s[16:17] offset:2048\n\t"
  asm volatile(
      "s_load_dwordx16 s[16:31], s[0:1], 0\n\ts_load_dwordx8 s[32:39], s[0:1], 64\n\ts_load_dwordx2 s[40:41], s[0:1], 96\n\t"
      "s_memrealtime s[46:47]\n\t"
      "s_getpc_b64 s[4:5]\n2:\n\ts_lshr_b32 s7, s2, 3\n\ts_and_b32 s10, s4, 0xffffff00\n\ts_mov_b32 s11, s5\n\ts_mul_i32 s8, s7, 0x600\n\t"
      "v_lshl_add_u32 v3, v0, 1, s8\n\ts_cmpk_lt_u32 s2, %[pn]\n\ts_cbranch_scc0 1f\n\t"
      "global_load_ushort v163, v3, s[10:11] offset:%[pf]\n"
      "1:\n\ts_cmpk_lt_u32 s2, 0x80\n\tv_readfirstlane_b32 s3, v0\n\tv_and_b32 v1, 63, v0\n\tv_mov_b64 v[4:5], 0\n\tv_mov_b64 v[6:7], 0\n\tv_lshlrev_b32 v2, 4, v1\n\tv_lshlrev_b32 v8, 2, v0\n\t"
      "s_mov_b32 s42, %[we]\n\ts_mov_b32 s43, 0x20000\n\ts_cbranch_scc0 3f\n\t"
      "s_lshr_b32 s3, s3, 6\n\ts_mul_i32 s9, s3, %[xr]\n\ts_min_u32 s9, s9, %[zm]\n\tv_add_u32 v3, s9, v2\n\tv_add_u32 v3, %[zo], v3\n\t"
      "s_and_b32 s7, s7, 7\n\ts_and_b32 s6, s2, 7\n\ts_and_b32 s8, s2, 64\n\ts_lshl3_add_u32 s8, s6, s8\n\ts_add_u32 s45, s8, s7\n\t"
      "ds_write_b128 v3, v[4:7]\n\ts_cmp_lt_u32 s3, 8\n\ts_cbranch_scc1 5f\n\t"
      "s_lshl_b32 s8, s45, 4\n\ts_lshl_b32 s9, s3, 18\n\ts_add_u32 s9, s9, s8\n\ts_bitset0_b32 s9, 21\n\tv_lshl_add_u32 v148, v1, 12, s9\n\t"
      "s_lshl3_add_u32 s9, s3, s7\n\ts_lshl_b32 s9, s9, 17\n\ts_add_u32 s9, s9, s8\n\ts_bitset0_b32 s9, 23\n\tv_lshl_add_u32 v9, v1, 11, s9\n\t"
      "v_add_u32 v11, 0x100000, v148\n\tv_add_u32 v12, 0x200000, v148\n\tv_add_u32 v13, 0x300000, v148\n\t"
      "v_lshlrev_b32 v149, 4, v0\n\tv_add_u32 v149, %[sg], v149\n\ts_movk_i32 s9, 0x1000\n\t"
      "s_waitcnt lgkmcnt(0)\n\ts_load_dword s44, s[40:41], 0\n\ts_load_dwordx4 s[48:51], s[28:29], s8\n\t"
      "s_barrier\n\t"
#define HLD "global_load_dword v162, v14, s[14:15]\n\t"
      "global_load_dword v160, v9, s[16:17]" PRENT "\n\t"
      GLD(100, 103, 104, 107, v148)
      GLD(108, 111, 112, 115, v11)
      GLD(116, 119, 120, 123, v12)
      "buffer_load_dwordx4 v[132:135], v149, s[40:43], 0 offen sc1\n\tbuffer_load_dwordx4 v[136:139], v149, s[40:43], s9 offen sc1\n\t"
      GLD(124, 127, 128, 131, v13)
      "s_branch .Lrole_p\n"
      "3:\n\ts_lshr_b32 s3, s3, 6\n\ts_cmp_lt_u32 s3, 8\n\ts_cbranch_scc1 4f\n\ts_barrier\n\ts_branch .Lrole_d\n4:\n\t"
      "v_add_u32 v3, %[fc], v2\n\ts_add_u32 s8, s3, s2\n\ts_and_b32 s45, s8, 7\n\ts_lshl3_add_u32 s8, s2, s45\n\t"
      "s_lshl_b32 s8, s8, 12\n\ts_bitset0_b32 s8, 22\n\tds_write_b128 v3, v[4:7]\n\tv_add_u32 v3, s8, v2\n\t"
      "s_waitcnt lgkmcnt(0)\n\tglobal_load_dwordx4 v[116:119], v2, s[30:31]\n\tglobal_load_dwordx4 v[120:123], v2, s[30:31] offset:1024\n\t"
      "s_load_dword s44, s[40:41], 0\n\ts_barrier\n\t"
      "global_load_dwordx4 v[100:103], v3, s[16:17]" LNNT "\n\tglobal_load_dwordx4 v[104:107], v3, s[16:17] offset:1024" LNNT "\n\t"
      "global_load_dwordx4 v[108:111], v3, s[16:17] offset:2048" LNNT "\n\tglobal_load_dwordx4 v[112:115], v3, s[16:17] offset:3072" LNNT "\n\t"
      "s_branch .Lrole_l\n"
      "5:\n\ts_add_u32 s4, s4, lane_tab@rel32@lo+(6f-2b-4)\n6:\n\ts_addc_u32 s5, s5, lane_tab@rel32@hi+(6b-2b+4)\n\t"
      "global_load_dwordx4 v[16:19], v2, s[4:5]\n\tglobal_load_dwordx4 v[20:23], v2, s[4:5] offset:1024\n\t"
      "s_lshl3_add_u32 s8, s45, s3\n\ts_lshl_b32 s9, s8, 1\n\tv_mov_b32 v9, s9\n\ts_lshl_b32 s9, s45, 11\n\tv_add_u32 v3, s9, v8\n\t"
      "s_waitcnt lgkmcnt(0)\n\tglobal_load_short_d16_hi v24, v9, s[26:27]\n\t"
      "global_load_dword v96, v3, s[18:19]" PNT "\n\tglobal_load_dword v97, v3, s[20:21]" PNT "\n\t"
      "global_load_dword v68, v3, s[22:23]" PNT "\n\tglobal_load_dword v69, v3, s[24:25]" PNT "\n\ts_barrier\n\ts_branch .Lrole_c"
      : "={s3}"(w0)
      : "{s[0:1]}"(__builtin_amdgcn_kernarg_segment_ptr()), "{s2}"(blockIdx.x), "{v0}"(threadIdx.x), [pf] "n"(PFO), [pn] "n"(8 * PFN), [we] "n"(WS_END), [xr] "n"(XREG),
        [zm] "n"(OMISC - 512 - (OX - 16)), [zo] "n"(OX - 16), [sg] "n"(O_SG - 8192), [fc] "n"(FC)
      : "memory");
#define TLREGS()
#define ROLE(name, text, ...) \
  gp x, dsk, wg, wu, wd; GL u16* out; rsrc_t rs; int b, w, t, l, g; u32 tag, st0; u32x4 dv; \
  asm volatile(".Lrole_" #name ":" : "={v0}"(t), "={v1}"(l) __VA_ARGS__ :: "memory"); \
  asm volatile(text : "={s2}"(b), "={s3}"(w), "={s[16:17]}"(x), "={s[28:29]}"(dsk), "={s[32:33]}"(wg), "={s[34:35]}"(wu), "={s[36:37]}"(wd), \
               "={s[38:39]}"(out), "={s[40:43]}"(rs), "={s44}"(tag), "={s45}"(g), "={s46}"(st0), "={s[48:51]}"(dv) :: "memory"); \
  SB(); \
  __builtin_assume((u32)l < 64u); __builtin_assume((u32)t < 768u); __builtin_assume((u32)w < 12u); __builtin_assume((u32)b < 256u); __builtin_assume((u32)g < 128u); \
  TLREGS(); \
  const int xk = b & 7, j = (b >> 3) & 15, n16 = l & 15, kg = l >> 4
#define TAGWAIT() do { asm volatile("s_waitcnt lgkmcnt(0)" : "+{s44}"(tag)::"memory"); tag += 1u; } while (0)
#define BAR() do { SB(); asm volatile("s_barrier" ::: "memory"); SB(); } while (0)
#define FLAG(o, v) do { asm volatile("" ::: "memory"); SB(); *(volatile LDS u32*)(LDS char*)(smem + (o)) = (v); SB(); } while (0)
#define ARRIVE(o) do { SB(); if (l == 0) asm volatile("ds_add_u32 %0, %1" ::"v"(lds0 + (o)), "v"(1u) : "memory"); SB(); } while (0)
#define CWAIT(o, n, nap) do { _Pragma("unroll 1") for (int spin = 0; spin < SPIN; spin++) { \
      if (__builtin_amdgcn_readfirstlane((int)*(const volatile LDS u32*)(LDS char*)(smem + (o))) >= (int)(n)) break; \
      asm volatile(nap ::: "memory"); } SB(); asm volatile("" ::: "memory"); } while (0)
#define AWAIT(o, n, nap) do { _Pragma("unroll 1") for (int spin = 0; spin < SPIN; spin++) { \
      if (__all(*(const volatile LDS u32*)(LDS char*)(smem + (o)) >= (u32)(n))) break; \
      asm volatile(nap ::: "memory"); } SB(); asm volatile("" ::: "memory"); } while (0)
#define POLL8(res_, off_) do { u32 n_ = SPIN; \
    asm volatile("1:\n\tbuffer_load_dwordx2 v[4:5], %2, %3, 0 offen sc1\n\ts_sub_u32 %1, %1, 1\n\ts_waitcnt vmcnt(0)\n\t" \
                 "v_cmp_ne_u32 vcc, %4, v5\n\ts_cbranch_scc1 2f\n\ts_cbranch_vccnz 1b\n2:" \
                 : "=&{v[4:5]}"(res_), "+s"(n_) : "v"(off_), "s"(rs), "s"(tag) : "vcc", "scc", "memory"); } while (0)
#define LANE(v_) int v_; asm volatile("v_mbcnt_lo_u32_b32 %0, -1, 0\n\tv_mbcnt_hi_u32_b32 %0, -1, %0" : "=v"(v_))
#define IXO(l_) (O_IDX + (2 * xk + (j & 1)) * 128 + ((l_) & 7) * 8)
#define IDS() const int id = 16 * xk + j, sb = id * 57 >> 10, mat = id & 1, u = (id - 18 * sb) >> 1; \
    const bool slow = u >= 6; \
    const int part = slow ? u - 6 : u & 1, cb = 4 * sb + (slow ? 1 : (u >> 1) + (u > 1)), r0 = part * (slow ? 320 : 512), ns = slow ? 5 + (u >> 3) : 8; \
    const int kp = 2 * sb + mat, ct = 0x51764320u >> (4 * (u & 7)) & 7
#define TS(a, b_)
#define TSC
#define TSGET(i0, i1, i2, i3)
  if (blockIdx.x >= 128) {
    if (w0 >= 8) {
      ROLE(d, "s_waitcnt lgkmcnt(0)");
      IDS();
      const int dw = w - 8;
      const u32 sw = (((n16 >> 1) ^ (kg & 1)) << 5) + ((l & 1) << 4);
      gp base = (mat ? wu : wg) + (size_t)(r0 + 4 * dw) * F + cb * 128;
      u32 v = kg * (2 * F) + sw, fa = lds0 + FC + 4 * dw, vp = 96 + dw, t_;
      asm volatile("" : "+v"(v), "+v"(fa), "+v"(vp));
      asm volatile("s_mov_b32 m0, %0" ::"s"(lds0 + 1024 * dw));
#define VMC "s_getreg_b32 s6, hwreg(HW_REG_IB_STS, 0, 4)\n\t"
#define RUN(k, q, lim, v_, b_, st_) asm volatile("1:\n\ts_lshr_b32 s6, m0, 4\n\ts_and_b32 s6, s6, 0xc0\n\tv_xor_b32 %[t], s6, %[v]\n\ts_waitcnt vmcnt(%[c])\n\t" VMC \
        "global_load_lds_dwordx4 %[t], %[b] nt\n\ts_add_u32 m0, m0, %[m]\n\tv_add_u32 %[v], %[d], %[v]\n\t.if %[m] == 3072\n\tv_mad_i32_i24 %[t], s6, -3, %[p]\n\tv_add_u32 %[p], 3, %[p]\n\t" \
        "s_mov_b64 exec, 1\n\tds_max_u32 %[a], %[t]\n\ts_mov_b64 exec, -1\n\t.endif\n\ts_cmp_lt_u32 m0, %[l]\n\ts_cbranch_scc1 1b" \
        : [v] "+v"(v_), [p] "+v"(vp), [t] "=&v"(t_) : [b] "s"(b_), [c] "n"((q) - 1), [m] "n"(1024 * (k)), [d] "n"((k) * (st_)), \
          [a] "v"(fa), [l] "s"(lim) : "scc", "s6", "memory")
#define MARK(c, val) "s_waitcnt vmcnt(" #c ")\n\t" val "\n\ts_mov_b64 exec, 1\n\tds_max_u32 %[a], %[t]\n\ts_mov_b64 exec, -1\n\t"
      __builtin_amdgcn_s_setprio(DPRIO);
      STAMP(8);
      if (id < 126) { if (slow) RUN(4, QAS, 32768, v, base, 8 * F); else RUN(4, 8, 32768, v, base, 8 * F); }
      if (dw < 3) {
        if (id >= 126) __builtin_amdgcn_endpgm();
        gp bd = wd + (size_t)(256 * kp + 4 * dw) * D + ct * 128;
        u32 vd = kg * (2 * D) + sw;
        asm volatile("" : "+v"(vd));
        if (slow) RUN(3, QS, 16384 * ns, v, base, 8 * F);
        else { RUN(3, 8, 32768 + 3072 * RAMP, v, base, 8 * F); RUN(3, QD, 131072, v, base, 8 * F); }
        STAMP(1);
        {
          u32 n_ = SPIN;
          asm volatile("1:\n\t" VMC MARK(63, "v_mad_i32_i24 %[t], s6, -3, %[p]") "ds_read_b32 %[t], %[f]\n\ts_sub_u32 %[n], %[n], 1\n\ts_waitcnt lgkmcnt(0)\n\t"
                       "v_cmp_lt_u32 vcc, %[k], %[t]\n\ts_cbranch_scc1 2f\n\ts_cbranch_vccz 1b\n2:\n\ts_mov_b32 m0, %[m]"
                       : [t] "=&v"(t_), [n] "+s"(n_) : [a] "v"(fa), [p] "v"(vp), [f] "v"(u == 8 ? fa : lds0 + FC + 20), [k] "s"(u == 8 ? 159 : HOLD), [m] "s"(lds0 + 1024 * dw)
                       : "vcc", "scc", "s6", "memory");
        }
        STAMP(3);
        if (u < 8) {
          if (slow) RUN(3, QS, 65536, vd, bd, 8 * D); else RUN(3, QE, 65536, vd, bd, 8 * D);
          STAMP(6);
          asm volatile("s_waitcnt vmcnt(%[h])\n\t" MARK(63, "v_mov_b32 %[t], 0x8000") MARK(0, "v_mov_b32 %[t], 0x10000") : [t] "=&v"(t_) : [a] "v"(fa), [h] "n"(HQ) : "memory");
          STAMP(4);
        }
        FLUSH();
        __builtin_amdgcn_endpgm();
      }
      asm volatile(MARK(0, "v_mov_b32 %[t], -1") : [t] "=&v"(t_) : [a] "v"(fa) : "memory");
      u32 km, rk;
      u32 hi, lo, k2, k3, gd, thr, sg = 0x0c0c0b09u;
      u32 ka = lds0 + FC + 384 + 8 * (l >> 3), kc = 2047 * 2048 + 2 * r0 + 16 * ((l & 7) ^ ((l >> 2) & 4)), kq = lds0 + FC + 512 + (l & 48), kr = lds0 + FC + 512 + 4 * n16;
      u32 c1 = 896, c2 = 1792, c3 = 2688, cm = (u32)-2048, cs = O_SG + 8 * 2047, ca = lds0 + FC + 512;
      rsrc_t rx = __builtin_amdgcn_make_buffer_rsrc((void*)(x - 1344), 0, 2 * L * D + 4096, 0x00020000);
      asm volatile("" : "+v"(ka), "+v"(kc), "+v"(kq), "+v"(kr));
      asm volatile("" : "+s"(c1), "+s"(c2), "+s"(c3), "+s"(cm), "+s"(cs), "+s"(ca), "+s"(sg), "+s"(rx));
      *(u32x4*)(smem + FA + 6144 + 16 * l) = u32x4{0u, 0u, 0u, 0u};
      *(u32x4*)(smem + FA + 7168 + 16 * l) = u32x4{0u, 0u, 0u, 0u};
      CWAIT(FC + 256, 1, "s_sleep 4");
      tag = (u32)__builtin_amdgcn_readfirstlane((int)*(const volatile LDS u32*)(LDS char*)(smem + FC + 24));
      {
        u32 n_ = SPIN, t0, t1, t2, t3, se;
#define TLD(a, b_, c, d) "buffer_load_dwordx4 v[" #a ":" #b_ "], %[o], %[r], 0 offen sc1\n\tbuffer_load_dwordx4 v[" #c ":" #d "], %[o], %[r], %[ts] offen sc1\n\t"
#define TCK(r0, r1, r2, r3, r4, r5, r6, r7) "s_waitcnt vmcnt(4)\n\tv_xor_b32 %[t0], %[t], v" #r1 "\n\tv_bitop3_b32 %[t0], %[t0], %[t], v" #r3 " bitop3:0xf6\n\t" \
          "v_bitop3_b32 %[t0], %[t0], %[t], v" #r5 " bitop3:0xf6\n\tv_bitop3_b32 %[t0], %[t0], %[t], v" #r7 " bitop3:0xf6\n\t" \
          "v_cmp_ne_u32 vcc, 0, %[t0]\n\tv_mov_b32 %[hi], v" #r0 "\n\tv_mov_b32 %[lo], v" #r2 "\n\tv_mov_b32 %[k2], v" #r4 "\n\tv_mov_b32 %[k3], v" #r6 "\n\t" \
          "s_cbranch_vccz 5f\n\t" TLD(r0, r3, r4, r7)
        asm volatile("2:\n\ts_sleep 1\n\ts_memrealtime s[8:9]\n\ts_waitcnt lgkmcnt(0)\n\ts_sub_u32 s8, s8, %[st]\n\ts_cmp_lt_u32 s8, %[tw]\n\ts_cbranch_scc1 2b\n\t"
                     TLD(128, 131, 132, 135) "s_sleep " STR(RSL) "\n\t" TLD(136, 139, 140, 143) "s_sleep " STR(RSL) "\n\t" TLD(144, 147, 148, 151)
                     "1:\n\t" TCK(128, 129, 130, 131, 132, 133, 134, 135) TCK(136, 137, 138, 139, 140, 141, 142, 143) TCK(144, 145, 146, 147, 148, 149, 150, 151)
                     "s_sub_u32 %[n], %[n], 1\n\ts_cbranch_scc0 1b\n5:\n\t" TS(60, 61)
                     "v_and_or_b32 %[t0], %[hi], %[mk], %[pa]\n\tv_and_or_b32 %[t1], %[lo], %[mk], %[pb]\n\t"
                     "v_max_u32 %[t2], %[t0], %[t1]\n\tv_max_u32 %[t3], %[t0], %[t1]\n\tv_perm_b32 %[gd], %[hi], %[lo], %[p0]\n\tv_perm_b32 %[se], %[k2], %[k3], %[p0]\n\t"
                     "v_permlane16_swap_b32 %[t2], %[t3]\n\tv_max_u32 %[km], %[t2], %[t3]\n\tv_max_u32 %[rk], %[t2], %[t3]\n\t"
                     "v_max_u32 %[hi], %[t0], %[t1]\n\tv_min_u32 %[lo], %[t0], %[t1]\n\t"
                     "v_permlane32_swap_b32 %[km], %[rk]\n\tv_max_u32 %[km], %[km], %[rk]\n\tv_not_b32 %[t2], %[km]\n\t"
                     "v_and_or_b32 %[k2], %[k2], %[mk], %[pa]\n\t"
                     "v_not_b32_dpp %[t2], %[km] row_ror:4 row_mask:0x2 bank_mask:0xf\n\tv_not_b32_dpp %[t2], %[km] row_ror:8 row_mask:0x4 bank_mask:0xf\n\t"
                     "v_not_b32_dpp %[t2], %[km] row_ror:12 row_mask:0x8 bank_mask:0xf\n\t"
                     "v_pk_lshlrev_b16 %[gd], 4, %[gd] op_sel_hi:[0,1]\n\tv_add_u32 %[t0], %[km], %[t2]\n\t"
                     "v_add_u32_dpp %[t1], %[t2], %[km] row_ror:1 row_mask:0xf bank_mask:0xf\n\tv_add_u32_dpp %[t3], %[t2], %[km] row_ror:2 row_mask:0xf bank_mask:0xf\n\t"
                     "v_add_u32_dpp %[t2], %[t2], %[km] row_ror:3 row_mask:0xf bank_mask:0xf\n\t"
                     RANK8("%[t0]", "%[t1]", "%[t2]", "%[t3]", "%[rk]", "%[t3]", "-2", "v_lshrrev_b32 %[se], 4, %[se]\n\tv_perm_b32 %[gd], %[gd], %[se], %[p1]",
                           "v_pk_max_u16 %[gd], %[gd], %[gd] op_sel:[0,1] op_sel_hi:[1,0]\n\tv_and_or_b32 %[k3], %[k3], %[mk], %[pb]")
                     "v_cmp_eq_u32 vcc, 56, %[rk]\n\tv_max3_u32 %[se], %[lo], %[k2], %[k3]\n\tv_lshrrev_b32 %[gd], 1, %[gd]\n\ts_ff1_i32_b32 s6, vcc_lo\n\t"
                     "v_readlane_b32 s6, %[km], s6\n\tv_or_b32 %[gd], 0x7fff, %[gd]\n\tv_med3_u32 %[t2], %[lo], %[k2], %[k3]\n\t"
                     "v_cmp_le_u32 s[8:9], s6, %[hi]\n\tv_cmp_le_u32 s[10:11], s6, %[se]\n\tv_max_u32 %[t2], %[t2], %[gd]\n\t"
                     "v_mbcnt_lo_u32_b32 %[t0], s8, 0\n\tv_mbcnt_lo_u32_b32 %[t1], s10, -15\n\tv_mbcnt_hi_u32_b32 %[t0], s9, %[t0]\n\tv_mbcnt_hi_u32_b32 %[t1], s11, %[t1]\n\t"
                     "v_lshl_add_u32 %[t0], %[t0], 2, %[ca]\n\tv_mad_i32_i24 %[t1], %[t1], -4, %[ca]\n\tv_cmp_le_u32 vcc, s6, %[t2]\n\t"
                     "s_mov_b64 exec, s[8:9]\n\tds_write_b32 %[t0], %[hi]\n\ts_mov_b64 exec, s[10:11]\n\tds_write_b32 %[t1], %[se]\n\ts_mov_b64 exec, -1\n\t"
                     "ds_read_b32 %[se], %[kr]\n\tds_read_b128 v[152:155], %[kq]\n\t"
                     "s_bcnt1_i32_b64 s8, s[8:9]\n\ts_bcnt1_i32_b64 s10, s[10:11]\n\ts_add_u32 s8, s8, s10\n\ts_cmp_gt_u32 s8, 16\n\ts_cselect_b64 s[8:9], -1, vcc\n\t"
                     "s_cmp_lg_u64 s[8:9], 0\n\ts_waitcnt lgkmcnt(0)\n\ts_cbranch_scc1 6f\n\t"
                     "v_sub_u32 %[t0], %[se], v152\n\tv_sub_u32 %[t1], %[se], v153\n\tv_sub_u32 %[t2], %[se], v154\n\tv_sub_u32 %[t3], %[se], v155\n\t"
                     RANK8("%[t0]", "%[t1]", "%[t2]", "%[t3]", "%[rk]", "%[t3]", "0", "v_mov_b32 %[km], %[se]\n\ts_nop 0", "s_nop 1") "6:\n\t" TS(62, 63)
                     : [hi] "=&v"(hi), [lo] "=&v"(lo), [k2] "=&v"(k2), [k3] "=&v"(k3), [gd] "=&v"(gd), [se] "=&v"(se), [km] "=&v"(km), [rk] "=&v"(rk),
                       [t0] "=&v"(t0), [t1] "=&v"(t1), [t2] "=&v"(t2), [t3] "=&v"(t3), [n] "+s"(n_)
                     : [o] "v"(O_TOP + 16 * (l & 7) + TL * (l >> 3)), [r] "s"(rs), [t] "s"(tag), [ts] "s"(TOPS), [st] "s"(st0), [tw] "n"(TW), [mk] "s"(0x7fff800fu),
                       [pa] "v"((127 - 2 * l) << 4), [pb] "v"((126 - 2 * l) << 4), [sg] "s"(sg), [p0] "s"(0x05040100u), [p1] "s"(0x07020500u), [ca] "s"(ca),
                       [kr] "v"(kr), [kq] "v"(kq)
                     : "vcc", "scc", "s6", "s8", "s9", "s10", "s11", "memory" TSC);
      }
      unsigned long long more;
      asm volatile("" : "={s6}"(thr), "={s[8:9]}"(more));
      if (__builtin_expect(more != 0, 0)) {
        const u32 m0 = km;
        unsigned long long all = __ballot(gd >= thr);
        if (gd >= thr) hi = lo = k2 = k3 = 0u;
#pragma unroll 1
        for (int i = 0; i < 4;) {
          u32 k, m = m0;
          if (all) {
            const int a = __builtin_ctzll(all);
            all &= all - 1;
            u32x2 r;
            const u32 o = O_RG + 256 * a + 8 * (l & 31);
            POLL8(r, o);
            k = l < 32 ? r.x : 0u;
            m = (u32)__builtin_amdgcn_readlane((int)m0, a);
          } else { k = hi; hi = lo; lo = k2; k2 = k3; i++; }
          asm volatile("v_cmp_le_u32 s[8:9], %[t], %[k]\n\tv_cmp_ne_u32 vcc, %[k], %[m0]\n\ts_and_b64 s[8:9], s[8:9], vcc\n\ts_cbranch_scc0 2f\n"
                       "1:\n\ts_ff1_i32_b64 s7, s[8:9]\n\tv_readlane_b32 s12, %[k], s7\n\ts_lshl_b64 s[10:11], 1, s7\n\ts_nop 0\n\t"
                       "v_cmpx_gt_u32 vcc, s12, %[km]\n\tv_add_u32 %[rk], 8, %[rk]\n\ts_bcnt0_i32_b32 s7, vcc_lo\n\tv_cmpx_eq_u32 vcc, 0x80, %[rk]\n\t"
                       "v_mov_b32 %[km], s12\n\tv_lshlrev_b32 %[rk], 2, s7\n\ts_mov_b64 exec, -1\n\ts_andn2_b64 s[8:9], s[8:9], s[10:11]\n\ts_cbranch_scc1 1b\n2:"
                       : [km] "+v"(km), [rk] "+v"(rk) : [k] "v"(k), [m0] "v"(m), [t] "s"(thr) : "vcc", "scc", "s7", "s8", "s9", "s10", "s11", "s12");
        }
      }
      if (id < 126) {
        u32 a_, v_, n_ = SPIN;
#define RLD(o, s_) "buffer_load_dwordx4 %[v], %[rx], " s_ " offen offset:" #o " lds\n\t"
        asm volatile("v_add_u32 %[a], %[kb], %[rk]\n\tv_and_b32 %[v], 0x7ff, %[km]\n\ts_mov_b32 m0, %[fa]\n\tds_write_b32 %[a], %[v]\n\tds_read_b32 %[a], %[ka]\n\t"
                     "s_cmp_eq_u32 %[ns], 8\n\ts_waitcnt lgkmcnt(0)\n\tv_mad_i32_i24 %[v], %[a], %[cm], %[kc]\n\t"
                     RLD(0, "%[c3]") RLD(1024, "%[c2]") RLD(2048, "%[c1]") RLD(3072, "0")
                     "s_mov_b32 m0, %[fb]\n\tv_add_u32 %[v], 0x200, %[v]\n\t"
                     RLD(0, "%[c3]") RLD(1024, "%[c2]") "s_cbranch_scc0 3f\n\t" RLD(2048, "%[c1]") RLD(3072, "0")
                     "3:\n\tv_mad_i32_i24 %[a], %[a], -8, %[cs]\n\tbuffer_load_dwordx2 v[160:161], %[a], %[rs], 0 offen sc1\n\t"
                     "v_mov_b32 %[v], " STR(HOLD) " - 7\n\tv_subrev_u32 %[ka], %[kd], %[ka]\n\ts_mov_b64 exec, 1\n\tds_add_u32 %[fc], %[v] offset:8\n\ts_mov_b64 exec, -1\n\tv_mov_b32 %[v], -1\n\t" TS(64, 65)
                     "1:\n\ts_waitcnt vmcnt(0)\n\tv_cmp_ne_u32 vcc, %[t], v161\n\tv_and_b32 v156, 0xffff0000, v160\n\tv_lshlrev_b32 v157, 16, v160\n\tv_rcp_f32 v156, v156\n\t"
                     "s_sub_u32 %[n], %[n], 1\n\ts_cbranch_vccz 2f\n\ts_cbranch_scc1 2f\n\t"
                     "buffer_load_dwordx2 v[160:161], %[a], %[rs], 0 offen sc1\n\ts_branch 1b\n2:\n\tv_mul_f32 v157, -v157, v156\n\t"
                     "ds_write_b64 %[ka], v[156:157]\n\tds_write_b32 %[fc], %[v] offset:4\n\t" TS(66, 67)
                     : [a] "=&v"(a_), [v] "=&v"(v_), [ka] "+v"(ka), [n] "+s"(n_)
                     : [rk] "v"(rk), [km] "v"(km), [kc] "v"(kc), [kb] "n"(FC + 384), [kd] "n"(384 - 32), [fa] "n"(FA), [fb] "n"(FA + 4096), [rx] "s"(rx), [rs] "s"(rs),
                       [c1] "s"(c1), [c2] "s"(c2), [c3] "s"(c3), [cm] "s"(cm), [cs] "s"(cs), [ns] "s"(ns), [fc] "v"(fa), [t] "s"(tag) : "vcc", "scc", "memory" TSC);
        TSGET(12, 13, 14, 15);
      } else {
        const u32 cd = km << 1;
        const u32x2 r = {(2047u - (km & 0x7ffu)) | ((cd ^ ((u32)((int)~cd >> 15) | 0x80000000u)) & 0xffff0000u), tag};
        const u32 o = rk < 64u ? O_IDX + 1024 * mat + 128 * kg + rk : 0x80000000u;
        st8(rs, o, r);
        st8(rs, o + 512, r);
        TSGET(12, 13, 14, 15);
        STAMP(14);
        STAMP(15);
        if (mat) {
#pragma unroll 1
          for (int spin = 0; spin < SPIN; spin++) {
            const u32x4 a = ld16(rs, O_ARR + 16 * l);
            if (__all(a.y == tag && a.w == tag)) break;
            asm volatile("s_sleep 16" ::: "memory");
          }
          if (l == 0) __builtin_amdgcn_raw_buffer_store_b32(tag, rs, 0, 0, 16);
        }
      }
      FLUSH();
      __builtin_amdgcn_endpgm();
    }
    u32x4 xv[4];
    u32x8 wv;
    ROLE(l, "", , "={v[100:103]}"(xv[0]), "={v[104:107]}"(xv[1]), "={v[108:111]}"(xv[2]), "={v[112:115]}"(xv[3]), "={v[116:123]}"(wv));
    STAMP(11);
    u32 sg = 0x0c0c0b09u;
    asm volatile("" : "+s"(sg));
    ln_rows(xv, wv, rs, tag, b, g, l, lds0 + FC + 128, sg TLVAL);
    STAMP(10);
    IDS();
    if (id >= 126) {
      FLUSH();
      __builtin_amdgcn_endpgm();
    }
    const int wr = 16 * (kg >> 1) + 4 * (kg & 1) + (n16 & 8) + ((n16 >> 2) & 1);
    u32 wa = lds0 + 256 * wr + ((w ^ ((wr & 1) | ((wr >> 1) & 6))) << 5) + 8 * (l & 3), sel = (l & 8) ? 0xeeeeu : 0x4444u;
    u32 so = (l & 1) ? 0x80000000u : 512 * w + 256 * (n16 >> 3) + 64 * kg + 8 * (l & 6);
    asm volatile("" : "+v"(wa), "+v"(sel), "+v"(so));
#define OUT4(a0, a1, a2, a3, mid, o) "v_permlane32_swap_b32 " a0 ", " a2 "\n\tv_permlane32_swap_b32 " a1 ", " a3 "\n\t" mid \
      "v_cvt_pk_bf16_f32 v12, " a0 ", " a1 "\n\ts_nop 1\n\tv_mov_b32_dpp v14, v12 quad_perm:[1,1,3,3] row_mask:0xf bank_mask:0xf\n\t" \
      "buffer_store_dwordx4 v[12:15], %[so], %[rs], " o " offen sc1\n\t"
#define TR4(a, b_, c, d, e, f, g_, h, adr, o) "ds_read_b64_tr_b16 v[" #a ":" #b_ "], " adr " offset:" #o "\n\tds_read_b64_tr_b16 v[" #c ":" #d "], " adr " offset:" #o "+512\n\t" \
      "ds_read_b64_tr_b16 v[" #e ":" #f "], " adr " offset:" #o "+8192\n\tds_read_b64_tr_b16 v[" #g_ ":" #h "], " adr " offset:" #o "+8704\n\t"
#define SM(d, a, b_) "v_smfmac_f32_16x16x64_bf16 v[" d "], v[" a "], v[" b_ "], %[sel]\n\t"
#define CALL "s_call_b64 s[10:11], 1f\n\t"
#define USE(c, xa, wf) "s_add_u32 s6, s6, 16\n\ts_waitcnt lgkmcnt(" #c ")\n\t" SM("16:19", "20:23", wf) SM("8:11", xa, wf)
#define XR(xa, o) "ds_read_b128 v[" xa "], %[xa] offset:" #o "\n\t"
#define XU(c, xa, wf) "s_waitcnt lgkmcnt(" #c ")\n\t" SM("8:11", xa, wf)
#define ZERO(r) "v_mfma_f32_32x32x16_bf16 v[" r "], v[8:11], v[8:11], 0\n\t"
    asm volatile("v_mov_b64 v[8:9], 0\n\tv_mov_b64 v[10:11], 0\n\tv_mov_b64 v[16:17], 0\n\tv_mov_b64 v[18:19], 0\n\t"
                 ZERO("32:47") ZERO("48:63") ZERO("64:79") ZERO("80:95") ZERO("96:111") ZERO("112:127")
                 "v_mov_b32 v20, 0x3f803f80\n\tv_mov_b32 v21, v20\n\tv_mov_b64 v[22:23], v[20:21]\n\tv_mov_b32 v13, %[t]\n\tv_mov_b32 v15, %[t]\n\tv_mov_b32 v28, 1\n\t"
                 "v_add_u32 v29, 0x10000, %[wa]\n\ts_movk_i32 s6, 96\n\ts_mov_b32 s7, 1\n\ts_mov_b32 s8, 15\n\ts_mov_b32 s9, %[spin]\n\ts_nop 7\n\t"
                 CALL TR4(32, 33, 34, 35, 36, 37, 38, 39, "%[wa]", 0) TR4(40, 41, 42, 43, 44, 45, 46, 47, "%[wa]", 16384)
                 USE(4, "96:99", "32:39") "s_waitcnt lgkmcnt(0)\n\t" SM("16:19", "20:23", "40:47") SM("8:11", "100:103", "40:47")
                 CALL TR4(48, 49, 50, 51, 52, 53, 54, 55, "%[wa]", 32768) USE(0, "104:107", "48:55")
                 CALL TR4(56, 57, 58, 59, 60, 61, 62, 63, "%[wa]", 49152) "s_mov_b64 exec, 1\n\tds_add_u32 %[fl], v28 offset:20\n\ts_mov_b64 exec, -1\n\t" TS(60, 61)
                 USE(1, "108:111", "56:63")
                 CALL TR4(64, 65, 66, 67, 68, 69, 70, 71, "v29", 0) USE(0, "112:115", "64:71") "s_cmp_eq_u32 %[ns], 5\n\ts_cbranch_scc1 5f\n\t"
                 CALL TR4(72, 73, 74, 75, 76, 77, 78, 79, "v29", 16384) USE(0, "116:119", "72:79") "s_cmp_eq_u32 %[ns], 6\n\ts_cbranch_scc1 5f\n\t"
                 CALL TR4(80, 81, 82, 83, 84, 85, 86, 87, "v29", 32768) USE(0, "120:123", "80:87")
                 CALL TR4(88, 89, 90, 91, 92, 93, 94, 95, "v29", 49152) USE(0, "124:127", "88:95")
                 "5:\n\t" TS(62, 63) "s_cmp_eq_u32 s7, 0\n\ts_cbranch_scc1 4f\n\ts_mov_b32 s8, 16\n"
                 "1:\n\tds_read_b32 v30, %[fl]\n\ts_waitcnt lgkmcnt(0)\n\tv_cmp_le_u32 vcc, s6, v30\n\ts_and_b32 s12, vcc_hi, s7\n\ts_cbranch_scc1 3f\n"
                 "2:\n\ts_andn2_b32 s12, s8, vcc_lo\n\ts_cbranch_scc1 6f\n\ts_setpc_b64 s[10:11]\n"
                 "6:\n\ts_sub_u32 s9, s9, 1\n\ts_cbranch_scc1 4f\n\t" GNAP "\n\ts_branch 1b\n"
                 "3:\n\t" XR("96:99", 0) XR("100:103", 1024) XR("104:107", 2048) XR("108:111", 3072) XR("112:115", 4096) XR("116:119", 5120) XR("120:123", 6144) XR("124:127", 7168)
                 "ds_read_b128 v[24:27], %[pa]\n\t" TS(64, 65) "s_mov_b32 s7, 0\n\t"
                 XU(8, "96:99", "32:39") XU(7, "100:103", "40:47") XU(6, "104:107", "48:55") XU(5, "108:111", "56:63")
                 XU(4, "112:115", "64:71") XU(3, "116:119", "72:79") XU(2, "120:123", "80:87") XU(0, "124:127", "88:95")
                 "s_cmp_eq_u32 s8, 16\n\ts_cbranch_scc0 2b\n"
                 "4:\n\ts_nop 4\n\tv_permlane32_swap_b32 v16, v17\n\tv_add_f32 v16, v16, v17\n\tv_mul_f32 v25, v25, v16\n\tv_mul_f32 v27, v27, v16\n\t"
                 OUT4("v8", "v9", "v10", "v11", "v_fmac_f32 v25, v10, v24\n\tv_fmac_f32 v27, v11, v26\n\tv_fma_f32 v8, v8, v24, v25\n\tv_fma_f32 v9, v9, v26, v27\n\t", "%[ho]")
                 TS(66, 67) "s_nop 1"
                 :: [wa] "v"(wa), [sel] "v"(sel), [so] "v"(so), [fl] "v"(lds0 + FC + 4 * min(l, 4)), [xa] "v"(lds0 + FA + 128 * (l & 7) + 16 * ((2 * kg + (n16 >> 3)) ^ (2 * l & 4))),
                    [pa] "v"(lds0 + FC + 32 + 32 * (kg & 1) + 16 * (kg >> 1)), [ns] "s"(ns), [t] "s"(tag), [rs] "s"(rs), [ho] "s"(O_HG + (6 * cb + 3 * mat + part) * 4096), [spin] "n"(SPIN)
                 : "vcc", "scc", "s6", "s7", "s8", "s9", "s10", "s11", "s12", "memory", G_CLOBBER TSC);
    TSGET(1, 4, 3, 5);
    if (u < 8) {
      const u32 off = O_HG + (2 * kp + (w >> 2)) * 24576 + (w & 3) * 1024 + 16 * l;
      const bool six = !mat && w >= 4;
      const u32 of3 = six ? off : 0x80000000u, tg3 = six ? tag : 0u;
      u32 hw = lds0 + FH + (((l >> 2) & 1) * 4 + ((l >> 3) & 1) * 2) * HP + 64 * w + (l & 48) + 4 * (l & 3);
      u32 hr = lds0 + FH + (l & 7) * HP + 32 * kg + 2 * (l & 8);
      u32 e0 = 0x3f80u, e1 = 0x3f800000u;
      asm volatile("" : "+v"(hw), "+v"(hr));
      asm volatile("" : "+s"(e0), "+s"(e1));
      AWAIT(FC + 4 * (l & 3), 0x8000, "s_sleep 1");
      u32x4 ha[6];
#pragma unroll
      for (int i = 0; i < 6; i++) ha[i] = __builtin_amdgcn_raw_buffer_load_b128(rs, (i % 3 == 2 ? of3 : off) + 4096 * i, 0, HAUX);
      SB();
      AWAIT(FC + 4 * (l & 3), 0x10000, "s_sleep 1");
      STAMP(6);
      u32x8 d0, d1, d2, d3;
      asm volatile(TR4(32, 33, 34, 35, 36, 37, 38, 39, "%[wa]", 0) TR4(40, 41, 42, 43, 44, 45, 46, 47, "%[wa]", 16384)
                   TR4(48, 49, 50, 51, 52, 53, 54, 55, "%[wa]", 32768) TR4(56, 57, 58, 59, 60, 61, 62, 63, "%[wa]", 49152) "s_waitcnt lgkmcnt(0)"
                   : "=&{v[32:39]}"(d0), "=&{v[40:47]}"(d1), "=&{v[48:55]}"(d2), "=&{v[56:63]}"(d3) : [wa] "v"(wa) : "memory");
      SB();
#pragma unroll 1
      for (int spin = 0; spin < SPIN; spin++) {
        bool bad[6];
#pragma unroll
        for (int i = 0; i < 6; i++) { const u32 tg = i % 3 == 2 ? tg3 : tag; bad[i] = ha[i].y != tg || ha[i].w != tg; }
        if (!__any(bad[0] || bad[1] || bad[2] || bad[3] || bad[4] || bad[5])) break;
        asm volatile("s_sleep " STR(HNAP) ::: "memory");
#pragma unroll
        for (int i = 0; i < 6; i++) if (bad[i]) ha[i] = ld16(rs, off + 4096 * i);
      }
      STAMP(7);
      float hv[2][2];
#pragma unroll
      for (int i = 0; i < 2; i++) {
        u32 s2[2];
#pragma unroll
        for (int m = 0; m < 2; m++) {
          const u32 p0 = ha[3 * m][2 * i], p1 = ha[3 * m + 1][2 * i], p2 = ha[3 * m + 2][2 * i];
          s2[m] = pack2(dot2b(p2, e0, dot2b(p1, e0, lo2f(p0))), dot2b(p2, e1, dot2b(p1, e1, hi2f(p0))));
        }
        const u32 sl = pack2(lo2f(s2[0]) * sigm(lo2f(s2[0])), hi2f(s2[0]) * sigm(hi2f(s2[0])));
        hv[i][0] = lo2f(sl) * lo2f(s2[1]); hv[i][1] = hi2f(sl) * hi2f(s2[1]);
      }
      asm volatile("ds_write2_b32 %[hw], %[h0], %[h1] offset1:%[hp]\n\tv_mov_b64 v[8:9], 0\n\tv_mov_b64 v[10:11], 0\n\tv_mov_b32 v13, %[t]\n\tv_mov_b32 v15, %[t]\n\t"
                   "s_waitcnt lgkmcnt(0)\n\ts_barrier\n\t" TS(60, 61)
                   XR("16:19", 0) XR("20:23", 128) XR("24:27", 256) XR("28:31", 384)
                   XU(3, "16:19", "32:39") XU(2, "20:23", "40:47") XU(1, "24:27", "48:55") XU(0, "28:31", "56:63") "s_nop 7\n\t"
                   OUT4("v8", "v9", "v10", "v11", "v_add_f32 v8, v8, v10\n\tv_add_f32 v9, v9, v11\n\t", "%[po]") TS(62, 63)
                   :: [hw] "v"(hw), [h0] "v"(pack2(hv[0][0], hv[1][0])), [h1] "v"(pack2(hv[0][1], hv[1][1])), [hp] "n"(HP / 4), [xa] "v"(hr), [sel] "v"(sel), [so] "v"(so),
                      [t] "s"(tag), [rs] "s"(rs), [po] "s"(O_PG + (8 * kp + ct) * 4096), "{v[32:39]}"(d0), "{v[40:47]}"(d1), "{v[48:55]}"(d2), "{v[56:63]}"(d3)
                   : "memory", D_CLOBBER TSC);
      TSGET(8, 9, 2, 2);
    }
    FLUSH();
    __builtin_amdgcn_endpgm();
  }

  if (w0 >= 8) {
    u32x8 xa0, xa1, xa2, xa3, sa0, sa1;
    u32x4 junk;
    u32 op, so;
    ROLE(p, "", , "={v[100:107]}"(xa0), "={v[108:115]}"(xa1), "={v[116:123]}"(xa2), "={v[124:131]}"(xa3), "={v[132:139]}"(sa0),
         "={v148}"(op), "={v149}"(so), "={v[160:163]}"(junk));
    const int p = w - 8;
    f32x2 sc[4];
    asm volatile("s_waitcnt lgkmcnt(0)" : "+{s44}"(tag), "+{s[48:51]}"(dv)::"memory");
    tag += 1u;
#pragma unroll
    for (int i = 0; i < 4; i++) { sc[i] = f32x2{1.f + lo2f(dv[i]), 1.f + hi2f(dv[i])}; asm volatile("" : "+v"(sc[i])); }
    STAMP(9);
    __builtin_amdgcn_s_setprio(3);
    f32x2 xq[4][8];
    {
      asm volatile("s_mov_b32 m0, %0\n\ts_nop 0" ::"s"(lds0 + OX + 2 * XO + 256 * p));
#pragma unroll
      for (int i = 0; i < 4; i++) {
#define SWAIT(c, r0, r1, r3, r4, r5, r7, ...) do { u32 n_ = SPIN, d_; \
          asm volatile("s_waitcnt vmcnt(" #c ")\n1:\n\tv_xor_b32 %[d], %[t], v" #r1 "\n\tv_bitop3_b32 %[d], %[d], %[t], v" #r3 " bitop3:0xf6\n\t" \
                       "v_bitop3_b32 %[d], %[d], %[t], v" #r5 " bitop3:0xf6\n\tv_bitop3_b32 %[d], %[d], %[t], v" #r7 " bitop3:0xf6\n\t" \
                       "s_sub_u32 %[n], %[n], 1\n\tv_cmp_ne_u32 vcc, 0, %[d]\n\ts_cbranch_scc1 2f\n\ts_cbranch_vccz 2f\n\t" \
                       "buffer_load_dwordx4 v[" #r0 ":" #r3 "], %[so], %[rs], %[k] offen sc1\n\tbuffer_load_dwordx4 v[" #r4 ":" #r7 "], %[so], %[rs], %[k1] offen sc1\n\t" \
                       "s_waitcnt vmcnt(0)\n\ts_branch 1b\n2:" \
                       : [d] "=&v"(d_), [n] "+s"(n_), __VA_ARGS__ \
                       : [t] "s"(tag), [so] "v"(so), [rs] "s"(rs), [k] "s"(4096 * i), [k1] "s"(4096 * i + 4096) : "vcc", "scc", "memory"); } while (0)
        if (i == 0) SWAIT(2, 132, 133, 135, 136, 137, 139, "+{v[132:139]}"(sa0), "+{v[100:107]}"(xa0), "+{v[108:115]}"(xa1), "+{v[160:163]}"(junk));
        if (i == 1) asm volatile("buffer_load_dwordx4 v[140:143], %[so], %[rs], %[k2] offen sc1\n\tbuffer_load_dwordx4 v[144:147], %[so], %[rs], %[k3] offen sc1"
                                 : "=&{v[140:147]}"(sa1) : [so] "v"(so), [rs] "s"(rs), [k2] "s"(8192), [k3] "s"(12288) : "memory");
        if (i == 2) {
          SWAIT(0, 140, 141, 143, 144, 145, 147, "+{v[140:147]}"(sa1), "+{v[116:123]}"(xa2), "+{v[124:131]}"(xa3));
        }
        const u32x8 xs = i == 0 ? xa0 : i == 1 ? xa1 : i == 2 ? xa2 : xa3;
        const u32 sa = i < 2 ? sa0[4 * i] : sa1[4 * i - 8], sb = i < 2 ? sa0[4 * i + 2] : sa1[4 * i - 6];
        if (i == 0) STAMP(3);
        if (i == 2) STAMP(10);
        const f32x2 r2 = {__builtin_amdgcn_rcpf(hi2f(sa)), __builtin_amdgcn_rcpf(hi2f(sb))};
        const f32x2 m2 = {-lo2f(sa) * r2.x, -lo2f(sb) * r2.y};
        f32x2 xx[8], n2[8];
        u32 o[8], fv = i + 1;
#pragma unroll
        for (int c = 0; c < 8; c++) {
          const u32 a = xs[c >> 1], bb = xs[4 + (c >> 1)];
          xx[c] = (c & 1) ? f32x2{hi2f(a), hi2f(bb)} : f32x2{lo2f(a), lo2f(bb)};
        }
        asm volatile("" : "+v"(fv));
        SB();
#pragma unroll
        for (int c = 0; c < 8; c++) n2[c] = pkfma(xx[c], r2, m2);
        SB();
#pragma unroll
        for (int c = 0; c < 8; c++) o[c] = packh(n2[c].x, n2[c].y);
        SB();
#pragma unroll
        for (int c = 0; c < 8; c++) asm volatile("ds_write_addtid_b32 %0 offset:%1" ::"v"(o[c]), "n"(c * XREG + 1024 * i) : "memory");
        FLAG(OXF + 4 * p, fv);
#pragma unroll
        for (int c = 0; c < 8; c++) xq[i][c] = (c & 1) ? pkfmas<1>(n2[c], sc[c >> 1], xx[c]) : pkfmas<0>(n2[c], sc[c >> 1], xx[c]);
        SB();
        if (i == 3) __builtin_amdgcn_s_setprio(PPRIO);
      }
    }
    STAMP(4);
    const rsrc_t ro = __builtin_amdgcn_make_buffer_rsrc((void*)out, 0, 2 * L * D, 0x00020000);
    const int u = 64 * (p & 1) + l;
    const char* yp = smem + OY + 512 * (p >> 1) + 4 * (u ^ ((u >> 4) & 6));
    const char* yq = yp + 4 * CHR;
    u32 ys[8];
    if (p == 0) {
      u32x2 ix;
      LANE(ln);
      const u32 ixo = IXO(ln);
      POLL8(ix, ixo);
      *(u32x2*)(smem + OIX + 8 * (ln & 7)) = ix;
      if (l < 8) atomicOr((u32*)(smem + OMISC) + ((ix.x & 2047u) >> 5), 1u << (ix.x & 31u));
      FLAG(OYP + 32, 8u);
      asm volatile("s_mov_b32 m0, %0\n\ts_nop 0\n\tglobal_load_lds_dword %1, %2" ::"s"(lds0 + OPX), "v"((ix.x & 2047u) * (2 * D) + 16 * g + ((ln >> 1) & 12)), "s"(x) : "memory");
      *(u32x4*)(smem + OIX + 64) = dv;
      STAMP(11);
    }
#pragma unroll
    for (int i = 0; i < 4; i++) {
      if (i == 2 && p == 0) { asm volatile("s_waitcnt vmcnt(5)" ::: "memory"); FLAG(OYP + 36, 8u); }
      AWAIT(OYP + 4 * min(l, 8), 2 * i + 1 + (p >> 1), YNAP);
      if (i == 0) STAMP(5); else if (i == 1) STAMP(6); else if (i == 2) STAMP(7); else STAMP(8);
      u32 y[8];
#pragma unroll
      for (int c = 0; c < 8; c++) y[c] = *(const u32*)((c < 4 ? yp : yq) + (c & 3) * CHR + 1024 * i);
      const u32 bm = *(const u32*)(smem + OMISC + 64 * i + 16 * p + 4 * kg) >> (2 * n16);
      if (i == 0) {
        if (t == 512) st8(rs, O_ARR + b * 8, u32x2{0u, tag});
        const u32x4 a = *(const u32x4*)(smem + OSC), c = *(const u32x4*)(smem + OSC + 16);
#pragma unroll
        for (int k = 0; k < 4; k++) { ys[k] = (u32)__builtin_amdgcn_readfirstlane((int)a[k]); ys[4 + k] = (u32)__builtin_amdgcn_readfirstlane((int)c[k]); }
      }
      SB();
#pragma unroll
      for (int r = 0; r < 2; r++) {
        u32x4 v;
#pragma unroll
        for (int k = 0; k < 4; k++)
          v[k] = pack2(dot2b(y[2 * k], ys[2 * k] << (16 * r), xq[i][2 * k][r]), dot2b(y[2 * k + 1], ys[2 * k + 1] << (16 * r), xq[i][2 * k + 1][r]));
        __builtin_amdgcn_raw_buffer_store_b128(v, ro, op | (bm << (31 - r) & 0x80000000u), (512 * i + r) * (2 * D), OAUX);
        SB();
        if (r) asm volatile("s_nop 1");
        SB();
      }
    }
    asm volatile("" ::"{v[160:163]}"(junk));
    STAMP(13);
    FLUSH();
    __builtin_amdgcn_endpgm();
  }

  u32 pl, pa, pr, pi, ldtv;
  u32x4 tv, fx, junk;
  ROLE(c, "", , "={v[16:19]}"(tv), "={v[20:23]}"(fx), "={v24}"(ldtv), "={v96}"(pl), "={v97}"(pa), "={v68}"(pr), "={v69}"(pi), "={v[160:163]}"(junk));
  char* W = smem + w * CHR;
  __builtin_amdgcn_s_setprio(2);
  STAMP(3);
  const u32x4 zero4 = {0u, 0u, 0u, 0u};
#pragma unroll
  for (int i = 0; i < 7; i++) *(u32x4*)(W + (i < 6 ? TB + 1024 * i : CHR - 1024) + 16 * l) = zero4;
  SB();
  const u32 wb0 = lds0 + w * CHR, xb0 = lds0 + OX + w * XREG;
  asm volatile("s_mov_b32 m0, %0\n\ts_nop 0\n\tds_write_addtid_b32 %1\n\tds_write_addtid_b32 %1 offset:%2" ::"s"(wb0), "v"(0x3c00u), "n"(PB) : "memory");
  typedef LDS char* lp;
  u32 ape = wb0 + (n16 == 12 ? 12 : (n16 ^ 3) - 7 * (n16 >> 3)) * RS + 16 * kg, apo = wb0 + (n16 < 12 ? n16 : n16 - 8) * RS + 16 * kg;
  u32x2 kb = {wb0 + ((15 - n16) & 3) * CPE + 2 * ((15 - n16) & 12) + 16 * kg, wb0 + (((n16 >> 2) & 1) + (n16 & 8) + 2 * (3 - (n16 & 3))) * RS + 16 * kg};
  u32x8 ka = {xb0 + 32 * n16 + 16 * kg, xb0 + 8 * l, wb0 + 8 * l, xb0 + 16 * l, wb0 + 16 * l,
              wb0 + 32 * n16 + 8 * (kg ^ ((l >> 2) & 3)),
              lds0 + OXF + 4 * (l & 3), wb0 + ((15 - n16) & 3) * CPO + 2 * ((15 - n16) & 12) + 8 * kg};
  asm volatile("" : "+{v70}"(ape), "+{v71}"(apo), "+{v[28:35]}"(ka), "+{v[26:27]}"(kb));
  STAMP(4);
  asm volatile("s_waitcnt vmcnt(4)" : "+{v24}"(ldtv), "+{v[160:163]}"(junk));
  float dt = rbt(fexp(hi2f(ldtv)));
  asm volatile("" : "+v"(dt));
  const f32x2 cdt = {rdl(dt * -1.4426950408889634f, 0), rdl(dt * 0.15915494309189535f, 0)};
  dt = rdl(dt, 0);
  SB();
  STAMP(10);
  STAMPC(11);
  {
    u32 e, tmp, o0, o1, of;
    asm volatile(GEN_ASM
                 : [e] "=&s"(e), [t] "=&s"(tmp), "=&{v41}"(o0), "=&{v46}"(o1), "=&{v48}"(of), "+{v68}"(pr), "+{v69}"(pi), "+{v70}"(ape),
                   "+{v71}"(apo), "+{v96}"(pl), "+{v97}"(pa), "+{v[28:35]}"(ka), "+{v[26:27]}"(kb), "+{v[16:19]}"(tv), "+{v[20:23]}"(fx)
                 : [w] "s"(wb0), [c] "s"(cdt), [k] "s"(f32x2{1.4426950408889634f, 256.f})
                 : "memory", "scc", GEN_CLOBBER);
    STAMP(1);
    STAMPC(12);
    const u32 k0 = (u32)__builtin_amdgcn_readlane((int)o0, 3), k256 = (u32)__builtin_amdgcn_readlane((int)o1, 60);
    const u32 k512 = (u32)__builtin_amdgcn_readlane((int)of, 51), k1024 = (u32)__builtin_amdgcn_readlane((int)of, 55);
    *(LDS u16*)(size_t)(wb0 + (tv.z & 0xffffu)) = (u16)__builtin_amdgcn_perm((k512 & 0xffffu) | (k1024 << 16), (k0 >> 16) | (k256 << 16), tv.w);
    *(u32*)(smem + OSC + 4 * w) = __float_as_uint(__uint_as_float((e - 5) << 23) * dt) >> 16;
    fx &= ((k1024 & 0xffffu) | (k1024 << 16)) ^ 0x80008000u;
  }
  const u32 axb = ka[0], axu = ka[1], awb = ka[2], axq = ka[3], awo = ka[4], ay = ka[5], axf = ka[6], atf = ka[7], ate = kb[0];
#define FRAG(o0, o1) mkh(*(const volatile LDS u32x2*)(tb + (o0)), *(const volatile LDS u32x2*)(tb + (o1)))
  f16x8 FR[9], F1[4][2], F2[2][3], F3[4];
  const LDS char* tb = (lp)(size_t)ate;
#pragma unroll
  for (int c = 0; c < 9; c++) FR[c] = FRAG(64 * c, 64 * c + 8);
  tb = (lp)(size_t)atf + TB;
#pragma unroll
  for (int p = 0; p < 4; p++)
#pragma unroll
    for (int dl = 0; dl < 2; dl++) F1[p][dl] = FRAG(2 * (2 * C1 * p + 16 - 16 * dl), 2 * (2 * C1 * p + C1 + 16 - 16 * dl));
#pragma unroll
  for (int p = 0; p < 2; p++)
#pragma unroll
    for (int dl = 0; dl < 3; dl++) F2[p][dl] = FRAG(2 * (T2B + 2 * C2 * p + 32 - 16 * dl), 2 * (T2B + 2 * C2 * p + C2 + 32 - 16 * dl));
  *(LDS u32x4*)(size_t)(wb0 + (tv.z >> 16)) = zero4;
  STAMP(2);
  const u32 wbk = (u32)(b >> 3) * 16 + w;
#define WBL2() asm volatile("s_cmp_lg_u32 %0, %1\n\ts_cbranch_scc1 1f\n\tbuffer_wbl2 sc1\n1:" ::"s"(wbk), "n"(WBW) : "scc", "memory")
#define XWAIT(n) do { _Pragma("unroll 1") for (int spin = 0; spin < SPIN; spin++) { \
      if (__all(*(const volatile LDS u32*)(size_t)axf >= (u32)(n))) break; \
      NAP(); } SB(); asm volatile("" ::: "memory"); } while (0)
  XWAIT(1);
  STAMP(5);
  STAMPC(14);
  asm volatile("s_cmp_lt_u32 %0, 4\n\ts_cbranch_scc0 1f\n\ts_setprio 1\n1:" ::"s"(w) : "scc");
  {
    const lp xb = (lp)(size_t)axb, xu = (lp)(size_t)axu + 2 * XO, wb = (lp)(size_t)awb + 32, xq = (lp)(size_t)axq + 512;
    const lp wo = (lp)(size_t)awo + 16, Y = (lp)(size_t)ay + OY;
    f32x4 acc[8];
    u32x4 A[NOPS];
    u32x4 Q1, Q2, W7, W9;
    u32 QZ = 0u, Uo[8], P2[8], P4[8], W3[8], W5[8], xf = 0u, fl = 0u, fad = lds0 + OYP + 4 * w;
    u32x2 yv;
    asm volatile("" : "+v"(fad));
    auto winl = [&](auto P_) __attribute__((always_inline)) {
      constexpr int pp = decltype(P_)::value;
      Q1 = *(const LDS u32x4*)(xq + 1024 * pp);
      if (pp < 2) { Q2 = *(const LDS u32x4*)(xq + 1024 * pp + 16); QZ = *(const LDS u32*)(xq + 1024 * pp - 4); }
      else { const u32x2 v = *(const LDS u32x2*)(xq + 1024 * pp + 16); Q2.x = v.x; Q2.y = v.y; }
    };
    auto win = [&](auto K_) __attribute__((always_inline)) {
      constexpr int k = decltype(K_)::value;
      const u32 Dd[8] = {QZ, Q1.x, Q1.y, Q1.z, Q1.w, Q2.x, Q2.y, Q2.z};
      if constexpr (k < 5) Uo[2 + k] = __builtin_amdgcn_alignbit(Dd[2 + k], Dd[1 + k], 16);
      else if constexpr (k < 9) P2[k - 3] = hadd(Dd[k - 3], Uo[k - 3]);
      else if constexpr (k < 13) W3[k - 7] = hadd(P2[k - 7], Uo[k - 6]);
      else if constexpr (k == 13) P2[6] = hadd(Dd[6], Uo[6]);
      else if constexpr (k < 18) P4[k - 11] = hadd(P2[k - 11], P2[k - 12]);
      else if constexpr (k < 22) W5[k - 16] = hadd(P4[k - 15], Dd[k - 17]);
      else if constexpr (k == 22) Uo[1] = __builtin_amdgcn_alignbit(Dd[1], Dd[0], 16);
      else if constexpr (k == 23) Uo[7] = __builtin_amdgcn_alignbit(Dd[7], Dd[6], 16);
      else if constexpr (k == 24) P2[1] = hadd(Dd[1], Uo[1]);
      else if constexpr (k == 25) P2[7] = hadd(Dd[7], Uo[7]);
      else if constexpr (k == 26) P4[2] = hadd(P2[2], P2[1]);
      else if constexpr (k == 27) P4[7] = hadd(P2[7], P2[6]);
      else if constexpr (k == 28) W3[6] = hadd(P2[6], Uo[7]);
      else if constexpr (k == 29) W5[1] = hadd(P4[2], Dd[0]);
      else if constexpr (k < 34) W7[k - 30] = hadd(W3[k - 27], P4[k - 28]);
      else W9[k - 34] = hadd(P4[k - 30], W5[k - 33]);
      if constexpr (k == 13) asm volatile("" : "+v"(P2[6]));
      if constexpr (k > 13 && k < 18) asm volatile("" : "+v"(P4[k - 11]));
      if constexpr (k >= 18 && k < 22) asm volatile("" : "+v"(W5[k - 16]));
    };
    auto loads = [&](auto M_) __attribute__((always_inline)) {
      constexpr int m = decltype(M_)::value;
      sfor<0, 2>([&](auto N_) __attribute__((always_inline)) {
        constexpr int mm = m + LA + decltype(N_)::value;
        if constexpr (mm < NSLOT) {
          constexpr int s = OPS.op[mm], k = OPS.k[s], T = OPS.a[s], p = OPS.b[s];
          if constexpr (OPS.first[s] == mm) {
            if constexpr (k == 0) { if (OPS.ld[s][1] == m) A[s] = *(const LDS u32x4*)(xb + 512 * T + 64 * p); }
            else {
              if (OPS.ld[s][0] == m) {
                const u32x2 a = trl(k == 3 ? wb + OW7 + 512 * T - 16 : k == 2 ? wb + OW3 + 512 * T - 2 * (4 + 8 * p) : xu + 512 * T - 2 * (2 + 4 * p));
                A[s].x = a.x; A[s].y = a.y;
              }
              if (OPS.ld[s][1] == m) {
                const u32x2 c = trl(k == 3 ? wb + OW9 + 512 * T - 32 : k == 2 ? wb + OW5 + 512 * T - 2 * (8 + 8 * p) : wb + OW3 + 512 * T - 2 * (4 + 4 * p));
                A[s].z = c.x; A[s].w = c.y;
              }
            }
          }
        }
      });
    };
    constexpr int FT[8] = {0, OPS.first[opat(0, 1, 0)] + 1, OPS.first[opat(0, 2, 0)] + 1, OPS.first[opat(2, 0, 0)] + 1, OPS.first[opat(0, 1, 0)] + 2,
                           OPS.first[opat(0, 2, 0)] + 2, OPS.first[opat(0, 3, 0)] + 2, OPS.first[opat(0, 4, 0)] + 2};
    loads(IC<-LA>{});
    winl(IC<0>{});
    sfor<1 - LA, 0>(loads);
    auto step = [&](auto M_) __attribute__((always_inline)) {
      constexpr int m = decltype(M_)::value, s = OPS.op[m], u = m - OPS.first[s], k = OPS.k[s], T = OPS.a[s], p = OPS.b[s];
      SB();
      const f16x8 a = ash(A[s]);
#define MM(I, x_, y_) do { if constexpr (m == FT[I]) asm volatile("v_mfma_f32_16x16x32_f16 %0, %1, %2, 0" : "=&v"(acc[I]) : "v"(x_), "v"(y_)); \
      else asm volatile("v_mfma_f32_16x16x32_f16 %0, %1, %2, %0" : "+v"(acc[I]) : "v"(x_), "v"(y_)); } while (0)
      if constexpr (k == 0) {
        if constexpr (p == 0 && T > 0 && u == 0) MM(T - 1, FR[8], a);
        else if constexpr (u == 2) MM(T + 3, fx, a);
        else MM(T, FR[p], a);
      }
      else if constexpr (k == 3) MM(T + 4 + u, a, F3[u]);
      else if constexpr (k == 2) MM(T + 2 + u, a, F2[p][u]);
      else MM(T + 1 + u, a, F1[p][u]);
      SB();
      if constexpr (m == OPS.end[PH / 100 % 10] + 5) asm volatile("s_cmp_lt_u32 %0, 4\n\ts_cbranch_scc1 1f\n\ts_setprio 1\n1:" ::"s"(w) : "scc");
      if constexpr (m == OPS.end[PH / 10 % 10] + 5) asm volatile("s_cmp_lt_u32 %0, 4\n\ts_cbranch_scc0 1f\n\ts_setprio 0\n1:" ::"s"(w) : "scc");
      if constexpr (m == OPS.end[PH % 10] + 5) asm volatile("s_setprio 0");
      sfor<0, 3>([&](auto N_) __attribute__((always_inline)) {
        constexpr int n = decltype(N_)::value;
        if (m == XC[n]) { if (!__all(xf >= n + 2u)) XWAIT(n + 2); }
        if (m == XC[n] - 4) xf = *(const volatile LDS u32*)(size_t)axf;
      });
      sfor<0, 7>([&](auto I_) __attribute__((always_inline)) {
        constexpr int I = decltype(I_)::value;
        if (m == OPS.end[I] + 4) {
          *(LDS u32x2*)(Y + 512 * I) = yv;
          asm volatile("ds_write_b32 %0, %1" ::"v"(fad), "v"(fl) : "memory");
          if (WBM >> I & 1) WBL2();
          if (I == 0) STAMP(6);
          if (I == 3) STAMP(7);
        }
      });
      sfor<0, 4>([&](auto P_) __attribute__((always_inline)) {
        constexpr int pp = decltype(P_)::value;
#if !(ABL & 4)
        if (m == wslot(pp, 12) + 1) *(LDS u32x4*)(wo + 1024 * pp + OW3) = u32x4{W3[2], W3[3], W3[4], W3[5]};
        if (pp < 3 && m == wslot(pp, 21) + 1) *(LDS u32x4*)(wo + 1024 * pp + OW5) = u32x4{W5[2], W5[3], W5[4], W5[5]};
        if (pp < 2 && m == wslot(pp, 33) + 1) *(LDS u32x4*)(wo + 1024 * pp + OW7) = W7;
        if (pp < 2 && m == wslot(pp, 37) + 1) *(LDS u32x4*)(wo + 1024 * pp + OW9) = W9;
#endif
        if (m == WL[pp]) winl(P_);
        if (m == OPS.first[opat(3, 0, 0)] - 8 + pp) F3[pp] = FRAG(2 * (T3B + 48 - 16 * pp), 2 * (T3B + C3 + 48 - 16 * pp));
      });
      loads(M_);
      SB();
      sfor<0, 4>([&](auto P_) __attribute__((always_inline)) {
        constexpr int pp = decltype(P_)::value;
        if (pp < 2 && m == wslot(pp, 3)) asm volatile("" : "+v"(Q2));
        if (!(ABL & 6)) sfor<0, WN[pp]>([&](auto K_) __attribute__((always_inline)) { if constexpr (wslot(pp, decltype(K_)::value) == m) win(K_); });
      });
      sfor<0, 7>([&](auto I_) __attribute__((always_inline)) {
        constexpr int I = decltype(I_)::value;
        if (m == OPS.end[I] + 3) {
          yv = u32x2{pack2(acc[I][0], acc[I][1]), pack2(acc[I][2], acc[I][3])};
          fl = I + 1;
          asm volatile("" : "+v"(fl), "+v"(yv));
        }
      });
    };
    sfor<0, NSLOT>(step);
    SB();
    asm volatile("s_nop 7\n\ts_nop 1" : "+v"(acc[7]));
    *(LDS u32x2*)(Y + 512 * 7) = u32x2{pack2(acc[7][0], acc[7][1]), pack2(acc[7][2], acc[7][3])};
    FLAG(OYP + 4 * w, 8u);
    if (WBM >> 7 & 1) WBL2();
  }
  STAMP(13);
  STAMPC(15);
  if (w != 0) {
    FLUSH();
    __builtin_amdgcn_endpgm();
  }
  {
    __builtin_amdgcn_s_setprio(0);
    LANE(ln);
    const u32 po = O_PG + 256 * g + 16 * (ln & 15) + 32768 * (ln >> 4), p3 = ln < 32 ? po + 3 * 131072 : 0x80000000u;
    u32x4 pv[4];
#pragma unroll
    for (int q = 0; q < 4; q++) pv[q] = ld16(rs, q < 3 ? po + 131072 * q : p3);
    SB();
    {
      const char* fp = smem + OYP + 4 * min(ln, 9);
#pragma unroll 1
      for (int spin = 0; spin < SPIN; spin++) {
        if (__all(*(const volatile LDS u32*)(LDS char*)fp >= 8u)) break;
        asm volatile("s_sleep 1" ::: "memory");
      }
      asm volatile("" ::: "memory");
    }
    const int c = 2 * (ln & 3) + (ln >> 5), tk = (ln & 4) + ((ln >> 2) & 2) + ((ln >> 4) & 1);
    const u32x2 ix = *(const u32x2*)(smem + OIX + 8 * tk);
    const u32 px = *(const u16*)(smem + OPX + 32 * (c >> 1) + 4 * tk + 2 * (c & 1)), pd = *(const u16*)(smem + OIX + 64 + 2 * c);
    tag = (u32)__builtin_amdgcn_readfirstlane((int)ix.y);
    const int prow = (int)(ix.x & 2047u), v = prow >> 1;
    float rsel = rbt(sigm(hi2f(ix.x)));
    const float yl = lo2f(*(const u16*)(smem + OY + CHR * c + 4 * (v ^ ((v >> 4) & 6)) + 2 * (prow & 1)));
    const float ql = hlo(*(const u16*)(smem + OX + 2 * XO + c * XREG + 2 * prow));
    float e = lo2f(px) + yl * lo2f(*(const u32*)(smem + OSC + 4 * c)) + ql * (1.f + lo2f(pd));
    GL u16* op = out + (size_t)prow * D + 8 * g + c;
    asm volatile("" : "+v"(e), "+v"(op), "+v"(rsel));
    STAMP(8);
#pragma unroll 1
    for (int spin = 0; spin < SPIN; spin++) {
      bool ok[4];
#pragma unroll
      for (int q = 0; q < 4; q++) ok[q] = __all((pv[q].y == tag && pv[q].w == tag) || (q == 3 && ln >= 32));
      if (ok[0] && ok[1] && ok[2] && ok[3]) break;
#pragma unroll
      for (int q = 0; q < 4; q++) if (!ok[q]) pv[q] = ld16(rs, q < 3 ? po + 131072 * q : p3);
    }
    STAMP(9);
    float s[4] = {};
#pragma unroll
    for (int q = 0; q < 4; q++) { s[0] += lo2f(pv[q].x); s[1] += hi2f(pv[q].x); s[2] += lo2f(pv[q].z); s[3] += hi2f(pv[q].z); }
    swap32(s[0], s[2]); swap32(s[1], s[3]);
    s[0] += s[2]; s[1] += s[3];
    swap16(s[0], s[1]);
    const float upd = rb(rb(s[0] + s[1]) * rsel);
    *op = (u16)(pack2(e + upd, 0.f) & 0xffffu);
    STAMP(4);
  }
  FLUSH();
  __builtin_amdgcn_endpgm();
}

extern "C" void kernel_launch(void* const* d_in, const int* in_sizes, int n_in, void* d_out, int out_size,
                              void* d_ws, size_t ws_size, hipStream_t stream) {
  const u16* const* in = (const u16* const*)d_in;
  fused<<<256, 768, 0, stream>>>(in[0], in[1], in[2], in[3], in[4], in[5], in[6], in[8], in[9], in[10], in[11],
                                 (u16*)d_out, (char*)d_ws);
}
```

```cpp
#include <hip/hip_runtime.h>
#include <stdint.h>

#define STR2(x) #x
#define STR(x) STR2(x)
#define LA 4
#define WL2 44
#define WL3 100
#define PH 1246
#define ABL 0
#define PPRIO 0
#define CPRIO 1
#define GDELAY 0
#define ROT 1
#define PRE 2
#define GNT 0
#define OAUX 0
#define LNT 1
#define PNT ""
#define LNNT ""
#define PRENT ""
#define PFN 15
#define PFO 1024
#define DBAR 2
#define THR 1
#define QD 11
#define HNAP0 4
#define HNAP 2
#define KR 3
#define KI 2
#define QE 11
#define QA 5
#define NA 0
#define CH 0
#define G0E 0
#define DDL 0
#define WBM 0
#define WBW 1
#define HQ 3
#define RSL 5
#define DPF 1
#define HELP 0
#define WARM 0
#define HPI 0
#define Q0 6
#define NP 10
#define HW 28
#define NE 0
#define YNAP "s_sleep 2"
#define QS 7
#define QAS 6
#define RAMP 4
#define GNAP "s_sleep 1"
#define HOLD 8
#define DPRIO 3
#define HAUX 16
#define DPO 128
#define TH 620
#define CLD 300
#define IXI 1
#define LPRIO 0
#define DPI 2
#define PDROP 3
#define TE 0
#define CNAP 2
#define XI 1
#define TSG 290
#define SSL 5
#define TW 310
#define TL 4352
#define CODE_KB 24
typedef unsigned short u16;
typedef unsigned int u32;
typedef __attribute__((ext_vector_type(8))) __bf16 bf16x8;
typedef __attribute__((ext_vector_type(2))) __bf16 bf16x2;
typedef __attribute__((ext_vector_type(8))) _Float16 f16x8;
typedef __attribute__((ext_vector_type(2))) _Float16 f16x2;
typedef __attribute__((ext_vector_type(4))) short v4i16;
typedef __attribute__((ext_vector_type(4))) float f32x4;
typedef __attribute__((ext_vector_type(2))) float f32x2;
typedef __attribute__((ext_vector_type(4))) u32 u32x4;
typedef __attribute__((ext_vector_type(2))) u32 u32x2;
typedef __attribute__((ext_vector_type(8))) u32 u32x8;
typedef __attribute__((ext_vector_type(2), aligned(4))) u32 u32x2_a4;
typedef __amdgpu_buffer_rsrc_t rsrc_t;
#define LDS __attribute__((address_space(3)))

#define TLARG
#define TLVAL
#define STAMP(I)
#define STAMPC(I)
#define FLUSH()

constexpr int L = 2048, D = 1024, NM = 128, F = 3584;
constexpr u32 O_ARR = 0x1000, O_CLR = 0x2000, O_RG = 0x4000, O_SG = 0x8000, O_IDX = 0x10000, O_HG = 0x20000,
              O_PG = 0xd0000, O_TOP = 0x140000, TOPS = 8 * TL, WS_END = O_TOP + 2 * TOPS;
constexpr int SPIN = 1 << 16;

__device__ __forceinline__ float lo2f(u32 w) { return __uint_as_float(w << 16); }
__device__ __forceinline__ float hi2f(u32 w) { return __uint_as_float(w & 0xffff0000u); }
__device__ __forceinline__ u32 pack2(float a, float b) {
  bf16x2 r = {(__bf16)a, (__bf16)b};
  return __builtin_bit_cast(u32, r);
}
__device__ __forceinline__ u32 packh(float a, float b) {
  f16x2 r = {(_Float16)a, (_Float16)b};
  return __builtin_bit_cast(u32, r);
}
__device__ __forceinline__ float rbt(float f) { return __uint_as_float(pack2(0.f, f)); }
__device__ __forceinline__ float rb(float f) {
  float r;
  asm("v_cvt_pk_bf16_f32 %0, 0, %1" : "=v"(r) : "v"(f));
  return r;
}
__device__ __forceinline__ u32 hadd(u32 a, u32 b) {
  return __builtin_bit_cast(u32, __builtin_bit_cast(f16x2, a) + __builtin_bit_cast(f16x2, b));
}
__device__ __forceinline__ float hlo(u32 a) { return (float)__builtin_bit_cast(f16x2, a).x; }
__device__ __forceinline__ float hhi(u32 a) { return (float)__builtin_bit_cast(f16x2, a).y; }

template <int CTRL, int RM>
__device__ __forceinline__ float dppf(float v) {
  return __builtin_bit_cast(float, __builtin_amdgcn_update_dpp(0, __builtin_bit_cast(int, v), CTRL, RM, 0xf, true));
}
template <int CTRL, int RM>
__device__ __forceinline__ u32 dppu(u32 v) {
  return (u32)__builtin_amdgcn_update_dpp(0, (int)v, CTRL, RM, 0xf, true);
}
__device__ __forceinline__ float rdl(float v, int i) {
  return __builtin_bit_cast(float, __builtin_amdgcn_readlane(__builtin_bit_cast(int, v), i));
}
__device__ __forceinline__ u32 wmaxu(u32 v) {
  v = max(v, dppu<0x111, 0xf>(v)); v = max(v, dppu<0x112, 0xf>(v)); v = max(v, dppu<0x114, 0xf>(v));
  v = max(v, dppu<0x118, 0xf>(v)); v = max(v, dppu<0x142, 0xa>(v)); v = max(v, dppu<0x143, 0xc>(v));
  return (u32)__builtin_amdgcn_readlane((int)v, 63);
}
__device__ __forceinline__ void swap32(float& a, float& b) {
  auto r = __builtin_amdgcn_permlane32_swap(__float_as_uint(a), __float_as_uint(b), false, false);
  a = __uint_as_float(r[0]); b = __uint_as_float(r[1]);
}
__device__ __forceinline__ void swap16(float& a, float& b) {
  auto r = __builtin_amdgcn_permlane16_swap(__float_as_uint(a), __float_as_uint(b), false, false);
  a = __uint_as_float(r[0]); b = __uint_as_float(r[1]);
}
__device__ __forceinline__ u32x2 trread(const char* p) {
  v4i16 r = __builtin_amdgcn_ds_read_tr16_b64_v4i16((LDS v4i16*)p);
  return __builtin_bit_cast(u32x2, r);
}
__device__ __forceinline__ u32x2 trl(LDS char* p) { return __builtin_bit_cast(u32x2, __builtin_amdgcn_ds_read_tr16_b64_v4i16((LDS v4i16*)p)); }
__device__ __forceinline__ f16x8 mkh(u32x2 a, u32x2 b) {
  u32x4 v = {a.x, a.y, b.x, b.y};
  return __builtin_bit_cast(f16x8, v);
}
__device__ __forceinline__ bf16x8 mkb(u32x2 a, u32x2 b) {
  u32x4 v = {a.x, a.y, b.x, b.y};
  return __builtin_bit_cast(bf16x8, v);
}
__device__ __forceinline__ bf16x8 asb(u32x4 v) { return __builtin_bit_cast(bf16x8, v); }
__device__ __forceinline__ f16x8 ash(u32x4 v) { return __builtin_bit_cast(f16x8, v); }
#define MFMAB(a, b, c) __builtin_amdgcn_mfma_f32_16x16x32_bf16(a, b, c, 0, 0, 0)
#define MFMAH(a, b, c) __builtin_amdgcn_mfma_f32_16x16x32_f16(a, b, c, 0, 0, 0)

__device__ __forceinline__ u32x4 ld16(rsrc_t r, u32 off) { return __builtin_amdgcn_raw_buffer_load_b128(r, off, 0, 16); }
__device__ __forceinline__ u32x2 ld8(rsrc_t r, u32 off) { return __builtin_amdgcn_raw_buffer_load_b64(r, off, 0, 16); }
__device__ __forceinline__ void st16(rsrc_t r, u32 off, u32x4 v) { __builtin_amdgcn_raw_buffer_store_b128(v, r, off, 0, 16); }
__device__ __forceinline__ void st8(rsrc_t r, u32 off, u32x2 v) { __builtin_amdgcn_raw_buffer_store_b64(v, r, off, 0, 16); }
#define NAP() asm volatile("s_sleep 1" ::: "memory")
#define SB() __builtin_amdgcn_sched_barrier(0)
#define LBAR() asm volatile("s_waitcnt lgkmcnt(0)\n\ts_barrier" ::: "memory")

__device__ __forceinline__ float fexp(float v) { return __builtin_amdgcn_exp2f(v * 1.4426950408889634f); }
__device__ __forceinline__ float dot2b(u32 a, u32 b, float c) {
  return __builtin_amdgcn_fdot2_f32_bf16(__builtin_bit_cast(bf16x2, a), __builtin_bit_cast(bf16x2, b), c, false);
}
__device__ __forceinline__ float sigm(float v) { return __builtin_amdgcn_rcpf(1.f + __builtin_amdgcn_exp2f(-1.4426950408889634f * v)); }

constexpr int RS = 288, PB = 16 * RS;
constexpr int CPE = 1216, TB = 2 * PB, CPO = 832;
constexpr int T2B = 4, T3B = 224, C2 = 52, C3 = 84;
constexpr int OY = 0, CHR = TB + 4 * CPO;
constexpr int OX = 8 * CHR, XREG = 4640, XO = 266;
constexpr int OMISC = OX + 8 * XREG;
constexpr int OYP = OMISC + 256, OXF = OMISC + 320, OCN = OMISC + 336, OSC = OMISC + 352, OIX = OMISC + 384;
constexpr int OPX = OMISC + 512, OB = OMISC + 768, SMEM = 148480;
constexpr int FA = 131072, FH = FA + 8192, HP = 576, FC = FH + 8 * HP;
static_assert(FC + 1024 <= SMEM && OB + 4096 <= SMEM, "lds map");

struct Tab { u32 v[6][64][4]; };
constexpr Tab mktab() {
  Tab T = {};
  for (int l = 0; l < 64; l++) {
    const int n16 = l & 15, kg = l >> 4, pc = ((n16 >> 2) & 1) + (n16 & 8) + 2 * (3 - (n16 & 3)), odd = kg >> 1, s = l & 3;
    const int e0 = 534 - 258 - 128 - 8 * pc - 4 * (kg & 1) - odd;
    const int ez = ((l >> 3) & 3) * CPE + ((l & 4) ? 1056 - 64 : 0) + 16 * (l & 7);
    const int b2 = T2B + 44 + C2 * ((n16 & 1) + 2 * (kg & 1)) - 8 * (n16 & 2), b3 = T3B + 60 + C3 * (kg & 1) - 16 * s;
    const int to = TB + 2 * ((n16 < 8 ? b2 : b3) - 4 * odd - 8) + ((l & 4) ? 0 : 2 * CPO - 4);
    const int q = (l >> 2) & 7, tap = q == 0 ? ((s & 1) ? 0 : 256) : q == 1 ? 257 : 509 + q;
    const int sp = q < 6 ? s * (CPE - 2) + 2 * (537 - tap) : TB + s * (CPO - 2) + 2 * (T2B + 15);
    T.v[0][l][0] = (2 * e0 + (2 - odd) * (CPE - 2)) | (2 * e0 + 3 * odd * (CPE - 2)) << 16;
    T.v[0][l][1] = ez | to << 16;
    T.v[0][l][2] = sp;
    T.v[0][l][3] = q >= 6 ? 0x0706u : q >= 2 ? 0x0504u : tap ? 0x0302u : 0x0100u;
    for (int k = 0; k < 8; k++) {
      const int d = 8 * kg + k - n16;
      if (d == 13) T.v[1][l][k >> 1] |= 0xffffu << 16 * (k & 1);
      for (int h = 1; h <= 4; h++)
        if (d >= (h == 1 ? 14 : 10) - h && d <= (h == 1 ? 14 : 10) + h) T.v[1 + h][l][k >> 1] |= 0x3c00u << 16 * (k & 1);
    }
  }
  return T;
}
extern "C" __device__ const __attribute__((aligned(128), used, visibility("hidden"))) Tab lane_tab = mktab();

#define GEN_ASM \
  "s_waitcnt vmcnt(2)\n\tv_lshlrev_b32 v56, 16, v96\n\t" \
  "v_and_b32 v57, 0xffff0000, v96\n\tv_pk_mul_f32 v[56:57], %[k], v[56:57] op_sel_hi:[0,1]\n\t" \
  "v_lshlrev_b32 v49, 16, v97\n\tv_exp_f32 v56, v56\n\t" \
  "v_exp_f32 v57, v57\n\tv_and_b32 v51, 0xffff0000, v97\n\t" \
  "v_cvt_pk_bf16_f32 v56, v56, v57\n\tv_lshlrev_b32 v48, 16, v56\n\t" \
  "v_and_b32 v50, 0xffff0000, v56\n\tv_pk_mul_f32 v[48:49], %[c], v[48:49]\n\t" \
  "v_pk_mul_f32 v[50:51], %[c], v[50:51]\n\tv_pk_mul_f32 v[98:99], %[k], v[48:49] op_sel:[1,0]\n\t" \
  "v_pk_mul_f32 v[100:101], %[k], v[50:51] op_sel:[1,0]\n\tv_fract_f32 v49, v49\n\t" \
  "v_fract_f32 v51, v51\n\tv_exp_f32 v48, v48\n\t" \
  "v_exp_f32 v50, v50\n\tv_cos_f32 v40, v49\n\t" \
  "v_sin_f32 v41, v49\n\tv_cos_f32 v42, v51\n\t" \
  "v_sin_f32 v43, v51\n\tv_pk_mul_f32 v[40:41], v[40:41], v[48:49] op_sel_hi:[1,0]\n\t" \
  "v_pk_mul_f32 v[42:43], v[42:43], v[50:51] op_sel_hi:[1,0]\n\tv_pk_mul_f32 v[48:49], v[40:41], v[40:41] op_sel:[1,1] op_sel_hi:[1,0] neg_lo:[1,0]\n\t" \
  "v_pk_mul_f32 v[50:51], v[42:43], v[42:43] op_sel:[1,1] op_sel_hi:[1,0] neg_lo:[1,0]\n\tv_pk_fma_f32 v[48:49], v[40:41], v[40:41], v[48:49] op_sel:[0,0,0] op_sel_hi:[0,1,1]\n\t" \
  "v_pk_fma_f32 v[50:51], v[42:43], v[42:43], v[50:51] op_sel:[0,0,0] op_sel_hi:[0,1,1]\n\tv_cvt_pk_f16_f32 v72, v40, -v41\n\t" \
  "v_cvt_pk_f16_f32 v73, v42, -v43\n\tv_pk_mul_f32 v[52:53], v[48:49], v[40:41] op_sel:[1,1] op_sel_hi:[1,0] neg_lo:[1,0]\n\t" \
  "v_pk_mul_f32 v[54:55], v[50:51], v[42:43] op_sel:[1,1] op_sel_hi:[1,0] neg_lo:[1,0]\n\tv_pk_fma_f32 v[52:53], v[48:49], v[40:41], v[52:53] op_sel:[0,0,0] op_sel_hi:[0,1,1]\n\t" \
  "v_pk_fma_f32 v[54:55], v[50:51], v[42:43], v[54:55] op_sel:[0,0,0] op_sel_hi:[0,1,1]\n\tv_pk_mul_f32 v[56:57], v[48:49], v[48:49] op_sel:[1,1] op_sel_hi:[1,0] neg_lo:[1,0]\n\t" \
  "v_pk_mul_f32 v[58:59], v[50:51], v[50:51] op_sel:[1,1] op_sel_hi:[1,0] neg_lo:[1,0]\n\tv_pk_fma_f32 v[56:57], v[48:49], v[48:49], v[56:57] op_sel:[0,0,0] op_sel_hi:[0,1,1]\n\t" \
  "v_pk_fma_f32 v[58:59], v[50:51], v[50:51], v[58:59] op_sel:[0,0,0] op_sel_hi:[0,1,1]\n\tv_cvt_pk_f16_f32 v74, v48, -v49\n\t" \
  "v_cvt_pk_f16_f32 v75, v50, -v51\n\tv_cvt_pk_f16_f32 v76, v52, -v53\n\t" \
  "v_cvt_pk_f16_f32 v77, v54, -v55\n\tv_pk_mul_f32 v[52:53], v[56:57], v[48:49] op_sel:[1,1] op_sel_hi:[1,0] neg_lo:[1,0]\n\t" \
  "v_pk_mul_f32 v[54:55], v[58:59], v[50:51] op_sel:[1,1] op_sel_hi:[1,0] neg_lo:[1,0]\n\tv_pk_fma_f32 v[52:53], v[56:57], v[48:49], v[52:53] op_sel:[0,0,0] op_sel_hi:[0,1,1]\n\t" \
  "v_pk_fma_f32 v[54:55], v[58:59], v[50:51], v[54:55] op_sel:[0,0,0] op_sel_hi:[0,1,1]\n\tv_pk_mul_f32 v[60:61], v[56:57], v[56:57] op_sel:[1,1] op_sel_hi:[1,0] neg_lo:[1,0]\n\t" \
  "v_pk_mul_f32 v[62:63], v[58:59], v[58:59] op_sel:[1,1] op_sel_hi:[1,0] neg_lo:[1,0]\n\tv_pk_fma_f32 v[60:61], v[56:57], v[56:57], v[60:61] op_sel:[0,0,0] op_sel_hi:[0,1,1]\n\t" \
  "v_pk_fma_f32 v[62:63], v[58:59], v[58:59], v[62:63] op_sel:[0,0,0] op_sel_hi:[0,1,1]\n\tv_cvt_pk_f16_f32 v78, v56, -v57\n\t" \
  "v_cvt_pk_f16_f32 v79, v58, -v59\n\tv_cvt_pk_f16_f32 v82, v52, -v53\n\t" \
  "v_cvt_pk_f16_f32 v83, v54, -v55\n\tv_pk_mul_f16 v80, v78, v72 op_sel:[1,1] op_sel_hi:[1,0] neg_lo:[1,0]\n\t" \
  "v_pk_mul_f16 v81, v79, v73 op_sel:[1,1] op_sel_hi:[1,0] neg_lo:[1,0]\n\tv_pk_fma_f16 v80, v78, v72, v80 op_sel:[0,0,0] op_sel_hi:[0,1,1]\n\t" \
  "v_pk_fma_f16 v81, v79, v73, v81 op_sel:[0,0,0] op_sel_hi:[0,1,1]\n\tv_pk_mul_f16 v84, v78, v76 op_sel:[1,1] op_sel_hi:[1,0] neg_lo:[1,0]\n\t" \
  "v_pk_mul_f16 v85, v79, v77 op_sel:[1,1] op_sel_hi:[1,0] neg_lo:[1,0]\n\tv_pk_fma_f16 v84, v78, v76, v84 op_sel:[0,0,0] op_sel_hi:[0,1,1]\n\t" \
  "v_pk_fma_f16 v85, v79, v77, v85 op_sel:[0,0,0] op_sel_hi:[0,1,1]\n\tv_cvt_pk_f16_f32 v86, v60, -v61\n\t" \
  "v_cvt_pk_f16_f32 v87, v62, -v63\n\tds_write_addtid_b32 v72 offset:288\n\t" \
  "ds_write_addtid_b32 v73 offset:4896\n\tds_write_addtid_b32 v74 offset:576\n\t" \
  "ds_write_addtid_b32 v75 offset:5184\n\tds_write_addtid_b32 v76 offset:864\n\t" \
  "ds_write_addtid_b32 v77 offset:5472\n\tds_write_addtid_b32 v78 offset:1152\n\t" \
  "ds_write_addtid_b32 v79 offset:5760\n\tds_write_addtid_b32 v80 offset:1440\n\t" \
  "ds_write_addtid_b32 v81 offset:6048\n\tds_write_addtid_b32 v82 offset:1728\n\t" \
  "ds_write_addtid_b32 v83 offset:6336\n\tds_write_addtid_b32 v84 offset:2016\n\t" \
  "ds_write_addtid_b32 v85 offset:6624\n\tds_write_addtid_b32 v86 offset:3456\n\t" \
  "ds_write_addtid_b32 v87 offset:8064\n\tds_read_b128 v[104:107], v70 offset:0\n\t" \
  "ds_read_b128 v[108:111], v70 offset:64\n\tds_read_b128 v[112:115], v70 offset:128\n\t" \
  "ds_read_b128 v[116:119], v70 offset:192\n\tds_read_b128 v[120:123], v70 offset:4608\n\t" \
  "ds_read_b128 v[124:127], v70 offset:4672\n\tds_read_b128 v[128:131], v70 offset:4736\n\t" \
  "ds_read_b128 v[132:135], v70 offset:4800\n\tv_fract_f32 v99, v99\n\t" \
  "v_fract_f32 v101, v101\n\tv_exp_f32 v98, v98\n\t" \
  "v_exp_f32 v100, v100\n\tv_cos_f32 v44, v99\n\t" \
  "v_sin_f32 v45, v99\n\tv_cos_f32 v46, v101\n\t" \
  "v_sin_f32 v47, v101\n\tv_pk_mul_f32 v[44:45], v[44:45], v[98:99] op_sel_hi:[1,0]\n\t" \
  "v_pk_mul_f32 v[46:47], v[46:47], v[100:101] op_sel_hi:[1,0]\n\ts_waitcnt vmcnt(0)\n\t" \
  "v_pk_mul_f32 v[48:49], v[44:45], v[44:45] op_sel:[1,1] op_sel_hi:[1,0] neg_lo:[1,0]\n\tv_pk_mul_f32 v[50:51], v[46:47], v[46:47] op_sel:[1,1] op_sel_hi:[1,0] neg_lo:[1,0]\n\t" \
  "v_pk_fma_f32 v[48:49], v[44:45], v[44:45], v[48:49] op_sel:[0,0,0] op_sel_hi:[0,1,1]\n\tv_pk_fma_f32 v[50:51], v[46:47], v[46:47], v[50:51] op_sel:[0,0,0] op_sel_hi:[0,1,1]\n\t" \
  "v_and_b32 v102, 0x7fff7fff, v68\n\tv_cvt_pk_f16_f32 v88, v44, -v45\n\t" \
  "v_cvt_pk_f16_f32 v89, v46, -v47\n\tv_pk_mul_f32 v[52:53], v[48:49], v[44:45] op_sel:[1,1] op_sel_hi:[1,0] neg_lo:[1,0]\n\t" \
  "v_pk_mul_f32 v[54:55], v[50:51], v[46:47] op_sel:[1,1] op_sel_hi:[1,0] neg_lo:[1,0]\n\tv_and_b32 v103, 0x7fff7fff, v69\n\t" \
  "v_pk_fma_f32 v[52:53], v[48:49], v[44:45], v[52:53] op_sel:[0,0,0] op_sel_hi:[0,1,1]\n\tv_pk_fma_f32 v[54:55], v[50:51], v[46:47], v[54:55] op_sel:[0,0,0] op_sel_hi:[0,1,1]\n\t" \
  "v_pk_mul_f32 v[56:57], v[48:49], v[48:49] op_sel:[1,1] op_sel_hi:[1,0] neg_lo:[1,0]\n\tv_pk_mul_f32 v[58:59], v[50:51], v[50:51] op_sel:[1,1] op_sel_hi:[1,0] neg_lo:[1,0]\n\t" \
  "v_pk_max_u16 v102, v102, v103\n\tv_pk_fma_f32 v[56:57], v[48:49], v[48:49], v[56:57] op_sel:[0,0,0] op_sel_hi:[0,1,1]\n\t" \
  "v_pk_fma_f32 v[58:59], v[50:51], v[50:51], v[58:59] op_sel:[0,0,0] op_sel_hi:[0,1,1]\n\tv_cvt_pk_f16_f32 v90, v48, -v49\n\t" \
  "v_cvt_pk_f16_f32 v91, v50, -v51\n\tv_pk_max_u16 v102, v102, v102 op_sel:[1,0] op_sel_hi:[0,1]\n\t" \
  "v_pk_mul_f32 v[64:65], v[56:57], v[60:61] op_sel:[1,1] op_sel_hi:[1,0] neg_lo:[1,0]\n\tv_pk_mul_f32 v[66:67], v[58:59], v[62:63] op_sel:[1,1] op_sel_hi:[1,0] neg_lo:[1,0]\n\t" \
  "v_pk_fma_f32 v[64:65], v[56:57], v[60:61], v[64:65] op_sel:[0,0,0] op_sel_hi:[0,1,1]\n\tv_pk_fma_f32 v[66:67], v[58:59], v[62:63], v[66:67] op_sel:[0,0,0] op_sel_hi:[0,1,1]\n\t" \
  "v_max_u32_dpp v102, v102, v102 row_shr:1 row_mask:0xf bank_mask:0xf\n\tv_cvt_pk_f16_f32 v92, v52, -v53\n\t" \
  "v_cvt_pk_f16_f32 v93, v54, -v55\n\tv_pk_mul_f16 v136, v88, v74 op_sel:[1,1] op_sel_hi:[1,0] neg_lo:[1,0]\n\t" \
  "v_pk_mul_f16 v137, v89, v75 op_sel:[1,1] op_sel_hi:[1,0] neg_lo:[1,0]\n\tv_max_u32_dpp v102, v102, v102 row_shr:2 row_mask:0xf bank_mask:0xf\n\t" \
  "v_pk_fma_f16 v136, v88, v74, v136 op_sel:[0,0,0] op_sel_hi:[0,1,1]\n\tv_pk_fma_f16 v137, v89, v75, v137 op_sel:[0,0,0] op_sel_hi:[0,1,1]\n\t" \
  "v_pk_mul_f16 v138, v88, v78 op_sel:[1,1] op_sel_hi:[1,0] neg_lo:[1,0]\n\tv_pk_mul_f16 v139, v89, v79 op_sel:[1,1] op_sel_hi:[1,0] neg_lo:[1,0]\n\t" \
  "v_max_u32_dpp v102, v102, v102 row_shr:4 row_mask:0xf bank_mask:0xf\n\tv_pk_fma_f16 v138, v88, v78, v138 op_sel:[0,0,0] op_sel_hi:[0,1,1]\n\t" \
  "v_pk_fma_f16 v139, v89, v79, v139 op_sel:[0,0,0] op_sel_hi:[0,1,1]\n\tv_cvt_pk_f16_f32 v94, v64, -v65\n\t" \
  "v_cvt_pk_f16_f32 v95, v66, -v67\n\tv_max_u32_dpp v102, v102, v102 row_shr:8 row_mask:0xf bank_mask:0xf\n\t" \
  "v_pk_mul_f16 v140, v88, v82 op_sel:[1,1] op_sel_hi:[1,0] neg_lo:[1,0]\n\tv_pk_mul_f16 v141, v89, v83 op_sel:[1,1] op_sel_hi:[1,0] neg_lo:[1,0]\n\t" \
  "v_pk_fma_f16 v140, v88, v82, v140 op_sel:[0,0,0] op_sel_hi:[0,1,1]\n\tv_pk_fma_f16 v141, v89, v83, v141 op_sel:[0,0,0] op_sel_hi:[0,1,1]\n\t" \
  "ds_write_addtid_b32 v88 offset:3168\n\tds_write_addtid_b32 v89 offset:7776\n\t" \
  "ds_write_addtid_b32 v136 offset:0\n\tds_write_addtid_b32 v137 offset:4608\n\t" \
  "ds_write_addtid_b32 v138 offset:288\n\tds_write_addtid_b32 v139 offset:4896\n\t" \
  "ds_write_addtid_b32 v94 offset:1152\n\tds_write_addtid_b32 v95 offset:5760\n\t" \
  "ds_write_addtid_b32 v140 offset:576\n\tds_write_addtid_b32 v141 offset:5184\n\t" \
  "v_max_u32_dpp v102, v102, v102 row_bcast:15 row_mask:0xa bank_mask:0xf\n\tv_pk_mul_f16 v142, v88, v86 op_sel:[1,1] op_sel_hi:[1,0] neg_lo:[1,0]\n\t" \
  "v_pk_mul_f16 v143, v89, v87 op_sel:[1,1] op_sel_hi:[1,0] neg_lo:[1,0]\n\tv_pk_fma_f16 v142, v88, v86, v142 op_sel:[0,0,0] op_sel_hi:[0,1,1]\n\t" \
  "v_pk_fma_f16 v143, v89, v87, v143 op_sel:[0,0,0] op_sel_hi:[0,1,1]\n\tv_max_u32_dpp v102, v102, v102 row_bcast:31 row_mask:0xc bank_mask:0xf\n\t" \
  "v_pk_mul_f16 v144, v90, v78 op_sel:[1,1] op_sel_hi:[1,0] neg_lo:[1,0]\n\tv_pk_mul_f16 v145, v91, v79 op_sel:[1,1] op_sel_hi:[1,0] neg_lo:[1,0]\n\t" \
  "v_pk_fma_f16 v144, v90, v78, v144 op_sel:[0,0,0] op_sel_hi:[0,1,1]\n\tv_pk_fma_f16 v145, v91, v79, v145 op_sel:[0,0,0] op_sel_hi:[0,1,1]\n\t" \
  "v_readlane_b32 %[e], v102, 63\n\tv_pk_mul_f16 v146, v90, v86 op_sel:[1,1] op_sel_hi:[1,0] neg_lo:[1,0]\n\t" \
  "v_pk_mul_f16 v147, v91, v87 op_sel:[1,1] op_sel_hi:[1,0] neg_lo:[1,0]\n\tv_pk_fma_f16 v146, v90, v86, v146 op_sel:[0,0,0] op_sel_hi:[0,1,1]\n\t" \
  "v_pk_fma_f16 v147, v91, v87, v147 op_sel:[0,0,0] op_sel_hi:[0,1,1]\n\tv_pk_mul_f16 v148, v92, v78 op_sel:[1,1] op_sel_hi:[1,0] neg_lo:[1,0]\n\t" \
  "v_pk_mul_f16 v149, v93, v79 op_sel:[1,1] op_sel_hi:[1,0] neg_lo:[1,0]\n\tv_pk_fma_f16 v148, v92, v78, v148 op_sel:[0,0,0] op_sel_hi:[0,1,1]\n\t" \
  "v_pk_fma_f16 v149, v93, v79, v149 op_sel:[0,0,0] op_sel_hi:[0,1,1]\n\tv_pk_mul_f16 v150, v92, v86 op_sel:[1,1] op_sel_hi:[1,0] neg_lo:[1,0]\n\t" \
  "v_pk_mul_f16 v151, v93, v87 op_sel:[1,1] op_sel_hi:[1,0] neg_lo:[1,0]\n\tv_pk_fma_f16 v150, v92, v86, v150 op_sel:[0,0,0] op_sel_hi:[0,1,1]\n\t" \
  "v_pk_fma_f16 v151, v93, v87, v151 op_sel:[0,0,0] op_sel_hi:[0,1,1]\n\ts_bfe_u32 %[e], %[e], 0x80007\n\t" \
  "s_max_u32 %[e], %[e], 8\n\ts_sub_u32 %[t], 260, %[e]\n\t" \
  "s_lshl_b32 %[t], %[t], 23\n\tds_write_addtid_b32 v142 offset:864\n\t" \
  "ds_write_addtid_b32 v143 offset:5472\n\tds_write_addtid_b32 v144 offset:3456\n\t" \
  "ds_write_addtid_b32 v145 offset:8064\n\tds_write_addtid_b32 v146 offset:3744\n\t" \
  "ds_write_addtid_b32 v147 offset:8352\n\tds_write_addtid_b32 v148 offset:4032\n\t" \
  "ds_write_addtid_b32 v149 offset:8640\n\tds_write_addtid_b32 v150 offset:4320\n\t" \
  "ds_write_addtid_b32 v151 offset:8928\n\tv_pk_mul_f16 v96, v94, v88 op_sel:[1,1] op_sel_hi:[1,0] neg_lo:[1,0]\n\t" \
  "v_pk_mul_f16 v97, v95, v89 op_sel:[1,1] op_sel_hi:[1,0] neg_lo:[1,0]\n\tv_pk_fma_f16 v96, v94, v88, v96 op_sel:[0,0,0] op_sel_hi:[0,1,1]\n\t" \
  "v_pk_fma_f16 v97, v95, v89, v97 op_sel:[0,0,0] op_sel_hi:[0,1,1]\n\tv_pk_mul_f16 v98, v94, v90 op_sel:[1,1] op_sel_hi:[1,0] neg_lo:[1,0]\n\t" \
  "v_pk_mul_f16 v99, v95, v91 op_sel:[1,1] op_sel_hi:[1,0] neg_lo:[1,0]\n\tv_pk_fma_f16 v98, v94, v90, v98 op_sel:[0,0,0] op_sel_hi:[0,1,1]\n\t" \
  "v_pk_fma_f16 v99, v95, v91, v99 op_sel:[0,0,0] op_sel_hi:[0,1,1]\n\tv_pk_mul_f16 v100, v94, v92 op_sel:[1,1] op_sel_hi:[1,0] neg_lo:[1,0]\n\t" \
  "v_pk_mul_f16 v101, v95, v93 op_sel:[1,1] op_sel_hi:[1,0] neg_lo:[1,0]\n\tv_pk_fma_f16 v100, v94, v92, v100 op_sel:[0,0,0] op_sel_hi:[0,1,1]\n\t" \
  "v_pk_fma_f16 v101, v95, v93, v101 op_sel:[0,0,0] op_sel_hi:[0,1,1]\n\tds_write_addtid_b32 v96 offset:1440\n\t" \
  "ds_write_addtid_b32 v97 offset:6048\n\tds_write_addtid_b32 v98 offset:1728\n\t" \
  "ds_write_addtid_b32 v99 offset:6336\n\tds_write_addtid_b32 v100 offset:2016\n\t" \
  "ds_write_addtid_b32 v101 offset:6624\n\tds_read_b128 v[136:139], v71 offset:0\n\t" \
  "ds_read_b128 v[140:143], v71 offset:64\n\tds_read_b128 v[144:147], v71 offset:128\n\t" \
  "ds_read_b128 v[148:151], v71 offset:192\n\tds_read_b128 v[152:155], v71 offset:4608\n\t" \
  "ds_read_b128 v[156:159], v71 offset:4672\n\tds_read_b128 v[160:163], v71 offset:4736\n\t" \
  "ds_read_b128 v[164:167], v71 offset:4800\n\tv_pk_mul_f32 v[48:49], v[60:61], v[60:61] op_sel:[1,1] op_sel_hi:[1,0] neg_lo:[1,0]\n\t" \
  "v_pk_mul_f32 v[50:51], v[62:63], v[62:63] op_sel:[1,1] op_sel_hi:[1,0] neg_lo:[1,0]\n\tv_pk_fma_f32 v[48:49], v[60:61], v[60:61], v[48:49] op_sel:[0,0,0] op_sel_hi:[0,1,1]\n\t" \
  "s_cmp_lt_u32 %[w], 50176\n\ts_cbranch_scc0 1f\n\t" \
  "s_setprio 1\n\t1:\n\t" \
  "v_pk_fma_f32 v[50:51], v[62:63], v[62:63], v[50:51] op_sel:[0,0,0] op_sel_hi:[0,1,1]\n\tv_cvt_pk_f16_f32 v44, v60, v61\n\t" \
  "v_cvt_pk_f16_f32 v45, v62, v63\n\tv_pk_mul_f32 v[52:53], v[48:49], v[60:61] op_sel:[1,1] op_sel_hi:[1,0] neg_lo:[1,0]\n\t" \
  "v_pk_mul_f32 v[54:55], v[50:51], v[62:63] op_sel:[1,1] op_sel_hi:[1,0] neg_lo:[1,0]\n\tv_pk_fma_f32 v[52:53], v[48:49], v[60:61], v[52:53] op_sel:[0,0,0] op_sel_hi:[0,1,1]\n\t" \
  "v_pk_fma_f32 v[54:55], v[50:51], v[62:63], v[54:55] op_sel:[0,0,0] op_sel_hi:[0,1,1]\n\tv_pk_mul_f32 v[40:41], v[48:49], v[48:49] op_sel:[1,1] op_sel_hi:[1,0] neg_lo:[1,0]\n\t" \
  "v_pk_mul_f32 v[42:43], v[50:51], v[50:51] op_sel:[1,1] op_sel_hi:[1,0] neg_lo:[1,0]\n\tv_pk_fma_f32 v[40:41], v[48:49], v[48:49], v[40:41] op_sel:[0,0,0] op_sel_hi:[0,1,1]\n\t" \
  "v_pk_fma_f32 v[42:43], v[50:51], v[50:51], v[42:43] op_sel:[0,0,0] op_sel_hi:[0,1,1]\n\tv_cvt_pk_f16_f32 v46, v48, v49\n\t" \
  "v_cvt_pk_f16_f32 v47, v50, v51\n\tv_cvt_pk_f16_f32 v56, v52, v53\n\t" \
  "v_cvt_pk_f16_f32 v57, v54, v55\n\tv_lshlrev_b32 v64, 16, v68\n\t" \
  "v_lshlrev_b32 v65, 16, v69\n\tv_and_b32 v66, 0xffff0000, v68\n\t" \
  "v_and_b32 v67, 0xffff0000, v69\n\tv_mul_f32 v64, %[t], v64\n\t" \
  "v_mul_f32 v65, %[t], v65\n\tv_mul_f32 v66, %[t], v66\n\t" \
  "v_mul_f32 v67, %[t], v67\n\tv_cvt_pk_f16_f32 v48, v64, v65\n\t" \
  "v_cvt_pk_f16_f32 v49, v66, v67\n\tv_pk_mul_f32 v[68:69], v[64:65], v[40:41] op_sel:[1,1] op_sel_hi:[1,0] neg_lo:[1,0]\n\t" \
  "v_pk_mul_f32 v[70:71], v[66:67], v[42:43] op_sel:[1,1] op_sel_hi:[1,0] neg_lo:[1,0]\n\tv_pk_fma_f32 v[68:69], v[64:65], v[40:41], v[68:69] op_sel:[0,0,0] op_sel_hi:[0,1,1]\n\t" \
  "v_pk_fma_f32 v[70:71], v[66:67], v[42:43], v[70:71] op_sel:[0,0,0] op_sel_hi:[0,1,1]\n\tv_pk_mul_f16 v50, v48, v44 op_sel:[1,1] op_sel_hi:[1,0] neg_lo:[1,0]\n\t" \
  "v_pk_mul_f16 v51, v49, v45 op_sel:[1,1] op_sel_hi:[1,0] neg_lo:[1,0]\n\tv_pk_fma_f16 v50, v48, v44, v50 op_sel:[0,0,0] op_sel_hi:[0,1,1]\n\t" \
  "v_pk_fma_f16 v51, v49, v45, v51 op_sel:[0,0,0] op_sel_hi:[0,1,1]\n\tv_pk_mul_f16 v52, v48, v46 op_sel:[1,1] op_sel_hi:[1,0] neg_lo:[1,0]\n\t" \
  "v_pk_mul_f16 v53, v49, v47 op_sel:[1,1] op_sel_hi:[1,0] neg_lo:[1,0]\n\tv_pk_fma_f16 v52, v48, v46, v52 op_sel:[0,0,0] op_sel_hi:[0,1,1]\n\t" \
  "v_pk_fma_f16 v53, v49, v47, v53 op_sel:[0,0,0] op_sel_hi:[0,1,1]\n\tv_pk_mul_f16 v54, v48, v56 op_sel:[1,1] op_sel_hi:[1,0] neg_lo:[1,0]\n\t" \
  "v_pk_mul_f16 v55, v49, v57 op_sel:[1,1] op_sel_hi:[1,0] neg_lo:[1,0]\n\tv_pk_fma_f16 v54, v48, v56, v54 op_sel:[0,0,0] op_sel_hi:[0,1,1]\n\t" \
  "v_pk_fma_f16 v55, v49, v57, v55 op_sel:[0,0,0] op_sel_hi:[0,1,1]\n\tds_write_addtid_b32 v48 offset:0\n\t" \
  "ds_write_addtid_b32 v49 offset:4608\n\tds_write_addtid_b32 v50 offset:288\n\t" \
  "ds_write_addtid_b32 v51 offset:4896\n\tds_write_addtid_b32 v52 offset:576\n\t" \
  "ds_write_addtid_b32 v53 offset:5184\n\tds_write_addtid_b32 v54 offset:864\n\t" \
  "ds_write_addtid_b32 v55 offset:5472\n\tv_pk_mul_f32 v[60:61], v[40:41], v[40:41] op_sel:[1,1] op_sel_hi:[1,0] neg_lo:[1,0]\n\t" \
  "v_pk_mul_f32 v[62:63], v[42:43], v[42:43] op_sel:[1,1] op_sel_hi:[1,0] neg_lo:[1,0]\n\tv_pk_fma_f32 v[60:61], v[40:41], v[40:41], v[60:61] op_sel:[0,0,0] op_sel_hi:[0,1,1]\n\t" \
  "v_pk_fma_f32 v[62:63], v[42:43], v[42:43], v[62:63] op_sel:[0,0,0] op_sel_hi:[0,1,1]\n\tv_cvt_pk_f16_f32 v48, v68, v69\n\t" \
  "v_cvt_pk_f16_f32 v49, v70, v71\n\tv_pk_mul_f32 v[64:65], v[68:69], v[40:41] op_sel:[1,1] op_sel_hi:[1,0] neg_lo:[1,0]\n\t" \
  "v_pk_mul_f32 v[66:67], v[70:71], v[42:43] op_sel:[1,1] op_sel_hi:[1,0] neg_lo:[1,0]\n\tv_pk_fma_f32 v[64:65], v[68:69], v[40:41], v[64:65] op_sel:[0,0,0] op_sel_hi:[0,1,1]\n\t" \
  "v_pk_fma_f32 v[66:67], v[70:71], v[42:43], v[66:67] op_sel:[0,0,0] op_sel_hi:[0,1,1]\n\tv_pk_mul_f16 v50, v48, v44 op_sel:[1,1] op_sel_hi:[1,0] neg_lo:[1,0]\n\t" \
  "v_pk_mul_f16 v51, v49, v45 op_sel:[1,1] op_sel_hi:[1,0] neg_lo:[1,0]\n\tv_pk_fma_f16 v50, v48, v44, v50 op_sel:[0,0,0] op_sel_hi:[0,1,1]\n\t" \
  "v_pk_fma_f16 v51, v49, v45, v51 op_sel:[0,0,0] op_sel_hi:[0,1,1]\n\tv_pk_mul_f16 v52, v48, v46 op_sel:[1,1] op_sel_hi:[1,0] neg_lo:[1,0]\n\t" \
  "v_pk_mul_f16 v53, v49, v47 op_sel:[1,1] op_sel_hi:[1,0] neg_lo:[1,0]\n\tv_pk_fma_f16 v52, v48, v46, v52 op_sel:[0,0,0] op_sel_hi:[0,1,1]\n\t" \
  "v_pk_fma_f16 v53, v49, v47, v53 op_sel:[0,0,0] op_sel_hi:[0,1,1]\n\tv_pk_mul_f16 v54, v48, v56 op_sel:[1,1] op_sel_hi:[1,0] neg_lo:[1,0]\n\t" \
  "v_pk_mul_f16 v55, v49, v57 op_sel:[1,1] op_sel_hi:[1,0] neg_lo:[1,0]\n\tv_pk_fma_f16 v54, v48, v56, v54 op_sel:[0,0,0] op_sel_hi:[0,1,1]\n\t" \
  "v_pk_fma_f16 v55, v49, v57, v55 op_sel:[0,0,0] op_sel_hi:[0,1,1]\n\tds_write_addtid_b32 v48 offset:1152\n\t" \
  "ds_write_addtid_b32 v49 offset:5760\n\tds_write_addtid_b32 v50 offset:1440\n\t" \
  "ds_write_addtid_b32 v51 offset:6048\n\tds_write_addtid_b32 v52 offset:1728\n\t" \
  "ds_write_addtid_b32 v53 offset:6336\n\tds_write_addtid_b32 v54 offset:2016\n\t" \
  "ds_write_addtid_b32 v55 offset:6624\n\tv_pk_mul_f32 v[36:37], v[60:61], v[60:61] op_sel:[1,1] op_sel_hi:[1,0] neg_lo:[1,0]\n\t" \
  "v_pk_mul_f32 v[38:39], v[62:63], v[62:63] op_sel:[1,1] op_sel_hi:[1,0] neg_lo:[1,0]\n\tv_pk_fma_f32 v[36:37], v[60:61], v[60:61], v[36:37] op_sel:[0,0,0] op_sel_hi:[0,1,1]\n\t" \
  "v_pk_fma_f32 v[38:39], v[62:63], v[62:63], v[38:39] op_sel:[0,0,0] op_sel_hi:[0,1,1]\n\tv_cvt_pk_f16_f32 v48, v64, v65\n\t" \
  "v_cvt_pk_f16_f32 v49, v66, v67\n\tv_pk_mul_f32 v[68:69], v[64:65], v[40:41] op_sel:[1,1] op_sel_hi:[1,0] neg_lo:[1,0]\n\t" \
  "v_pk_mul_f32 v[70:71], v[66:67], v[42:43] op_sel:[1,1] op_sel_hi:[1,0] neg_lo:[1,0]\n\tv_pk_fma_f32 v[68:69], v[64:65], v[40:41], v[68:69] op_sel:[0,0,0] op_sel_hi:[0,1,1]\n\t" \
  "v_pk_fma_f32 v[70:71], v[66:67], v[42:43], v[70:71] op_sel:[0,0,0] op_sel_hi:[0,1,1]\n\tv_pk_mul_f16 v50, v48, v44 op_sel:[1,1] op_sel_hi:[1,0] neg_lo:[1,0]\n\t" \
  "v_pk_mul_f16 v51, v49, v45 op_sel:[1,1] op_sel_hi:[1,0] neg_lo:[1,0]\n\tv_pk_fma_f16 v50, v48, v44, v50 op_sel:[0,0,0] op_sel_hi:[0,1,1]\n\t" \
  "v_pk_fma_f16 v51, v49, v45, v51 op_sel:[0,0,0] op_sel_hi:[0,1,1]\n\tv_pk_mul_f16 v52, v48, v46 op_sel:[1,1] op_sel_hi:[1,0] neg_lo:[1,0]\n\t" \
  "v_pk_mul_f16 v53, v49, v47 op_sel:[1,1] op_sel_hi:[1,0] neg_lo:[1,0]\n\tv_pk_fma_f16 v52, v48, v46, v52 op_sel:[0,0,0] op_sel_hi:[0,1,1]\n\t" \
  "v_pk_fma_f16 v53, v49, v47, v53 op_sel:[0,0,0] op_sel_hi:[0,1,1]\n\tv_pk_mul_f16 v54, v48, v56 op_sel:[1,1] op_sel_hi:[1,0] neg_lo:[1,0]\n\t" \
  "v_pk_mul_f16 v55, v49, v57 op_sel:[1,1] op_sel_hi:[1,0] neg_lo:[1,0]\n\tv_pk_fma_f16 v54, v48, v56, v54 op_sel:[0,0,0] op_sel_hi:[0,1,1]\n\t" \
  "v_pk_fma_f16 v55, v49, v57, v55 op_sel:[0,0,0] op_sel_hi:[0,1,1]\n\tds_write_addtid_b32 v48 offset:2304\n\t" \
  "ds_write_addtid_b32 v49 offset:6912\n\tds_write_addtid_b32 v50 offset:2592\n\t" \
  "ds_write_addtid_b32 v51 offset:7200\n\tds_write_addtid_b32 v52 offset:2880\n\t" \
  "ds_write_addtid_b32 v53 offset:7488\n\tds_write_addtid_b32 v54 offset:3168\n\t" \
  "ds_write_addtid_b32 v55 offset:7776\n\tv_cvt_pk_f16_f32 v58, v36, v37\n\t" \
  "v_cvt_pk_f16_f32 v59, v38, v39\n\tv_cvt_pk_f16_f32 v48, v68, v69\n\t" \
  "v_cvt_pk_f16_f32 v49, v70, v71\n\tv_pk_mul_f16 v50, v48, v44 op_sel:[1,1] op_sel_hi:[1,0] neg_lo:[1,0]\n\t" \
  "v_pk_mul_f16 v51, v49, v45 op_sel:[1,1] op_sel_hi:[1,0] neg_lo:[1,0]\n\tv_pk_fma_f16 v50, v48, v44, v50 op_sel:[0,0,0] op_sel_hi:[0,1,1]\n\t" \
  "v_pk_fma_f16 v51, v49, v45, v51 op_sel:[0,0,0] op_sel_hi:[0,1,1]\n\tv_pk_mul_f16 v52, v48, v46 op_sel:[1,1] op_sel_hi:[1,0] neg_lo:[1,0]\n\t" \
  "v_pk_mul_f16 v53, v49, v47 op_sel:[1,1] op_sel_hi:[1,0] neg_lo:[1,0]\n\tv_pk_fma_f16 v52, v48, v46, v52 op_sel:[0,0,0] op_sel_hi:[0,1,1]\n\t" \
  "v_pk_fma_f16 v53, v49, v47, v53 op_sel:[0,0,0] op_sel_hi:[0,1,1]\n\tv_pk_mul_f16 v54, v48, v56 op_sel:[1,1] op_sel_hi:[1,0] neg_lo:[1,0]\n\t" \
  "v_pk_mul_f16 v55, v49, v57 op_sel:[1,1] op_sel_hi:[1,0] neg_lo:[1,0]\n\tv_pk_fma_f16 v54, v48, v56, v54 op_sel:[0,0,0] op_sel_hi:[0,1,1]\n\t" \
  "v_pk_fma_f16 v55, v49, v57, v55 op_sel:[0,0,0] op_sel_hi:[0,1,1]\n\tds_write_addtid_b32 v48 offset:3456\n\t" \
  "ds_write_addtid_b32 v49 offset:8064\n\tds_write_addtid_b32 v50 offset:3744\n\t" \
  "ds_write_addtid_b32 v51 offset:8352\n\tds_write_addtid_b32 v52 offset:4032\n\t" \
  "ds_write_addtid_b32 v53 offset:8640\n\tds_write_addtid_b32 v54 offset:4320\n\t" \
  "ds_write_addtid_b32 v55 offset:8928\n\tds_read_b128 v[72:75], v27 offset:0\n\t" \
  "ds_read_b128 v[76:79], v27 offset:64\n\tds_read_b128 v[80:83], v27 offset:128\n\t" \
  "ds_read_b128 v[84:87], v27 offset:192\n\tds_read_b128 v[88:91], v27 offset:4608\n\t" \
  "ds_read_b128 v[92:95], v27 offset:4672\n\tds_read_b128 v[96:99], v27 offset:4736\n\t" \
  "ds_read_b128 v[100:103], v27 offset:4800\n\tds_write_addtid_b32 v58 offset:0\n\t" \
  "ds_write_addtid_b32 v59 offset:4608\n\tds_read_b128 v[40:43], v12 offset:0\n\t" \
  "ds_read_b128 v[44:47], v12 offset:64\n\tds_read_b128 v[48:51], v12 offset:128\n\t" \
  "v_add_u32_sdwa v56, %[w], v16 dst_sel:DWORD dst_unused:UNUSED_PAD src0_sel:DWORD src1_sel:WORD_0\n\tv_add_u32_sdwa v57, %[w], v16 dst_sel:DWORD dst_unused:UNUSED_PAD src0_sel:DWORD src1_sel:WORD_1\n\t" \
  "v_add_u32_sdwa v58, %[w], v17 dst_sel:DWORD dst_unused:UNUSED_PAD src0_sel:DWORD src1_sel:WORD_0\n\tv_add_u32_sdwa v59, %[w], v17 dst_sel:DWORD dst_unused:UNUSED_PAD src0_sel:DWORD src1_sel:WORD_1\n\t" \
  "s_cmp_lt_u32 %[w], 50176\n\ts_cbranch_scc1 1f\n\t" \
  "s_setprio 1\n\t1:\n\t" \
  "v_add_u32 v14, v56, v35\n\tv_add_u32 v15, v57, v35\n\t" \
  "s_waitcnt lgkmcnt(12)\n\tv_mfma_f32_16x16x32_f16 v[60:63], v[104:107], v[72:75], 0\n\t" \
  "v_mfma_f32_16x16x32_f16 v[36:39], v[72:75], v[136:139], 0\n\tv_mfma_f32_16x16x32_f16 v[4:7], v[136:139], v[72:75], 0\n\t" \
  "s_waitcnt lgkmcnt(2)\n\tv_mfma_f32_16x16x32_f16 v[60:63], v[108:111], v[76:79], v[60:63]\n\t" \
  "v_pk_mul_f16 v52, v40, v72 op_sel:[1,1] op_sel_hi:[1,0] neg_lo:[1,0]\n\tv_pk_mul_f16 v53, v41, v73 op_sel:[1,1] op_sel_hi:[1,0] neg_lo:[1,0]\n\t" \
  "v_pk_mul_f16 v54, v42, v74 op_sel:[1,1] op_sel_hi:[1,0] neg_lo:[1,0]\n\tv_mfma_f32_16x16x32_f16 v[36:39], v[76:79], v[140:143], v[36:39]\n\t" \
  "v_pk_mul_f16 v55, v43, v75 op_sel:[1,1] op_sel_hi:[1,0] neg_lo:[1,0]\n\tv_pk_fma_f16 v40, v40, v72, v52 op_sel:[0,0,0] op_sel_hi:[0,1,1]\n\t" \
  "v_pk_fma_f16 v41, v41, v73, v53 op_sel:[0,0,0] op_sel_hi:[0,1,1]\n\tv_mfma_f32_16x16x32_f16 v[4:7], v[140:143], v[76:79], v[4:7]\n\t" \
  "v_pk_fma_f16 v42, v42, v74, v54 op_sel:[0,0,0] op_sel_hi:[0,1,1]\n\tv_pk_fma_f16 v43, v43, v75, v55 op_sel:[0,0,0] op_sel_hi:[0,1,1]\n\t" \
  "s_waitcnt lgkmcnt(1)\n\tv_mfma_f32_16x16x32_f16 v[60:63], v[112:115], v[80:83], v[60:63]\n\t" \
  "ds_read_b128 v[72:75], v12 offset:192\n\tv_pk_mul_f16 v52, v44, v76 op_sel:[1,1] op_sel_hi:[1,0] neg_lo:[1,0]\n\t" \
  "v_pk_mul_f16 v53, v45, v77 op_sel:[1,1] op_sel_hi:[1,0] neg_lo:[1,0]\n\tv_pk_mul_f16 v54, v46, v78 op_sel:[1,1] op_sel_hi:[1,0] neg_lo:[1,0]\n\t" \
  "v_mfma_f32_16x16x32_f16 v[36:39], v[80:83], v[144:147], v[36:39]\n\tv_pk_mul_f16 v55, v47, v79 op_sel:[1,1] op_sel_hi:[1,0] neg_lo:[1,0]\n\t" \
  "v_pk_fma_f16 v44, v44, v76, v52 op_sel:[0,0,0] op_sel_hi:[0,1,1]\n\tv_pk_fma_f16 v45, v45, v77, v53 op_sel:[0,0,0] op_sel_hi:[0,1,1]\n\t" \
  "v_mfma_f32_16x16x32_f16 v[4:7], v[144:147], v[80:83], v[4:7]\n\tv_pk_fma_f16 v46, v46, v78, v54 op_sel:[0,0,0] op_sel_hi:[0,1,1]\n\t" \
  "v_pk_fma_f16 v47, v47, v79, v55 op_sel:[0,0,0] op_sel_hi:[0,1,1]\n\ts_waitcnt lgkmcnt(1)\n\t" \
  "v_mfma_f32_16x16x32_f16 v[60:63], v[116:119], v[84:87], v[60:63]\n\tds_read_b128 v[76:79], v12 offset:4608\n\t" \
  "v_pk_mul_f16 v52, v48, v80 op_sel:[1,1] op_sel_hi:[1,0] neg_lo:[1,0]\n\tv_pk_mul_f16 v53, v49, v81 op_sel:[1,1] op_sel_hi:[1,0] neg_lo:[1,0]\n\t" \
  "v_pk_mul_f16 v54, v50, v82 op_sel:[1,1] op_sel_hi:[1,0] neg_lo:[1,0]\n\tv_mfma_f32_16x16x32_f16 v[36:39], v[84:87], v[148:151], v[36:39]\n\t" \
  "v_pk_mul_f16 v55, v51, v83 op_sel:[1,1] op_sel_hi:[1,0] neg_lo:[1,0]\n\tv_pk_fma_f16 v48, v48, v80, v52 op_sel:[0,0,0] op_sel_hi:[0,1,1]\n\t" \
  "v_pk_fma_f16 v49, v49, v81, v53 op_sel:[0,0,0] op_sel_hi:[0,1,1]\n\tv_mfma_f32_16x16x32_f16 v[4:7], v[148:151], v[84:87], v[4:7]\n\t" \
  "v_pk_fma_f16 v50, v50, v82, v54 op_sel:[0,0,0] op_sel_hi:[0,1,1]\n\tv_pk_fma_f16 v51, v51, v83, v55 op_sel:[0,0,0] op_sel_hi:[0,1,1]\n\t" \
  "s_waitcnt lgkmcnt(1)\n\tv_mfma_f32_16x16x32_f16 v[60:63], v[120:123], v[88:91], v[60:63]\n\t" \
  "ds_read_b128 v[80:83], v12 offset:4672\n\tv_pk_mul_f16 v52, v72, v84 op_sel:[1,1] op_sel_hi:[1,0] neg_lo:[1,0]\n\t" \
  "v_pk_mul_f16 v53, v73, v85 op_sel:[1,1] op_sel_hi:[1,0] neg_lo:[1,0]\n\tv_pk_mul_f16 v54, v74, v86 op_sel:[1,1] op_sel_hi:[1,0] neg_lo:[1,0]\n\t" \
  "v_mfma_f32_16x16x32_f16 v[36:39], v[88:91], v[152:155], v[36:39]\n\tv_pk_mul_f16 v55, v75, v87 op_sel:[1,1] op_sel_hi:[1,0] neg_lo:[1,0]\n\t" \
  "v_pk_fma_f16 v72, v72, v84, v52 op_sel:[0,0,0] op_sel_hi:[0,1,1]\n\tv_pk_fma_f16 v73, v73, v85, v53 op_sel:[0,0,0] op_sel_hi:[0,1,1]\n\t" \
  "v_mfma_f32_16x16x32_f16 v[4:7], v[152:155], v[88:91], v[4:7]\n\tv_pk_fma_f16 v74, v74, v86, v54 op_sel:[0,0,0] op_sel_hi:[0,1,1]\n\t" \
  "v_pk_fma_f16 v75, v75, v87, v55 op_sel:[0,0,0] op_sel_hi:[0,1,1]\n\ts_waitcnt lgkmcnt(1)\n\t" \
  "v_mfma_f32_16x16x32_f16 v[60:63], v[124:127], v[92:95], v[60:63]\n\tds_read_b128 v[84:87], v12 offset:4736\n\t" \
  "v_pk_mul_f16 v52, v76, v88 op_sel:[1,1] op_sel_hi:[1,0] neg_lo:[1,0]\n\tv_pk_mul_f16 v53, v77, v89 op_sel:[1,1] op_sel_hi:[1,0] neg_lo:[1,0]\n\t" \
  "v_pk_mul_f16 v54, v78, v90 op_sel:[1,1] op_sel_hi:[1,0] neg_lo:[1,0]\n\tv_mfma_f32_16x16x32_f16 v[36:39], v[92:95], v[156:159], v[36:39]\n\t" \
  "v_pk_mul_f16 v55, v79, v91 op_sel:[1,1] op_sel_hi:[1,0] neg_lo:[1,0]\n\tv_pk_fma_f16 v76, v76, v88, v52 op_sel:[0,0,0] op_sel_hi:[0,1,1]\n\t" \
  "v_pk_fma_f16 v77, v77, v89, v53 op_sel:[0,0,0] op_sel_hi:[0,1,1]\n\tv_mfma_f32_16x16x32_f16 v[4:7], v[156:159], v[92:95], v[4:7]\n\t" \
  "v_pk_fma_f16 v78, v78, v90, v54 op_sel:[0,0,0] op_sel_hi:[0,1,1]\n\tv_pk_fma_f16 v79, v79, v91, v55 op_sel:[0,0,0] op_sel_hi:[0,1,1]\n\t" \
  "s_waitcnt lgkmcnt(1)\n\tv_mfma_f32_16x16x32_f16 v[60:63], v[128:131], v[96:99], v[60:63]\n\t" \
  "ds_read_b128 v[88:91], v12 offset:4800\n\tv_pk_mul_f16 v52, v80, v92 op_sel:[1,1] op_sel_hi:[1,0] neg_lo:[1,0]\n\t" \
  "v_pk_mul_f16 v53, v81, v93 op_sel:[1,1] op_sel_hi:[1,0] neg_lo:[1,0]\n\tv_pk_mul_f16 v54, v82, v94 op_sel:[1,1] op_sel_hi:[1,0] neg_lo:[1,0]\n\t" \
  "v_mfma_f32_16x16x32_f16 v[36:39], v[96:99], v[160:163], v[36:39]\n\tv_pk_mul_f16 v55, v83, v95 op_sel:[1,1] op_sel_hi:[1,0] neg_lo:[1,0]\n\t" \
  "v_pk_fma_f16 v80, v80, v92, v52 op_sel:[0,0,0] op_sel_hi:[0,1,1]\n\tv_pk_fma_f16 v81, v81, v93, v53 op_sel:[0,0,0] op_sel_hi:[0,1,1]\n\t" \
  "v_mfma_f32_16x16x32_f16 v[4:7], v[160:163], v[96:99], v[4:7]\n\tv_pk_fma_f16 v82, v82, v94, v54 op_sel:[0,0,0] op_sel_hi:[0,1,1]\n\t" \
  "v_pk_fma_f16 v83, v83, v95, v55 op_sel:[0,0,0] op_sel_hi:[0,1,1]\n\ts_waitcnt lgkmcnt(1)\n\t" \
  "v_mfma_f32_16x16x32_f16 v[60:63], v[132:135], v[100:103], v[60:63]\n\tv_pk_mul_f16 v52, v84, v96 op_sel:[1,1] op_sel_hi:[1,0] neg_lo:[1,0]\n\t" \
  "v_pk_mul_f16 v53, v85, v97 op_sel:[1,1] op_sel_hi:[1,0] neg_lo:[1,0]\n\tv_pk_mul_f16 v54, v86, v98 op_sel:[1,1] op_sel_hi:[1,0] neg_lo:[1,0]\n\t" \
  "v_mfma_f32_16x16x32_f16 v[36:39], v[100:103], v[164:167], v[36:39]\n\tv_pk_mul_f16 v55, v87, v99 op_sel:[1,1] op_sel_hi:[1,0] neg_lo:[1,0]\n\t" \
  "v_pk_fma_f16 v84, v84, v96, v52 op_sel:[0,0,0] op_sel_hi:[0,1,1]\n\tv_pk_fma_f16 v85, v85, v97, v53 op_sel:[0,0,0] op_sel_hi:[0,1,1]\n\t" \
  "v_mfma_f32_16x16x32_f16 v[4:7], v[164:167], v[100:103], v[4:7]\n\tv_pk_fma_f16 v86, v86, v98, v54 op_sel:[0,0,0] op_sel_hi:[0,1,1]\n\t" \
  "v_pk_fma_f16 v87, v87, v99, v55 op_sel:[0,0,0] op_sel_hi:[0,1,1]\n\ts_waitcnt lgkmcnt(0)\n\t" \
  "v_mfma_f32_16x16x32_f16 v[64:67], v[104:107], v[40:43], 0\n\tv_pk_mul_f16 v52, v88, v100 op_sel:[1,1] op_sel_hi:[1,0] neg_lo:[1,0]\n\t" \
  "v_pk_mul_f16 v53, v89, v101 op_sel:[1,1] op_sel_hi:[1,0] neg_lo:[1,0]\n\tv_pk_mul_f16 v54, v90, v102 op_sel:[1,1] op_sel_hi:[1,0] neg_lo:[1,0]\n\t" \
  "v_mfma_f32_16x16x32_f16 v[68:71], v[40:43], v[136:139], 0\n\tv_pk_mul_f16 v55, v91, v103 op_sel:[1,1] op_sel_hi:[1,0] neg_lo:[1,0]\n\t" \
  "v_pk_fma_f16 v88, v88, v100, v52 op_sel:[0,0,0] op_sel_hi:[0,1,1]\n\tv_pk_fma_f16 v89, v89, v101, v53 op_sel:[0,0,0] op_sel_hi:[0,1,1]\n\t" \
  "v_mfma_f32_16x16x32_f16 v[8:11], v[136:139], v[40:43], 0\n\tv_pk_fma_f16 v90, v90, v102, v54 op_sel:[0,0,0] op_sel_hi:[0,1,1]\n\t" \
  "v_pk_fma_f16 v91, v91, v103, v55 op_sel:[0,0,0] op_sel_hi:[0,1,1]\n\tv_mov_b64 v[100:101], 0\n\t" \
  "v_mfma_f32_16x16x32_f16 v[64:67], v[108:111], v[44:47], v[64:67]\n\tv_mov_b64 v[102:103], 0\n\t" \
  "v_cvt_pk_f16_f32 v94, v36, v37\n\tv_cvt_pk_f16_f32 v95, v38, v39\n\t" \
  "v_mfma_f32_16x16x32_f16 v[68:71], v[44:47], v[140:143], v[68:71]\n\tv_cvt_pk_f16_f32 v4, v4, v5\n\t" \
  "v_cvt_pk_f16_f32 v5, v6, v7\n\tv_mov_b32_dpp v94, v94 row_shl:4 row_mask:0xf bank_mask:0x5\n\t" \
  "v_mfma_f32_16x16x32_f16 v[8:11], v[140:143], v[44:47], v[8:11]\n\tv_mov_b32_dpp v95, v95 row_shl:4 row_mask:0xf bank_mask:0x5\n\t" \
  "v_permlane16_swap_b32 v4, v5\n\tv_cvt_pk_f16_f32 v92, v60, v61\n\t" \
  "v_mfma_f32_16x16x32_f16 v[64:67], v[112:115], v[48:51], v[64:67]\n\tv_cvt_pk_f16_f32 v93, v62, v63\n\t" \
  "v_alignbit_b32 v96, v95, v94, 16\n\tv_perm_b32 v6, v4, v4, v33\n\t" \
  "v_mfma_f32_16x16x32_f16 v[68:71], v[48:51], v[144:147], v[68:71]\n\tv_perm_b32 v7, v4, v4, v34\n\t" \
  "v_mfma_f32_16x16x32_f16 v[8:11], v[144:147], v[48:51], v[8:11]\n\tds_write_b128 v58, v[100:103]\n\t" \
  "v_mfma_f32_16x16x32_f16 v[64:67], v[116:119], v[72:75], v[64:67]\n\tds_write2_b32 v56, v92, v93 offset0:193 offset1:194\n\t" \
  "v_mfma_f32_16x16x32_f16 v[68:71], v[72:75], v[148:151], v[68:71]\n\tds_write2_b32 v57, v92, v93 offset0:193 offset1:194\n\t" \
  "v_mfma_f32_16x16x32_f16 v[8:11], v[148:151], v[72:75], v[8:11]\n\tds_write2_b32 v14, v6, v7 offset0:64 offset1:65\n\t" \
  "v_mfma_f32_16x16x32_f16 v[64:67], v[120:123], v[76:79], v[64:67]\n\tds_write2_b32 v15, v6, v7 offset0:64 offset1:65\n\t" \
  "v_mfma_f32_16x16x32_f16 v[68:71], v[76:79], v[152:155], v[68:71]\n\tds_write2_b32 v59, v94, v95 offset0:4 offset1:5\n\t" \
  "v_mfma_f32_16x16x32_f16 v[8:11], v[152:155], v[76:79], v[8:11]\n\tds_write_b16 v59, v94 offset:846\n\t" \
  "v_mfma_f32_16x16x32_f16 v[64:67], v[124:127], v[80:83], v[64:67]\n\tds_write_b32 v59, v96 offset:848\n\t" \
  "v_mfma_f32_16x16x32_f16 v[68:71], v[80:83], v[156:159], v[68:71]\n\tds_write_b16_d16_hi v59, v95 offset:852\n\t" \
  "v_mfma_f32_16x16x32_f16 v[8:11], v[156:159], v[80:83], v[8:11]\n\tv_mfma_f32_16x16x32_f16 v[64:67], v[128:131], v[84:87], v[64:67]\n\t" \
  "v_mfma_f32_16x16x32_f16 v[68:71], v[84:87], v[160:163], v[68:71]\n\tv_mfma_f32_16x16x32_f16 v[8:11], v[160:163], v[84:87], v[8:11]\n\t" \
  "v_mfma_f32_16x16x32_f16 v[68:71], v[88:91], v[164:167], v[68:71]\n\tv_mfma_f32_16x16x32_f16 v[8:11], v[164:167], v[88:91], v[8:11]\n\t" \
  "v_mfma_f32_16x16x32_f16 v[64:67], v[132:135], v[88:91], v[64:67]\n\ts_nop 5\n\t" \
  "v_cvt_pk_f16_f32 v48, v68, v69\n\tv_cvt_pk_f16_f32 v49, v70, v71\n\t" \
  "v_cvt_pk_f16_f32 v8, v8, v9\n\tv_cvt_pk_f16_f32 v9, v10, v11\n\t" \
  "v_mov_b32_dpp v48, v48 row_shl:4 row_mask:0xf bank_mask:0x5\n\tv_mov_b32_dpp v49, v49 row_shl:4 row_mask:0xf bank_mask:0x5\n\t" \
  "v_permlane16_swap_b32 v8, v9\n\tv_cvt_pk_f16_f32 v46, v64, v65\n\t" \
  "v_cvt_pk_f16_f32 v47, v66, v67\n\tv_alignbit_b32 v50, v49, v48, 16\n\t" \
  "v_perm_b32 v10, v8, v8, v33\n\tv_perm_b32 v11, v8, v8, v34\n\t" \
  "ds_write2_b32 v56, v46, v47 offset0:129 offset1:130\n\tds_write2_b32 v57, v46, v47 offset0:129 offset1:130\n\t" \
  "ds_write2_b32 v14, v10, v11 offset0:0 offset1:1\n\tds_write2_b32 v15, v10, v11 offset0:0 offset1:1\n\t" \
  "ds_write2_b32 v59, v48, v49 offset0:0 offset1:1\n\tds_write_b16 v59, v48 offset:830\n\t" \
  "ds_write_b32 v59, v50 offset:832\n\tds_write_b16_d16_hi v59, v49 offset:836\n\t"
#define GEN_CLOBBER "v4", "v5", "v6", "v7", "v9", "v10", "v11", "v13", "v14", "v15", "v36", "v37", "v38", "v39", "v40", "v41", "v42", "v43", "v44", "v45", "v47", "v49", "v50", "v51", "v52", "v53", "v54", "v55", "v56", "v57", "v58", "v59", "v60", "v61", "v62", "v63", "v64", "v65", "v66", "v67", "v72", "v73", "v74", "v75", "v76", "v77", "v78", "v79", "v80", "v81", "v82", "v83", "v84", "v85", "v86", "v87", "v88", "v89", "v90", "v91", "v92", "v94", "v95", "v98", "v99", "v100", "v101", "v102", "v103", "v104", "v105", "v106", "v107", "v108", "v109", "v110", "v111", "v112", "v113", "v114", "v115", "v116", "v117", "v118", "v119", "v120", "v121", "v122", "v123", "v124", "v125", "v126", "v127", "v128", "v129", "v130", "v131", "v132", "v133", "v134", "v135", "v136", "v137", "v138", "v139", "v140", "v141", "v142", "v143", "v144", "v145", "v146", "v147", "v148", "v149", "v150", "v151", "v152", "v153", "v154", "v155", "v156", "v157", "v158", "v159", "v160", "v161", "v162", "v163", "v164", "v165", "v166", "v167"
#define CONV_ASM \
  "ds_read_b32 v34, v30\n\tds_read_b64 v[132:133], v26 offset:512\n\tds_read_b64 v[134:135], v26 offset:520\n\t" \
  "ds_read_b64 v[100:101], v26 offset:0\n\tds_read_b64 v[102:103], v26 offset:8\n\tds_read_b64 v[136:137], v26 offset:576\n\t" \
  "ds_read_b64 v[138:139], v26 offset:584\n\tds_read_b64 v[104:105], v26 offset:64\n\tds_read_b64 v[106:107], v26 offset:72\n\t" \
  "ds_read_b64 v[140:141], v26 offset:640\n\tds_read_b64 v[142:143], v26 offset:648\n\tds_read_b64 v[108:109], v26 offset:128\n\t" \
  "ds_read_b64 v[110:111], v26 offset:136\n\tds_read_b64 v[144:145], v26 offset:704\n\tds_read_b64 v[146:147], v26 offset:712\n\t" \
  "ds_read_b64 v[112:113], v26 offset:192\n\tds_read_b64 v[114:115], v26 offset:200\n\tds_read_b64 v[148:149], v26 offset:768\n\t" \
  "ds_read_b64 v[150:151], v26 offset:776\n\tds_read_b64 v[116:117], v26 offset:256\n\tds_read_b64 v[118:119], v26 offset:264\n\t" \
  TS(60, 61) "s_waitcnt lgkmcnt(15)\n\tv_cmp_gt_u32 vcc, 1, v34\n\t" \
  "s_cbranch_vccz 2f\n\ts_mov_b32 s6, 0x10000\n\t1:\n\t" \
  "s_sleep 1\n\tds_read_b32 v34, v30\n\ts_sub_u32 s6, s6, 1\n\t" \
  "s_waitcnt lgkmcnt(0)\n\tv_cmp_gt_u32 vcc, 1, v34\n\ts_cbranch_scc1 2f\n\t" \
  "s_cbranch_vccnz 1b\n\t2:\n\t" TS(62, 63) \
  "s_cmp_lt_u32 %[w], 4\n\ts_setprio 1\n\ts_cbranch_scc1 1f\n\t" \
  "s_setprio 2\n\t1:\n\tds_read_b128 v[0:3], v28 offset:0\n\t" \
  "ds_read_b128 v[4:7], v28 offset:64\n\tds_read_b128 v[8:11], v28 offset:128\n\tds_read_b128 v[12:15], v28 offset:192\n\t" \
  "ds_read_b128 v[16:19], v28 offset:256\n\tds_read_b128 v[20:23], v28 offset:320\n\ts_waitcnt lgkmcnt(5)\n\t" \
  "v_mfma_f32_16x16x32_f16 v[52:55], v[132:135], v[0:3], 0\n\tds_read_b64 v[152:153], v26 offset:832\n\tds_read_b64 v[154:155], v26 offset:840\n\t" \
  "v_mfma_f32_16x16x32_f16 v[56:59], v[100:103], v[0:3], 0\n\tds_read_b64 v[120:121], v26 offset:320\n\tds_read_b64 v[122:123], v26 offset:328\n\t" \
  "ds_read_b128 v[0:3], v28 offset:384\n\ts_waitcnt lgkmcnt(9)\n\tv_mfma_f32_16x16x32_f16 v[52:55], v[136:139], v[4:7], v[52:55]\n\t" \
  "ds_read_b64 v[156:157], v26 offset:896\n\tds_read_b64 v[158:159], v26 offset:904\n\tv_mfma_f32_16x16x32_f16 v[56:59], v[104:107], v[4:7], v[56:59]\n\t" \
  "ds_read_b64 v[124:125], v26 offset:384\n\tds_read_b64 v[126:127], v26 offset:392\n\tds_read_b128 v[4:7], v28 offset:448\n\t" \
  "s_waitcnt lgkmcnt(13)\n\tv_mfma_f32_16x16x32_f16 v[52:55], v[140:143], v[8:11], v[52:55]\n\tds_read_b64 v[160:161], v26 offset:960\n\t" \
  "ds_read_b64 v[162:163], v26 offset:968\n\tv_mfma_f32_16x16x32_f16 v[56:59], v[108:111], v[8:11], v[56:59]\n\tds_read_b64 v[128:129], v26 offset:448\n\t" \
  "ds_read_b64 v[130:131], v26 offset:456\n\tds_read_b128 v[8:11], v28 offset:512\n\ts_waitcnt lgkmcnt(15)\n\t" \
  "v_mfma_f32_16x16x32_f16 v[52:55], v[144:147], v[12:15], v[52:55]\n\tds_read_b64 v[164:165], v26 offset:1024\n\tds_read_b64 v[166:167], v26 offset:1032\n\t" \
  "v_mfma_f32_16x16x32_f16 v[56:59], v[112:115], v[12:15], v[56:59]\n\tds_read_b64 v[76:77], v31 offset:72\n\tds_read_b64 v[78:79], v31 offset:176\n\t" \
  "ds_read_b128 v[12:15], v28 offset:576\n\tv_mfma_f32_16x16x32_f16 v[52:55], v[148:151], v[16:19], v[52:55]\n\tds_read_b64 v[80:81], v31 offset:40\n\t" \
  "ds_read_b64 v[82:83], v31 offset:144\n\tv_mfma_f32_16x16x32_f16 v[56:59], v[116:119], v[16:19], v[56:59]\n\tds_read_b64 v[84:85], v31 offset:8\n\t" \
  "ds_read_b64 v[86:87], v31 offset:112\n\tds_read_b128 v[16:19], v28 offset:640\n\ts_waitcnt lgkmcnt(15)\n\t" \
  "v_mfma_f32_16x16x32_f16 v[52:55], v[152:155], v[20:23], v[52:55]\n\tds_read_b64 v[88:89], v31 offset:280\n\tds_read_b64 v[90:91], v31 offset:384\n\t" \
  "v_mfma_f32_16x16x32_f16 v[56:59], v[120:123], v[20:23], v[56:59]\n\tds_read_b64 v[92:93], v31 offset:248\n\tds_read_b64 v[94:95], v31 offset:352\n\t" \
  "ds_read_b128 v[20:23], v28 offset:704\n\tv_mfma_f32_16x16x32_f16 v[52:55], v[156:159], v[0:3], v[52:55]\n\tds_read_b64 v[96:97], v31 offset:216\n\t" \
  "ds_read_b64 v[98:99], v31 offset:320\n\tv_mfma_f32_16x16x32_f16 v[56:59], v[124:127], v[0:3], v[56:59]\n\tds_read_b128 v[0:3], v28 offset:768\n\t" \
  "s_waitcnt lgkmcnt(15)\n\tv_mfma_f32_16x16x32_f16 v[52:55], v[160:163], v[4:7], v[52:55]\n\tv_mfma_f32_16x16x32_f16 v[56:59], v[128:131], v[4:7], v[56:59]\n\t" \
  "ds_read_b128 v[4:7], v28 offset:832\n\tv_mfma_f32_16x16x32_f16 v[52:55], v[164:167], v[8:11], v[52:55]\n\tv_mov_b32 v33, 1\n\t" \
  "v_mfma_f32_16x16x32_f16 v[56:59], v[132:135], v[8:11], v[56:59]\n\tv_mfma_f32_16x16x32_f16 v[60:63], v[100:103], v[8:11], 0\n\tds_read_b128 v[8:11], v28 offset:896\n\t" \
  "s_waitcnt lgkmcnt(15)\n\tv_mfma_f32_16x16x32_f16 v[56:59], v[136:139], v[12:15], v[56:59]\n\tds_read_b32 v34, v30\n\t" \
  "v_mfma_f32_16x16x32_f16 v[60:63], v[104:107], v[12:15], v[60:63]\n\tds_read_b128 v[12:15], v28 offset:960\n\tv_cvt_pk_bf16_f32 v52, v52, v53\n\t" \
  "v_cvt_pk_bf16_f32 v53, v54, v55\n\t" TS(64, 65) "s_waitcnt lgkmcnt(12)\n\t" \
  "v_mfma_f32_16x16x32_f16 v[56:59], v[140:143], v[16:19], v[56:59]\n\tds_write_b64 v29, v[52:53] offset:0\n\tds_write_b32 v25, v33\n\t" \
  "v_mfma_f32_16x16x32_f16 v[60:63], v[108:111], v[16:19], v[60:63]\n\ts_waitcnt lgkmcnt(3)\n\tv_cmp_gt_u32 vcc, 2, v34\n\t" \
  "s_cbranch_vccz 2f\n\ts_mov_b32 s6, 0x10000\n\t1:\n\t" \
  "s_sleep 1\n\tds_read_b32 v34, v30\n\ts_sub_u32 s6, s6, 1\n\t" \
  "s_waitcnt lgkmcnt(0)\n\tv_cmp_gt_u32 vcc, 2, v34\n\ts_cbranch_scc1 2f\n\t" \
  "s_cbranch_vccnz 1b\n\t2:\n\tds_read_b128 v[16:19], v28 offset:1024\n\t" \
  "v_mfma_f32_16x16x32_f16 v[56:59], v[144:147], v[20:23], v[56:59]\n\tv_mfma_f32_16x16x32_f16 v[60:63], v[112:115], v[20:23], v[60:63]\n\tds_read_b128 v[68:71], v32\n\t" \
  "ds_read_b128 v[72:75], v32 offset:1024\n\tds_read_b128 v[20:23], v28 offset:1088\n\tv_mfma_f32_16x16x32_f16 v[56:59], v[148:151], v[0:3], v[56:59]\n\t" \
  "v_mfma_f32_16x16x32_f16 v[60:63], v[116:119], v[0:3], v[60:63]\n\tds_read_b128 v[0:3], v28 offset:496\n\tv_mfma_f32_16x16x32_f16 v[56:59], v[152:155], v[4:7], v[56:59]\n\t" \
  "v_mfma_f32_16x16x32_f16 v[60:63], v[120:123], v[4:7], v[60:63]\n\tds_read_b128 v[4:7], v28 offset:1152\n\tv_mfma_f32_16x16x32_f16 v[56:59], v[156:159], v[8:11], v[56:59]\n\t" \
  "v_mfma_f32_16x16x32_f16 v[60:63], v[124:127], v[8:11], v[60:63]\n\tds_read_b128 v[8:11], v28 offset:1216\n\ts_waitcnt lgkmcnt(9)\n\t" \
  "v_mfma_f32_16x16x32_f16 v[56:59], v[160:163], v[12:15], v[56:59]\n\tv_mfma_f32_16x16x32_f16 v[60:63], v[128:131], v[12:15], v[60:63]\n\tds_read_b128 v[12:15], v28 offset:480\n\t" \
  "s_waitcnt lgkmcnt(7)\n\tv_mfma_f32_16x16x32_f16 v[56:59], v[164:167], v[16:19], v[56:59]\n\tv_mov_b32 v33, 2\n\t" \
  "v_mfma_f32_16x16x32_f16 v[60:63], v[132:135], v[16:19], v[60:63]\n\tv_mfma_f32_16x16x32_f16 v[64:67], v[100:103], v[16:19], 0\n\tds_read_b128 v[16:19], v28 offset:1280\n\t" \
  "s_waitcnt lgkmcnt(5)\n\tv_mfma_f32_16x16x32_f16 v[60:63], v[136:139], v[20:23], v[60:63]\n\tv_mfma_f32_16x16x32_f16 v[64:67], v[104:107], v[20:23], v[64:67]\n\t" \
  "ds_read_b128 v[20:23], v28 offset:1344\n\tv_cvt_pk_bf16_f32 v56, v56, v57\n\tv_cvt_pk_bf16_f32 v57, v58, v59\n\t" \
  "s_waitcnt lgkmcnt(5)\n\tv_mfma_f32_16x16x32_f16 v[36:39], v[0:3], v[68:71], 0\n\tds_write_b64 v29, v[56:57] offset:512\n\t" \
  "ds_write_b32 v25, v33\n\tv_mfma_f32_16x16x32_f16 v[40:43], v[0:3], v[72:75], 0\n\tds_read_b128 v[0:3], v28 offset:1408\n\t" \
  "s_waitcnt lgkmcnt(7)\n\tv_mfma_f32_16x16x32_f16 v[60:63], v[140:143], v[4:7], v[60:63]\n\tv_mfma_f32_16x16x32_f16 v[64:67], v[108:111], v[4:7], v[64:67]\n\t" \
  "ds_read_b128 v[4:7], v28 offset:1472\n\tv_cvt_pk_f16_f32 v36, v36, v37\n\tv_cvt_pk_f16_f32 v37, v38, v39\n\t" \
  "s_waitcnt lgkmcnt(7)\n\tv_mfma_f32_16x16x32_f16 v[60:63], v[144:147], v[8:11], v[60:63]\n\tv_cvt_pk_f16_f32 v38, v40, v41\n\t" \
  "v_cvt_pk_f16_f32 v39, v42, v43\n\tv_mfma_f32_16x16x32_f16 v[64:67], v[112:115], v[8:11], v[64:67]\n\tds_read_b128 v[8:11], v28 offset:1536\n\t" \
  "v_mfma_f32_16x16x32_f16 v[60:63], v[36:39], v[76:79], v[60:63]\n\tv_mfma_f32_16x16x32_f16 v[64:67], v[36:39], v[80:83], v[64:67]\n\tv_mfma_f32_16x16x32_f16 v[52:55], v[36:39], v[84:87], 0\n\t" \
  "s_waitcnt lgkmcnt(7)\n\tv_mfma_f32_16x16x32_f16 v[44:47], v[12:15], v[68:71], 0\n\tv_mfma_f32_16x16x32_f16 v[40:43], v[12:15], v[72:75], 0\n\t" \
  "ds_read_b128 v[12:15], v28 offset:1600\n\ts_waitcnt lgkmcnt(7)\n\tv_mfma_f32_16x16x32_f16 v[60:63], v[148:151], v[16:19], v[60:63]\n\t" \
  "v_mfma_f32_16x16x32_f16 v[64:67], v[116:119], v[16:19], v[64:67]\n\tds_read_b128 v[16:19], v28 offset:1008\n\ts_waitcnt lgkmcnt(7)\n\t" \
  "v_mfma_f32_16x16x32_f16 v[60:63], v[152:155], v[20:23], v[60:63]\n\tv_cvt_pk_f16_f32 v44, v44, v45\n\tv_cvt_pk_f16_f32 v45, v46, v47\n\t" \
  "v_cvt_pk_f16_f32 v46, v40, v41\n\tv_cvt_pk_f16_f32 v47, v42, v43\n\tv_mfma_f32_16x16x32_f16 v[64:67], v[120:123], v[20:23], v[64:67]\n\t" \
  "ds_read_b128 v[20:23], v28 offset:1664\n\tv_mfma_f32_16x16x32_f16 v[60:63], v[44:47], v[88:91], v[60:63]\n\tv_mfma_f32_16x16x32_f16 v[64:67], v[44:47], v[92:95], v[64:67]\n\t" \
  "v_mfma_f32_16x16x32_f16 v[52:55], v[44:47], v[96:99], v[52:55]\n\ts_waitcnt lgkmcnt(5)\n\tv_mfma_f32_16x16x32_f16 v[60:63], v[156:159], v[0:3], v[60:63]\n\t" \
  "v_mfma_f32_16x16x32_f16 v[64:67], v[124:127], v[0:3], v[64:67]\n\tds_read_b128 v[0:3], v28 offset:1728\n\ts_waitcnt lgkmcnt(5)\n\t" \
  "v_mfma_f32_16x16x32_f16 v[60:63], v[160:163], v[4:7], v[60:63]\n\tv_mfma_f32_16x16x32_f16 v[64:67], v[128:131], v[4:7], v[64:67]\n\tds_read_b128 v[4:7], v28 offset:992\n\t" \
  "s_waitcnt lgkmcnt(5)\n\tv_mfma_f32_16x16x32_f16 v[60:63], v[164:167], v[8:11], v[60:63]\n\tv_mov_b32 v33, 3\n\t" \
  "v_mfma_f32_16x16x32_f16 v[64:67], v[132:135], v[8:11], v[64:67]\n\tv_mfma_f32_16x16x32_f16 v[52:55], v[100:103], v[8:11], v[52:55]\n\tds_read_b128 v[8:11], v28 offset:1792\n\t" \
  "s_waitcnt lgkmcnt(5)\n\tv_mfma_f32_16x16x32_f16 v[64:67], v[136:139], v[12:15], v[64:67]\n\tv_mfma_f32_16x16x32_f16 v[52:55], v[104:107], v[12:15], v[52:55]\n\t" \
  "ds_read_b128 v[12:15], v28 offset:1856\n\tv_cvt_pk_bf16_f32 v60, v60, v61\n\tv_cvt_pk_bf16_f32 v61, v62, v63\n\t" \
  "s_cmp_lt_u32 %[w], 4\n\ts_cbranch_scc1 1f\n\ts_setprio 1\n\t" \
  "1:\n\ts_waitcnt lgkmcnt(5)\n\tv_mfma_f32_16x16x32_f16 v[36:39], v[16:19], v[68:71], 0\n\t" \
  "ds_write_b64 v29, v[60:61] offset:1024\n\tds_write_b32 v25, v33\n\tv_mfma_f32_16x16x32_f16 v[40:43], v[16:19], v[72:75], 0\n\t" \
  "ds_read_b128 v[16:19], v28 offset:1920\n\ts_waitcnt lgkmcnt(7)\n\tv_mfma_f32_16x16x32_f16 v[64:67], v[140:143], v[20:23], v[64:67]\n\t" \
  "ds_read_b32 v34, v30\n\tv_mfma_f32_16x16x32_f16 v[52:55], v[108:111], v[20:23], v[52:55]\n\tds_read_b128 v[20:23], v28 offset:1984\n\t" \
  "v_cvt_pk_f16_f32 v36, v36, v37\n\tv_cvt_pk_f16_f32 v37, v38, v39\n\ts_waitcnt lgkmcnt(8)\n\t" \
  "v_mfma_f32_16x16x32_f16 v[64:67], v[144:147], v[0:3], v[64:67]\n\tv_cvt_pk_f16_f32 v38, v40, v41\n\tv_cvt_pk_f16_f32 v39, v42, v43\n\t" \
  "v_mfma_f32_16x16x32_f16 v[52:55], v[112:115], v[0:3], v[52:55]\n\ts_waitcnt lgkmcnt(1)\n\tv_cmp_gt_u32 vcc, 3, v34\n\t" \
  "s_cbranch_vccz 2f\n\ts_mov_b32 s6, 0x10000\n\t1:\n\t" \
  "s_sleep 1\n\tds_read_b32 v34, v30\n\ts_sub_u32 s6, s6, 1\n\t" \
  "s_waitcnt lgkmcnt(0)\n\tv_cmp_gt_u32 vcc, 3, v34\n\ts_cbranch_scc1 2f\n\t" \
  "s_cbranch_vccnz 1b\n\t2:\n\tds_read_b128 v[0:3], v28 offset:2048\n\t" \
  "v_mfma_f32_16x16x32_f16 v[64:67], v[36:39], v[76:79], v[64:67]\n\tv_mfma_f32_16x16x32_f16 v[52:55], v[36:39], v[80:83], v[52:55]\n\tv_mfma_f32_16x16x32_f16 v[56:59], v[36:39], v[84:87], 0\n\t" \
  "v_mfma_f32_16x16x32_f16 v[44:47], v[4:7], v[68:71], 0\n\tv_mfma_f32_16x16x32_f16 v[40:43], v[4:7], v[72:75], 0\n\tds_read_b128 v[4:7], v28 offset:2112\n\t" \
  "v_mfma_f32_16x16x32_f16 v[64:67], v[148:151], v[8:11], v[64:67]\n\tv_mfma_f32_16x16x32_f16 v[52:55], v[116:119], v[8:11], v[52:55]\n\tds_read_b128 v[8:11], v28 offset:1520\n\t" \
  "v_mfma_f32_16x16x32_f16 v[64:67], v[152:155], v[12:15], v[64:67]\n\tv_mfma_f32_16x16x32_f16 v[52:55], v[120:123], v[12:15], v[52:55]\n\tds_read_b128 v[12:15], v28 offset:2176\n\t" \
  "v_cvt_pk_f16_f32 v44, v44, v45\n\tv_cvt_pk_f16_f32 v45, v46, v47\n\tv_cvt_pk_f16_f32 v46, v40, v41\n\t" \
  "v_cvt_pk_f16_f32 v47, v42, v43\n\ts_nop 1\n\tv_mfma_f32_16x16x32_f16 v[64:67], v[44:47], v[88:91], v[64:67]\n\t" \
  "v_mfma_f32_16x16x32_f16 v[52:55], v[44:47], v[92:95], v[52:55]\n\tv_mfma_f32_16x16x32_f16 v[56:59], v[44:47], v[96:99], v[56:59]\n\tv_mfma_f32_16x16x32_f16 v[64:67], v[156:159], v[16:19], v[64:67]\n\t" \
  "v_mfma_f32_16x16x32_f16 v[52:55], v[124:127], v[16:19], v[52:55]\n\tds_read_b128 v[16:19], v28 offset:2240\n\ts_waitcnt lgkmcnt(5)\n\t" \
  "v_mfma_f32_16x16x32_f16 v[64:67], v[160:163], v[20:23], v[64:67]\n\tv_mfma_f32_16x16x32_f16 v[52:55], v[128:131], v[20:23], v[52:55]\n\tds_read_b128 v[20:23], v28 offset:1504\n\t" \
  "s_waitcnt lgkmcnt(5)\n\tv_mfma_f32_16x16x32_f16 v[64:67], v[164:167], v[0:3], v[64:67]\n\tv_mov_b32 v33, 4\n\t" \
  "v_mfma_f32_16x16x32_f16 v[52:55], v[132:135], v[0:3], v[52:55]\n\tv_mfma_f32_16x16x32_f16 v[56:59], v[100:103], v[0:3], v[56:59]\n\tds_read_b128 v[0:3], v28 offset:2304\n\t" \
  "s_waitcnt lgkmcnt(5)\n\tv_mfma_f32_16x16x32_f16 v[52:55], v[136:139], v[4:7], v[52:55]\n\tv_mfma_f32_16x16x32_f16 v[56:59], v[104:107], v[4:7], v[56:59]\n\t" \
  "ds_read_b128 v[4:7], v28 offset:2368\n\tv_cvt_pk_bf16_f32 v64, v64, v65\n\tv_cvt_pk_bf16_f32 v65, v66, v67\n\t" \
  TS(66, 67) "s_waitcnt lgkmcnt(5)\n\tv_mfma_f32_16x16x32_f16 v[36:39], v[8:11], v[68:71], 0\n\t" \
  "ds_write_b64 v29, v[64:65] offset:1536\n\tds_write_b32 v25, v33\n\tv_mfma_f32_16x16x32_f16 v[40:43], v[8:11], v[72:75], 0\n\t" \
  "ds_read_b128 v[8:11], v28 offset:480\n\ts_waitcnt lgkmcnt(7)\n\tv_mfma_f32_16x16x32_f16 v[52:55], v[140:143], v[12:15], v[52:55]\n\t" \
  "v_mfma_f32_16x16x32_f16 v[56:59], v[108:111], v[12:15], v[56:59]\n\tds_read_b128 v[12:15], v32 offset:3072\n\tv_cvt_pk_f16_f32 v36, v36, v37\n\t" \
  "v_cvt_pk_f16_f32 v37, v38, v39\n\ts_waitcnt lgkmcnt(7)\n\tv_mfma_f32_16x16x32_f16 v[52:55], v[144:147], v[16:19], v[52:55]\n\t" \
  "v_cvt_pk_f16_f32 v38, v40, v41\n\tv_cvt_pk_f16_f32 v39, v42, v43\n\tv_mfma_f32_16x16x32_f16 v[56:59], v[112:115], v[16:19], v[56:59]\n\t" \
  "ds_read_b128 v[16:19], v28 offset:496\n\tv_mfma_f32_16x16x32_f16 v[52:55], v[36:39], v[76:79], v[52:55]\n\tv_mfma_f32_16x16x32_f16 v[56:59], v[36:39], v[80:83], v[56:59]\n\t" \
  "v_mfma_f32_16x16x32_f16 v[60:63], v[36:39], v[84:87], 0\n\ts_waitcnt lgkmcnt(7)\n\tv_mfma_f32_16x16x32_f16 v[44:47], v[20:23], v[68:71], 0\n\t" \
  "v_mfma_f32_16x16x32_f16 v[40:43], v[20:23], v[72:75], 0\n\tds_read_b128 v[20:23], v32 offset:2048\n\ts_waitcnt lgkmcnt(7)\n\t" \
  "v_mfma_f32_16x16x32_f16 v[52:55], v[148:151], v[0:3], v[52:55]\n\tv_mfma_f32_16x16x32_f16 v[56:59], v[116:119], v[0:3], v[56:59]\n\tds_read_b128 v[0:3], v28 offset:2432\n\t" \
  "s_waitcnt lgkmcnt(7)\n\tv_mfma_f32_16x16x32_f16 v[52:55], v[152:155], v[4:7], v[52:55]\n\tv_cvt_pk_f16_f32 v44, v44, v45\n\t" \
  "v_cvt_pk_f16_f32 v45, v46, v47\n\tv_cvt_pk_f16_f32 v46, v40, v41\n\tv_cvt_pk_f16_f32 v47, v42, v43\n\t" \
  "v_mfma_f32_16x16x32_f16 v[56:59], v[120:123], v[4:7], v[56:59]\n\tds_read_b128 v[4:7], v28 offset:2496\n\tv_mfma_f32_16x16x32_f16 v[52:55], v[44:47], v[88:91], v[52:55]\n\t" \
  "v_mfma_f32_16x16x32_f16 v[56:59], v[44:47], v[92:95], v[56:59]\n\tv_mfma_f32_16x16x32_f16 v[60:63], v[44:47], v[96:99], v[60:63]\n\ts_waitcnt lgkmcnt(4)\n\t" \
  "v_mfma_f32_16x16x32_f16 v[40:43], v[8:11], v[12:15], 0\n\tds_read_b64 v[8:9], v31 offset:544\n\tds_read_b64 v[10:11], v31 offset:712\n\t" \
  "ds_read_b64 v[12:13], v31 offset:512\n\tds_read_b64 v[14:15], v31 offset:680\n\ts_waitcnt lgkmcnt(7)\n\t" \
  "v_mfma_f32_16x16x32_f16 v[52:55], v[48:51], v[16:19], v[52:55]\n\ts_waitcnt lgkmcnt(6)\n\tv_mfma_f32_16x16x32_f16 v[36:39], v[16:19], v[20:23], 0\n\t" \
  "ds_read_b64 v[16:17], v31 offset:480\n\tds_read_b64 v[18:19], v31 offset:648\n\tds_read_b64 v[20:21], v31 offset:448\n\t" \
  "ds_read_b64 v[22:23], v31 offset:616\n\ts_waitcnt lgkmcnt(9)\n\tv_mfma_f32_16x16x32_f16 v[52:55], v[156:159], v[0:3], v[52:55]\n\t" \
  "v_mfma_f32_16x16x32_f16 v[56:59], v[124:127], v[0:3], v[56:59]\n\tds_read_b128 v[0:3], v28 offset:2560\n\tv_cvt_pk_f16_f32 v36, v36, v37\n\t" \
  "v_cvt_pk_f16_f32 v37, v38, v39\n\ts_waitcnt lgkmcnt(9)\n\tv_mfma_f32_16x16x32_f16 v[52:55], v[160:163], v[4:7], v[52:55]\n\t" \
  "v_cvt_pk_f16_f32 v38, v40, v41\n\tv_cvt_pk_f16_f32 v39, v42, v43\n\tv_mfma_f32_16x16x32_f16 v[56:59], v[128:131], v[4:7], v[56:59]\n\t" \
  "ds_read_b128 v[4:7], v28 offset:2624\n\ts_waitcnt lgkmcnt(8)\n\tv_mfma_f32_16x16x32_f16 v[52:55], v[36:39], v[8:11], v[52:55]\n\t" \
  "ds_read_b128 v[8:11], v28 offset:2032\n\ts_waitcnt lgkmcnt(7)\n\tv_mfma_f32_16x16x32_f16 v[56:59], v[36:39], v[12:15], v[56:59]\n\t" \
  "ds_read_b128 v[12:15], v28 offset:2688\n\ts_waitcnt lgkmcnt(6)\n\tv_mfma_f32_16x16x32_f16 v[60:63], v[36:39], v[16:19], v[60:63]\n\t" \
  "ds_read_b128 v[16:19], v28 offset:2752\n\ts_waitcnt lgkmcnt(5)\n\tv_mfma_f32_16x16x32_f16 v[64:67], v[36:39], v[20:23], 0\n\t" \
  "ds_read_b128 v[20:23], v28 offset:2016\n\ts_waitcnt lgkmcnt(5)\n\tv_mfma_f32_16x16x32_f16 v[52:55], v[164:167], v[0:3], v[52:55]\n\t" \
  "v_mov_b32 v33, 5\n\tv_mfma_f32_16x16x32_f16 v[56:59], v[132:135], v[0:3], v[56:59]\n\tv_mfma_f32_16x16x32_f16 v[60:63], v[100:103], v[0:3], v[60:63]\n\t" \
  "ds_read_b128 v[0:3], v28 offset:2816\n\ts_waitcnt lgkmcnt(5)\n\tv_mfma_f32_16x16x32_f16 v[56:59], v[136:139], v[4:7], v[56:59]\n\t" \
  "v_mfma_f32_16x16x32_f16 v[60:63], v[104:107], v[4:7], v[60:63]\n\tds_read_b128 v[4:7], v28 offset:2880\n\tv_cvt_pk_bf16_f32 v52, v52, v53\n\t" \
  "v_cvt_pk_bf16_f32 v53, v54, v55\n\ts_cmp_lt_u32 %[w], 4\n\ts_cbranch_scc0 1f\n\t" \
  "s_setprio 0\n\t1:\n\ts_waitcnt lgkmcnt(5)\n\t" \
  "v_mfma_f32_16x16x32_f16 v[44:47], v[8:11], v[68:71], 0\n\tds_write_b64 v29, v[52:53] offset:2048\n\tds_write_b32 v25, v33\n\t" \
  "v_mfma_f32_16x16x32_f16 v[40:43], v[8:11], v[72:75], 0\n\tds_read_b128 v[8:11], v28 offset:992\n\ts_waitcnt lgkmcnt(7)\n\t" \
  "v_mfma_f32_16x16x32_f16 v[56:59], v[140:143], v[12:15], v[56:59]\n\tv_mfma_f32_16x16x32_f16 v[60:63], v[108:111], v[12:15], v[60:63]\n\tds_read_b128 v[12:15], v32 offset:3072\n\t" \
  "v_cvt_pk_f16_f32 v44, v44, v45\n\tv_cvt_pk_f16_f32 v45, v46, v47\n\ts_waitcnt lgkmcnt(7)\n\t" \
  "v_mfma_f32_16x16x32_f16 v[56:59], v[144:147], v[16:19], v[56:59]\n\tv_cvt_pk_f16_f32 v46, v40, v41\n\tv_cvt_pk_f16_f32 v47, v42, v43\n\t" \
  "v_mfma_f32_16x16x32_f16 v[60:63], v[112:115], v[16:19], v[60:63]\n\tds_read_b128 v[16:19], v28 offset:1008\n\tv_mfma_f32_16x16x32_f16 v[56:59], v[44:47], v[76:79], v[56:59]\n\t" \
  "v_mfma_f32_16x16x32_f16 v[60:63], v[44:47], v[80:83], v[60:63]\n\tv_mfma_f32_16x16x32_f16 v[64:67], v[44:47], v[84:87], v[64:67]\n\ts_waitcnt lgkmcnt(7)\n\t" \
  "v_mfma_f32_16x16x32_f16 v[36:39], v[20:23], v[68:71], 0\n\tv_mfma_f32_16x16x32_f16 v[40:43], v[20:23], v[72:75], 0\n\tds_read_b128 v[20:23], v32 offset:2048\n\t" \
  "s_waitcnt lgkmcnt(7)\n\tv_mfma_f32_16x16x32_f16 v[56:59], v[148:151], v[0:3], v[56:59]\n\tv_mfma_f32_16x16x32_f16 v[60:63], v[116:119], v[0:3], v[60:63]\n\t" \
  "ds_read_b128 v[0:3], v28 offset:2944\n\ts_waitcnt lgkmcnt(7)\n\tv_mfma_f32_16x16x32_f16 v[56:59], v[152:155], v[4:7], v[56:59]\n\t" \
  "v_cvt_pk_f16_f32 v36, v36, v37\n\tv_cvt_pk_f16_f32 v37, v38, v39\n\tv_cvt_pk_f16_f32 v38, v40, v41\n\t" \
  "v_cvt_pk_f16_f32 v39, v42, v43\n\tv_mfma_f32_16x16x32_f16 v[60:63], v[120:123], v[4:7], v[60:63]\n\tds_read_b128 v[4:7], v28 offset:3008\n\t" \
  "v_mfma_f32_16x16x32_f16 v[56:59], v[36:39], v[88:91], v[56:59]\n\tv_mfma_f32_16x16x32_f16 v[60:63], v[36:39], v[92:95], v[60:63]\n\tv_mfma_f32_16x16x32_f16 v[64:67], v[36:39], v[96:99], v[64:67]\n\t" \
  "ds_read_b32 v34, v30\n\ts_waitcnt lgkmcnt(5)\n\tv_mfma_f32_16x16x32_f16 v[40:43], v[8:11], v[12:15], 0\n\t" \
  "ds_read_b64 v[8:9], v31 offset:544\n\tds_read_b64 v[10:11], v31 offset:712\n\tds_read_b64 v[12:13], v31 offset:512\n\t" \
  "ds_read_b64 v[14:15], v31 offset:680\n\ts_waitcnt lgkmcnt(8)\n\tv_mfma_f32_16x16x32_f16 v[56:59], v[48:51], v[16:19], v[56:59]\n\t" \
  "s_waitcnt lgkmcnt(7)\n\tv_mfma_f32_16x16x32_f16 v[44:47], v[16:19], v[20:23], 0\n\tds_read_b64 v[16:17], v31 offset:480\n\t" \
  "ds_read_b64 v[18:19], v31 offset:648\n\ts_waitcnt lgkmcnt(6)\n\tv_cmp_gt_u32 vcc, 4, v34\n\t" \
  "s_cbranch_vccz 2f\n\ts_mov_b32 s6, 0x10000\n\t1:\n\t" \
  "s_sleep 1\n\tds_read_b32 v34, v30\n\ts_sub_u32 s6, s6, 1\n\t" \
  "s_waitcnt lgkmcnt(0)\n\tv_cmp_gt_u32 vcc, 4, v34\n\ts_cbranch_scc1 2f\n\t" \
  "s_cbranch_vccnz 1b\n\t2:\n\tds_read_b128 v[20:23], v28 offset:3072\n\t" \
  "v_mfma_f32_16x16x32_f16 v[56:59], v[156:159], v[0:3], v[56:59]\n\tv_mfma_f32_16x16x32_f16 v[60:63], v[124:127], v[0:3], v[60:63]\n\tds_read_b128 v[0:3], v28 offset:3136\n\t" \
  "v_cvt_pk_f16_f32 v44, v44, v45\n\tv_cvt_pk_f16_f32 v45, v46, v47\n\tv_mfma_f32_16x16x32_f16 v[56:59], v[160:163], v[4:7], v[56:59]\n\t" \
  "v_cvt_pk_f16_f32 v46, v40, v41\n\tv_cvt_pk_f16_f32 v47, v42, v43\n\tv_mfma_f32_16x16x32_f16 v[60:63], v[128:131], v[4:7], v[60:63]\n\t" \
  "ds_read_b128 v[4:7], v28 offset:2544\n\ts_waitcnt lgkmcnt(7)\n\tv_mfma_f32_16x16x32_f16 v[56:59], v[44:47], v[8:11], v[56:59]\n\t" \
  "ds_read_b128 v[8:11], v28 offset:3200\n\ts_waitcnt lgkmcnt(6)\n\tv_mfma_f32_16x16x32_f16 v[60:63], v[44:47], v[12:15], v[60:63]\n\t" \
  "ds_read_b128 v[12:15], v28 offset:3264\n\ts_waitcnt lgkmcnt(5)\n\tv_mfma_f32_16x16x32_f16 v[64:67], v[44:47], v[16:19], v[64:67]\n\t" \
  "ds_read_b128 v[16:19], v28 offset:2528\n\ts_waitcnt lgkmcnt(5)\n\tv_mfma_f32_16x16x32_f16 v[56:59], v[164:167], v[20:23], v[56:59]\n\t" \
  "v_mov_b32 v33, 6\n\tv_mfma_f32_16x16x32_f16 v[60:63], v[132:135], v[20:23], v[60:63]\n\tv_mfma_f32_16x16x32_f16 v[64:67], v[100:103], v[20:23], v[64:67]\n\t" \
  "ds_read_b128 v[20:23], v28 offset:3328\n\ts_waitcnt lgkmcnt(5)\n\tv_mfma_f32_16x16x32_f16 v[60:63], v[136:139], v[0:3], v[60:63]\n\t" \
  "v_mfma_f32_16x16x32_f16 v[64:67], v[104:107], v[0:3], v[64:67]\n\tds_read_b128 v[0:3], v28 offset:3392\n\tv_cvt_pk_bf16_f32 v56, v56, v57\n\t" \
  "v_cvt_pk_bf16_f32 v57, v58, v59\n\ts_waitcnt lgkmcnt(5)\n\tv_mfma_f32_16x16x32_f16 v[36:39], v[4:7], v[68:71], 0\n\t" \
  "ds_write_b64 v29, v[56:57] offset:2560\n\tds_write_b32 v25, v33\n\tv_mfma_f32_16x16x32_f16 v[40:43], v[4:7], v[72:75], 0\n\t" \
  "ds_read_b128 v[4:7], v28 offset:1504\n\ts_waitcnt lgkmcnt(7)\n\tv_mfma_f32_16x16x32_f16 v[60:63], v[140:143], v[8:11], v[60:63]\n\t" \
  "v_mfma_f32_16x16x32_f16 v[64:67], v[108:111], v[8:11], v[64:67]\n\tds_read_b128 v[8:11], v32 offset:3072\n\tv_cvt_pk_f16_f32 v36, v36, v37\n\t" \
  "v_cvt_pk_f16_f32 v37, v38, v39\n\ts_waitcnt lgkmcnt(7)\n\tv_mfma_f32_16x16x32_f16 v[60:63], v[144:147], v[12:15], v[60:63]\n\t" \
  "v_cvt_pk_f16_f32 v38, v40, v41\n\tv_cvt_pk_f16_f32 v39, v42, v43\n\tv_mfma_f32_16x16x32_f16 v[64:67], v[112:115], v[12:15], v[64:67]\n\t" \
  "ds_read_b128 v[12:15], v28 offset:1520\n\tv_mfma_f32_16x16x32_f16 v[60:63], v[36:39], v[76:79], v[60:63]\n\tv_mfma_f32_16x16x32_f16 v[64:67], v[36:39], v[80:83], v[64:67]\n\t" \
  "s_waitcnt lgkmcnt(7)\n\tv_mfma_f32_16x16x32_f16 v[44:47], v[16:19], v[68:71], 0\n\tv_mfma_f32_16x16x32_f16 v[40:43], v[16:19], v[72:75], 0\n\t" \
  "ds_read_b128 v[16:19], v32 offset:2048\n\ts_waitcnt lgkmcnt(7)\n\tv_mfma_f32_16x16x32_f16 v[60:63], v[148:151], v[20:23], v[60:63]\n\t" \
  "v_mfma_f32_16x16x32_f16 v[64:67], v[116:119], v[20:23], v[64:67]\n\tds_read_b128 v[20:23], v28 offset:3456\n\ts_waitcnt lgkmcnt(7)\n\t" \
  "v_mfma_f32_16x16x32_f16 v[60:63], v[152:155], v[0:3], v[60:63]\n\tv_cvt_pk_f16_f32 v44, v44, v45\n\tv_cvt_pk_f16_f32 v45, v46, v47\n\t" \
  "v_cvt_pk_f16_f32 v46, v40, v41\n\tv_cvt_pk_f16_f32 v47, v42, v43\n\tv_mfma_f32_16x16x32_f16 v[64:67], v[120:123], v[0:3], v[64:67]\n\t" \
  "ds_read_b128 v[0:3], v28 offset:3520\n\tv_mfma_f32_16x16x32_f16 v[60:63], v[44:47], v[88:91], v[60:63]\n\tv_mfma_f32_16x16x32_f16 v[64:67], v[44:47], v[92:95], v[64:67]\n\t" \
  "s_waitcnt lgkmcnt(4)\n\tv_mfma_f32_16x16x32_f16 v[40:43], v[4:7], v[8:11], 0\n\tds_read_b64 v[4:5], v31 offset:544\n\t" \
  "ds_read_b64 v[6:7], v31 offset:712\n\tds_read_b64 v[8:9], v31 offset:512\n\tds_read_b64 v[10:11], v31 offset:680\n\t" \
  "s_waitcnt lgkmcnt(7)\n\tv_mfma_f32_16x16x32_f16 v[60:63], v[48:51], v[12:15], v[60:63]\n\ts_waitcnt lgkmcnt(6)\n\t" \
  "v_mfma_f32_16x16x32_f16 v[36:39], v[12:15], v[16:19], 0\n\tds_read_b128 v[12:15], v28 offset:3584\n\tds_read_b128 v[16:19], v28 offset:3648\n\t" \
  "s_waitcnt lgkmcnt(7)\n\tv_mfma_f32_16x16x32_f16 v[60:63], v[156:159], v[20:23], v[60:63]\n\tv_mfma_f32_16x16x32_f16 v[64:67], v[124:127], v[20:23], v[64:67]\n\t" \
  "ds_read_b128 v[20:23], v28 offset:3056\n\ts_waitcnt lgkmcnt(7)\n\tv_mfma_f32_16x16x32_f16 v[60:63], v[160:163], v[0:3], v[60:63]\n\t" \
  "v_cvt_pk_f16_f32 v36, v36, v37\n\tv_cvt_pk_f16_f32 v37, v38, v39\n\tv_cvt_pk_f16_f32 v38, v40, v41\n\t" \
  "v_cvt_pk_f16_f32 v39, v42, v43\n\tv_mfma_f32_16x16x32_f16 v[64:67], v[128:131], v[0:3], v[64:67]\n\tds_read_b128 v[0:3], v28 offset:3712\n\t" \
  "s_waitcnt lgkmcnt(6)\n\tv_mfma_f32_16x16x32_f16 v[60:63], v[36:39], v[4:7], v[60:63]\n\tds_read_b128 v[4:7], v28 offset:3776\n\t" \
  "s_waitcnt lgkmcnt(5)\n\tv_mfma_f32_16x16x32_f16 v[64:67], v[36:39], v[8:11], v[64:67]\n\tds_read_b128 v[8:11], v28 offset:3040\n\t" \
  "s_waitcnt lgkmcnt(5)\n\tv_mfma_f32_16x16x32_f16 v[60:63], v[164:167], v[12:15], v[60:63]\n\tv_mov_b32 v33, 7\n\t" \
  "v_mfma_f32_16x16x32_f16 v[64:67], v[132:135], v[12:15], v[64:67]\n\tds_read_b128 v[12:15], v28 offset:3840\n\ts_waitcnt lgkmcnt(5)\n\t" \
  "v_mfma_f32_16x16x32_f16 v[64:67], v[136:139], v[16:19], v[64:67]\n\tds_read_b128 v[16:19], v28 offset:3904\n\ts_waitcnt lgkmcnt(5)\n\t" \
  "v_mfma_f32_16x16x32_f16 v[44:47], v[20:23], v[68:71], 0\n\tv_cvt_pk_bf16_f32 v60, v60, v61\n\tv_cvt_pk_bf16_f32 v61, v62, v63\n\t" \
  "s_cmp_lt_u32 %[w], 4\n\ts_cbranch_scc1 1f\n\ts_setprio 0\n\t" \
  "1:\n\tv_mfma_f32_16x16x32_f16 v[40:43], v[20:23], v[72:75], 0\n\tds_write_b64 v29, v[60:61] offset:3072\n\t" \
  "ds_write_b32 v25, v33\n\tds_read_b128 v[20:23], v28 offset:2016\n\ts_waitcnt lgkmcnt(7)\n\t" \
  "v_mfma_f32_16x16x32_f16 v[64:67], v[140:143], v[0:3], v[64:67]\n\tds_read_b128 v[0:3], v32 offset:3072\n\tv_cvt_pk_f16_f32 v44, v44, v45\n\t" \
  "v_cvt_pk_f16_f32 v45, v46, v47\n\tv_cvt_pk_f16_f32 v46, v40, v41\n\tv_cvt_pk_f16_f32 v47, v42, v43\n\t" \
  "s_waitcnt lgkmcnt(7)\n\tv_mfma_f32_16x16x32_f16 v[64:67], v[144:147], v[4:7], v[64:67]\n\tds_read_b128 v[4:7], v28 offset:2032\n\t" \
  "v_mfma_f32_16x16x32_f16 v[64:67], v[44:47], v[76:79], v[64:67]\n\ts_waitcnt lgkmcnt(7)\n\tv_mfma_f32_16x16x32_f16 v[36:39], v[8:11], v[68:71], 0\n\t" \
  "v_mfma_f32_16x16x32_f16 v[40:43], v[8:11], v[72:75], 0\n\tds_read_b128 v[8:11], v32 offset:2048\n\ts_waitcnt lgkmcnt(7)\n\t" \
  "v_mfma_f32_16x16x32_f16 v[64:67], v[148:151], v[12:15], v[64:67]\n\tds_read_b128 v[12:15], v28 offset:3968\n\ts_waitcnt lgkmcnt(7)\n\t" \
  "v_mfma_f32_16x16x32_f16 v[64:67], v[152:155], v[16:19], v[64:67]\n\tds_read_b128 v[16:19], v28 offset:4032\n\tv_cvt_pk_f16_f32 v36, v36, v37\n\t" \
  "v_cvt_pk_f16_f32 v37, v38, v39\n\tv_cvt_pk_f16_f32 v38, v40, v41\n\tv_cvt_pk_f16_f32 v39, v42, v43\n\t" \
  "s_nop 1\n\tv_mfma_f32_16x16x32_f16 v[64:67], v[36:39], v[88:91], v[64:67]\n\ts_waitcnt lgkmcnt(4)\n\t" \
  "v_mfma_f32_16x16x32_f16 v[40:43], v[20:23], v[0:3], 0\n\tds_read_b64 v[0:1], v31 offset:544\n\tds_read_b64 v[2:3], v31 offset:712\n\t" \
  "ds_read_b128 v[20:23], v28 offset:4096\n\ts_waitcnt lgkmcnt(6)\n\tv_mfma_f32_16x16x32_f16 v[64:67], v[48:51], v[4:7], v[64:67]\n\t" \
  "s_waitcnt lgkmcnt(5)\n\tv_mfma_f32_16x16x32_f16 v[44:47], v[4:7], v[8:11], 0\n\ts_waitcnt lgkmcnt(4)\n\t" \
  "v_mfma_f32_16x16x32_f16 v[64:67], v[156:159], v[12:15], v[64:67]\n\ts_waitcnt lgkmcnt(3)\n\tv_mfma_f32_16x16x32_f16 v[64:67], v[160:163], v[16:19], v[64:67]\n\t" \
  "s_nop 3\n\tv_cvt_pk_f16_f32 v44, v44, v45\n\tv_cvt_pk_f16_f32 v45, v46, v47\n\t" \
  "v_cvt_pk_f16_f32 v46, v40, v41\n\tv_cvt_pk_f16_f32 v47, v42, v43\n\ts_waitcnt lgkmcnt(1)\n\t" \
  "s_nop 0\n\tv_mfma_f32_16x16x32_f16 v[64:67], v[44:47], v[0:3], v[64:67]\n\ts_waitcnt lgkmcnt(0)\n\t" \
  "v_mfma_f32_16x16x32_f16 v[64:67], v[164:167], v[20:23], v[64:67]\n\tv_mov_b32 v33, 8\n\ts_nop 6\n\t" \
  "v_cvt_pk_bf16_f32 v64, v64, v65\n\tv_cvt_pk_bf16_f32 v65, v66, v67\n\tds_write_b64 v29, v[64:65] offset:3584\n\t" \
  "ds_write_b32 v25, v33\n\t"
#define CONV_CLOBBER "v0", "v1", "v2", "v3", "v4", "v5", "v6", "v7", "v8", "v9", "v10", "v11", "v12", "v13", "v14", "v15", "v16", "v17", "v18", "v19", "v20", "v21", "v22", "v23", "v36", "v37", "v38", "v39", "v40", "v41", "v42", "v43", "v44", "v45", "v46", "v47", "v52", "v53", "v54", "v55", "v56", "v57", "v58", "v59", "v60", "v61", "v62", "v63", "v64", "v65", "v66", "v67", "v68", "v69", "v70", "v71", "v72", "v73", "v74", "v75", "v76", "v77", "v78", "v79", "v80", "v81", "v82", "v83", "v84", "v85", "v86", "v87", "v88", "v89", "v90", "v91", "v92", "v93", "v94", "v95", "v96", "v97", "v98", "v99", "v100", "v101", "v102", "v103", "v104", "v105", "v106", "v107", "v108", "v109", "v110", "v111", "v112", "v113", "v114", "v115", "v116", "v117", "v118", "v119", "v120", "v121", "v122", "v123", "v124", "v125", "v126", "v127", "v128", "v129", "v130", "v131", "v132", "v133", "v134", "v135", "v136", "v137", "v138", "v139", "v140", "v141", "v142", "v143", "v144", "v145", "v146", "v147", "v148", "v149", "v150", "v151", "v152", "v153", "v154", "v155", "v156", "v157", "v158", "v159", "v160", "v161", "v162", "v163", "v164", "v165", "v166", "v167"
#define G_CLOBBER "v8", "v9", "v10", "v11", "v12", "v13", "v14", "v15", "v16", "v17", "v18", "v19", "v20", "v21", "v22", "v23", "v24", "v25", "v26", "v27", "v28", "v29", "v30", "v32", "v33", "v34", "v35", "v36", "v37", "v38", "v39", "v40", "v41", "v42", "v43", "v44", "v45", "v46", "v47", "v48", "v49", "v50", "v51", "v52", "v53", "v54", "v55", "v56", "v57", "v58", "v59", "v60", "v61", "v62", "v63", "v64", "v65", "v66", "v67", "v68", "v69", "v70", "v71", "v72", "v73", "v74", "v75", "v76", "v77", "v78", "v79", "v80", "v81", "v82", "v83", "v84", "v85", "v86", "v87", "v88", "v89", "v90", "v91", "v92", "v93", "v94", "v95", "v96", "v97", "v98", "v99", "v100", "v101", "v102", "v103", "v104", "v105", "v106", "v107", "v108", "v109", "v110", "v111", "v112", "v113", "v114", "v115", "v116", "v117", "v118", "v119", "v120", "v121", "v122", "v123", "v124", "v125", "v126", "v127"
#define D_CLOBBER "v8", "v9", "v10", "v11", "v12", "v13", "v14", "v15", "v16", "v17", "v18", "v19", "v20", "v21", "v22", "v23", "v24", "v25", "v26", "v27", "v28", "v29", "v30", "v31"
__device__ __forceinline__ f32x2 pkfma(f32x2 a, f32x2 b, f32x2 c) {
  f32x2 r;
  asm("v_pk_fma_f32 %0, %1, %2, %3" : "=v"(r) : "v"(a), "v"(b), "v"(c));
  return r;
}
__device__ __forceinline__ float fmav(float a, float b, float c) {
  float r;
  asm("v_fma_f32 %0, %1, %2, %3" : "=v"(r) : "v"(a), "v"(b), "v"(c));
  return r;
}
__device__ __forceinline__ float fmac(float a, float b, float c) {
  asm("v_fmac_f32 %0, %1, %2" : "+v"(c) : "v"(a), "v"(b));
  return c;
}
__device__ __forceinline__ float fmas(float a, u32 s, float c) {
  asm("v_fmac_f32 %0, %2, %1" : "+v"(c) : "v"(a), "s"(s));
  return c;
}
template <int HI>
__device__ __forceinline__ f32x2 pkfmas(f32x2 a, f32x2 s, f32x2 c) {
  f32x2 r;
  if (HI) asm("v_pk_fma_f32 %0, %2, %1, %3 op_sel:[1,0,0] op_sel_hi:[1,1,1]" : "=v"(r) : "v"(a), "v"(s), "v"(c));
  else asm("v_pk_fma_f32 %0, %2, %1, %3 op_sel:[0,0,0] op_sel_hi:[0,1,1]" : "=v"(r) : "v"(a), "v"(s), "v"(c));
  return r;
}
#define RANK8(d0, d1, d2, d3, c0, c1, i0, f0, f1) "v_perm_b32 " d0 ", " d0 ", " d1 ", %[sg]\n\tv_perm_b32 " d2 ", " d2 ", " d3 ", %[sg]\n\t" \
  "v_bcnt_u32_b32 " d0 ", " d0 ", " i0 "\n\tv_bcnt_u32_b32 " c0 ", " d2 ", " d0 "\n\tv_bcnt_u32_b32 " c1 ", " d2 ", " d0 "\n\t" f0 "\n\t" \
  "v_permlane16_swap_b32 " c0 ", " c1 "\n\tv_add_u32 " d0 ", " c0 ", " c1 "\n\tv_add_u32 " d1 ", " c0 ", " c1 "\n\t" f1 "\n\t" \
  "v_permlane32_swap_b32 " d0 ", " d1 "\n\tv_add_u32 " c0 ", " d0 ", " d1 "\n\t"
__device__ __forceinline__ void ln_rows(u32x4 (&xv)[4], u32x8 wv, rsrc_t rs, u32& tag, int b, int w, int l, u32 st, u32 sg TLARG) {
  float s[2][3] = {}, c[2], sw = 0.f;
  u32 os = l < 16 ? O_SG + 128 * (b - 128) + 8 * l : 0x80000000u;
  u32 ot = l < 16 ? O_TOP + 8 * (b & 15) + TL * ((b >> 4) & 7) : l < 32 ? O_RG - 128 + 128 * (b - 128) + 8 * l : 0x80000000u, om = l < 16 ? TOPS / 8 : 0u, fm = l < 16 ? 0xff0u : 0u;
  u32 aw = (l & 16) ? st + 8 * w + (l >> 5) * 4 : st + 136, ac = st + 128, ar = st + 4 * (l & 15), aq = st + (l & 48), one = 1u;
  u32 kl = (2047 - (16 * (b - 128) + 2 * w + (l >> 5))) << 1;
  asm volatile("" : "+v"(os), "+v"(aw), "+v"(ac), "+v"(ar), "+v"(aq), "+v"(one), "+v"(kl), "+v"(ot), "+v"(om), "+v"(fm));
  SB();
  asm volatile("s_waitcnt vmcnt(4) lgkmcnt(0)" : "+{s44}"(tag)::"memory");
  asm volatile("" : "+{v[116:123]}"(wv));
  tag += 1u;
  *(volatile LDS u32*)(size_t)(st - 104) = tag;
  STAMP(12);
#pragma unroll
  for (int i = 0; i < 8; i++) sw = dot2b(wv[i], 0x3f803f80u, sw);
  sw += dppf<0x121, 0xf>(sw); sw += dppf<0x122, 0xf>(sw); sw += dppf<0x124, 0xf>(sw); sw += dppf<0x128, 0xf>(sw);
  sw = (rdl(sw, 0) + rdl(sw, 16)) + (rdl(sw, 32) + rdl(sw, 48));
  asm volatile("" : "+v"(sw));
  SB();
  float cv = 0.f;
#pragma unroll
  for (int r = 0; r < 2; r++) {
#pragma unroll
    for (int i = 0; i < 8; i++) {
      if (r == 0 && i == 0) { asm volatile("s_waitcnt vmcnt(3)" : "+{v[100:103]}"(xv[0])); STAMP(13); }
      if (r == 0 && i == 4) asm volatile("s_waitcnt vmcnt(2)" : "+{v[104:107]}"(xv[1]));
      if (r == 1 && i == 0) asm volatile("s_waitcnt vmcnt(1)" : "+{v[108:111]}"(xv[2]));
      if (r == 1 && i == 4) asm volatile("s_waitcnt vmcnt(0)" : "+{v[112:115]}"(xv[3]));
      const u32 a = xv[2 * r + (i >> 2)][i & 3];
      s[r][0] = dot2b(a, 0x3f803f80u, s[r][0]); s[r][1] = dot2b(a, a, s[r][1]); s[r][2] = dot2b(a, wv[i], s[r][2]);
      if (i == 3) {
        c[r] = rdl(s[r][0] * 0.125f, 0);
        if (r) { cv = l < 32 ? c[0] : c[1]; asm volatile("" : "+v"(cv)); }
        SB();
      }
    }
    const float ml = s[r][0] * 0.0625f, dl = __builtin_fmaf(s[r][0], 0.25f, -4.f * c[r]);
    s[r][1] = __builtin_fmaf(dl, dl, __builtin_fmaf(-s[r][0], ml, s[r][1]));
    SB();
  }
#pragma unroll
  for (int k = 0; k < 3; k++) swap32(s[0][k ? 3 - k : 0], s[1][k ? 3 - k : 0]);
  float q[3];
#pragma unroll
  for (int k = 0; k < 3; k++) q[k] = s[0][k] + s[1][k];
#define RED3(m) "v_add_f32_dpp %0, %0, %0 " m " bank_mask:0xf\n\tv_add_f32_dpp %1, %1, %1 " m " bank_mask:0xf\n\tv_add_f32_dpp %2, %2, %2 " m " bank_mask:0xf\n\t"
  asm volatile("s_nop 1\n\t" RED3("row_ror:1 row_mask:0xf") RED3("row_ror:2 row_mask:0xf") RED3("row_ror:4 row_mask:0xf") RED3("row_ror:8 row_mask:0xf")
               RED3("row_bcast:15 row_mask:0xa") : "+v"(q[0]), "+v"(q[1]), "+v"(q[2]));
  const float mu = q[0] * (1.f / D), dv = mu - cv;
  const float var = fmaxf(__builtin_fmaf(-dv, dv, q[1] * (1.f / D)), 0.f);
  const float m = rb(mu);
  float sd = __builtin_amdgcn_sqrtf(rb(rb(var) + 1e-5f)), num = __builtin_fmaf(-m, sw, q[2]);
  asm volatile("s_nop 0\n\tv_cvt_pk_bf16_f32 %0, 0, %0" : "+v"(sd), "+v"(num));
  u32 rv = __float_as_uint(rb(num * __builtin_amdgcn_rcpf(sd)));
  rv ^= (u32)((int)rv >> 15) | 0x80000000u;
  rv = __builtin_amdgcn_perm(rv, kl, 0x07060100u) >> 1;
  asm volatile("ds_write2_b32 %[aw], %[sv], %[rv] offset1:16\n\ts_mov_b64 exec, 1\n\tds_add_rtn_u32 v104, %[ac], %[one]\n\ts_mov_b64 exec, -1\n\t"
               "ds_read_b32 v106, %[ar]\n\tds_read_b32 v105, %[ar] offset:64\n\tds_read_b128 v[100:103], %[aq] offset:64\n\tv_mov_b32 v107, %[t]\n\tv_mov_b32 v109, %[t]\n\t"
               "s_waitcnt lgkmcnt(3)\n\tv_cmp_eq_u32 vcc, 7, v104\n\ts_bitcmp1_b32 vcc_lo, 0\n\ts_cbranch_scc0 1f\n\t"
               "s_waitcnt lgkmcnt(2)\n\tbuffer_store_dwordx2 v[106:107], %[os], %[rs], 0 offen sc1\n\ts_waitcnt lgkmcnt(0)\n\t"
               "v_sub_u32 v100, v105, v100\n\tv_sub_u32 v101, v105, v101\n\tv_sub_u32 v102, v105, v102\n\tv_sub_u32 v103, v105, v103\n\t"
               RANK8("v100", "v101", "v102", "v103", "v104", "v103", "0", "s_nop 1", "s_nop 1") "v_cmp_eq_u32 vcc, 16, v104\n\t"
               "v_sub_u32 v108, 19, v104\n\ts_ff1_i32_b32 s6, vcc_lo\n\tv_readlane_b32 s6, v105, s6\n\t"
               "v_mad_u32_u24 v104, v104, %[om], %[ot]\n\ts_nop 0\n\tv_lshrrev_b32 v108, v108, s6\n\tv_bfi_b32 v108, %[fm], v108, v105\n\t"
               "buffer_store_dwordx2 v[108:109], v104, %[rs], 0 offen sc1\n1:\n\ts_waitcnt lgkmcnt(0)"
               :: [aw] "v"(aw), [sv] "v"(__builtin_amdgcn_perm(__float_as_uint(sd), __float_as_uint(m), 0x07060302u)), [rv] "v"(rv), [ac] "v"(ac), [one] "v"(one),
                  [ar] "v"(ar), [aq] "v"(aq), [os] "v"(os), [om] "v"(om), [ot] "v"(ot), [fm] "v"(fm), [sg] "s"(sg), [t] "s"(tag), [rs] "s"(rs)
               : "vcc", "scc", "s6", "v100", "v101", "v102", "v103", "v104", "v105", "v106", "v107", "v108", "v109", "memory");
  SB();
}

#define GL __attribute__((address_space(1)))
typedef const GL u16* gp;

__global__ __launch_bounds__(768, 3) void fused(const u16*, const u16*, const u16*, const u16*, const u16*, const u16*, const u16*, const u16*,
    const u16*, const u16*, const u16*, u16*, char*) {
  __shared__ __attribute__((aligned(256))) char smem[SMEM];
  const u32 lds0 = (u32)(size_t)(LDS char*)smem;
  int w0;
#define GLD(r0, r1, r2, r3, o_) "global_load_dwordx4 v[" #r0 ":" #r1 "], " #o_ ", s[16:17]\n\tglobal_load_dwordx4 v[" #r2 ":" #r3 "], " #o_ ", s[16:17] offset:2048\n\t"
  asm volatile(
      "s_load_dwordx16 s[16:31], s[0:1], 0\n\ts_load_dwordx8 s[32:39], s[0:1], 64\n\ts_load_dwordx2 s[40:41], s[0:1], 96\n\t"
      "s_memrealtime s[46:47]\n\t"
      "s_getpc_b64 s[4:5]\n2:\n\ts_lshr_b32 s7, s2, 3\n\ts_and_b32 s10, s4, 0xffffff00\n\ts_mov_b32 s11, s5\n\ts_mul_i32 s8, s7, 0x600\n\t"
      "v_lshl_add_u32 v3, v0, 1, s8\n\ts_cmpk_lt_u32 s2, %[pn]\n\ts_cbranch_scc0 1f\n\t"
      "global_load_ushort v163, v3, s[10:11] offset:%[pf]\n"
      "1:\n\ts_cmpk_lt_u32 s2, 0x80\n\tv_readfirstlane_b32 s3, v0\n\tv_and_b32 v1, 63, v0\n\tv_mov_b64 v[4:5], 0\n\tv_mov_b64 v[6:7], 0\n\tv_lshlrev_b32 v2, 4, v1\n\tv_lshlrev_b32 v8, 2, v0\n\t"
      "s_mov_b32 s42, %[we]\n\ts_mov_b32 s43, 0x20000\n\ts_cbranch_scc0 3f\n\t"
      "s_lshr_b32 s3, s3, 6\n\ts_mul_i32 s9, s3, %[xr]\n\ts_min_u32 s9, s9, %[zm]\n\tv_add_u32 v3, s9, v2\n\tv_add_u32 v3, %[zo], v3\n\t"
      "s_and_b32 s7, s7, 7\n\ts_and_b32 s6, s2, 7\n\ts_and_b32 s8, s2, 64\n\ts_lshl3_add_u32 s8, s6, s8\n\ts_add_u32 s45, s8, s7\n\t"
      "s_add_u32 s4, s4, lane_tab@rel32@lo+(6f-2b-4)\n6:\n\ts_addc_u32 s5, s5, lane_tab@rel32@hi+(6b-2b+4)\n\t"
      "ds_write_b128 v3, v[4:7]\n\ts_cmp_lt_u32 s3, 8\n\ts_cbranch_scc1 5f\n\t"
      "s_lshl_b32 s8, s45, 4\n\ts_lshl_b32 s9, s3, 18\n\ts_add_u32 s9, s9, s8\n\ts_bitset0_b32 s9, 21\n\tv_lshl_add_u32 v148, v1, 12, s9\n\t"
      "s_lshl3_add_u32 s9, s3, s7\n\ts_lshl_b32 s9, s9, 17\n\ts_add_u32 s9, s9, s8\n\ts_bitset0_b32 s9, 23\n\tv_lshl_add_u32 v9, v1, 11, s9\n\t"
      "v_add_u32 v11, 0x100000, v148\n\tv_add_u32 v12, 0x200000, v148\n\t"
      "v_lshlrev_b32 v149, 4, v0\n\ts_lshl_b32 s9, s3, 10\n\ts_add_u32 m0, s9, %[ob]\n\tv_add_u32 v10, 0xffffe800, v149\n\tglobal_load_lds_dwordx4 v10, s[4:5]\n\t"
      "v_add_u32 v149, %[sg], v149\n\t"
      "s_waitcnt lgkmcnt(0)\n\ts_load_dword s44, s[40:41], 0\n\ts_load_dwordx4 s[48:51], s[28:29], s8\n\t"
      "s_barrier\n\t"
      "global_load_dword v160, v9, s[16:17]" PRENT "\n\t"
      GLD(100, 103, 104, 107, v148)
      GLD(108, 111, 112, 115, v11)
      "s_cmp_eq_u32 s6, 3\n\ts_cbranch_scc1 .Lrole_p\n\t"
      GLD(116, 119, 120, 123, v12)
      "s_branch .Lrole_p\n"
      "3:\n\ts_lshr_b32 s3, s3, 6\n\ts_cmp_lt_u32 s3, 8\n\ts_cbranch_scc1 4f\n\ts_barrier\n\ts_branch .Lrole_d\n4:\n\t"
      "v_add_u32 v3, %[fc], v2\n\ts_add_u32 s8, s3, s2\n\ts_and_b32 s45, s8, 7\n\ts_lshl3_add_u32 s8, s2, s45\n\t"
      "s_lshl_b32 s8, s8, 12\n\ts_bitset0_b32 s8, 22\n\tds_write_b128 v3, v[4:7]\n\tv_add_u32 v3, s8, v2\n\t"
      "s_waitcnt lgkmcnt(0)\n\tglobal_load_dwordx4 v[116:119], v2, s[30:31]\n\tglobal_load_dwordx4 v[120:123], v2, s[30:31] offset:1024\n\t"
      "s_load_dword s44, s[40:41], 0\n\ts_barrier\n\t"
      "global_load_dwordx4 v[100:103], v3, s[16:17]" LNNT "\n\tglobal_load_dwordx4 v[104:107], v3, s[16:17] offset:1024" LNNT "\n\t"
      "global_load_dwordx4 v[108:111], v3, s[16:17] offset:2048" LNNT "\n\tglobal_load_dwordx4 v[112:115], v3, s[16:17] offset:3072" LNNT "\n\t"
      "s_branch .Lrole_l\n"
      "5:\n\t"
      "global_load_dwordx4 v[16:19], v2, s[4:5]\n\tglobal_load_dwordx4 v[20:23], v2, s[4:5] offset:1024\n\t"
      "s_lshl3_add_u32 s8, s45, s3\n\ts_lshl_b32 s9, s8, 1\n\tv_mov_b32 v9, s9\n\ts_lshl_b32 s9, s45, 11\n\tv_add_u32 v3, s9, v8\n\t"
      "s_waitcnt lgkmcnt(0)\n\tglobal_load_short_d16_hi v24, v9, s[26:27]\n\t"
      "global_load_dword v96, v3, s[18:19]" PNT "\n\tglobal_load_dword v97, v3, s[20:21]" PNT "\n\t"
      "global_load_dword v68, v3, s[22:23]" PNT "\n\tglobal_load_dword v69, v3, s[24:25]" PNT "\n\ts_barrier\n\ts_branch .Lrole_c"
      : "={s3}"(w0)
      : "{s[0:1]}"(__builtin_amdgcn_kernarg_segment_ptr()), "{s2}"(blockIdx.x), "{v0}"(threadIdx.x), [pf] "n"(PFO), [pn] "n"(8 * PFN), [we] "n"(WS_END), [xr] "n"(XREG),
        [zm] "n"(OMISC - 512 - (OX - 16)), [zo] "n"(OX - 16), [sg] "n"(O_SG - 8192), [fc] "n"(FC), [ob] "n"(OB - 8192)
      : "memory");
#define TLREGS()
#define ROLE(name, text, ...) \
  gp x, dsk, wg, wu, wd; GL u16* out; rsrc_t rs; int b, w, t, l, g; u32 tag, st0; u32x4 dv; \
  asm volatile(".Lrole_" #name ":" : "={v0}"(t), "={v1}"(l) __VA_ARGS__ :: "memory"); \
  asm volatile(text : "={s2}"(b), "={s3}"(w), "={s[16:17]}"(x), "={s[28:29]}"(dsk), "={s[32:33]}"(wg), "={s[34:35]}"(wu), "={s[36:37]}"(wd), \
               "={s[38:39]}"(out), "={s[40:43]}"(rs), "={s44}"(tag), "={s45}"(g), "={s46}"(st0), "={s[48:51]}"(dv) :: "memory"); \
  SB(); \
  __builtin_assume((u32)l < 64u); __builtin_assume((u32)t < 768u); __builtin_assume((u32)w < 12u); __builtin_assume((u32)b < 256u); __builtin_assume((u32)g < 128u); \
  TLREGS(); \
  const int xk = b & 7, j = (b >> 3) & 15, n16 = l & 15, kg = l >> 4
#define TAGWAIT() do { asm volatile("s_waitcnt lgkmcnt(0)" : "+{s44}"(tag)::"memory"); tag += 1u; } while (0)
#define BAR() do { SB(); asm volatile("s_barrier" ::: "memory"); SB(); } while (0)
#define FLAG(o, v) do { asm volatile("" ::: "memory"); SB(); *(volatile LDS u32*)(LDS char*)(smem + (o)) = (v); SB(); } while (0)
#define ARRIVE(o) do { SB(); if (l == 0) asm volatile("ds_add_u32 %0, %1" ::"v"(lds0 + (o)), "v"(1u) : "memory"); SB(); } while (0)
#define CWAIT(o, n, nap) do { _Pragma("unroll 1") for (int spin = 0; spin < SPIN; spin++) { \
      if (__builtin_amdgcn_readfirstlane((int)*(const volatile LDS u32*)(LDS char*)(smem + (o))) >= (int)(n)) break; \
      asm volatile(nap ::: "memory"); } SB(); asm volatile("" ::: "memory"); } while (0)
#define AWAIT(o, n, nap) do { _Pragma("unroll 1") for (int spin = 0; spin < SPIN; spin++) { \
      if (__all(*(const volatile LDS u32*)(LDS char*)(smem + (o)) >= (u32)(n))) break; \
      asm volatile(nap ::: "memory"); } SB(); asm volatile("" ::: "memory"); } while (0)
#define POLL8(res_, off_) do { u32 n_ = SPIN; \
    asm volatile("1:\n\tbuffer_load_dwordx2 v[4:5], %2, %3, 0 offen sc1\n\ts_sub_u32 %1, %1, 1\n\ts_waitcnt vmcnt(0)\n\t" \
                 "v_cmp_ne_u32 vcc, %4, v5\n\ts_cbranch_scc1 2f\n\ts_cbranch_vccnz 1b\n2:" \
                 : "=&{v[4:5]}"(res_), "+s"(n_) : "v"(off_), "s"(rs), "s"(tag) : "vcc", "scc", "memory"); } while (0)
#define UNTIL(t_) "2:\n\ts_sleep 1\n\ts_memrealtime s[8:9]\n\ts_waitcnt lgkmcnt(0)\n\ts_sub_u32 s8, s8, %[st]\n\ts_cmp_lt_u32 s8, " t_ "\n\ts_cbranch_scc1 2b\n\t"
#define LANE(v_) int v_; asm volatile("v_mbcnt_lo_u32_b32 %0, -1, 0\n\tv_mbcnt_hi_u32_b32 %0, -1, %0" : "=v"(v_))
#define IXO(l_) (O_IDX + (2 * xk + (j & 1)) * 128 + ((l_) & 7) * 8)
#define IDS() const int id = 16 * xk + j, sb = id * 57 >> 10, mat = id & 1, u = (id - 18 * sb) >> 1; \
    const bool slow = u >= 6; \
    const int part = slow ? u - 6 : u & 1, cb = 4 * sb + (slow ? 1 : (u >> 1) + (u > 1)), r0 = part * (slow ? 320 : 512), ns = slow ? 5 + (u >> 3) : 8; \
    const int kp = 4 * sb + ((u >> 1) & 3), ct = 2 * (u & 1) + mat
#define TS(a, b_)
#define TSC
#define TSD
#define TSGET(i0, i1, i2, i3)
  if (blockIdx.x >= 128) {
    if (w0 >= 8) {
      ROLE(d, "s_waitcnt lgkmcnt(0)");
      IDS();
      const int dw = w - 8;
      const u32 sw = (((n16 >> 1) ^ (kg & 1)) << 5) + ((l & 1) << 4);
      gp base = (mat ? wu : wg) + (size_t)(r0 + 4 * dw) * F + cb * 128;
      u32 v = kg * (2 * F) + sw, fa = lds0 + FC + 4 * dw, vp = 96 + dw, t_;
      asm volatile("" : "+v"(v), "+v"(fa), "+v"(vp));
      asm volatile("s_mov_b32 m0, %0" ::"s"(lds0 + 1024 * dw));
#define VMC "s_getreg_b32 s6, hwreg(HW_REG_IB_STS, 0, 4)\n\t"
#define RUN(k, q, lim, v_, b_, st_) asm volatile("1:\n\ts_lshr_b32 s6, m0, 4\n\ts_and_b32 s6, s6, 0xc0\n\tv_xor_b32 %[t], s6, %[v]\n\ts_waitcnt vmcnt(%[c])\n\t" VMC \
        "global_load_lds_dwordx4 %[t], %[b] nt\n\ts_add_u32 m0, m0, %[m]\n\tv_add_u32 %[v], %[d], %[v]\n\t.if %[m] == 3072\n\tv_mad_i32_i24 %[t], s6, -3, %[p]\n\tv_add_u32 %[p], 3, %[p]\n\t" \
        "s_mov_b64 exec, 1\n\tds_max_u32 %[a], %[t]\n\ts_mov_b64 exec, -1\n\t.endif\n\ts_cmp_lt_u32 m0, %[l]\n\ts_cbranch_scc1 1b" \
        : [v] "+v"(v_), [p] "+v"(vp), [t] "=&v"(t_) : [b] "s"(b_), [c] "n"((q) - 1), [m] "n"(1024 * (k)), [d] "n"((k) * (st_)), \
          [a] "v"(fa), [l] "s"(lim) : "scc", "s6", "memory")
#define MARK(c, val) "s_waitcnt vmcnt(" #c ")\n\t" val "\n\ts_mov_b64 exec, 1\n\tds_max_u32 %[a], %[t]\n\ts_mov_b64 exec, -1\n\t"
      __builtin_amdgcn_s_setprio(DPRIO);
      STAMP(8);
      if (id < 126) { if (slow) RUN(4, QAS, 32768, v, base, 8 * F); else RUN(4, 8, 32768, v, base, 8 * F); }
      if (dw < 3) {
        if (id >= 126) __builtin_amdgcn_endpgm();
        gp bd = wd + (size_t)(128 * kp + 2 * dw) * D + ct * 256;
        u32 vd = (l >> 5) * (2 * D + 32) ^ 16 * (l & 31);
        asm volatile("" : "+v"(vd));
        if (slow) RUN(3, QS, 16384 * ns, v, base, 8 * F);
        else { RUN(3, 8, 32768 + 3072 * RAMP, v, base, 8 * F); RUN(3, QD, 131072, v, base, 8 * F); }
        STAMP(1);
        {
          u32 n_ = SPIN;
          asm volatile("1:\n\t" VMC MARK(63, "v_mad_i32_i24 %[t], s6, -3, %[p]") "ds_read_b32 %[t], %[f]\n\ts_sub_u32 %[n], %[n], 1\n\ts_waitcnt lgkmcnt(0)\n\t"
                       "v_cmp_lt_u32 vcc, %[k], %[t]\n\ts_cbranch_scc1 2f\n\ts_cbranch_vccz 1b\n2:\n\ts_mov_b32 m0, %[m]"
                       : [t] "=&v"(t_), [n] "+s"(n_) : [a] "v"(fa), [p] "v"(vp), [f] "v"(u == 8 ? fa : lds0 + FC + 20), [k] "s"(u == 8 ? 159 : 16 * HOLD - 105), [m] "s"(lds0 + 1024 * dw)
                       : "vcc", "scc", "s6", "memory");
        }
        STAMP(3);
        if (u < 8) {
          RUN(3, QE, 65536, vd, bd, 4 * D);
          STAMP(6);
          asm volatile(MARK(0, "v_mov_b32 %[t], -1") : [t] "=&v"(t_) : [a] "v"(fa) : "memory");
          STAMP(4);
        }
        FLUSH();
        __builtin_amdgcn_endpgm();
      }
      asm volatile(MARK(0, "v_mov_b32 %[t], -1") : [t] "=&v"(t_) : [a] "v"(fa) : "memory");
      u32 km, rk;
      u32 hi, lo, k2, k3, gd, thr, sg = 0x0c0c0b09u;
      u32 ka = lds0 + FC + 384 + 8 * (l >> 3), kc = 2047 * 2048 + 2 * r0 + 16 * ((l & 7) ^ ((l >> 2) & 4)), kq = lds0 + FC + 512 + (l & 48), kr = lds0 + FC + 512 + 4 * n16;
      u32 c1 = 896, c2 = 1792, c3 = 2688, cm = (u32)-2048, cs = O_SG + 8 * 2047, ca = lds0 + FC + 512;
      rsrc_t rx = __builtin_amdgcn_make_buffer_rsrc((void*)(x - 1344), 0, 2 * L * D + 4096, 0x00020000);
      u32 co = O_CLR + 8 * (18 * sb + (0x84c0 >> 4 * (kp & 3) & 15) + min(l, (kp & 3) == 1 ? 5 : 3));
      asm volatile("" : "+v"(ka), "+v"(kc), "+v"(kq), "+v"(kr), "+v"(co));
      asm volatile("" : "+s"(c1), "+s"(c2), "+s"(c3), "+s"(cm), "+s"(cs), "+s"(ca), "+s"(sg), "+s"(rx));
      *(u32x4*)(smem + FA + 6144 + 16 * l) = u32x4{0u, 0u, 0u, 0u};
      *(u32x4*)(smem + FA + 7168 + 16 * l) = u32x4{0u, 0u, 0u, 0u};
      CWAIT(FC + 256, 1, "s_sleep 4");
      tag = (u32)__builtin_amdgcn_readfirstlane((int)*(const volatile LDS u32*)(LDS char*)(smem + FC + 24));
      {
        u32 n_ = SPIN, t0, t1, t2, t3, se;
#define TLD(a, b_, c, d) "buffer_load_dwordx4 v[" #a ":" #b_ "], %[o], %[r], 0 offen sc1\n\tbuffer_load_dwordx4 v[" #c ":" #d "], %[o], %[r], %[ts] offen sc1\n\t"
#define TCK(r0, r1, r2, r3, r4, r5, r6, r7) "s_waitcnt vmcnt(4)\n\tv_xor_b32 %[t0], %[t], v" #r1 "\n\tv_bitop3_b32 %[t0], %[t0], %[t], v" #r3 " bitop3:0xf6\n\t" \
          "v_bitop3_b32 %[t0], %[t0], %[t], v" #r5 " bitop3:0xf6\n\tv_bitop3_b32 %[t0], %[t0], %[t], v" #r7 " bitop3:0xf6\n\t" \
          "v_cmp_ne_u32 vcc, 0, %[t0]\n\tv_mov_b32 %[hi], v" #r0 "\n\tv_mov_b32 %[lo], v" #r2 "\n\tv_mov_b32 %[k2], v" #r4 "\n\tv_mov_b32 %[k3], v" #r6 "\n\t" \
          "s_cbranch_vccz 5f\n\t" TLD(r0, r3, r4, r7)
        asm volatile("2:\n\ts_sleep 1\n\ts_memrealtime s[8:9]\n\ts_waitcnt lgkmcnt(0)\n\ts_sub_u32 s8, s8, %[st]\n\ts_cmp_lt_u32 s8, %[tw]\n\ts_cbranch_scc1 2b\n\t"
                     TLD(128, 131, 132, 135) "s_sleep " STR(RSL) "\n\t" TLD(136, 139, 140, 143) "s_sleep " STR(RSL) "\n\t" TLD(144, 147, 148, 151)
                     "1:\n\t" TCK(128, 129, 130, 131, 132, 133, 134, 135) TCK(136, 137, 138, 139, 140, 141, 142, 143) TCK(144, 145, 146, 147, 148, 149, 150, 151)
                     "s_sub_u32 %[n], %[n], 1\n\ts_cbranch_scc0 1b\n5:\n\t" TS(60, 61)
                     "v_and_or_b32 %[t0], %[hi], %[mk], %[pa]\n\tv_and_or_b32 %[t1], %[lo], %[mk], %[pb]\n\t"
                     "v_max_u32 %[t2], %[t0], %[t1]\n\tv_max_u32 %[t3], %[t0], %[t1]\n\tv_perm_b32 %[gd], %[hi], %[lo], %[p0]\n\tv_perm_b32 %[se], %[k2], %[k3], %[p0]\n\t"
                     "v_permlane16_swap_b32 %[t2], %[t3]\n\tv_max_u32 %[km], %[t2], %[t3]\n\tv_max_u32 %[rk], %[t2], %[t3]\n\t"
                     "v_max_u32 %[hi], %[t0], %[t1]\n\tv_min_u32 %[lo], %[t0], %[t1]\n\t"
                     "v_permlane32_swap_b32 %[km], %[rk]\n\tv_max_u32 %[km], %[km], %[rk]\n\tv_not_b32 %[t2], %[km]\n\t"
                     "v_and_or_b32 %[k2], %[k2], %[mk], %[pa]\n\t"
                     "v_not_b32_dpp %[t2], %[km] row_ror:4 row_mask:0x2 bank_mask:0xf\n\tv_not_b32_dpp %[t2], %[km] row_ror:8 row_mask:0x4 bank_mask:0xf\n\t"
                     "v_not_b32_dpp %[t2], %[km] row_ror:12 row_mask:0x8 bank_mask:0xf\n\t"
                     "v_pk_lshlrev_b16 %[gd], 4, %[gd] op_sel_hi:[0,1]\n\tv_add_u32 %[t0], %[km], %[t2]\n\t"
                     "v_add_u32_dpp %[t1], %[t2], %[km] row_ror:1 row_mask:0xf bank_mask:0xf\n\tv_add_u32_dpp %[t3], %[t2], %[km] row_ror:2 row_mask:0xf bank_mask:0xf\n\t"
                     "v_add_u32_dpp %[t2], %[t2], %[km] row_ror:3 row_mask:0xf bank_mask:0xf\n\t"
                     RANK8("%[t0]", "%[t1]", "%[t2]", "%[t3]", "%[rk]", "%[t3]", "-2", "v_lshrrev_b32 %[se], 4, %[se]\n\tv_perm_b32 %[gd], %[gd], %[se], %[p1]",
                           "v_pk_max_u16 %[gd], %[gd], %[gd] op_sel:[0,1] op_sel_hi:[1,0]\n\tv_and_or_b32 %[k3], %[k3], %[mk], %[pb]")
                     "v_cmp_eq_u32 vcc, 56, %[rk]\n\tv_max3_u32 %[se], %[lo], %[k2], %[k3]\n\tv_lshrrev_b32 %[gd], 1, %[gd]\n\ts_ff1_i32_b32 s6, vcc_lo\n\t"
                     "v_readlane_b32 s6, %[km], s6\n\tv_or_b32 %[gd], 0x7fff, %[gd]\n\tv_med3_u32 %[t2], %[lo], %[k2], %[k3]\n\t"
                     "v_cmp_le_u32 s[8:9], s6, %[hi]\n\tv_cmp_le_u32 s[10:11], s6, %[se]\n\tv_max_u32 %[t2], %[t2], %[gd]\n\t"
                     "v_mbcnt_lo_u32_b32 %[t0], s8, 0\n\tv_mbcnt_lo_u32_b32 %[t1], s10, -15\n\tv_mbcnt_hi_u32_b32 %[t0], s9, %[t0]\n\tv_mbcnt_hi_u32_b32 %[t1], s11, %[t1]\n\t"
                     "v_lshl_add_u32 %[t0], %[t0], 2, %[ca]\n\tv_mad_i32_i24 %[t1], %[t1], -4, %[ca]\n\tv_cmp_le_u32 vcc, s6, %[t2]\n\t"
                     "s_mov_b64 exec, s[8:9]\n\tds_write_b32 %[t0], %[hi]\n\ts_mov_b64 exec, s[10:11]\n\tds_write_b32 %[t1], %[se]\n\ts_mov_b64 exec, -1\n\t"
                     "ds_read_b32 %[se], %[kr]\n\tds_read_b128 v[152:155], %[kq]\n\t"
                     "s_bcnt1_i32_b64 s8, s[8:9]\n\ts_bcnt1_i32_b64 s10, s[10:11]\n\ts_add_u32 s8, s8, s10\n\ts_cmp_gt_u32 s8, 16\n\ts_cselect_b64 s[8:9], -1, vcc\n\t"
                     "s_cmp_lg_u64 s[8:9], 0\n\ts_waitcnt lgkmcnt(0)\n\ts_cbranch_scc1 6f\n\t"
                     "v_sub_u32 %[t0], %[se], v152\n\tv_sub_u32 %[t1], %[se], v153\n\tv_sub_u32 %[t2], %[se], v154\n\tv_sub_u32 %[t3], %[se], v155\n\t"
                     RANK8("%[t0]", "%[t1]", "%[t2]", "%[t3]", "%[rk]", "%[t3]", "0", "v_mov_b32 %[km], %[se]\n\ts_nop 0", "s_nop 1") "6:\n\t" TS(62, 63)
                     : [hi] "=&v"(hi), [lo] "=&v"(lo), [k2] "=&v"(k2), [k3] "=&v"(k3), [gd] "=&v"(gd), [se] "=&v"(se), [km] "=&v"(km), [rk] "=&v"(rk),
                       [t0] "=&v"(t0), [t1] "=&v"(t1), [t2] "=&v"(t2), [t3] "=&v"(t3), [n] "+s"(n_)
                     : [o] "v"(O_TOP + 16 * (l & 7) + TL * (l >> 3)), [r] "s"(rs), [t] "s"(tag), [ts] "s"(TOPS), [st] "s"(st0), [tw] "n"(TW), [mk] "s"(0x7fff800fu),
                       [pa] "v"((127 - 2 * l) << 4), [pb] "v"((126 - 2 * l) << 4), [sg] "s"(sg), [p0] "s"(0x05040100u), [p1] "s"(0x07020500u), [ca] "s"(ca),
                       [kr] "v"(kr), [kq] "v"(kq)
                     : "vcc", "scc", "s6", "s8", "s9", "s10", "s11", "memory" TSC);
      }
      unsigned long long more;
      asm volatile("" : "={s6}"(thr), "={s[8:9]}"(more));
      if (__builtin_expect(more != 0, 0)) {
        const u32 m0 = km;
        unsigned long long all = __ballot(gd >= thr);
        if (gd >= thr) hi = lo = k2 = k3 = 0u;
#pragma unroll 1
        for (int i = 0; i < 4;) {
          u32 k, m = m0;
          if (all) {
            const int a = __builtin_ctzll(all);
            all &= all - 1;
            u32x2 r;
            const u32 o = O_RG + 256 * a + 8 * (l & 31);
            POLL8(r, o);
            k = l < 32 ? r.x : 0u;
            m = (u32)__builtin_amdgcn_readlane((int)m0, a);
          } else { k = hi; hi = lo; lo = k2; k2 = k3; i++; }
          asm volatile("v_cmp_le_u32 s[8:9], %[t], %[k]\n\tv_cmp_ne_u32 vcc, %[k], %[m0]\n\ts_and_b64 s[8:9], s[8:9], vcc\n\ts_cbranch_scc0 2f\n"
                       "1:\n\ts_ff1_i32_b64 s7, s[8:9]\n\tv_readlane_b32 s12, %[k], s7\n\ts_lshl_b64 s[10:11], 1, s7\n\ts_nop 0\n\t"
                       "v_cmpx_gt_u32 vcc, s12, %[km]\n\tv_add_u32 %[rk], 8, %[rk]\n\ts_bcnt0_i32_b32 s7, vcc_lo\n\tv_cmpx_eq_u32 vcc, 0x80, %[rk]\n\t"
                       "v_mov_b32 %[km], s12\n\tv_lshlrev_b32 %[rk], 2, s7\n\ts_mov_b64 exec, -1\n\ts_andn2_b64 s[8:9], s[8:9], s[10:11]\n\ts_cbranch_scc1 1b\n2:"
                       : [km] "+v"(km), [rk] "+v"(rk) : [k] "v"(k), [m0] "v"(m), [t] "s"(thr) : "vcc", "scc", "s7", "s8", "s9", "s10", "s11", "s12");
        }
      }
      if (id < 126) {
        u32 a_, v_, n_ = SPIN;
        u32x2 cl;
#define RLD(o, s_) "buffer_load_dwordx4 %[v], %[rx], " s_ " offen offset:" #o " lds\n\t"
        asm volatile("v_add_u32 %[a], %[kb], %[rk]\n\tv_and_b32 %[v], 0x7ff, %[km]\n\ts_mov_b32 m0, %[fa]\n\tds_write_b32 %[a], %[v]\n\tds_read_b32 %[a], %[ka]\n\t"
                     "s_cmp_eq_u32 %[ns], 8\n\ts_waitcnt lgkmcnt(0)\n\tv_mad_i32_i24 %[v], %[a], %[cm], %[kc]\n\t"
                     RLD(0, "%[c3]") RLD(1024, "%[c2]") RLD(2048, "%[c1]") RLD(3072, "0")
                     "s_mov_b32 m0, %[fb]\n\tv_add_u32 %[v], 0x200, %[v]\n\t"
                     RLD(0, "%[c3]") RLD(1024, "%[c2]") "s_cbranch_scc0 3f\n\t" RLD(2048, "%[c1]") RLD(3072, "0")
                     "3:\n\tv_mad_i32_i24 %[a], %[a], -8, %[cs]\n\tbuffer_load_dwordx2 v[160:161], %[a], %[rs], 0 offen sc1\n\tbuffer_load_dwordx2 %[cl], %[co], %[rs], 0 offen sc1\n\t"
                     "v_mov_b32 %[v], 16 * " STR(HOLD) " - 112\n\tv_subrev_u32 %[ka], %[kd], %[ka]\n\ts_mov_b64 exec, 1\n\tds_add_u32 %[fc], %[v] offset:8\n\ts_mov_b64 exec, -1\n\tv_mov_b32 %[v], -1\n\t" TS(64, 65)
                     "s_waitcnt vmcnt(1)\n1:\n\tv_cmp_ne_u32 vcc, %[t], v161\n\tv_and_b32 v156, 0xffff0000, v160\n\tv_lshlrev_b32 v157, 16, v160\n\tv_rcp_f32 v156, v156\n\t"
                     "s_sub_u32 %[n], %[n], 1\n\ts_cbranch_vccz 2f\n\ts_cbranch_scc1 2f\n\t"
                     "buffer_load_dwordx2 v[160:161], %[a], %[rs], 0 offen sc1\n\ts_waitcnt vmcnt(0)\n\ts_branch 1b\n2:\n\tv_mul_f32 v157, -v157, v156\n\t"
                     "ds_write_b64 %[ka], v[156:157]\n\tds_write_b32 %[fc], %[v] offset:4\n\t" TS(66, 67) "s_waitcnt vmcnt(0)"
                     : [a] "=&v"(a_), [v] "=&v"(v_), [ka] "+v"(ka), [n] "+s"(n_), [cl] "=&v"(cl)
                     : [co] "v"(co), [rk] "v"(rk), [km] "v"(km), [kc] "v"(kc), [kb] "n"(FC + 384), [kd] "n"(384 - 32), [fa] "n"(FA), [fb] "n"(FA + 4096), [rx] "s"(rx), [rs] "s"(rs),
                       [c1] "s"(c1), [c2] "s"(c2), [c3] "s"(c3), [cm] "s"(cm), [cs] "s"(cs), [ns] "s"(ns), [fc] "v"(fa), [t] "s"(tag) : "vcc", "scc", "memory" TSC);
        TSGET(12, 13, 14, 15);
#pragma unroll 1
        for (int spin = 0; spin < SPIN; spin++) {
          if (__all(cl.y == tag)) break;
          asm volatile("s_sleep " STR(CNAP) ::: "memory");
          cl = ld8(rs, co);
        }
        FLAG(FC + 28, 1u);
        STAMP(10);
      } else {
        const u32 cd = km << 1;
        const u32x2 r = {(2047u - (km & 0x7ffu)) | ((cd ^ ((u32)((int)~cd >> 15) | 0x80000000u)) & 0xffff0000u), tag};
        const u32 o = rk < 64u ? O_IDX + 1024 * mat + 128 * kg + rk : 0x80000000u;
        st8(rs, o, r);
        st8(rs, o + 512, r);
        TSGET(12, 13, 14, 15);
        STAMP(14);
        STAMP(15);
        if (mat) {
#pragma unroll 1
          for (int spin = 0; spin < SPIN; spin++) {
            const u32x4 a = ld16(rs, O_ARR + 16 * l);
            if (__all(a.y == tag && a.w == tag)) break;
            asm volatile("s_sleep 16" ::: "memory");
          }
          if (l == 0) __builtin_amdgcn_raw_buffer_store_b32(tag, rs, 0, 0, 16);
        }
      }
      FLUSH();
      __builtin_amdgcn_endpgm();
    }
    u32x4 xv[4];
    u32x8 wv;
    ROLE(l, "", , "={v[100:103]}"(xv[0]), "={v[104:107]}"(xv[1]), "={v[108:111]}"(xv[2]), "={v[112:115]}"(xv[3]), "={v[116:123]}"(wv));
    STAMP(11);
    u32 sg = 0x0c0c0b09u;
    asm volatile("" : "+s"(sg));
    ln_rows(xv, wv, rs, tag, b, g, l, lds0 + FC + 128, sg TLVAL);
    STAMP(10);
    IDS();
    if (id >= 126) {
      FLUSH();
      __builtin_amdgcn_endpgm();
    }
    const int wr = 16 * (kg >> 1) + 4 * (kg & 1) + (n16 & 8) + ((n16 >> 2) & 1);
    u32 wa = lds0 + 256 * wr + ((w ^ ((wr & 1) | ((wr >> 1) & 6))) << 5) + 8 * (l & 3), sel = (l & 8) ? 0xeeeeu : 0x4444u;
    u32 so = 4 * t;
    asm volatile("" : "+v"(wa), "+v"(sel), "+v"(so));
    asm volatile(UNTIL(STR(CLD)) :: [st] "s"(st0) : "s8", "s9", "scc");
    __builtin_amdgcn_raw_buffer_store_b32(~0u, rs, so, O_HG + (6 * cb + 3 * mat + part) * 2048, 16);
    st8(rs, u < 8 ? O_PG + (4 * kp + ct) * 4096 + 2 * so : 0x80000000u, u32x2{~0u, ~0u});
#define SWP(a0, a1, a2, a3) "v_permlane32_swap_b32 " a0 ", " a2 "\n\tv_permlane32_swap_b32 " a1 ", " a3 "\n\t"
#define OUT(a0, a1, o) "v_cvt_pk_bf16_f32 " a0 ", " a0 ", " a1 "\n\tv_min_u32 " a0 ", -2, " a0 "\n\tbuffer_store_dword " a0 ", %[so], %[rs], " o " sc1\n\t"
#define TR4(a, b_, c, d, e, f, g_, h, adr, o) "ds_read_b64_tr_b16 v[" #a ":" #b_ "], " adr " offset:" #o "\n\tds_read_b64_tr_b16 v[" #c ":" #d "], " adr " offset:" #o "+512\n\t" \
      "ds_read_b64_tr_b16 v[" #e ":" #f "], " adr " offset:" #o "+8192\n\tds_read_b64_tr_b16 v[" #g_ ":" #h "], " adr " offset:" #o "+8704\n\t"
#define SM(d, a, b_) "v_smfmac_f32_16x16x64_bf16 v[" d "], v[" a "], v[" b_ "], %[sel]\n\t"
#define CALL "s_call_b64 s[10:11], 1f\n\t"
#define USE(c, xa, wf) "s_add_u32 s6, s6, 16\n\ts_waitcnt lgkmcnt(" #c ")\n\t" SM("16:19", "20:23", wf) SM("8:11", xa, wf)
#define XR(xa, o) "ds_read_b128 v[" xa "], %[xa] offset:" #o "\n\t"
#define XU(c, xa, wf) "s_waitcnt lgkmcnt(" #c ")\n\t" SM("8:11", xa, wf)
#define ZERO(r) "v_mfma_f32_32x32x16_bf16 v[" r "], v[8:11], v[8:11], 0\n\t"
    asm volatile("v_mov_b64 v[8:9], 0\n\tv_mov_b64 v[10:11], 0\n\tv_mov_b64 v[16:17], 0\n\tv_mov_b64 v[18:19], 0\n\t"
                 ZERO("32:47") ZERO("48:63") ZERO("64:79") ZERO("80:95") ZERO("96:111") ZERO("112:127")
                 "v_mov_b32 v20, 0x3f803f80\n\tv_mov_b32 v21, v20\n\tv_mov_b64 v[22:23], v[20:21]\n\tv_mov_b32 v13, %[t]\n\tv_mov_b32 v28, 1\n\t"
                 "v_add_u32 v29, 0x10000, %[wa]\n\ts_movk_i32 s6, 96\n\ts_mov_b32 s7, 1\n\ts_mov_b32 s8, 15\n\ts_mov_b32 s9, %[spin]\n\ts_nop 7\n\t"
                 CALL TR4(32, 33, 34, 35, 36, 37, 38, 39, "%[wa]", 0) TR4(40, 41, 42, 43, 44, 45, 46, 47, "%[wa]", 16384)
                 USE(4, "96:99", "32:39") "s_waitcnt lgkmcnt(0)\n\t" SM("16:19", "20:23", "40:47") SM("8:11", "100:103", "40:47")
                 CALL TR4(48, 49, 50, 51, 52, 53, 54, 55, "%[wa]", 32768) USE(0, "104:107", "48:55")
                 CALL TR4(56, 57, 58, 59, 60, 61, 62, 63, "%[wa]", 49152) "s_waitcnt vmcnt(0)\n\ts_mov_b64 exec, 1\n\tds_add_rtn_u32 v28, %[fl], v28 offset:20\n\ts_mov_b64 exec, -1\n\t" TS(60, 61)
                 USE(0, "108:111", "56:63") "v_and_b32 v28, 15, v28\n\tv_cmpx_eq_u32 vcc, 7, v28\n\tbuffer_store_dwordx2 v[12:13], off, %[rs], %[co] sc1\n\ts_mov_b64 exec, -1\n\t"
                 CALL TR4(64, 65, 66, 67, 68, 69, 70, 71, "v29", 0) USE(0, "112:115", "64:71") "s_cmp_eq_u32 %[ns], 5\n\ts_cbranch_scc1 5f\n\t"
                 CALL TR4(72, 73, 74, 75, 76, 77, 78, 79, "v29", 16384) USE(0, "116:119", "72:79") "s_cmp_eq_u32 %[ns], 6\n\ts_cbranch_scc1 5f\n\t"
                 CALL TR4(80, 81, 82, 83, 84, 85, 86, 87, "v29", 32768) USE(0, "120:123", "80:87")
                 CALL TR4(88, 89, 90, 91, 92, 93, 94, 95, "v29", 49152) USE(0, "124:127", "88:95")
                 "5:\n\t" TS(62, 63) "s_cmp_eq_u32 s7, 0\n\ts_cbranch_scc1 4f\n\ts_mov_b32 s8, 16\n"
                 "1:\n\tds_read_b32 v30, %[fl]\n\ts_waitcnt lgkmcnt(0)\n\tv_cmp_le_u32 vcc, s6, v30\n\ts_and_b32 s12, vcc_hi, s7\n\ts_cbranch_scc1 3f\n"
                 "2:\n\ts_andn2_b32 s12, s8, vcc_lo\n\ts_cbranch_scc1 6f\n\ts_setpc_b64 s[10:11]\n"
                 "6:\n\ts_sub_u32 s9, s9, 1\n\ts_cbranch_scc1 4f\n\t" GNAP "\n\ts_branch 1b\n"
                 "3:\n\t" XR("96:99", 0) XR("100:103", 1024) XR("104:107", 2048) XR("108:111", 3072) XR("112:115", 4096) XR("116:119", 5120) XR("120:123", 6144) XR("124:127", 7168)
                 "ds_read_b128 v[24:27], %[pa]\n\t" TS(64, 65) "s_mov_b32 s7, 0\n\t"
                 XU(8, "96:99", "32:39") XU(7, "100:103", "40:47") XU(6, "104:107", "48:55") XU(5, "108:111", "56:63")
                 XU(4, "112:115", "64:71") XU(3, "116:119", "72:79") XU(2, "120:123", "80:87") XU(0, "124:127", "88:95")
                 "s_cmp_eq_u32 s8, 16\n\ts_cbranch_scc0 2b\n"
                 "4:\n\ts_nop 4\n\tv_permlane32_swap_b32 v16, v17\n\tv_add_f32 v16, v16, v17\n\tv_mul_f32 v25, v25, v16\n\tv_mul_f32 v27, v27, v16\n\t"
                 SWP("v8", "v9", "v10", "v11") "v_fmac_f32 v25, v10, v24\n\tv_fmac_f32 v27, v11, v26\n\tv_fma_f32 v8, v8, v24, v25\n\tv_fma_f32 v9, v9, v26, v27\n\t"
                 OUT("v8", "v9", "%[ho] offen") TS(66, 67)
                 :: [wa] "v"(wa), [sel] "v"(sel), [so] "v"(so), [fl] "v"(lds0 + FC + 4 * min(l, 4)), [xa] "v"(lds0 + FA + 128 * (l & 7) + 16 * ((2 * kg + (n16 >> 3)) ^ (2 * l & 4))),
                    [pa] "v"(lds0 + FC + 32 + 32 * (kg & 1) + 16 * (kg >> 1)), [ns] "s"(ns), [t] "s"(tag), [co] "s"(O_CLR + 8 * id), [rs] "s"(rs), [ho] "s"(O_HG + (6 * cb + 3 * mat + part) * 2048), [spin] "n"(SPIN)
                 : "vcc", "scc", "s6", "s7", "s8", "s9", "s10", "s11", "s12", "memory", G_CLOBBER TSC);
    TSGET(1, 4, 3, 5);
    if (u < 8) {
      const int tp = 2 * (kg & 1) + (kg >> 1), r7 = 4 * (kg & 1) + (n16 >> 2);
      u32 o3 = (kp & 3) == 1 ? so + 4096 : 0x80000000u, h0 = O_HG + kp * 12288, h1 = h0 + 6144;
      u32 hw = lds0 + FH + 2 * tp * HP + 32 * w + 8 * (l & 2) + (l & 4) * 2 + ((l & 8) >> 1) + 2 * (l & 1);
      u32 hr = lds0 + FH + (l & 7) * HP + 32 * kg + 2 * (l & 8);
      u32 da = lds0 + 512 * (16 * (kg >> 1) + r7) + ((2 * w ^ r7) << 5) + 8 * (l & 3), db = da ^ 32, fl = lds0 + FC + 4 * (l & 3);
      u32 po = 512 * w + 16 * (l & 8) + 32 * tp + 4 * (l & 7);
      u32 e0 = 0x3f80u, e1 = 0x3f800000u;
      asm volatile("" : "+v"(hw), "+v"(hr), "+v"(da), "+v"(db), "+v"(fl), "+v"(po), "+v"(o3));
      asm volatile("" : "+s"(e0), "+s"(e1), "+s"(h0), "+s"(h1));
      u32 ha[6], m_, pend;
      u32x8 d0, d1, d2, d3;
#define TRD(a, b_, c, d, e, f, g_, h, adr, o) "ds_read_b64_tr_b16 v[" #a ":" #b_ "], " adr " offset:" #o "\n\tds_read_b64_tr_b16 v[" #c ":" #d "], " adr " offset:" #o "+4096\n\t" \
      "ds_read_b64_tr_b16 v[" #e ":" #f "], " adr " offset:" #o "+16384\n\tds_read_b64_tr_b16 v[" #g_ ":" #h "], " adr " offset:" #o "+20480\n\t"
#define HLD(X) X("%[a0]", "%[so]", "%[h0] offen") X("%[a3]", "%[so]", "%[h1] offen") X("%[a1]", "%[so]", "%[h0] offen offset:2048") X("%[a4]", "%[so]", "%[h1] offen offset:2048") \
      X("%[a2]", "%[o3]", "%[h0] offen") X("%[a5]", "%[o3]", "%[h1] offen")
#define HL0(a, o, s_) "buffer_load_dword " a ", " o ", %[rs], " s_ " sc1\n\t"
#define HL1(a, o, s_) "v_cmpx_eq_u32 vcc, -1, " a "\n\t" HL0(a, o, s_) "s_mov_b64 exec, -1\n\t"
      asm volatile("s_mov_b32 s7, 1\n\ts_mov_b32 s12, %[spin]\n"
                   "2:\n\ts_sleep 1\n\tds_read_b32 %[m], %[fl] offset:16\n\ts_memrealtime s[8:9]\n\ts_waitcnt lgkmcnt(0)\n\tv_cmp_eq_u32 vcc, 0, %[m]\n\ts_sub_u32 s8, s8, %[st]\n\t"
                   "s_cmp_lt_u32 s8, %[th]\n\ts_cbranch_scc1 2b\n\ts_bitcmp1_b32 vcc_lo, 3\n\ts_cbranch_scc1 2b\n\t" TS(64, 65)
                   HLD(HL0)
                   "1:\n\ts_call_b64 s[10:11], .Ldown\n\ts_waitcnt vmcnt(0)\n\tv_max3_u32 %[m], %[a0], %[a1], %[a2]\n\tv_max3_u32 %[m], %[m], %[a3], %[a4]\n\tv_max_u32 %[m], %[m], %[a5]\n\t"
                   "v_cmp_eq_u32 vcc, -1, %[m]\n\ts_sub_u32 s12, s12, 1\n\ts_cbranch_vccz 3f\n\ts_cbranch_scc1 3f\n\ts_sleep " STR(HNAP) "\n\t" HLD(HL1) "s_branch 1b\n"
                   ".Ldown:\n\tds_read_b32 %[m], %[fl]\n\ts_cmp_eq_u32 s7, 0\n\ts_waitcnt lgkmcnt(0)\n\tv_cmp_ne_u32 vcc, -1, %[m]\n\ts_cbranch_scc1 4f\n\ts_cbranch_vccnz 4f\n\t"
                   TRD(32, 33, 34, 35, 36, 37, 38, 39, "%[da]", 0) TRD(40, 41, 42, 43, 44, 45, 46, 47, "%[db]", 0)
                   TRD(48, 49, 50, 51, 52, 53, 54, 55, "%[da]", 32768) TRD(56, 57, 58, 59, 60, 61, 62, 63, "%[db]", 32768) "s_mov_b32 s7, 0\n"
                   "4:\n\ts_setpc_b64 s[10:11]\n3:\n\t" TS(66, 67) "s_waitcnt lgkmcnt(0)"
                   : [a0] "=&v"(ha[0]), [a1] "=&v"(ha[1]), [a2] "=&v"(ha[2]), [a3] "=&v"(ha[3]), [a4] "=&v"(ha[4]), [a5] "=&v"(ha[5]), [m] "=&{v64}"(m_),
                     "=&{v[32:39]}"(d0), "=&{v[40:47]}"(d1), "=&{v[48:55]}"(d2), "=&{v[56:63]}"(d3)
                   : [so] "v"(so), [o3] "v"(o3), [fl] "{v65}"(fl), [da] "{v66}"(da), [db] "{v67}"(db), [rs] "s"(rs), [h0] "s"(h0), [h1] "s"(h1), [st] "s"(st0), [th] "n"(TH), [spin] "n"(SPIN)
                   : "vcc", "scc", "s7", "s8", "s9", "s10", "s11", "s12", "memory" TSC);
      asm volatile("" : "={s7}"(pend));
      u32 s2[2];
#pragma unroll
      for (int m = 0; m < 2; m++) {
        const u32 p0 = ha[3 * m], p1 = ha[3 * m + 1], p2 = ha[3 * m + 2];
        s2[m] = pack2(dot2b(p2, e0, dot2b(p1, e0, lo2f(p0))), dot2b(p2, e1, dot2b(p1, e1, hi2f(p0))));
      }
      const u32 sl = pack2(lo2f(s2[0]) * sigm(lo2f(s2[0])), hi2f(s2[0]) * sigm(hi2f(s2[0])));
      asm volatile("ds_write_b16 %[hw], %[h]\n\tds_write_b16_d16_hi %[hw], %[h] offset:%[hp]\n\tv_mov_b64 v[8:9], 0\n\tv_mov_b64 v[10:11], 0\n\tv_mov_b64 v[12:13], 0\n\tv_mov_b64 v[14:15], 0\n\t"
                   "s_cmp_eq_u32 s7, 0\n\ts_cbranch_scc1 2f\n\ts_mov_b32 s12, %[spin]\n1:\n\ts_call_b64 s[10:11], .Ldown\n\ts_mul_i32 s12, s12, s7\n\ts_sub_u32 s12, s12, 1\n\ts_cbranch_scc0 1b\n"
                   "2:\n\ts_waitcnt lgkmcnt(0)\n\ts_barrier\n\t" TS(60, 61)
                   XR("16:19", 0) XR("20:23", 128)
                   XU(1, "16:19", "32:39") SM("12:15", "16:19", "40:47") XU(0, "20:23", "48:55") SM("12:15", "20:23", "56:63") "s_nop 6\n\t"
                   SWP("v8", "v9", "v10", "v11") "v_add_f32 v8, v8, v10\n\tv_add_f32 v9, v9, v11\n\t" SWP("v12", "v13", "v14", "v15") OUT("v8", "v9", "%[po] offen")
                   "v_add_f32 v12, v12, v14\n\tv_add_f32 v13, v13, v15\n\t" OUT("v12", "v13", "%[po] offen offset:256") TS(62, 63)
                   :: [hw] "v"(hw), [h] "v"(pack2(lo2f(sl) * lo2f(s2[1]), hi2f(sl) * hi2f(s2[1]))), [hp] "n"(HP), [xa] "v"(hr), [sel] "v"(sel), [so] "v"(po),
                      [rs] "s"(rs), [po] "s"(O_PG + (4 * kp + ct) * 4096), [spin] "n"(SPIN), "{s7}"(pend), "{v65}"(fl), "{v66}"(da), "{v67}"(db),
                      "{v[32:39]}"(d0), "{v[40:47]}"(d1), "{v[48:55]}"(d2), "{v[56:63]}"(d3)
                   : "vcc", "scc", "s10", "s11", "s12", "v64", "memory", D_CLOBBER TSD);
      TSGET(8, 9, 6, 7);
    }
    FLUSH();
    __builtin_amdgcn_endpgm();
  }

  if (w0 >= 8) {
    u32x8 xa0, xa1, xa2, xa3, sa1;
    u32x4 junk;
    u32 op, so;
    ROLE(p, "", , "={v[100:107]}"(xa0), "={v[108:115]}"(xa1), "={v[116:123]}"(xa2),
         "={v148}"(op), "={v149}"(so), "={v[160:163]}"(junk));
    const int p = w - 8;
    float sc[8];
    __builtin_amdgcn_s_setprio(3);
    asm volatile("s_waitcnt lgkmcnt(0)" : "+{s44}"(tag), "+{s[48:51]}"(dv)::"memory");
    tag += 1u;
    STAMP(9);
    f32x2 xq[4][8];
    u32x8 pa;
    {
#define SCK(r1, r3, r5, r7) "v_xor_b32 %[d], %[t], v" #r1 "\n\tv_bitop3_b32 %[d], %[d], %[t], v" #r3 " bitop3:0xf6\n\t" \
      "v_bitop3_b32 %[d], %[d], %[t], v" #r5 " bitop3:0xf6\n\tv_bitop3_b32 %[d], %[d], %[t], v" #r7 " bitop3:0xf6\n\t"
#define SLD(r0, r3, r4, r7, k, k1) "buffer_load_dwordx4 v[" #r0 ":" #r3 "], %[so], %[rs], " k " offen sc1\n\tbuffer_load_dwordx4 v[" #r4 ":" #r7 "], %[so], %[rs], " k1 " offen sc1\n\t"
#define SLOOP(r0, r1, r3, r4, r5, r7, k, k1) "1:\n\ts_waitcnt vmcnt(0)\n4:\n\t" SCK(r1, r3, r5, r7) "s_sub_u32 %[n], %[n], 1\n\tv_cmp_ne_u32 vcc, 0, %[d]\n\ts_cbranch_scc1 2f\n\ts_cbranch_vccz 2f\n\t" \
      SLD(r0, r3, r4, r7, k, k1) "s_branch 1b\n2:\n\t"
      u32 d_, n_ = SPIN, k1 = 4096, k2 = 8192, k3 = 12288, s0[4];
      asm volatile("s_mov_b32 m0, %[m]\n\ts_cmp_eq_u32 %[k], 3\n\ts_waitcnt vmcnt(2)\n\ts_cbranch_scc0 1f\n\ts_waitcnt vmcnt(0)\n1:"
                   : "+{v[100:107]}"(xa0), "+{v[108:115]}"(xa1), "+{v[160:163]}"(junk) : [m] "s"(lds0 + OX + 2 * XO + 256 * p), [k] "s"(xk) : "scc", "memory");
      SB();
#define UNPACK(i, xs) _Pragma("unroll") for (int c = 0; c < 8; c++) { \
        xq[i][c] = (c & 1) ? f32x2{hi2f(xs[c >> 1]), hi2f(xs[4 + (c >> 1)])} : f32x2{lo2f(xs[c >> 1]), lo2f(xs[4 + (c >> 1)])}; asm volatile("" : "+v"(xq[i][c].x), "+v"(xq[i][c].y)); }
      UNPACK(0, xa0);
      UNPACK(1, xa1);
#pragma unroll
      for (int c = 0; c < 8; c++) asm volatile("v_add_f32 %0, 1.0, %1" : "=v"(sc[c]) : "s"((c & 1) ? dv[c >> 1] & 0xffff0000u : dv[c >> 1] << 16));
      u32 fa = lds0 + OXF + 4 * p, fv = 1;
      asm volatile("" : "+v"(fa), "+v"(fv));
      asm volatile("" : "+s"(k1), "+s"(k2), "+s"(k3), "+s"(n_));
      SB();
      STAMP(1);
#define SCP(r0, r1, r2, r3, r4, r5, r6, r7) "s_waitcnt vmcnt(2)\n\t" SCK(r1, r3, r5, r7) "v_cmp_ne_u32 vcc, 0, %[d]\n\tv_mov_b32 %[a0], v" #r0 "\n\tv_mov_b32 %[a1], v" #r2 "\n\t" \
      "v_mov_b32 %[a2], v" #r4 "\n\tv_mov_b32 %[a3], v" #r6 "\n\ts_cbranch_vccz 3f\n\t" SLD(r0, r3, r4, r7, "0", "%[k1]")
      asm volatile(UNTIL(STR(TSG)) SLD(132, 135, 136, 139, "0", "%[k1]") "s_sleep " STR(SSL) "\n\t" SLD(140, 143, 144, 147, "0", "%[k1]")
                   "1:\n\t" SCP(132, 133, 134, 135, 136, 137, 138, 139) SCP(140, 141, 142, 143, 144, 145, 146, 147) "s_sub_u32 %[n], %[n], 1\n\ts_cbranch_scc0 1b\n"
                   "3:\n\t" SLD(140, 143, 144, 147, "%[k2]", "%[k3]")
                   : [d] "=&v"(d_), [n] "+s"(n_), [a0] "=&v"(s0[0]), [a1] "=&v"(s0[1]), [a2] "=&v"(s0[2]), [a3] "=&v"(s0[3]), "=&{v[132:139]}"(pa), "=&{v[140:147]}"(sa1)
                   : [st] "s"(st0), [t] "s"(tag), [so] "v"(so), [rs] "s"(rs), [k1] "s"(k1), [k2] "s"(k2), [k3] "s"(k3) : "vcc", "scc", "s8", "s9", "memory");
#pragma unroll
      for (int i = 0; i < 5; i++) {
#define XLD(r0, r1, r2, r3, o_) "v_add_u32 %[d], " #o_ ", %[o]\n\tglobal_load_dwordx4 v[" #r0 ":" #r1 "], %[d], %[x]\n\tglobal_load_dwordx4 v[" #r2 ":" #r3 "], %[d], %[x] offset:2048\n"
        if (i == XI) asm volatile("s_cmp_eq_u32 %[k], 3\n\ts_cbranch_scc0 1f\n\t" XLD(116, 119, 120, 123, 0x200000) "1:\n\t" XLD(124, 127, 128, 131, 0x300000)
                                 : [d] "=&v"(d_), "+{v[116:123]}"(xa2), "=&{v[124:131]}"(xa3) : [k] "s"(xk), [o] "v"(op), [x] "s"(x) : "scc", "memory");
        if (i == 2) {
          asm volatile("s_waitcnt vmcnt(2)\n\ts_branch 4f\n" SLOOP(140, 141, 143, 144, 145, 147, "%[k2]", "%[k3]") : [d] "=&v"(d_), [n] "+s"(n_), "+{v[140:147]}"(sa1), "+{v[116:123]}"(xa2), "+{v[132:139]}"(pa)
                       : [t] "s"(tag), [so] "v"(so), [rs] "s"(rs), [k2] "s"(k2), [k3] "s"(k3) : "vcc", "scc", "memory");
          UNPACK(2, xa2);
        }
        if (i == 3) asm volatile("s_waitcnt vmcnt(%1)" : "+{v[124:131]}"(xa3) : "n"(DPI == 2 ? DPF : 0));
        if (i == DPI && DPF) {
          u32 lx = l;
          asm volatile("" : "+v"(lx));
          const u32 ti = min(16 * xk + 4 * p + (lx >> 4), 125u), tsb = __umul24(ti, 57) >> 10, tu = min(ti - __umul24(18, tsb), 15u);
          const u32 o = (128 * (4 * tsb + (tu >> 2)) + DPO / 2 + 4 * j + ((lx >> 2) & 3)) * (2 * D) + 512 * (tu & 3) + 128 * (lx & 3);
          if (DPI == 4) AWAIT(OXF + 4 * (l & 3), 3, "s_sleep 1");
          if (DPF == 1) asm volatile("global_load_dword v160, %1, %2" : "=&{v[160:163]}"(junk) : "v"(o), "s"(wd) : "memory");
          else asm volatile("global_load_dword v160, %1, %2\n\tglobal_load_dword v161, %1, %3" : "=&{v[160:163]}"(junk) : "v"(o), "s"(wd), "s"(wd + 64 * D) : "memory");
        }
        if (i == 4) break;
        if (i == 3) UNPACK(3, xa3);
        const u32 sa = i < 2 ? s0[2 * i] : sa1[4 * i - 8], sb = i < 2 ? s0[2 * i + 1] : sa1[4 * i - 6];
        if (i == 0) STAMP(3);
        if (i == 2) STAMP(10);
        const f32x2 r2 = {__builtin_amdgcn_rcpf(hi2f(sa)), __builtin_amdgcn_rcpf(hi2f(sb))};
        const f32x2 m2 = {-lo2f(sa) * r2.x, -lo2f(sb) * r2.y};
        f32x2 n2[8];
        u32 o[8];
        SB();
#pragma unroll
        for (int c = 0; c < 8; c++) n2[c] = f32x2{fmav(xq[i][c].x, r2.x, m2.x), fmav(xq[i][c].y, r2.y, m2.y)};
        SB();
#pragma unroll
        for (int c = 0; c < 8; c++) o[c] = packh(n2[c].x, n2[c].y);
        SB();
#pragma unroll
        for (int c = 0; c < 8; c++) asm volatile("ds_write_addtid_b32 %0 offset:%1" ::"v"(o[c]), "n"(c * XREG + 1024 * i) : "memory");
        asm volatile("ds_write_b32 %1, %0\n\tv_add_u32 %0, 1, %0" : "+v"(fv) : "v"(fa) : "memory");
        if (i == PDROP) __builtin_amdgcn_s_setprio(PPRIO);
        SB();
#pragma unroll
        for (int c = 0; c < 8; c++) xq[i][c] = f32x2{fmac(n2[c].x, sc[c], xq[i][c].x), fmac(n2[c].y, sc[c], xq[i][c].y)};
        SB();
      }
    }
    STAMP(4);
    const rsrc_t ro = __builtin_amdgcn_make_buffer_rsrc((void*)out, 0, 2 * L * D, 0x00020000);
    u32 ys[8], y[2][8] = {};
    u32x2 ix;
    u32x4 cl;
    u32x8 pb;
    float yc, rsel;
    const u32 ixo = IXO((l & 6) + (n16 >> 3));
    if (p == 0) asm volatile("buffer_load_dwordx2 v[4:5], %1, %2, 0 offen sc1" : "=&{v[4:5]}"(ix) : "v"(ixo), "s"(rs) : "memory");
    const int rc = 4 * (l & 1) + kg, u = 64 * (p & 1) + l;
    *(u32x4*)(smem + OIX + 16 * p) = dv;
    u32x4 pick;
    u32 po = O_PG + 128 * g + 16 * (l & 7) + 16384 * (l >> 3), p3 = l < 32 ? po + 3 * 131072 : 0x80000000u, yf = lds0 + OYP + 4 * min(n16, 12);
    u32 ya = lds0 + OY + CHR * rc, qa = lds0 + OX + 2 * XO + rc * XREG, oo = 16 * g + 2 * rc, clo = O_CLR + 16 * min(l, 62);
    u32 yp = lds0 + OY + 512 * (p >> 1) + 4 * (u ^ ((u >> 4) & 6)), yq = yp + 4 * CHR, bp = lds0 + OMISC + 16 * p + 4 * kg, bs = 30 - 2 * n16;
    float e = 1.f + lo2f(*(const u16*)(smem + OIX + 16 * p + 2 * rc));
#pragma unroll
    for (int q = 0; q < 4; q++) pick[q] = (n16 >> 2) == q ? 0x3f80u << 16 * (l & 1) : 0u;
    asm volatile("" : "+v"(e), "+v"(pick), "+v"(ya), "+v"(qa), "+v"(oo), "+v"(po), "+v"(p3), "+v"(yf), "+v"(clo), "+v"(yp), "+v"(yq), "+v"(bp), "+v"(bs));
#pragma unroll
    for (int c = 0; c < 8; c++) asm volatile("" : "+v"(y[0][c]), "+v"(y[1][c]));
    if (TE) asm volatile(UNTIL(STR(TE)) :: [st] "s"(st0) : "s8", "s9", "scc");
#pragma unroll
    for (int i = 0; i < 4; i++) {
      if (i == IXI && p == 0) {
        u32 n_ = SPIN;
        asm volatile("s_waitcnt vmcnt(%5)\n1:\n\tv_cmp_ne_u32 vcc, %4, v5\n\ts_sub_u32 %1, %1, 1\n\ts_cbranch_vccz 2f\n\ts_cbranch_scc1 2f\n\t"
                     "buffer_load_dwordx2 v[4:5], %2, %3, 0 offen sc1\n\ts_waitcnt vmcnt(0)\n\ts_branch 1b\n2:"
                     : "+{v[4:5]}"(ix), "+s"(n_) : "v"(ixo), "s"(rs), "s"(tag), "n"(2 * IXI + 1) : "vcc", "scc", "memory");
        const u32 prow = ix.x & 2047u, v = prow >> 1;
        if (l < 16) atomicOr((u32*)(smem + OMISC) + (prow >> 5), 1u << (ix.x & 31u));
        FLAG(OYP + 32, 8u);
        oo += prow * (2 * D);
        asm volatile("v_mov_b32 v162, 0\n\tglobal_load_short_d16_hi v162, %1, %2" : "+{v[160:163]}"(junk) : "v"(oo), "s"(x) : "memory");
        AWAIT(OXF + 4 * (l & 3), 4, "s_sleep 1");
        e *= hlo(*(const LDS u16*)(size_t)(qa + 2 * prow));
        yc = lo2f(*(const u32*)(smem + OSC + 4 * rc)); rsel = rbt(sigm(hi2f(ix.x)));
        ya += 4 * (v ^ ((v >> 4) & 6)) + 2 * (prow & 1);
        asm volatile("" : "+v"(e), "+v"(yc), "+v"(rsel), "+v"(ya));
        STAMP(11);
      }
      if (i == 2 && p == 0) cl = ld16(rs, clo);
      if (i == 3) {
        asm volatile("s_waitcnt vmcnt(2)" ::: "memory");
        FLAG(OYP + 36 + 4 * p, 8u);
        if (p == 0) {
#pragma unroll 1
          for (int spin = 0; spin < SPIN; spin++) {
            if (__all(cl.y == tag && cl.w == tag)) break;
            cl = ld16(rs, clo);
          }
          STAMP(12);
        }
        __builtin_amdgcn_s_setprio(LPRIO);
      }
      {
        u32 n_ = SPIN, f_;
        asm volatile("1:\n\tds_read_b32 %0, %2\n\ts_sub_u32 %1, %1, 1\n\ts_waitcnt lgkmcnt(0)\n\tv_cmp_gt_u32 vcc, %3, %0\n\ts_cbranch_scc1 2f\n\ts_and_b32 vcc_lo, vcc_lo, %4\n\t"
                     "s_cbranch_scc0 2f\n\t" YNAP "\n\ts_branch 1b\n2:" : "=&v"(f_), "+s"(n_) : "v"(yf), "s"(2 * i + 1 + (p >> 1)), "n"(i < 2 ? 255 : 511) : "vcc", "scc", "memory");
      }
      if (i == 0) STAMP(5); else if (i == 1) STAMP(6); else if (i == 2) STAMP(7); else STAMP(8);
      u32 bm = 0;
      if (i >= 2) asm volatile("ds_read_b32 %0, %1 offset:%2" : "=v"(bm) : "v"(bp), "n"(64 * i) : "memory");
#pragma unroll
      for (int r = 0; r < 2; r++)
#pragma unroll
        for (int c = 0; c < 8; c++) asm volatile("ds_read_u16_d16_hi %0, %1 offset:%2" : "+v"(y[r][c]) : "v"(c < 4 ? yp : yq), "n"((c & 3) * CHR + 1024 * i + 2 * r) : "memory");
      if (i == 3 && p == 0) {
#define PLD(X) X(132, 133, 134, 135, "%[po]", "0") X(136, 137, 138, 139, "%[po]", "%[k1]") X(140, 141, 142, 143, "%[po]", "%[k2]") X(144, 145, 146, 147, "%[p3]", "0")
#define PL0(a, b_, c, d, o, s_) "buffer_load_dwordx4 v[" #a ":" #d "], " o ", %[rs], " s_ " offen sc1\n\t"
        asm volatile(PLD(PL0) : "=&{v[132:139]}"(pa), "=&{v[140:147]}"(pb) : [po] "v"(po), [p3] "v"(p3), [rs] "s"(rs), [k1] "s"(131072), [k2] "s"(262144) : "memory");
      }
      if (i == 0) {
        st8(rs, t == 512 ? O_ARR + b * 8 : 0x80000000u, u32x2{0u, tag});
        const u32x4 a = *(const u32x4*)(smem + OSC), c = *(const u32x4*)(smem + OSC + 16);
#pragma unroll
        for (int k = 0; k < 4; k++) { ys[k] = (u32)__builtin_amdgcn_readfirstlane((int)a[k]) << 16; ys[4 + k] = (u32)__builtin_amdgcn_readfirstlane((int)c[k]) << 16; }
      }
      SB();
#pragma unroll
      for (int r = 0; r < 2; r++) {
        asm volatile("s_waitcnt lgkmcnt(%9)" : "+v"(y[r][0]), "+v"(y[r][1]), "+v"(y[r][2]), "+v"(y[r][3]), "+v"(y[r][4]), "+v"(y[r][5]), "+v"(y[r][6]), "+v"(y[r][7]), "+v"(bm) : "n"(8 - 8 * r));
        u32x4 v;
#pragma unroll
        for (int k = 0; k < 4; k++)
          v[k] = pack2(fmas(__uint_as_float(y[r][2 * k]), ys[2 * k], xq[i][2 * k][r]), fmas(__uint_as_float(y[r][2 * k + 1]), ys[2 * k + 1], xq[i][2 * k + 1][r]));
        if (i >= 2 && r == 0) bm <<= bs;
        __builtin_amdgcn_raw_buffer_store_b128(v, ro, i < 2 ? op : op | (bm & 0x40000000u << r), (512 * i + r) * (2 * D), OAUX);
        SB();
        asm volatile("s_nop 1");
        SB();
      }
    }
    asm volatile("" ::"{v[160:163]}"(junk));
    STAMP(13);
    if (p == 0) {
      u32 n_ = SPIN;
#define PL1(a, b_, c, d, o, s_) "v_max3_u32 v150, v" #a ", v" #b_ ", v" #c "\n\tv_max_u32 v150, v150, v" #d "\n\tv_cmpx_eq_u32 vcc, -1, v150\n\t" PL0(a, b_, c, d, o, s_) "s_mov_b64 exec, -1\n\t"
#define PMX(a, b_) "v_max3_u32 v150, v150, v" #a ", v" #b_ "\n\t"
#define PMM(d, a, c) "v_mfma_f32_16x16x32_bf16 v[" d "], %[pk], v[" a "], " c "\n\t"
      asm volatile("s_waitcnt vmcnt(2)\n1:\n\t" PMM("152:155", "132:135", "0") "v_max_u32 v150, v132, v133\n\t" PMX(134, 135) PMM("156:159", "136:139", "0") PMX(136, 137) PMX(138, 139)
                   PMM("152:155", "140:143", "v[152:155]") PMX(140, 141) PMX(142, 143) PMM("156:159", "144:147", "v[156:159]") PMX(144, 145) PMX(146, 147)
                   "v_cmp_eq_u32 vcc, -1, v150\n\ts_sub_u32 %[n], %[n], 1\n\ts_cbranch_vccz 2f\n\ts_cbranch_scc1 2f\n\t" PLD(PL1) "s_waitcnt vmcnt(0)\n\ts_branch 1b\n"
                   "2:\n\t" TS(60, 61) "ds_read_b32 v150, %[yf]\n\tds_read_u16_d16_hi v151, %[ya]\n\ts_mov_b32 %[n], %[spin]\n\tv_add_f32 %[e], %[e], v162\n\ts_nop 0\n\tv_add_f32 v152, v152, v156\n\tv_add_f32 v153, v153, v157\n\ts_nop 0\n\t"
                   "v_add_f32_dpp v152, v152, v152 row_ror:8 row_mask:0xf bank_mask:0x3\n\tv_add_f32_dpp v152, v153, v153 row_ror:8 row_mask:0xf bank_mask:0xc\n\t"
                   "v_cvt_pk_bf16_f32 v152, 0, v152\n\tv_mul_f32 v152, v152, %[rl]\n\tv_cvt_pk_bf16_f32 v152, 0, v152\n\tv_add_f32 %[e], %[e], v152\n"
                   "3:\n\ts_waitcnt lgkmcnt(0)\n\tv_cmp_gt_u32 vcc, 8, v150\n\tv_and_b32 v152, 0xffff0000, v151\n\ts_sub_u32 %[n], %[n], 1\n\ts_cbranch_vccz 4f\n\ts_cbranch_scc1 4f\n\t"
                   "ds_read_b32 v150, %[yf]\n\tds_read_u16_d16_hi v151, %[ya]\n\ts_branch 3b\n"
                   "4:\n\tv_fmac_f32 %[e], v152, %[yc]\n\tv_cvt_pk_bf16_f32 %[e], %[e], %[e]\n\tbuffer_store_short %[e], %[oo], %[ro], 0 offen"
                   : [e] "+v"(e), [n] "+s"(n_), "+{v[132:139]}"(pa), "+{v[140:147]}"(pb)
                   : [po] "v"(po), [p3] "v"(p3), [rs] "s"(rs), [k1] "s"(131072), [k2] "s"(262144), [pk] "v"(pick), [yf] "v"(yf), [ya] "v"(ya), "{v[160:163]}"(junk), [rl] "v"(rsel), [yc] "v"(yc),
                     [oo] "v"(oo), [ro] "s"(ro), [spin] "n"(SPIN)
                   : "vcc", "scc", "memory", "v150", "v151", "v152", "v153", "v154", "v155", "v156", "v157", "v158", "v159" TSD);
      TSGET(14, 2, 2, 2);
      STAMP(15);
    }
    FLUSH();
    __builtin_amdgcn_endpgm();
  }

  u32 pl, pa, pr, pi, ldtv;
  u32x4 tv, fx, junk;
  ROLE(c, "", , "={v[16:19]}"(tv), "={v[20:23]}"(fx), "={v24}"(ldtv), "={v96}"(pl), "={v97}"(pa), "={v68}"(pr), "={v69}"(pi), "={v[160:163]}"(junk));
  char* W = smem + w * CHR;
  __builtin_amdgcn_s_setprio(2);
  STAMP(3);
  const u32x4 zero4 = {0u, 0u, 0u, 0u};
#pragma unroll
  for (int i = 0; i < 4; i++) *(u32x4*)(W + (i < 3 ? TB + 1024 * i : CHR - 1024) + 16 * l) = zero4;
  SB();
  const u32 wb0 = lds0 + w * CHR, xb0 = lds0 + OX + w * XREG;
  asm volatile("s_mov_b32 m0, %0\n\ts_nop 0\n\tds_write_addtid_b32 %1\n\tds_write_addtid_b32 %1 offset:%2" ::"s"(wb0), "v"(0x3c00u), "n"(PB) : "memory");
  u32 ape = wb0 + (n16 == 12 ? 12 : (n16 ^ 3) - 7 * (n16 >> 3)) * RS + 16 * kg, apo = wb0 + (0x765412b0fedc2301ull >> 4 * n16 & 15) * RS + 16 * kg;
  u32x2 kb = {wb0 + ((15 - n16) & 3) * CPE + 2 * ((15 - n16) & 12) + 16 * kg, wb0 + (((n16 >> 2) & 1) + (n16 & 8) + 2 * (3 - (n16 & 3))) * RS + 16 * kg};
  u32x8 ka = {xb0 + 32 * n16 + 16 * kg, wb0 + OY + 32 * n16 + 8 * (kg ^ ((l >> 2) & 3)),
              lds0 + OXF + 4 * (l & 3), wb0 + TB + ((15 - n16) & 3) * CPO + 2 * ((15 - n16) & 12) + 8 * kg, lds0 + OB + 16 * l,
              l < 32 ? 0x01000100u : 0x03020100u, l < 32 ? 0x03020100u : 0x03020302u, (u32)(l >> 2) & 8u};
  u32 fad = lds0 + OYP + 4 * w, za = wb0 + 16 * kg;
  asm volatile("" : "+{v70}"(ape), "+{v71}"(apo), "+{v[28:35]}"(ka), "+{v[26:27]}"(kb), "+{v25}"(fad), "+{v12}"(za));
  STAMP(4);
  asm volatile("s_waitcnt vmcnt(4)" : "+{v24}"(ldtv), "+{v[160:163]}"(junk));
  float dt = rbt(fexp(hi2f(ldtv)));
  asm volatile("" : "+v"(dt));
  const f32x2 cdt = {rdl(dt * -1.4426950408889634f, 0), rdl(dt * 0.15915494309189535f, 0)};
  dt = rdl(dt, 0);
  SB();
  STAMP(10);
  STAMPC(11);
  {
    u32 e, tmp, o0, o1, of, os;
    asm volatile(GEN_ASM
                 : [e] "=&s"(e), [t] "=&s"(tmp), "=&{v93}"(o0), "=&{v46}"(o1), "=&{v48}"(of), "=&{v8}"(os), "+{v25}"(fad), "+{v12}"(za), "+{v68}"(pr), "+{v69}"(pi), "+{v70}"(ape),
                   "+{v71}"(apo), "+{v96}"(pl), "+{v97}"(pa), "+{v[28:35]}"(ka), "+{v[26:27]}"(kb), "+{v[16:19]}"(tv), "+{v[20:23]}"(fx)
                 : [w] "s"(wb0), [c] "s"(cdt), [k] "s"(f32x2{1.4426950408889634f, 256.f})
                 : "memory", "scc", GEN_CLOBBER);
    STAMP(1);
    STAMPC(12);
    const u32 k0 = (u32)__builtin_amdgcn_readlane((int)o0, 3), k256 = (u32)__builtin_amdgcn_readlane((int)o1, 60);
    const u32 k512 = (u32)__builtin_amdgcn_readlane((int)os, 28), k1024 = (u32)__builtin_amdgcn_readlane((int)of, 55);
    *(LDS u16*)(size_t)(wb0 + tv.z) = (u16)__builtin_amdgcn_perm((k512 & 0xffffu) | (k1024 << 16), (k0 >> 16) | (k256 << 16), tv.w);
    *(u32*)(smem + OSC + 4 * w) = __float_as_uint(__uint_as_float((e - 5) << 23) * dt) >> 16;
    fx &= ((k1024 & 0xffffu) | (k1024 << 16)) ^ 0x80008000u;
  }
  asm volatile(CONV_ASM : "+{v[28:35]}"(ka) : "{v[26:27]}"(kb), "{v25}"(fad), "{v[48:51]}"(fx), [w] "s"(w) : "vcc", "scc", "s6", "memory", CONV_CLOBBER TSC);
  TSGET(2, 5, 6, 7);
  STAMP(13);
  STAMPC(15);
  FLUSH();
  __builtin_amdgcn_endpgm();
}

extern "C" void kernel_launch(void* const* d_in, const int* in_sizes, int n_in, void* d_out, int out_size,
                              void* d_ws, size_t ws_size, hipStream_t stream) {
  const u16* const* in = (const u16* const*)d_in;
  fused<<<256, 768, 0, stream>>>(in[0], in[1], in[2], in[3], in[4], in[5], in[6], in[8], in[9], in[10], in[11],
                                 (u16*)d_out, (char*)d_ws);
}
```

```cpp
#include <hip/hip_runtime.h>
#include <stdint.h>

#define STR2(x) #x
#define STR(x) STR2(x)
#define LA 4
#define WL2 44
#define WL3 100
#define PH 1246
#define ABL 0
#define PPRIO 0
#define CPRIO 1
#define GDELAY 0
#define ROT 1
#define PRE 2
#define GNT 0
#define OAUX 0
#define LNT 1
#define PNT ""
#define LNNT ""
#define PRENT ""
#define PFN 15
#define PFO 1024
#define DBAR 2
#define THR 1
#define QD 11
#define HNAP0 4
#define HNAP 4
#define KR 3
#define KI 2
#define QE 11
#define QA 5
#define NA 0
#define CH 0
#define G0E 0
#define DDL 0
#define WBM 0
#define WBW 1
#define HQ 3
#define RSL 5
#define DPF 1
#define HELP 0
#define WARM 0
#define HPI 0
#define Q0 6
#define NP 10
#define HW 28
#define NE 0
#define YNAP "s_sleep 2"
#define QS 7
#define QAS 6
#define RAMP 4
#define GNAP "s_sleep 1"
#define HOLD 8
#define DPRIO 3
#define HAUX 16
#define DPO 128
#define TH 620
#define CLD 300
#define IXI 1
#define LPRIO 0
#define DPI 2
#define PDROP 3
#define TE 0
#define CNAP 2
#define XI 1
#define TSG 290
#define SSL 5
#define PNAP 4
#define TW 310
#define TL 4352
#define CODE_KB 24
typedef unsigned short u16;
typedef unsigned int u32;
typedef __attribute__((ext_vector_type(8))) __bf16 bf16x8;
typedef __attribute__((ext_vector_type(2))) __bf16 bf16x2;
typedef __attribute__((ext_vector_type(8))) _Float16 f16x8;
typedef __attribute__((ext_vector_type(2))) _Float16 f16x2;
typedef __attribute__((ext_vector_type(4))) short v4i16;
typedef __attribute__((ext_vector_type(4))) float f32x4;
typedef __attribute__((ext_vector_type(2))) float f32x2;
typedef __attribute__((ext_vector_type(4))) u32 u32x4;
typedef __attribute__((ext_vector_type(2))) u32 u32x2;
typedef __attribute__((ext_vector_type(8))) u32 u32x8;
typedef __attribute__((ext_vector_type(2), aligned(4))) u32 u32x2_a4;
typedef __amdgpu_buffer_rsrc_t rsrc_t;
#define LDS __attribute__((address_space(3)))

#define TLARG
#define TLVAL
#define STAMP(I)
#define STAMPC(I)
#define FLUSH()

constexpr int L = 2048, D = 1024, NM = 128, F = 3584;
constexpr u32 O_ARR = 0x1000, O_CLR = 0x2000, O_RG = 0x4000, O_SG = 0x8000, O_IDX = 0x10000, O_HG = 0x20000,
              O_PG = 0xd0000, O_TOP = 0x140000, TOPS = 8 * TL, WS_END = O_TOP + 2 * TOPS;
constexpr int SPIN = 1 << 16;

__device__ __forceinline__ float lo2f(u32 w) { return __uint_as_float(w << 16); }
__device__ __forceinline__ float hi2f(u32 w) { return __uint_as_float(w & 0xffff0000u); }
__device__ __forceinline__ u32 pack2(float a, float b) {
  bf16x2 r = {(__bf16)a, (__bf16)b};
  return __builtin_bit_cast(u32, r);
}
__device__ __forceinline__ u32 packh(float a, float b) {
  f16x2 r = {(_Float16)a, (_Float16)b};
  return __builtin_bit_cast(u32, r);
}
__device__ __forceinline__ float rbt(float f) { return __uint_as_float(pack2(0.f, f)); }
__device__ __forceinline__ float rb(float f) {
  float r;
  asm("v_cvt_pk_bf16_f32 %0, 0, %1" : "=v"(r) : "v"(f));
  return r;
}
__device__ __forceinline__ u32 hadd(u32 a, u32 b) {
  return __builtin_bit_cast(u32, __builtin_bit_cast(f16x2, a) + __builtin_bit_cast(f16x2, b));
}
__device__ __forceinline__ float hlo(u32 a) { return (float)__builtin_bit_cast(f16x2, a).x; }
__device__ __forceinline__ float hhi(u32 a) { return (float)__builtin_bit_cast(f16x2, a).y; }

template <int CTRL, int RM>
__device__ __forceinline__ float dppf(float v) {
  return __builtin_bit_cast(float, __builtin_amdgcn_update_dpp(0, __builtin_bit_cast(int, v), CTRL, RM, 0xf, true));
}
template <int CTRL, int RM>
__device__ __forceinline__ u32 dppu(u32 v) {
  return (u32)__builtin_amdgcn_update_dpp(0, (int)v, CTRL, RM, 0xf, true);
}
__device__ __forceinline__ float rdl(float v, int i) {
  return __builtin_bit_cast(float, __builtin_amdgcn_readlane(__builtin_bit_cast(int, v), i));
}
__device__ __forceinline__ u32 wmaxu(u32 v) {
  v = max(v, dppu<0x111, 0xf>(v)); v = max(v, dppu<0x112, 0xf>(v)); v = max(v, dppu<0x114, 0xf>(v));
  v = max(v, dppu<0x118, 0xf>(v)); v = max(v, dppu<0x142, 0xa>(v)); v = max(v, dppu<0x143, 0xc>(v));
  return (u32)__builtin_amdgcn_readlane((int)v, 63);
}
__device__ __forceinline__ void swap32(float& a, float& b) {
  auto r = __builtin_amdgcn_permlane32_swap(__float_as_uint(a), __float_as_uint(b), false, false);
  a = __uint_as_float(r[0]); b = __uint_as_float(r[1]);
}
__device__ __forceinline__ void swap16(float& a, float& b) {
  auto r = __builtin_amdgcn_permlane16_swap(__float_as_uint(a), __float_as_uint(b), false, false);
  a = __uint_as_float(r[0]); b = __uint_as_float(r[1]);
}
__device__ __forceinline__ u32x2 trread(const char* p) {
  v4i16 r = __builtin_amdgcn_ds_read_tr16_b64_v4i16((LDS v4i16*)p);
  return __builtin_bit_cast(u32x2, r);
}
__device__ __forceinline__ u32x2 trl(LDS char* p) { return __builtin_bit_cast(u32x2, __builtin_amdgcn_ds_read_tr16_b64_v4i16((LDS v4i16*)p)); }
__device__ __forceinline__ f16x8 mkh(u32x2 a, u32x2 b) {
  u32x4 v = {a.x, a.y, b.x, b.y};
  return __builtin_bit_cast(f16x8, v);
}
__device__ __forceinline__ bf16x8 mkb(u32x2 a, u32x2 b) {
  u32x4 v = {a.x, a.y, b.x, b.y};
  return __builtin_bit_cast(bf16x8, v);
}
__device__ __forceinline__ bf16x8 asb(u32x4 v) { return __builtin_bit_cast(bf16x8, v); }
__device__ __forceinline__ f16x8 ash(u32x4 v) { return __builtin_bit_cast(f16x8, v); }
#define MFMAB(a, b, c) __builtin_amdgcn_mfma_f32_16x16x32_bf16(a, b, c, 0, 0, 0)
#define MFMAH(a, b, c) __builtin_amdgcn_mfma_f32_16x16x32_f16(a, b, c, 0, 0, 0)

__device__ __forceinline__ u32x4 ld16(rsrc_t r, u32 off) { return __builtin_amdgcn_raw_buffer_load_b128(r, off, 0, 16); }
__device__ __forceinline__ u32x2 ld8(rsrc_t r, u32 off) { return __builtin_amdgcn_raw_buffer_load_b64(r, off, 0, 16); }
__device__ __forceinline__ void st16(rsrc_t r, u32 off, u32x4 v) { __builtin_amdgcn_raw_buffer_store_b128(v, r, off, 0, 16); }
__device__ __forceinline__ void st8(rsrc_t r, u32 off, u32x2 v) { __builtin_amdgcn_raw_buffer_store_b64(v, r, off, 0, 16); }
#define NAP() asm volatile("s_sleep 1" ::: "memory")
#define SB() __builtin_amdgcn_sched_barrier(0)
#define LBAR() asm volatile("s_waitcnt lgkmcnt(0)\n\ts_barrier" ::: "memory")

__device__ __forceinline__ float fexp(float v) { return __builtin_amdgcn_exp2f(v * 1.4426950408889634f); }
__device__ __forceinline__ float dot2b(u32 a, u32 b, float c) {
  return __builtin_amdgcn_fdot2_f32_bf16(__builtin_bit_cast(bf16x2, a), __builtin_bit_cast(bf16x2, b), c, false);
}
__device__ __forceinline__ float sigm(float v) { return __builtin_amdgcn_rcpf(1.f + __builtin_amdgcn_exp2f(-1.4426950408889634f * v)); }

constexpr int RS = 288, PB = 16 * RS;
constexpr int CPE = 1216, TB = 2 * PB, CPO = 832;
constexpr int T2B = 4, T3B = 224, C2 = 52, C3 = 84;
constexpr int OY = 0, CHR = TB + 4 * CPO;
constexpr int OX = 8 * CHR, XREG = 4640, XO = 266;
constexpr int OMISC = OX + 8 * XREG;
constexpr int OYP = OMISC + 256, OXF = OMISC + 320, OCN = OMISC + 336, OSC = OMISC + 352, OIX = OMISC + 384;
constexpr int OPX = OMISC + 512, OB = OMISC + 768, SMEM = 148480;
constexpr int FA = 131072, FH = FA + 8192, HP = 576, FC = FH + 8 * HP;
static_assert(FC + 1024 <= SMEM && OB + 4096 <= SMEM, "lds map");

struct Tab { u32 v[6][64][4]; };
constexpr Tab mktab() {
  Tab T = {};
  for (int l = 0; l < 64; l++) {
    const int n16 = l & 15, kg = l >> 4, pc = ((n16 >> 2) & 1) + (n16 & 8) + 2 * (3 - (n16 & 3)), odd = kg >> 1, s = l & 3;
    const int e0 = 534 - 258 - 128 - 8 * pc - 4 * (kg & 1) - odd;
    const int ez = ((l >> 3) & 3) * CPE + ((l & 4) ? 1056 - 64 : 0) + 16 * (l & 7);
    const int b2 = T2B + 44 + C2 * ((n16 & 1) + 2 * (kg & 1)) - 8 * (n16 & 2), b3 = T3B + 60 + C3 * (kg & 1) - 16 * s;
    const int to = TB + 2 * ((n16 < 8 ? b2 : b3) - 4 * odd - 8) + ((l & 4) ? 0 : 2 * CPO - 4);
    const int q = (l >> 2) & 7, tap = q == 0 ? ((s & 1) ? 0 : 256) : q == 1 ? 257 : 509 + q;
    const int sp = q < 6 ? s * (CPE - 2) + 2 * (537 - tap) : TB + s * (CPO - 2) + 2 * (T2B + 15);
    T.v[0][l][0] = (2 * e0 + (2 - odd) * (CPE - 2)) | (2 * e0 + 3 * odd * (CPE - 2)) << 16;
    T.v[0][l][1] = ez | to << 16;
    T.v[0][l][2] = sp;
    T.v[0][l][3] = q >= 6 ? 0x0706u : q >= 2 ? 0x0504u : tap ? 0x0302u : 0x0100u;
    for (int k = 0; k < 8; k++) {
      const int d = 8 * kg + k - n16;
      if (d == 13) T.v[1][l][k >> 1] |= 0xffffu << 16 * (k & 1);
      for (int h = 1; h <= 4; h++)
        if (d >= (h == 1 ? 14 : 10) - h && d <= (h == 1 ? 14 : 10) + h) T.v[1 + h][l][k >> 1] |= 0x3c00u << 16 * (k & 1);
    }
  }
  return T;
}
extern "C" __device__ const __attribute__((aligned(128), used, visibility("hidden"))) Tab lane_tab = mktab();

#define GEN_ASM \
  "s_waitcnt vmcnt(2)\n\tv_lshlrev_b32 v56, 16, v96\n\t" \
  "v_and_b32 v57, 0xffff0000, v96\n\tv_pk_mul_f32 v[56:57], %[k], v[56:57] op_sel_hi:[0,1]\n\t" \
  "v_lshlrev_b32 v49, 16, v97\n\tv_exp_f32 v56, v56\n\t" \
  "v_exp_f32 v57, v57\n\tv_and_b32 v51, 0xffff0000, v97\n\t" \
  "v_cvt_pk_bf16_f32 v56, v56, v57\n\tv_lshlrev_b32 v48, 16, v56\n\t" \
  "v_and_b32 v50, 0xffff0000, v56\n\tv_pk_mul_f32 v[48:49], %[c], v[48:49]\n\t" \
  "v_pk_mul_f32 v[50:51], %[c], v[50:51]\n\tv_pk_mul_f32 v[98:99], %[k], v[48:49] op_sel:[1,0]\n\t" \
  "v_pk_mul_f32 v[100:101], %[k], v[50:51] op_sel:[1,0]\n\tv_fract_f32 v49, v49\n\t" \
  "v_fract_f32 v51, v51\n\tv_exp_f32 v48, v48\n\t" \
  "v_exp_f32 v50, v50\n\tv_cos_f32 v40, v49\n\t" \
  "v_sin_f32 v41, v49\n\tv_cos_f32 v42, v51\n\t" \
  "v_sin_f32 v43, v51\n\tv_pk_mul_f32 v[40:41], v[40:41], v[48:49] op_sel_hi:[1,0]\n\t" \
  "v_pk_mul_f32 v[42:43], v[42:43], v[50:51] op_sel_hi:[1,0]\n\tv_pk_mul_f32 v[48:49], v[40:41], v[40:41] op_sel:[1,1] op_sel_hi:[1,0] neg_lo:[1,0]\n\t" \
  "v_pk_mul_f32 v[50:51], v[42:43], v[42:43] op_sel:[1,1] op_sel_hi:[1,0] neg_lo:[1,0]\n\tv_pk_fma_f32 v[48:49], v[40:41], v[40:41], v[48:49] op_sel:[0,0,0] op_sel_hi:[0,1,1]\n\t" \
  "v_pk_fma_f32 v[50:51], v[42:43], v[42:43], v[50:51] op_sel:[0,0,0] op_sel_hi:[0,1,1]\n\tv_cvt_pk_f16_f32 v72, v40, -v41\n\t" \
  "v_cvt_pk_f16_f32 v73, v42, -v43\n\tv_pk_mul_f32 v[52:53], v[48:49], v[40:41] op_sel:[1,1] op_sel_hi:[1,0] neg_lo:[1,0]\n\t" \
  "v_pk_mul_f32 v[54:55], v[50:51], v[42:43] op_sel:[1,1] op_sel_hi:[1,0] neg_lo:[1,0]\n\tv_pk_fma_f32 v[52:53], v[48:49], v[40:41], v[52:53] op_sel:[0,0,0] op_sel_hi:[0,1,1]\n\t" \
  "v_pk_fma_f32 v[54:55], v[50:51], v[42:43], v[54:55] op_sel:[0,0,0] op_sel_hi:[0,1,1]\n\tv_pk_mul_f32 v[56:57], v[48:49], v[48:49] op_sel:[1,1] op_sel_hi:[1,0] neg_lo:[1,0]\n\t" \
  "v_pk_mul_f32 v[58:59], v[50:51], v[50:51] op_sel:[1,1] op_sel_hi:[1,0] neg_lo:[1,0]\n\tv_pk_fma_f32 v[56:57], v[48:49], v[48:49], v[56:57] op_sel:[0,0,0] op_sel_hi:[0,1,1]\n\t" \
  "v_pk_fma_f32 v[58:59], v[50:51], v[50:51], v[58:59] op_sel:[0,0,0] op_sel_hi:[0,1,1]\n\tv_cvt_pk_f16_f32 v74, v48, -v49\n\t" \
  "v_cvt_pk_f16_f32 v75, v50, -v51\n\tv_cvt_pk_f16_f32 v76, v52, -v53\n\t" \
  "v_cvt_pk_f16_f32 v77, v54, -v55\n\tv_pk_mul_f32 v[52:53], v[56:57], v[48:49] op_sel:[1,1] op_sel_hi:[1,0] neg_lo:[1,0]\n\t" \
  "v_pk_mul_f32 v[54:55], v[58:59], v[50:51] op_sel:[1,1] op_sel_hi:[1,0] neg_lo:[1,0]\n\tv_pk_fma_f32 v[52:53], v[56:57], v[48:49], v[52:53] op_sel:[0,0,0] op_sel_hi:[0,1,1]\n\t" \
  "v_pk_fma_f32 v[54:55], v[58:59], v[50:51], v[54:55] op_sel:[0,0,0] op_sel_hi:[0,1,1]\n\tv_pk_mul_f32 v[60:61], v[56:57], v[56:57] op_sel:[1,1] op_sel_hi:[1,0] neg_lo:[1,0]\n\t" \
  "v_pk_mul_f32 v[62:63], v[58:59], v[58:59] op_sel:[1,1] op_sel_hi:[1,0] neg_lo:[1,0]\n\tv_pk_fma_f32 v[60:61], v[56:57], v[56:57], v[60:61] op_sel:[0,0,0] op_sel_hi:[0,1,1]\n\t" \
  "v_pk_fma_f32 v[62:63], v[58:59], v[58:59], v[62:63] op_sel:[0,0,0] op_sel_hi:[0,1,1]\n\tv_cvt_pk_f16_f32 v78, v56, -v57\n\t" \
  "v_cvt_pk_f16_f32 v79, v58, -v59\n\tv_cvt_pk_f16_f32 v82, v52, -v53\n\t" \
  "v_cvt_pk_f16_f32 v83, v54, -v55\n\tv_pk_mul_f16 v80, v78, v72 op_sel:[1,1] op_sel_hi:[1,0] neg_lo:[1,0]\n\t" \
  "v_pk_mul_f16 v81, v79, v73 op_sel:[1,1] op_sel_hi:[1,0] neg_lo:[1,0]\n\tv_pk_fma_f16 v80, v78, v72, v80 op_sel:[0,0,0] op_sel_hi:[0,1,1]\n\t" \
  "v_pk_fma_f16 v81, v79, v73, v81 op_sel:[0,0,0] op_sel_hi:[0,1,1]\n\tv_pk_mul_f16 v84, v78, v76 op_sel:[1,1] op_sel_hi:[1,0] neg_lo:[1,0]\n\t" \
  "v_pk_mul_f16 v85, v79, v77 op_sel:[1,1] op_sel_hi:[1,0] neg_lo:[1,0]\n\tv_pk_fma_f16 v84, v78, v76, v84 op_sel:[0,0,0] op_sel_hi:[0,1,1]\n\t" \
  "v_pk_fma_f16 v85, v79, v77, v85 op_sel:[0,0,0] op_sel_hi:[0,1,1]\n\tv_cvt_pk_f16_f32 v86, v60, -v61\n\t" \
  "v_cvt_pk_f16_f32 v87, v62, -v63\n\tds_write_addtid_b32 v72 offset:288\n\t" \
  "ds_write_addtid_b32 v73 offset:4896\n\tds_write_addtid_b32 v74 offset:576\n\t" \
  "ds_write_addtid_b32 v75 offset:5184\n\tds_write_addtid_b32 v76 offset:864\n\t" \
  "ds_write_addtid_b32 v77 offset:5472\n\tds_write_addtid_b32 v78 offset:1152\n\t" \
  "ds_write_addtid_b32 v79 offset:5760\n\tds_write_addtid_b32 v80 offset:1440\n\t" \
  "ds_write_addtid_b32 v81 offset:6048\n\tds_write_addtid_b32 v82 offset:1728\n\t" \
  "ds_write_addtid_b32 v83 offset:6336\n\tds_write_addtid_b32 v84 offset:2016\n\t" \
  "ds_write_addtid_b32 v85 offset:6624\n\tds_write_addtid_b32 v86 offset:3456\n\t" \
  "ds_write_addtid_b32 v87 offset:8064\n\tds_read_b128 v[104:107], v70 offset:0\n\t" \
  "ds_read_b128 v[108:111], v70 offset:64\n\tds_read_b128 v[112:115], v70 offset:128\n\t" \
  "ds_read_b128 v[116:119], v70 offset:192\n\tds_read_b128 v[120:123], v70 offset:4608\n\t" \
  "ds_read_b128 v[124:127], v70 offset:4672\n\tds_read_b128 v[128:131], v70 offset:4736\n\t" \
  "ds_read_b128 v[132:135], v70 offset:4800\n\tv_fract_f32 v99, v99\n\t" \
  "v_fract_f32 v101, v101\n\tv_exp_f32 v98, v98\n\t" \
  "v_exp_f32 v100, v100\n\tv_cos_f32 v44, v99\n\t" \
  "v_sin_f32 v45, v99\n\tv_cos_f32 v46, v101\n\t" \
  "v_sin_f32 v47, v101\n\tv_pk_mul_f32 v[44:45], v[44:45], v[98:99] op_sel_hi:[1,0]\n\t" \
  "v_pk_mul_f32 v[46:47], v[46:47], v[100:101] op_sel_hi:[1,0]\n\ts_waitcnt vmcnt(0)\n\t" \
  "v_pk_mul_f32 v[48:49], v[44:45], v[44:45] op_sel:[1,1] op_sel_hi:[1,0] neg_lo:[1,0]\n\tv_pk_mul_f32 v[50:51], v[46:47], v[46:47] op_sel:[1,1] op_sel_hi:[1,0] neg_lo:[1,0]\n\t" \
  "v_pk_fma_f32 v[48:49], v[44:45], v[44:45], v[48:49] op_sel:[0,0,0] op_sel_hi:[0,1,1]\n\tv_pk_fma_f32 v[50:51], v[46:47], v[46:47], v[50:51] op_sel:[0,0,0] op_sel_hi:[0,1,1]\n\t" \
  "v_and_b32 v102, 0x7fff7fff, v68\n\tv_cvt_pk_f16_f32 v88, v44, -v45\n\t" \
  "v_cvt_pk_f16_f32 v89, v46, -v47\n\tv_pk_mul_f32 v[52:53], v[48:49], v[44:45] op_sel:[1,1] op_sel_hi:[1,0] neg_lo:[1,0]\n\t" \
  "v_pk_mul_f32 v[54:55], v[50:51], v[46:47] op_sel:[1,1] op_sel_hi:[1,0] neg_lo:[1,0]\n\tv_and_b32 v103, 0x7fff7fff, v69\n\t" \
  "v_pk_fma_f32 v[52:53], v[48:49], v[44:45], v[52:53] op_sel:[0,0,0] op_sel_hi:[0,1,1]\n\tv_pk_fma_f32 v[54:55], v[50:51], v[46:47], v[54:55] op_sel:[0,0,0] op_sel_hi:[0,1,1]\n\t" \
  "v_pk_mul_f32 v[56:57], v[48:49], v[48:49] op_sel:[1,1] op_sel_hi:[1,0] neg_lo:[1,0]\n\tv_pk_mul_f32 v[58:59], v[50:51], v[50:51] op_sel:[1,1] op_sel_hi:[1,0] neg_lo:[1,0]\n\t" \
  "v_pk_max_u16 v102, v102, v103\n\tv_pk_fma_f32 v[56:57], v[48:49], v[48:49], v[56:57] op_sel:[0,0,0] op_sel_hi:[0,1,1]\n\t" \
  "v_pk_fma_f32 v[58:59], v[50:51], v[50:51], v[58:59] op_sel:[0,0,0] op_sel_hi:[0,1,1]\n\tv_cvt_pk_f16_f32 v90, v48, -v49\n\t" \
  "v_cvt_pk_f16_f32 v91, v50, -v51\n\tv_pk_max_u16 v102, v102, v102 op_sel:[1,0] op_sel_hi:[0,1]\n\t" \
  "v_pk_mul_f32 v[64:65], v[56:57], v[60:61] op_sel:[1,1] op_sel_hi:[1,0] neg_lo:[1,0]\n\tv_pk_mul_f32 v[66:67], v[58:59], v[62:63] op_sel:[1,1] op_sel_hi:[1,0] neg_lo:[1,0]\n\t" \
  "v_pk_fma_f32 v[64:65], v[56:57], v[60:61], v[64:65] op_sel:[0,0,0] op_sel_hi:[0,1,1]\n\tv_pk_fma_f32 v[66:67], v[58:59], v[62:63], v[66:67] op_sel:[0,0,0] op_sel_hi:[0,1,1]\n\t" \
  "v_max_u32_dpp v102, v102, v102 row_shr:1 row_mask:0xf bank_mask:0xf\n\tv_cvt_pk_f16_f32 v92, v52, -v53\n\t" \
  "v_cvt_pk_f16_f32 v93, v54, -v55\n\tv_pk_mul_f16 v136, v88, v74 op_sel:[1,1] op_sel_hi:[1,0] neg_lo:[1,0]\n\t" \
  "v_pk_mul_f16 v137, v89, v75 op_sel:[1,1] op_sel_hi:[1,0] neg_lo:[1,0]\n\tv_max_u32_dpp v102, v102, v102 row_shr:2 row_mask:0xf bank_mask:0xf\n\t" \
  "v_pk_fma_f16 v136, v88, v74, v136 op_sel:[0,0,0] op_sel_hi:[0,1,1]\n\tv_pk_fma_f16 v137, v89, v75, v137 op_sel:[0,0,0] op_sel_hi:[0,1,1]\n\t" \
  "v_pk_mul_f16 v138, v88, v78 op_sel:[1,1] op_sel_hi:[1,0] neg_lo:[1,0]\n\tv_pk_mul_f16 v139, v89, v79 op_sel:[1,1] op_sel_hi:[1,0] neg_lo:[1,0]\n\t" \
  "v_max_u32_dpp v102, v102, v102 row_shr:4 row_mask:0xf bank_mask:0xf\n\tv_pk_fma_f16 v138, v88, v78, v138 op_sel:[0,0,0] op_sel_hi:[0,1,1]\n\t" \
  "v_pk_fma_f16 v139, v89, v79, v139 op_sel:[0,0,0] op_sel_hi:[0,1,1]\n\tv_cvt_pk_f16_f32 v94, v64, -v65\n\t" \
  "v_cvt_pk_f16_f32 v95, v66, -v67\n\tv_max_u32_dpp v102, v102, v102 row_shr:8 row_mask:0xf bank_mask:0xf\n\t" \
  "v_pk_mul_f16 v140, v88, v82 op_sel:[1,1] op_sel_hi:[1,0] neg_lo:[1,0]\n\tv_pk_mul_f16 v141, v89, v83 op_sel:[1,1] op_sel_hi:[1,0] neg_lo:[1,0]\n\t" \
  "v_pk_fma_f16 v140, v88, v82, v140 op_sel:[0,0,0] op_sel_hi:[0,1,1]\n\tv_pk_fma_f16 v141, v89, v83, v141 op_sel:[0,0,0] op_sel_hi:[0,1,1]\n\t" \
  "ds_write_addtid_b32 v88 offset:3168\n\tds_write_addtid_b32 v89 offset:7776\n\t" \
  "ds_write_addtid_b32 v136 offset:0\n\tds_write_addtid_b32 v137 offset:4608\n\t" \
  "ds_write_addtid_b32 v138 offset:288\n\tds_write_addtid_b32 v139 offset:4896\n\t" \
  "ds_write_addtid_b32 v94 offset:1152\n\tds_write_addtid_b32 v95 offset:5760\n\t" \
  "ds_write_addtid_b32 v140 offset:576\n\tds_write_addtid_b32 v141 offset:5184\n\t" \
  "v_max_u32_dpp v102, v102, v102 row_bcast:15 row_mask:0xa bank_mask:0xf\n\tv_pk_mul_f16 v142, v88, v86 op_sel:[1,1] op_sel_hi:[1,0] neg_lo:[1,0]\n\t" \
  "v_pk_mul_f16 v143, v89, v87 op_sel:[1,1] op_sel_hi:[1,0] neg_lo:[1,0]\n\tv_pk_fma_f16 v142, v88, v86, v142 op_sel:[0,0,0] op_sel_hi:[0,1,1]\n\t" \
  "v_pk_fma_f16 v143, v89, v87, v143 op_sel:[0,0,0] op_sel_hi:[0,1,1]\n\tv_max_u32_dpp v102, v102, v102 row_bcast:31 row_mask:0xc bank_mask:0xf\n\t" \
  "v_pk_mul_f16 v144, v90, v78 op_sel:[1,1] op_sel_hi:[1,0] neg_lo:[1,0]\n\tv_pk_mul_f16 v145, v91, v79 op_sel:[1,1] op_sel_hi:[1,0] neg_lo:[1,0]\n\t" \
  "v_pk_fma_f16 v144, v90, v78, v144 op_sel:[0,0,0] op_sel_hi:[0,1,1]\n\tv_pk_fma_f16 v145, v91, v79, v145 op_sel:[0,0,0] op_sel_hi:[0,1,1]\n\t" \
  "v_readlane_b32 %[e], v102, 63\n\tv_pk_mul_f16 v146, v90, v86 op_sel:[1,1] op_sel_hi:[1,0] neg_lo:[1,0]\n\t" \
  "v_pk_mul_f16 v147, v91, v87 op_sel:[1,1] op_sel_hi:[1,0] neg_lo:[1,0]\n\tv_pk_fma_f16 v146, v90, v86, v146 op_sel:[0,0,0] op_sel_hi:[0,1,1]\n\t" \
  "v_pk_fma_f16 v147, v91, v87, v147 op_sel:[0,0,0] op_sel_hi:[0,1,1]\n\tv_pk_mul_f16 v148, v92, v78 op_sel:[1,1] op_sel_hi:[1,0] neg_lo:[1,0]\n\t" \
  "v_pk_mul_f16 v149, v93, v79 op_sel:[1,1] op_sel_hi:[1,0] neg_lo:[1,0]\n\tv_pk_fma_f16 v148, v92, v78, v148 op_sel:[0,0,0] op_sel_hi:[0,1,1]\n\t" \
  "v_pk_fma_f16 v149, v93, v79, v149 op_sel:[0,0,0] op_sel_hi:[0,1,1]\n\tv_pk_mul_f16 v150, v92, v86 op_sel:[1,1] op_sel_hi:[1,0] neg_lo:[1,0]\n\t" \
  "v_pk_mul_f16 v151, v93, v87 op_sel:[1,1] op_sel_hi:[1,0] neg_lo:[1,0]\n\tv_pk_fma_f16 v150, v92, v86, v150 op_sel:[0,0,0] op_sel_hi:[0,1,1]\n\t" \
  "v_pk_fma_f16 v151, v93, v87, v151 op_sel:[0,0,0] op_sel_hi:[0,1,1]\n\ts_bfe_u32 %[e], %[e], 0x80007\n\t" \
  "s_max_u32 %[e], %[e], 8\n\ts_sub_u32 %[t], 260, %[e]\n\t" \
  "s_lshl_b32 %[t], %[t], 23\n\tds_write_addtid_b32 v142 offset:864\n\t" \
  "ds_write_addtid_b32 v143 offset:5472\n\tds_write_addtid_b32 v144 offset:3456\n\t" \
  "ds_write_addtid_b32 v145 offset:8064\n\tds_write_addtid_b32 v146 offset:3744\n\t" \
  "ds_write_addtid_b32 v147 offset:8352\n\tds_write_addtid_b32 v148 offset:4032\n\t" \
  "ds_write_addtid_b32 v149 offset:8640\n\tds_write_addtid_b32 v150 offset:4320\n\t" \
  "ds_write_addtid_b32 v151 offset:8928\n\tv_pk_mul_f16 v96, v94, v88 op_sel:[1,1] op_sel_hi:[1,0] neg_lo:[1,0]\n\t" \
  "v_pk_mul_f16 v97, v95, v89 op_sel:[1,1] op_sel_hi:[1,0] neg_lo:[1,0]\n\tv_pk_fma_f16 v96, v94, v88, v96 op_sel:[0,0,0] op_sel_hi:[0,1,1]\n\t" \
  "v_pk_fma_f16 v97, v95, v89, v97 op_sel:[0,0,0] op_sel_hi:[0,1,1]\n\tv_pk_mul_f16 v98, v94, v90 op_sel:[1,1] op_sel_hi:[1,0] neg_lo:[1,0]\n\t" \
  "v_pk_mul_f16 v99, v95, v91 op_sel:[1,1] op_sel_hi:[1,0] neg_lo:[1,0]\n\tv_pk_fma_f16 v98, v94, v90, v98 op_sel:[0,0,0] op_sel_hi:[0,1,1]\n\t" \
  "v_pk_fma_f16 v99, v95, v91, v99 op_sel:[0,0,0] op_sel_hi:[0,1,1]\n\tv_pk_mul_f16 v100, v94, v92 op_sel:[1,1] op_sel_hi:[1,0] neg_lo:[1,0]\n\t" \
  "v_pk_mul_f16 v101, v95, v93 op_sel:[1,1] op_sel_hi:[1,0] neg_lo:[1,0]\n\tv_pk_fma_f16 v100, v94, v92, v100 op_sel:[0,0,0] op_sel_hi:[0,1,1]\n\t" \
  "v_pk_fma_f16 v101, v95, v93, v101 op_sel:[0,0,0] op_sel_hi:[0,1,1]\n\tds_write_addtid_b32 v96 offset:1440\n\t" \
  "ds_write_addtid_b32 v97 offset:6048\n\tds_write_addtid_b32 v98 offset:1728\n\t" \
  "ds_write_addtid_b32 v99 offset:6336\n\tds_write_addtid_b32 v100 offset:2016\n\t" \
  "ds_write_addtid_b32 v101 offset:6624\n\tds_read_b128 v[136:139], v71 offset:0\n\t" \
  "ds_read_b128 v[140:143], v71 offset:64\n\tds_read_b128 v[144:147], v71 offset:128\n\t" \
  "ds_read_b128 v[148:151], v71 offset:192\n\tds_read_b128 v[152:155], v71 offset:4608\n\t" \
  "ds_read_b128 v[156:159], v71 offset:4672\n\tds_read_b128 v[160:163], v71 offset:4736\n\t" \
  "ds_read_b128 v[164:167], v71 offset:4800\n\tv_pk_mul_f32 v[48:49], v[60:61], v[60:61] op_sel:[1,1] op_sel_hi:[1,0] neg_lo:[1,0]\n\t" \
  "v_pk_mul_f32 v[50:51], v[62:63], v[62:63] op_sel:[1,1] op_sel_hi:[1,0] neg_lo:[1,0]\n\tv_pk_fma_f32 v[48:49], v[60:61], v[60:61], v[48:49] op_sel:[0,0,0] op_sel_hi:[0,1,1]\n\t" \
  "s_cmp_lt_u32 %[w], 50176\n\ts_cbranch_scc0 1f\n\t" \
  "s_setprio 1\n\t1:\n\t" \
  "v_pk_fma_f32 v[50:51], v[62:63], v[62:63], v[50:51] op_sel:[0,0,0] op_sel_hi:[0,1,1]\n\tv_cvt_pk_f16_f32 v44, v60, v61\n\t" \
  "v_cvt_pk_f16_f32 v45, v62, v63\n\tv_pk_mul_f32 v[52:53], v[48:49], v[60:61] op_sel:[1,1] op_sel_hi:[1,0] neg_lo:[1,0]\n\t" \
  "v_pk_mul_f32 v[54:55], v[50:51], v[62:63] op_sel:[1,1] op_sel_hi:[1,0] neg_lo:[1,0]\n\tv_pk_fma_f32 v[52:53], v[48:49], v[60:61], v[52:53] op_sel:[0,0,0] op_sel_hi:[0,1,1]\n\t" \
  "v_pk_fma_f32 v[54:55], v[50:51], v[62:63], v[54:55] op_sel:[0,0,0] op_sel_hi:[0,1,1]\n\tv_pk_mul_f32 v[40:41], v[48:49], v[48:49] op_sel:[1,1] op_sel_hi:[1,0] neg_lo:[1,0]\n\t" \
  "v_pk_mul_f32 v[42:43], v[50:51], v[50:51] op_sel:[1,1] op_sel_hi:[1,0] neg_lo:[1,0]\n\tv_pk_fma_f32 v[40:41], v[48:49], v[48:49], v[40:41] op_sel:[0,0,0] op_sel_hi:[0,1,1]\n\t" \
  "v_pk_fma_f32 v[42:43], v[50:51], v[50:51], v[42:43] op_sel:[0,0,0] op_sel_hi:[0,1,1]\n\tv_cvt_pk_f16_f32 v46, v48, v49\n\t" \
  "v_cvt_pk_f16_f32 v47, v50, v51\n\tv_cvt_pk_f16_f32 v56, v52, v53\n\t" \
  "v_cvt_pk_f16_f32 v57, v54, v55\n\tv_lshlrev_b32 v64, 16, v68\n\t" \
  "v_lshlrev_b32 v65, 16, v69\n\tv_and_b32 v66, 0xffff0000, v68\n\t" \
  "v_and_b32 v67, 0xffff0000, v69\n\tv_mul_f32 v64, %[t], v64\n\t" \
  "v_mul_f32 v65, %[t], v65\n\tv_mul_f32 v66, %[t], v66\n\t" \
  "v_mul_f32 v67, %[t], v67\n\tv_cvt_pk_f16_f32 v48, v64, v65\n\t" \
  "v_cvt_pk_f16_f32 v49, v66, v67\n\tv_pk_mul_f32 v[68:69], v[64:65], v[40:41] op_sel:[1,1] op_sel_hi:[1,0] neg_lo:[1,0]\n\t" \
  "v_pk_mul_f32 v[70:71], v[66:67], v[42:43] op_sel:[1,1] op_sel_hi:[1,0] neg_lo:[1,0]\n\tv_pk_fma_f32 v[68:69], v[64:65], v[40:41], v[68:69] op_sel:[0,0,0] op_sel_hi:[0,1,1]\n\t" \
  "v_pk_fma_f32 v[70:71], v[66:67], v[42:43], v[70:71] op_sel:[0,0,0] op_sel_hi:[0,1,1]\n\tv_pk_mul_f16 v50, v48, v44 op_sel:[1,1] op_sel_hi:[1,0] neg_lo:[1,0]\n\t" \
  "v_pk_mul_f16 v51, v49, v45 op_sel:[1,1] op_sel_hi:[1,0] neg_lo:[1,0]\n\tv_pk_fma_f16 v50, v48, v44, v50 op_sel:[0,0,0] op_sel_hi:[0,1,1]\n\t" \
  "v_pk_fma_f16 v51, v49, v45, v51 op_sel:[0,0,0] op_sel_hi:[0,1,1]\n\tv_pk_mul_f16 v52, v48, v46 op_sel:[1,1] op_sel_hi:[1,0] neg_lo:[1,0]\n\t" \
  "v_pk_mul_f16 v53, v49, v47 op_sel:[1,1] op_sel_hi:[1,0] neg_lo:[1,0]\n\tv_pk_fma_f16 v52, v48, v46, v52 op_sel:[0,0,0] op_sel_hi:[0,1,1]\n\t" \
  "v_pk_fma_f16 v53, v49, v47, v53 op_sel:[0,0,0] op_sel_hi:[0,1,1]\n\tv_pk_mul_f16 v54, v48, v56 op_sel:[1,1] op_sel_hi:[1,0] neg_lo:[1,0]\n\t" \
  "v_pk_mul_f16 v55, v49, v57 op_sel:[1,1] op_sel_hi:[1,0] neg_lo:[1,0]\n\tv_pk_fma_f16 v54, v48, v56, v54 op_sel:[0,0,0] op_sel_hi:[0,1,1]\n\t" \
  "v_pk_fma_f16 v55, v49, v57, v55 op_sel:[0,0,0] op_sel_hi:[0,1,1]\n\tds_write_addtid_b32 v48 offset:0\n\t" \
  "ds_write_addtid_b32 v49 offset:4608\n\tds_write_addtid_b32 v50 offset:288\n\t" \
  "ds_write_addtid_b32 v51 offset:4896\n\tds_write_addtid_b32 v52 offset:576\n\t" \
  "ds_write_addtid_b32 v53 offset:5184\n\tds_write_addtid_b32 v54 offset:864\n\t" \
  "ds_write_addtid_b32 v55 offset:5472\n\tv_pk_mul_f32 v[60:61], v[40:41], v[40:41] op_sel:[1,1] op_sel_hi:[1,0] neg_lo:[1,0]\n\t" \
  "v_pk_mul_f32 v[62:63], v[42:43], v[42:43] op_sel:[1,1] op_sel_hi:[1,0] neg_lo:[1,0]\n\tv_pk_fma_f32 v[60:61], v[40:41], v[40:41], v[60:61] op_sel:[0,0,0] op_sel_hi:[0,1,1]\n\t" \
  "v_pk_fma_f32 v[62:63], v[42:43], v[42:43], v[62:63] op_sel:[0,0,0] op_sel_hi:[0,1,1]\n\tv_cvt_pk_f16_f32 v48, v68, v69\n\t" \
  "v_cvt_pk_f16_f32 v49, v70, v71\n\tv_pk_mul_f32 v[64:65], v[68:69], v[40:41] op_sel:[1,1] op_sel_hi:[1,0] neg_lo:[1,0]\n\t" \
  "v_pk_mul_f32 v[66:67], v[70:71], v[42:43] op_sel:[1,1] op_sel_hi:[1,0] neg_lo:[1,0]\n\tv_pk_fma_f32 v[64:65], v[68:69], v[40:41], v[64:65] op_sel:[0,0,0] op_sel_hi:[0,1,1]\n\t" \
  "v_pk_fma_f32 v[66:67], v[70:71], v[42:43], v[66:67] op_sel:[0,0,0] op_sel_hi:[0,1,1]\n\tv_pk_mul_f16 v50, v48, v44 op_sel:[1,1] op_sel_hi:[1,0] neg_lo:[1,0]\n\t" \
  "v_pk_mul_f16 v51, v49, v45 op_sel:[1,1] op_sel_hi:[1,0] neg_lo:[1,0]\n\tv_pk_fma_f16 v50, v48, v44, v50 op_sel:[0,0,0] op_sel_hi:[0,1,1]\n\t" \
  "v_pk_fma_f16 v51, v49, v45, v51 op_sel:[0,0,0] op_sel_hi:[0,1,1]\n\tv_pk_mul_f16 v52, v48, v46 op_sel:[1,1] op_sel_hi:[1,0] neg_lo:[1,0]\n\t" \
  "v_pk_mul_f16 v53, v49, v47 op_sel:[1,1] op_sel_hi:[1,0] neg_lo:[1,0]\n\tv_pk_fma_f16 v52, v48, v46, v52 op_sel:[0,0,0] op_sel_hi:[0,1,1]\n\t" \
  "v_pk_fma_f16 v53, v49, v47, v53 op_sel:[0,0,0] op_sel_hi:[0,1,1]\n\tv_pk_mul_f16 v54, v48, v56 op_sel:[1,1] op_sel_hi:[1,0] neg_lo:[1,0]\n\t" \
  "v_pk_mul_f16 v55, v49, v57 op_sel:[1,1] op_sel_hi:[1,0] neg_lo:[1,0]\n\tv_pk_fma_f16 v54, v48, v56, v54 op_sel:[0,0,0] op_sel_hi:[0,1,1]\n\t" \
  "v_pk_fma_f16 v55, v49, v57, v55 op_sel:[0,0,0] op_sel_hi:[0,1,1]\n\tds_write_addtid_b32 v48 offset:1152\n\t" \
  "ds_write_addtid_b32 v49 offset:5760\n\tds_write_addtid_b32 v50 offset:1440\n\t" \
  "ds_write_addtid_b32 v51 offset:6048\n\tds_write_addtid_b32 v52 offset:1728\n\t" \
  "ds_write_addtid_b32 v53 offset:6336\n\tds_write_addtid_b32 v54 offset:2016\n\t" \
  "ds_write_addtid_b32 v55 offset:6624\n\tv_pk_mul_f32 v[36:37], v[60:61], v[60:61] op_sel:[1,1] op_sel_hi:[1,0] neg_lo:[1,0]\n\t" \
  "v_pk_mul_f32 v[38:39], v[62:63], v[62:63] op_sel:[1,1] op_sel_hi:[1,0] neg_lo:[1,0]\n\tv_pk_fma_f32 v[36:37], v[60:61], v[60:61], v[36:37] op_sel:[0,0,0] op_sel_hi:[0,1,1]\n\t" \
  "v_pk_fma_f32 v[38:39], v[62:63], v[62:63], v[38:39] op_sel:[0,0,0] op_sel_hi:[0,1,1]\n\tv_cvt_pk_f16_f32 v48, v64, v65\n\t" \
  "v_cvt_pk_f16_f32 v49, v66, v67\n\tv_pk_mul_f32 v[68:69], v[64:65], v[40:41] op_sel:[1,1] op_sel_hi:[1,0] neg_lo:[1,0]\n\t" \
  "v_pk_mul_f32 v[70:71], v[66:67], v[42:43] op_sel:[1,1] op_sel_hi:[1,0] neg_lo:[1,0]\n\tv_pk_fma_f32 v[68:69], v[64:65], v[40:41], v[68:69] op_sel:[0,0,0] op_sel_hi:[0,1,1]\n\t" \
  "v_pk_fma_f32 v[70:71], v[66:67], v[42:43], v[70:71] op_sel:[0,0,0] op_sel_hi:[0,1,1]\n\tv_pk_mul_f16 v50, v48, v44 op_sel:[1,1] op_sel_hi:[1,0] neg_lo:[1,0]\n\t" \
  "v_pk_mul_f16 v51, v49, v45 op_sel:[1,1] op_sel_hi:[1,0] neg_lo:[1,0]\n\tv_pk_fma_f16 v50, v48, v44, v50 op_sel:[0,0,0] op_sel_hi:[0,1,1]\n\t" \
  "v_pk_fma_f16 v51, v49, v45, v51 op_sel:[0,0,0] op_sel_hi:[0,1,1]\n\tv_pk_mul_f16 v52, v48, v46 op_sel:[1,1] op_sel_hi:[1,0] neg_lo:[1,0]\n\t" \
  "v_pk_mul_f16 v53, v49, v47 op_sel:[1,1] op_sel_hi:[1,0] neg_lo:[1,0]\n\tv_pk_fma_f16 v52, v48, v46, v52 op_sel:[0,0,0] op_sel_hi:[0,1,1]\n\t" \
  "v_pk_fma_f16 v53, v49, v47, v53 op_sel:[0,0,0] op_sel_hi:[0,1,1]\n\tv_pk_mul_f16 v54, v48, v56 op_sel:[1,1] op_sel_hi:[1,0] neg_lo:[1,0]\n\t" \
  "v_pk_mul_f16 v55, v49, v57 op_sel:[1,1] op_sel_hi:[1,0] neg_lo:[1,0]\n\tv_pk_fma_f16 v54, v48, v56, v54 op_sel:[0,0,0] op_sel_hi:[0,1,1]\n\t" \
  "v_pk_fma_f16 v55, v49, v57, v55 op_sel:[0,0,0] op_sel_hi:[0,1,1]\n\tds_write_addtid_b32 v48 offset:2304\n\t" \
  "ds_write_addtid_b32 v49 offset:6912\n\tds_write_addtid_b32 v50 offset:2592\n\t" \
  "ds_write_addtid_b32 v51 offset:7200\n\tds_write_addtid_b32 v52 offset:2880\n\t" \
  "ds_write_addtid_b32 v53 offset:7488\n\tds_write_addtid_b32 v54 offset:3168\n\t" \
  "ds_write_addtid_b32 v55 offset:7776\n\tv_cvt_pk_f16_f32 v58, v36, v37\n\t" \
  "v_cvt_pk_f16_f32 v59, v38, v39\n\tv_cvt_pk_f16_f32 v48, v68, v69\n\t" \
  "v_cvt_pk_f16_f32 v49, v70, v71\n\tv_pk_mul_f16 v50, v48, v44 op_sel:[1,1] op_sel_hi:[1,0] neg_lo:[1,0]\n\t" \
  "v_pk_mul_f16 v51, v49, v45 op_sel:[1,1] op_sel_hi:[1,0] neg_lo:[1,0]\n\tv_pk_fma_f16 v50, v48, v44, v50 op_sel:[0,0,0] op_sel_hi:[0,1,1]\n\t" \
  "v_pk_fma_f16 v51, v49, v45, v51 op_sel:[0,0,0] op_sel_hi:[0,1,1]\n\tv_pk_mul_f16 v52, v48, v46 op_sel:[1,1] op_sel_hi:[1,0] neg_lo:[1,0]\n\t" \
  "v_pk_mul_f16 v53, v49, v47 op_sel:[1,1] op_sel_hi:[1,0] neg_lo:[1,0]\n\tv_pk_fma_f16 v52, v48, v46, v52 op_sel:[0,0,0] op_sel_hi:[0,1,1]\n\t" \
  "v_pk_fma_f16 v53, v49, v47, v53 op_sel:[0,0,0] op_sel_hi:[0,1,1]\n\tv_pk_mul_f16 v54, v48, v56 op_sel:[1,1] op_sel_hi:[1,0] neg_lo:[1,0]\n\t" \
  "v_pk_mul_f16 v55, v49, v57 op_sel:[1,1] op_sel_hi:[1,0] neg_lo:[1,0]\n\tv_pk_fma_f16 v54, v48, v56, v54 op_sel:[0,0,0] op_sel_hi:[0,1,1]\n\t" \
  "v_pk_fma_f16 v55, v49, v57, v55 op_sel:[0,0,0] op_sel_hi:[0,1,1]\n\tds_write_addtid_b32 v48 offset:3456\n\t" \
  "ds_write_addtid_b32 v49 offset:8064\n\tds_write_addtid_b32 v50 offset:3744\n\t" \
  "ds_write_addtid_b32 v51 offset:8352\n\tds_write_addtid_b32 v52 offset:4032\n\t" \
  "ds_write_addtid_b32 v53 offset:8640\n\tds_write_addtid_b32 v54 offset:4320\n\t" \
  "ds_write_addtid_b32 v55 offset:8928\n\tds_read_b128 v[72:75], v27 offset:0\n\t" \
  "ds_read_b128 v[76:79], v27 offset:64\n\tds_read_b128 v[80:83], v27 offset:128\n\t" \
  "ds_read_b128 v[84:87], v27 offset:192\n\tds_read_b128 v[88:91], v27 offset:4608\n\t" \
  "ds_read_b128 v[92:95], v27 offset:4672\n\tds_read_b128 v[96:99], v27 offset:4736\n\t" \
  "ds_read_b128 v[100:103], v27 offset:4800\n\tds_write_addtid_b32 v58 offset:0\n\t" \
  "ds_write_addtid_b32 v59 offset:4608\n\tds_read_b128 v[40:43], v12 offset:0\n\t" \
  "ds_read_b128 v[44:47], v12 offset:64\n\tds_read_b128 v[48:51], v12 offset:128\n\t" \
  "v_add_u32_sdwa v56, %[w], v16 dst_sel:DWORD dst_unused:UNUSED_PAD src0_sel:DWORD src1_sel:WORD_0\n\tv_add_u32_sdwa v57, %[w], v16 dst_sel:DWORD dst_unused:UNUSED_PAD src0_sel:DWORD src1_sel:WORD_1\n\t" \
  "v_add_u32_sdwa v58, %[w], v17 dst_sel:DWORD dst_unused:UNUSED_PAD src0_sel:DWORD src1_sel:WORD_0\n\tv_add_u32_sdwa v59, %[w], v17 dst_sel:DWORD dst_unused:UNUSED_PAD src0_sel:DWORD src1_sel:WORD_1\n\t" \
  "s_cmp_lt_u32 %[w], 50176\n\ts_cbranch_scc1 1f\n\t" \
  "s_setprio 1\n\t1:\n\t" \
  "v_add_u32 v14, v56, v35\n\tv_add_u32 v15, v57, v35\n\t" \
  "s_waitcnt lgkmcnt(12)\n\tv_mfma_f32_16x16x32_f16 v[60:63], v[104:107], v[72:75], 0\n\t" \
  "v_mfma_f32_16x16x32_f16 v[36:39], v[72:75], v[136:139], 0\n\tv_mfma_f32_16x16x32_f16 v[4:7], v[136:139], v[72:75], 0\n\t" \
  "s_waitcnt lgkmcnt(2)\n\tv_mfma_f32_16x16x32_f16 v[60:63], v[108:111], v[76:79], v[60:63]\n\t" \
  "v_pk_mul_f16 v52, v40, v72 op_sel:[1,1] op_sel_hi:[1,0] neg_lo:[1,0]\n\tv_pk_mul_f16 v53, v41, v73 op_sel:[1,1] op_sel_hi:[1,0] neg_lo:[1,0]\n\t" \
  "v_pk_mul_f16 v54, v42, v74 op_sel:[1,1] op_sel_hi:[1,0] neg_lo:[1,0]\n\tv_mfma_f32_16x16x32_f16 v[36:39], v[76:79], v[140:143], v[36:39]\n\t" \
  "v_pk_mul_f16 v55, v43, v75 op_sel:[1,1] op_sel_hi:[1,0] neg_lo:[1,0]\n\tv_pk_fma_f16 v40, v40, v72, v52 op_sel:[0,0,0] op_sel_hi:[0,1,1]\n\t" \
  "v_pk_fma_f16 v41, v41, v73, v53 op_sel:[0,0,0] op_sel_hi:[0,1,1]\n\tv_mfma_f32_16x16x32_f16 v[4:7], v[140:143], v[76:79], v[4:7]\n\t" \
  "v_pk_fma_f16 v42, v42, v74, v54 op_sel:[0,0,0] op_sel_hi:[0,1,1]\n\tv_pk_fma_f16 v43, v43, v75, v55 op_sel:[0,0,0] op_sel_hi:[0,1,1]\n\t" \
  "s_waitcnt lgkmcnt(1)\n\tv_mfma_f32_16x16x32_f16 v[60:63], v[112:115], v[80:83], v[60:63]\n\t" \
  "ds_read_b128 v[72:75], v12 offset:192\n\tv_pk_mul_f16 v52, v44, v76 op_sel:[1,1] op_sel_hi:[1,0] neg_lo:[1,0]\n\t" \
  "v_pk_mul_f16 v53, v45, v77 op_sel:[1,1] op_sel_hi:[1,0] neg_lo:[1,0]\n\tv_pk_mul_f16 v54, v46, v78 op_sel:[1,1] op_sel_hi:[1,0] neg_lo:[1,0]\n\t" \
  "v_mfma_f32_16x16x32_f16 v[36:39], v[80:83], v[144:147], v[36:39]\n\tv_pk_mul_f16 v55, v47, v79 op_sel:[1,1] op_sel_hi:[1,0] neg_lo:[1,0]\n\t" \
  "v_pk_fma_f16 v44, v44, v76, v52 op_sel:[0,0,0] op_sel_hi:[0,1,1]\n\tv_pk_fma_f16 v45, v45, v77, v53 op_sel:[0,0,0] op_sel_hi:[0,1,1]\n\t" \
  "v_mfma_f32_16x16x32_f16 v[4:7], v[144:147], v[80:83], v[4:7]\n\tv_pk_fma_f16 v46, v46, v78, v54 op_sel:[0,0,0] op_sel_hi:[0,1,1]\n\t" \
  "v_pk_fma_f16 v47, v47, v79, v55 op_sel:[0,0,0] op_sel_hi:[0,1,1]\n\ts_waitcnt lgkmcnt(1)\n\t" \
  "v_mfma_f32_16x16x32_f16 v[60:63], v[116:119], v[84:87], v[60:63]\n\tds_read_b128 v[76:79], v12 offset:4608\n\t" \
  "v_pk_mul_f16 v52, v48, v80 op_sel:[1,1] op_sel_hi:[1,0] neg_lo:[1,0]\n\tv_pk_mul_f16 v53, v49, v81 op_sel:[1,1] op_sel_hi:[1,0] neg_lo:[1,0]\n\t" \
  "v_pk_mul_f16 v54, v50, v82 op_sel:[1,1] op_sel_hi:[1,0] neg_lo:[1,0]\n\tv_mfma_f32_16x16x32_f16 v[36:39], v[84:87], v[148:151], v[36:39]\n\t" \
  "v_pk_mul_f16 v55, v51, v83 op_sel:[1,1] op_sel_hi:[1,0] neg_lo:[1,0]\n\tv_pk_fma_f16 v48, v48, v80, v52 op_sel:[0,0,0] op_sel_hi:[0,1,1]\n\t" \
  "v_pk_fma_f16 v49, v49, v81, v53 op_sel:[0,0,0] op_sel_hi:[0,1,1]\n\tv_mfma_f32_16x16x32_f16 v[4:7], v[148:151], v[84:87], v[4:7]\n\t" \
  "v_pk_fma_f16 v50, v50, v82, v54 op_sel:[0,0,0] op_sel_hi:[0,1,1]\n\tv_pk_fma_f16 v51, v51, v83, v55 op_sel:[0,0,0] op_sel_hi:[0,1,1]\n\t" \
  "s_waitcnt lgkmcnt(1)\n\tv_mfma_f32_16x16x32_f16 v[60:63], v[120:123], v[88:91], v[60:63]\n\t" \
  "ds_read_b128 v[80:83], v12 offset:4672\n\tv_pk_mul_f16 v52, v72, v84 op_sel:[1,1] op_sel_hi:[1,0] neg_lo:[1,0]\n\t" \
  "v_pk_mul_f16 v53, v73, v85 op_sel:[1,1] op_sel_hi:[1,0] neg_lo:[1,0]\n\tv_pk_mul_f16 v54, v74, v86 op_sel:[1,1] op_sel_hi:[1,0] neg_lo:[1,0]\n\t" \
  "v_mfma_f32_16x16x32_f16 v[36:39], v[88:91], v[152:155], v[36:39]\n\tv_pk_mul_f16 v55, v75, v87 op_sel:[1,1] op_sel_hi:[1,0] neg_lo:[1,0]\n\t" \
  "v_pk_fma_f16 v72, v72, v84, v52 op_sel:[0,0,0] op_sel_hi:[0,1,1]\n\tv_pk_fma_f16 v73, v73, v85, v53 op_sel:[0,0,0] op_sel_hi:[0,1,1]\n\t" \
  "v_mfma_f32_16x16x32_f16 v[4:7], v[152:155], v[88:91], v[4:7]\n\tv_pk_fma_f16 v74, v74, v86, v54 op_sel:[0,0,0] op_sel_hi:[0,1,1]\n\t" \
  "v_pk_fma_f16 v75, v75, v87, v55 op_sel:[0,0,0] op_sel_hi:[0,1,1]\n\ts_waitcnt lgkmcnt(1)\n\t" \
  "v_mfma_f32_16x16x32_f16 v[60:63], v[124:127], v[92:95], v[60:63]\n\tds_read_b128 v[84:87], v12 offset:4736\n\t" \
  "v_pk_mul_f16 v52, v76, v88 op_sel:[1,1] op_sel_hi:[1,0] neg_lo:[1,0]\n\tv_pk_mul_f16 v53, v77, v89 op_sel:[1,1] op_sel_hi:[1,0] neg_lo:[1,0]\n\t" \
  "v_pk_mul_f16 v54, v78, v90 op_sel:[1,1] op_sel_hi:[1,0] neg_lo:[1,0]\n\tv_mfma_f32_16x16x32_f16 v[36:39], v[92:95], v[156:159], v[36:39]\n\t" \
  "v_pk_mul_f16 v55, v79, v91 op_sel:[1,1] op_sel_hi:[1,0] neg_lo:[1,0]\n\tv_pk_fma_f16 v76, v76, v88, v52 op_sel:[0,0,0] op_sel_hi:[0,1,1]\n\t" \
  "v_pk_fma_f16 v77, v77, v89, v53 op_sel:[0,0,0] op_sel_hi:[0,1,1]\n\tv_mfma_f32_16x16x32_f16 v[4:7], v[156:159], v[92:95], v[4:7]\n\t" \
  "v_pk_fma_f16 v78, v78, v90, v54 op_sel:[0,0,0] op_sel_hi:[0,1,1]\n\tv_pk_fma_f16 v79, v79, v91, v55 op_sel:[0,0,0] op_sel_hi:[0,1,1]\n\t" \
  "s_waitcnt lgkmcnt(1)\n\tv_mfma_f32_16x16x32_f16 v[60:63], v[128:131], v[96:99], v[60:63]\n\t" \
  "ds_read_b128 v[88:91], v12 offset:4800\n\tv_pk_mul_f16 v52, v80, v92 op_sel:[1,1] op_sel_hi:[1,0] neg_lo:[1,0]\n\t" \
  "v_pk_mul_f16 v53, v81, v93 op_sel:[1,1] op_sel_hi:[1,0] neg_lo:[1,0]\n\tv_pk_mul_f16 v54, v82, v94 op_sel:[1,1] op_sel_hi:[1,0] neg_lo:[1,0]\n\t" \
  "v_mfma_f32_16x16x32_f16 v[36:39], v[96:99], v[160:163], v[36:39]\n\tv_pk_mul_f16 v55, v83, v95 op_sel:[1,1] op_sel_hi:[1,0] neg_lo:[1,0]\n\t" \
  "v_pk_fma_f16 v80, v80, v92, v52 op_sel:[0,0,0] op_sel_hi:[0,1,1]\n\tv_pk_fma_f16 v81, v81, v93, v53 op_sel:[0,0,0] op_sel_hi:[0,1,1]\n\t" \
  "v_mfma_f32_16x16x32_f16 v[4:7], v[160:163], v[96:99], v[4:7]\n\tv_pk_fma_f16 v82, v82, v94, v54 op_sel:[0,0,0] op_sel_hi:[0,1,1]\n\t" \
  "v_pk_fma_f16 v83, v83, v95, v55 op_sel:[0,0,0] op_sel_hi:[0,1,1]\n\ts_waitcnt lgkmcnt(1)\n\t" \
  "v_mfma_f32_16x16x32_f16 v[60:63], v[132:135], v[100:103], v[60:63]\n\tv_pk_mul_f16 v52, v84, v96 op_sel:[1,1] op_sel_hi:[1,0] neg_lo:[1,0]\n\t" \
  "v_pk_mul_f16 v53, v85, v97 op_sel:[1,1] op_sel_hi:[1,0] neg_lo:[1,0]\n\tv_pk_mul_f16 v54, v86, v98 op_sel:[1,1] op_sel_hi:[1,0] neg_lo:[1,0]\n\t" \
  "v_mfma_f32_16x16x32_f16 v[36:39], v[100:103], v[164:167], v[36:39]\n\tv_pk_mul_f16 v55, v87, v99 op_sel:[1,1] op_sel_hi:[1,0] neg_lo:[1,0]\n\t" \
  "v_pk_fma_f16 v84, v84, v96, v52 op_sel:[0,0,0] op_sel_hi:[0,1,1]\n\tv_pk_fma_f16 v85, v85, v97, v53 op_sel:[0,0,0] op_sel_hi:[0,1,1]\n\t" \
  "v_mfma_f32_16x16x32_f16 v[4:7], v[164:167], v[100:103], v[4:7]\n\tv_pk_fma_f16 v86, v86, v98, v54 op_sel:[0,0,0] op_sel_hi:[0,1,1]\n\t" \
  "v_pk_fma_f16 v87, v87, v99, v55 op_sel:[0,0,0] op_sel_hi:[0,1,1]\n\ts_waitcnt lgkmcnt(0)\n\t" \
  "v_mfma_f32_16x16x32_f16 v[64:67], v[104:107], v[40:43], 0\n\tv_pk_mul_f16 v52, v88, v100 op_sel:[1,1] op_sel_hi:[1,0] neg_lo:[1,0]\n\t" \
  "v_pk_mul_f16 v53, v89, v101 op_sel:[1,1] op_sel_hi:[1,0] neg_lo:[1,0]\n\tv_pk_mul_f16 v54, v90, v102 op_sel:[1,1] op_sel_hi:[1,0] neg_lo:[1,0]\n\t" \
  "v_mfma_f32_16x16x32_f16 v[68:71], v[40:43], v[136:139], 0\n\tv_pk_mul_f16 v55, v91, v103 op_sel:[1,1] op_sel_hi:[1,0] neg_lo:[1,0]\n\t" \
  "v_pk_fma_f16 v88, v88, v100, v52 op_sel:[0,0,0] op_sel_hi:[0,1,1]\n\tv_pk_fma_f16 v89, v89, v101, v53 op_sel:[0,0,0] op_sel_hi:[0,1,1]\n\t" \
  "v_mfma_f32_16x16x32_f16 v[8:11], v[136:139], v[40:43], 0\n\tv_pk_fma_f16 v90, v90, v102, v54 op_sel:[0,0,0] op_sel_hi:[0,1,1]\n\t" \
  "v_pk_fma_f16 v91, v91, v103, v55 op_sel:[0,0,0] op_sel_hi:[0,1,1]\n\tv_mov_b64 v[100:101], 0\n\t" \
  "v_mfma_f32_16x16x32_f16 v[64:67], v[108:111], v[44:47], v[64:67]\n\tv_mov_b64 v[102:103], 0\n\t" \
  "v_cvt_pk_f16_f32 v94, v36, v37\n\tv_cvt_pk_f16_f32 v95, v38, v39\n\t" \
  "v_mfma_f32_16x16x32_f16 v[68:71], v[44:47], v[140:143], v[68:71]\n\tv_cvt_pk_f16_f32 v4, v4, v5\n\t" \
  "v_cvt_pk_f16_f32 v5, v6, v7\n\tv_mov_b32_dpp v94, v94 row_shl:4 row_mask:0xf bank_mask:0x5\n\t" \
  "v_mfma_f32_16x16x32_f16 v[8:11], v[140:143], v[44:47], v[8:11]\n\tv_mov_b32_dpp v95, v95 row_shl:4 row_mask:0xf bank_mask:0x5\n\t" \
  "v_permlane16_swap_b32 v4, v5\n\tv_cvt_pk_f16_f32 v92, v60, v61\n\t" \
  "v_mfma_f32_16x16x32_f16 v[64:67], v[112:115], v[48:51], v[64:67]\n\tv_cvt_pk_f16_f32 v93, v62, v63\n\t" \
  "v_alignbit_b32 v96, v95, v94, 16\n\tv_perm_b32 v6, v4, v4, v33\n\t" \
  "v_mfma_f32_16x16x32_f16 v[68:71], v[48:51], v[144:147], v[68:71]\n\tv_perm_b32 v7, v4, v4, v34\n\t" \
  "v_mfma_f32_16x16x32_f16 v[8:11], v[144:147], v[48:51], v[8:11]\n\tds_write_b128 v58, v[100:103]\n\t" \
  "v_mfma_f32_16x16x32_f16 v[64:67], v[116:119], v[72:75], v[64:67]\n\tds_write2_b32 v56, v92, v93 offset0:193 offset1:194\n\t" \
  "v_mfma_f32_16x16x32_f16 v[68:71], v[72:75], v[148:151], v[68:71]\n\tds_write2_b32 v57, v92, v93 offset0:193 offset1:194\n\t" \
  "v_mfma_f32_16x16x32_f16 v[8:11], v[148:151], v[72:75], v[8:11]\n\tds_write2_b32 v14, v6, v7 offset0:64 offset1:65\n\t" \
  "v_mfma_f32_16x16x32_f16 v[64:67], v[120:123], v[76:79], v[64:67]\n\tds_write2_b32 v15, v6, v7 offset0:64 offset1:65\n\t" \
  "v_mfma_f32_16x16x32_f16 v[68:71], v[76:79], v[152:155], v[68:71]\n\tds_write2_b32 v59, v94, v95 offset0:4 offset1:5\n\t" \
  "v_mfma_f32_16x16x32_f16 v[8:11], v[152:155], v[76:79], v[8:11]\n\tds_write_b16 v59, v94 offset:846\n\t" \
  "v_mfma_f32_16x16x32_f16 v[64:67], v[124:127], v[80:83], v[64:67]\n\tds_write_b32 v59, v96 offset:848\n\t" \
  "v_mfma_f32_16x16x32_f16 v[68:71], v[80:83], v[156:159], v[68:71]\n\tds_write_b16_d16_hi v59, v95 offset:852\n\t" \
  "v_mfma_f32_16x16x32_f16 v[8:11], v[156:159], v[80:83], v[8:11]\n\tv_mfma_f32_16x16x32_f16 v[64:67], v[128:131], v[84:87], v[64:67]\n\t" \
  "v_mfma_f32_16x16x32_f16 v[68:71], v[84:87], v[160:163], v[68:71]\n\tv_mfma_f32_16x16x32_f16 v[8:11], v[160:163], v[84:87], v[8:11]\n\t" \
  "v_mfma_f32_16x16x32_f16 v[68:71], v[88:91], v[164:167], v[68:71]\n\tv_mfma_f32_16x16x32_f16 v[8:11], v[164:167], v[88:91], v[8:11]\n\t" \
  "v_mfma_f32_16x16x32_f16 v[64:67], v[132:135], v[88:91], v[64:67]\n\ts_nop 5\n\t" \
  "v_cvt_pk_f16_f32 v48, v68, v69\n\tv_cvt_pk_f16_f32 v49, v70, v71\n\t" \
  "v_cvt_pk_f16_f32 v8, v8, v9\n\tv_cvt_pk_f16_f32 v9, v10, v11\n\t" \
  "v_mov_b32_dpp v48, v48 row_shl:4 row_mask:0xf bank_mask:0x5\n\tv_mov_b32_dpp v49, v49 row_shl:4 row_mask:0xf bank_mask:0x5\n\t" \
  "v_permlane16_swap_b32 v8, v9\n\tv_cvt_pk_f16_f32 v46, v64, v65\n\t" \
  "v_cvt_pk_f16_f32 v47, v66, v67\n\tv_alignbit_b32 v50, v49, v48, 16\n\t" \
  "v_perm_b32 v10, v8, v8, v33\n\tv_perm_b32 v11, v8, v8, v34\n\t" \
  "ds_write2_b32 v56, v46, v47 offset0:129 offset1:130\n\tds_write2_b32 v57, v46, v47 offset0:129 offset1:130\n\t" \
  "ds_write2_b32 v14, v10, v11 offset0:0 offset1:1\n\tds_write2_b32 v15, v10, v11 offset0:0 offset1:1\n\t" \
  "ds_write2_b32 v59, v48, v49 offset0:0 offset1:1\n\tds_write_b16 v59, v48 offset:830\n\t" \
  "ds_write_b32 v59, v50 offset:832\n\tds_write_b16_d16_hi v59, v49 offset:836\n\t"
#define GEN_CLOBBER "v4", "v5", "v6", "v7", "v9", "v10", "v11", "v13", "v14", "v15", "v36", "v37", "v38", "v39", "v40", "v41", "v42", "v43", "v44", "v45", "v47", "v49", "v50", "v51", "v52", "v53", "v54", "v55", "v56", "v57", "v58", "v59", "v60", "v61", "v62", "v63", "v64", "v65", "v66", "v67", "v72", "v73", "v74", "v75", "v76", "v77", "v78", "v79", "v80", "v81", "v82", "v83", "v84", "v85", "v86", "v87", "v88", "v89", "v90", "v91", "v92", "v94", "v95", "v98", "v99", "v100", "v101", "v102", "v103", "v104", "v105", "v106", "v107", "v108", "v109", "v110", "v111", "v112", "v113", "v114", "v115", "v116", "v117", "v118", "v119", "v120", "v121", "v122", "v123", "v124", "v125", "v126", "v127", "v128", "v129", "v130", "v131", "v132", "v133", "v134", "v135", "v136", "v137", "v138", "v139", "v140", "v141", "v142", "v143", "v144", "v145", "v146", "v147", "v148", "v149", "v150", "v151", "v152", "v153", "v154", "v155", "v156", "v157", "v158", "v159", "v160", "v161", "v162", "v163", "v164", "v165", "v166", "v167"
#define CONV_ASM \
  "ds_read_b32 v34, v30\n\tds_read_b64 v[132:133], v26 offset:512\n\tds_read_b64 v[134:135], v26 offset:520\n\t" \
  "ds_read_b64 v[100:101], v26 offset:0\n\tds_read_b64 v[102:103], v26 offset:8\n\tds_read_b64 v[136:137], v26 offset:576\n\t" \
  "ds_read_b64 v[138:139], v26 offset:584\n\tds_read_b64 v[104:105], v26 offset:64\n\tds_read_b64 v[106:107], v26 offset:72\n\t" \
  "ds_read_b64 v[140:141], v26 offset:640\n\tds_read_b64 v[142:143], v26 offset:648\n\tds_read_b64 v[108:109], v26 offset:128\n\t" \
  "ds_read_b64 v[110:111], v26 offset:136\n\tds_read_b64 v[144:145], v26 offset:704\n\tds_read_b64 v[146:147], v26 offset:712\n\t" \
  "ds_read_b64 v[112:113], v26 offset:192\n\tds_read_b64 v[114:115], v26 offset:200\n\tds_read_b64 v[148:149], v26 offset:768\n\t" \
  "ds_read_b64 v[150:151], v26 offset:776\n\tds_read_b64 v[116:117], v26 offset:256\n\tds_read_b64 v[118:119], v26 offset:264\n\t" \
  TS(60, 61) "s_waitcnt lgkmcnt(15)\n\tv_cmp_gt_u32 vcc, 1, v34\n\t" \
  "s_cbranch_vccz 2f\n\ts_mov_b32 s6, 0x10000\n\t1:\n\t" \
  "s_sleep 1\n\tds_read_b32 v34, v30\n\ts_sub_u32 s6, s6, 1\n\t" \
  "s_waitcnt lgkmcnt(0)\n\tv_cmp_gt_u32 vcc, 1, v34\n\ts_cbranch_scc1 2f\n\t" \
  "s_cbranch_vccnz 1b\n\t2:\n\t" TS(62, 63) \
  "s_cmp_lt_u32 %[w], 4\n\ts_setprio 1\n\ts_cbranch_scc1 1f\n\t" \
  "s_setprio 2\n\t1:\n\tds_read_b128 v[0:3], v28 offset:0\n\t" \
  "ds_read_b128 v[4:7], v28 offset:64\n\tds_read_b128 v[8:11], v28 offset:128\n\tds_read_b128 v[12:15], v28 offset:192\n\t" \
  "ds_read_b128 v[16:19], v28 offset:256\n\tds_read_b128 v[20:23], v28 offset:320\n\ts_waitcnt lgkmcnt(5)\n\t" \
  "v_mfma_f32_16x16x32_f16 v[52:55], v[132:135], v[0:3], 0\n\tds_read_b64 v[152:153], v26 offset:832\n\tds_read_b64 v[154:155], v26 offset:840\n\t" \
  "v_mfma_f32_16x16x32_f16 v[56:59], v[100:103], v[0:3], 0\n\tds_read_b64 v[120:121], v26 offset:320\n\tds_read_b64 v[122:123], v26 offset:328\n\t" \
  "ds_read_b128 v[0:3], v28 offset:384\n\ts_waitcnt lgkmcnt(9)\n\tv_mfma_f32_16x16x32_f16 v[52:55], v[136:139], v[4:7], v[52:55]\n\t" \
  "ds_read_b64 v[156:157], v26 offset:896\n\tds_read_b64 v[158:159], v26 offset:904\n\tv_mfma_f32_16x16x32_f16 v[56:59], v[104:107], v[4:7], v[56:59]\n\t" \
  "ds_read_b64 v[124:125], v26 offset:384\n\tds_read_b64 v[126:127], v26 offset:392\n\tds_read_b128 v[4:7], v28 offset:448\n\t" \
  "s_waitcnt lgkmcnt(13)\n\tv_mfma_f32_16x16x32_f16 v[52:55], v[140:143], v[8:11], v[52:55]\n\tds_read_b64 v[160:161], v26 offset:960\n\t" \
  "ds_read_b64 v[162:163], v26 offset:968\n\tv_mfma_f32_16x16x32_f16 v[56:59], v[108:111], v[8:11], v[56:59]\n\tds_read_b64 v[128:129], v26 offset:448\n\t" \
  "ds_read_b64 v[130:131], v26 offset:456\n\tds_read_b128 v[8:11], v28 offset:512\n\ts_waitcnt lgkmcnt(15)\n\t" \
  "v_mfma_f32_16x16x32_f16 v[52:55], v[144:147], v[12:15], v[52:55]\n\tds_read_b64 v[164:165], v26 offset:1024\n\tds_read_b64 v[166:167], v26 offset:1032\n\t" \
  "v_mfma_f32_16x16x32_f16 v[56:59], v[112:115], v[12:15], v[56:59]\n\tds_read_b64 v[76:77], v31 offset:72\n\tds_read_b64 v[78:79], v31 offset:176\n\t" \
  "ds_read_b128 v[12:15], v28 offset:576\n\tv_mfma_f32_16x16x32_f16 v[52:55], v[148:151], v[16:19], v[52:55]\n\tds_read_b64 v[80:81], v31 offset:40\n\t" \
  "ds_read_b64 v[82:83], v31 offset:144\n\tv_mfma_f32_16x16x32_f16 v[56:59], v[116:119], v[16:19], v[56:59]\n\tds_read_b64 v[84:85], v31 offset:8\n\t" \
  "ds_read_b64 v[86:87], v31 offset:112\n\tds_read_b128 v[16:19], v28 offset:640\n\ts_waitcnt lgkmcnt(15)\n\t" \
  "v_mfma_f32_16x16x32_f16 v[52:55], v[152:155], v[20:23], v[52:55]\n\tds_read_b64 v[88:89], v31 offset:280\n\tds_read_b64 v[90:91], v31 offset:384\n\t" \
  "v_mfma_f32_16x16x32_f16 v[56:59], v[120:123], v[20:23], v[56:59]\n\tds_read_b64 v[92:93], v31 offset:248\n\tds_read_b64 v[94:95], v31 offset:352\n\t" \
  "ds_read_b128 v[20:23], v28 offset:704\n\tv_mfma_f32_16x16x32_f16 v[52:55], v[156:159], v[0:3], v[52:55]\n\tds_read_b64 v[96:97], v31 offset:216\n\t" \
  "ds_read_b64 v[98:99], v31 offset:320\n\tv_mfma_f32_16x16x32_f16 v[56:59], v[124:127], v[0:3], v[56:59]\n\tds_read_b128 v[0:3], v28 offset:768\n\t" \
  "s_waitcnt lgkmcnt(15)\n\tv_mfma_f32_16x16x32_f16 v[52:55], v[160:163], v[4:7], v[52:55]\n\tv_mfma_f32_16x16x32_f16 v[56:59], v[128:131], v[4:7], v[56:59]\n\t" \
  "ds_read_b128 v[4:7], v28 offset:832\n\tv_mfma_f32_16x16x32_f16 v[52:55], v[164:167], v[8:11], v[52:55]\n\tv_mov_b32 v33, 1\n\t" \
  "v_mfma_f32_16x16x32_f16 v[56:59], v[132:135], v[8:11], v[56:59]\n\tv_mfma_f32_16x16x32_f16 v[60:63], v[100:103], v[8:11], 0\n\tds_read_b128 v[8:11], v28 offset:896\n\t" \
  "s_waitcnt lgkmcnt(15)\n\tv_mfma_f32_16x16x32_f16 v[56:59], v[136:139], v[12:15], v[56:59]\n\tds_read_b32 v34, v30\n\t" \
  "v_mfma_f32_16x16x32_f16 v[60:63], v[104:107], v[12:15], v[60:63]\n\tds_read_b128 v[12:15], v28 offset:960\n\tv_cvt_pk_bf16_f32 v52, v52, v53\n\t" \
  "v_cvt_pk_bf16_f32 v53, v54, v55\n\t" TS(64, 65) "s_waitcnt lgkmcnt(12)\n\t" \
  "v_mfma_f32_16x16x32_f16 v[56:59], v[140:143], v[16:19], v[56:59]\n\tds_write_b64 v29, v[52:53] offset:0\n\tds_write_b32 v25, v33\n\t" \
  "v_mfma_f32_16x16x32_f16 v[60:63], v[108:111], v[16:19], v[60:63]\n\ts_waitcnt lgkmcnt(3)\n\tv_cmp_gt_u32 vcc, 2, v34\n\t" \
  "s_cbranch_vccz 2f\n\ts_mov_b32 s6, 0x10000\n\t1:\n\t" \
  "s_sleep 1\n\tds_read_b32 v34, v30\n\ts_sub_u32 s6, s6, 1\n\t" \
  "s_waitcnt lgkmcnt(0)\n\tv_cmp_gt_u32 vcc, 2, v34\n\ts_cbranch_scc1 2f\n\t" \
  "s_cbranch_vccnz 1b\n\t2:\n\tds_read_b128 v[16:19], v28 offset:1024\n\t" \
  "v_mfma_f32_16x16x32_f16 v[56:59], v[144:147], v[20:23], v[56:59]\n\tv_mfma_f32_16x16x32_f16 v[60:63], v[112:115], v[20:23], v[60:63]\n\tds_read_b128 v[68:71], v32\n\t" \
  "ds_read_b128 v[72:75], v32 offset:1024\n\tds_read_b128 v[20:23], v28 offset:1088\n\tv_mfma_f32_16x16x32_f16 v[56:59], v[148:151], v[0:3], v[56:59]\n\t" \
  "v_mfma_f32_16x16x32_f16 v[60:63], v[116:119], v[0:3], v[60:63]\n\tds_read_b128 v[0:3], v28 offset:496\n\tv_mfma_f32_16x16x32_f16 v[56:59], v[152:155], v[4:7], v[56:59]\n\t" \
  "v_mfma_f32_16x16x32_f16 v[60:63], v[120:123], v[4:7], v[60:63]\n\tds_read_b128 v[4:7], v28 offset:1152\n\tv_mfma_f32_16x16x32_f16 v[56:59], v[156:159], v[8:11], v[56:59]\n\t" \
  "v_mfma_f32_16x16x32_f16 v[60:63], v[124:127], v[8:11], v[60:63]\n\tds_read_b128 v[8:11], v28 offset:1216\n\ts_waitcnt lgkmcnt(9)\n\t" \
  "v_mfma_f32_16x16x32_f16 v[56:59], v[160:163], v[12:15], v[56:59]\n\tv_mfma_f32_16x16x32_f16 v[60:63], v[128:131], v[12:15], v[60:63]\n\tds_read_b128 v[12:15], v28 offset:480\n\t" \
  "s_waitcnt lgkmcnt(7)\n\tv_mfma_f32_16x16x32_f16 v[56:59], v[164:167], v[16:19], v[56:59]\n\tv_mov_b32 v33, 2\n\t" \
  "v_mfma_f32_16x16x32_f16 v[60:63], v[132:135], v[16:19], v[60:63]\n\tv_mfma_f32_16x16x32_f16 v[64:67], v[100:103], v[16:19], 0\n\tds_read_b128 v[16:19], v28 offset:1280\n\t" \
  "s_waitcnt lgkmcnt(5)\n\tv_mfma_f32_16x16x32_f16 v[60:63], v[136:139], v[20:23], v[60:63]\n\tv_mfma_f32_16x16x32_f16 v[64:67], v[104:107], v[20:23], v[64:67]\n\t" \
  "ds_read_b128 v[20:23], v28 offset:1344\n\tv_cvt_pk_bf16_f32 v56, v56, v57\n\tv_cvt_pk_bf16_f32 v57, v58, v59\n\t" \
  "s_waitcnt lgkmcnt(5)\n\tv_mfma_f32_16x16x32_f16 v[36:39], v[0:3], v[68:71], 0\n\tds_write_b64 v29, v[56:57] offset:512\n\t" \
  "ds_write_b32 v25, v33\n\tv_mfma_f32_16x16x32_f16 v[40:43], v[0:3], v[72:75], 0\n\tds_read_b128 v[0:3], v28 offset:1408\n\t" \
  "s_waitcnt lgkmcnt(7)\n\tv_mfma_f32_16x16x32_f16 v[60:63], v[140:143], v[4:7], v[60:63]\n\tv_mfma_f32_16x16x32_f16 v[64:67], v[108:111], v[4:7], v[64:67]\n\t" \
  "ds_read_b128 v[4:7], v28 offset:1472\n\tv_cvt_pk_f16_f32 v36, v36, v37\n\tv_cvt_pk_f16_f32 v37, v38, v39\n\t" \
  "s_waitcnt lgkmcnt(7)\n\tv_mfma_f32_16x16x32_f16 v[60:63], v[144:147], v[8:11], v[60:63]\n\tv_cvt_pk_f16_f32 v38, v40, v41\n\t" \
  "v_cvt_pk_f16_f32 v39, v42, v43\n\tv_mfma_f32_16x16x32_f16 v[64:67], v[112:115], v[8:11], v[64:67]\n\tds_read_b128 v[8:11], v28 offset:1536\n\t" \
  "v_mfma_f32_16x16x32_f16 v[60:63], v[36:39], v[76:79], v[60:63]\n\tv_mfma_f32_16x16x32_f16 v[64:67], v[36:39], v[80:83], v[64:67]\n\tv_mfma_f32_16x16x32_f16 v[52:55], v[36:39], v[84:87], 0\n\t" \
  "s_waitcnt lgkmcnt(7)\n\tv_mfma_f32_16x16x32_f16 v[44:47], v[12:15], v[68:71], 0\n\tv_mfma_f32_16x16x32_f16 v[40:43], v[12:15], v[72:75], 0\n\t" \
  "ds_read_b128 v[12:15], v28 offset:1600\n\ts_waitcnt lgkmcnt(7)\n\tv_mfma_f32_16x16x32_f16 v[60:63], v[148:151], v[16:19], v[60:63]\n\t" \
  "v_mfma_f32_16x16x32_f16 v[64:67], v[116:119], v[16:19], v[64:67]\n\tds_read_b128 v[16:19], v28 offset:1008\n\ts_waitcnt lgkmcnt(7)\n\t" \
  "v_mfma_f32_16x16x32_f16 v[60:63], v[152:155], v[20:23], v[60:63]\n\tv_cvt_pk_f16_f32 v44, v44, v45\n\tv_cvt_pk_f16_f32 v45, v46, v47\n\t" \
  "v_cvt_pk_f16_f32 v46, v40, v41\n\tv_cvt_pk_f16_f32 v47, v42, v43\n\tv_mfma_f32_16x16x32_f16 v[64:67], v[120:123], v[20:23], v[64:67]\n\t" \
  "ds_read_b128 v[20:23], v28 offset:1664\n\tv_mfma_f32_16x16x32_f16 v[60:63], v[44:47], v[88:91], v[60:63]\n\tv_mfma_f32_16x16x32_f16 v[64:67], v[44:47], v[92:95], v[64:67]\n\t" \
  "v_mfma_f32_16x16x32_f16 v[52:55], v[44:47], v[96:99], v[52:55]\n\ts_waitcnt lgkmcnt(5)\n\tv_mfma_f32_16x16x32_f16 v[60:63], v[156:159], v[0:3], v[60:63]\n\t" \
  "v_mfma_f32_16x16x32_f16 v[64:67], v[124:127], v[0:3], v[64:67]\n\tds_read_b128 v[0:3], v28 offset:1728\n\ts_waitcnt lgkmcnt(5)\n\t" \
  "v_mfma_f32_16x16x32_f16 v[60:63], v[160:163], v[4:7], v[60:63]\n\tv_mfma_f32_16x16x32_f16 v[64:67], v[128:131], v[4:7], v[64:67]\n\tds_read_b128 v[4:7], v28 offset:992\n\t" \
  "s_waitcnt lgkmcnt(5)\n\tv_mfma_f32_16x16x32_f16 v[60:63], v[164:167], v[8:11], v[60:63]\n\tv_mov_b32 v33, 3\n\t" \
  "v_mfma_f32_16x16x32_f16 v[64:67], v[132:135], v[8:11], v[64:67]\n\tv_mfma_f32_16x16x32_f16 v[52:55], v[100:103], v[8:11], v[52:55]\n\tds_read_b128 v[8:11], v28 offset:1792\n\t" \
  "s_waitcnt lgkmcnt(5)\n\tv_mfma_f32_16x16x32_f16 v[64:67], v[136:139], v[12:15], v[64:67]\n\tv_mfma_f32_16x16x32_f16 v[52:55], v[104:107], v[12:15], v[52:55]\n\t" \
  "ds_read_b128 v[12:15], v28 offset:1856\n\tv_cvt_pk_bf16_f32 v60, v60, v61\n\tv_cvt_pk_bf16_f32 v61, v62, v63\n\t" \
  "s_cmp_lt_u32 %[w], 4\n\ts_cbranch_scc1 1f\n\ts_setprio 1\n\t" \
  "1:\n\ts_waitcnt lgkmcnt(5)\n\tv_mfma_f32_16x16x32_f16 v[36:39], v[16:19], v[68:71], 0\n\t" \
  "ds_write_b64 v29, v[60:61] offset:1024\n\tds_write_b32 v25, v33\n\tv_mfma_f32_16x16x32_f16 v[40:43], v[16:19], v[72:75], 0\n\t" \
  "ds_read_b128 v[16:19], v28 offset:1920\n\ts_waitcnt lgkmcnt(7)\n\tv_mfma_f32_16x16x32_f16 v[64:67], v[140:143], v[20:23], v[64:67]\n\t" \
  "ds_read_b32 v34, v30\n\tv_mfma_f32_16x16x32_f16 v[52:55], v[108:111], v[20:23], v[52:55]\n\tds_read_b128 v[20:23], v28 offset:1984\n\t" \
  "v_cvt_pk_f16_f32 v36, v36, v37\n\tv_cvt_pk_f16_f32 v37, v38, v39\n\ts_waitcnt lgkmcnt(8)\n\t" \
  "v_mfma_f32_16x16x32_f16 v[64:67], v[144:147], v[0:3], v[64:67]\n\tv_cvt_pk_f16_f32 v38, v40, v41\n\tv_cvt_pk_f16_f32 v39, v42, v43\n\t" \
  "v_mfma_f32_16x16x32_f16 v[52:55], v[112:115], v[0:3], v[52:55]\n\ts_waitcnt lgkmcnt(1)\n\tv_cmp_gt_u32 vcc, 3, v34\n\t" \
  "s_cbranch_vccz 2f\n\ts_mov_b32 s6, 0x10000\n\t1:\n\t" \
  "s_sleep 1\n\tds_read_b32 v34, v30\n\ts_sub_u32 s6, s6, 1\n\t" \
  "s_waitcnt lgkmcnt(0)\n\tv_cmp_gt_u32 vcc, 3, v34\n\ts_cbranch_scc1 2f\n\t" \
  "s_cbranch_vccnz 1b\n\t2:\n\tds_read_b128 v[0:3], v28 offset:2048\n\t" \
  "v_mfma_f32_16x16x32_f16 v[64:67], v[36:39], v[76:79], v[64:67]\n\tv_mfma_f32_16x16x32_f16 v[52:55], v[36:39], v[80:83], v[52:55]\n\tv_mfma_f32_16x16x32_f16 v[56:59], v[36:39], v[84:87], 0\n\t" \
  "v_mfma_f32_16x16x32_f16 v[44:47], v[4:7], v[68:71], 0\n\tv_mfma_f32_16x16x32_f16 v[40:43], v[4:7], v[72:75], 0\n\tds_read_b128 v[4:7], v28 offset:2112\n\t" \
  "v_mfma_f32_16x16x32_f16 v[64:67], v[148:151], v[8:11], v[64:67]\n\tv_mfma_f32_16x16x32_f16 v[52:55], v[116:119], v[8:11], v[52:55]\n\tds_read_b128 v[8:11], v28 offset:1520\n\t" \
  "v_mfma_f32_16x16x32_f16 v[64:67], v[152:155], v[12:15], v[64:67]\n\tv_mfma_f32_16x16x32_f16 v[52:55], v[120:123], v[12:15], v[52:55]\n\tds_read_b128 v[12:15], v28 offset:2176\n\t" \
  "v_cvt_pk_f16_f32 v44, v44, v45\n\tv_cvt_pk_f16_f32 v45, v46, v47\n\tv_cvt_pk_f16_f32 v46, v40, v41\n\t" \
  "v_cvt_pk_f16_f32 v47, v42, v43\n\ts_nop 1\n\tv_mfma_f32_16x16x32_f16 v[64:67], v[44:47], v[88:91], v[64:67]\n\t" \
  "v_mfma_f32_16x16x32_f16 v[52:55], v[44:47], v[92:95], v[52:55]\n\tv_mfma_f32_16x16x32_f16 v[56:59], v[44:47], v[96:99], v[56:59]\n\tv_mfma_f32_16x16x32_f16 v[64:67], v[156:159], v[16:19], v[64:67]\n\t" \
  "v_mfma_f32_16x16x32_f16 v[52:55], v[124:127], v[16:19], v[52:55]\n\tds_read_b128 v[16:19], v28 offset:2240\n\ts_waitcnt lgkmcnt(5)\n\t" \
  "v_mfma_f32_16x16x32_f16 v[64:67], v[160:163], v[20:23], v[64:67]\n\tv_mfma_f32_16x16x32_f16 v[52:55], v[128:131], v[20:23], v[52:55]\n\tds_read_b128 v[20:23], v28 offset:1504\n\t" \
  "s_waitcnt lgkmcnt(5)\n\tv_mfma_f32_16x16x32_f16 v[64:67], v[164:167], v[0:3], v[64:67]\n\tv_mov_b32 v33, 4\n\t" \
  "v_mfma_f32_16x16x32_f16 v[52:55], v[132:135], v[0:3], v[52:55]\n\tv_mfma_f32_16x16x32_f16 v[56:59], v[100:103], v[0:3], v[56:59]\n\tds_read_b128 v[0:3], v28 offset:2304\n\t" \
  "s_waitcnt lgkmcnt(5)\n\tv_mfma_f32_16x16x32_f16 v[52:55], v[136:139], v[4:7], v[52:55]\n\tv_mfma_f32_16x16x32_f16 v[56:59], v[104:107], v[4:7], v[56:59]\n\t" \
  "ds_read_b128 v[4:7], v28 offset:2368\n\tv_cvt_pk_bf16_f32 v64, v64, v65\n\tv_cvt_pk_bf16_f32 v65, v66, v67\n\t" \
  TS(66, 67) "s_waitcnt lgkmcnt(5)\n\tv_mfma_f32_16x16x32_f16 v[36:39], v[8:11], v[68:71], 0\n\t" \
  "ds_write_b64 v29, v[64:65] offset:1536\n\tds_write_b32 v25, v33\n\tv_mfma_f32_16x16x32_f16 v[40:43], v[8:11], v[72:75], 0\n\t" \
  "ds_read_b128 v[8:11], v28 offset:480\n\ts_waitcnt lgkmcnt(7)\n\tv_mfma_f32_16x16x32_f16 v[52:55], v[140:143], v[12:15], v[52:55]\n\t" \
  "v_mfma_f32_16x16x32_f16 v[56:59], v[108:111], v[12:15], v[56:59]\n\tds_read_b128 v[12:15], v32 offset:3072\n\tv_cvt_pk_f16_f32 v36, v36, v37\n\t" \
  "v_cvt_pk_f16_f32 v37, v38, v39\n\ts_waitcnt lgkmcnt(7)\n\tv_mfma_f32_16x16x32_f16 v[52:55], v[144:147], v[16:19], v[52:55]\n\t" \
  "v_cvt_pk_f16_f32 v38, v40, v41\n\tv_cvt_pk_f16_f32 v39, v42, v43\n\tv_mfma_f32_16x16x32_f16 v[56:59], v[112:115], v[16:19], v[56:59]\n\t" \
  "ds_read_b128 v[16:19], v28 offset:496\n\tv_mfma_f32_16x16x32_f16 v[52:55], v[36:39], v[76:79], v[52:55]\n\tv_mfma_f32_16x16x32_f16 v[56:59], v[36:39], v[80:83], v[56:59]\n\t" \
  "v_mfma_f32_16x16x32_f16 v[60:63], v[36:39], v[84:87], 0\n\ts_waitcnt lgkmcnt(7)\n\tv_mfma_f32_16x16x32_f16 v[44:47], v[20:23], v[68:71], 0\n\t" \
  "v_mfma_f32_16x16x32_f16 v[40:43], v[20:23], v[72:75], 0\n\tds_read_b128 v[20:23], v32 offset:2048\n\ts_waitcnt lgkmcnt(7)\n\t" \
  "v_mfma_f32_16x16x32_f16 v[52:55], v[148:151], v[0:3], v[52:55]\n\tv_mfma_f32_16x16x32_f16 v[56:59], v[116:119], v[0:3], v[56:59]\n\tds_read_b128 v[0:3], v28 offset:2432\n\t" \
  "s_waitcnt lgkmcnt(7)\n\tv_mfma_f32_16x16x32_f16 v[52:55], v[152:155], v[4:7], v[52:55]\n\tv_cvt_pk_f16_f32 v44, v44, v45\n\t" \
  "v_cvt_pk_f16_f32 v45, v46, v47\n\tv_cvt_pk_f16_f32 v46, v40, v41\n\tv_cvt_pk_f16_f32 v47, v42, v43\n\t" \
  "v_mfma_f32_16x16x32_f16 v[56:59], v[120:123], v[4:7], v[56:59]\n\tds_read_b128 v[4:7], v28 offset:2496\n\tv_mfma_f32_16x16x32_f16 v[52:55], v[44:47], v[88:91], v[52:55]\n\t" \
  "v_mfma_f32_16x16x32_f16 v[56:59], v[44:47], v[92:95], v[56:59]\n\tv_mfma_f32_16x16x32_f16 v[60:63], v[44:47], v[96:99], v[60:63]\n\ts_waitcnt lgkmcnt(4)\n\t" \
  "v_mfma_f32_16x16x32_f16 v[40:43], v[8:11], v[12:15], 0\n\tds_read_b64 v[8:9], v31 offset:544\n\tds_read_b64 v[10:11], v31 offset:712\n\t" \
  "ds_read_b64 v[12:13], v31 offset:512\n\tds_read_b64 v[14:15], v31 offset:680\n\ts_waitcnt lgkmcnt(7)\n\t" \
  "v_mfma_f32_16x16x32_f16 v[52:55], v[48:51], v[16:19], v[52:55]\n\ts_waitcnt lgkmcnt(6)\n\tv_mfma_f32_16x16x32_f16 v[36:39], v[16:19], v[20:23], 0\n\t" \
  "ds_read_b64 v[16:17], v31 offset:480\n\tds_read_b64 v[18:19], v31 offset:648\n\tds_read_b64 v[20:21], v31 offset:448\n\t" \
  "ds_read_b64 v[22:23], v31 offset:616\n\ts_waitcnt lgkmcnt(9)\n\tv_mfma_f32_16x16x32_f16 v[52:55], v[156:159], v[0:3], v[52:55]\n\t" \
  "v_mfma_f32_16x16x32_f16 v[56:59], v[124:127], v[0:3], v[56:59]\n\tds_read_b128 v[0:3], v28 offset:2560\n\tv_cvt_pk_f16_f32 v36, v36, v37\n\t" \
  "v_cvt_pk_f16_f32 v37, v38, v39\n\ts_waitcnt lgkmcnt(9)\n\tv_mfma_f32_16x16x32_f16 v[52:55], v[160:163], v[4:7], v[52:55]\n\t" \
  "v_cvt_pk_f16_f32 v38, v40, v41\n\tv_cvt_pk_f16_f32 v39, v42, v43\n\tv_mfma_f32_16x16x32_f16 v[56:59], v[128:131], v[4:7], v[56:59]\n\t" \
  "ds_read_b128 v[4:7], v28 offset:2624\n\ts_waitcnt lgkmcnt(8)\n\tv_mfma_f32_16x16x32_f16 v[52:55], v[36:39], v[8:11], v[52:55]\n\t" \
  "ds_read_b128 v[8:11], v28 offset:2032\n\ts_waitcnt lgkmcnt(7)\n\tv_mfma_f32_16x16x32_f16 v[56:59], v[36:39], v[12:15], v[56:59]\n\t" \
  "ds_read_b128 v[12:15], v28 offset:2688\n\ts_waitcnt lgkmcnt(6)\n\tv_mfma_f32_16x16x32_f16 v[60:63], v[36:39], v[16:19], v[60:63]\n\t" \
  "ds_read_b128 v[16:19], v28 offset:2752\n\ts_waitcnt lgkmcnt(5)\n\tv_mfma_f32_16x16x32_f16 v[64:67], v[36:39], v[20:23], 0\n\t" \
  "ds_read_b128 v[20:23], v28 offset:2016\n\ts_waitcnt lgkmcnt(5)\n\tv_mfma_f32_16x16x32_f16 v[52:55], v[164:167], v[0:3], v[52:55]\n\t" \
  "v_mov_b32 v33, 5\n\tv_mfma_f32_16x16x32_f16 v[56:59], v[132:135], v[0:3], v[56:59]\n\tv_mfma_f32_16x16x32_f16 v[60:63], v[100:103], v[0:3], v[60:63]\n\t" \
  "ds_read_b128 v[0:3], v28 offset:2816\n\ts_waitcnt lgkmcnt(5)\n\tv_mfma_f32_16x16x32_f16 v[56:59], v[136:139], v[4:7], v[56:59]\n\t" \
  "v_mfma_f32_16x16x32_f16 v[60:63], v[104:107], v[4:7], v[60:63]\n\tds_read_b128 v[4:7], v28 offset:2880\n\tv_cvt_pk_bf16_f32 v52, v52, v53\n\t" \
  "v_cvt_pk_bf16_f32 v53, v54, v55\n\ts_cmp_lt_u32 %[w], 4\n\ts_cbranch_scc0 1f\n\t" \
  "s_setprio 0\n\t1:\n\ts_waitcnt lgkmcnt(5)\n\t" \
  "v_mfma_f32_16x16x32_f16 v[44:47], v[8:11], v[68:71], 0\n\tds_write_b64 v29, v[52:53] offset:2048\n\tds_write_b32 v25, v33\n\t" \
  "v_mfma_f32_16x16x32_f16 v[40:43], v[8:11], v[72:75], 0\n\tds_read_b128 v[8:11], v28 offset:992\n\ts_waitcnt lgkmcnt(7)\n\t" \
  "v_mfma_f32_16x16x32_f16 v[56:59], v[140:143], v[12:15], v[56:59]\n\tv_mfma_f32_16x16x32_f16 v[60:63], v[108:111], v[12:15], v[60:63]\n\tds_read_b128 v[12:15], v32 offset:3072\n\t" \
  "v_cvt_pk_f16_f32 v44, v44, v45\n\tv_cvt_pk_f16_f32 v45, v46, v47\n\ts_waitcnt lgkmcnt(7)\n\t" \
  "v_mfma_f32_16x16x32_f16 v[56:59], v[144:147], v[16:19], v[56:59]\n\tv_cvt_pk_f16_f32 v46, v40, v41\n\tv_cvt_pk_f16_f32 v47, v42, v43\n\t" \
  "v_mfma_f32_16x16x32_f16 v[60:63], v[112:115], v[16:19], v[60:63]\n\tds_read_b128 v[16:19], v28 offset:1008\n\tv_mfma_f32_16x16x32_f16 v[56:59], v[44:47], v[76:79], v[56:59]\n\t" \
  "v_mfma_f32_16x16x32_f16 v[60:63], v[44:47], v[80:83], v[60:63]\n\tv_mfma_f32_16x16x32_f16 v[64:67], v[44:47], v[84:87], v[64:67]\n\ts_waitcnt lgkmcnt(7)\n\t" \
  "v_mfma_f32_16x16x32_f16 v[36:39], v[20:23], v[68:71], 0\n\tv_mfma_f32_16x16x32_f16 v[40:43], v[20:23], v[72:75], 0\n\tds_read_b128 v[20:23], v32 offset:2048\n\t" \
  "s_waitcnt lgkmcnt(7)\n\tv_mfma_f32_16x16x32_f16 v[56:59], v[148:151], v[0:3], v[56:59]\n\tv_mfma_f32_16x16x32_f16 v[60:63], v[116:119], v[0:3], v[60:63]\n\t" \
  "ds_read_b128 v[0:3], v28 offset:2944\n\ts_waitcnt lgkmcnt(7)\n\tv_mfma_f32_16x16x32_f16 v[56:59], v[152:155], v[4:7], v[56:59]\n\t" \
  "v_cvt_pk_f16_f32 v36, v36, v37\n\tv_cvt_pk_f16_f32 v37, v38, v39\n\tv_cvt_pk_f16_f32 v38, v40, v41\n\t" \
  "v_cvt_pk_f16_f32 v39, v42, v43\n\tv_mfma_f32_16x16x32_f16 v[60:63], v[120:123], v[4:7], v[60:63]\n\tds_read_b128 v[4:7], v28 offset:3008\n\t" \
  "v_mfma_f32_16x16x32_f16 v[56:59], v[36:39], v[88:91], v[56:59]\n\tv_mfma_f32_16x16x32_f16 v[60:63], v[36:39], v[92:95], v[60:63]\n\tv_mfma_f32_16x16x32_f16 v[64:67], v[36:39], v[96:99], v[64:67]\n\t" \
  "ds_read_b32 v34, v30\n\ts_waitcnt lgkmcnt(5)\n\tv_mfma_f32_16x16x32_f16 v[40:43], v[8:11], v[12:15], 0\n\t" \
  "ds_read_b64 v[8:9], v31 offset:544\n\tds_read_b64 v[10:11], v31 offset:712\n\tds_read_b64 v[12:13], v31 offset:512\n\t" \
  "ds_read_b64 v[14:15], v31 offset:680\n\ts_waitcnt lgkmcnt(8)\n\tv_mfma_f32_16x16x32_f16 v[56:59], v[48:51], v[16:19], v[56:59]\n\t" \
  "s_waitcnt lgkmcnt(7)\n\tv_mfma_f32_16x16x32_f16 v[44:47], v[16:19], v[20:23], 0\n\tds_read_b64 v[16:17], v31 offset:480\n\t" \
  "ds_read_b64 v[18:19], v31 offset:648\n\ts_waitcnt lgkmcnt(6)\n\tv_cmp_gt_u32 vcc, 4, v34\n\t" \
  "s_cbranch_vccz 2f\n\ts_mov_b32 s6, 0x10000\n\t1:\n\t" \
  "s_sleep 1\n\tds_read_b32 v34, v30\n\ts_sub_u32 s6, s6, 1\n\t" \
  "s_waitcnt lgkmcnt(0)\n\tv_cmp_gt_u32 vcc, 4, v34\n\ts_cbranch_scc1 2f\n\t" \
  "s_cbranch_vccnz 1b\n\t2:\n\tds_read_b128 v[20:23], v28 offset:3072\n\t" \
  "v_mfma_f32_16x16x32_f16 v[56:59], v[156:159], v[0:3], v[56:59]\n\tv_mfma_f32_16x16x32_f16 v[60:63], v[124:127], v[0:3], v[60:63]\n\tds_read_b128 v[0:3], v28 offset:3136\n\t" \
  "v_cvt_pk_f16_f32 v44, v44, v45\n\tv_cvt_pk_f16_f32 v45, v46, v47\n\tv_mfma_f32_16x16x32_f16 v[56:59], v[160:163], v[4:7], v[56:59]\n\t" \
  "v_cvt_pk_f16_f32 v46, v40, v41\n\tv_cvt_pk_f16_f32 v47, v42, v43\n\tv_mfma_f32_16x16x32_f16 v[60:63], v[128:131], v[4:7], v[60:63]\n\t" \
  "ds_read_b128 v[4:7], v28 offset:2544\n\ts_waitcnt lgkmcnt(7)\n\tv_mfma_f32_16x16x32_f16 v[56:59], v[44:47], v[8:11], v[56:59]\n\t" \
  "ds_read_b128 v[8:11], v28 offset:3200\n\ts_waitcnt lgkmcnt(6)\n\tv_mfma_f32_16x16x32_f16 v[60:63], v[44:47], v[12:15], v[60:63]\n\t" \
  "ds_read_b128 v[12:15], v28 offset:3264\n\ts_waitcnt lgkmcnt(5)\n\tv_mfma_f32_16x16x32_f16 v[64:67], v[44:47], v[16:19], v[64:67]\n\t" \
  "ds_read_b128 v[16:19], v28 offset:2528\n\ts_waitcnt lgkmcnt(5)\n\tv_mfma_f32_16x16x32_f16 v[56:59], v[164:167], v[20:23], v[56:59]\n\t" \
  "v_mov_b32 v33, 6\n\tv_mfma_f32_16x16x32_f16 v[60:63], v[132:135], v[20:23], v[60:63]\n\tv_mfma_f32_16x16x32_f16 v[64:67], v[100:103], v[20:23], v[64:67]\n\t" \
  "ds_read_b128 v[20:23], v28 offset:3328\n\ts_waitcnt lgkmcnt(5)\n\tv_mfma_f32_16x16x32_f16 v[60:63], v[136:139], v[0:3], v[60:63]\n\t" \
  "v_mfma_f32_16x16x32_f16 v[64:67], v[104:107], v[0:3], v[64:67]\n\tds_read_b128 v[0:3], v28 offset:3392\n\tv_cvt_pk_bf16_f32 v56, v56, v57\n\t" \
  "v_cvt_pk_bf16_f32 v57, v58, v59\n\ts_waitcnt lgkmcnt(5)\n\tv_mfma_f32_16x16x32_f16 v[36:39], v[4:7], v[68:71], 0\n\t" \
  "ds_write_b64 v29, v[56:57] offset:2560\n\tds_write_b32 v25, v33\n\tv_mfma_f32_16x16x32_f16 v[40:43], v[4:7], v[72:75], 0\n\t" \
  "ds_read_b128 v[4:7], v28 offset:1504\n\ts_waitcnt lgkmcnt(7)\n\tv_mfma_f32_16x16x32_f16 v[60:63], v[140:143], v[8:11], v[60:63]\n\t" \
  "v_mfma_f32_16x16x32_f16 v[64:67], v[108:111], v[8:11], v[64:67]\n\tds_read_b128 v[8:11], v32 offset:3072\n\tv_cvt_pk_f16_f32 v36, v36, v37\n\t" \
  "v_cvt_pk_f16_f32 v37, v38, v39\n\ts_waitcnt lgkmcnt(7)\n\tv_mfma_f32_16x16x32_f16 v[60:63], v[144:147], v[12:15], v[60:63]\n\t" \
  "v_cvt_pk_f16_f32 v38, v40, v41\n\tv_cvt_pk_f16_f32 v39, v42, v43\n\tv_mfma_f32_16x16x32_f16 v[64:67], v[112:115], v[12:15], v[64:67]\n\t" \
  "ds_read_b128 v[12:15], v28 offset:1520\n\tv_mfma_f32_16x16x32_f16 v[60:63], v[36:39], v[76:79], v[60:63]\n\tv_mfma_f32_16x16x32_f16 v[64:67], v[36:39], v[80:83], v[64:67]\n\t" \
  "s_waitcnt lgkmcnt(7)\n\tv_mfma_f32_16x16x32_f16 v[44:47], v[16:19], v[68:71], 0\n\tv_mfma_f32_16x16x32_f16 v[40:43], v[16:19], v[72:75], 0\n\t" \
  "ds_read_b128 v[16:19], v32 offset:2048\n\ts_waitcnt lgkmcnt(7)\n\tv_mfma_f32_16x16x32_f16 v[60:63], v[148:151], v[20:23], v[60:63]\n\t" \
  "v_mfma_f32_16x16x32_f16 v[64:67], v[116:119], v[20:23], v[64:67]\n\tds_read_b128 v[20:23], v28 offset:3456\n\ts_waitcnt lgkmcnt(7)\n\t" \
  "v_mfma_f32_16x16x32_f16 v[60:63], v[152:155], v[0:3], v[60:63]\n\tv_cvt_pk_f16_f32 v44, v44, v45\n\tv_cvt_pk_f16_f32 v45, v46, v47\n\t" \
  "v_cvt_pk_f16_f32 v46, v40, v41\n\tv_cvt_pk_f16_f32 v47, v42, v43\n\tv_mfma_f32_16x16x32_f16 v[64:67], v[120:123], v[0:3], v[64:67]\n\t" \
  "ds_read_b128 v[0:3], v28 offset:3520\n\tv_mfma_f32_16x16x32_f16 v[60:63], v[44:47], v[88:91], v[60:63]\n\tv_mfma_f32_16x16x32_f16 v[64:67], v[44:47], v[92:95], v[64:67]\n\t" \
  "s_waitcnt lgkmcnt(4)\n\tv_mfma_f32_16x16x32_f16 v[40:43], v[4:7], v[8:11], 0\n\tds_read_b64 v[4:5], v31 offset:544\n\t" \
  "ds_read_b64 v[6:7], v31 offset:712\n\tds_read_b64 v[8:9], v31 offset:512\n\tds_read_b64 v[10:11], v31 offset:680\n\t" \
  "s_waitcnt lgkmcnt(7)\n\tv_mfma_f32_16x16x32_f16 v[60:63], v[48:51], v[12:15], v[60:63]\n\ts_waitcnt lgkmcnt(6)\n\t" \
  "v_mfma_f32_16x16x32_f16 v[36:39], v[12:15], v[16:19], 0\n\tds_read_b128 v[12:15], v28 offset:3584\n\tds_read_b128 v[16:19], v28 offset:3648\n\t" \
  "s_waitcnt lgkmcnt(7)\n\tv_mfma_f32_16x16x32_f16 v[60:63], v[156:159], v[20:23], v[60:63]\n\tv_mfma_f32_16x16x32_f16 v[64:67], v[124:127], v[20:23], v[64:67]\n\t" \
  "ds_read_b128 v[20:23], v28 offset:3056\n\ts_waitcnt lgkmcnt(7)\n\tv_mfma_f32_16x16x32_f16 v[60:63], v[160:163], v[0:3], v[60:63]\n\t" \
  "v_cvt_pk_f16_f32 v36, v36, v37\n\tv_cvt_pk_f16_f32 v37, v38, v39\n\tv_cvt_pk_f16_f32 v38, v40, v41\n\t" \
  "v_cvt_pk_f16_f32 v39, v42, v43\n\tv_mfma_f32_16x16x32_f16 v[64:67], v[128:131], v[0:3], v[64:67]\n\tds_read_b128 v[0:3], v28 offset:3712\n\t" \
  "s_waitcnt lgkmcnt(6)\n\tv_mfma_f32_16x16x32_f16 v[60:63], v[36:39], v[4:7], v[60:63]\n\tds_read_b128 v[4:7], v28 offset:3776\n\t" \
  "s_waitcnt lgkmcnt(5)\n\tv_mfma_f32_16x16x32_f16 v[64:67], v[36:39], v[8:11], v[64:67]\n\tds_read_b128 v[8:11], v28 offset:3040\n\t" \
  "s_waitcnt lgkmcnt(5)\n\tv_mfma_f32_16x16x32_f16 v[60:63], v[164:167], v[12:15], v[60:63]\n\tv_mov_b32 v33, 7\n\t" \
  "v_mfma_f32_16x16x32_f16 v[64:67], v[132:135], v[12:15], v[64:67]\n\tds_read_b128 v[12:15], v28 offset:3840\n\ts_waitcnt lgkmcnt(5)\n\t" \
  "v_mfma_f32_16x16x32_f16 v[64:67], v[136:139], v[16:19], v[64:67]\n\tds_read_b128 v[16:19], v28 offset:3904\n\ts_waitcnt lgkmcnt(5)\n\t" \
  "v_mfma_f32_16x16x32_f16 v[44:47], v[20:23], v[68:71], 0\n\tv_cvt_pk_bf16_f32 v60, v60, v61\n\tv_cvt_pk_bf16_f32 v61, v62, v63\n\t" \
  "s_cmp_lt_u32 %[w], 4\n\ts_cbranch_scc1 1f\n\ts_setprio 0\n\t" \
  "1:\n\tv_mfma_f32_16x16x32_f16 v[40:43], v[20:23], v[72:75], 0\n\tds_write_b64 v29, v[60:61] offset:3072\n\t" \
  "ds_write_b32 v25, v33\n\tds_read_b128 v[20:23], v28 offset:2016\n\ts_waitcnt lgkmcnt(7)\n\t" \
  "v_mfma_f32_16x16x32_f16 v[64:67], v[140:143], v[0:3], v[64:67]\n\tds_read_b128 v[0:3], v32 offset:3072\n\tv_cvt_pk_f16_f32 v44, v44, v45\n\t" \
  "v_cvt_pk_f16_f32 v45, v46, v47\n\tv_cvt_pk_f16_f32 v46, v40, v41\n\tv_cvt_pk_f16_f32 v47, v42, v43\n\t" \
  "s_waitcnt lgkmcnt(7)\n\tv_mfma_f32_16x16x32_f16 v[64:67], v[144:147], v[4:7], v[64:67]\n\tds_read_b128 v[4:7], v28 offset:2032\n\t" \
  "v_mfma_f32_16x16x32_f16 v[64:67], v[44:47], v[76:79], v[64:67]\n\ts_waitcnt lgkmcnt(7)\n\tv_mfma_f32_16x16x32_f16 v[36:39], v[8:11], v[68:71], 0\n\t" \
  "v_mfma_f32_16x16x32_f16 v[40:43], v[8:11], v[72:75], 0\n\tds_read_b128 v[8:11], v32 offset:2048\n\ts_waitcnt lgkmcnt(7)\n\t" \
  "v_mfma_f32_16x16x32_f16 v[64:67], v[148:151], v[12:15], v[64:67]\n\tds_read_b128 v[12:15], v28 offset:3968\n\ts_waitcnt lgkmcnt(7)\n\t" \
  "v_mfma_f32_16x16x32_f16 v[64:67], v[152:155], v[16:19], v[64:67]\n\tds_read_b128 v[16:19], v28 offset:4032\n\tv_cvt_pk_f16_f32 v36, v36, v37\n\t" \
  "v_cvt_pk_f16_f32 v37, v38, v39\n\tv_cvt_pk_f16_f32 v38, v40, v41\n\tv_cvt_pk_f16_f32 v39, v42, v43\n\t" \
  "s_nop 1\n\tv_mfma_f32_16x16x32_f16 v[64:67], v[36:39], v[88:91], v[64:67]\n\ts_waitcnt lgkmcnt(4)\n\t" \
  "v_mfma_f32_16x16x32_f16 v[40:43], v[20:23], v[0:3], 0\n\tds_read_b64 v[0:1], v31 offset:544\n\tds_read_b64 v[2:3], v31 offset:712\n\t" \
  "ds_read_b128 v[20:23], v28 offset:4096\n\ts_waitcnt lgkmcnt(6)\n\tv_mfma_f32_16x16x32_f16 v[64:67], v[48:51], v[4:7], v[64:67]\n\t" \
  "s_waitcnt lgkmcnt(5)\n\tv_mfma_f32_16x16x32_f16 v[44:47], v[4:7], v[8:11], 0\n\ts_waitcnt lgkmcnt(4)\n\t" \
  "v_mfma_f32_16x16x32_f16 v[64:67], v[156:159], v[12:15], v[64:67]\n\ts_waitcnt lgkmcnt(3)\n\tv_mfma_f32_16x16x32_f16 v[64:67], v[160:163], v[16:19], v[64:67]\n\t" \
  "s_nop 3\n\tv_cvt_pk_f16_f32 v44, v44, v45\n\tv_cvt_pk_f16_f32 v45, v46, v47\n\t" \
  "v_cvt_pk_f16_f32 v46, v40, v41\n\tv_cvt_pk_f16_f32 v47, v42, v43\n\ts_waitcnt lgkmcnt(1)\n\t" \
  "s_nop 0\n\tv_mfma_f32_16x16x32_f16 v[64:67], v[44:47], v[0:3], v[64:67]\n\ts_waitcnt lgkmcnt(0)\n\t" \
  "v_mfma_f32_16x16x32_f16 v[64:67], v[164:167], v[20:23], v[64:67]\n\tv_mov_b32 v33, 8\n\ts_nop 6\n\t" \
  "v_cvt_pk_bf16_f32 v64, v64, v65\n\tv_cvt_pk_bf16_f32 v65, v66, v67\n\tds_write_b64 v29, v[64:65] offset:3584\n\t" \
  "ds_write_b32 v25, v33\n\t"
#define CONV_CLOBBER "v0", "v1", "v2", "v3", "v4", "v5", "v6", "v7", "v8", "v9", "v10", "v11", "v12", "v13", "v14", "v15", "v16", "v17", "v18", "v19", "v20", "v21", "v22", "v23", "v36", "v37", "v38", "v39", "v40", "v41", "v42", "v43", "v44", "v45", "v46", "v47", "v52", "v53", "v54", "v55", "v56", "v57", "v58", "v59", "v60", "v61", "v62", "v63", "v64", "v65", "v66", "v67", "v68", "v69", "v70", "v71", "v72", "v73", "v74", "v75", "v76", "v77", "v78", "v79", "v80", "v81", "v82", "v83", "v84", "v85", "v86", "v87", "v88", "v89", "v90", "v91", "v92", "v93", "v94", "v95", "v96", "v97", "v98", "v99", "v100", "v101", "v102", "v103", "v104", "v105", "v106", "v107", "v108", "v109", "v110", "v111", "v112", "v113", "v114", "v115", "v116", "v117", "v118", "v119", "v120", "v121", "v122", "v123", "v124", "v125", "v126", "v127", "v128", "v129", "v130", "v131", "v132", "v133", "v134", "v135", "v136", "v137", "v138", "v139", "v140", "v141", "v142", "v143", "v144", "v145", "v146", "v147", "v148", "v149", "v150", "v151", "v152", "v153", "v154", "v155", "v156", "v157", "v158", "v159", "v160", "v161", "v162", "v163", "v164", "v165", "v166", "v167"
#define G_CLOBBER "v8", "v9", "v10", "v11", "v12", "v13", "v14", "v15", "v16", "v17", "v18", "v19", "v20", "v21", "v22", "v23", "v24", "v25", "v26", "v27", "v28", "v29", "v30", "v32", "v33", "v34", "v35", "v36", "v37", "v38", "v39", "v40", "v41", "v42", "v43", "v44", "v45", "v46", "v47", "v48", "v49", "v50", "v51", "v52", "v53", "v54", "v55", "v56", "v57", "v58", "v59", "v60", "v61", "v62", "v63", "v64", "v65", "v66", "v67", "v68", "v69", "v70", "v71", "v72", "v73", "v74", "v75", "v76", "v77", "v78", "v79", "v80", "v81", "v82", "v83", "v84", "v85", "v86", "v87", "v88", "v89", "v90", "v91", "v92", "v93", "v94", "v95", "v96", "v97", "v98", "v99", "v100", "v101", "v102", "v103", "v104", "v105", "v106", "v107", "v108", "v109", "v110", "v111", "v112", "v113", "v114", "v115", "v116", "v117", "v118", "v119", "v120", "v121", "v122", "v123", "v124", "v125", "v126", "v127"
#define D_CLOBBER "v8", "v9", "v10", "v11", "v12", "v13", "v14", "v15", "v16", "v17", "v18", "v19", "v20", "v21", "v22", "v23", "v24", "v25", "v26", "v27", "v28", "v29", "v30", "v31"
__device__ __forceinline__ f32x2 pkfma(f32x2 a, f32x2 b, f32x2 c) {
  f32x2 r;
  asm("v_pk_fma_f32 %0, %1, %2, %3" : "=v"(r) : "v"(a), "v"(b), "v"(c));
  return r;
}
__device__ __forceinline__ float fmav(float a, float b, float c) {
  float r;
  asm("v_fma_f32 %0, %1, %2, %3" : "=v"(r) : "v"(a), "v"(b), "v"(c));
  return r;
}
__device__ __forceinline__ float fmac(float a, float b, float c) {
  asm("v_fmac_f32 %0, %1, %2" : "+v"(c) : "v"(a), "v"(b));
  return c;
}
__device__ __forceinline__ float fmas(float a, u32 s, float c) {
  asm("v_fmac_f32 %0, %2, %1" : "+v"(c) : "v"(a), "s"(s));
  return c;
}
template <int HI>
__device__ __forceinline__ f32x2 pkfmas(f32x2 a, f32x2 s, f32x2 c) {
  f32x2 r;
  if (HI) asm("v_pk_fma_f32 %0, %2, %1, %3 op_sel:[1,0,0] op_sel_hi:[1,1,1]" : "=v"(r) : "v"(a), "v"(s), "v"(c));
  else asm("v_pk_fma_f32 %0, %2, %1, %3 op_sel:[0,0,0] op_sel_hi:[0,1,1]" : "=v"(r) : "v"(a), "v"(s), "v"(c));
  return r;
}
#define RANK8(d0, d1, d2, d3, c0, c1, i0, f0, f1) "v_perm_b32 " d0 ", " d0 ", " d1 ", %[sg]\n\tv_perm_b32 " d2 ", " d2 ", " d3 ", %[sg]\n\t" \
  "v_bcnt_u32_b32 " d0 ", " d0 ", " i0 "\n\tv_bcnt_u32_b32 " c0 ", " d2 ", " d0 "\n\tv_bcnt_u32_b32 " c1 ", " d2 ", " d0 "\n\t" f0 "\n\t" \
  "v_permlane16_swap_b32 " c0 ", " c1 "\n\tv_add_u32 " d0 ", " c0 ", " c1 "\n\tv_add_u32 " d1 ", " c0 ", " c1 "\n\t" f1 "\n\t" \
  "v_permlane32_swap_b32 " d0 ", " d1 "\n\tv_add_u32 " c0 ", " d0 ", " d1 "\n\t"
__device__ __forceinline__ void ln_rows(u32x4 (&xv)[4], u32x8 wv, rsrc_t rs, u32& tag, int b, int w, int l, u32 st, u32 sg TLARG) {
  float s[2][3] = {}, c[2], sw = 0.f;
  u32 os = l < 16 ? O_SG + 128 * (b - 128) + 8 * l : 0x80000000u;
  u32 ot = l < 16 ? O_TOP + 8 * (b & 15) + TL * ((b >> 4) & 7) : l < 32 ? O_RG - 128 + 128 * (b - 128) + 8 * l : 0x80000000u, om = l < 16 ? TOPS / 8 : 0u, fm = l < 16 ? 0xff0u : 0u;
  u32 aw = (l & 16) ? st + 8 * w + (l >> 5) * 4 : st + 136, ac = st + 128, ar = st + 4 * (l & 15), aq = st + (l & 48), one = 1u;
  u32 kl = (2047 - (16 * (b - 128) + 2 * w + (l >> 5))) << 1;
  asm volatile("" : "+v"(os), "+v"(aw), "+v"(ac), "+v"(ar), "+v"(aq), "+v"(one), "+v"(kl), "+v"(ot), "+v"(om), "+v"(fm));
  SB();
  asm volatile("s_waitcnt vmcnt(4) lgkmcnt(0)" : "+{s44}"(tag)::"memory");
  asm volatile("" : "+{v[116:123]}"(wv));
  tag += 1u;
  *(volatile LDS u32*)(size_t)(st - 104) = tag;
  STAMP(12);
#pragma unroll
  for (int i = 0; i < 8; i++) sw = dot2b(wv[i], 0x3f803f80u, sw);
  sw += dppf<0x121, 0xf>(sw); sw += dppf<0x122, 0xf>(sw); sw += dppf<0x124, 0xf>(sw); sw += dppf<0x128, 0xf>(sw);
  sw = (rdl(sw, 0) + rdl(sw, 16)) + (rdl(sw, 32) + rdl(sw, 48));
  asm volatile("" : "+v"(sw));
  SB();
  float cv = 0.f;
#pragma unroll
  for (int r = 0; r < 2; r++) {
#pragma unroll
    for (int i = 0; i < 8; i++) {
      if (r == 0 && i == 0) { asm volatile("s_waitcnt vmcnt(3)" : "+{v[100:103]}"(xv[0])); STAMP(13); }
      if (r == 0 && i == 4) asm volatile("s_waitcnt vmcnt(2)" : "+{v[104:107]}"(xv[1]));
      if (r == 1 && i == 0) asm volatile("s_waitcnt vmcnt(1)" : "+{v[108:111]}"(xv[2]));
      if (r == 1 && i == 4) asm volatile("s_waitcnt vmcnt(0)" : "+{v[112:115]}"(xv[3]));
      const u32 a = xv[2 * r + (i >> 2)][i & 3];
      s[r][0] = dot2b(a, 0x3f803f80u, s[r][0]); s[r][1] = dot2b(a, a, s[r][1]); s[r][2] = dot2b(a, wv[i], s[r][2]);
      if (i == 3) {
        c[r] = rdl(s[r][0] * 0.125f, 0);
        if (r) { cv = l < 32 ? c[0] : c[1]; asm volatile("" : "+v"(cv)); }
        SB();
      }
    }
    const float ml = s[r][0] * 0.0625f, dl = __builtin_fmaf(s[r][0], 0.25f, -4.f * c[r]);
    s[r][1] = __builtin_fmaf(dl, dl, __builtin_fmaf(-s[r][0], ml, s[r][1]));
    SB();
  }
#pragma unroll
  for (int k = 0; k < 3; k++) swap32(s[0][k ? 3 - k : 0], s[1][k ? 3 - k : 0]);
  float q[3];
#pragma unroll
  for (int k = 0; k < 3; k++) q[k] = s[0][k] + s[1][k];
#define RED3(m) "v_add_f32_dpp %0, %0, %0 " m " bank_mask:0xf\n\tv_add_f32_dpp %1, %1, %1 " m " bank_mask:0xf\n\tv_add_f32_dpp %2, %2, %2 " m " bank_mask:0xf\n\t"
  asm volatile("s_nop 1\n\t" RED3("row_ror:1 row_mask:0xf") RED3("row_ror:2 row_mask:0xf") RED3("row_ror:4 row_mask:0xf") RED3("row_ror:8 row_mask:0xf")
               RED3("row_bcast:15 row_mask:0xa") : "+v"(q[0]), "+v"(q[1]), "+v"(q[2]));
  const float mu = q[0] * (1.f / D), dv = mu - cv;
  const float var = fmaxf(__builtin_fmaf(-dv, dv, q[1] * (1.f / D)), 0.f);
  const float m = rb(mu);
  float sd = __builtin_amdgcn_sqrtf(rb(rb(var) + 1e-5f)), num = __builtin_fmaf(-m, sw, q[2]);
  asm volatile("s_nop 0\n\tv_cvt_pk_bf16_f32 %0, 0, %0" : "+v"(sd), "+v"(num));
  u32 rv = __float_as_uint(rb(num * __builtin_amdgcn_rcpf(sd)));
  rv ^= (u32)((int)rv >> 15) | 0x80000000u;
  rv = __builtin_amdgcn_perm(rv, kl, 0x07060100u) >> 1;
  asm volatile("ds_write2_b32 %[aw], %[sv], %[rv] offset1:16\n\ts_mov_b64 exec, 1\n\tds_add_rtn_u32 v104, %[ac], %[one]\n\ts_mov_b64 exec, -1\n\t"
               "ds_read_b32 v106, %[ar]\n\tds_read_b32 v105, %[ar] offset:64\n\tds_read_b128 v[100:103], %[aq] offset:64\n\tv_mov_b32 v107, %[t]\n\tv_mov_b32 v109, %[t]\n\t"
               "s_waitcnt lgkmcnt(3)\n\tv_cmp_eq_u32 vcc, 7, v104\n\ts_bitcmp1_b32 vcc_lo, 0\n\ts_cbranch_scc0 1f\n\t"
               "s_waitcnt lgkmcnt(2)\n\tbuffer_store_dwordx2 v[106:107], %[os], %[rs], 0 offen sc1\n\ts_waitcnt lgkmcnt(0)\n\t"
               "v_sub_u32 v100, v105, v100\n\tv_sub_u32 v101, v105, v101\n\tv_sub_u32 v102, v105, v102\n\tv_sub_u32 v103, v105, v103\n\t"
               RANK8("v100", "v101", "v102", "v103", "v104", "v103", "0", "s_nop 1", "s_nop 1") "v_cmp_eq_u32 vcc, 16, v104\n\t"
               "v_sub_u32 v108, 19, v104\n\ts_ff1_i32_b32 s6, vcc_lo\n\tv_readlane_b32 s6, v105, s6\n\t"
               "v_mad_u32_u24 v104, v104, %[om], %[ot]\n\ts_nop 0\n\tv_lshrrev_b32 v108, v108, s6\n\tv_bfi_b32 v108, %[fm], v108, v105\n\t"
               "buffer_store_dwordx2 v[108:109], v104, %[rs], 0 offen sc1\n1:\n\ts_waitcnt lgkmcnt(0)"
               :: [aw] "v"(aw), [sv] "v"(__builtin_amdgcn_perm(__float_as_uint(sd), __float_as_uint(m), 0x07060302u)), [rv] "v"(rv), [ac] "v"(ac), [one] "v"(one),
                  [ar] "v"(ar), [aq] "v"(aq), [os] "v"(os), [om] "v"(om), [ot] "v"(ot), [fm] "v"(fm), [sg] "s"(sg), [t] "s"(tag), [rs] "s"(rs)
               : "vcc", "scc", "s6", "v100", "v101", "v102", "v103", "v104", "v105", "v106", "v107", "v108", "v109", "memory");
  SB();
}

#define GL __attribute__((address_space(1)))
typedef const GL u16* gp;

__global__ __launch_bounds__(768, 3) void fused(const u16*, const u16*, const u16*, const u16*, const u16*, const u16*, const u16*, const u16*,
    const u16*, const u16*, const u16*, u16*, char*) {
  __shared__ __attribute__((aligned(256))) char smem[SMEM];
  const u32 lds0 = (u32)(size_t)(LDS char*)smem;
  int w0;
#define GLD(r0, r1, r2, r3, o_) "global_load_dwordx4 v[" #r0 ":" #r1 "], " #o_ ", s[16:17]\n\tglobal_load_dwordx4 v[" #r2 ":" #r3 "], " #o_ ", s[16:17] offset:2048\n\t"
  asm volatile(
      "s_load_dwordx16 s[16:31], s[0:1], 0\n\ts_load_dwordx8 s[32:39], s[0:1], 64\n\ts_load_dwordx2 s[40:41], s[0:1], 96\n\t"
      "s_memrealtime s[46:47]\n\t"
      "s_getpc_b64 s[4:5]\n2:\n\ts_lshr_b32 s7, s2, 3\n\ts_and_b32 s10, s4, 0xffffff00\n\ts_mov_b32 s11, s5\n\ts_mul_i32 s8, s7, 0x600\n\t"
      "v_lshl_add_u32 v3, v0, 1, s8\n\ts_cmpk_lt_u32 s2, %[pn]\n\ts_cbranch_scc0 1f\n\t"
      "global_load_ushort v163, v3, s[10:11] offset:%[pf]\n"
      "1:\n\ts_cmpk_lt_u32 s2, 0x80\n\tv_readfirstlane_b32 s3, v0\n\tv_and_b32 v1, 63, v0\n\tv_mov_b64 v[4:5], 0\n\tv_mov_b64 v[6:7], 0\n\tv_lshlrev_b32 v2, 4, v1\n\tv_lshlrev_b32 v8, 2, v0\n\t"
      "s_mov_b32 s42, %[we]\n\ts_mov_b32 s43, 0x20000\n\ts_cbranch_scc0 3f\n\t"
      "s_lshr_b32 s3, s3, 6\n\ts_mul_i32 s9, s3, %[xr]\n\ts_min_u32 s9, s9, %[zm]\n\tv_add_u32 v3, s9, v2\n\tv_add_u32 v3, %[zo], v3\n\t"
      "s_and_b32 s7, s7, 7\n\ts_and_b32 s6, s2, 7\n\ts_and_b32 s8, s2, 64\n\ts_lshl3_add_u32 s8, s6, s8\n\ts_add_u32 s45, s8, s7\n\t"
      "s_add_u32 s4, s4, lane_tab@rel32@lo+(6f-2b-4)\n6:\n\ts_addc_u32 s5, s5, lane_tab@rel32@hi+(6b-2b+4)\n\t"
      "ds_write_b128 v3, v[4:7]\n\ts_cmp_lt_u32 s3, 8\n\ts_cbranch_scc1 5f\n\t"
      "s_lshl_b32 s8, s45, 4\n\ts_lshl_b32 s9, s3, 18\n\ts_add_u32 s9, s9, s8\n\ts_bitset0_b32 s9, 21\n\tv_lshl_add_u32 v148, v1, 12, s9\n\t"
      "s_lshl3_add_u32 s9, s3, s7\n\ts_lshl_b32 s9, s9, 17\n\ts_add_u32 s9, s9, s8\n\ts_bitset0_b32 s9, 23\n\tv_lshl_add_u32 v9, v1, 11, s9\n\t"
      "v_add_u32 v11, 0x100000, v148\n\tv_add_u32 v12, 0x200000, v148\n\t"
      "v_lshlrev_b32 v149, 4, v0\n\ts_lshl_b32 s9, s3, 10\n\ts_add_u32 m0, s9, %[ob]\n\tv_add_u32 v10, 0xffffe800, v149\n\tglobal_load_lds_dwordx4 v10, s[4:5]\n\t"
      "v_add_u32 v149, %[sg], v149\n\t"
      "s_waitcnt lgkmcnt(0)\n\ts_load_dword s44, s[40:41], 0\n\ts_load_dwordx4 s[48:51], s[28:29], s8\n\t"
      "s_barrier\n\t"
      "global_load_dword v160, v9, s[16:17]" PRENT "\n\t"
      GLD(100, 103, 104, 107, v148)
      GLD(108, 111, 112, 115, v11)
      "s_cmp_eq_u32 s6, 3\n\ts_cbranch_scc1 .Lrole_p\n\t"
      GLD(116, 119, 120, 123, v12)
      "s_branch .Lrole_p\n"
      "3:\n\ts_lshr_b32 s3, s3, 6\n\ts_cmp_lt_u32 s3, 8\n\ts_cbranch_scc1 4f\n\ts_barrier\n\ts_branch .Lrole_d\n4:\n\t"
      "v_add_u32 v3, %[fc], v2\n\ts_add_u32 s8, s3, s2\n\ts_and_b32 s45, s8, 7\n\ts_lshl3_add_u32 s8, s2, s45\n\t"
      "s_lshl_b32 s8, s8, 12\n\ts_bitset0_b32 s8, 22\n\tds_write_b128 v3, v[4:7]\n\tv_add_u32 v3, s8, v2\n\t"
      "s_waitcnt lgkmcnt(0)\n\tglobal_load_dwordx4 v[116:119], v2, s[30:31]\n\tglobal_load_dwordx4 v[120:123], v2, s[30:31] offset:1024\n\t"
      "s_load_dword s44, s[40:41], 0\n\ts_barrier\n\t"
      "global_load_dwordx4 v[100:103], v3, s[16:17]" LNNT "\n\tglobal_load_dwordx4 v[104:107], v3, s[16:17] offset:1024" LNNT "\n\t"
      "global_load_dwordx4 v[108:111], v3, s[16:17] offset:2048" LNNT "\n\tglobal_load_dwordx4 v[112:115], v3, s[16:17] offset:3072" LNNT "\n\t"
      "s_branch .Lrole_l\n"
      "5:\n\t"
      "global_load_dwordx4 v[16:19], v2, s[4:5]\n\tglobal_load_dwordx4 v[20:23], v2, s[4:5] offset:1024\n\t"
      "s_lshl3_add_u32 s8, s45, s3\n\ts_lshl_b32 s9, s8, 1\n\tv_mov_b32 v9, s9\n\ts_lshl_b32 s9, s45, 11\n\tv_add_u32 v3, s9, v8\n\t"
      "s_waitcnt lgkmcnt(0)\n\tglobal_load_short_d16_hi v24, v9, s[26:27]\n\t"
      "global_load_dword v96, v3, s[18:19]" PNT "\n\tglobal_load_dword v97, v3, s[20:21]" PNT "\n\t"
      "global_load_dword v68, v3, s[22:23]" PNT "\n\tglobal_load_dword v69, v3, s[24:25]" PNT "\n\ts_barrier\n\ts_branch .Lrole_c"
      : "={s3}"(w0)
      : "{s[0:1]}"(__builtin_amdgcn_kernarg_segment_ptr()), "{s2}"(blockIdx.x), "{v0}"(threadIdx.x), [pf] "n"(PFO), [pn] "n"(8 * PFN), [we] "n"(WS_END), [xr] "n"(XREG),
        [zm] "n"(OMISC - 512 - (OX - 16)), [zo] "n"(OX - 16), [sg] "n"(O_SG - 8192), [fc] "n"(FC), [ob] "n"(OB - 8192)
      : "memory");
#define TLREGS()
#define ROLE(name, text, ...) \
  gp x, dsk, wg, wu, wd; GL u16* out; rsrc_t rs; int b, w, t, l, g; u32 tag, st0; u32x4 dv; \
  asm volatile(".Lrole_" #name ":" : "={v0}"(t), "={v1}"(l) __VA_ARGS__ :: "memory"); \
  asm volatile(text : "={s2}"(b), "={s3}"(w), "={s[16:17]}"(x), "={s[28:29]}"(dsk), "={s[32:33]}"(wg), "={s[34:35]}"(wu), "={s[36:37]}"(wd), \
               "={s[38:39]}"(out), "={s[40:43]}"(rs), "={s44}"(tag), "={s45}"(g), "={s46}"(st0), "={s[48:51]}"(dv) :: "memory"); \
  SB(); \
  __builtin_assume((u32)l < 64u); __builtin_assume((u32)t < 768u); __builtin_assume((u32)w < 12u); __builtin_assume((u32)b < 256u); __builtin_assume((u32)g < 128u); \
  TLREGS(); \
  const int xk = b & 7, j = (b >> 3) & 15, n16 = l & 15, kg = l >> 4
#define TAGWAIT() do { asm volatile("s_waitcnt lgkmcnt(0)" : "+{s44}"(tag)::"memory"); tag += 1u; } while (0)
#define BAR() do { SB(); asm volatile("s_barrier" ::: "memory"); SB(); } while (0)
#define FLAG(o, v) do { asm volatile("" ::: "memory"); SB(); *(volatile LDS u32*)(LDS char*)(smem + (o)) = (v); SB(); } while (0)
#define ARRIVE(o) do { SB(); if (l == 0) asm volatile("ds_add_u32 %0, %1" ::"v"(lds0 + (o)), "v"(1u) : "memory"); SB(); } while (0)
#define CWAIT(o, n, nap) do { _Pragma("unroll 1") for (int spin = 0; spin < SPIN; spin++) { \
      if (__builtin_amdgcn_readfirstlane((int)*(const volatile LDS u32*)(LDS char*)(smem + (o))) >= (int)(n)) break; \
      asm volatile(nap ::: "memory"); } SB(); asm volatile("" ::: "memory"); } while (0)
#define AWAIT(o, n, nap) do { _Pragma("unroll 1") for (int spin = 0; spin < SPIN; spin++) { \
      if (__all(*(const volatile LDS u32*)(LDS char*)(smem + (o)) >= (u32)(n))) break; \
      asm volatile(nap ::: "memory"); } SB(); asm volatile("" ::: "memory"); } while (0)
#define POLL8(res_, off_) do { u32 n_ = SPIN; \
    asm volatile("1:\n\tbuffer_load_dwordx2 v[4:5], %2, %3, 0 offen sc1\n\ts_sub_u32 %1, %1, 1\n\ts_waitcnt vmcnt(0)\n\t" \
                 "v_cmp_ne_u32 vcc, %4, v5\n\ts_cbranch_scc1 2f\n\ts_cbranch_vccnz 1b\n2:" \
                 : "=&{v[4:5]}"(res_), "+s"(n_) : "v"(off_), "s"(rs), "s"(tag) : "vcc", "scc", "memory"); } while (0)
#define UNTIL(t_) "2:\n\ts_sleep 1\n\ts_memrealtime s[8:9]\n\ts_waitcnt lgkmcnt(0)\n\ts_sub_u32 s8, s8, %[st]\n\ts_cmp_lt_u32 s8, " t_ "\n\ts_cbranch_scc1 2b\n\t"
#define LANE(v_) int v_; asm volatile("v_mbcnt_lo_u32_b32 %0, -1, 0\n\tv_mbcnt_hi_u32_b32 %0, -1, %0" : "=v"(v_))
#define IXO(l_) (O_IDX + (2 * xk + (j & 1)) * 128 + ((l_) & 7) * 8)
#define IDS() const int id = 16 * xk + j, sb = id * 57 >> 10, mat = id & 1, u = (id - 18 * sb) >> 1; \
    const bool slow = u >= 6; \
    const int part = slow ? u - 6 : u & 1, cb = 4 * sb + (slow ? 1 : (u >> 1) + (u > 1)), r0 = part * (slow ? 320 : 512), ns = slow ? 5 + (u >> 3) : 8; \
    const int kp = 4 * sb + ((u >> 1) & 3), ct = 2 * (u & 1) + mat
#define TS(a, b_)
#define TSC
#define DONE "s_endpgm\n"
#define TSD
#define TSGET(i0, i1, i2, i3)
  if (blockIdx.x >= 128) {
    if (w0 >= 8) {
      ROLE(d, "s_waitcnt lgkmcnt(0)");
      IDS();
      const int dw = w - 8;
      const u32 sw = (((n16 >> 1) ^ (kg & 1)) << 5) + ((l & 1) << 4);
      gp base = (mat ? wu : wg) + (size_t)(r0 + 4 * dw) * F + cb * 128;
      u32 v = kg * (2 * F) + sw, fa = lds0 + FC + 4 * dw, vp = 96 + dw, t_;
      asm volatile("" : "+v"(v), "+v"(fa), "+v"(vp));
      asm volatile("s_mov_b32 m0, %0" ::"s"(lds0 + 1024 * dw));
#define VMC "s_getreg_b32 s6, hwreg(HW_REG_IB_STS, 0, 4)\n\t"
#define RUN(k, q, lim, v_, b_, st_) asm volatile("1:\n\ts_lshr_b32 s6, m0, 4\n\ts_and_b32 s6, s6, 0xc0\n\tv_xor_b32 %[t], s6, %[v]\n\ts_waitcnt vmcnt(%[c])\n\t" VMC \
        "global_load_lds_dwordx4 %[t], %[b] nt\n\ts_add_u32 m0, m0, %[m]\n\tv_add_u32 %[v], %[d], %[v]\n\t.if %[m] == 3072\n\tv_mad_i32_i24 %[t], s6, -3, %[p]\n\tv_add_u32 %[p], 3, %[p]\n\t" \
        "s_mov_b64 exec, 1\n\tds_max_u32 %[a], %[t]\n\ts_mov_b64 exec, -1\n\t.endif\n\ts_cmp_lt_u32 m0, %[l]\n\ts_cbranch_scc1 1b" \
        : [v] "+v"(v_), [p] "+v"(vp), [t] "=&v"(t_) : [b] "s"(b_), [c] "n"((q) - 1), [m] "n"(1024 * (k)), [d] "n"((k) * (st_)), \
          [a] "v"(fa), [l] "s"(lim) : "scc", "s6", "memory")
#define MARK(c, val) "s_waitcnt vmcnt(" #c ")\n\t" val "\n\ts_mov_b64 exec, 1\n\tds_max_u32 %[a], %[t]\n\ts_mov_b64 exec, -1\n\t"
      __builtin_amdgcn_s_setprio(DPRIO);
      STAMP(8);
      if (id < 126) { if (slow) RUN(4, QAS, 32768, v, base, 8 * F); else RUN(4, 8, 32768, v, base, 8 * F); }
      if (dw < 3) {
        if (id >= 126) __builtin_amdgcn_endpgm();
        gp bd = wd + (size_t)(128 * kp + 2 * dw) * D + ct * 256;
        u32 vd = (l >> 5) * (2 * D + 32) ^ 16 * (l & 31);
        asm volatile("" : "+v"(vd));
        if (slow) RUN(3, QS, 16384 * ns, v, base, 8 * F);
        else { RUN(3, 8, 32768 + 3072 * RAMP, v, base, 8 * F); RUN(3, QD, 131072, v, base, 8 * F); }
        STAMP(1);
        {
          u32 n_ = SPIN;
          asm volatile("1:\n\t" VMC MARK(63, "v_mad_i32_i24 %[t], s6, -3, %[p]") "ds_read_b32 %[t], %[f]\n\ts_sub_u32 %[n], %[n], 1\n\ts_waitcnt lgkmcnt(0)\n\t"
                       "v_cmp_lt_u32 vcc, %[k], %[t]\n\ts_cbranch_scc1 2f\n\ts_cbranch_vccz 1b\n2:\n\ts_mov_b32 m0, %[m]"
                       : [t] "=&v"(t_), [n] "+s"(n_) : [a] "v"(fa), [p] "v"(vp), [f] "v"(u == 8 ? fa : lds0 + FC + 20), [k] "s"(u == 8 ? 159 : 16 * HOLD - 105), [m] "s"(lds0 + 1024 * dw)
                       : "vcc", "scc", "s6", "memory");
        }
        STAMP(3);
        if (u < 8) {
          RUN(3, QE, 65536, vd, bd, 4 * D);
          STAMP(6);
          asm volatile(MARK(0, "v_mov_b32 %[t], -1") : [t] "=&v"(t_) : [a] "v"(fa) : "memory");
          STAMP(4);
        }
        FLUSH();
        __builtin_amdgcn_endpgm();
      }
      asm volatile(MARK(0, "v_mov_b32 %[t], -1") : [t] "=&v"(t_) : [a] "v"(fa) : "memory");
      u32 km, rk;
      u32 hi, lo, k2, k3, gd, thr, sg = 0x0c0c0b09u;
      u32 ka = lds0 + FC + 384 + 8 * (l >> 3), kc = 2047 * 2048 + 2 * r0 + 16 * ((l & 7) ^ ((l >> 2) & 4)), kq = lds0 + FC + 512 + (l & 48), kr = lds0 + FC + 512 + 4 * n16;
      u32 c1 = 896, c2 = 1792, c3 = 2688, cm = (u32)-2048, cs = O_SG + 8 * 2047, ca = lds0 + FC + 512;
      rsrc_t rx = __builtin_amdgcn_make_buffer_rsrc((void*)(x - 1344), 0, 2 * L * D + 4096, 0x00020000);
      u32 co = O_CLR + 8 * (18 * sb + (0x84c0 >> 4 * (kp & 3) & 15) + min(l, (kp & 3) == 1 ? 5 : 3));
      asm volatile("" : "+v"(ka), "+v"(kc), "+v"(kq), "+v"(kr), "+v"(co));
      asm volatile("" : "+s"(c1), "+s"(c2), "+s"(c3), "+s"(cm), "+s"(cs), "+s"(ca), "+s"(sg), "+s"(rx));
      *(u32x4*)(smem + FA + 6144 + 16 * l) = u32x4{0u, 0u, 0u, 0u};
      *(u32x4*)(smem + FA + 7168 + 16 * l) = u32x4{0u, 0u, 0u, 0u};
      CWAIT(FC + 256, 1, "s_sleep 4");
      tag = (u32)__builtin_amdgcn_readfirstlane((int)*(const volatile LDS u32*)(LDS char*)(smem + FC + 24));
      {
        u32 n_ = SPIN, t0, t1, t2, t3, se;
#define TLD(a, b_, c, d) "buffer_load_dwordx4 v[" #a ":" #b_ "], %[o], %[r], 0 offen sc1\n\tbuffer_load_dwordx4 v[" #c ":" #d "], %[o], %[r], %[ts] offen sc1\n\t"
#define TCK(r0, r1, r2, r3, r4, r5, r6, r7) "s_waitcnt vmcnt(4)\n\tv_xor_b32 %[t0], %[t], v" #r1 "\n\tv_bitop3_b32 %[t0], %[t0], %[t], v" #r3 " bitop3:0xf6\n\t" \
          "v_bitop3_b32 %[t0], %[t0], %[t], v" #r5 " bitop3:0xf6\n\tv_bitop3_b32 %[t0], %[t0], %[t], v" #r7 " bitop3:0xf6\n\t" \
          "v_cmp_ne_u32 vcc, 0, %[t0]\n\tv_mov_b32 %[hi], v" #r0 "\n\tv_mov_b32 %[lo], v" #r2 "\n\tv_mov_b32 %[k2], v" #r4 "\n\tv_mov_b32 %[k3], v" #r6 "\n\t" \
          "s_cbranch_vccz 5f\n\t" TLD(r0, r3, r4, r7)
        asm volatile("2:\n\ts_sleep 1\n\ts_memrealtime s[8:9]\n\ts_waitcnt lgkmcnt(0)\n\ts_sub_u32 s8, s8, %[st]\n\ts_cmp_lt_u32 s8, %[tw]\n\ts_cbranch_scc1 2b\n\t"
                     TLD(128, 131, 132, 135) "s_sleep " STR(RSL) "\n\t" TLD(136, 139, 140, 143) "s_sleep " STR(RSL) "\n\t" TLD(144, 147, 148, 151)
                     "1:\n\t" TCK(128, 129, 130, 131, 132, 133, 134, 135) TCK(136, 137, 138, 139, 140, 141, 142, 143) TCK(144, 145, 146, 147, 148, 149, 150, 151)
                     "s_sub_u32 %[n], %[n], 1\n\ts_cbranch_scc0 1b\n5:\n\t" TS(60, 61)
                     "v_and_or_b32 %[t0], %[hi], %[mk], %[pa]\n\tv_and_or_b32 %[t1], %[lo], %[mk], %[pb]\n\t"
                     "v_max_u32 %[t2], %[t0], %[t1]\n\tv_max_u32 %[t3], %[t0], %[t1]\n\tv_perm_b32 %[gd], %[hi], %[lo], %[p0]\n\tv_perm_b32 %[se], %[k2], %[k3], %[p0]\n\t"
                     "v_permlane16_swap_b32 %[t2], %[t3]\n\tv_max_u32 %[km], %[t2], %[t3]\n\tv_max_u32 %[rk], %[t2], %[t3]\n\t"
                     "v_max_u32 %[hi], %[t0], %[t1]\n\tv_min_u32 %[lo], %[t0], %[t1]\n\t"
                     "v_permlane32_swap_b32 %[km], %[rk]\n\tv_max_u32 %[km], %[km], %[rk]\n\tv_not_b32 %[t2], %[km]\n\t"
                     "v_and_or_b32 %[k2], %[k2], %[mk], %[pa]\n\t"
                     "v_not_b32_dpp %[t2], %[km] row_ror:4 row_mask:0x2 bank_mask:0xf\n\tv_not_b32_dpp %[t2], %[km] row_ror:8 row_mask:0x4 bank_mask:0xf\n\t"
                     "v_not_b32_dpp %[t2], %[km] row_ror:12 row_mask:0x8 bank_mask:0xf\n\t"
                     "v_pk_lshlrev_b16 %[gd], 4, %[gd] op_sel_hi:[0,1]\n\tv_add_u32 %[t0], %[km], %[t2]\n\t"
                     "v_add_u32_dpp %[t1], %[t2], %[km] row_ror:1 row_mask:0xf bank_mask:0xf\n\tv_add_u32_dpp %[t3], %[t2], %[km] row_ror:2 row_mask:0xf bank_mask:0xf\n\t"
                     "v_add_u32_dpp %[t2], %[t2], %[km] row_ror:3 row_mask:0xf bank_mask:0xf\n\t"
                     RANK8("%[t0]", "%[t1]", "%[t2]", "%[t3]", "%[rk]", "%[t3]", "-2", "v_lshrrev_b32 %[se], 4, %[se]\n\tv_perm_b32 %[gd], %[gd], %[se], %[p1]",
                           "v_pk_max_u16 %[gd], %[gd], %[gd] op_sel:[0,1] op_sel_hi:[1,0]\n\tv_and_or_b32 %[k3], %[k3], %[mk], %[pb]")
                     "v_cmp_eq_u32 vcc, 56, %[rk]\n\tv_max3_u32 %[se], %[lo], %[k2], %[k3]\n\tv_lshrrev_b32 %[gd], 1, %[gd]\n\ts_ff1_i32_b32 s6, vcc_lo\n\t"
                     "v_readlane_b32 s6, %[km], s6\n\tv_or_b32 %[gd], 0x7fff, %[gd]\n\tv_med3_u32 %[t2], %[lo], %[k2], %[k3]\n\t"
                     "v_cmp_le_u32 s[8:9], s6, %[hi]\n\tv_cmp_le_u32 s[10:11], s6, %[se]\n\tv_max_u32 %[t2], %[t2], %[gd]\n\t"
                     "v_mbcnt_lo_u32_b32 %[t0], s8, 0\n\tv_mbcnt_lo_u32_b32 %[t1], s10, -15\n\tv_mbcnt_hi_u32_b32 %[t0], s9, %[t0]\n\tv_mbcnt_hi_u32_b32 %[t1], s11, %[t1]\n\t"
                     "v_lshl_add_u32 %[t0], %[t0], 2, %[ca]\n\tv_mad_i32_i24 %[t1], %[t1], -4, %[ca]\n\tv_cmp_le_u32 vcc, s6, %[t2]\n\t"
                     "s_mov_b64 exec, s[8:9]\n\tds_write_b32 %[t0], %[hi]\n\ts_mov_b64 exec, s[10:11]\n\tds_write_b32 %[t1], %[se]\n\ts_mov_b64 exec, -1\n\t"
                     "ds_read_b32 %[se], %[kr]\n\tds_read_b128 v[152:155], %[kq]\n\t"
                     "s_bcnt1_i32_b64 s8, s[8:9]\n\ts_bcnt1_i32_b64 s10, s[10:11]\n\ts_add_u32 s8, s8, s10\n\ts_cmp_gt_u32 s8, 16\n\ts_cselect_b64 s[8:9], -1, vcc\n\t"
                     "s_cmp_lg_u64 s[8:9], 0\n\ts_waitcnt lgkmcnt(0)\n\ts_cbranch_scc1 6f\n\t"
                     "v_sub_u32 %[t0], %[se], v152\n\tv_sub_u32 %[t1], %[se], v153\n\tv_sub_u32 %[t2], %[se], v154\n\tv_sub_u32 %[t3], %[se], v155\n\t"
                     RANK8("%[t0]", "%[t1]", "%[t2]", "%[t3]", "%[rk]", "%[t3]", "0", "v_mov_b32 %[km], %[se]\n\ts_nop 0", "s_nop 1") "6:\n\t" TS(62, 63)
                     : [hi] "=&v"(hi), [lo] "=&v"(lo), [k2] "=&v"(k2), [k3] "=&v"(k3), [gd] "=&v"(gd), [se] "=&v"(se), [km] "=&v"(km), [rk] "=&v"(rk),
                       [t0] "=&v"(t0), [t1] "=&v"(t1), [t2] "=&v"(t2), [t3] "=&v"(t3), [n] "+s"(n_)
                     : [o] "v"(O_TOP + 16 * (l & 7) + TL * (l >> 3)), [r] "s"(rs), [t] "s"(tag), [ts] "s"(TOPS), [st] "s"(st0), [tw] "n"(TW), [mk] "s"(0x7fff800fu),
                       [pa] "v"((127 - 2 * l) << 4), [pb] "v"((126 - 2 * l) << 4), [sg] "s"(sg), [p0] "s"(0x05040100u), [p1] "s"(0x07020500u), [ca] "s"(ca),
                       [kr] "v"(kr), [kq] "v"(kq)
                     : "vcc", "scc", "s6", "s8", "s9", "s10", "s11", "memory" TSC);
      }
      unsigned long long more;
      asm volatile("" : "={s6}"(thr), "={s[8:9]}"(more));
      if (__builtin_expect(more != 0, 0)) {
        const u32 m0 = km;
        unsigned long long all = __ballot(gd >= thr);
        if (gd >= thr) hi = lo = k2 = k3 = 0u;
#pragma unroll 1
        for (int i = 0; i < 4;) {
          u32 k, m = m0;
          if (all) {
            const int a = __builtin_ctzll(all);
            all &= all - 1;
            u32x2 r;
            const u32 o = O_RG + 256 * a + 8 * (l & 31);
            POLL8(r, o);
            k = l < 32 ? r.x : 0u;
            m = (u32)__builtin_amdgcn_readlane((int)m0, a);
          } else { k = hi; hi = lo; lo = k2; k2 = k3; i++; }
          asm volatile("v_cmp_le_u32 s[8:9], %[t], %[k]\n\tv_cmp_ne_u32 vcc, %[k], %[m0]\n\ts_and_b64 s[8:9], s[8:9], vcc\n\ts_cbranch_scc0 2f\n"
                       "1:\n\ts_ff1_i32_b64 s7, s[8:9]\n\tv_readlane_b32 s12, %[k], s7\n\ts_lshl_b64 s[10:11], 1, s7\n\ts_nop 0\n\t"
                       "v_cmpx_gt_u32 vcc, s12, %[km]\n\tv_add_u32 %[rk], 8, %[rk]\n\ts_bcnt0_i32_b32 s7, vcc_lo\n\tv_cmpx_eq_u32 vcc, 0x80, %[rk]\n\t"
                       "v_mov_b32 %[km], s12\n\tv_lshlrev_b32 %[rk], 2, s7\n\ts_mov_b64 exec, -1\n\ts_andn2_b64 s[8:9], s[8:9], s[10:11]\n\ts_cbranch_scc1 1b\n2:"
                       : [km] "+v"(km), [rk] "+v"(rk) : [k] "v"(k), [m0] "v"(m), [t] "s"(thr) : "vcc", "scc", "s7", "s8", "s9", "s10", "s11", "s12");
        }
      }
      if (id < 126) {
        u32 a_, v_, n_ = SPIN;
        u32x2 cl;
#define RLD(o, s_) "buffer_load_dwordx4 %[v], %[rx], " s_ " offen offset:" #o " lds\n\t"
        asm volatile("v_add_u32 %[a], %[kb], %[rk]\n\tv_and_b32 %[v], 0x7ff, %[km]\n\ts_mov_b32 m0, %[fa]\n\tds_write_b32 %[a], %[v]\n\tds_read_b32 %[a], %[ka]\n\t"
                     "s_cmp_eq_u32 %[ns], 8\n\ts_waitcnt lgkmcnt(0)\n\tv_mad_i32_i24 %[v], %[a], %[cm], %[kc]\n\t"
                     RLD(0, "%[c3]") RLD(1024, "%[c2]") RLD(2048, "%[c1]") RLD(3072, "0")
                     "s_mov_b32 m0, %[fb]\n\tv_add_u32 %[v], 0x200, %[v]\n\t"
                     RLD(0, "%[c3]") RLD(1024, "%[c2]") "s_cbranch_scc0 3f\n\t" RLD(2048, "%[c1]") RLD(3072, "0")
                     "3:\n\tv_mad_i32_i24 %[a], %[a], -8, %[cs]\n\tbuffer_load_dwordx2 v[160:161], %[a], %[rs], 0 offen sc1\n\tbuffer_load_dwordx2 %[cl], %[co], %[rs], 0 offen sc1\n\t"
                     "v_mov_b32 %[v], 16 * " STR(HOLD) " - 112\n\tv_subrev_u32 %[ka], %[kd], %[ka]\n\ts_mov_b64 exec, 1\n\tds_add_u32 %[fc], %[v] offset:8\n\ts_mov_b64 exec, -1\n\tv_mov_b32 %[v], -1\n\t" TS(64, 65)
                     "s_waitcnt vmcnt(1)\n1:\n\tv_cmp_ne_u32 vcc, %[t], v161\n\tv_and_b32 v156, 0xffff0000, v160\n\tv_lshlrev_b32 v157, 16, v160\n\tv_rcp_f32 v156, v156\n\t"
                     "s_sub_u32 %[n], %[n], 1\n\ts_cbranch_vccz 2f\n\ts_cbranch_scc1 2f\n\t"
                     "buffer_load_dwordx2 v[160:161], %[a], %[rs], 0 offen sc1\n\ts_waitcnt vmcnt(0)\n\ts_branch 1b\n2:\n\tv_mul_f32 v157, -v157, v156\n\t"
                     "ds_write_b64 %[ka], v[156:157]\n\tds_write_b32 %[fc], %[v] offset:4\n\t" TS(66, 67) "s_waitcnt vmcnt(0)"
                     : [a] "=&v"(a_), [v] "=&v"(v_), [ka] "+v"(ka), [n] "+s"(n_), [cl] "=&v"(cl)
                     : [co] "v"(co), [rk] "v"(rk), [km] "v"(km), [kc] "v"(kc), [kb] "n"(FC + 384), [kd] "n"(384 - 32), [fa] "n"(FA), [fb] "n"(FA + 4096), [rx] "s"(rx), [rs] "s"(rs),
                       [c1] "s"(c1), [c2] "s"(c2), [c3] "s"(c3), [cm] "s"(cm), [cs] "s"(cs), [ns] "s"(ns), [fc] "v"(fa), [t] "s"(tag) : "vcc", "scc", "memory" TSC);
        TSGET(12, 13, 14, 15);
#pragma unroll 1
        for (int spin = 0; spin < SPIN; spin++) {
          if (__all(cl.y == tag)) break;
          asm volatile("s_sleep " STR(CNAP) ::: "memory");
          cl = ld8(rs, co);
        }
        FLAG(FC + 28, 1u);
        STAMP(10);
      } else {
        const u32 cd = km << 1;
        const u32x2 r = {(2047u - (km & 0x7ffu)) | ((cd ^ ((u32)((int)~cd >> 15) | 0x80000000u)) & 0xffff0000u), tag};
        const u32 o = rk < 64u ? O_IDX + 1024 * mat + 128 * kg + rk : 0x80000000u;
        st8(rs, o, r);
        st8(rs, o + 512, r);
        TSGET(12, 13, 14, 15);
        STAMP(14);
        STAMP(15);
        if (mat) {
#pragma unroll 1
          for (int spin = 0; spin < SPIN; spin++) {
            const u32x4 a = ld16(rs, O_ARR + 16 * l);
            if (__all(a.y == tag && a.w == tag)) break;
            asm volatile("s_sleep 16" ::: "memory");
          }
          if (l == 0) __builtin_amdgcn_raw_buffer_store_b32(tag, rs, 0, 0, 16);
        }
      }
      FLUSH();
      __builtin_amdgcn_endpgm();
    }
    u32x4 xv[4];
    u32x8 wv;
    ROLE(l, "", , "={v[100:103]}"(xv[0]), "={v[104:107]}"(xv[1]), "={v[108:111]}"(xv[2]), "={v[112:115]}"(xv[3]), "={v[116:123]}"(wv));
    STAMP(11);
    u32 sg = 0x0c0c0b09u;
    asm volatile("" : "+s"(sg));
    ln_rows(xv, wv, rs, tag, b, g, l, lds0 + FC + 128, sg TLVAL);
    STAMP(10);
    IDS();
    if (id >= 126) {
      FLUSH();
      __builtin_amdgcn_endpgm();
    }
    const int wr = 16 * (kg >> 1) + 4 * (kg & 1) + (n16 & 8) + ((n16 >> 2) & 1);
    u32 wa = lds0 + 256 * wr + ((w ^ ((wr & 1) | ((wr >> 1) & 6))) << 5) + 8 * (l & 3), sel = (l & 8) ? 0xeeeeu : 0x4444u;
    u32 so = 4 * t;
    asm volatile("" : "+v"(wa), "+v"(sel), "+v"(so));
    asm volatile(UNTIL(STR(CLD)) :: [st] "s"(st0) : "s8", "s9", "scc");
    __builtin_amdgcn_raw_buffer_store_b32(~0u, rs, so, O_HG + (6 * cb + 3 * mat + part) * 2048, 16);
    st8(rs, u < 8 ? O_PG + (4 * kp + ct) * 4096 + 2 * so : 0x80000000u, u32x2{~0u, ~0u});
#define SWP(a0, a1, a2, a3) "v_permlane32_swap_b32 " a0 ", " a2 "\n\tv_permlane32_swap_b32 " a1 ", " a3 "\n\t"
#define OUT(a0, a1, o) "v_cvt_pk_bf16_f32 " a0 ", " a0 ", " a1 "\n\tv_min_u32 " a0 ", -2, " a0 "\n\tbuffer_store_dword " a0 ", %[so], %[rs], " o " sc1\n\t"
#define TR4(a, b_, c, d, e, f, g_, h, adr, o) "ds_read_b64_tr_b16 v[" #a ":" #b_ "], " adr " offset:" #o "\n\tds_read_b64_tr_b16 v[" #c ":" #d "], " adr " offset:" #o "+512\n\t" \
      "ds_read_b64_tr_b16 v[" #e ":" #f "], " adr " offset:" #o "+8192\n\tds_read_b64_tr_b16 v[" #g_ ":" #h "], " adr " offset:" #o "+8704\n\t"
#define SM(d, a, b_) "v_smfmac_f32_16x16x64_bf16 v[" d "], v[" a "], v[" b_ "], %[sel]\n\t"
#define CALL "s_call_b64 s[10:11], 1f\n\t"
#define USE(c, xa, wf) "s_add_u32 s6, s6, 16\n\ts_waitcnt lgkmcnt(" #c ")\n\t" SM("16:19", "20:23", wf) SM("8:11", xa, wf)
#define XR(xa, o) "ds_read_b128 v[" xa "], %[xa] offset:" #o "\n\t"
#define XU(c, xa, wf) "s_waitcnt lgkmcnt(" #c ")\n\t" SM("8:11", xa, wf)
#define ZERO(r) "v_mfma_f32_32x32x16_bf16 v[" r "], v[8:11], v[8:11], 0\n\t"
    asm volatile("v_mov_b64 v[8:9], 0\n\tv_mov_b64 v[10:11], 0\n\tv_mov_b64 v[16:17], 0\n\tv_mov_b64 v[18:19], 0\n\t"
                 ZERO("32:47") ZERO("48:63") ZERO("64:79") ZERO("80:95") ZERO("96:111") ZERO("112:127")
                 "v_mov_b32 v20, 0x3f803f80\n\tv_mov_b32 v21, v20\n\tv_mov_b64 v[22:23], v[20:21]\n\tv_mov_b32 v13, %[t]\n\tv_mov_b32 v28, 1\n\t"
                 "v_add_u32 v29, 0x10000, %[wa]\n\ts_movk_i32 s6, 96\n\ts_mov_b32 s7, 1\n\ts_mov_b32 s8, 15\n\ts_mov_b32 s9, %[spin]\n\ts_nop 7\n\t"
                 CALL TR4(32, 33, 34, 35, 36, 37, 38, 39, "%[wa]", 0) TR4(40, 41, 42, 43, 44, 45, 46, 47, "%[wa]", 16384)
                 USE(4, "96:99", "32:39") "s_waitcnt lgkmcnt(0)\n\t" SM("16:19", "20:23", "40:47") SM("8:11", "100:103", "40:47")
                 CALL TR4(48, 49, 50, 51, 52, 53, 54, 55, "%[wa]", 32768) USE(0, "104:107", "48:55")
                 CALL TR4(56, 57, 58, 59, 60, 61, 62, 63, "%[wa]", 49152) "s_waitcnt vmcnt(0)\n\ts_mov_b64 exec, 1\n\tds_add_rtn_u32 v28, %[fl], v28 offset:20\n\ts_mov_b64 exec, -1\n\t" TS(60, 61)
                 USE(0, "108:111", "56:63") "v_and_b32 v28, 15, v28\n\tv_cmpx_eq_u32 vcc, 7, v28\n\tbuffer_store_dwordx2 v[12:13], off, %[rs], %[co] sc1\n\ts_mov_b64 exec, -1\n\t"
                 CALL TR4(64, 65, 66, 67, 68, 69, 70, 71, "v29", 0) USE(0, "112:115", "64:71") "s_cmp_eq_u32 %[ns], 5\n\ts_cbranch_scc1 5f\n\t"
                 CALL TR4(72, 73, 74, 75, 76, 77, 78, 79, "v29", 16384) USE(0, "116:119", "72:79") "s_cmp_eq_u32 %[ns], 6\n\ts_cbranch_scc1 5f\n\t"
                 CALL TR4(80, 81, 82, 83, 84, 85, 86, 87, "v29", 32768) USE(0, "120:123", "80:87")
                 CALL TR4(88, 89, 90, 91, 92, 93, 94, 95, "v29", 49152) USE(0, "124:127", "88:95")
                 "5:\n\t" TS(62, 63) "s_cmp_eq_u32 s7, 0\n\ts_cbranch_scc1 4f\n\ts_mov_b32 s8, 16\n"
                 "1:\n\tds_read_b32 v30, %[fl]\n\ts_waitcnt lgkmcnt(0)\n\tv_cmp_le_u32 vcc, s6, v30\n\ts_and_b32 s12, vcc_hi, s7\n\ts_cbranch_scc1 3f\n"
                 "2:\n\ts_andn2_b32 s12, s8, vcc_lo\n\ts_cbranch_scc1 6f\n\ts_setpc_b64 s[10:11]\n"
                 "6:\n\ts_sub_u32 s9, s9, 1\n\ts_cbranch_scc1 4f\n\t" GNAP "\n\ts_branch 1b\n"
                 "3:\n\t" XR("96:99", 0) XR("100:103", 1024) XR("104:107", 2048) XR("108:111", 3072) XR("112:115", 4096) XR("116:119", 5120) XR("120:123", 6144) XR("124:127", 7168)
                 "ds_read_b128 v[24:27], %[pa]\n\t" TS(64, 65) "s_mov_b32 s7, 0\n\t"
                 XU(8, "96:99", "32:39") XU(7, "100:103", "40:47") XU(6, "104:107", "48:55") XU(5, "108:111", "56:63")
                 XU(4, "112:115", "64:71") XU(3, "116:119", "72:79") XU(2, "120:123", "80:87") XU(0, "124:127", "88:95")
                 "s_cmp_eq_u32 s8, 16\n\ts_cbranch_scc0 2b\n"
                 "4:\n\ts_nop 4\n\tv_permlane32_swap_b32 v16, v17\n\tv_add_f32 v16, v16, v17\n\tv_mul_f32 v25, v25, v16\n\tv_mul_f32 v27, v27, v16\n\t"
                 SWP("v8", "v9", "v10", "v11") "v_fmac_f32 v25, v10, v24\n\tv_fmac_f32 v27, v11, v26\n\tv_fma_f32 v8, v8, v24, v25\n\tv_fma_f32 v9, v9, v26, v27\n\t"
                 OUT("v8", "v9", "%[ho] offen") TS(66, 67)
                 :: [wa] "v"(wa), [sel] "v"(sel), [so] "v"(so), [fl] "v"(lds0 + FC + 4 * min(l, 4)), [xa] "v"(lds0 + FA + 128 * (l & 7) + 16 * ((2 * kg + (n16 >> 3)) ^ (2 * l & 4))),
                    [pa] "v"(lds0 + FC + 32 + 32 * (kg & 1) + 16 * (kg >> 1)), [ns] "s"(ns), [t] "s"(tag), [co] "s"(O_CLR + 8 * id), [rs] "s"(rs), [ho] "s"(O_HG + (6 * cb + 3 * mat + part) * 2048), [spin] "n"(SPIN)
                 : "vcc", "scc", "s6", "s7", "s8", "s9", "s10", "s11", "s12", "memory", G_CLOBBER TSC);
    TSGET(1, 4, 3, 5);
    if (u < 8) {
      const int tp = 2 * (kg & 1) + (kg >> 1), r7 = 4 * (kg & 1) + (n16 >> 2);
      u32 o3 = (kp & 3) == 1 ? so + 4096 : 0x80000000u, h0 = O_HG + kp * 12288, h1 = h0 + 6144;
      u32 hw = lds0 + FH + 2 * tp * HP + 32 * w + 8 * (l & 2) + (l & 4) * 2 + ((l & 8) >> 1) + 2 * (l & 1);
      u32 hr = lds0 + FH + (l & 7) * HP + 32 * kg + 2 * (l & 8);
      u32 da = lds0 + 512 * (16 * (kg >> 1) + r7) + ((2 * w ^ r7) << 5) + 8 * (l & 3), db = da ^ 32, fl = lds0 + FC + 4 * (l & 3);
      u32 po = 512 * w + 16 * (l & 8) + 32 * tp + 4 * (l & 7);
      u32 e0 = 0x3f80u, e1 = 0x3f800000u;
      asm volatile("" : "+v"(hw), "+v"(hr), "+v"(da), "+v"(db), "+v"(fl), "+v"(po), "+v"(o3));
      asm volatile("" : "+s"(e0), "+s"(e1), "+s"(h0), "+s"(h1));
#define TRD(a, b_, c, d, e, f, g_, h, adr, o) "ds_read_b64_tr_b16 v[" #a ":" #b_ "], " adr " offset:" #o "\n\tds_read_b64_tr_b16 v[" #c ":" #d "], " adr " offset:" #o "+4096\n\t" \
      "ds_read_b64_tr_b16 v[" #e ":" #f "], " adr " offset:" #o "+16384\n\tds_read_b64_tr_b16 v[" #g_ ":" #h "], " adr " offset:" #o "+20480\n\t"
#define HLD(X, r) X(r "0", "%[s1]", "%[h0] offen") X(r "3", "%[s1]", "%[h1] offen") X(r "1", "%[s1]", "%[h0] offen offset:2048") X(r "4", "%[s1]", "%[h1] offen offset:2048") \
      X(r "2", "%[o3]", "%[h0] offen") X(r "5", "%[o3]", "%[h1] offen")
#define HL0(a, o, s_) "buffer_load_dword " a ", " o ", %[rs], " s_ " sc1\n\t"
#define HL1(a, o, s_) "v_cmpx_eq_u32 vcc, -1, " a "\n\t" HL0(a, o, s_) "s_mov_b64 exec, -1\n\t"
#define DOT(d, e, a) "v_dot2c_f32_bf16 " d ", %[" e "], " a "\n\t"
#define HCK(c, r) "s_cmp_eq_u32 s7, 0\n\ts_cbranch_scc1 5f\n\ts_call_b64 s[10:11], 4f\n5:\n\ts_waitcnt vmcnt(" #c ")\n\tv_max3_u32 v28, " r "0, " r "1, " r "2\n\tv_lshlrev_b32 v24, 16, " r "0\n\tv_max3_u32 v28, v28, " r "3, " r "4\n\t" \
      "v_and_b32 v25, %[hm], " r "0\n\tv_max_u32 v28, v28, " r "5\n\t" DOT("v24", "e0", r "1") "v_cmp_eq_u32 vcc, -1, v28\n\t" DOT("v25", "e1", r "1") DOT("v24", "e0", r "2") DOT("v25", "e1", r "2") \
      "v_lshlrev_b32 v26, 16, " r "3\n\tv_and_b32 v27, %[hm], " r "3\n\t" DOT("v26", "e0", r "4") DOT("v27", "e1", r "4") DOT("v26", "e0", r "5") DOT("v27", "e1", r "5")
#define RB2(a, b_) "v_cvt_pk_bf16_f32 " a ", 0, " a "\n\tv_cvt_pk_bf16_f32 " b_ ", 0, " b_ "\n\t"
      asm volatile("s_mov_b32 s7, 1\n\ts_mov_b32 s12, %[spin]\n\tv_mov_b64 v[8:9], 0\n\tv_mov_b64 v[10:11], 0\n\tv_mov_b64 v[12:13], 0\n\tv_mov_b64 v[14:15], 0\n"
                   "2:\n\ts_sleep 1\n\tds_read_b32 v28, %[fl] offset:16\n\ts_memrealtime s[8:9]\n\ts_waitcnt lgkmcnt(0)\n\tv_cmp_eq_u32 vcc, 0, v28\n\ts_sub_u32 s8, s8, %[st]\n\t"
                   "s_cmp_lt_u32 s8, %[th]\n\ts_cbranch_scc1 2b\n\ts_bitcmp1_b32 vcc_lo, 3\n\ts_cbranch_scc1 2b\n\t" TS(64, 65)
                   HLD(HL0, "v7") HCK(0, "v7") "s_cbranch_vccnz 9f\n"
                   "3:\n\t" TS(66, 67) RB2("v24", "v25") "v_mul_f32 v28, %[le], v24\n\tv_mul_f32 v29, %[le], v25\n\tv_exp_f32 v28, v28\n\tv_exp_f32 v29, v29\n\t" RB2("v26", "v27")
                   "v_add_f32 v28, 1.0, v28\n\tv_add_f32 v29, 1.0, v29\n\tv_rcp_f32 v28, v28\n\tv_rcp_f32 v29, v29\n\ts_cmp_eq_u32 s7, 0\n\tv_mul_f32 v24, v24, v28\n\tv_mul_f32 v25, v25, v29\n\t"
                   RB2("v24", "v25") "v_mul_f32 v24, v24, v26\n\tv_mul_f32 v25, v25, v27\n\tv_cvt_pk_bf16_f32 v24, v24, v25\n\ts_mov_b32 s12, %[spin]\n\t"
                   "ds_write_b16 %[hw], v24\n\tds_write_b16_d16_hi %[hw], v24 offset:%[hp]\n\ts_cbranch_scc1 8f\n"
                   "7:\n\ts_call_b64 s[10:11], 4f\n\ts_mul_i32 s12, s12, s7\n\ts_sub_u32 s12, s12, 1\n\ts_cbranch_scc0 7b\n"
                   "8:\n\ts_waitcnt lgkmcnt(0)\n\ts_barrier\n\t" TS(60, 61)
                   XR("16:19", 0) XR("20:23", 128)
                   XU(1, "16:19", "32:39") SM("12:15", "16:19", "40:47") XU(0, "20:23", "48:55") SM("12:15", "20:23", "56:63") "s_nop 6\n\t"
                   SWP("v8", "v9", "v10", "v11") "v_add_f32 v8, v8, v10\n\tv_add_f32 v9, v9, v11\n\t" SWP("v12", "v13", "v14", "v15") OUT("v8", "v9", "%[po] offen")
                   "v_add_f32 v12, v12, v14\n\tv_add_f32 v13, v13, v15\n\t" OUT("v12", "v13", "%[po] offen offset:256") TS(62, 63) DONE
                   "9:\n\tv_mov_b64 v[80:81], v[70:71]\n\tv_mov_b64 v[82:83], v[72:73]\n\tv_mov_b64 v[84:85], v[74:75]\n\t" HLD(HL1, "v7") "s_sleep " STR(HNAP) "\n"
                   "1:\n\t" HLD(HL1, "v8") HCK(6, "v7") "s_cbranch_vccz 3b\n\t" HLD(HL1, "v7") HCK(6, "v8") "s_sub_u32 s12, s12, 1\n\ts_cbranch_vccz 3b\n\ts_cbranch_scc0 1b\n\ts_branch 3b\n"
                   "4:\n\tds_read_b32 v28, %[fl]\n\ts_waitcnt lgkmcnt(0)\n\tv_cmp_ne_u32 vcc, -1, v28\n\ts_cbranch_vccnz 6f\n\t"
                   TRD(32, 33, 34, 35, 36, 37, 38, 39, "%[da]", 0) TRD(40, 41, 42, 43, 44, 45, 46, 47, "%[db]", 0)
                   TRD(48, 49, 50, 51, 52, 53, 54, 55, "%[da]", 32768) TRD(56, 57, 58, 59, 60, 61, 62, 63, "%[db]", 32768) "s_mov_b32 s7, 0\n"
                   "6:\n\ts_setpc_b64 s[10:11]\n0:"
                   :: [s1] "v"(so), [o3] "v"(o3), [fl] "v"(fl), [da] "v"(da), [db] "v"(db), [hw] "v"(hw), [xa] "v"(hr), [sel] "v"(sel), [so] "v"(po), [rs] "s"(rs), [h0] "s"(h0), [h1] "s"(h1),
                      [st] "s"(st0), [th] "n"(TH), [spin] "n"(SPIN), [hp] "n"(HP), [po] "s"(O_PG + (4 * kp + ct) * 4096), [e0] "s"(e0), [e1] "s"(e1), [hm] "s"(0xffff0000u),
                      [le] "s"(-1.4426950408889634f)
                   : "vcc", "scc", "s7", "s8", "s9", "s10", "s11", "s12", "memory", G_CLOBBER TSC);
      TSGET(8, 9, 6, 7);
    }
    FLUSH();
    __builtin_amdgcn_endpgm();
  }

  if (w0 >= 8) {
    u32x8 xa0, xa1, xa2, xa3, sa1;
    u32x4 junk;
    u32 op, so;
    ROLE(p, "", , "={v[100:107]}"(xa0), "={v[108:115]}"(xa1), "={v[116:123]}"(xa2),
         "={v148}"(op), "={v149}"(so), "={v[160:163]}"(junk));
    const int p = w - 8;
    float sc[8];
    __builtin_amdgcn_s_setprio(3);
    asm volatile("s_waitcnt lgkmcnt(0)" : "+{s44}"(tag), "+{s[48:51]}"(dv)::"memory");
    tag += 1u;
    STAMP(9);
    f32x2 xq[4][8];
    u32x8 pa;
    {
#define SCK(r1, r3, r5, r7) "v_xor_b32 %[d], %[t], v" #r1 "\n\tv_bitop3_b32 %[d], %[d], %[t], v" #r3 " bitop3:0xf6\n\t" \
      "v_bitop3_b32 %[d], %[d], %[t], v" #r5 " bitop3:0xf6\n\tv_bitop3_b32 %[d], %[d], %[t], v" #r7 " bitop3:0xf6\n\t"
#define SLD(r0, r3, r4, r7, k, k1) "buffer_load_dwordx4 v[" #r0 ":" #r3 "], %[so], %[rs], " k " offen sc1\n\tbuffer_load_dwordx4 v[" #r4 ":" #r7 "], %[so], %[rs], " k1 " offen sc1\n\t"
#define SLOOP(r0, r1, r3, r4, r5, r7, k, k1) "1:\n\ts_waitcnt vmcnt(0)\n4:\n\t" SCK(r1, r3, r5, r7) "s_sub_u32 %[n], %[n], 1\n\tv_cmp_ne_u32 vcc, 0, %[d]\n\ts_cbranch_scc1 2f\n\ts_cbranch_vccz 2f\n\t" \
      SLD(r0, r3, r4, r7, k, k1) "s_branch 1b\n2:\n\t"
      u32 d_, n_ = SPIN, k1 = 4096, k2 = 8192, k3 = 12288, s0[4];
      asm volatile("s_mov_b32 m0, %[m]\n\ts_cmp_eq_u32 %[k], 3\n\ts_waitcnt vmcnt(2)\n\ts_cbranch_scc0 1f\n\ts_waitcnt vmcnt(0)\n1:"
                   : "+{v[100:107]}"(xa0), "+{v[108:115]}"(xa1), "+{v[160:163]}"(junk) : [m] "s"(lds0 + OX + 2 * XO + 256 * p), [k] "s"(xk) : "scc", "memory");
      SB();
#define UNPACK(i, xs) _Pragma("unroll") for (int c = 0; c < 8; c++) { \
        xq[i][c] = (c & 1) ? f32x2{hi2f(xs[c >> 1]), hi2f(xs[4 + (c >> 1)])} : f32x2{lo2f(xs[c >> 1]), lo2f(xs[4 + (c >> 1)])}; asm volatile("" : "+v"(xq[i][c].x), "+v"(xq[i][c].y)); }
      UNPACK(0, xa0);
      UNPACK(1, xa1);
#pragma unroll
      for (int c = 0; c < 8; c++) asm volatile("v_add_f32 %0, 1.0, %1" : "=v"(sc[c]) : "s"((c & 1) ? dv[c >> 1] & 0xffff0000u : dv[c >> 1] << 16));
      u32 fa = lds0 + OXF + 4 * p, fv = 1;
      asm volatile("" : "+v"(fa), "+v"(fv));
      asm volatile("" : "+s"(k1), "+s"(k2), "+s"(k3), "+s"(n_));
      SB();
      STAMP(1);
#define SCP(r0, r1, r2, r3, r4, r5, r6, r7) "s_waitcnt vmcnt(2)\n\t" SCK(r1, r3, r5, r7) "v_cmp_ne_u32 vcc, 0, %[d]\n\tv_mov_b32 %[a0], v" #r0 "\n\tv_mov_b32 %[a1], v" #r2 "\n\t" \
      "v_mov_b32 %[a2], v" #r4 "\n\tv_mov_b32 %[a3], v" #r6 "\n\ts_cbranch_vccz 3f\n\t" SLD(r0, r3, r4, r7, "0", "%[k1]")
      asm volatile(UNTIL(STR(TSG)) SLD(132, 135, 136, 139, "0", "%[k1]") "s_sleep " STR(SSL) "\n\t" SLD(140, 143, 144, 147, "0", "%[k1]")
                   "1:\n\t" SCP(132, 133, 134, 135, 136, 137, 138, 139) SCP(140, 141, 142, 143, 144, 145, 146, 147) "s_sub_u32 %[n], %[n], 1\n\ts_cbranch_scc0 1b\n"
                   "3:\n\t" SLD(140, 143, 144, 147, "%[k2]", "%[k3]")
                   : [d] "=&v"(d_), [n] "+s"(n_), [a0] "=&v"(s0[0]), [a1] "=&v"(s0[1]), [a2] "=&v"(s0[2]), [a3] "=&v"(s0[3]), "=&{v[132:139]}"(pa), "=&{v[140:147]}"(sa1)
                   : [st] "s"(st0), [t] "s"(tag), [so] "v"(so), [rs] "s"(rs), [k1] "s"(k1), [k2] "s"(k2), [k3] "s"(k3) : "vcc", "scc", "s8", "s9", "memory");
#pragma unroll
      for (int i = 0; i < 5; i++) {
#define XLD(r0, r1, r2, r3, o_) "v_add_u32 %[d], " #o_ ", %[o]\n\tglobal_load_dwordx4 v[" #r0 ":" #r1 "], %[d], %[x]\n\tglobal_load_dwordx4 v[" #r2 ":" #r3 "], %[d], %[x] offset:2048\n"
        if (i == XI) asm volatile("s_cmp_eq_u32 %[k], 3\n\ts_cbranch_scc0 1f\n\t" XLD(116, 119, 120, 123, 0x200000) "1:\n\t" XLD(124, 127, 128, 131, 0x300000)
                                 : [d] "=&v"(d_), "+{v[116:123]}"(xa2), "=&{v[124:131]}"(xa3) : [k] "s"(xk), [o] "v"(op), [x] "s"(x) : "scc", "memory");
        if (i == 2) {
          asm volatile("s_waitcnt vmcnt(2)\n\ts_branch 4f\n" SLOOP(140, 141, 143, 144, 145, 147, "%[k2]", "%[k3]") : [d] "=&v"(d_), [n] "+s"(n_), "+{v[140:147]}"(sa1), "+{v[116:123]}"(xa2), "+{v[132:139]}"(pa)
                       : [t] "s"(tag), [so] "v"(so), [rs] "s"(rs), [k2] "s"(k2), [k3] "s"(k3) : "vcc", "scc", "memory");
          UNPACK(2, xa2);
        }
        if (i == 3) asm volatile("s_waitcnt vmcnt(%1)" : "+{v[124:131]}"(xa3) : "n"(DPI == 2 ? DPF : 0));
        if (i == DPI && DPF) {
          u32 lx = l;
          asm volatile("" : "+v"(lx));
          const u32 ti = min(16 * xk + 4 * p + (lx >> 4), 125u), tsb = __umul24(ti, 57) >> 10, tu = min(ti - __umul24(18, tsb), 15u);
          const u32 o = (128 * (4 * tsb + (tu >> 2)) + DPO / 2 + 4 * j + ((lx >> 2) & 3)) * (2 * D) + 512 * (tu & 3) + 128 * (lx & 3);
          if (DPI == 4) AWAIT(OXF + 4 * (l & 3), 3, "s_sleep 1");
          if (DPF == 1) asm volatile("global_load_dword v160, %1, %2" : "=&{v[160:163]}"(junk) : "v"(o), "s"(wd) : "memory");
          else asm volatile("global_load_dword v160, %1, %2\n\tglobal_load_dword v161, %1, %3" : "=&{v[160:163]}"(junk) : "v"(o), "s"(wd), "s"(wd + 64 * D) : "memory");
        }
        if (i == 4) break;
        if (i == 3) UNPACK(3, xa3);
        const u32 sa = i < 2 ? s0[2 * i] : sa1[4 * i - 8], sb = i < 2 ? s0[2 * i + 1] : sa1[4 * i - 6];
        if (i == 0) STAMP(3);
        if (i == 2) STAMP(10);
        const f32x2 r2 = {__builtin_amdgcn_rcpf(hi2f(sa)), __builtin_amdgcn_rcpf(hi2f(sb))};
        const f32x2 m2 = {-lo2f(sa) * r2.x, -lo2f(sb) * r2.y};
        f32x2 n2[8];
        u32 o[8];
        SB();
#pragma unroll
        for (int c = 0; c < 8; c++) n2[c] = f32x2{fmav(xq[i][c].x, r2.x, m2.x), fmav(xq[i][c].y, r2.y, m2.y)};
        SB();
#pragma unroll
        for (int c = 0; c < 8; c++) o[c] = packh(n2[c].x, n2[c].y);
        SB();
#pragma unroll
        for (int c = 0; c < 8; c++) asm volatile("ds_write_addtid_b32 %0 offset:%1" ::"v"(o[c]), "n"(c * XREG + 1024 * i) : "memory");
        asm volatile("ds_write_b32 %1, %0\n\tv_add_u32 %0, 1, %0" : "+v"(fv) : "v"(fa) : "memory");
        if (i == PDROP) __builtin_amdgcn_s_setprio(PPRIO);
        SB();
#pragma unroll
        for (int c = 0; c < 8; c++) xq[i][c] = f32x2{fmac(n2[c].x, sc[c], xq[i][c].x), fmac(n2[c].y, sc[c], xq[i][c].y)};
        SB();
      }
    }
    STAMP(4);
    const rsrc_t ro = __builtin_amdgcn_make_buffer_rsrc((void*)out, 0, 2 * L * D, 0x00020000);
    u32 ys[8], y[2][8] = {};
    u32x2 ix;
    u32x4 cl;
    u32x8 pb;
    float yc, rsel;
    const u32 ixo = IXO((l & 6) + (n16 >> 3));
    if (p == 0) asm volatile("buffer_load_dwordx2 v[4:5], %1, %2, 0 offen sc1" : "=&{v[4:5]}"(ix) : "v"(ixo), "s"(rs) : "memory");
    const int rc = 4 * (l & 1) + kg, u = 64 * (p & 1) + l;
    *(u32x4*)(smem + OIX + 16 * p) = dv;
    u32x4 pick;
    u32 po = O_PG + 128 * g + 16 * (l & 7) + 16384 * (l >> 3), p3 = l < 32 ? po + 3 * 131072 : 0x80000000u, yf = lds0 + OYP + 4 * min(n16, 12);
    u32 ya = lds0 + OY + CHR * rc, qa = lds0 + OX + 2 * XO + rc * XREG, oo = 16 * g + 2 * rc, clo = O_CLR + 16 * min(l, 62);
    u32 yp = lds0 + OY + 512 * (p >> 1) + 4 * (u ^ ((u >> 4) & 6)), yq = yp + 4 * CHR, bp = lds0 + OMISC + 16 * p + 4 * kg, bs = 30 - 2 * n16;
    float e = 1.f + lo2f(*(const u16*)(smem + OIX + 16 * p + 2 * rc));
#pragma unroll
    for (int q = 0; q < 4; q++) pick[q] = (n16 >> 2) == q ? 0x3f80u << 16 * (l & 1) : 0u;
    asm volatile("" : "+v"(e), "+v"(pick), "+v"(ya), "+v"(qa), "+v"(oo), "+v"(po), "+v"(p3), "+v"(yf), "+v"(clo), "+v"(yp), "+v"(yq), "+v"(bp), "+v"(bs));
#pragma unroll
    for (int c = 0; c < 8; c++) asm volatile("" : "+v"(y[0][c]), "+v"(y[1][c]));
    if (TE) asm volatile(UNTIL(STR(TE)) :: [st] "s"(st0) : "s8", "s9", "scc");
#pragma unroll
    for (int i = 0; i < 4; i++) {
      if (i == IXI && p == 0) {
        u32 n_ = SPIN;
        asm volatile("s_waitcnt vmcnt(%5)\n1:\n\tv_cmp_ne_u32 vcc, %4, v5\n\ts_sub_u32 %1, %1, 1\n\ts_cbranch_vccz 2f\n\ts_cbranch_scc1 2f\n\t"
                     "buffer_load_dwordx2 v[4:5], %2, %3, 0 offen sc1\n\ts_waitcnt vmcnt(0)\n\ts_branch 1b\n2:"
                     : "+{v[4:5]}"(ix), "+s"(n_) : "v"(ixo), "s"(rs), "s"(tag), "n"(2 * IXI + 1) : "vcc", "scc", "memory");
        const u32 prow = ix.x & 2047u, v = prow >> 1;
        if (l < 16) atomicOr((u32*)(smem + OMISC) + (prow >> 5), 1u << (ix.x & 31u));
        FLAG(OYP + 32, 8u);
        oo += prow * (2 * D);
        asm volatile("v_mov_b32 v162, 0\n\tglobal_load_short_d16_hi v162, %1, %2" : "+{v[160:163]}"(junk) : "v"(oo), "s"(x) : "memory");
        AWAIT(OXF + 4 * (l & 3), 4, "s_sleep 1");
        e *= hlo(*(const LDS u16*)(size_t)(qa + 2 * prow));
        yc = lo2f(*(const u32*)(smem + OSC + 4 * rc)); rsel = rbt(sigm(hi2f(ix.x)));
        ya += 4 * (v ^ ((v >> 4) & 6)) + 2 * (prow & 1);
        asm volatile("" : "+v"(e), "+v"(yc), "+v"(rsel), "+v"(ya));
        STAMP(11);
      }
      if (i == 2 && p == 0) cl = ld16(rs, clo);
      if (i == 3) {
        asm volatile("s_waitcnt vmcnt(2)" ::: "memory");
        FLAG(OYP + 36 + 4 * p, 8u);
        if (p == 0) {
#pragma unroll 1
          for (int spin = 0; spin < SPIN; spin++) {
            if (__all(cl.y == tag && cl.w == tag)) break;
            cl = ld16(rs, clo);
          }
          STAMP(12);
        }
        __builtin_amdgcn_s_setprio(LPRIO);
      }
      {
        u32 n_ = SPIN, f_;
        asm volatile("1:\n\tds_read_b32 %0, %2\n\ts_sub_u32 %1, %1, 1\n\ts_waitcnt lgkmcnt(0)\n\tv_cmp_gt_u32 vcc, %3, %0\n\ts_cbranch_scc1 2f\n\ts_and_b32 vcc_lo, vcc_lo, %4\n\t"
                     "s_cbranch_scc0 2f\n\t" YNAP "\n\ts_branch 1b\n2:" : "=&v"(f_), "+s"(n_) : "v"(yf), "s"(2 * i + 1 + (p >> 1)), "n"(i < 2 ? 255 : 511) : "vcc", "scc", "memory");
      }
      if (i == 0) STAMP(5); else if (i == 1) STAMP(6); else if (i == 2) STAMP(7); else STAMP(8);
      u32 bm = 0;
      if (i >= 2) asm volatile("ds_read_b32 %0, %1 offset:%2" : "=v"(bm) : "v"(bp), "n"(64 * i) : "memory");
#pragma unroll
      for (int r = 0; r < 2; r++)
#pragma unroll
        for (int c = 0; c < 8; c++) asm volatile("ds_read_u16_d16_hi %0, %1 offset:%2" : "+v"(y[r][c]) : "v"(c < 4 ? yp : yq), "n"((c & 3) * CHR + 1024 * i + 2 * r) : "memory");
      if (i == 3 && p == 0) {
#define PLD(X) X(132, 133, 134, 135, "%[po]", "0") X(136, 137, 138, 139, "%[po]", "%[k1]") X(140, 141, 142, 143, "%[po]", "%[k2]") X(144, 145, 146, 147, "%[p3]", "0")
#define PL0(a, b_, c, d, o, s_) "buffer_load_dwordx4 v[" #a ":" #d "], " o ", %[rs], " s_ " offen sc1\n\t"
        asm volatile(PLD(PL0) : "=&{v[132:139]}"(pa), "=&{v[140:147]}"(pb) : [po] "v"(po), [p3] "v"(p3), [rs] "s"(rs), [k1] "s"(131072), [k2] "s"(262144) : "memory");
      }
      if (i == 0) {
        st8(rs, t == 512 ? O_ARR + b * 8 : 0x80000000u, u32x2{0u, tag});
        const u32x4 a = *(const u32x4*)(smem + OSC), c = *(const u32x4*)(smem + OSC + 16);
#pragma unroll
        for (int k = 0; k < 4; k++) { ys[k] = (u32)__builtin_amdgcn_readfirstlane((int)a[k]) << 16; ys[4 + k] = (u32)__builtin_amdgcn_readfirstlane((int)c[k]) << 16; }
      }
      SB();
#pragma unroll
      for (int r = 0; r < 2; r++) {
        asm volatile("s_waitcnt lgkmcnt(%9)" : "+v"(y[r][0]), "+v"(y[r][1]), "+v"(y[r][2]), "+v"(y[r][3]), "+v"(y[r][4]), "+v"(y[r][5]), "+v"(y[r][6]), "+v"(y[r][7]), "+v"(bm) : "n"(8 - 8 * r));
        u32x4 v;
#pragma unroll
        for (int k = 0; k < 4; k++)
          v[k] = pack2(fmas(__uint_as_float(y[r][2 * k]), ys[2 * k], xq[i][2 * k][r]), fmas(__uint_as_float(y[r][2 * k + 1]), ys[2 * k + 1], xq[i][2 * k + 1][r]));
        if (i >= 2 && r == 0) bm <<= bs;
        __builtin_amdgcn_raw_buffer_store_b128(v, ro, i < 2 ? op : op | (bm & 0x40000000u << r), (512 * i + r) * (2 * D), OAUX);
        SB();
        asm volatile("s_nop 1");
        SB();
      }
    }
    asm volatile("" ::"{v[160:163]}"(junk));
    STAMP(13);
    if (p == 0) {
      u32 n_ = SPIN;
#define PL1(a, b_, c, d, o, s_) "v_max3_u32 v150, v" #a ", v" #b_ ", v" #c "\n\tv_max_u32 v150, v150, v" #d "\n\tv_cmpx_eq_u32 vcc, -1, v150\n\t" PL0(a, b_, c, d, o, s_) "s_mov_b64 exec, -1\n\t"
#define PLE(X) X(112, 113, 114, 115, "%[po]", "0") X(116, 117, 118, 119, "%[po]", "%[k1]") X(120, 121, 122, 123, "%[po]", "%[k2]") X(124, 125, 126, 127, "%[p3]", "0")
#define PMX(a, b_) "v_max3_u32 v150, v150, v" a ", v" b_ "\n\t"
#define PMM(d, a, c) "v_mfma_f32_16x16x32_bf16 v[" d "], %[pk], v[" a "], " c "\n\t"
#define PCK(c, p, q) "s_waitcnt vmcnt(" #c ")\n\t" PMM("152:155", p "2:" p "5", "0") "v_max_u32 v150, v" p "2, v" p "3\n\t" PMX(p "4", p "5") PMM("156:159", p "6:" p "9", "0") PMX(p "6", p "7") PMX(p "8", p "9") \
      PMM("152:155", q "0:" q "3", "v[152:155]") PMX(q "0", q "1") PMX(q "2", q "3") PMM("156:159", q "4:" q "7", "v[156:159]") PMX(q "4", q "5") PMX(q "6", q "7") \
      "v_cmp_eq_u32 vcc, -1, v150\n\ts_sub_u32 %[n], %[n], 1\n\t"
#define PCP(a, b_, c, d) "v_mov_b64 v[" #a ":" #b_ "], v[" #c ":" #d "]\n\t"
      asm volatile(PCK(2, "13", "14") "s_cbranch_vccnz 9f\n"
                   "2:\n\t" TS(60, 61) "ds_read_b32 v150, %[yf]\n\tds_read_u16_d16_hi v151, %[ya]\n\ts_mov_b32 %[n], %[spin]\n\tv_add_f32 %[e], %[e], v162\n\ts_nop 0\n\tv_add_f32 v152, v152, v156\n\tv_add_f32 v153, v153, v157\n\ts_nop 0\n\t"
                   "v_add_f32_dpp v152, v152, v152 row_ror:8 row_mask:0xf bank_mask:0x3\n\tv_add_f32_dpp v152, v153, v153 row_ror:8 row_mask:0xf bank_mask:0xc\n\t"
                   "v_cvt_pk_bf16_f32 v152, 0, v152\n\tv_mul_f32 v152, v152, %[rl]\n\tv_cvt_pk_bf16_f32 v152, 0, v152\n\tv_add_f32 %[e], %[e], v152\n"
                   "3:\n\ts_waitcnt lgkmcnt(0)\n\tv_cmp_gt_u32 vcc, 8, v150\n\tv_and_b32 v152, 0xffff0000, v151\n\ts_sub_u32 %[n], %[n], 1\n\ts_cbranch_vccz 4f\n\ts_cbranch_scc1 4f\n\t"
                   "ds_read_b32 v150, %[yf]\n\tds_read_u16_d16_hi v151, %[ya]\n\ts_branch 3b\n"
                   "4:\n\tv_fmac_f32 %[e], v152, %[yc]\n\tv_cvt_pk_bf16_f32 %[e], %[e], %[e]\n\tbuffer_store_short %[e], %[oo], %[ro], 0 offen\n\t" DONE
                   "9:\n\t" PCP(112, 113, 132, 133) PCP(114, 115, 134, 135) PCP(116, 117, 136, 137) PCP(118, 119, 138, 139) PCP(120, 121, 140, 141) PCP(122, 123, 142, 143)
                   PCP(124, 125, 144, 145) PCP(126, 127, 146, 147) PLD(PL1) "s_sleep " STR(PNAP) "\n1:\n\t" PLE(PL1) PCK(4, "13", "14") "s_cbranch_vccz 2b\n\t" PLD(PL1) PCK(4, "11", "12")
                   "s_cbranch_vccz 2b\n\ts_cbranch_scc0 1b\n\ts_branch 2b\n0:"
                   : [e] "+v"(e), [n] "+s"(n_), "+{v[132:139]}"(pa), "+{v[140:147]}"(pb)
                   : [po] "v"(po), [p3] "v"(p3), [rs] "s"(rs), [k1] "s"(131072), [k2] "s"(262144), [pk] "v"(pick), [yf] "v"(yf), [ya] "v"(ya), "{v[160:163]}"(junk), [rl] "v"(rsel), [yc] "v"(yc),
                     [oo] "v"(oo), [ro] "s"(ro), [spin] "n"(SPIN)
                   : "vcc", "scc", "memory", "v112", "v113", "v114", "v115", "v116", "v117", "v118", "v119", "v120", "v121", "v122", "v123", "v124", "v125", "v126", "v127", "v150", "v151", "v152", "v153", "v154", "v155", "v156", "v157", "v158", "v159" TSD);
      TSGET(14, 2, 2, 2);
      STAMP(15);
    }
    FLUSH();
    __builtin_amdgcn_endpgm();
  }

  u32 pl, pa, pr, pi, ldtv;
  u32x4 tv, fx, junk;
  ROLE(c, "", , "={v[16:19]}"(tv), "={v[20:23]}"(fx), "={v24}"(ldtv), "={v96}"(pl), "={v97}"(pa), "={v68}"(pr), "={v69}"(pi), "={v[160:163]}"(junk));
  char* W = smem + w * CHR;
  __builtin_amdgcn_s_setprio(2);
  STAMP(3);
  const u32x4 zero4 = {0u, 0u, 0u, 0u};
#pragma unroll
  for (int i = 0; i < 4; i++) *(u32x4*)(W + (i < 3 ? TB + 1024 * i : CHR - 1024) + 16 * l) = zero4;
  SB();
  const u32 wb0 = lds0 + w * CHR, xb0 = lds0 + OX + w * XREG;
  asm volatile("s_mov_b32 m0, %0\n\ts_nop 0\n\tds_write_addtid_b32 %1\n\tds_write_addtid_b32 %1 offset:%2" ::"s"(wb0), "v"(0x3c00u), "n"(PB) : "memory");
  u32 ape = wb0 + (n16 == 12 ? 12 : (n16 ^ 3) - 7 * (n16 >> 3)) * RS + 16 * kg, apo = wb0 + (0x765412b0fedc2301ull >> 4 * n16 & 15) * RS + 16 * kg;
  u32x2 kb = {wb0 + ((15 - n16) & 3) * CPE + 2 * ((15 - n16) & 12) + 16 * kg, wb0 + (((n16 >> 2) & 1) + (n16 & 8) + 2 * (3 - (n16 & 3))) * RS + 16 * kg};
  u32x8 ka = {xb0 + 32 * n16 + 16 * kg, wb0 + OY + 32 * n16 + 8 * (kg ^ ((l >> 2) & 3)),
              lds0 + OXF + 4 * (l & 3), wb0 + TB + ((15 - n16) & 3) * CPO + 2 * ((15 - n16) & 12) + 8 * kg, lds0 + OB + 16 * l,
              l < 32 ? 0x01000100u : 0x03020100u, l < 32 ? 0x03020100u : 0x03020302u, (u32)(l >> 2) & 8u};
  u32 fad = lds0 + OYP + 4 * w, za = wb0 + 16 * kg;
  asm volatile("" : "+{v70}"(ape), "+{v71}"(apo), "+{v[28:35]}"(ka), "+{v[26:27]}"(kb), "+{v25}"(fad), "+{v12}"(za));
  STAMP(4);
  asm volatile("s_waitcnt vmcnt(4)" : "+{v24}"(ldtv), "+{v[160:163]}"(junk));
  float dt = rbt(fexp(hi2f(ldtv)));
  asm volatile("" : "+v"(dt));
  const f32x2 cdt = {rdl(dt * -1.4426950408889634f, 0), rdl(dt * 0.15915494309189535f, 0)};
  dt = rdl(dt, 0);
  SB();
  STAMP(10);
  STAMPC(11);
  {
    u32 e, tmp, o0, o1, of, os;
    asm volatile(GEN_ASM
                 : [e] "=&s"(e), [t] "=&s"(tmp), "=&{v93}"(o0), "=&{v46}"(o1), "=&{v48}"(of), "=&{v8}"(os), "+{v25}"(fad), "+{v12}"(za), "+{v68}"(pr), "+{v69}"(pi), "+{v70}"(ape),
                   "+{v71}"(apo), "+{v96}"(pl), "+{v97}"(pa), "+{v[28:35]}"(ka), "+{v[26:27]}"(kb), "+{v[16:19]}"(tv), "+{v[20:23]}"(fx)
                 : [w] "s"(wb0), [c] "s"(cdt), [k] "s"(f32x2{1.4426950408889634f, 256.f})
                 : "memory", "scc", GEN_CLOBBER);
    STAMP(1);
    STAMPC(12);
    const u32 k0 = (u32)__builtin_amdgcn_readlane((int)o0, 3), k256 = (u32)__builtin_amdgcn_readlane((int)o1, 60);
    const u32 k512 = (u32)__builtin_amdgcn_readlane((int)os, 28), k1024 = (u32)__builtin_amdgcn_readlane((int)of, 55);
    *(LDS u16*)(size_t)(wb0 + tv.z) = (u16)__builtin_amdgcn_perm((k512 & 0xffffu) | (k1024 << 16), (k0 >> 16) | (k256 << 16), tv.w);
    *(u32*)(smem + OSC + 4 * w) = __float_as_uint(__uint_as_float((e - 5) << 23) * dt) >> 16;
    fx &= ((k1024 & 0xffffu) | (k1024 << 16)) ^ 0x80008000u;
  }
  asm volatile(CONV_ASM : "+{v[28:35]}"(ka) : "{v[26:27]}"(kb), "{v25}"(fad), "{v[48:51]}"(fx), [w] "s"(w) : "vcc", "scc", "s6", "memory", CONV_CLOBBER TSC);
  TSGET(2, 5, 6, 7);
  STAMP(13);
  STAMPC(15);
  FLUSH();
  __builtin_amdgcn_endpgm();
}

extern "C" void kernel_launch(void* const* d_in, const int* in_sizes, int n_in, void* d_out, int out_size,
                              void* d_ws, size_t ws_size, hipStream_t stream) {
  const u16* const* in = (const u16* const*)d_in;
  fused<<<256, 768, 0, stream>>>(in[0], in[1], in[2], in[3], in[4], in[5], in[6], in[8], in[9], in[10], in[11],
                                 (u16*)d_out, (char*)d_ws);
}
```

```cpp
#include <hip/hip_runtime.h>
#include <stdint.h>

#define STR2(x) #x
#define STR(x) STR2(x)
#define LA 4
#define WL2 44
#define WL3 100
#define PH 1246
#define ABL 0
#define PPRIO 0
#define CPRIO 1
#define GDELAY 0
#define ROT 1
#define PRE 2
#define GNT 0
#define OAUX 0
#define LNT 1
#define PNT ""
#define LNNT ""
#define PRENT ""
#define PFN 15
#define PFO 512
#define DBAR 2
#define THR 1
#define QD 11
#define HNAP0 4
#define HNAP 4
#define KR 3
#define KI 2
#define QE 11
#define QA 5
#define NA 0
#define CH 0
#define G0E 0
#define DDL 0
#define WBM 0
#define WBW 1
#define HQ 3
#define RSL 5
#define DPF 1
#define HELP 0
#define WARM 0
#define HPI 0
#define Q0 6
#define NP 10
#define HW 28
#define NE 0
#define YNAP "s_sleep 2"
#define QS 7
#define QAS 6
#define RAMP 4
#define GNAP "s_sleep 1"
#define RNAP "s_sleep 1"
#define CLP 0
#define CSL 8
#define XSP 0
#define SPEC 0
#define HOLD 8
#define DPRIO 3
#define HAUX 16
#define DPO 128
#define TH 590
#define CLD 245
#define IXI 1
#define LPRIO 0
#define DPI 2
#define PDROP 3
#define TE 0
#define CNAP 2
#define XI 1
#define SCA " sc1"
#define SCB " sc1"
#define SCC " sc1"
#define SCT " sc1"
#define SCU " sc1"
#define SCH " sc1"
#define SCR " sc1"
#define TSG 280
#define SSL 5
#define PNAP 4
#define TW 310
#define TL 4352
#define CODE_KB 24
typedef unsigned short u16;
typedef unsigned int u32;
typedef __attribute__((ext_vector_type(8))) __bf16 bf16x8;
typedef __attribute__((ext_vector_type(2))) __bf16 bf16x2;
typedef __attribute__((ext_vector_type(8))) _Float16 f16x8;
typedef __attribute__((ext_vector_type(2))) _Float16 f16x2;
typedef __attribute__((ext_vector_type(4))) short v4i16;
typedef __attribute__((ext_vector_type(4))) float f32x4;
typedef __attribute__((ext_vector_type(2))) float f32x2;
typedef __attribute__((ext_vector_type(4))) u32 u32x4;
typedef __attribute__((ext_vector_type(2))) u32 u32x2;
typedef __attribute__((ext_vector_type(8))) u32 u32x8;
typedef __attribute__((ext_vector_type(2), aligned(4))) u32 u32x2_a4;
typedef __amdgpu_buffer_rsrc_t rsrc_t;
#define LDS __attribute__((address_space(3)))

#define TLARG
#define TLVAL
#define STAMP(I)
#define STAMPC(I)
#define FLUSH()

constexpr int L = 2048, D = 1024, NM = 128, F = 3584;
constexpr u32 O_ARR = 0x1000, O_CLR = 0x2000, O_RG = 0x4000, O_SG = 0x8000, O_IDX = 0x10000, O_HG = 0x20000,
              O_PG = 0xd0000, O_TOP = 0x140000, TOPS = 8 * TL, WS_END = O_TOP + 2 * TOPS;
constexpr int SPIN = 1 << 16;

__device__ __forceinline__ float lo2f(u32 w) { return __uint_as_float(w << 16); }
__device__ __forceinline__ float hi2f(u32 w) { return __uint_as_float(w & 0xffff0000u); }
__device__ __forceinline__ u32 pack2(float a, float b) {
  bf16x2 r = {(__bf16)a, (__bf16)b};
  return __builtin_bit_cast(u32, r);
}
__device__ __forceinline__ u32 packh(float a, float b) {
  f16x2 r = {(_Float16)a, (_Float16)b};
  return __builtin_bit_cast(u32, r);
}
__device__ __forceinline__ float rbt(float f) { return __uint_as_float(pack2(0.f, f)); }
__device__ __forceinline__ float rb(float f) {
  float r;
  asm("v_cvt_pk_bf16_f32 %0, 0, %1" : "=v"(r) : "v"(f));
  return r;
}
__device__ __forceinline__ u32 hadd(u32 a, u32 b) {
  return __builtin_bit_cast(u32, __builtin_bit_cast(f16x2, a) + __builtin_bit_cast(f16x2, b));
}
__device__ __forceinline__ float hlo(u32 a) { return (float)__builtin_bit_cast(f16x2, a).x; }
__device__ __forceinline__ float hhi(u32 a) { return (float)__builtin_bit_cast(f16x2, a).y; }

template <int CTRL, int RM>
__device__ __forceinline__ float dppf(float v) {
  return __builtin_bit_cast(float, __builtin_amdgcn_update_dpp(0, __builtin_bit_cast(int, v), CTRL, RM, 0xf, true));
}
template <int CTRL, int RM>
__device__ __forceinline__ u32 dppu(u32 v) {
  return (u32)__builtin_amdgcn_update_dpp(0, (int)v, CTRL, RM, 0xf, true);
}
__device__ __forceinline__ float rdl(float v, int i) {
  return __builtin_bit_cast(float, __builtin_amdgcn_readlane(__builtin_bit_cast(int, v), i));
}
__device__ __forceinline__ u32 wmaxu(u32 v) {
  v = max(v, dppu<0x111, 0xf>(v)); v = max(v, dppu<0x112, 0xf>(v)); v = max(v, dppu<0x114, 0xf>(v));
  v = max(v, dppu<0x118, 0xf>(v)); v = max(v, dppu<0x142, 0xa>(v)); v = max(v, dppu<0x143, 0xc>(v));
  return (u32)__builtin_amdgcn_readlane((int)v, 63);
}
__device__ __forceinline__ void swap32(float& a, float& b) {
  auto r = __builtin_amdgcn_permlane32_swap(__float_as_uint(a), __float_as_uint(b), false, false);
  a = __uint_as_float(r[0]); b = __uint_as_float(r[1]);
}
__device__ __forceinline__ void swap16(float& a, float& b) {
  auto r = __builtin_amdgcn_permlane16_swap(__float_as_uint(a), __float_as_uint(b), false, false);
  a = __uint_as_float(r[0]); b = __uint_as_float(r[1]);
}
__device__ __forceinline__ u32x2 trread(const char* p) {
  v4i16 r = __builtin_amdgcn_ds_read_tr16_b64_v4i16((LDS v4i16*)p);
  return __builtin_bit_cast(u32x2, r);
}
__device__ __forceinline__ u32x2 trl(LDS char* p) { return __builtin_bit_cast(u32x2, __builtin_amdgcn_ds_read_tr16_b64_v4i16((LDS v4i16*)p)); }
__device__ __forceinline__ f16x8 mkh(u32x2 a, u32x2 b) {
  u32x4 v = {a.x, a.y, b.x, b.y};
  return __builtin_bit_cast(f16x8, v);
}
__device__ __forceinline__ bf16x8 mkb(u32x2 a, u32x2 b) {
  u32x4 v = {a.x, a.y, b.x, b.y};
  return __builtin_bit_cast(bf16x8, v);
}
__device__ __forceinline__ bf16x8 asb(u32x4 v) { return __builtin_bit_cast(bf16x8, v); }
__device__ __forceinline__ f16x8 ash(u32x4 v) { return __builtin_bit_cast(f16x8, v); }
#define MFMAB(a, b, c) __builtin_amdgcn_mfma_f32_16x16x32_bf16(a, b, c, 0, 0, 0)
#define MFMAH(a, b, c) __builtin_amdgcn_mfma_f32_16x16x32_f16(a, b, c, 0, 0, 0)

__device__ __forceinline__ u32x4 ld16(rsrc_t r, u32 off) { return __builtin_amdgcn_raw_buffer_load_b128(r, off, 0, 16); }
__device__ __forceinline__ u32x2 ld8(rsrc_t r, u32 off) { return __builtin_amdgcn_raw_buffer_load_b64(r, off, 0, 16); }
__device__ __forceinline__ void st16(rsrc_t r, u32 off, u32x4 v) { __builtin_amdgcn_raw_buffer_store_b128(v, r, off, 0, 16); }
__device__ __forceinline__ void st8(rsrc_t r, u32 off, u32x2 v) { __builtin_amdgcn_raw_buffer_store_b64(v, r, off, 0, 16); }
#define NAP() asm volatile("s_sleep 1" ::: "memory")
#define SB() __builtin_amdgcn_sched_barrier(0)
#define LBAR() asm volatile("s_waitcnt lgkmcnt(0)\n\ts_barrier" ::: "memory")

__device__ __forceinline__ float fexp(float v) { return __builtin_amdgcn_exp2f(v * 1.4426950408889634f); }
__device__ __forceinline__ float dot2b(u32 a, u32 b, float c) {
  return __builtin_amdgcn_fdot2_f32_bf16(__builtin_bit_cast(bf16x2, a), __builtin_bit_cast(bf16x2, b), c, false);
}
__device__ __forceinline__ float sigm(float v) { return __builtin_amdgcn_rcpf(1.f + __builtin_amdgcn_exp2f(-1.4426950408889634f * v)); }

constexpr int RS = 288, PB = 16 * RS;
constexpr int CPE = 1216, TB = 2 * PB, CPO = 832;
constexpr int T2B = 4, T3B = 224, C2 = 52, C3 = 84;
constexpr int OY = 0, CHR = TB + 4 * CPO;
constexpr int OX = 8 * CHR, XREG = 4640, XO = 266;
constexpr int OMISC = OX + 8 * XREG;
constexpr int OXF = OMISC + 320, OSC = OMISC + 352, OYP = OMISC + 448;
constexpr int OB = OMISC + 1024, SMEM = 148480;
constexpr int FA = 131072, FH = FA + 8192, HP = 576, FC = FH + 8 * HP;
static_assert(FC + 1024 <= SMEM && OB + 4096 <= SMEM, "lds map");

struct Tab { u32 v[6][64][4]; };
constexpr Tab mktab() {
  Tab T = {};
  for (int l = 0; l < 64; l++) {
    const int n16 = l & 15, kg = l >> 4, pc = ((n16 >> 2) & 1) + (n16 & 8) + 2 * (3 - (n16 & 3)), odd = kg >> 1, s = l & 3;
    const int e0 = 534 - 258 - 128 - 8 * pc - 4 * (kg & 1) - odd;
    const int ez = ((l >> 3) & 3) * CPE + ((l & 4) ? 1056 - 64 : 0) + 16 * (l & 7);
    const int b2 = T2B + 44 + C2 * ((n16 & 1) + 2 * (kg & 1)) - 8 * (n16 & 2), b3 = T3B + 60 + C3 * (kg & 1) - 16 * s;
    const int to = TB + 2 * ((n16 < 8 ? b2 : b3) - 4 * odd - 8) + ((l & 4) ? 0 : 2 * CPO - 4);
    const int q = (l >> 2) & 7, tap = q == 0 ? ((s & 1) ? 0 : 256) : q == 1 ? 257 : q == 7 ? 0 : 509 + q;
    const int sp = q != 6 ? s * (CPE - 2) + 2 * (537 - tap) : TB + s * (CPO - 2) + 2 * (T2B + 15);
    T.v[0][l][0] = (2 * e0 + (2 - odd) * (CPE - 2)) | (2 * e0 + 3 * odd * (CPE - 2)) << 16;
    T.v[0][l][1] = ez | to << 16;
    T.v[0][l][2] = sp;
    T.v[0][l][3] = !tap ? 0x0100u : q == 6 ? 0x0706u : q >= 2 ? 0x0504u : 0x0302u;
    for (int k = 0; k < 8; k++) {
      const int d = 8 * kg + k - n16;
      if (d == 13) T.v[1][l][k >> 1] |= 0xffffu << 16 * (k & 1);
      for (int h = 1; h <= 4; h++)
        if (d >= (h == 1 ? 14 : 10) - h && d <= (h == 1 ? 14 : 10) + h) T.v[1 + h][l][k >> 1] |= 0x3c00u << 16 * (k & 1);
    }
  }
  return T;
}
extern "C" __device__ const __attribute__((aligned(128), used, visibility("hidden"))) Tab lane_tab = mktab();

#define GEN_ASM \
  "s_waitcnt vmcnt(2)\n\tv_lshlrev_b32 v56, 16, v96\n\t" \
  "v_and_b32 v57, 0xffff0000, v96\n\tv_pk_mul_f32 v[56:57], %[k], v[56:57] op_sel_hi:[0,1]\n\t" \
  "v_lshlrev_b32 v49, 16, v97\n\tv_exp_f32 v56, v56\n\t" \
  "v_exp_f32 v57, v57\n\tv_and_b32 v51, 0xffff0000, v97\n\t" \
  "v_cvt_pk_bf16_f32 v56, v56, v57\n\tv_lshlrev_b32 v48, 16, v56\n\t" \
  "v_and_b32 v50, 0xffff0000, v56\n\tv_pk_mul_f32 v[48:49], %[c], v[48:49]\n\t" \
  "v_pk_mul_f32 v[50:51], %[c], v[50:51]\n\tv_pk_mul_f32 v[98:99], %[k], v[48:49] op_sel:[1,0]\n\t" \
  "v_pk_mul_f32 v[100:101], %[k], v[50:51] op_sel:[1,0]\n\tv_fract_f32 v49, v49\n\t" \
  "v_fract_f32 v51, v51\n\tv_exp_f32 v48, v48\n\t" \
  "v_exp_f32 v50, v50\n\tv_cos_f32 v40, v49\n\t" \
  "v_sin_f32 v41, v49\n\tv_cos_f32 v42, v51\n\t" \
  "v_sin_f32 v43, v51\n\tv_pk_mul_f32 v[40:41], v[40:41], v[48:49] op_sel_hi:[1,0]\n\t" \
  "v_pk_mul_f32 v[42:43], v[42:43], v[50:51] op_sel_hi:[1,0]\n\tv_pk_mul_f32 v[48:49], v[40:41], v[40:41] op_sel:[1,1] op_sel_hi:[1,0] neg_lo:[1,0]\n\t" \
  "v_pk_mul_f32 v[50:51], v[42:43], v[42:43] op_sel:[1,1] op_sel_hi:[1,0] neg_lo:[1,0]\n\tv_pk_fma_f32 v[48:49], v[40:41], v[40:41], v[48:49] op_sel:[0,0,0] op_sel_hi:[0,1,1]\n\t" \
  "v_pk_fma_f32 v[50:51], v[42:43], v[42:43], v[50:51] op_sel:[0,0,0] op_sel_hi:[0,1,1]\n\tv_cvt_pk_f16_f32 v72, v40, -v41\n\t" \
  "v_cvt_pk_f16_f32 v73, v42, -v43\n\tv_pk_mul_f32 v[52:53], v[48:49], v[40:41] op_sel:[1,1] op_sel_hi:[1,0] neg_lo:[1,0]\n\t" \
  "v_pk_mul_f32 v[54:55], v[50:51], v[42:43] op_sel:[1,1] op_sel_hi:[1,0] neg_lo:[1,0]\n\tv_pk_fma_f32 v[52:53], v[48:49], v[40:41], v[52:53] op_sel:[0,0,0] op_sel_hi:[0,1,1]\n\t" \
  "v_pk_fma_f32 v[54:55], v[50:51], v[42:43], v[54:55] op_sel:[0,0,0] op_sel_hi:[0,1,1]\n\tv_pk_mul_f32 v[56:57], v[48:49], v[48:49] op_sel:[1,1] op_sel_hi:[1,0] neg_lo:[1,0]\n\t" \
  "v_pk_mul_f32 v[58:59], v[50:51], v[50:51] op_sel:[1,1] op_sel_hi:[1,0] neg_lo:[1,0]\n\tv_pk_fma_f32 v[56:57], v[48:49], v[48:49], v[56:57] op_sel:[0,0,0] op_sel_hi:[0,1,1]\n\t" \
  "v_pk_fma_f32 v[58:59], v[50:51], v[50:51], v[58:59] op_sel:[0,0,0] op_sel_hi:[0,1,1]\n\tv_cvt_pk_f16_f32 v74, v48, -v49\n\t" \
  "v_cvt_pk_f16_f32 v75, v50, -v51\n\tv_cvt_pk_f16_f32 v76, v52, -v53\n\t" \
  "v_cvt_pk_f16_f32 v77, v54, -v55\n\tv_pk_mul_f32 v[52:53], v[56:57], v[48:49] op_sel:[1,1] op_sel_hi:[1,0] neg_lo:[1,0]\n\t" \
  "v_pk_mul_f32 v[54:55], v[58:59], v[50:51] op_sel:[1,1] op_sel_hi:[1,0] neg_lo:[1,0]\n\tv_pk_fma_f32 v[52:53], v[56:57], v[48:49], v[52:53] op_sel:[0,0,0] op_sel_hi:[0,1,1]\n\t" \
  "v_pk_fma_f32 v[54:55], v[58:59], v[50:51], v[54:55] op_sel:[0,0,0] op_sel_hi:[0,1,1]\n\tv_pk_mul_f32 v[60:61], v[56:57], v[56:57] op_sel:[1,1] op_sel_hi:[1,0] neg_lo:[1,0]\n\t" \
  "v_pk_mul_f32 v[62:63], v[58:59], v[58:59] op_sel:[1,1] op_sel_hi:[1,0] neg_lo:[1,0]\n\tv_pk_fma_f32 v[60:61], v[56:57], v[56:57], v[60:61] op_sel:[0,0,0] op_sel_hi:[0,1,1]\n\t" \
  "v_pk_fma_f32 v[62:63], v[58:59], v[58:59], v[62:63] op_sel:[0,0,0] op_sel_hi:[0,1,1]\n\tv_cvt_pk_f16_f32 v78, v56, -v57\n\t" \
  "v_cvt_pk_f16_f32 v79, v58, -v59\n\tv_cvt_pk_f16_f32 v82, v52, -v53\n\t" \
  "v_cvt_pk_f16_f32 v83, v54, -v55\n\tv_pk_mul_f16 v80, v78, v72 op_sel:[1,1] op_sel_hi:[1,0] neg_lo:[1,0]\n\t" \
  "v_pk_mul_f16 v81, v79, v73 op_sel:[1,1] op_sel_hi:[1,0] neg_lo:[1,0]\n\tv_pk_fma_f16 v80, v78, v72, v80 op_sel:[0,0,0] op_sel_hi:[0,1,1]\n\t" \
  "v_pk_fma_f16 v81, v79, v73, v81 op_sel:[0,0,0] op_sel_hi:[0,1,1]\n\tv_pk_mul_f16 v84, v78, v76 op_sel:[1,1] op_sel_hi:[1,0] neg_lo:[1,0]\n\t" \
  "v_pk_mul_f16 v85, v79, v77 op_sel:[1,1] op_sel_hi:[1,0] neg_lo:[1,0]\n\tv_pk_fma_f16 v84, v78, v76, v84 op_sel:[0,0,0] op_sel_hi:[0,1,1]\n\t" \
  "v_pk_fma_f16 v85, v79, v77, v85 op_sel:[0,0,0] op_sel_hi:[0,1,1]\n\tv_cvt_pk_f16_f32 v86, v60, -v61\n\t" \
  "v_cvt_pk_f16_f32 v87, v62, -v63\n\tds_write_addtid_b32 v72 offset:288\n\t" \
  "ds_write_addtid_b32 v73 offset:4896\n\tds_write_addtid_b32 v74 offset:576\n\t" \
  "ds_write_addtid_b32 v75 offset:5184\n\tds_write_addtid_b32 v76 offset:864\n\t" \
  "ds_write_addtid_b32 v77 offset:5472\n\tds_write_addtid_b32 v78 offset:1152\n\t" \
  "ds_write_addtid_b32 v79 offset:5760\n\tds_write_addtid_b32 v80 offset:1440\n\t" \
  "ds_write_addtid_b32 v81 offset:6048\n\tds_write_addtid_b32 v82 offset:1728\n\t" \
  "ds_write_addtid_b32 v83 offset:6336\n\tds_write_addtid_b32 v84 offset:2016\n\t" \
  "ds_write_addtid_b32 v85 offset:6624\n\tds_write_addtid_b32 v86 offset:3456\n\t" \
  "ds_write_addtid_b32 v87 offset:8064\n\tds_read_b128 v[104:107], v70 offset:0\n\t" \
  "ds_read_b128 v[108:111], v70 offset:64\n\tds_read_b128 v[112:115], v70 offset:128\n\t" \
  "ds_read_b128 v[116:119], v70 offset:192\n\tds_read_b128 v[120:123], v70 offset:4608\n\t" \
  "ds_read_b128 v[124:127], v70 offset:4672\n\tds_read_b128 v[128:131], v70 offset:4736\n\t" \
  "ds_read_b128 v[132:135], v70 offset:4800\n\tv_fract_f32 v99, v99\n\t" \
  "v_fract_f32 v101, v101\n\tv_exp_f32 v98, v98\n\t" \
  "v_exp_f32 v100, v100\n\tv_cos_f32 v44, v99\n\t" \
  "v_sin_f32 v45, v99\n\tv_cos_f32 v46, v101\n\t" \
  "v_sin_f32 v47, v101\n\tv_pk_mul_f32 v[44:45], v[44:45], v[98:99] op_sel_hi:[1,0]\n\t" \
  "v_pk_mul_f32 v[46:47], v[46:47], v[100:101] op_sel_hi:[1,0]\n\ts_waitcnt vmcnt(0)\n\t" \
  "v_pk_mul_f32 v[48:49], v[44:45], v[44:45] op_sel:[1,1] op_sel_hi:[1,0] neg_lo:[1,0]\n\tv_pk_mul_f32 v[50:51], v[46:47], v[46:47] op_sel:[1,1] op_sel_hi:[1,0] neg_lo:[1,0]\n\t" \
  "v_pk_fma_f32 v[48:49], v[44:45], v[44:45], v[48:49] op_sel:[0,0,0] op_sel_hi:[0,1,1]\n\tv_pk_fma_f32 v[50:51], v[46:47], v[46:47], v[50:51] op_sel:[0,0,0] op_sel_hi:[0,1,1]\n\t" \
  "v_and_b32 v102, 0x7fff7fff, v68\n\tv_cvt_pk_f16_f32 v88, v44, -v45\n\t" \
  "v_cvt_pk_f16_f32 v89, v46, -v47\n\tv_pk_mul_f32 v[52:53], v[48:49], v[44:45] op_sel:[1,1] op_sel_hi:[1,0] neg_lo:[1,0]\n\t" \
  "v_pk_mul_f32 v[54:55], v[50:51], v[46:47] op_sel:[1,1] op_sel_hi:[1,0] neg_lo:[1,0]\n\tv_and_b32 v103, 0x7fff7fff, v69\n\t" \
  "v_pk_fma_f32 v[52:53], v[48:49], v[44:45], v[52:53] op_sel:[0,0,0] op_sel_hi:[0,1,1]\n\tv_pk_fma_f32 v[54:55], v[50:51], v[46:47], v[54:55] op_sel:[0,0,0] op_sel_hi:[0,1,1]\n\t" \
  "v_pk_mul_f32 v[56:57], v[48:49], v[48:49] op_sel:[1,1] op_sel_hi:[1,0] neg_lo:[1,0]\n\tv_pk_mul_f32 v[58:59], v[50:51], v[50:51] op_sel:[1,1] op_sel_hi:[1,0] neg_lo:[1,0]\n\t" \
  "v_pk_max_u16 v102, v102, v103\n\tv_pk_fma_f32 v[56:57], v[48:49], v[48:49], v[56:57] op_sel:[0,0,0] op_sel_hi:[0,1,1]\n\t" \
  "v_pk_fma_f32 v[58:59], v[50:51], v[50:51], v[58:59] op_sel:[0,0,0] op_sel_hi:[0,1,1]\n\tv_cvt_pk_f16_f32 v90, v48, -v49\n\t" \
  "v_cvt_pk_f16_f32 v91, v50, -v51\n\tv_pk_max_u16 v102, v102, v102 op_sel:[1,0] op_sel_hi:[0,1]\n\t" \
  "v_pk_mul_f32 v[64:65], v[56:57], v[60:61] op_sel:[1,1] op_sel_hi:[1,0] neg_lo:[1,0]\n\tv_pk_mul_f32 v[66:67], v[58:59], v[62:63] op_sel:[1,1] op_sel_hi:[1,0] neg_lo:[1,0]\n\t" \
  "v_pk_fma_f32 v[64:65], v[56:57], v[60:61], v[64:65] op_sel:[0,0,0] op_sel_hi:[0,1,1]\n\tv_pk_fma_f32 v[66:67], v[58:59], v[62:63], v[66:67] op_sel:[0,0,0] op_sel_hi:[0,1,1]\n\t" \
  "v_max_u32_dpp v102, v102, v102 row_shr:1 row_mask:0xf bank_mask:0xf\n\tv_cvt_pk_f16_f32 v92, v52, -v53\n\t" \
  "v_cvt_pk_f16_f32 v93, v54, -v55\n\tv_pk_mul_f16 v136, v88, v74 op_sel:[1,1] op_sel_hi:[1,0] neg_lo:[1,0]\n\t" \
  "v_pk_mul_f16 v137, v89, v75 op_sel:[1,1] op_sel_hi:[1,0] neg_lo:[1,0]\n\tv_max_u32_dpp v102, v102, v102 row_shr:2 row_mask:0xf bank_mask:0xf\n\t" \
  "v_pk_fma_f16 v136, v88, v74, v136 op_sel:[0,0,0] op_sel_hi:[0,1,1]\n\tv_pk_fma_f16 v137, v89, v75, v137 op_sel:[0,0,0] op_sel_hi:[0,1,1]\n\t" \
  "v_pk_mul_f16 v138, v88, v78 op_sel:[1,1] op_sel_hi:[1,0] neg_lo:[1,0]\n\tv_pk_mul_f16 v139, v89, v79 op_sel:[1,1] op_sel_hi:[1,0] neg_lo:[1,0]\n\t" \
  "v_max_u32_dpp v102, v102, v102 row_shr:4 row_mask:0xf bank_mask:0xf\n\tv_pk_fma_f16 v138, v88, v78, v138 op_sel:[0,0,0] op_sel_hi:[0,1,1]\n\t" \
  "v_pk_fma_f16 v139, v89, v79, v139 op_sel:[0,0,0] op_sel_hi:[0,1,1]\n\tv_cvt_pk_f16_f32 v94, v64, -v65\n\t" \
  "v_cvt_pk_f16_f32 v95, v66, -v67\n\tv_max_u32_dpp v102, v102, v102 row_shr:8 row_mask:0xf bank_mask:0xf\n\t" \
  "v_pk_mul_f16 v140, v88, v82 op_sel:[1,1] op_sel_hi:[1,0] neg_lo:[1,0]\n\tv_pk_mul_f16 v141, v89, v83 op_sel:[1,1] op_sel_hi:[1,0] neg_lo:[1,0]\n\t" \
  "v_pk_fma_f16 v140, v88, v82, v140 op_sel:[0,0,0] op_sel_hi:[0,1,1]\n\tv_pk_fma_f16 v141, v89, v83, v141 op_sel:[0,0,0] op_sel_hi:[0,1,1]\n\t" \
  "ds_write_addtid_b32 v88 offset:3168\n\tds_write_addtid_b32 v89 offset:7776\n\t" \
  "ds_write_addtid_b32 v136 offset:0\n\tds_write_addtid_b32 v137 offset:4608\n\t" \
  "ds_write_addtid_b32 v138 offset:288\n\tds_write_addtid_b32 v139 offset:4896\n\t" \
  "ds_write_addtid_b32 v94 offset:1152\n\tds_write_addtid_b32 v95 offset:5760\n\t" \
  "ds_write_addtid_b32 v140 offset:576\n\tds_write_addtid_b32 v141 offset:5184\n\t" \
  "v_max_u32_dpp v102, v102, v102 row_bcast:15 row_mask:0xa bank_mask:0xf\n\tv_pk_mul_f16 v142, v88, v86 op_sel:[1,1] op_sel_hi:[1,0] neg_lo:[1,0]\n\t" \
  "v_pk_mul_f16 v143, v89, v87 op_sel:[1,1] op_sel_hi:[1,0] neg_lo:[1,0]\n\tv_pk_fma_f16 v142, v88, v86, v142 op_sel:[0,0,0] op_sel_hi:[0,1,1]\n\t" \
  "v_pk_fma_f16 v143, v89, v87, v143 op_sel:[0,0,0] op_sel_hi:[0,1,1]\n\tv_max_u32_dpp v102, v102, v102 row_bcast:31 row_mask:0xc bank_mask:0xf\n\t" \
  "v_pk_mul_f16 v144, v90, v78 op_sel:[1,1] op_sel_hi:[1,0] neg_lo:[1,0]\n\tv_pk_mul_f16 v145, v91, v79 op_sel:[1,1] op_sel_hi:[1,0] neg_lo:[1,0]\n\t" \
  "v_pk_fma_f16 v144, v90, v78, v144 op_sel:[0,0,0] op_sel_hi:[0,1,1]\n\tv_pk_fma_f16 v145, v91, v79, v145 op_sel:[0,0,0] op_sel_hi:[0,1,1]\n\t" \
  "v_readlane_b32 %[e], v102, 63\n\tv_pk_mul_f16 v146, v90, v86 op_sel:[1,1] op_sel_hi:[1,0] neg_lo:[1,0]\n\t" \
  "v_pk_mul_f16 v147, v91, v87 op_sel:[1,1] op_sel_hi:[1,0] neg_lo:[1,0]\n\tv_pk_fma_f16 v146, v90, v86, v146 op_sel:[0,0,0] op_sel_hi:[0,1,1]\n\t" \
  "v_pk_fma_f16 v147, v91, v87, v147 op_sel:[0,0,0] op_sel_hi:[0,1,1]\n\tv_pk_mul_f16 v148, v92, v78 op_sel:[1,1] op_sel_hi:[1,0] neg_lo:[1,0]\n\t" \
  "v_pk_mul_f16 v149, v93, v79 op_sel:[1,1] op_sel_hi:[1,0] neg_lo:[1,0]\n\tv_pk_fma_f16 v148, v92, v78, v148 op_sel:[0,0,0] op_sel_hi:[0,1,1]\n\t" \
  "v_pk_fma_f16 v149, v93, v79, v149 op_sel:[0,0,0] op_sel_hi:[0,1,1]\n\tv_pk_mul_f16 v150, v92, v86 op_sel:[1,1] op_sel_hi:[1,0] neg_lo:[1,0]\n\t" \
  "v_pk_mul_f16 v151, v93, v87 op_sel:[1,1] op_sel_hi:[1,0] neg_lo:[1,0]\n\tv_pk_fma_f16 v150, v92, v86, v150 op_sel:[0,0,0] op_sel_hi:[0,1,1]\n\t" \
  "v_pk_fma_f16 v151, v93, v87, v151 op_sel:[0,0,0] op_sel_hi:[0,1,1]\n\ts_bfe_u32 %[e], %[e], 0x80007\n\t" \
  "s_max_u32 %[e], %[e], %[em]\n\ts_sub_u32 %[t], 260, %[e]\n\t" \
  "s_lshl_b32 %[t], %[t], 23\n\tds_write_addtid_b32 v142 offset:864\n\t" \
  "ds_write_addtid_b32 v143 offset:5472\n\tds_write_addtid_b32 v144 offset:3456\n\t" \
  "ds_write_addtid_b32 v145 offset:8064\n\tds_write_addtid_b32 v146 offset:3744\n\t" \
  "ds_write_addtid_b32 v147 offset:8352\n\tds_write_addtid_b32 v148 offset:4032\n\t" \
  "ds_write_addtid_b32 v149 offset:8640\n\tds_write_addtid_b32 v150 offset:4320\n\t" \
  "ds_write_addtid_b32 v151 offset:8928\n\tv_pk_mul_f16 v96, v94, v88 op_sel:[1,1] op_sel_hi:[1,0] neg_lo:[1,0]\n\t" \
  "v_pk_mul_f16 v97, v95, v89 op_sel:[1,1] op_sel_hi:[1,0] neg_lo:[1,0]\n\tv_pk_fma_f16 v96, v94, v88, v96 op_sel:[0,0,0] op_sel_hi:[0,1,1]\n\t" \
  "v_pk_fma_f16 v97, v95, v89, v97 op_sel:[0,0,0] op_sel_hi:[0,1,1]\n\tv_pk_mul_f16 v98, v94, v90 op_sel:[1,1] op_sel_hi:[1,0] neg_lo:[1,0]\n\t" \
  "v_pk_mul_f16 v99, v95, v91 op_sel:[1,1] op_sel_hi:[1,0] neg_lo:[1,0]\n\tv_pk_fma_f16 v98, v94, v90, v98 op_sel:[0,0,0] op_sel_hi:[0,1,1]\n\t" \
  "v_pk_fma_f16 v99, v95, v91, v99 op_sel:[0,0,0] op_sel_hi:[0,1,1]\n\tv_pk_mul_f16 v100, v94, v92 op_sel:[1,1] op_sel_hi:[1,0] neg_lo:[1,0]\n\t" \
  "v_pk_mul_f16 v101, v95, v93 op_sel:[1,1] op_sel_hi:[1,0] neg_lo:[1,0]\n\tv_pk_fma_f16 v100, v94, v92, v100 op_sel:[0,0,0] op_sel_hi:[0,1,1]\n\t" \
  "v_pk_fma_f16 v101, v95, v93, v101 op_sel:[0,0,0] op_sel_hi:[0,1,1]\n\tds_write_addtid_b32 v96 offset:1440\n\t" \
  "ds_write_addtid_b32 v97 offset:6048\n\tds_write_addtid_b32 v98 offset:1728\n\t" \
  "ds_write_addtid_b32 v99 offset:6336\n\tds_write_addtid_b32 v100 offset:2016\n\t" \
  "ds_write_addtid_b32 v101 offset:6624\n\tds_read_b128 v[136:139], v71 offset:0\n\t" \
  "ds_read_b128 v[140:143], v71 offset:64\n\tds_read_b128 v[144:147], v71 offset:128\n\t" \
  "ds_read_b128 v[148:151], v71 offset:192\n\tds_read_b128 v[152:155], v71 offset:4608\n\t" \
  "ds_read_b128 v[156:159], v71 offset:4672\n\tds_read_b128 v[160:163], v71 offset:4736\n\t" \
  "ds_read_b128 v[164:167], v71 offset:4800\n\tv_pk_mul_f32 v[48:49], v[60:61], v[60:61] op_sel:[1,1] op_sel_hi:[1,0] neg_lo:[1,0]\n\t" \
  "v_pk_mul_f32 v[50:51], v[62:63], v[62:63] op_sel:[1,1] op_sel_hi:[1,0] neg_lo:[1,0]\n\tv_pk_fma_f32 v[48:49], v[60:61], v[60:61], v[48:49] op_sel:[0,0,0] op_sel_hi:[0,1,1]\n\t" \
  "s_cmp_lt_u32 %[w], 50176\n\ts_cbranch_scc0 1f\n\t" \
  "s_setprio 1\n\t1:\n\t" \
  "v_pk_fma_f32 v[50:51], v[62:63], v[62:63], v[50:51] op_sel:[0,0,0] op_sel_hi:[0,1,1]\n\tv_cvt_pk_f16_f32 v44, v60, v61\n\t" \
  "v_cvt_pk_f16_f32 v45, v62, v63\n\tv_pk_mul_f32 v[52:53], v[48:49], v[60:61] op_sel:[1,1] op_sel_hi:[1,0] neg_lo:[1,0]\n\t" \
  "v_pk_mul_f32 v[54:55], v[50:51], v[62:63] op_sel:[1,1] op_sel_hi:[1,0] neg_lo:[1,0]\n\tv_pk_fma_f32 v[52:53], v[48:49], v[60:61], v[52:53] op_sel:[0,0,0] op_sel_hi:[0,1,1]\n\t" \
  "v_pk_fma_f32 v[54:55], v[50:51], v[62:63], v[54:55] op_sel:[0,0,0] op_sel_hi:[0,1,1]\n\tv_pk_mul_f32 v[40:41], v[48:49], v[48:49] op_sel:[1,1] op_sel_hi:[1,0] neg_lo:[1,0]\n\t" \
  "v_pk_mul_f32 v[42:43], v[50:51], v[50:51] op_sel:[1,1] op_sel_hi:[1,0] neg_lo:[1,0]\n\tv_pk_fma_f32 v[40:41], v[48:49], v[48:49], v[40:41] op_sel:[0,0,0] op_sel_hi:[0,1,1]\n\t" \
  "v_pk_fma_f32 v[42:43], v[50:51], v[50:51], v[42:43] op_sel:[0,0,0] op_sel_hi:[0,1,1]\n\tv_cvt_pk_f16_f32 v46, v48, v49\n\t" \
  "v_cvt_pk_f16_f32 v47, v50, v51\n\tv_cvt_pk_f16_f32 v56, v52, v53\n\t" \
  "v_cvt_pk_f16_f32 v57, v54, v55\n\tv_lshlrev_b32 v64, 16, v68\n\t" \
  "v_lshlrev_b32 v65, 16, v69\n\tv_and_b32 v66, 0xffff0000, v68\n\t" \
  "v_and_b32 v67, 0xffff0000, v69\n\tv_mul_f32 v64, %[t], v64\n\t" \
  "v_mul_f32 v65, %[t], v65\n\tv_mul_f32 v66, %[t], v66\n\t" \
  "v_mul_f32 v67, %[t], v67\n\tv_cvt_pk_f16_f32 v48, v64, v65\n\t" \
  "v_cvt_pk_f16_f32 v49, v66, v67\n\tv_pk_mul_f32 v[68:69], v[64:65], v[40:41] op_sel:[1,1] op_sel_hi:[1,0] neg_lo:[1,0]\n\t" \
  "v_pk_mul_f32 v[70:71], v[66:67], v[42:43] op_sel:[1,1] op_sel_hi:[1,0] neg_lo:[1,0]\n\tv_pk_fma_f32 v[68:69], v[64:65], v[40:41], v[68:69] op_sel:[0,0,0] op_sel_hi:[0,1,1]\n\t" \
  "v_pk_fma_f32 v[70:71], v[66:67], v[42:43], v[70:71] op_sel:[0,0,0] op_sel_hi:[0,1,1]\n\tv_pk_mul_f16 v50, v48, v44 op_sel:[1,1] op_sel_hi:[1,0] neg_lo:[1,0]\n\t" \
  "v_pk_mul_f16 v51, v49, v45 op_sel:[1,1] op_sel_hi:[1,0] neg_lo:[1,0]\n\tv_pk_fma_f16 v50, v48, v44, v50 op_sel:[0,0,0] op_sel_hi:[0,1,1]\n\t" \
  "v_pk_fma_f16 v51, v49, v45, v51 op_sel:[0,0,0] op_sel_hi:[0,1,1]\n\tv_pk_mul_f16 v52, v48, v46 op_sel:[1,1] op_sel_hi:[1,0] neg_lo:[1,0]\n\t" \
  "v_pk_mul_f16 v53, v49, v47 op_sel:[1,1] op_sel_hi:[1,0] neg_lo:[1,0]\n\tv_pk_fma_f16 v52, v48, v46, v52 op_sel:[0,0,0] op_sel_hi:[0,1,1]\n\t" \
  "v_pk_fma_f16 v53, v49, v47, v53 op_sel:[0,0,0] op_sel_hi:[0,1,1]\n\tv_pk_mul_f16 v54, v48, v56 op_sel:[1,1] op_sel_hi:[1,0] neg_lo:[1,0]\n\t" \
  "v_pk_mul_f16 v55, v49, v57 op_sel:[1,1] op_sel_hi:[1,0] neg_lo:[1,0]\n\tv_pk_fma_f16 v54, v48, v56, v54 op_sel:[0,0,0] op_sel_hi:[0,1,1]\n\t" \
  "v_pk_fma_f16 v55, v49, v57, v55 op_sel:[0,0,0] op_sel_hi:[0,1,1]\n\tds_write_addtid_b32 v48 offset:0\n\t" \
  "ds_write_addtid_b32 v49 offset:4608\n\tds_write_addtid_b32 v50 offset:288\n\t" \
  "ds_write_addtid_b32 v51 offset:4896\n\tds_write_addtid_b32 v52 offset:576\n\t" \
  "ds_write_addtid_b32 v53 offset:5184\n\tds_write_addtid_b32 v54 offset:864\n\t" \
  "ds_write_addtid_b32 v55 offset:5472\n\tv_pk_mul_f32 v[60:61], v[40:41], v[40:41] op_sel:[1,1] op_sel_hi:[1,0] neg_lo:[1,0]\n\t" \
  "v_pk_mul_f32 v[62:63], v[42:43], v[42:43] op_sel:[1,1] op_sel_hi:[1,0] neg_lo:[1,0]\n\tv_pk_fma_f32 v[60:61], v[40:41], v[40:41], v[60:61] op_sel:[0,0,0] op_sel_hi:[0,1,1]\n\t" \
  "v_pk_fma_f32 v[62:63], v[42:43], v[42:43], v[62:63] op_sel:[0,0,0] op_sel_hi:[0,1,1]\n\tv_cvt_pk_f16_f32 v48, v68, v69\n\t" \
  "v_cvt_pk_f16_f32 v49, v70, v71\n\tv_pk_mul_f32 v[64:65], v[68:69], v[40:41] op_sel:[1,1] op_sel_hi:[1,0] neg_lo:[1,0]\n\t" \
  "v_pk_mul_f32 v[66:67], v[70:71], v[42:43] op_sel:[1,1] op_sel_hi:[1,0] neg_lo:[1,0]\n\tv_pk_fma_f32 v[64:65], v[68:69], v[40:41], v[64:65] op_sel:[0,0,0] op_sel_hi:[0,1,1]\n\t" \
  "v_pk_fma_f32 v[66:67], v[70:71], v[42:43], v[66:67] op_sel:[0,0,0] op_sel_hi:[0,1,1]\n\tv_pk_mul_f16 v50, v48, v44 op_sel:[1,1] op_sel_hi:[1,0] neg_lo:[1,0]\n\t" \
  "v_pk_mul_f16 v51, v49, v45 op_sel:[1,1] op_sel_hi:[1,0] neg_lo:[1,0]\n\tv_pk_fma_f16 v50, v48, v44, v50 op_sel:[0,0,0] op_sel_hi:[0,1,1]\n\t" \
  "v_pk_fma_f16 v51, v49, v45, v51 op_sel:[0,0,0] op_sel_hi:[0,1,1]\n\tv_pk_mul_f16 v52, v48, v46 op_sel:[1,1] op_sel_hi:[1,0] neg_lo:[1,0]\n\t" \
  "v_pk_mul_f16 v53, v49, v47 op_sel:[1,1] op_sel_hi:[1,0] neg_lo:[1,0]\n\tv_pk_fma_f16 v52, v48, v46, v52 op_sel:[0,0,0] op_sel_hi:[0,1,1]\n\t" \
  "v_pk_fma_f16 v53, v49, v47, v53 op_sel:[0,0,0] op_sel_hi:[0,1,1]\n\tv_pk_mul_f16 v54, v48, v56 op_sel:[1,1] op_sel_hi:[1,0] neg_lo:[1,0]\n\t" \
  "v_pk_mul_f16 v55, v49, v57 op_sel:[1,1] op_sel_hi:[1,0] neg_lo:[1,0]\n\tv_pk_fma_f16 v54, v48, v56, v54 op_sel:[0,0,0] op_sel_hi:[0,1,1]\n\t" \
  "v_pk_fma_f16 v55, v49, v57, v55 op_sel:[0,0,0] op_sel_hi:[0,1,1]\n\tds_write_addtid_b32 v48 offset:1152\n\t" \
  "ds_write_addtid_b32 v49 offset:5760\n\tds_write_addtid_b32 v50 offset:1440\n\t" \
  "ds_write_addtid_b32 v51 offset:6048\n\tds_write_addtid_b32 v52 offset:1728\n\t" \
  "ds_write_addtid_b32 v53 offset:6336\n\tds_write_addtid_b32 v54 offset:2016\n\t" \
  "ds_write_addtid_b32 v55 offset:6624\n\tv_pk_mul_f32 v[36:37], v[60:61], v[60:61] op_sel:[1,1] op_sel_hi:[1,0] neg_lo:[1,0]\n\t" \
  "v_pk_mul_f32 v[38:39], v[62:63], v[62:63] op_sel:[1,1] op_sel_hi:[1,0] neg_lo:[1,0]\n\tv_pk_fma_f32 v[36:37], v[60:61], v[60:61], v[36:37] op_sel:[0,0,0] op_sel_hi:[0,1,1]\n\t" \
  "v_pk_fma_f32 v[38:39], v[62:63], v[62:63], v[38:39] op_sel:[0,0,0] op_sel_hi:[0,1,1]\n\tv_cvt_pk_f16_f32 v48, v64, v65\n\t" \
  "v_cvt_pk_f16_f32 v49, v66, v67\n\tv_pk_mul_f32 v[68:69], v[64:65], v[40:41] op_sel:[1,1] op_sel_hi:[1,0] neg_lo:[1,0]\n\t" \
  "v_pk_mul_f32 v[70:71], v[66:67], v[42:43] op_sel:[1,1] op_sel_hi:[1,0] neg_lo:[1,0]\n\tv_pk_fma_f32 v[68:69], v[64:65], v[40:41], v[68:69] op_sel:[0,0,0] op_sel_hi:[0,1,1]\n\t" \
  "v_pk_fma_f32 v[70:71], v[66:67], v[42:43], v[70:71] op_sel:[0,0,0] op_sel_hi:[0,1,1]\n\tv_pk_mul_f16 v50, v48, v44 op_sel:[1,1] op_sel_hi:[1,0] neg_lo:[1,0]\n\t" \
  "v_pk_mul_f16 v51, v49, v45 op_sel:[1,1] op_sel_hi:[1,0] neg_lo:[1,0]\n\tv_pk_fma_f16 v50, v48, v44, v50 op_sel:[0,0,0] op_sel_hi:[0,1,1]\n\t" \
  "v_pk_fma_f16 v51, v49, v45, v51 op_sel:[0,0,0] op_sel_hi:[0,1,1]\n\tv_pk_mul_f16 v52, v48, v46 op_sel:[1,1] op_sel_hi:[1,0] neg_lo:[1,0]\n\t" \
  "v_pk_mul_f16 v53, v49, v47 op_sel:[1,1] op_sel_hi:[1,0] neg_lo:[1,0]\n\tv_pk_fma_f16 v52, v48, v46, v52 op_sel:[0,0,0] op_sel_hi:[0,1,1]\n\t" \
  "v_pk_fma_f16 v53, v49, v47, v53 op_sel:[0,0,0] op_sel_hi:[0,1,1]\n\tv_pk_mul_f16 v54, v48, v56 op_sel:[1,1] op_sel_hi:[1,0] neg_lo:[1,0]\n\t" \
  "v_pk_mul_f16 v55, v49, v57 op_sel:[1,1] op_sel_hi:[1,0] neg_lo:[1,0]\n\tv_pk_fma_f16 v54, v48, v56, v54 op_sel:[0,0,0] op_sel_hi:[0,1,1]\n\t" \
  "v_pk_fma_f16 v55, v49, v57, v55 op_sel:[0,0,0] op_sel_hi:[0,1,1]\n\tds_write_addtid_b32 v48 offset:2304\n\t" \
  "ds_write_addtid_b32 v49 offset:6912\n\tds_write_addtid_b32 v50 offset:2592\n\t" \
  "ds_write_addtid_b32 v51 offset:7200\n\tds_write_addtid_b32 v52 offset:2880\n\t" \
  "ds_write_addtid_b32 v53 offset:7488\n\tds_write_addtid_b32 v54 offset:3168\n\t" \
  "ds_write_addtid_b32 v55 offset:7776\n\tv_cvt_pk_f16_f32 v58, v36, v37\n\t" \
  "v_cvt_pk_f16_f32 v59, v38, v39\n\tv_cvt_pk_f16_f32 v48, v68, v69\n\t" \
  "v_cvt_pk_f16_f32 v49, v70, v71\n\tv_pk_mul_f16 v50, v48, v44 op_sel:[1,1] op_sel_hi:[1,0] neg_lo:[1,0]\n\t" \
  "v_pk_mul_f16 v51, v49, v45 op_sel:[1,1] op_sel_hi:[1,0] neg_lo:[1,0]\n\tv_pk_fma_f16 v50, v48, v44, v50 op_sel:[0,0,0] op_sel_hi:[0,1,1]\n\t" \
  "v_pk_fma_f16 v51, v49, v45, v51 op_sel:[0,0,0] op_sel_hi:[0,1,1]\n\tv_pk_mul_f16 v52, v48, v46 op_sel:[1,1] op_sel_hi:[1,0] neg_lo:[1,0]\n\t" \
  "v_pk_mul_f16 v53, v49, v47 op_sel:[1,1] op_sel_hi:[1,0] neg_lo:[1,0]\n\tv_pk_fma_f16 v52, v48, v46, v52 op_sel:[0,0,0] op_sel_hi:[0,1,1]\n\t" \
  "v_pk_fma_f16 v53, v49, v47, v53 op_sel:[0,0,0] op_sel_hi:[0,1,1]\n\tv_pk_mul_f16 v54, v48, v56 op_sel:[1,1] op_sel_hi:[1,0] neg_lo:[1,0]\n\t" \
  "v_pk_mul_f16 v55, v49, v57 op_sel:[1,1] op_sel_hi:[1,0] neg_lo:[1,0]\n\tv_pk_fma_f16 v54, v48, v56, v54 op_sel:[0,0,0] op_sel_hi:[0,1,1]\n\t" \
  "v_pk_fma_f16 v55, v49, v57, v55 op_sel:[0,0,0] op_sel_hi:[0,1,1]\n\tds_write_addtid_b32 v48 offset:3456\n\t" \
  "ds_write_addtid_b32 v49 offset:8064\n\tds_write_addtid_b32 v50 offset:3744\n\t" \
  "ds_write_addtid_b32 v51 offset:8352\n\tds_write_addtid_b32 v52 offset:4032\n\t" \
  "ds_write_addtid_b32 v53 offset:8640\n\tds_write_addtid_b32 v54 offset:4320\n\t" \
  "ds_write_addtid_b32 v55 offset:8928\n\tds_read_b128 v[72:75], v27 offset:0\n\t" \
  "ds_read_b128 v[76:79], v27 offset:64\n\tds_read_b128 v[80:83], v27 offset:128\n\t" \
  "ds_read_b128 v[84:87], v27 offset:192\n\tds_read_b128 v[88:91], v27 offset:4608\n\t" \
  "ds_read_b128 v[92:95], v27 offset:4672\n\tds_read_b128 v[96:99], v27 offset:4736\n\t" \
  "ds_read_b128 v[100:103], v27 offset:4800\n\tds_write_addtid_b32 v58 offset:0\n\t" \
  "ds_write_addtid_b32 v59 offset:4608\n\tds_read_b128 v[40:43], v12 offset:0\n\t" \
  "ds_read_b128 v[44:47], v12 offset:64\n\tds_read_b128 v[48:51], v12 offset:128\n\t" \
  "v_add_u32_sdwa v56, %[w], v16 dst_sel:DWORD dst_unused:UNUSED_PAD src0_sel:DWORD src1_sel:WORD_0\n\tv_add_u32_sdwa v57, %[w], v16 dst_sel:DWORD dst_unused:UNUSED_PAD src0_sel:DWORD src1_sel:WORD_1\n\t" \
  "v_add_u32_sdwa v58, %[w], v17 dst_sel:DWORD dst_unused:UNUSED_PAD src0_sel:DWORD src1_sel:WORD_0\n\tv_add_u32_sdwa v59, %[w], v17 dst_sel:DWORD dst_unused:UNUSED_PAD src0_sel:DWORD src1_sel:WORD_1\n\t" \
  "s_cmp_lt_u32 %[w], 50176\n\ts_cbranch_scc1 1f\n\t" \
  "s_setprio 1\n\t1:\n\t" \
  "v_add_u32 v14, v56, v35\n\tv_add_u32 v15, v57, v35\n\t" \
  "s_waitcnt lgkmcnt(12)\n\tv_mfma_f32_16x16x32_f16 v[60:63], v[104:107], v[72:75], 0\n\t" \
  "v_mfma_f32_16x16x32_f16 v[36:39], v[72:75], v[136:139], 0\n\tv_mfma_f32_16x16x32_f16 v[4:7], v[136:139], v[72:75], 0\n\t" \
  "s_waitcnt lgkmcnt(2)\n\tv_mfma_f32_16x16x32_f16 v[60:63], v[108:111], v[76:79], v[60:63]\n\t" \
  "v_pk_mul_f16 v52, v40, v72 op_sel:[1,1] op_sel_hi:[1,0] neg_lo:[1,0]\n\tv_pk_mul_f16 v53, v41, v73 op_sel:[1,1] op_sel_hi:[1,0] neg_lo:[1,0]\n\t" \
  "v_pk_mul_f16 v54, v42, v74 op_sel:[1,1] op_sel_hi:[1,0] neg_lo:[1,0]\n\tv_mfma_f32_16x16x32_f16 v[36:39], v[76:79], v[140:143], v[36:39]\n\t" \
  "v_pk_mul_f16 v55, v43, v75 op_sel:[1,1] op_sel_hi:[1,0] neg_lo:[1,0]\n\tv_pk_fma_f16 v40, v40, v72, v52 op_sel:[0,0,0] op_sel_hi:[0,1,1]\n\t" \
  "v_pk_fma_f16 v41, v41, v73, v53 op_sel:[0,0,0] op_sel_hi:[0,1,1]\n\tv_mfma_f32_16x16x32_f16 v[4:7], v[140:143], v[76:79], v[4:7]\n\t" \
  "v_pk_fma_f16 v42, v42, v74, v54 op_sel:[0,0,0] op_sel_hi:[0,1,1]\n\tv_pk_fma_f16 v43, v43, v75, v55 op_sel:[0,0,0] op_sel_hi:[0,1,1]\n\t" \
  "s_waitcnt lgkmcnt(1)\n\tv_mfma_f32_16x16x32_f16 v[60:63], v[112:115], v[80:83], v[60:63]\n\t" \
  "ds_read_b128 v[72:75], v12 offset:192\n\tv_pk_mul_f16 v52, v44, v76 op_sel:[1,1] op_sel_hi:[1,0] neg_lo:[1,0]\n\t" \
  "v_pk_mul_f16 v53, v45, v77 op_sel:[1,1] op_sel_hi:[1,0] neg_lo:[1,0]\n\tv_pk_mul_f16 v54, v46, v78 op_sel:[1,1] op_sel_hi:[1,0] neg_lo:[1,0]\n\t" \
  "v_mfma_f32_16x16x32_f16 v[36:39], v[80:83], v[144:147], v[36:39]\n\tv_pk_mul_f16 v55, v47, v79 op_sel:[1,1] op_sel_hi:[1,0] neg_lo:[1,0]\n\t" \
  "v_pk_fma_f16 v44, v44, v76, v52 op_sel:[0,0,0] op_sel_hi:[0,1,1]\n\tv_pk_fma_f16 v45, v45, v77, v53 op_sel:[0,0,0] op_sel_hi:[0,1,1]\n\t" \
  "v_mfma_f32_16x16x32_f16 v[4:7], v[144:147], v[80:83], v[4:7]\n\tv_pk_fma_f16 v46, v46, v78, v54 op_sel:[0,0,0] op_sel_hi:[0,1,1]\n\t" \
  "v_pk_fma_f16 v47, v47, v79, v55 op_sel:[0,0,0] op_sel_hi:[0,1,1]\n\ts_waitcnt lgkmcnt(1)\n\t" \
  "v_mfma_f32_16x16x32_f16 v[60:63], v[116:119], v[84:87], v[60:63]\n\tds_read_b128 v[76:79], v12 offset:4608\n\t" \
  "v_pk_mul_f16 v52, v48, v80 op_sel:[1,1] op_sel_hi:[1,0] neg_lo:[1,0]\n\tv_pk_mul_f16 v53, v49, v81 op_sel:[1,1] op_sel_hi:[1,0] neg_lo:[1,0]\n\t" \
  "v_pk_mul_f16 v54, v50, v82 op_sel:[1,1] op_sel_hi:[1,0] neg_lo:[1,0]\n\tv_mfma_f32_16x16x32_f16 v[36:39], v[84:87], v[148:151], v[36:39]\n\t" \
  "v_pk_mul_f16 v55, v51, v83 op_sel:[1,1] op_sel_hi:[1,0] neg_lo:[1,0]\n\tv_pk_fma_f16 v48, v48, v80, v52 op_sel:[0,0,0] op_sel_hi:[0,1,1]\n\t" \
  "v_pk_fma_f16 v49, v49, v81, v53 op_sel:[0,0,0] op_sel_hi:[0,1,1]\n\tv_mfma_f32_16x16x32_f16 v[4:7], v[148:151], v[84:87], v[4:7]\n\t" \
  "v_pk_fma_f16 v50, v50, v82, v54 op_sel:[0,0,0] op_sel_hi:[0,1,1]\n\tv_pk_fma_f16 v51, v51, v83, v55 op_sel:[0,0,0] op_sel_hi:[0,1,1]\n\t" \
  "s_waitcnt lgkmcnt(1)\n\tv_mfma_f32_16x16x32_f16 v[60:63], v[120:123], v[88:91], v[60:63]\n\t" \
  "ds_read_b128 v[80:83], v12 offset:4672\n\tv_pk_mul_f16 v52, v72, v84 op_sel:[1,1] op_sel_hi:[1,0] neg_lo:[1,0]\n\t" \
  "v_pk_mul_f16 v53, v73, v85 op_sel:[1,1] op_sel_hi:[1,0] neg_lo:[1,0]\n\tv_pk_mul_f16 v54, v74, v86 op_sel:[1,1] op_sel_hi:[1,0] neg_lo:[1,0]\n\t" \
  "v_mfma_f32_16x16x32_f16 v[36:39], v[88:91], v[152:155], v[36:39]\n\tv_pk_mul_f16 v55, v75, v87 op_sel:[1,1] op_sel_hi:[1,0] neg_lo:[1,0]\n\t" \
  "v_pk_fma_f16 v72, v72, v84, v52 op_sel:[0,0,0] op_sel_hi:[0,1,1]\n\tv_pk_fma_f16 v73, v73, v85, v53 op_sel:[0,0,0] op_sel_hi:[0,1,1]\n\t" \
  "v_mfma_f32_16x16x32_f16 v[4:7], v[152:155], v[88:91], v[4:7]\n\tv_pk_fma_f16 v74, v74, v86, v54 op_sel:[0,0,0] op_sel_hi:[0,1,1]\n\t" \
  "v_pk_fma_f16 v75, v75, v87, v55 op_sel:[0,0,0] op_sel_hi:[0,1,1]\n\ts_waitcnt lgkmcnt(1)\n\t" \
  "v_mfma_f32_16x16x32_f16 v[60:63], v[124:127], v[92:95], v[60:63]\n\tds_read_b128 v[84:87], v12 offset:4736\n\t" \
  "v_pk_mul_f16 v52, v76, v88 op_sel:[1,1] op_sel_hi:[1,0] neg_lo:[1,0]\n\tv_pk_mul_f16 v53, v77, v89 op_sel:[1,1] op_sel_hi:[1,0] neg_lo:[1,0]\n\t" \
  "v_pk_mul_f16 v54, v78, v90 op_sel:[1,1] op_sel_hi:[1,0] neg_lo:[1,0]\n\tv_mfma_f32_16x16x32_f16 v[36:39], v[92:95], v[156:159], v[36:39]\n\t" \
  "v_pk_mul_f16 v55, v79, v91 op_sel:[1,1] op_sel_hi:[1,0] neg_lo:[1,0]\n\tv_pk_fma_f16 v76, v76, v88, v52 op_sel:[0,0,0] op_sel_hi:[0,1,1]\n\t" \
  "v_pk_fma_f16 v77, v77, v89, v53 op_sel:[0,0,0] op_sel_hi:[0,1,1]\n\tv_mfma_f32_16x16x32_f16 v[4:7], v[156:159], v[92:95], v[4:7]\n\t" \
  "v_pk_fma_f16 v78, v78, v90, v54 op_sel:[0,0,0] op_sel_hi:[0,1,1]\n\tv_pk_fma_f16 v79, v79, v91, v55 op_sel:[0,0,0] op_sel_hi:[0,1,1]\n\t" \
  "s_waitcnt lgkmcnt(1)\n\tv_mfma_f32_16x16x32_f16 v[60:63], v[128:131], v[96:99], v[60:63]\n\t" \
  "ds_read_b128 v[88:91], v12 offset:4800\n\tv_pk_mul_f16 v52, v80, v92 op_sel:[1,1] op_sel_hi:[1,0] neg_lo:[1,0]\n\t" \
  "v_pk_mul_f16 v53, v81, v93 op_sel:[1,1] op_sel_hi:[1,0] neg_lo:[1,0]\n\tv_pk_mul_f16 v54, v82, v94 op_sel:[1,1] op_sel_hi:[1,0] neg_lo:[1,0]\n\t" \
  "v_mfma_f32_16x16x32_f16 v[36:39], v[96:99], v[160:163], v[36:39]\n\tv_pk_mul_f16 v55, v83, v95 op_sel:[1,1] op_sel_hi:[1,0] neg_lo:[1,0]\n\t" \
  "v_pk_fma_f16 v80, v80, v92, v52 op_sel:[0,0,0] op_sel_hi:[0,1,1]\n\tv_pk_fma_f16 v81, v81, v93, v53 op_sel:[0,0,0] op_sel_hi:[0,1,1]\n\t" \
  "v_mfma_f32_16x16x32_f16 v[4:7], v[160:163], v[96:99], v[4:7]\n\tv_pk_fma_f16 v82, v82, v94, v54 op_sel:[0,0,0] op_sel_hi:[0,1,1]\n\t" \
  "v_pk_fma_f16 v83, v83, v95, v55 op_sel:[0,0,0] op_sel_hi:[0,1,1]\n\ts_waitcnt lgkmcnt(1)\n\t" \
  "v_mfma_f32_16x16x32_f16 v[60:63], v[132:135], v[100:103], v[60:63]\n\tv_pk_mul_f16 v52, v84, v96 op_sel:[1,1] op_sel_hi:[1,0] neg_lo:[1,0]\n\t" \
  "v_pk_mul_f16 v53, v85, v97 op_sel:[1,1] op_sel_hi:[1,0] neg_lo:[1,0]\n\tv_pk_mul_f16 v54, v86, v98 op_sel:[1,1] op_sel_hi:[1,0] neg_lo:[1,0]\n\t" \
  "v_mfma_f32_16x16x32_f16 v[36:39], v[100:103], v[164:167], v[36:39]\n\tv_pk_mul_f16 v55, v87, v99 op_sel:[1,1] op_sel_hi:[1,0] neg_lo:[1,0]\n\t" \
  "v_pk_fma_f16 v84, v84, v96, v52 op_sel:[0,0,0] op_sel_hi:[0,1,1]\n\tv_pk_fma_f16 v85, v85, v97, v53 op_sel:[0,0,0] op_sel_hi:[0,1,1]\n\t" \
  "v_mfma_f32_16x16x32_f16 v[4:7], v[164:167], v[100:103], v[4:7]\n\tv_pk_fma_f16 v86, v86, v98, v54 op_sel:[0,0,0] op_sel_hi:[0,1,1]\n\t" \
  "v_pk_fma_f16 v87, v87, v99, v55 op_sel:[0,0,0] op_sel_hi:[0,1,1]\n\ts_waitcnt lgkmcnt(0)\n\t" \
  "v_mfma_f32_16x16x32_f16 v[64:67], v[104:107], v[40:43], 0\n\tv_pk_mul_f16 v52, v88, v100 op_sel:[1,1] op_sel_hi:[1,0] neg_lo:[1,0]\n\t" \
  "v_pk_mul_f16 v53, v89, v101 op_sel:[1,1] op_sel_hi:[1,0] neg_lo:[1,0]\n\tv_pk_mul_f16 v54, v90, v102 op_sel:[1,1] op_sel_hi:[1,0] neg_lo:[1,0]\n\t" \
  "v_mfma_f32_16x16x32_f16 v[68:71], v[40:43], v[136:139], 0\n\tv_pk_mul_f16 v55, v91, v103 op_sel:[1,1] op_sel_hi:[1,0] neg_lo:[1,0]\n\t" \
  "v_pk_fma_f16 v88, v88, v100, v52 op_sel:[0,0,0] op_sel_hi:[0,1,1]\n\tv_pk_fma_f16 v89, v89, v101, v53 op_sel:[0,0,0] op_sel_hi:[0,1,1]\n\t" \
  "v_mfma_f32_16x16x32_f16 v[8:11], v[136:139], v[40:43], 0\n\tv_pk_fma_f16 v90, v90, v102, v54 op_sel:[0,0,0] op_sel_hi:[0,1,1]\n\t" \
  "v_pk_fma_f16 v91, v91, v103, v55 op_sel:[0,0,0] op_sel_hi:[0,1,1]\n\tv_mov_b64 v[100:101], 0\n\t" \
  "v_mfma_f32_16x16x32_f16 v[64:67], v[108:111], v[44:47], v[64:67]\n\tv_mov_b64 v[102:103], 0\n\t" \
  "v_cvt_pk_f16_f32 v94, v36, v37\n\tv_cvt_pk_f16_f32 v95, v38, v39\n\t" \
  "v_mfma_f32_16x16x32_f16 v[68:71], v[44:47], v[140:143], v[68:71]\n\tv_cvt_pk_f16_f32 v4, v4, v5\n\t" \
  "v_cvt_pk_f16_f32 v5, v6, v7\n\tv_mov_b32_dpp v94, v94 row_shl:4 row_mask:0xf bank_mask:0x5\n\t" \
  "v_mfma_f32_16x16x32_f16 v[8:11], v[140:143], v[44:47], v[8:11]\n\tv_mov_b32_dpp v95, v95 row_shl:4 row_mask:0xf bank_mask:0x5\n\t" \
  "v_permlane16_swap_b32 v4, v5\n\tv_cvt_pk_f16_f32 v92, v60, v61\n\t" \
  "v_mfma_f32_16x16x32_f16 v[64:67], v[112:115], v[48:51], v[64:67]\n\tv_cvt_pk_f16_f32 v93, v62, v63\n\t" \
  "v_alignbit_b32 v96, v95, v94, 16\n\tv_perm_b32 v6, v4, v4, v33\n\t" \
  "v_mfma_f32_16x16x32_f16 v[68:71], v[48:51], v[144:147], v[68:71]\n\tv_perm_b32 v7, v4, v4, v34\n\t" \
  "v_mfma_f32_16x16x32_f16 v[8:11], v[144:147], v[48:51], v[8:11]\n\tds_write_b128 v58, v[100:103]\n\t" \
  "v_mfma_f32_16x16x32_f16 v[64:67], v[116:119], v[72:75], v[64:67]\n\tds_write2_b32 v56, v92, v93 offset0:193 offset1:194\n\t" \
  "v_mfma_f32_16x16x32_f16 v[68:71], v[72:75], v[148:151], v[68:71]\n\tds_write2_b32 v57, v92, v93 offset0:193 offset1:194\n\t" \
  "v_mfma_f32_16x16x32_f16 v[8:11], v[148:151], v[72:75], v[8:11]\n\tds_write2_b32 v14, v6, v7 offset0:64 offset1:65\n\t" \
  "v_mfma_f32_16x16x32_f16 v[64:67], v[120:123], v[76:79], v[64:67]\n\tds_write2_b32 v15, v6, v7 offset0:64 offset1:65\n\t" \
  "v_mfma_f32_16x16x32_f16 v[68:71], v[76:79], v[152:155], v[68:71]\n\tds_write2_b32 v59, v94, v95 offset0:4 offset1:5\n\t" \
  "v_mfma_f32_16x16x32_f16 v[8:11], v[152:155], v[76:79], v[8:11]\n\tds_write_b16 v59, v94 offset:846\n\t" \
  "v_mfma_f32_16x16x32_f16 v[64:67], v[124:127], v[80:83], v[64:67]\n\tds_write_b32 v59, v96 offset:848\n\t" \
  "v_mfma_f32_16x16x32_f16 v[68:71], v[80:83], v[156:159], v[68:71]\n\tds_write_b16_d16_hi v59, v95 offset:852\n\t" \
  "v_mfma_f32_16x16x32_f16 v[8:11], v[156:159], v[80:83], v[8:11]\n\tv_mfma_f32_16x16x32_f16 v[64:67], v[128:131], v[84:87], v[64:67]\n\t" \
  "v_mfma_f32_16x16x32_f16 v[68:71], v[84:87], v[160:163], v[68:71]\n\tv_mfma_f32_16x16x32_f16 v[8:11], v[160:163], v[84:87], v[8:11]\n\t" \
  "v_mfma_f32_16x16x32_f16 v[68:71], v[88:91], v[164:167], v[68:71]\n\tv_mfma_f32_16x16x32_f16 v[8:11], v[164:167], v[88:91], v[8:11]\n\t" \
  "v_mfma_f32_16x16x32_f16 v[64:67], v[132:135], v[88:91], v[64:67]\n\ts_nop 5\n\t" \
  "v_cvt_pk_f16_f32 v48, v68, v69\n\tv_cvt_pk_f16_f32 v49, v70, v71\n\t" \
  "v_cvt_pk_f16_f32 v8, v8, v9\n\tv_cvt_pk_f16_f32 v9, v10, v11\n\t" \
  "v_mov_b32_dpp v48, v48 row_shl:4 row_mask:0xf bank_mask:0x5\n\tv_mov_b32_dpp v49, v49 row_shl:4 row_mask:0xf bank_mask:0x5\n\t" \
  "v_permlane16_swap_b32 v8, v9\n\tv_cvt_pk_f16_f32 v46, v64, v65\n\t" \
  "v_cvt_pk_f16_f32 v47, v66, v67\n\tv_alignbit_b32 v50, v49, v48, 16\n\t" \
  "v_perm_b32 v10, v8, v8, v33\n\tv_perm_b32 v11, v8, v8, v34\n\t" \
  "ds_write2_b32 v56, v46, v47 offset0:129 offset1:130\n\tds_write2_b32 v57, v46, v47 offset0:129 offset1:130\n\t" \
  "ds_write2_b32 v14, v10, v11 offset0:0 offset1:1\n\tds_write2_b32 v15, v10, v11 offset0:0 offset1:1\n\t" \
  "ds_write2_b32 v59, v48, v49 offset0:0 offset1:1\n\tds_write_b16 v59, v48 offset:830\n\t" \
  "ds_write_b32 v59, v50 offset:832\n\tds_write_b16_d16_hi v59, v49 offset:836\n\t"
#define GEN_CLOBBER "v4", "v5", "v6", "v7", "v9", "v10", "v11", "v13", "v14", "v15", "v36", "v37", "v38", "v39", "v40", "v41", "v42", "v43", "v44", "v45", "v47", "v49", "v50", "v51", "v52", "v53", "v54", "v55", "v56", "v57", "v58", "v59", "v60", "v61", "v62", "v63", "v64", "v65", "v66", "v67", "v72", "v73", "v74", "v75", "v76", "v77", "v78", "v79", "v80", "v81", "v82", "v83", "v84", "v85", "v86", "v87", "v88", "v89", "v90", "v91", "v92", "v94", "v95", "v98", "v99", "v100", "v101", "v102", "v103", "v104", "v105", "v106", "v107", "v108", "v109", "v110", "v111", "v112", "v113", "v114", "v115", "v116", "v117", "v118", "v119", "v120", "v121", "v122", "v123", "v124", "v125", "v126", "v127", "v128", "v129", "v130", "v131", "v132", "v133", "v134", "v135", "v136", "v137", "v138", "v139", "v140", "v141", "v142", "v143", "v144", "v145", "v146", "v147", "v148", "v149", "v150", "v151", "v152", "v153", "v154", "v155", "v156", "v157", "v158", "v159", "v160", "v161", "v162", "v163", "v164", "v165", "v166", "v167"
#define CONV_ASM \
  "ds_read_b32 v34, v30\n\tds_read_b64 v[132:133], v26 offset:512\n\tds_read_b64 v[134:135], v26 offset:520\n\t" \
  "ds_read_b64 v[100:101], v26 offset:0\n\tds_read_b64 v[102:103], v26 offset:8\n\tds_read_b64 v[136:137], v26 offset:576\n\t" \
  "ds_read_b64 v[138:139], v26 offset:584\n\tds_read_b64 v[104:105], v26 offset:64\n\tds_read_b64 v[106:107], v26 offset:72\n\t" \
  "ds_read_b64 v[140:141], v26 offset:640\n\tds_read_b64 v[142:143], v26 offset:648\n\tds_read_b64 v[108:109], v26 offset:128\n\t" \
  "ds_read_b64 v[110:111], v26 offset:136\n\tds_read_b64 v[144:145], v26 offset:704\n\tds_read_b64 v[146:147], v26 offset:712\n\t" \
  "ds_read_b64 v[112:113], v26 offset:192\n\tds_read_b64 v[114:115], v26 offset:200\n\tds_read_b64 v[148:149], v26 offset:768\n\t" \
  "ds_read_b64 v[150:151], v26 offset:776\n\tds_read_b64 v[116:117], v26 offset:256\n\tds_read_b64 v[118:119], v26 offset:264\n\t" \
  TS(60, 61) "s_waitcnt lgkmcnt(15)\n\tv_cmp_gt_u32 vcc, 1, v34\n\t" \
  "s_cbranch_vccz 2f\n\ts_mov_b32 s6, 0x10000\n\t1:\n\t" \
  "s_sleep 1\n\tds_read_b32 v34, v30\n\ts_sub_u32 s6, s6, 1\n\t" \
  "s_waitcnt lgkmcnt(0)\n\tv_cmp_gt_u32 vcc, 1, v34\n\ts_cbranch_scc1 2f\n\t" \
  "s_cbranch_vccnz 1b\n\t2:\n\t" TS(62, 63) \
  "s_cmp_lt_u32 %[w], 4\n\ts_setprio 1\n\ts_cbranch_scc1 1f\n\t" \
  "s_setprio 2\n\t1:\n\tds_read_b128 v[0:3], v28 offset:0\n\t" \
  "ds_read_b128 v[4:7], v28 offset:64\n\tds_read_b128 v[8:11], v28 offset:128\n\tds_read_b128 v[12:15], v28 offset:192\n\t" \
  "ds_read_b128 v[16:19], v28 offset:256\n\tds_read_b128 v[20:23], v28 offset:320\n\ts_waitcnt lgkmcnt(5)\n\t" \
  "v_mfma_f32_16x16x32_f16 v[52:55], v[132:135], v[0:3], 0\n\tds_read_b64 v[152:153], v26 offset:832\n\tds_read_b64 v[154:155], v26 offset:840\n\t" \
  "v_mfma_f32_16x16x32_f16 v[56:59], v[100:103], v[0:3], 0\n\tds_read_b64 v[120:121], v26 offset:320\n\tds_read_b64 v[122:123], v26 offset:328\n\t" \
  "ds_read_b128 v[0:3], v28 offset:384\n\ts_waitcnt lgkmcnt(9)\n\tv_mfma_f32_16x16x32_f16 v[52:55], v[136:139], v[4:7], v[52:55]\n\t" \
  "ds_read_b64 v[156:157], v26 offset:896\n\tds_read_b64 v[158:159], v26 offset:904\n\tv_mfma_f32_16x16x32_f16 v[56:59], v[104:107], v[4:7], v[56:59]\n\t" \
  "ds_read_b64 v[124:125], v26 offset:384\n\tds_read_b64 v[126:127], v26 offset:392\n\tds_read_b128 v[4:7], v28 offset:448\n\t" \
  "s_waitcnt lgkmcnt(13)\n\tv_mfma_f32_16x16x32_f16 v[52:55], v[140:143], v[8:11], v[52:55]\n\tds_read_b64 v[160:161], v26 offset:960\n\t" \
  "ds_read_b64 v[162:163], v26 offset:968\n\tv_mfma_f32_16x16x32_f16 v[56:59], v[108:111], v[8:11], v[56:59]\n\tds_read_b64 v[128:129], v26 offset:448\n\t" \
  "ds_read_b64 v[130:131], v26 offset:456\n\tds_read_b128 v[8:11], v28 offset:512\n\ts_waitcnt lgkmcnt(15)\n\t" \
  "v_mfma_f32_16x16x32_f16 v[52:55], v[144:147], v[12:15], v[52:55]\n\tds_read_b64 v[164:165], v26 offset:1024\n\tds_read_b64 v[166:167], v26 offset:1032\n\t" \
  "v_mfma_f32_16x16x32_f16 v[56:59], v[112:115], v[12:15], v[56:59]\n\tds_read_b64 v[76:77], v31 offset:72\n\tds_read_b64 v[78:79], v31 offset:176\n\t" \
  "ds_read_b128 v[12:15], v28 offset:576\n\tv_mfma_f32_16x16x32_f16 v[52:55], v[148:151], v[16:19], v[52:55]\n\tds_read_b64 v[80:81], v31 offset:40\n\t" \
  "ds_read_b64 v[82:83], v31 offset:144\n\tv_mfma_f32_16x16x32_f16 v[56:59], v[116:119], v[16:19], v[56:59]\n\tds_read_b64 v[84:85], v31 offset:8\n\t" \
  "ds_read_b64 v[86:87], v31 offset:112\n\tds_read_b128 v[16:19], v28 offset:640\n\ts_waitcnt lgkmcnt(15)\n\t" \
  "v_mfma_f32_16x16x32_f16 v[52:55], v[152:155], v[20:23], v[52:55]\n\tds_read_b64 v[88:89], v31 offset:280\n\tds_read_b64 v[90:91], v31 offset:384\n\t" \
  "v_mfma_f32_16x16x32_f16 v[56:59], v[120:123], v[20:23], v[56:59]\n\tds_read_b64 v[92:93], v31 offset:248\n\tds_read_b64 v[94:95], v31 offset:352\n\t" \
  "ds_read_b128 v[20:23], v28 offset:704\n\tv_mfma_f32_16x16x32_f16 v[52:55], v[156:159], v[0:3], v[52:55]\n\tds_read_b64 v[96:97], v31 offset:216\n\t" \
  "ds_read_b64 v[98:99], v31 offset:320\n\tv_mfma_f32_16x16x32_f16 v[56:59], v[124:127], v[0:3], v[56:59]\n\tds_read_b128 v[0:3], v28 offset:768\n\t" \
  "s_waitcnt lgkmcnt(15)\n\tv_mfma_f32_16x16x32_f16 v[52:55], v[160:163], v[4:7], v[52:55]\n\tv_mfma_f32_16x16x32_f16 v[56:59], v[128:131], v[4:7], v[56:59]\n\t" \
  "ds_read_b128 v[4:7], v28 offset:832\n\tv_mfma_f32_16x16x32_f16 v[52:55], v[164:167], v[8:11], v[52:55]\n\tv_mfma_f32_16x16x32_f16 v[56:59], v[132:135], v[8:11], v[56:59]\n\t" \
  "v_mfma_f32_16x16x32_f16 v[60:63], v[100:103], v[8:11], 0\n\tds_read_b128 v[8:11], v28 offset:896\n\ts_waitcnt lgkmcnt(15)\n\t" \
  "v_mfma_f32_16x16x32_f16 v[56:59], v[136:139], v[12:15], v[56:59]\n\tds_read_b32 v34, v30\n\tv_mfma_f32_16x16x32_f16 v[60:63], v[104:107], v[12:15], v[60:63]\n\t" \
  "ds_read_b128 v[12:15], v28 offset:960\n\tv_cvt_pk_bf16_f32 v52, v52, v53\n\tv_cvt_pk_bf16_f32 v53, v54, v55\n\t" \
  TS(64, 65) "s_waitcnt lgkmcnt(12)\n\tv_mfma_f32_16x16x32_f16 v[56:59], v[140:143], v[16:19], v[56:59]\n\t" \
  "ds_write_b64 v29, v[52:53] offset:0\n\tds_write_b32 v25, v25 offset:0\n\tv_mfma_f32_16x16x32_f16 v[60:63], v[108:111], v[16:19], v[60:63]\n\t" \
  "s_waitcnt lgkmcnt(3)\n\tv_cmp_gt_u32 vcc, 2, v34\n\ts_cbranch_vccz 2f\n\t" \
  "s_mov_b32 s6, 0x10000\n\t1:\n\ts_sleep 1\n\t" \
  "ds_read_b32 v34, v30\n\ts_sub_u32 s6, s6, 1\n\ts_waitcnt lgkmcnt(0)\n\t" \
  "v_cmp_gt_u32 vcc, 2, v34\n\ts_cbranch_scc1 2f\n\ts_cbranch_vccnz 1b\n\t" \
  "2:\n\tds_read_b128 v[16:19], v28 offset:1024\n\tv_mfma_f32_16x16x32_f16 v[56:59], v[144:147], v[20:23], v[56:59]\n\t" \
  "v_mfma_f32_16x16x32_f16 v[60:63], v[112:115], v[20:23], v[60:63]\n\tds_read_b128 v[68:71], v32\n\tds_read_b128 v[72:75], v32 offset:1024\n\t" \
  "ds_read_b128 v[20:23], v28 offset:1088\n\tv_mfma_f32_16x16x32_f16 v[56:59], v[148:151], v[0:3], v[56:59]\n\tv_mfma_f32_16x16x32_f16 v[60:63], v[116:119], v[0:3], v[60:63]\n\t" \
  "ds_read_b128 v[0:3], v28 offset:496\n\tv_mfma_f32_16x16x32_f16 v[56:59], v[152:155], v[4:7], v[56:59]\n\tv_mfma_f32_16x16x32_f16 v[60:63], v[120:123], v[4:7], v[60:63]\n\t" \
  "ds_read_b128 v[4:7], v28 offset:1152\n\tv_mfma_f32_16x16x32_f16 v[56:59], v[156:159], v[8:11], v[56:59]\n\tv_mfma_f32_16x16x32_f16 v[60:63], v[124:127], v[8:11], v[60:63]\n\t" \
  "ds_read_b128 v[8:11], v28 offset:1216\n\ts_waitcnt lgkmcnt(9)\n\tv_mfma_f32_16x16x32_f16 v[56:59], v[160:163], v[12:15], v[56:59]\n\t" \
  "v_mfma_f32_16x16x32_f16 v[60:63], v[128:131], v[12:15], v[60:63]\n\tds_read_b128 v[12:15], v28 offset:480\n\ts_waitcnt lgkmcnt(7)\n\t" \
  "v_mfma_f32_16x16x32_f16 v[56:59], v[164:167], v[16:19], v[56:59]\n\tv_mfma_f32_16x16x32_f16 v[60:63], v[132:135], v[16:19], v[60:63]\n\tv_mfma_f32_16x16x32_f16 v[64:67], v[100:103], v[16:19], 0\n\t" \
  "ds_read_b128 v[16:19], v28 offset:1280\n\ts_waitcnt lgkmcnt(5)\n\tv_mfma_f32_16x16x32_f16 v[60:63], v[136:139], v[20:23], v[60:63]\n\t" \
  "v_mfma_f32_16x16x32_f16 v[64:67], v[104:107], v[20:23], v[64:67]\n\tds_read_b128 v[20:23], v28 offset:1344\n\ts_waitcnt lgkmcnt(5)\n\t" \
  "v_mfma_f32_16x16x32_f16 v[36:39], v[0:3], v[68:71], 0\n\tv_cvt_pk_bf16_f32 v56, v56, v57\n\tv_cvt_pk_bf16_f32 v57, v58, v59\n\t" \
  "v_mfma_f32_16x16x32_f16 v[40:43], v[0:3], v[72:75], 0\n\tds_write_b64 v29, v[56:57] offset:512\n\tds_write_b32 v25, v25 offset:64\n\t" \
  "ds_read_b128 v[0:3], v28 offset:1408\n\ts_waitcnt lgkmcnt(7)\n\tv_mfma_f32_16x16x32_f16 v[60:63], v[140:143], v[4:7], v[60:63]\n\t" \
  "v_cvt_pk_f16_f32 v36, v36, v37\n\tv_cvt_pk_f16_f32 v37, v38, v39\n\tv_mfma_f32_16x16x32_f16 v[64:67], v[108:111], v[4:7], v[64:67]\n\t" \
  "ds_read_b128 v[4:7], v28 offset:1472\n\tv_cvt_pk_f16_f32 v38, v40, v41\n\tv_cvt_pk_f16_f32 v39, v42, v43\n\t" \
  "s_waitcnt lgkmcnt(7)\n\tv_mfma_f32_16x16x32_f16 v[60:63], v[144:147], v[8:11], v[60:63]\n\tv_mfma_f32_16x16x32_f16 v[64:67], v[112:115], v[8:11], v[64:67]\n\t" \
  "ds_read_b128 v[8:11], v28 offset:1536\n\tv_mfma_f32_16x16x32_f16 v[60:63], v[36:39], v[76:79], v[60:63]\n\tv_mfma_f32_16x16x32_f16 v[64:67], v[36:39], v[80:83], v[64:67]\n\t" \
  "v_mfma_f32_16x16x32_f16 v[52:55], v[36:39], v[84:87], 0\n\ts_waitcnt lgkmcnt(7)\n\tv_mfma_f32_16x16x32_f16 v[44:47], v[12:15], v[68:71], 0\n\t" \
  "v_mfma_f32_16x16x32_f16 v[40:43], v[12:15], v[72:75], 0\n\tds_read_b128 v[12:15], v28 offset:1600\n\ts_waitcnt lgkmcnt(7)\n\t" \
  "v_mfma_f32_16x16x32_f16 v[60:63], v[148:151], v[16:19], v[60:63]\n\tv_mfma_f32_16x16x32_f16 v[64:67], v[116:119], v[16:19], v[64:67]\n\tds_read_b128 v[16:19], v28 offset:1008\n\t" \
  "s_waitcnt lgkmcnt(7)\n\tv_mfma_f32_16x16x32_f16 v[60:63], v[152:155], v[20:23], v[60:63]\n\tv_cvt_pk_f16_f32 v44, v44, v45\n\t" \
  "v_cvt_pk_f16_f32 v45, v46, v47\n\tv_cvt_pk_f16_f32 v46, v40, v41\n\tv_cvt_pk_f16_f32 v47, v42, v43\n\t" \
  "v_mfma_f32_16x16x32_f16 v[64:67], v[120:123], v[20:23], v[64:67]\n\tds_read_b128 v[20:23], v28 offset:1664\n\tv_mfma_f32_16x16x32_f16 v[60:63], v[44:47], v[88:91], v[60:63]\n\t" \
  "v_mfma_f32_16x16x32_f16 v[64:67], v[44:47], v[92:95], v[64:67]\n\tv_mfma_f32_16x16x32_f16 v[52:55], v[44:47], v[96:99], v[52:55]\n\ts_waitcnt lgkmcnt(5)\n\t" \
  "v_mfma_f32_16x16x32_f16 v[60:63], v[156:159], v[0:3], v[60:63]\n\tv_mfma_f32_16x16x32_f16 v[64:67], v[124:127], v[0:3], v[64:67]\n\tds_read_b128 v[0:3], v28 offset:1728\n\t" \
  "s_waitcnt lgkmcnt(5)\n\tv_mfma_f32_16x16x32_f16 v[60:63], v[160:163], v[4:7], v[60:63]\n\tv_mfma_f32_16x16x32_f16 v[64:67], v[128:131], v[4:7], v[64:67]\n\t" \
  "ds_read_b128 v[4:7], v28 offset:992\n\ts_waitcnt lgkmcnt(5)\n\tv_mfma_f32_16x16x32_f16 v[60:63], v[164:167], v[8:11], v[60:63]\n\t" \
  "v_mfma_f32_16x16x32_f16 v[64:67], v[132:135], v[8:11], v[64:67]\n\tv_mfma_f32_16x16x32_f16 v[52:55], v[100:103], v[8:11], v[52:55]\n\tds_read_b128 v[8:11], v28 offset:1792\n\t" \
  "s_waitcnt lgkmcnt(5)\n\tv_mfma_f32_16x16x32_f16 v[64:67], v[136:139], v[12:15], v[64:67]\n\tv_mfma_f32_16x16x32_f16 v[52:55], v[104:107], v[12:15], v[52:55]\n\t" \
  "ds_read_b128 v[12:15], v28 offset:1856\n\ts_waitcnt lgkmcnt(5)\n\tv_mfma_f32_16x16x32_f16 v[36:39], v[16:19], v[68:71], 0\n\t" \
  "v_cvt_pk_bf16_f32 v60, v60, v61\n\tv_cvt_pk_bf16_f32 v61, v62, v63\n\tv_mfma_f32_16x16x32_f16 v[40:43], v[16:19], v[72:75], 0\n\t" \
  "ds_write_b64 v29, v[60:61] offset:1024\n\tds_write_b32 v25, v25 offset:128\n\tds_read_b128 v[16:19], v28 offset:1920\n\t" \
  "s_waitcnt lgkmcnt(7)\n\tv_mfma_f32_16x16x32_f16 v[64:67], v[140:143], v[20:23], v[64:67]\n\tds_read_b32 v34, v30\n\t" \
  "v_cvt_pk_f16_f32 v36, v36, v37\n\tv_cvt_pk_f16_f32 v37, v38, v39\n\tv_mfma_f32_16x16x32_f16 v[52:55], v[108:111], v[20:23], v[52:55]\n\t" \
  "ds_read_b128 v[20:23], v28 offset:1984\n\tv_cvt_pk_f16_f32 v38, v40, v41\n\tv_cvt_pk_f16_f32 v39, v42, v43\n\t" \
  "s_waitcnt lgkmcnt(8)\n\tv_mfma_f32_16x16x32_f16 v[64:67], v[144:147], v[0:3], v[64:67]\n\tv_mfma_f32_16x16x32_f16 v[52:55], v[112:115], v[0:3], v[52:55]\n\t" \
  "s_waitcnt lgkmcnt(1)\n\tv_cmp_gt_u32 vcc, 3, v34\n\ts_cbranch_vccz 2f\n\t" \
  "s_mov_b32 s6, 0x10000\n\t1:\n\ts_sleep 1\n\t" \
  "ds_read_b32 v34, v30\n\ts_sub_u32 s6, s6, 1\n\ts_waitcnt lgkmcnt(0)\n\t" \
  "v_cmp_gt_u32 vcc, 3, v34\n\ts_cbranch_scc1 2f\n\ts_cbranch_vccnz 1b\n\t" \
  "2:\n\tds_read_b128 v[0:3], v28 offset:2048\n\tv_mfma_f32_16x16x32_f16 v[64:67], v[36:39], v[76:79], v[64:67]\n\t" \
  "v_mfma_f32_16x16x32_f16 v[52:55], v[36:39], v[80:83], v[52:55]\n\tv_mfma_f32_16x16x32_f16 v[56:59], v[36:39], v[84:87], 0\n\tv_mfma_f32_16x16x32_f16 v[44:47], v[4:7], v[68:71], 0\n\t" \
  "v_mfma_f32_16x16x32_f16 v[40:43], v[4:7], v[72:75], 0\n\tds_read_b128 v[4:7], v28 offset:2112\n\tv_mfma_f32_16x16x32_f16 v[64:67], v[148:151], v[8:11], v[64:67]\n\t" \
  "v_mfma_f32_16x16x32_f16 v[52:55], v[116:119], v[8:11], v[52:55]\n\tds_read_b128 v[8:11], v28 offset:1520\n\tv_mfma_f32_16x16x32_f16 v[64:67], v[152:155], v[12:15], v[64:67]\n\t" \
  "v_mfma_f32_16x16x32_f16 v[52:55], v[120:123], v[12:15], v[52:55]\n\tds_read_b128 v[12:15], v28 offset:2176\n\tv_cvt_pk_f16_f32 v44, v44, v45\n\t" \
  "v_cvt_pk_f16_f32 v45, v46, v47\n\tv_cvt_pk_f16_f32 v46, v40, v41\n\tv_cvt_pk_f16_f32 v47, v42, v43\n\t" \
  "s_nop 1\n\tv_mfma_f32_16x16x32_f16 v[64:67], v[44:47], v[88:91], v[64:67]\n\tv_mfma_f32_16x16x32_f16 v[52:55], v[44:47], v[92:95], v[52:55]\n\t" \
  "v_mfma_f32_16x16x32_f16 v[56:59], v[44:47], v[96:99], v[56:59]\n\tv_mfma_f32_16x16x32_f16 v[64:67], v[156:159], v[16:19], v[64:67]\n\tv_mfma_f32_16x16x32_f16 v[52:55], v[124:127], v[16:19], v[52:55]\n\t" \
  "ds_read_b128 v[16:19], v28 offset:2240\n\ts_waitcnt lgkmcnt(5)\n\tv_mfma_f32_16x16x32_f16 v[64:67], v[160:163], v[20:23], v[64:67]\n\t" \
  "v_mfma_f32_16x16x32_f16 v[52:55], v[128:131], v[20:23], v[52:55]\n\tds_read_b128 v[20:23], v28 offset:1504\n\ts_waitcnt lgkmcnt(5)\n\t" \
  "v_mfma_f32_16x16x32_f16 v[64:67], v[164:167], v[0:3], v[64:67]\n\tv_mfma_f32_16x16x32_f16 v[52:55], v[132:135], v[0:3], v[52:55]\n\tv_mfma_f32_16x16x32_f16 v[56:59], v[100:103], v[0:3], v[56:59]\n\t" \
  "ds_read_b128 v[0:3], v28 offset:2304\n\ts_waitcnt lgkmcnt(5)\n\tv_mfma_f32_16x16x32_f16 v[52:55], v[136:139], v[4:7], v[52:55]\n\t" \
  "v_mfma_f32_16x16x32_f16 v[56:59], v[104:107], v[4:7], v[56:59]\n\tds_read_b128 v[4:7], v28 offset:2368\n\ts_waitcnt lgkmcnt(5)\n\t" \
  "v_mfma_f32_16x16x32_f16 v[36:39], v[8:11], v[68:71], 0\n\tv_cvt_pk_bf16_f32 v64, v64, v65\n\tv_cvt_pk_bf16_f32 v65, v66, v67\n\t" \
  TS(66, 67) "s_cmp_lt_u32 %[w], 4\n\ts_cbranch_scc1 1f\n\t" \
  "s_setprio 1\n\t1:\n\tv_mfma_f32_16x16x32_f16 v[40:43], v[8:11], v[72:75], 0\n\t" \
  "ds_write_b64 v29, v[64:65] offset:1536\n\tds_write_b32 v25, v25 offset:192\n\tds_read_b128 v[8:11], v28 offset:480\n\t" \
  "s_waitcnt lgkmcnt(7)\n\tv_mfma_f32_16x16x32_f16 v[52:55], v[140:143], v[12:15], v[52:55]\n\tv_cvt_pk_f16_f32 v36, v36, v37\n\t" \
  "v_cvt_pk_f16_f32 v37, v38, v39\n\tv_mfma_f32_16x16x32_f16 v[56:59], v[108:111], v[12:15], v[56:59]\n\tds_read_b128 v[12:15], v32 offset:3072\n\t" \
  "v_cvt_pk_f16_f32 v38, v40, v41\n\tv_cvt_pk_f16_f32 v39, v42, v43\n\ts_waitcnt lgkmcnt(7)\n\t" \
  "v_mfma_f32_16x16x32_f16 v[52:55], v[144:147], v[16:19], v[52:55]\n\tv_mfma_f32_16x16x32_f16 v[56:59], v[112:115], v[16:19], v[56:59]\n\tds_read_b128 v[16:19], v28 offset:496\n\t" \
  "v_mfma_f32_16x16x32_f16 v[52:55], v[36:39], v[76:79], v[52:55]\n\tv_mfma_f32_16x16x32_f16 v[56:59], v[36:39], v[80:83], v[56:59]\n\tv_mfma_f32_16x16x32_f16 v[60:63], v[36:39], v[84:87], 0\n\t" \
  "s_waitcnt lgkmcnt(7)\n\tv_mfma_f32_16x16x32_f16 v[44:47], v[20:23], v[68:71], 0\n\tv_mfma_f32_16x16x32_f16 v[40:43], v[20:23], v[72:75], 0\n\t" \
  "ds_read_b128 v[20:23], v32 offset:2048\n\ts_waitcnt lgkmcnt(7)\n\tv_mfma_f32_16x16x32_f16 v[52:55], v[148:151], v[0:3], v[52:55]\n\t" \
  "v_mfma_f32_16x16x32_f16 v[56:59], v[116:119], v[0:3], v[56:59]\n\tds_read_b128 v[0:3], v28 offset:2432\n\ts_waitcnt lgkmcnt(7)\n\t" \
  "v_mfma_f32_16x16x32_f16 v[52:55], v[152:155], v[4:7], v[52:55]\n\tv_cvt_pk_f16_f32 v44, v44, v45\n\tv_cvt_pk_f16_f32 v45, v46, v47\n\t" \
  "v_cvt_pk_f16_f32 v46, v40, v41\n\tv_cvt_pk_f16_f32 v47, v42, v43\n\tv_mfma_f32_16x16x32_f16 v[56:59], v[120:123], v[4:7], v[56:59]\n\t" \
  "ds_read_b128 v[4:7], v28 offset:2496\n\tv_mfma_f32_16x16x32_f16 v[52:55], v[44:47], v[88:91], v[52:55]\n\tv_mfma_f32_16x16x32_f16 v[56:59], v[44:47], v[92:95], v[56:59]\n\t" \
  "v_mfma_f32_16x16x32_f16 v[60:63], v[44:47], v[96:99], v[60:63]\n\ts_waitcnt lgkmcnt(4)\n\tv_mfma_f32_16x16x32_f16 v[40:43], v[8:11], v[12:15], 0\n\t" \
  "ds_read_b64 v[8:9], v31 offset:544\n\tds_read_b64 v[10:11], v31 offset:712\n\tds_read_b64 v[12:13], v31 offset:512\n\t" \
  "ds_read_b64 v[14:15], v31 offset:680\n\ts_waitcnt lgkmcnt(7)\n\tv_mfma_f32_16x16x32_f16 v[52:55], v[48:51], v[16:19], v[52:55]\n\t" \
  "s_waitcnt lgkmcnt(6)\n\tv_mfma_f32_16x16x32_f16 v[36:39], v[16:19], v[20:23], 0\n\tds_read_b64 v[16:17], v31 offset:480\n\t" \
  "ds_read_b64 v[18:19], v31 offset:648\n\tds_read_b64 v[20:21], v31 offset:448\n\tds_read_b64 v[22:23], v31 offset:616\n\t" \
  "s_waitcnt lgkmcnt(9)\n\tv_mfma_f32_16x16x32_f16 v[52:55], v[156:159], v[0:3], v[52:55]\n\tv_mfma_f32_16x16x32_f16 v[56:59], v[124:127], v[0:3], v[56:59]\n\t" \
  "ds_read_b128 v[0:3], v28 offset:2560\n\tv_cvt_pk_f16_f32 v36, v36, v37\n\tv_cvt_pk_f16_f32 v37, v38, v39\n\t" \
  "s_waitcnt lgkmcnt(9)\n\tv_mfma_f32_16x16x32_f16 v[52:55], v[160:163], v[4:7], v[52:55]\n\tv_cvt_pk_f16_f32 v38, v40, v41\n\t" \
  "v_cvt_pk_f16_f32 v39, v42, v43\n\tv_mfma_f32_16x16x32_f16 v[56:59], v[128:131], v[4:7], v[56:59]\n\tds_read_b128 v[4:7], v28 offset:2624\n\t" \
  "s_waitcnt lgkmcnt(8)\n\tv_mfma_f32_16x16x32_f16 v[52:55], v[36:39], v[8:11], v[52:55]\n\tds_read_b128 v[8:11], v28 offset:2032\n\t" \
  "s_waitcnt lgkmcnt(7)\n\tv_mfma_f32_16x16x32_f16 v[56:59], v[36:39], v[12:15], v[56:59]\n\tds_read_b128 v[12:15], v28 offset:2688\n\t" \
  "s_waitcnt lgkmcnt(6)\n\tv_mfma_f32_16x16x32_f16 v[60:63], v[36:39], v[16:19], v[60:63]\n\tds_read_b128 v[16:19], v28 offset:2752\n\t" \
  "s_waitcnt lgkmcnt(5)\n\tv_mfma_f32_16x16x32_f16 v[64:67], v[36:39], v[20:23], 0\n\tds_read_b128 v[20:23], v28 offset:2016\n\t" \
  "s_waitcnt lgkmcnt(5)\n\tv_mfma_f32_16x16x32_f16 v[52:55], v[164:167], v[0:3], v[52:55]\n\tv_mfma_f32_16x16x32_f16 v[56:59], v[132:135], v[0:3], v[56:59]\n\t" \
  "v_mfma_f32_16x16x32_f16 v[60:63], v[100:103], v[0:3], v[60:63]\n\tds_read_b128 v[0:3], v28 offset:2816\n\ts_waitcnt lgkmcnt(5)\n\t" \
  "v_mfma_f32_16x16x32_f16 v[56:59], v[136:139], v[4:7], v[56:59]\n\tv_mfma_f32_16x16x32_f16 v[60:63], v[104:107], v[4:7], v[60:63]\n\tds_read_b128 v[4:7], v28 offset:2880\n\t" \
  "s_waitcnt lgkmcnt(5)\n\tv_mfma_f32_16x16x32_f16 v[44:47], v[8:11], v[68:71], 0\n\tv_cvt_pk_bf16_f32 v52, v52, v53\n\t" \
  "v_cvt_pk_bf16_f32 v53, v54, v55\n\ts_cmp_lt_u32 %[w], 4\n\ts_cbranch_scc0 1f\n\t" \
  "s_setprio 0\n\t1:\n\tv_mfma_f32_16x16x32_f16 v[40:43], v[8:11], v[72:75], 0\n\t" \
  "ds_write_b64 v29, v[52:53] offset:2048\n\tds_write_b32 v25, v25 offset:256\n\tds_read_b128 v[8:11], v28 offset:992\n\t" \
  "s_waitcnt lgkmcnt(7)\n\tv_mfma_f32_16x16x32_f16 v[56:59], v[140:143], v[12:15], v[56:59]\n\tv_cvt_pk_f16_f32 v44, v44, v45\n\t" \
  "v_cvt_pk_f16_f32 v45, v46, v47\n\tv_mfma_f32_16x16x32_f16 v[60:63], v[108:111], v[12:15], v[60:63]\n\tds_read_b128 v[12:15], v32 offset:3072\n\t" \
  "v_cvt_pk_f16_f32 v46, v40, v41\n\tv_cvt_pk_f16_f32 v47, v42, v43\n\ts_waitcnt lgkmcnt(7)\n\t" \
  "v_mfma_f32_16x16x32_f16 v[56:59], v[144:147], v[16:19], v[56:59]\n\tv_mfma_f32_16x16x32_f16 v[60:63], v[112:115], v[16:19], v[60:63]\n\tds_read_b128 v[16:19], v28 offset:1008\n\t" \
  "v_mfma_f32_16x16x32_f16 v[56:59], v[44:47], v[76:79], v[56:59]\n\tv_mfma_f32_16x16x32_f16 v[60:63], v[44:47], v[80:83], v[60:63]\n\tv_mfma_f32_16x16x32_f16 v[64:67], v[44:47], v[84:87], v[64:67]\n\t" \
  "s_waitcnt lgkmcnt(7)\n\tv_mfma_f32_16x16x32_f16 v[36:39], v[20:23], v[68:71], 0\n\tv_mfma_f32_16x16x32_f16 v[40:43], v[20:23], v[72:75], 0\n\t" \
  "ds_read_b128 v[20:23], v32 offset:2048\n\ts_waitcnt lgkmcnt(7)\n\tv_mfma_f32_16x16x32_f16 v[56:59], v[148:151], v[0:3], v[56:59]\n\t" \
  "v_mfma_f32_16x16x32_f16 v[60:63], v[116:119], v[0:3], v[60:63]\n\tds_read_b128 v[0:3], v28 offset:2944\n\ts_waitcnt lgkmcnt(7)\n\t" \
  "v_mfma_f32_16x16x32_f16 v[56:59], v[152:155], v[4:7], v[56:59]\n\tv_cvt_pk_f16_f32 v36, v36, v37\n\tv_cvt_pk_f16_f32 v37, v38, v39\n\t" \
  "v_cvt_pk_f16_f32 v38, v40, v41\n\tv_cvt_pk_f16_f32 v39, v42, v43\n\tv_mfma_f32_16x16x32_f16 v[60:63], v[120:123], v[4:7], v[60:63]\n\t" \
  "ds_read_b128 v[4:7], v28 offset:3008\n\tv_mfma_f32_16x16x32_f16 v[56:59], v[36:39], v[88:91], v[56:59]\n\tv_mfma_f32_16x16x32_f16 v[60:63], v[36:39], v[92:95], v[60:63]\n\t" \
  "v_mfma_f32_16x16x32_f16 v[64:67], v[36:39], v[96:99], v[64:67]\n\tds_read_b32 v34, v30\n\ts_waitcnt lgkmcnt(5)\n\t" \
  "v_mfma_f32_16x16x32_f16 v[40:43], v[8:11], v[12:15], 0\n\tds_read_b64 v[8:9], v31 offset:544\n\tds_read_b64 v[10:11], v31 offset:712\n\t" \
  "ds_read_b64 v[12:13], v31 offset:512\n\tds_read_b64 v[14:15], v31 offset:680\n\ts_waitcnt lgkmcnt(8)\n\t" \
  "v_mfma_f32_16x16x32_f16 v[56:59], v[48:51], v[16:19], v[56:59]\n\ts_waitcnt lgkmcnt(7)\n\tv_mfma_f32_16x16x32_f16 v[44:47], v[16:19], v[20:23], 0\n\t" \
  "ds_read_b64 v[16:17], v31 offset:480\n\tds_read_b64 v[18:19], v31 offset:648\n\ts_waitcnt lgkmcnt(6)\n\t" \
  "v_cmp_gt_u32 vcc, 4, v34\n\ts_cbranch_vccz 2f\n\ts_mov_b32 s6, 0x10000\n\t" \
  "1:\n\ts_sleep 1\n\tds_read_b32 v34, v30\n\t" \
  "s_sub_u32 s6, s6, 1\n\ts_waitcnt lgkmcnt(0)\n\tv_cmp_gt_u32 vcc, 4, v34\n\t" \
  "s_cbranch_scc1 2f\n\ts_cbranch_vccnz 1b\n\t2:\n\t" \
  "ds_read_b128 v[20:23], v28 offset:3072\n\tv_mfma_f32_16x16x32_f16 v[56:59], v[156:159], v[0:3], v[56:59]\n\tv_mfma_f32_16x16x32_f16 v[60:63], v[124:127], v[0:3], v[60:63]\n\t" \
  "ds_read_b128 v[0:3], v28 offset:3136\n\tv_cvt_pk_f16_f32 v44, v44, v45\n\tv_cvt_pk_f16_f32 v45, v46, v47\n\t" \
  "v_mfma_f32_16x16x32_f16 v[56:59], v[160:163], v[4:7], v[56:59]\n\tv_cvt_pk_f16_f32 v46, v40, v41\n\tv_cvt_pk_f16_f32 v47, v42, v43\n\t" \
  "v_mfma_f32_16x16x32_f16 v[60:63], v[128:131], v[4:7], v[60:63]\n\tds_read_b128 v[4:7], v28 offset:2544\n\ts_waitcnt lgkmcnt(7)\n\t" \
  "v_mfma_f32_16x16x32_f16 v[56:59], v[44:47], v[8:11], v[56:59]\n\tds_read_b128 v[8:11], v28 offset:3200\n\ts_waitcnt lgkmcnt(6)\n\t" \
  "v_mfma_f32_16x16x32_f16 v[60:63], v[44:47], v[12:15], v[60:63]\n\tds_read_b128 v[12:15], v28 offset:3264\n\ts_waitcnt lgkmcnt(5)\n\t" \
  "v_mfma_f32_16x16x32_f16 v[64:67], v[44:47], v[16:19], v[64:67]\n\tds_read_b128 v[16:19], v28 offset:2528\n\ts_waitcnt lgkmcnt(5)\n\t" \
  "v_mfma_f32_16x16x32_f16 v[56:59], v[164:167], v[20:23], v[56:59]\n\tv_mfma_f32_16x16x32_f16 v[60:63], v[132:135], v[20:23], v[60:63]\n\tv_mfma_f32_16x16x32_f16 v[64:67], v[100:103], v[20:23], v[64:67]\n\t" \
  "ds_read_b128 v[20:23], v28 offset:3328\n\ts_waitcnt lgkmcnt(5)\n\tv_mfma_f32_16x16x32_f16 v[60:63], v[136:139], v[0:3], v[60:63]\n\t" \
  "v_mfma_f32_16x16x32_f16 v[64:67], v[104:107], v[0:3], v[64:67]\n\tds_read_b128 v[0:3], v28 offset:3392\n\ts_waitcnt lgkmcnt(5)\n\t" \
  "v_mfma_f32_16x16x32_f16 v[36:39], v[4:7], v[68:71], 0\n\tv_cvt_pk_bf16_f32 v56, v56, v57\n\tv_cvt_pk_bf16_f32 v57, v58, v59\n\t" \
  "s_cmp_lt_u32 %[w], 4\n\ts_cbranch_scc1 1f\n\ts_setprio 0\n\t" \
  "1:\n\tv_mfma_f32_16x16x32_f16 v[40:43], v[4:7], v[72:75], 0\n\tds_write_b64 v29, v[56:57] offset:2560\n\t" \
  "ds_write_b32 v25, v25 offset:320\n\tds_read_b128 v[4:7], v28 offset:1504\n\ts_waitcnt lgkmcnt(7)\n\t" \
  "v_mfma_f32_16x16x32_f16 v[60:63], v[140:143], v[8:11], v[60:63]\n\tv_cvt_pk_f16_f32 v36, v36, v37\n\tv_cvt_pk_f16_f32 v37, v38, v39\n\t" \
  "v_mfma_f32_16x16x32_f16 v[64:67], v[108:111], v[8:11], v[64:67]\n\tds_read_b128 v[8:11], v32 offset:3072\n\tv_cvt_pk_f16_f32 v38, v40, v41\n\t" \
  "v_cvt_pk_f16_f32 v39, v42, v43\n\ts_waitcnt lgkmcnt(7)\n\tv_mfma_f32_16x16x32_f16 v[60:63], v[144:147], v[12:15], v[60:63]\n\t" \
  "v_mfma_f32_16x16x32_f16 v[64:67], v[112:115], v[12:15], v[64:67]\n\tds_read_b128 v[12:15], v28 offset:1520\n\tv_mfma_f32_16x16x32_f16 v[60:63], v[36:39], v[76:79], v[60:63]\n\t" \
  "v_mfma_f32_16x16x32_f16 v[64:67], v[36:39], v[80:83], v[64:67]\n\ts_waitcnt lgkmcnt(7)\n\tv_mfma_f32_16x16x32_f16 v[44:47], v[16:19], v[68:71], 0\n\t" \
  "v_mfma_f32_16x16x32_f16 v[40:43], v[16:19], v[72:75], 0\n\tds_read_b128 v[16:19], v32 offset:2048\n\ts_waitcnt lgkmcnt(7)\n\t" \
  "v_mfma_f32_16x16x32_f16 v[60:63], v[148:151], v[20:23], v[60:63]\n\tv_mfma_f32_16x16x32_f16 v[64:67], v[116:119], v[20:23], v[64:67]\n\tds_read_b128 v[20:23], v28 offset:3456\n\t" \
  "s_waitcnt lgkmcnt(7)\n\tv_mfma_f32_16x16x32_f16 v[60:63], v[152:155], v[0:3], v[60:63]\n\tv_cvt_pk_f16_f32 v44, v44, v45\n\t" \
  "v_cvt_pk_f16_f32 v45, v46, v47\n\tv_cvt_pk_f16_f32 v46, v40, v41\n\tv_cvt_pk_f16_f32 v47, v42, v43\n\t" \
  "v_mfma_f32_16x16x32_f16 v[64:67], v[120:123], v[0:3], v[64:67]\n\tds_read_b128 v[0:3], v28 offset:3520\n\tv_mfma_f32_16x16x32_f16 v[60:63], v[44:47], v[88:91], v[60:63]\n\t" \
  "v_mfma_f32_16x16x32_f16 v[64:67], v[44:47], v[92:95], v[64:67]\n\ts_waitcnt lgkmcnt(4)\n\tv_mfma_f32_16x16x32_f16 v[40:43], v[4:7], v[8:11], 0\n\t" \
  "ds_read_b64 v[4:5], v31 offset:544\n\tds_read_b64 v[6:7], v31 offset:712\n\tds_read_b64 v[8:9], v31 offset:512\n\t" \
  "ds_read_b64 v[10:11], v31 offset:680\n\ts_waitcnt lgkmcnt(7)\n\tv_mfma_f32_16x16x32_f16 v[60:63], v[48:51], v[12:15], v[60:63]\n\t" \
  "s_waitcnt lgkmcnt(6)\n\tv_mfma_f32_16x16x32_f16 v[36:39], v[12:15], v[16:19], 0\n\tds_read_b128 v[12:15], v28 offset:3584\n\t" \
  "ds_read_b128 v[16:19], v28 offset:3648\n\ts_waitcnt lgkmcnt(7)\n\tv_mfma_f32_16x16x32_f16 v[60:63], v[156:159], v[20:23], v[60:63]\n\t" \
  "v_mfma_f32_16x16x32_f16 v[64:67], v[124:127], v[20:23], v[64:67]\n\tds_read_b128 v[20:23], v28 offset:3056\n\ts_waitcnt lgkmcnt(7)\n\t" \
  "v_mfma_f32_16x16x32_f16 v[60:63], v[160:163], v[0:3], v[60:63]\n\tv_cvt_pk_f16_f32 v36, v36, v37\n\tv_cvt_pk_f16_f32 v37, v38, v39\n\t" \
  "v_cvt_pk_f16_f32 v38, v40, v41\n\tv_cvt_pk_f16_f32 v39, v42, v43\n\tv_mfma_f32_16x16x32_f16 v[64:67], v[128:131], v[0:3], v[64:67]\n\t" \
  "ds_read_b128 v[0:3], v28 offset:3712\n\ts_waitcnt lgkmcnt(6)\n\tv_mfma_f32_16x16x32_f16 v[60:63], v[36:39], v[4:7], v[60:63]\n\t" \
  "ds_read_b128 v[4:7], v28 offset:3776\n\ts_waitcnt lgkmcnt(5)\n\tv_mfma_f32_16x16x32_f16 v[64:67], v[36:39], v[8:11], v[64:67]\n\t" \
  "ds_read_b128 v[8:11], v28 offset:3040\n\ts_waitcnt lgkmcnt(5)\n\tv_mfma_f32_16x16x32_f16 v[60:63], v[164:167], v[12:15], v[60:63]\n\t" \
  "v_mfma_f32_16x16x32_f16 v[64:67], v[132:135], v[12:15], v[64:67]\n\tds_read_b128 v[12:15], v28 offset:3840\n\ts_waitcnt lgkmcnt(5)\n\t" \
  "v_mfma_f32_16x16x32_f16 v[64:67], v[136:139], v[16:19], v[64:67]\n\tds_read_b128 v[16:19], v28 offset:3904\n\ts_waitcnt lgkmcnt(5)\n\t" \
  "v_mfma_f32_16x16x32_f16 v[44:47], v[20:23], v[68:71], 0\n\tv_mfma_f32_16x16x32_f16 v[40:43], v[20:23], v[72:75], 0\n\tds_read_b128 v[20:23], v28 offset:2016\n\t" \
  "v_cvt_pk_bf16_f32 v60, v60, v61\n\tv_cvt_pk_bf16_f32 v61, v62, v63\n\ts_waitcnt lgkmcnt(5)\n\t" \
  "v_mfma_f32_16x16x32_f16 v[64:67], v[140:143], v[0:3], v[64:67]\n\tds_write_b64 v29, v[60:61] offset:3072\n\tds_write_b32 v25, v25 offset:384\n\t" \
  "ds_read_b128 v[0:3], v32 offset:3072\n\tv_cvt_pk_f16_f32 v44, v44, v45\n\tv_cvt_pk_f16_f32 v45, v46, v47\n\t" \
  "v_cvt_pk_f16_f32 v46, v40, v41\n\tv_cvt_pk_f16_f32 v47, v42, v43\n\ts_waitcnt lgkmcnt(7)\n\t" \
  "v_mfma_f32_16x16x32_f16 v[64:67], v[144:147], v[4:7], v[64:67]\n\tds_read_b128 v[4:7], v28 offset:2032\n\tv_mfma_f32_16x16x32_f16 v[64:67], v[44:47], v[76:79], v[64:67]\n\t" \
  "s_waitcnt lgkmcnt(7)\n\tv_mfma_f32_16x16x32_f16 v[36:39], v[8:11], v[68:71], 0\n\tv_mfma_f32_16x16x32_f16 v[40:43], v[8:11], v[72:75], 0\n\t" \
  "ds_read_b128 v[8:11], v32 offset:2048\n\ts_waitcnt lgkmcnt(7)\n\tv_mfma_f32_16x16x32_f16 v[64:67], v[148:151], v[12:15], v[64:67]\n\t" \
  "ds_read_b128 v[12:15], v28 offset:3968\n\ts_waitcnt lgkmcnt(7)\n\tv_mfma_f32_16x16x32_f16 v[64:67], v[152:155], v[16:19], v[64:67]\n\t" \
  "ds_read_b128 v[16:19], v28 offset:4032\n\tv_cvt_pk_f16_f32 v36, v36, v37\n\tv_cvt_pk_f16_f32 v37, v38, v39\n\t" \
  "v_cvt_pk_f16_f32 v38, v40, v41\n\tv_cvt_pk_f16_f32 v39, v42, v43\n\ts_nop 1\n\t" \
  "v_mfma_f32_16x16x32_f16 v[64:67], v[36:39], v[88:91], v[64:67]\n\ts_waitcnt lgkmcnt(4)\n\tv_mfma_f32_16x16x32_f16 v[40:43], v[20:23], v[0:3], 0\n\t" \
  "ds_read_b64 v[0:1], v31 offset:544\n\tds_read_b64 v[2:3], v31 offset:712\n\tds_read_b128 v[20:23], v28 offset:4096\n\t" \
  "s_waitcnt lgkmcnt(6)\n\tv_mfma_f32_16x16x32_f16 v[64:67], v[48:51], v[4:7], v[64:67]\n\ts_waitcnt lgkmcnt(5)\n\t" \
  "v_mfma_f32_16x16x32_f16 v[44:47], v[4:7], v[8:11], 0\n\ts_waitcnt lgkmcnt(4)\n\tv_mfma_f32_16x16x32_f16 v[64:67], v[156:159], v[12:15], v[64:67]\n\t" \
  "s_waitcnt lgkmcnt(3)\n\tv_mfma_f32_16x16x32_f16 v[64:67], v[160:163], v[16:19], v[64:67]\n\ts_nop 3\n\t" \
  "v_cvt_pk_f16_f32 v44, v44, v45\n\tv_cvt_pk_f16_f32 v45, v46, v47\n\tv_cvt_pk_f16_f32 v46, v40, v41\n\t" \
  "v_cvt_pk_f16_f32 v47, v42, v43\n\ts_waitcnt lgkmcnt(1)\n\ts_nop 0\n\t" \
  "v_mfma_f32_16x16x32_f16 v[64:67], v[44:47], v[0:3], v[64:67]\n\ts_waitcnt lgkmcnt(0)\n\tv_mfma_f32_16x16x32_f16 v[64:67], v[164:167], v[20:23], v[64:67]\n\t" \
  "s_nop 7\n\tv_cvt_pk_bf16_f32 v64, v64, v65\n\tv_cvt_pk_bf16_f32 v65, v66, v67\n\t" \
  "ds_write_b64 v29, v[64:65] offset:3584\n\tds_write_b32 v25, v25 offset:448\n\t"
#define CONV_CLOBBER "v0", "v1", "v2", "v3", "v4", "v5", "v6", "v7", "v8", "v9", "v10", "v11", "v12", "v13", "v14", "v15", "v16", "v17", "v18", "v19", "v20", "v21", "v22", "v23", "v36", "v37", "v38", "v39", "v40", "v41", "v42", "v43", "v44", "v45", "v46", "v47", "v52", "v53", "v54", "v55", "v56", "v57", "v58", "v59", "v60", "v61", "v62", "v63", "v64", "v65", "v66", "v67", "v68", "v69", "v70", "v71", "v72", "v73", "v74", "v75", "v76", "v77", "v78", "v79", "v80", "v81", "v82", "v83", "v84", "v85", "v86", "v87", "v88", "v89", "v90", "v91", "v92", "v93", "v94", "v95", "v96", "v97", "v98", "v99", "v100", "v101", "v102", "v103", "v104", "v105", "v106", "v107", "v108", "v109", "v110", "v111", "v112", "v113", "v114", "v115", "v116", "v117", "v118", "v119", "v120", "v121", "v122", "v123", "v124", "v125", "v126", "v127", "v128", "v129", "v130", "v131", "v132", "v133", "v134", "v135", "v136", "v137", "v138", "v139", "v140", "v141", "v142", "v143", "v144", "v145", "v146", "v147", "v148", "v149", "v150", "v151", "v152", "v153", "v154", "v155", "v156", "v157", "v158", "v159", "v160", "v161", "v162", "v163", "v164", "v165", "v166", "v167"
#define G_CLOBBER "v8", "v9", "v10", "v11", "v12", "v13", "v14", "v15", "v16", "v17", "v18", "v19", "v20", "v21", "v22", "v23", "v24", "v25", "v26", "v27", "v28", "v29", "v30", "v32", "v33", "v34", "v35", "v36", "v37", "v38", "v39", "v40", "v41", "v42", "v43", "v44", "v45", "v46", "v47", "v48", "v49", "v50", "v51", "v52", "v53", "v54", "v55", "v56", "v57", "v58", "v59", "v60", "v61", "v62", "v63", "v64", "v65", "v66", "v67", "v68", "v69", "v70", "v71", "v72", "v73", "v74", "v75", "v76", "v77", "v78", "v79", "v80", "v81", "v82", "v83", "v84", "v85", "v86", "v87", "v88", "v89", "v90", "v91", "v92", "v93", "v94", "v95", "v96", "v97", "v98", "v99", "v100", "v101", "v102", "v103", "v104", "v105", "v106", "v107", "v108", "v109", "v110", "v111", "v112", "v113", "v114", "v115", "v116", "v117", "v118", "v119", "v120", "v121", "v122", "v123", "v124", "v125", "v126", "v127"
#define D_CLOBBER "v8", "v9", "v10", "v11", "v12", "v13", "v14", "v15", "v16", "v17", "v18", "v19", "v20", "v21", "v22", "v23", "v24", "v25", "v26", "v27", "v28", "v29", "v30", "v31"
__device__ __forceinline__ f32x2 pkfma(f32x2 a, f32x2 b, f32x2 c) {
  f32x2 r;
  asm("v_pk_fma_f32 %0, %1, %2, %3" : "=v"(r) : "v"(a), "v"(b), "v"(c));
  return r;
}
__device__ __forceinline__ float fmav(float a, float b, float c) {
  float r;
  asm("v_fma_f32 %0, %1, %2, %3" : "=v"(r) : "v"(a), "v"(b), "v"(c));
  return r;
}
__device__ __forceinline__ float fmac(float a, float b, float c) {
  asm("v_fmac_f32 %0, %1, %2" : "+v"(c) : "v"(a), "v"(b));
  return c;
}
__device__ __forceinline__ float fmas(float a, u32 s, float c) {
  asm("v_fmac_f32 %0, %2, %1" : "+v"(c) : "v"(a), "s"(s));
  return c;
}
template <int HI>
__device__ __forceinline__ f32x2 pkfmas(f32x2 a, f32x2 s, f32x2 c) {
  f32x2 r;
  if (HI) asm("v_pk_fma_f32 %0, %2, %1, %3 op_sel:[1,0,0] op_sel_hi:[1,1,1]" : "=v"(r) : "v"(a), "v"(s), "v"(c));
  else asm("v_pk_fma_f32 %0, %2, %1, %3 op_sel:[0,0,0] op_sel_hi:[0,1,1]" : "=v"(r) : "v"(a), "v"(s), "v"(c));
  return r;
}
#define RANK8(d0, d1, d2, d3, c0, c1, i0, f0, f1) "v_perm_b32 " d0 ", " d0 ", " d1 ", %[sg]\n\tv_perm_b32 " d2 ", " d2 ", " d3 ", %[sg]\n\t" \
  "v_bcnt_u32_b32 " d0 ", " d0 ", " i0 "\n\tv_bcnt_u32_b32 " c0 ", " d2 ", " d0 "\n\tv_bcnt_u32_b32 " c1 ", " d2 ", " d0 "\n\t" f0 "\n\t" \
  "v_permlane16_swap_b32 " c0 ", " c1 "\n\tv_add_u32 " d0 ", " c0 ", " c1 "\n\tv_add_u32 " d1 ", " c0 ", " c1 "\n\t" f1 "\n\t" \
  "v_permlane32_swap_b32 " d0 ", " d1 "\n\tv_add_u32 " c0 ", " d0 ", " d1 "\n\t"
__device__ __forceinline__ void ln_rows(u32x4 (&xv)[4], u32x8 wv, rsrc_t rs, u32& tag, int b, int w, int l, u32 st, u32 sg TLARG) {
  float s[2][3] = {}, c[2], sw = 0.f;
  u32 os = l < 16 ? O_SG + 128 * (b - 128) + 8 * l : 0x80000000u;
  u32 ot = l < 16 ? O_TOP + 8 * (b & 15) + TL * ((b >> 4) & 7) : l < 32 ? O_RG - 128 + 128 * (b - 128) + 8 * l : 0x80000000u, om = l < 16 ? TOPS / 8 : 0u, fm = l < 16 ? 0xff0u : 0u;
  u32 aw = (l & 16) ? st + 8 * w + (l >> 5) * 4 : st + 136, ac = st + 128, ar = st + 4 * (l & 15), aq = st + (l & 48), one = 1u;
  u32 kl = (2047 - (16 * (b - 128) + 2 * w + (l >> 5))) << 1;
  asm volatile("" : "+v"(os), "+v"(aw), "+v"(ac), "+v"(ar), "+v"(aq), "+v"(one), "+v"(kl), "+v"(ot), "+v"(om), "+v"(fm));
  SB();
  asm volatile("s_waitcnt vmcnt(4) lgkmcnt(0)" : "+{s44}"(tag)::"memory");
  asm volatile("" : "+{v[116:123]}"(wv));
  tag += 1u;
  *(volatile LDS u32*)(size_t)(st - 104) = tag;
  STAMP(12);
#pragma unroll
  for (int i = 0; i < 8; i++) sw = dot2b(wv[i], 0x3f803f80u, sw);
  sw += dppf<0x121, 0xf>(sw); sw += dppf<0x122, 0xf>(sw); sw += dppf<0x124, 0xf>(sw); sw += dppf<0x128, 0xf>(sw);
  sw = (rdl(sw, 0) + rdl(sw, 16)) + (rdl(sw, 32) + rdl(sw, 48));
  asm volatile("" : "+v"(sw));
  SB();
  float cv = 0.f;
#pragma unroll
  for (int r = 0; r < 2; r++) {
#pragma unroll
    for (int i = 0; i < 8; i++) {
      if (r == 0 && i == 0) { asm volatile("s_waitcnt vmcnt(3)" : "+{v[100:103]}"(xv[0])); STAMP(13); }
      if (r == 0 && i == 4) asm volatile("s_waitcnt vmcnt(2)" : "+{v[104:107]}"(xv[1]));
      if (r == 1 && i == 0) asm volatile("s_waitcnt vmcnt(1)" : "+{v[108:111]}"(xv[2]));
      if (r == 1 && i == 4) asm volatile("s_waitcnt vmcnt(0)" : "+{v[112:115]}"(xv[3]));
      const u32 a = xv[2 * r + (i >> 2)][i & 3];
      s[r][0] = dot2b(a, 0x3f803f80u, s[r][0]); s[r][1] = dot2b(a, a, s[r][1]); s[r][2] = dot2b(a, wv[i], s[r][2]);
      if (i == 3) {
        c[r] = rdl(s[r][0] * 0.125f, 0);
        if (r) { cv = l < 32 ? c[0] : c[1]; asm volatile("" : "+v"(cv)); }
        SB();
      }
    }
    const float ml = s[r][0] * 0.0625f, dl = __builtin_fmaf(s[r][0], 0.25f, -4.f * c[r]);
    s[r][1] = __builtin_fmaf(dl, dl, __builtin_fmaf(-s[r][0], ml, s[r][1]));
    SB();
  }
#pragma unroll
  for (int k = 0; k < 3; k++) swap32(s[0][k ? 3 - k : 0], s[1][k ? 3 - k : 0]);
  float q[3];
#pragma unroll
  for (int k = 0; k < 3; k++) q[k] = s[0][k] + s[1][k];
#define RED3(m) "v_add_f32_dpp %0, %0, %0 " m " bank_mask:0xf\n\tv_add_f32_dpp %1, %1, %1 " m " bank_mask:0xf\n\tv_add_f32_dpp %2, %2, %2 " m " bank_mask:0xf\n\t"
  asm volatile("s_nop 1\n\t" RED3("row_ror:1 row_mask:0xf") RED3("row_ror:2 row_mask:0xf") RED3("row_ror:4 row_mask:0xf") RED3("row_ror:8 row_mask:0xf")
               RED3("row_bcast:15 row_mask:0xa") : "+v"(q[0]), "+v"(q[1]), "+v"(q[2]));
  const float mu = q[0] * (1.f / D), dv = mu - cv;
  const float var = fmaxf(__builtin_fmaf(-dv, dv, q[1] * (1.f / D)), 0.f);
  const float m = rb(mu);
  float sd = __builtin_amdgcn_sqrtf(rb(rb(var) + 1e-5f)), num = __builtin_fmaf(-m, sw, q[2]);
  asm volatile("s_nop 0\n\tv_cvt_pk_bf16_f32 %0, 0, %0" : "+v"(sd), "+v"(num));
  u32 rv = __float_as_uint(rb(num * __builtin_amdgcn_rcpf(sd)));
  rv ^= (u32)((int)rv >> 15) | 0x80000000u;
  rv = __builtin_amdgcn_perm(rv, kl, 0x07060100u) >> 1;
  asm volatile("ds_write2_b32 %[aw], %[sv], %[rv] offset1:16\n\ts_mov_b64 exec, 1\n\tds_add_rtn_u32 v104, %[ac], %[one]\n\ts_mov_b64 exec, -1\n\t"
               "ds_read_b32 v106, %[ar]\n\tds_read_b32 v105, %[ar] offset:64\n\tds_read_b128 v[100:103], %[aq] offset:64\n\tv_mov_b32 v107, %[t]\n\tv_mov_b32 v109, %[t]\n\t"
               "s_waitcnt lgkmcnt(3)\n\tv_cmp_eq_u32 vcc, 7, v104\n\ts_bitcmp1_b32 vcc_lo, 0\n\ts_cbranch_scc0 1f\n\t"
               "s_waitcnt lgkmcnt(2)\n\tbuffer_store_dwordx2 v[106:107], %[os], %[rs], 0 offen sc1\n\ts_waitcnt lgkmcnt(0)\n\t"
               "v_sub_u32 v100, v105, v100\n\tv_sub_u32 v101, v105, v101\n\tv_sub_u32 v102, v105, v102\n\tv_sub_u32 v103, v105, v103\n\t"
               RANK8("v100", "v101", "v102", "v103", "v104", "v103", "0", "s_nop 1", "s_nop 1") "v_cmp_eq_u32 vcc, 16, v104\n\t"
               "v_sub_u32 v108, 19, v104\n\ts_ff1_i32_b32 s6, vcc_lo\n\tv_readlane_b32 s6, v105, s6\n\t"
               "v_mad_u32_u24 v104, v104, %[om], %[ot]\n\ts_nop 0\n\tv_lshrrev_b32 v108, v108, s6\n\tv_bfi_b32 v108, %[fm], v108, v105\n\t"
               "buffer_store_dwordx2 v[108:109], v104, %[rs], 0 offen sc1\n1:\n\ts_waitcnt lgkmcnt(0)"
               :: [aw] "v"(aw), [sv] "v"(__builtin_amdgcn_perm(__float_as_uint(sd), __float_as_uint(m), 0x07060302u)), [rv] "v"(rv), [ac] "v"(ac), [one] "v"(one),
                  [ar] "v"(ar), [aq] "v"(aq), [os] "v"(os), [om] "v"(om), [ot] "v"(ot), [fm] "v"(fm), [sg] "s"(sg), [t] "s"(tag), [rs] "s"(rs)
               : "vcc", "scc", "s6", "v100", "v101", "v102", "v103", "v104", "v105", "v106", "v107", "v108", "v109", "memory");
  SB();
}

#define GL __attribute__((address_space(1)))
typedef const GL u16* gp;

__global__ __launch_bounds__(768, 3) void fused(const u16*, const u16*, const u16*, const u16*, const u16*, const u16*, const u16*, const u16*,
    const u16*, const u16*, const u16*, u16*, char*) {
  __shared__ __attribute__((aligned(256))) char smem[SMEM];
  const u32 lds0 = (u32)(size_t)(LDS char*)smem;
  int w0;
#define GLD(r0, r1, r2, r3, o_) "global_load_dwordx4 v[" #r0 ":" #r1 "], " #o_ ", s[16:17]\n\tglobal_load_dwordx4 v[" #r2 ":" #r3 "], " #o_ ", s[16:17] offset:2048\n\t"
  asm volatile(
      "s_load_dwordx16 s[16:31], s[0:1], 0\n\ts_load_dwordx8 s[32:39], s[0:1], 64\n\ts_load_dwordx2 s[40:41], s[0:1], 96\n\t"
      "s_memrealtime s[46:47]\n\t"
      "s_getpc_b64 s[4:5]\n2:\n\ts_lshr_b32 s7, s2, 3\n\ts_and_b32 s10, s4, 0xffffff00\n\ts_mov_b32 s11, s5\n\ts_mul_i32 s8, s7, 0x600\n\t"
      "v_lshl_add_u32 v3, v0, 1, s8\n\ts_cmpk_lt_u32 s2, %[pn]\n\ts_cbranch_scc0 1f\n\t"
      "global_load_ushort v163, v3, s[10:11] offset:%[pf]\n"
      "1:\n\ts_cmpk_lt_u32 s2, 0x80\n\tv_readfirstlane_b32 s3, v0\n\tv_and_b32 v1, 63, v0\n\tv_mov_b64 v[4:5], 0\n\tv_mov_b64 v[6:7], 0\n\tv_lshlrev_b32 v2, 4, v1\n\tv_lshlrev_b32 v8, 2, v0\n\t"
      "s_mov_b32 s42, %[we]\n\ts_mov_b32 s43, 0x20000\n\ts_cbranch_scc0 3f\n\t"
      "s_lshr_b32 s3, s3, 6\n\ts_mul_i32 s9, s3, %[xr]\n\ts_min_u32 s9, s9, %[zm]\n\tv_add_u32 v3, s9, v2\n\tv_add_u32 v3, %[zo], v3\n\t"
      "s_and_b32 s7, s7, 7\n\ts_and_b32 s6, s2, 7\n\ts_and_b32 s8, s2, 64\n\ts_lshl3_add_u32 s8, s6, s8\n\ts_add_u32 s45, s8, s7\n\t"
      "s_add_u32 s4, s4, lane_tab@rel32@lo+(6f-2b-4)\n6:\n\ts_addc_u32 s5, s5, lane_tab@rel32@hi+(6b-2b+4)\n\t"
      "ds_write_b128 v3, v[4:7]\n\ts_cmp_lt_u32 s3, 8\n\ts_cbranch_scc1 5f\n\t"
      "s_lshl_b32 s8, s45, 4\n\ts_lshl_b32 s9, s3, 18\n\ts_add_u32 s9, s9, s8\n\ts_bitset0_b32 s9, 21\n\tv_lshl_add_u32 v148, v1, 12, s9\n\t"
      "s_lshl3_add_u32 s9, s3, s7\n\ts_lshl_b32 s9, s9, 17\n\ts_add_u32 s9, s9, s8\n\ts_bitset0_b32 s9, 23\n\tv_lshl_add_u32 v9, v1, 11, s9\n\t"
      "v_add_u32 v11, 0x100000, v148\n\tv_add_u32 v12, 0x200000, v148\n\t"
      "v_lshlrev_b32 v149, 4, v0\n\ts_lshl_b32 s9, s3, 10\n\ts_add_u32 m0, s9, %[ob]\n\tv_add_u32 v10, 0xffffe800, v149\n\tglobal_load_lds_dwordx4 v10, s[4:5]\n\t"
      "v_add_u32 v149, %[sg], v149\n\t"
      "s_waitcnt lgkmcnt(0)\n\ts_load_dword s44, s[40:41], 0\n\t"
      "s_barrier\n\t"
      "global_load_dword v160, v9, s[16:17]" PRENT "\n\t"
      GLD(100, 103, 104, 107, v148)
      GLD(108, 111, 112, 115, v11)
      "s_cmp_eq_u32 s6, 3\n\ts_cbranch_scc1 .Lrole_p\n\t"
      GLD(116, 119, 120, 123, v12)
      "s_branch .Lrole_p\n"
      "3:\n\ts_lshr_b32 s3, s3, 6\n\ts_cmp_lt_u32 s3, 8\n\ts_cbranch_scc1 4f\n\ts_barrier\n\ts_branch .Lrole_d\n4:\n\t"
      "v_add_u32 v3, %[fc], v2\n\ts_add_u32 s8, s3, s2\n\ts_and_b32 s45, s8, 7\n\ts_lshl3_add_u32 s8, s2, s45\n\t"
      "s_lshl_b32 s8, s8, 12\n\ts_bitset0_b32 s8, 22\n\tds_write_b128 v3, v[4:7]\n\tv_add_u32 v3, s8, v2\n\t"
      "s_waitcnt lgkmcnt(0)\n\tglobal_load_dwordx4 v[116:119], v2, s[30:31]\n\tglobal_load_dwordx4 v[120:123], v2, s[30:31] offset:1024\n\t"
      "s_load_dword s44, s[40:41], 0\n\ts_barrier\n\t"
      "global_load_dwordx4 v[100:103], v3, s[16:17]" LNNT "\n\tglobal_load_dwordx4 v[104:107], v3, s[16:17] offset:1024" LNNT "\n\t"
      "global_load_dwordx4 v[108:111], v3, s[16:17] offset:2048" LNNT "\n\tglobal_load_dwordx4 v[112:115], v3, s[16:17] offset:3072" LNNT "\n\t"
      "s_branch .Lrole_l\n"
      "5:\n\t"
      "global_load_dwordx4 v[16:19], v2, s[4:5]\n\tglobal_load_dwordx4 v[20:23], v2, s[4:5] offset:1024\n\t"
      "s_lshl3_add_u32 s8, s45, s3\n\ts_lshl_b32 s9, s8, 1\n\tv_mov_b32 v9, s9\n\ts_lshl_b32 s9, s45, 11\n\tv_add_u32 v3, s9, v8\n\t"
      "s_waitcnt lgkmcnt(0)\n\tglobal_load_short_d16_hi v24, v9, s[26:27]\n\tglobal_load_ushort v160, v9, s[28:29]\n\t"
      "global_load_dword v96, v3, s[18:19]" PNT "\n\tglobal_load_dword v97, v3, s[20:21]" PNT "\n\t"
      "global_load_dword v68, v3, s[22:23]" PNT "\n\tglobal_load_dword v69, v3, s[24:25]" PNT "\n\ts_barrier\n\ts_branch .Lrole_c"
      : "={s3}"(w0)
      : "{s[0:1]}"(__builtin_amdgcn_kernarg_segment_ptr()), "{s2}"(blockIdx.x), "{v0}"(threadIdx.x), [pf] "n"(PFO), [pn] "n"(8 * PFN), [we] "n"(WS_END), [xr] "n"(XREG),
        [zm] "n"(8 * XREG), [zo] "n"(OX - 16), [sg] "n"(O_SG - 8192), [fc] "n"(FC), [ob] "n"(OB - 8192)
      : "memory");
#define TLREGS()
#define ROLE(name, text, ...) \
  gp x, wg, wu, wd; GL u16* out; rsrc_t rs; int b, w, t, l, g; u32 tag, st0; \
  asm volatile(".Lrole_" #name ":" : "={v0}"(t), "={v1}"(l) __VA_ARGS__ :: "memory"); \
  asm volatile(text : "={s2}"(b), "={s3}"(w), "={s[16:17]}"(x), "={s[32:33]}"(wg), "={s[34:35]}"(wu), "={s[36:37]}"(wd), \
               "={s[38:39]}"(out), "={s[40:43]}"(rs), "={s44}"(tag), "={s45}"(g), "={s46}"(st0) :: "memory"); \
  SB(); \
  __builtin_assume((u32)l < 64u); __builtin_assume((u32)t < 768u); __builtin_assume((u32)w < 12u); __builtin_assume((u32)b < 256u); __builtin_assume((u32)g < 128u); \
  TLREGS(); \
  const int xk = b & 7, j = (b >> 3) & 15, n16 = l & 15, kg = l >> 4
#define TAGWAIT() do { asm volatile("s_waitcnt lgkmcnt(0)" : "+{s44}"(tag)::"memory"); tag += 1u; } while (0)
#define BAR() do { SB(); asm volatile("s_barrier" ::: "memory"); SB(); } while (0)
#define FLAG(o, v) do { asm volatile("" ::: "memory"); SB(); *(volatile LDS u32*)(LDS char*)(smem + (o)) = (v); SB(); } while (0)
#define ARRIVE(o) do { SB(); if (l == 0) asm volatile("ds_add_u32 %0, %1" ::"v"(lds0 + (o)), "v"(1u) : "memory"); SB(); } while (0)
#define CWAIT(o, n, nap) do { _Pragma("unroll 1") for (int spin = 0; spin < SPIN; spin++) { \
      if (__builtin_amdgcn_readfirstlane((int)*(const volatile LDS u32*)(LDS char*)(smem + (o))) >= (int)(n)) break; \
      asm volatile(nap ::: "memory"); } SB(); asm volatile("" ::: "memory"); } while (0)
#define AWAIT(o, n, nap) do { _Pragma("unroll 1") for (int spin = 0; spin < SPIN; spin++) { \
      if (__all(*(const volatile LDS u32*)(LDS char*)(smem + (o)) >= (u32)(n))) break; \
      asm volatile(nap ::: "memory"); } SB(); asm volatile("" ::: "memory"); } while (0)
#define POLL8(res_, off_) do { u32 n_ = SPIN; \
    asm volatile("1:\n\tbuffer_load_dwordx2 v[4:5], %2, %3, 0 offen sc1\n\ts_sub_u32 %1, %1, 1\n\ts_waitcnt vmcnt(0)\n\t" \
                 "v_cmp_ne_u32 vcc, %4, v5\n\ts_cbranch_scc1 2f\n\ts_cbranch_vccnz 1b\n2:" \
                 : "=&{v[4:5]}"(res_), "+s"(n_) : "v"(off_), "s"(rs), "s"(tag) : "vcc", "scc", "memory"); } while (0)
#define UNTIL(t_) "2:\n\ts_sleep 1\n\ts_memrealtime s[8:9]\n\ts_waitcnt lgkmcnt(0)\n\ts_sub_u32 s8, s8, %[st]\n\ts_cmp_lt_u32 s8, " t_ "\n\ts_cbranch_scc1 2b\n\t"
#define LANE(v_) int v_; asm volatile("v_mbcnt_lo_u32_b32 %0, -1, 0\n\tv_mbcnt_hi_u32_b32 %0, -1, %0" : "=v"(v_))
#define IXO(l_) (O_IDX + (2 * xk + (j & 1)) * 128 + ((l_) & 7) * 8)
#define IDS() const int id = 16 * xk + j, sb = id * 57 >> 10, mat = id & 1, u = (id - 18 * sb) >> 1; \
    const bool slow = u >= 6; \
    const int part = slow ? u - 6 : u & 1, cb = 4 * sb + (slow ? 1 : (u >> 1) + (u > 1)), r0 = part * (slow ? 320 : 512), ns = slow ? 5 + (u >> 3) : 8; \
    const int kp = 4 * sb + ((u >> 1) & 3), ct = 2 * (u & 1) + mat
#define TS(a, b_)
#define TSC
#define DONE "s_endpgm\n"
#define TSD
#define TSGET(i0, i1, i2, i3)
  if (blockIdx.x >= 128) {
    if (w0 >= 8) {
      ROLE(d, "s_waitcnt lgkmcnt(0)");
      IDS();
      const int dw = w - 8;
      const u32 sw = (((n16 >> 1) ^ (kg & 1)) << 5) + ((l & 1) << 4);
      gp base = (mat ? wu : wg) + (size_t)(r0 + 4 * dw) * F + cb * 128;
      u32 v = kg * (2 * F) + sw, fa = lds0 + FC + 4 * dw, vp = 96 + dw, t_;
      asm volatile("" : "+v"(v), "+v"(fa), "+v"(vp));
      asm volatile("s_mov_b32 m0, %0" ::"s"(lds0 + 1024 * dw));
#define VMC "s_getreg_b32 s6, hwreg(HW_REG_IB_STS, 0, 4)\n\t"
#define RUN(k, q, lim, v_, b_, st_) asm volatile("1:\n\ts_lshr_b32 s6, m0, 4\n\ts_and_b32 s6, s6, 0xc0\n\tv_xor_b32 %[t], s6, %[v]\n\ts_waitcnt vmcnt(%[c])\n\t" VMC \
        "global_load_lds_dwordx4 %[t], %[b] nt\n\ts_add_u32 m0, m0, %[m]\n\tv_add_u32 %[v], %[d], %[v]\n\t.if %[m] == 3072\n\tv_mad_i32_i24 %[t], s6, -3, %[p]\n\tv_add_u32 %[p], 3, %[p]\n\t" \
        "s_mov_b64 exec, 1\n\tds_max_u32 %[a], %[t]\n\ts_mov_b64 exec, -1\n\t.endif\n\ts_cmp_lt_u32 m0, %[l]\n\ts_cbranch_scc1 1b" \
        : [v] "+v"(v_), [p] "+v"(vp), [t] "=&v"(t_) : [b] "s"(b_), [c] "n"((q) - 1), [m] "n"(1024 * (k)), [d] "n"((k) * (st_)), \
          [a] "v"(fa), [l] "s"(lim) : "scc", "s6", "memory")
#define MARK(c, val) "s_waitcnt vmcnt(" #c ")\n\t" val "\n\ts_mov_b64 exec, 1\n\tds_max_u32 %[a], %[t]\n\ts_mov_b64 exec, -1\n\t"
      __builtin_amdgcn_s_setprio(DPRIO);
      STAMP(8);
      if (id < 126) { if (slow) RUN(4, QAS, 32768, v, base, 8 * F); else RUN(4, 8, 32768, v, base, 8 * F); }
      if (dw < 3) {
        if (id >= 126) __builtin_amdgcn_endpgm();
        gp bd = wd + (size_t)(128 * kp + 2 * dw) * D + ct * 256;
        u32 vd = (l >> 5) * (2 * D + 32) ^ 16 * (l & 31);
        asm volatile("" : "+v"(vd));
        if (slow) RUN(3, QS, 16384 * ns, v, base, 8 * F);
        else { RUN(3, 8, 32768 + 3072 * RAMP, v, base, 8 * F); RUN(3, QD, 131072, v, base, 8 * F); }
        STAMP(1);
        {
          u32 n_ = SPIN;
          asm volatile("1:\n\t" VMC MARK(63, "v_mad_i32_i24 %[t], s6, -3, %[p]") "ds_read_b32 %[t], %[f]\n\ts_sub_u32 %[n], %[n], 1\n\ts_waitcnt lgkmcnt(0)\n\t"
                       "v_cmp_lt_u32 vcc, %[k], %[t]\n\ts_cbranch_scc1 2f\n\ts_cbranch_vccz 1b\n2:\n\ts_mov_b32 m0, %[m]"
                       : [t] "=&v"(t_), [n] "+s"(n_) : [a] "v"(fa), [p] "v"(vp), [f] "v"(u == 8 ? fa : lds0 + FC + 20), [k] "s"(u == 8 ? 159 : 16 * HOLD - 105), [m] "s"(lds0 + 1024 * dw)
                       : "vcc", "scc", "s6", "memory");
        }
        STAMP(3);
        if (u < 8) {
          RUN(3, QE, 65536, vd, bd, 4 * D);
          STAMP(6);
          asm volatile(MARK(0, "v_mov_b32 %[t], -1") : [t] "=&v"(t_) : [a] "v"(fa) : "memory");
          STAMP(4);
        }
        FLUSH();
        __builtin_amdgcn_endpgm();
      }
      asm volatile(MARK(0, "v_mov_b32 %[t], -1") : [t] "=&v"(t_) : [a] "v"(fa) : "memory");
      u32 km, rk;
      u32 hi, lo, k2, k3, gd, thr, sg = 0x0c0c0b09u;
      u32 ka = lds0 + FC + 384 + 8 * (l >> 3), kc = 2047 * 2048 + 2 * r0 + 16 * ((l & 7) ^ ((l >> 2) & 4)), kq = lds0 + FC + 512 + (l & 48), kr = lds0 + FC + 512 + 4 * n16;
      u32 c1 = 896, c2 = 1792, c3 = 2688, cm = (u32)-2048, cs = O_SG + 8 * 2047, ca = lds0 + FC + 512;
      rsrc_t rx = __builtin_amdgcn_make_buffer_rsrc((void*)(x - 1344), 0, 2 * L * D + 4096, 0x00020000);
      u32 co = O_CLR + 8 * (18 * sb + (0x84c0 >> 4 * (kp & 3) & 15) + min(l, (kp & 3) == 1 ? 5 : 3));
      asm volatile("" : "+v"(ka), "+v"(kc), "+v"(kq), "+v"(kr), "+v"(co));
      asm volatile("" : "+s"(c1), "+s"(c2), "+s"(c3), "+s"(cm), "+s"(cs), "+s"(ca), "+s"(sg), "+s"(rx));
      *(u32x4*)(smem + FA + 6144 + 16 * l) = u32x4{0u, 0u, 0u, 0u};
      *(u32x4*)(smem + FA + 7168 + 16 * l) = u32x4{0u, 0u, 0u, 0u};
      CWAIT(FC + 256, 1, "s_sleep 4");
      tag = (u32)__builtin_amdgcn_readfirstlane((int)*(const volatile LDS u32*)(LDS char*)(smem + FC + 24));
      {
        u32 n_ = SPIN, t0, t1, t2, t3, se;
#define TLX(a, b_, c, d, sc) "buffer_load_dwordx4 v[" #a ":" #b_ "], %[o], %[r], 0 offen" sc "\n\tbuffer_load_dwordx4 v[" #c ":" #d "], %[o], %[r], %[ts] offen" sc "\n\t"
#define TLD(a, b_, c, d) TLX(a, b_, c, d, " sc1")
#define TCK(r0, r1, r2, r3, r4, r5, r6, r7) "s_waitcnt vmcnt(4)\n\tv_xor_b32 %[t0], %[t], v" #r1 "\n\tv_bitop3_b32 %[t0], %[t0], %[t], v" #r3 " bitop3:0xf6\n\t" \
          "v_bitop3_b32 %[t0], %[t0], %[t], v" #r5 " bitop3:0xf6\n\tv_bitop3_b32 %[t0], %[t0], %[t], v" #r7 " bitop3:0xf6\n\t" \
          "v_cmp_ne_u32 vcc, 0, %[t0]\n\tv_mov_b32 %[hi], v" #r0 "\n\tv_mov_b32 %[lo], v" #r2 "\n\tv_mov_b32 %[k2], v" #r4 "\n\tv_mov_b32 %[k3], v" #r6 "\n\t" \
          "s_cbranch_vccz 5f\n\t" TLD(r0, r3, r4, r7)
        asm volatile("2:\n\ts_sleep 1\n\ts_memrealtime s[8:9]\n\ts_waitcnt lgkmcnt(0)\n\ts_sub_u32 s8, s8, %[st]\n\ts_cmp_lt_u32 s8, %[tw]\n\ts_cbranch_scc1 2b\n\t"
                     TLX(128, 131, 132, 135, SCT) "s_sleep " STR(RSL) "\n\t" TLD(136, 139, 140, 143) "s_sleep " STR(RSL) "\n\t" TLX(144, 147, 148, 151, SCU)
                     "1:\n\t" TCK(128, 129, 130, 131, 132, 133, 134, 135) TCK(136, 137, 138, 139, 140, 141, 142, 143) TCK(144, 145, 146, 147, 148, 149, 150, 151)
                     "s_sub_u32 %[n], %[n], 1\n\ts_cbranch_scc0 1b\n5:\n\t" TS(60, 61)
                     "v_and_or_b32 %[t0], %[hi], %[mk], %[pa]\n\tv_and_or_b32 %[t1], %[lo], %[mk], %[pb]\n\t"
                     "v_max_u32 %[t2], %[t0], %[t1]\n\tv_max_u32 %[t3], %[t0], %[t1]\n\tv_perm_b32 %[gd], %[hi], %[lo], %[p0]\n\tv_perm_b32 %[se], %[k2], %[k3], %[p0]\n\t"
                     "v_permlane16_swap_b32 %[t2], %[t3]\n\tv_max_u32 %[km], %[t2], %[t3]\n\tv_max_u32 %[rk], %[t2], %[t3]\n\t"
                     "v_max_u32 %[hi], %[t0], %[t1]\n\tv_min_u32 %[lo], %[t0], %[t1]\n\t"
                     "v_permlane32_swap_b32 %[km], %[rk]\n\tv_max_u32 %[km], %[km], %[rk]\n\tv_not_b32 %[t2], %[km]\n\t"
                     "v_and_or_b32 %[k2], %[k2], %[mk], %[pa]\n\t"
                     "v_not_b32_dpp %[t2], %[km] row_ror:4 row_mask:0x2 bank_mask:0xf\n\tv_not_b32_dpp %[t2], %[km] row_ror:8 row_mask:0x4 bank_mask:0xf\n\t"
                     "v_not_b32_dpp %[t2], %[km] row_ror:12 row_mask:0x8 bank_mask:0xf\n\t"
                     "v_pk_lshlrev_b16 %[gd], 4, %[gd] op_sel_hi:[0,1]\n\tv_add_u32 %[t0], %[km], %[t2]\n\t"
                     "v_add_u32_dpp %[t1], %[t2], %[km] row_ror:1 row_mask:0xf bank_mask:0xf\n\tv_add_u32_dpp %[t3], %[t2], %[km] row_ror:2 row_mask:0xf bank_mask:0xf\n\t"
                     "v_add_u32_dpp %[t2], %[t2], %[km] row_ror:3 row_mask:0xf bank_mask:0xf\n\t"
                     RANK8("%[t0]", "%[t1]", "%[t2]", "%[t3]", "%[rk]", "%[t3]", "-2", "v_lshrrev_b32 %[se], 4, %[se]\n\tv_perm_b32 %[gd], %[gd], %[se], %[p1]",
                           "v_pk_max_u16 %[gd], %[gd], %[gd] op_sel:[0,1] op_sel_hi:[1,0]\n\tv_and_or_b32 %[k3], %[k3], %[mk], %[pb]")
                     "v_cmp_eq_u32 vcc, 56, %[rk]\n\tv_max3_u32 %[se], %[lo], %[k2], %[k3]\n\tv_lshrrev_b32 %[gd], 1, %[gd]\n\ts_ff1_i32_b32 s6, vcc_lo\n\t"
                     "v_readlane_b32 s6, %[km], s6\n\tv_or_b32 %[gd], 0x7fff, %[gd]\n\tv_med3_u32 %[t2], %[lo], %[k2], %[k3]\n\t"
                     "v_cmp_le_u32 s[8:9], s6, %[hi]\n\tv_cmp_le_u32 s[10:11], s6, %[se]\n\tv_max_u32 %[t2], %[t2], %[gd]\n\t"
                     "v_mbcnt_lo_u32_b32 %[t0], s8, 0\n\tv_mbcnt_lo_u32_b32 %[t1], s10, -15\n\tv_mbcnt_hi_u32_b32 %[t0], s9, %[t0]\n\tv_mbcnt_hi_u32_b32 %[t1], s11, %[t1]\n\t"
                     "v_lshl_add_u32 %[t0], %[t0], 2, %[ca]\n\tv_mad_i32_i24 %[t1], %[t1], -4, %[ca]\n\tv_cmp_le_u32 vcc, s6, %[t2]\n\t"
                     "s_mov_b64 exec, s[8:9]\n\tds_write_b32 %[t0], %[hi]\n\ts_mov_b64 exec, s[10:11]\n\tds_write_b32 %[t1], %[se]\n\ts_mov_b64 exec, -1\n\t"
                     "ds_read_b32 %[se], %[kr]\n\tds_read_b128 v[152:155], %[kq]\n\t"
                     "s_bcnt1_i32_b64 s8, s[8:9]\n\ts_bcnt1_i32_b64 s10, s[10:11]\n\ts_add_u32 s8, s8, s10\n\ts_cmp_gt_u32 s8, 16\n\ts_cselect_b64 s[8:9], -1, vcc\n\t"
                     "s_cmp_lg_u64 s[8:9], 0\n\ts_waitcnt lgkmcnt(0)\n\ts_cbranch_scc1 6f\n\t"
                     "v_sub_u32 %[t0], %[se], v152\n\tv_sub_u32 %[t1], %[se], v153\n\tv_sub_u32 %[t2], %[se], v154\n\tv_sub_u32 %[t3], %[se], v155\n\t"
                     RANK8("%[t0]", "%[t1]", "%[t2]", "%[t3]", "%[rk]", "%[t3]", "0", "v_mov_b32 %[km], %[se]\n\ts_nop 0", "s_nop 1") "6:\n\t" TS(62, 63)
                     : [hi] "=&v"(hi), [lo] "=&v"(lo), [k2] "=&v"(k2), [k3] "=&v"(k3), [gd] "=&v"(gd), [se] "=&v"(se), [km] "=&v"(km), [rk] "=&v"(rk),
                       [t0] "=&v"(t0), [t1] "=&v"(t1), [t2] "=&v"(t2), [t3] "=&v"(t3), [n] "+s"(n_)
                     : [o] "v"(O_TOP + 16 * (l & 7) + TL * (l >> 3)), [r] "s"(rs), [t] "s"(tag), [ts] "s"(TOPS), [st] "s"(st0), [tw] "n"(TW), [mk] "s"(0x7fff800fu),
                       [pa] "v"((127 - 2 * l) << 4), [pb] "v"((126 - 2 * l) << 4), [sg] "s"(sg), [p0] "s"(0x05040100u), [p1] "s"(0x07020500u), [ca] "s"(ca),
                       [kr] "v"(kr), [kq] "v"(kq)
                     : "vcc", "scc", "s6", "s8", "s9", "s10", "s11", "memory" TSC);
      }
      unsigned long long more;
      asm volatile("" : "={s6}"(thr), "={s[8:9]}"(more));
      if (__builtin_expect(more != 0, 0)) {
        const u32 m0 = km;
        unsigned long long all = __ballot(gd >= thr);
        if (gd >= thr) hi = lo = k2 = k3 = 0u;
#pragma unroll 1
        for (int i = 0; i < 4;) {
          u32 k, m = m0;
          if (all) {
            const int a = __builtin_ctzll(all);
            all &= all - 1;
            u32x2 r;
            const u32 o = O_RG + 256 * a + 8 * (l & 31);
            POLL8(r, o);
            k = l < 32 ? r.x : 0u;
            m = (u32)__builtin_amdgcn_readlane((int)m0, a);
          } else { k = hi; hi = lo; lo = k2; k2 = k3; i++; }
          asm volatile("v_cmp_le_u32 s[8:9], %[t], %[k]\n\tv_cmp_ne_u32 vcc, %[k], %[m0]\n\ts_and_b64 s[8:9], s[8:9], vcc\n\ts_cbranch_scc0 2f\n"
                       "1:\n\ts_ff1_i32_b64 s7, s[8:9]\n\tv_readlane_b32 s12, %[k], s7\n\ts_lshl_b64 s[10:11], 1, s7\n\ts_nop 0\n\t"
                       "v_cmpx_gt_u32 vcc, s12, %[km]\n\tv_add_u32 %[rk], 8, %[rk]\n\ts_bcnt0_i32_b32 s7, vcc_lo\n\tv_cmpx_eq_u32 vcc, 0x80, %[rk]\n\t"
                       "v_mov_b32 %[km], s12\n\tv_lshlrev_b32 %[rk], 2, s7\n\ts_mov_b64 exec, -1\n\ts_andn2_b64 s[8:9], s[8:9], s[10:11]\n\ts_cbranch_scc1 1b\n2:"
                       : [km] "+v"(km), [rk] "+v"(rk) : [k] "v"(k), [m0] "v"(m), [t] "s"(thr) : "vcc", "scc", "s7", "s8", "s9", "s10", "s11", "s12");
        }
      }
      if (id < 126) {
        u32 a_, v_, n_ = SPIN;
#define RLD(o, s_) "buffer_load_dwordx4 %[v], %[rx], " s_ " offen offset:" #o " lds\n\t"
        asm volatile("v_add_u32 %[a], %[kb], %[rk]\n\tv_and_b32 %[v], 0x7ff, %[km]\n\ts_mov_b32 m0, %[fa]\n\tds_write_b32 %[a], %[v]\n\tds_read_b32 %[a], %[ka]\n\t"
                     "s_cmp_eq_u32 %[ns], 8\n\ts_waitcnt lgkmcnt(0)\n\tv_mad_i32_i24 %[v], %[a], %[cm], %[kc]\n\t"
                     RLD(0, "%[c3]") RLD(1024, "%[c2]") RLD(2048, "%[c1]") RLD(3072, "0")
                     "s_mov_b32 m0, %[fb]\n\tv_add_u32 %[v], 0x200, %[v]\n\tv_mad_i32_i24 %[a], %[a], -8, %[cs]\n\t"
                     "s_cbranch_scc0 3f\n\t" RLD(2048, "%[c1]") RLD(3072, "0")
                     "3:\n\tbuffer_load_dwordx2 v[160:161], %[a], %[rs], 0 offen sc1\n\t" RLD(0, "%[c3]") RLD(1024, "%[c2]") "buffer_load_dwordx2 v[158:159], %[co], %[rs], 0 offen sc1\n\t"
                     "v_mov_b32 %[v], 16 * " STR(HOLD) " - 112\n\tv_subrev_u32 %[ka], %[kd], %[ka]\n\ts_mov_b64 exec, 1\n\tds_add_u32 %[fc], %[v] offset:8\n\ts_mov_b64 exec, -1\n\tv_mov_b32 %[v], -1\n\t" TS(64, 65)
                     "s_sleep " STR(CSL) "\n\tbuffer_load_dwordx2 v[154:155], %[co], %[rs], 0 offen sc1\n\t"
                     "s_waitcnt vmcnt(4)\n1:\n\tv_cmp_ne_u32 vcc, %[t], v161\n\tv_and_b32 v156, 0xffff0000, v160\n\tv_lshlrev_b32 v157, 16, v160\n\tv_rcp_f32 v156, v156\n\t"
                     "s_sub_u32 %[n], %[n], 1\n\ts_cbranch_vccz 2f\n\ts_cbranch_scc1 2f\n\t"
                     "buffer_load_dwordx2 v[160:161], %[a], %[rs], 0 offen sc1\n\ts_waitcnt vmcnt(0)\n\ts_branch 1b\n2:\n\tv_mul_f32 v157, -v157, v156\n\t"
                     "ds_write_b64 %[ka], v[156:157]\n\ts_waitcnt vmcnt(2)\n\tds_write_b32 %[fc], %[v] offset:4\n\t" TS(66, 67)
                     "s_mov_b32 %[n], %[spin]\n\ts_waitcnt vmcnt(1)\n\tv_cmp_ne_u32 vcc, %[t], v159\n\ts_cbranch_vccz 5f\n"
                     "4:\n\ts_waitcnt vmcnt(0)\n\tv_cmp_ne_u32 vcc, %[t], v155\n\ts_sub_u32 %[n], %[n], 1\n\ts_cbranch_vccz 5f\n\t"
                     "buffer_load_dwordx2 v[154:155], %[co], %[rs], 0 offen sc1\n\ts_cbranch_scc0 4b\n5:"
                     : [a] "=&v"(a_), [v] "=&v"(v_), [ka] "+v"(ka), [n] "+s"(n_)
                     : [co] "v"(co), [rk] "v"(rk), [km] "v"(km), [kc] "v"(kc), [kb] "n"(FC + 384), [kd] "n"(384 - 32), [fa] "n"(FA), [fb] "n"(FA + 4096), [rx] "s"(rx), [rs] "s"(rs),
                       [c1] "s"(c1), [c2] "s"(c2), [c3] "s"(c3), [cm] "s"(cm), [cs] "s"(cs), [ns] "s"(ns), [fc] "v"(fa), [t] "s"(tag), [spin] "n"(SPIN) : "vcc", "scc", "memory" TSC);
        TSGET(12, 13, 14, 15);
        FLAG(FC + 28, 1u);
        STAMP(10);
      } else {
        const u32 cd = km << 1;
        const u32x2 r = {(2047u - (km & 0x7ffu)) | ((cd ^ ((u32)((int)~cd >> 15) | 0x80000000u)) & 0xffff0000u), tag};
        const u32 o = rk < 64u ? O_IDX + 1024 * mat + 128 * kg + rk : 0x80000000u;
        st8(rs, o, r);
        st8(rs, o + 512, r);
        TSGET(12, 13, 14, 15);
        STAMP(14);
        STAMP(15);
        if (mat) {
#pragma unroll 1
          for (int spin = 0; spin < SPIN; spin++) {
            const u32x4 a = ld16(rs, O_ARR + 16 * l);
            if (__all(a.y == tag && a.w == tag)) break;
            asm volatile("s_sleep 16" ::: "memory");
          }
          if (l == 0) __builtin_amdgcn_raw_buffer_store_b32(tag, rs, 0, 0, 16);
        }
      }
      FLUSH();
      __builtin_amdgcn_endpgm();
    }
    u32x4 xv[4];
    u32x8 wv;
    ROLE(l, "", , "={v[100:103]}"(xv[0]), "={v[104:107]}"(xv[1]), "={v[108:111]}"(xv[2]), "={v[112:115]}"(xv[3]), "={v[116:123]}"(wv));
    STAMP(11);
    u32 sg = 0x0c0c0b09u;
    asm volatile("" : "+s"(sg));
    ln_rows(xv, wv, rs, tag, b, g, l, lds0 + FC + 128, sg TLVAL);
    STAMP(10);
    IDS();
    if (id >= 126) {
      FLUSH();
      __builtin_amdgcn_endpgm();
    }
    const int wr = 16 * (kg >> 1) + 4 * (kg & 1) + (n16 & 8) + ((n16 >> 2) & 1);
    u32 wa = lds0 + 256 * wr + ((w ^ ((wr & 1) | ((wr >> 1) & 6))) << 5) + 8 * (l & 3), sel = (l & 8) ? 0xeeeeu : 0x4444u;
    u32 so = 4 * t;
    asm volatile("" : "+v"(wa), "+v"(sel), "+v"(so));
    asm volatile(UNTIL(STR(CLD)) :: [st] "s"(st0) : "s8", "s9", "scc");
    __builtin_amdgcn_raw_buffer_store_b32(~0u, rs, so, O_HG + (6 * cb + 3 * mat + part) * 2048, 16);
    st8(rs, u < 8 ? O_PG + (4 * kp + ct) * 4096 + 2 * so : 0x80000000u, u32x2{~0u, ~0u});
#define SWP(a0, a1, a2, a3) "v_permlane32_swap_b32 " a0 ", " a2 "\n\tv_permlane32_swap_b32 " a1 ", " a3 "\n\t"
#define OUT(a0, a1, o) "v_cvt_pk_bf16_f32 " a0 ", " a0 ", " a1 "\n\tv_min_u32 " a0 ", -2, " a0 "\n\tbuffer_store_dword " a0 ", %[so], %[rs], " o " sc1\n\t"
#define TR4(a, b_, c, d, e, f, g_, h, adr, o) "ds_read_b64_tr_b16 v[" #a ":" #b_ "], " adr " offset:" #o "\n\tds_read_b64_tr_b16 v[" #c ":" #d "], " adr " offset:" #o "+512\n\t" \
      "ds_read_b64_tr_b16 v[" #e ":" #f "], " adr " offset:" #o "+8192\n\tds_read_b64_tr_b16 v[" #g_ ":" #h "], " adr " offset:" #o "+8704\n\t"
#define SM(d, a, b_) "v_smfmac_f32_16x16x64_bf16 v[" d "], v[" a "], v[" b_ "], %[sel]\n\t"
#define USE(c, xa, wf) "s_add_u32 s6, s6, 16\n\ts_waitcnt lgkmcnt(" #c ")\n\t" SM("16:19", "20:23", wf) SM("8:11", xa, wf)
#define XR(xa, o) "ds_read_b128 v[" xa "], %[xa] offset:" #o "\n\t"
#define XU(c, xa, wf) "s_waitcnt lgkmcnt(" #c ")\n\t" SM("8:11", xa, wf)
#define ZERO(r) "v_mfma_f32_32x32x16_bf16 v[" r "], v[8:11], v[8:11], 0\n\t"
#define POLL(c, rd) "7:\n\tds_read_b32 v30, %[fl]\n\ts_waitcnt lgkmcnt(0)\n\ts_call_b64 s[10:11], 1f\n\ts_cbranch_scc1 7b\n\t" rd
#define CLR "s_waitcnt vmcnt(0)\n\ts_mov_b64 exec, 1\n\tds_add_rtn_u32 v14, %[fl], v28 offset:96\n\ts_waitcnt lgkmcnt(0)\n\tv_cmpx_eq_u32 vcc, 7, v14\n\t" \
      "buffer_store_dwordx2 v[12:13], off, %[rs], %[co] sc1\n\ts_mov_b64 exec, -1\n\t"
#define XE(k) "s_cmpk_eq_u32 s6, " #k "\n\ts_cbranch_scc1 9f\n\t"
    asm volatile("v_mov_b64 v[8:9], 0\n\tv_mov_b64 v[10:11], 0\n\tv_mov_b64 v[16:17], 0\n\tv_mov_b64 v[18:19], 0\n\t"
                 ZERO("96:111") ZERO("112:127")
                 "v_mov_b32 v20, 0x3f803f80\n\tv_mov_b32 v21, v20\n\tv_mov_b64 v[22:23], v[20:21]\n\tv_mov_b32 v13, %[t]\n\tv_mov_b32 v28, 1\n\tv_mov_b32 v30, 0\n\t"
                 "v_add_u32 v29, 0x10000, %[wa]\n\ts_movk_i32 s6, 96\n\ts_mov_b32 s7, 1\n\ts_mov_b32 s8, 15\n\ts_mov_b32 s9, %[spin]\n\ts_nop 7\n\t"
                 POLL(8, TR4(32, 33, 34, 35, 36, 37, 38, 39, "%[wa]", 0) TR4(40, 41, 42, 43, 44, 45, 46, 47, "%[wa]", 16384))
                 USE(4, "96:99", "32:39") "s_waitcnt lgkmcnt(0)\n\t" SM("16:19", "20:23", "40:47") SM("8:11", "100:103", "40:47")
                 CLR
                 POLL(4, TR4(48, 49, 50, 51, 52, 53, 54, 55, "%[wa]", 32768)) USE(0, "104:107", "48:55")
                 POLL(4, TR4(56, 57, 58, 59, 60, 61, 62, 63, "%[wa]", 49152)) "s_mov_b64 exec, 1\n\tds_add_u32 %[fl], v28 offset:20\n\ts_mov_b64 exec, -1\n\t" TS(60, 61)
                 USE(0, "108:111", "56:63")
                 POLL(4, TR4(64, 65, 66, 67, 68, 69, 70, 71, "v29", 0)) USE(0, "112:115", "64:71") "s_cmp_eq_u32 %[ns], 5\n\ts_cbranch_scc1 5f\n\t"
                 POLL(4, TR4(72, 73, 74, 75, 76, 77, 78, 79, "v29", 16384)) USE(0, "116:119", "72:79") "s_cmp_eq_u32 %[ns], 6\n\ts_cbranch_scc1 5f\n\t"
                 POLL(4, TR4(80, 81, 82, 83, 84, 85, 86, 87, "v29", 32768)) USE(0, "120:123", "80:87")
                 POLL(4, TR4(88, 89, 90, 91, 92, 93, 94, 95, "v29", 49152)) USE(0, "124:127", "88:95")
                 "5:\n\t" TS(62, 63) "s_cmp_eq_u32 s7, 0\n\ts_cbranch_scc1 4f\n\ts_mov_b32 s8, 16\n\t"
                 "s_getpc_b64 s[10:11]\n\tds_read_b32 v30, %[fl]\n\ts_waitcnt lgkmcnt(0)\n"

                 "1:\n\tv_cmp_le_u32 vcc, s6, v30\n\ts_and_b32 s12, vcc_hi, s7\n\ts_cbranch_scc1 3f\n"
                 "2:\n\ts_andn2_b32 s12, s8, vcc_lo\n\ts_cbranch_scc1 6f\n\ts_setpc_b64 s[10:11]\n"
                 "6:\n\ts_sub_u32 s9, s9, 1\n\ts_cbranch_scc1 4f\n\t" GNAP "\n\ts_cmp_eq_u32 s6, s6\n\ts_setpc_b64 s[10:11]\n"
                 "3:\n\t"
                 XR("96:99", 0) XR("100:103", 1024) XR("104:107", 2048) XR("108:111", 3072) XR("112:115", 4096) XR("116:119", 5120) XR("120:123", 6144) XR("124:127", 7168)
                 "ds_read_b128 v[24:27], %[pa]\n\ts_mov_b32 s7, 0\n\t"
                 TS(64, 65)
                 XE(96) XU(8, "96:99", "32:39") XU(7, "100:103", "40:47") XE(112) XU(6, "104:107", "48:55") XE(128) XU(5, "108:111", "56:63") XE(144)
                 XU(4, "112:115", "64:71") XE(160) XU(3, "116:119", "72:79") XE(176) XU(2, "120:123", "80:87") XE(192) XU(1, "124:127", "88:95")
                 "9:\n\ts_cmp_eq_u32 s8, 16\n\ts_waitcnt lgkmcnt(0)\n\ts_cbranch_scc0 2b\n"
                 "4:\n\ts_nop 4\n\tv_permlane32_swap_b32 v16, v17\n\tv_add_f32 v16, v16, v17\n\tv_mul_f32 v25, v25, v16\n\tv_mul_f32 v27, v27, v16\n\t"
                 SWP("v8", "v9", "v10", "v11") "v_fmac_f32 v25, v10, v24\n\tv_fmac_f32 v27, v11, v26\n\tv_fma_f32 v8, v8, v24, v25\n\tv_fma_f32 v9, v9, v26, v27\n\t"
                 OUT("v8", "v9", "%[ho] offen") TS(66, 67)
                 :: [wa] "v"(wa), [sel] "v"(sel), [so] "v"(so), [fl] "v"(lds0 + FC + 4 * min(l, 4)), [xa] "v"(lds0 + FA + 128 * (l & 7) + 16 * ((2 * kg + (n16 >> 3)) ^ (2 * l & 4))),
                    [pa] "v"(lds0 + FC + 32 + 32 * (kg & 1) + 16 * (kg >> 1)), [ns] "s"(ns), [t] "s"(tag), [co] "s"(O_CLR + 8 * id), [rs] "s"(rs), [ho] "s"(O_HG + (6 * cb + 3 * mat + part) * 2048), [spin] "n"(SPIN)
                 : "vcc", "scc", "s6", "s7", "s8", "s9", "s10", "s11", "s12", "s13", "memory", G_CLOBBER TSC);
    TSGET(1, 4, 3, 5);
    if (u < 8) {
      const int tp = 2 * (kg & 1) + (kg >> 1), r7 = 4 * (kg & 1) + (n16 >> 2);
      u32 o3 = (kp & 3) == 1 ? so + 4096 : 0x80000000u, h0 = O_HG + kp * 12288, h1 = h0 + 6144;
      u32 hw = lds0 + FH + 2 * tp * HP + 32 * w + 8 * (l & 2) + (l & 4) * 2 + ((l & 8) >> 1) + 2 * (l & 1);
      u32 hr = lds0 + FH + (l & 7) * HP + 32 * kg + 2 * (l & 8);
      u32 da = lds0 + 512 * (16 * (kg >> 1) + r7) + ((2 * w ^ r7) << 5) + 8 * (l & 3), db = da ^ 32, fl = lds0 + FC + 4 * (l & 3);
      u32 po = 512 * w + 16 * (l & 8) + 32 * tp + 4 * (l & 7);
      u32 e0 = 0x3f80u, e1 = 0x3f800000u;
      asm volatile("" : "+v"(hw), "+v"(hr), "+v"(da), "+v"(db), "+v"(fl), "+v"(po), "+v"(o3));
      asm volatile("" : "+s"(e0), "+s"(e1), "+s"(h0), "+s"(h1));
#define TRD(a, b_, c, d, e, f, g_, h, adr, o) "ds_read_b64_tr_b16 v[" #a ":" #b_ "], " adr " offset:" #o "\n\tds_read_b64_tr_b16 v[" #c ":" #d "], " adr " offset:" #o "+4096\n\t" \
      "ds_read_b64_tr_b16 v[" #e ":" #f "], " adr " offset:" #o "+16384\n\tds_read_b64_tr_b16 v[" #g_ ":" #h "], " adr " offset:" #o "+20480\n\t"
#define HLD(X, r) X(r "0", "%[s1]", "%[h0] offen") X(r "3", "%[s1]", "%[h1] offen") X(r "1", "%[s1]", "%[h0] offen offset:2048") X(r "4", "%[s1]", "%[h1] offen offset:2048") \
      X(r "2", "%[o3]", "%[h0] offen") X(r "5", "%[o3]", "%[h1] offen")
#define HL0(a, o, s_) "buffer_load_dword " a ", " o ", %[rs], " s_ " sc1\n\t"
#define HLF(a, o, s_) "buffer_load_dword " a ", " o ", %[rs], " s_ SCH "\n\t"
#define HL1(a, o, s_) "v_cmpx_eq_u32 vcc, -1, " a "\n\t" HL0(a, o, s_) "s_mov_b64 exec, -1\n\t"
#define DOT(d, e, a) "v_dot2c_f32_bf16 " d ", %[" e "], " a "\n\t"
#define HCK(c, r) "s_cmp_eq_u32 s7, 0\n\ts_cbranch_scc1 5f\n\ts_call_b64 s[10:11], 4f\n5:\n\ts_waitcnt vmcnt(" #c ")\n\tv_max3_u32 v28, " r "0, " r "1, " r "2\n\tv_lshlrev_b32 v24, 16, " r "0\n\tv_max3_u32 v28, v28, " r "3, " r "4\n\t" \
      "v_and_b32 v25, %[hm], " r "0\n\tv_max_u32 v28, v28, " r "5\n\t" DOT("v24", "e0", r "1") "v_cmp_eq_u32 vcc, -1, v28\n\t" DOT("v25", "e1", r "1") DOT("v24", "e0", r "2") DOT("v25", "e1", r "2") \
      "v_lshlrev_b32 v26, 16, " r "3\n\tv_and_b32 v27, %[hm], " r "3\n\t" DOT("v26", "e0", r "4") DOT("v27", "e1", r "4") DOT("v26", "e0", r "5") DOT("v27", "e1", r "5")
#define RB2(a, b_) "v_cvt_pk_bf16_f32 " a ", 0, " a "\n\tv_cvt_pk_bf16_f32 " b_ ", 0, " b_ "\n\t"
      asm volatile("s_mov_b32 s7, 1\n\ts_mov_b32 s12, %[spin]\n\tv_mov_b64 v[8:9], 0\n\tv_mov_b64 v[10:11], 0\n\tv_mov_b64 v[12:13], 0\n\tv_mov_b64 v[14:15], 0\n"
                   "2:\n\ts_sleep 1\n\tds_read_b32 v28, %[fl] offset:16\n\ts_memrealtime s[8:9]\n\ts_waitcnt lgkmcnt(0)\n\tv_cmp_eq_u32 vcc, 0, v28\n\ts_sub_u32 s8, s8, %[st]\n\t"
                   "s_cmp_lt_u32 s8, %[th]\n\ts_cbranch_scc1 2b\n\ts_bitcmp1_b32 vcc_lo, 3\n\ts_cbranch_scc1 2b\n\t" TS(64, 65)
                   HLD(HLF, "v7") HCK(0, "v7") "s_cbranch_vccnz 9f\n"
                   "3:\n\t" TS(66, 67) RB2("v24", "v25") "v_mul_f32 v28, %[le], v24\n\tv_mul_f32 v29, %[le], v25\n\tv_exp_f32 v28, v28\n\tv_exp_f32 v29, v29\n\t" RB2("v26", "v27")
                   "v_add_f32 v28, 1.0, v28\n\tv_add_f32 v29, 1.0, v29\n\tv_rcp_f32 v28, v28\n\tv_rcp_f32 v29, v29\n\ts_cmp_eq_u32 s7, 0\n\tv_mul_f32 v24, v24, v28\n\tv_mul_f32 v25, v25, v29\n\t"
                   RB2("v24", "v25") "v_mul_f32 v24, v24, v26\n\tv_mul_f32 v25, v25, v27\n\tv_cvt_pk_bf16_f32 v24, v24, v25\n\ts_mov_b32 s12, %[spin]\n\t"
                   "ds_write_b16 %[hw], v24\n\tds_write_b16_d16_hi %[hw], v24 offset:%[hp]\n\ts_cbranch_scc1 8f\n"
                   "7:\n\ts_call_b64 s[10:11], 4f\n\ts_mul_i32 s12, s12, s7\n\ts_sub_u32 s12, s12, 1\n\ts_cbranch_scc0 7b\n"
                   "8:\n\ts_waitcnt lgkmcnt(0)\n\ts_barrier\n\t" TS(60, 61)
                   XR("16:19", 0) XR("20:23", 128)
                   XU(1, "16:19", "32:39") SM("12:15", "16:19", "40:47") XU(0, "20:23", "48:55") SM("12:15", "20:23", "56:63") "s_nop 6\n\t"
                   SWP("v8", "v9", "v10", "v11") "v_add_f32 v8, v8, v10\n\tv_add_f32 v9, v9, v11\n\t" SWP("v12", "v13", "v14", "v15") OUT("v8", "v9", "%[po] offen")
                   "v_add_f32 v12, v12, v14\n\tv_add_f32 v13, v13, v15\n\t" OUT("v12", "v13", "%[po] offen offset:256") TS(62, 63) DONE
                   "9:\n\tv_mov_b64 v[80:81], v[70:71]\n\tv_mov_b64 v[82:83], v[72:73]\n\tv_mov_b64 v[84:85], v[74:75]\n\t" HLD(HL1, "v7") "s_sleep " STR(HNAP) "\n"
                   "1:\n\t" HLD(HL1, "v8") HCK(6, "v7") "s_cbranch_vccz 3b\n\t" HLD(HL1, "v7") HCK(6, "v8") "s_sub_u32 s12, s12, 1\n\ts_cbranch_vccz 3b\n\ts_cbranch_scc0 1b\n\ts_branch 3b\n"
                   "4:\n\tds_read_b32 v28, %[fl]\n\ts_waitcnt lgkmcnt(0)\n\tv_cmp_ne_u32 vcc, -1, v28\n\ts_cbranch_vccnz 6f\n\t"
                   TRD(32, 33, 34, 35, 36, 37, 38, 39, "%[da]", 0) TRD(40, 41, 42, 43, 44, 45, 46, 47, "%[db]", 0)
                   TRD(48, 49, 50, 51, 52, 53, 54, 55, "%[da]", 32768) TRD(56, 57, 58, 59, 60, 61, 62, 63, "%[db]", 32768) "s_mov_b32 s7, 0\n"
                   "6:\n\ts_setpc_b64 s[10:11]\n0:"
                   :: [s1] "v"(so), [o3] "v"(o3), [fl] "v"(fl), [da] "v"(da), [db] "v"(db), [hw] "v"(hw), [xa] "v"(hr), [sel] "v"(sel), [so] "v"(po), [rs] "s"(rs), [h0] "s"(h0), [h1] "s"(h1),
                      [st] "s"(st0), [th] "n"(TH), [spin] "n"(SPIN), [hp] "n"(HP), [po] "s"(O_PG + (4 * kp + ct) * 4096), [e0] "s"(e0), [e1] "s"(e1), [hm] "s"(0xffff0000u),
                      [le] "s"(-1.4426950408889634f)
                   : "vcc", "scc", "s7", "s8", "s9", "s10", "s11", "s12", "memory", G_CLOBBER TSC);
      TSGET(8, 9, 6, 7);
    }
    FLUSH();
    __builtin_amdgcn_endpgm();
  }

  if (w0 >= 8) {
    u32x8 xa0, xa1, xa2, xa3, sa1;
    u32x4 junk;
    u32 op, so;
    ROLE(p, "", , "={v[100:107]}"(xa0), "={v[108:115]}"(xa1), "={v[116:123]}"(xa2),
         "={v148}"(op), "={v149}"(so), "={v[160:163]}"(junk));
    const int p = w - 8;
    __builtin_amdgcn_s_setprio(3);
    asm volatile("s_waitcnt lgkmcnt(0)" : "+{s44}"(tag)::"memory");
    tag += 1u;
    STAMP(9);
    f32x2 xq[4][8];
    u32x8 pa;
    {
#define SCK(r1, r3, r5, r7) "v_xor_b32 %[d], %[t], v" #r1 "\n\tv_bitop3_b32 %[d], %[d], %[t], v" #r3 " bitop3:0xf6\n\t" \
      "v_bitop3_b32 %[d], %[d], %[t], v" #r5 " bitop3:0xf6\n\tv_bitop3_b32 %[d], %[d], %[t], v" #r7 " bitop3:0xf6\n\t"
#define SLX(r0, r3, r4, r7, k, k1, sc) "buffer_load_dwordx4 v[" #r0 ":" #r3 "], %[so], %[rs], " k " offen" sc "\n\tbuffer_load_dwordx4 v[" #r4 ":" #r7 "], %[so], %[rs], " k1 " offen" sc "\n\t"
#define SLD(r0, r3, r4, r7, k, k1) SLX(r0, r3, r4, r7, k, k1, " sc1")
#define SLOOP(r0, r1, r3, r4, r5, r7, k, k1) "1:\n\ts_waitcnt vmcnt(0)\n4:\n\t" SCK(r1, r3, r5, r7) "s_sub_u32 %[n], %[n], 1\n\tv_cmp_ne_u32 vcc, 0, %[d]\n\ts_cbranch_scc1 2f\n\ts_cbranch_vccz 2f\n\t" \
      SLD(r0, r3, r4, r7, k, k1) "s_branch 1b\n2:\n\t"
      u32 d_, n_ = SPIN, k1 = 4096, k2 = 8192, k3 = 12288, s0[4];
      asm volatile("s_mov_b32 m0, %[m]\n\ts_cmp_eq_u32 %[k], 3\n\ts_waitcnt vmcnt(2)\n\ts_cbranch_scc0 1f\n\ts_waitcnt vmcnt(0)\n1:"
                   : "+{v[100:107]}"(xa0), "+{v[108:115]}"(xa1), "+{v[160:163]}"(junk) : [m] "s"(lds0 + OX + 2 * XO + 256 * p), [k] "s"(xk) : "scc", "memory");
      SB();
#define UNPACK(i, xs) _Pragma("unroll") for (int c = 0; c < 8; c++) { \
        xq[i][c] = (c & 1) ? f32x2{hi2f(xs[c >> 1]), hi2f(xs[4 + (c >> 1)])} : f32x2{lo2f(xs[c >> 1]), lo2f(xs[4 + (c >> 1)])}; asm volatile("" : "+v"(xq[i][c].x), "+v"(xq[i][c].y)); }
      UNPACK(0, xa0);
      UNPACK(1, xa1);
      u32 fa = lds0 + OXF + 4 * p, fv = 1;
      asm volatile("" : "+v"(fa), "+v"(fv));
      asm volatile("" : "+s"(k1), "+s"(k2), "+s"(k3), "+s"(n_));
      SB();
      STAMP(1);
#define SCP(r0, r1, r2, r3, r4, r5, r6, r7) "s_waitcnt vmcnt(2)\n\t" SCK(r1, r3, r5, r7) "v_cmp_ne_u32 vcc, 0, %[d]\n\tv_mov_b32 %[a0], v" #r0 "\n\tv_mov_b32 %[a1], v" #r2 "\n\t" \
      "v_mov_b32 %[a2], v" #r4 "\n\tv_mov_b32 %[a3], v" #r6 "\n\ts_cbranch_vccz 3f\n\t" SLD(r0, r3, r4, r7, "0", "%[k1]")
      asm volatile(UNTIL(STR(TSG)) SLX(132, 135, 136, 139, "0", "%[k1]", SCA) "s_sleep " STR(SSL) "\n\t" SLX(140, 143, 144, 147, "0", "%[k1]", SCC)
                   "1:\n\t" SCP(132, 133, 134, 135, 136, 137, 138, 139) SCP(140, 141, 142, 143, 144, 145, 146, 147) "s_sub_u32 %[n], %[n], 1\n\ts_cbranch_scc0 1b\n"
                   "3:\n\t" SLX(140, 143, 144, 147, "%[k2]", "%[k3]", SCB)
                   : [d] "=&v"(d_), [n] "+s"(n_), [a0] "=&v"(s0[0]), [a1] "=&v"(s0[1]), [a2] "=&v"(s0[2]), [a3] "=&v"(s0[3]), "=&{v[132:139]}"(pa), "=&{v[140:147]}"(sa1)
                   : [st] "s"(st0), [t] "s"(tag), [so] "v"(so), [rs] "s"(rs), [k1] "s"(k1), [k2] "s"(k2), [k3] "s"(k3) : "vcc", "scc", "s8", "s9", "memory");
#pragma unroll
      for (int i = 0; i < 5; i++) {
#define XLD(r0, r1, r2, r3, o_) "v_add_u32 %[d], " #o_ ", %[o]\n\tglobal_load_dwordx4 v[" #r0 ":" #r1 "], %[d], %[x]\n\tglobal_load_dwordx4 v[" #r2 ":" #r3 "], %[d], %[x] offset:2048\n"
        if (i == XI) asm volatile("s_cmp_eq_u32 %[k], 3\n\ts_cbranch_scc0 1f\n\t" XLD(116, 119, 120, 123, 0x200000) "1:\n\t" XLD(124, 127, 128, 131, 0x300000)
                                 : [d] "=&v"(d_), "+{v[116:123]}"(xa2), "=&{v[124:131]}"(xa3) : [k] "s"(xk), [o] "v"(op), [x] "s"(x) : "scc", "memory");
        if (i == 2) {
          asm volatile("s_waitcnt vmcnt(2)\n\ts_branch 4f\n" SLOOP(140, 141, 143, 144, 145, 147, "%[k2]", "%[k3]") : [d] "=&v"(d_), [n] "+s"(n_), "+{v[140:147]}"(sa1), "+{v[116:123]}"(xa2), "+{v[132:139]}"(pa)
                       : [t] "s"(tag), [so] "v"(so), [rs] "s"(rs), [k2] "s"(k2), [k3] "s"(k3) : "vcc", "scc", "memory");
          UNPACK(2, xa2);
        }
        if (i == 3) asm volatile("s_waitcnt vmcnt(%1)" : "+{v[124:131]}"(xa3) : "n"(DPI == 2 ? DPF : 0));
        if (i == DPI && DPF) {
          u32 lx = l;
          asm volatile("" : "+v"(lx));
          const u32 ti = min(16 * xk + 4 * p + (lx >> 4), 125u), tsb = __umul24(ti, 57) >> 10, tu = min(ti - __umul24(18, tsb), 15u);
          const u32 o = (128 * (4 * tsb + (tu >> 2)) + DPO / 2 + 4 * j + ((lx >> 2) & 3)) * (2 * D) + 512 * (tu & 3) + 128 * (lx & 3);
          if (DPI == 4) AWAIT(OXF + 4 * (l & 3), 3, "s_sleep 1");
          if (DPF == 1) asm volatile("global_load_dword v160, %1, %2" : "=&{v[160:163]}"(junk) : "v"(o), "s"(wd) : "memory");
          else asm volatile("global_load_dword v160, %1, %2\n\tglobal_load_dword v161, %1, %3" : "=&{v[160:163]}"(junk) : "v"(o), "s"(wd), "s"(wd + 64 * D) : "memory");
        }
        if (i == 4) break;
        if (i == 3) UNPACK(3, xa3);
        const u32 sa = i < 2 ? s0[2 * i] : sa1[4 * i - 8], sb = i < 2 ? s0[2 * i + 1] : sa1[4 * i - 6];
        if (i == 0) STAMP(3);
        if (i == 2) STAMP(10);
        const f32x2 r2 = {__builtin_amdgcn_rcpf(hi2f(sa)), __builtin_amdgcn_rcpf(hi2f(sb))};
        const f32x2 m2 = {-lo2f(sa) * r2.x, -lo2f(sb) * r2.y};
        f32x2 n2[8];
        u32 o[8];
        SB();
#pragma unroll
        for (int c = 0; c < 8; c++) n2[c] = f32x2{fmav(xq[i][c].x, r2.x, m2.x), fmav(xq[i][c].y, r2.y, m2.y)};
        SB();
#pragma unroll
        for (int c = 0; c < 8; c++) o[c] = packh(n2[c].x, n2[c].y);
        SB();
#pragma unroll
        for (int c = 0; c < 8; c++) asm volatile("ds_write_addtid_b32 %0 offset:%1" ::"v"(o[c]), "n"(c * XREG + 1024 * i) : "memory");
        asm volatile("ds_write_b32 %1, %0\n\tv_add_u32 %0, 1, %0" : "+v"(fv) : "v"(fa) : "memory");
        if (i == PDROP) __builtin_amdgcn_s_setprio(PPRIO);
        SB();
      }
    }
    STAMP(4);
    const rsrc_t ro = __builtin_amdgcn_make_buffer_rsrc((void*)out, 0, 2 * L * D, 0x00020000);
    u32 ys[8], y[2][8] = {};
    u32x2 ix;
    u32x4 cl;
    u32x8 pb;
    float yc, rsel;
    const u32 ixo = IXO((l & 6) + (n16 >> 3));
    if (p == 0) asm volatile("buffer_load_dwordx2 v[4:5], %1, %2, 0 offen sc1" : "=&{v[4:5]}"(ix) : "v"(ixo), "s"(rs) : "memory");
    const int rc = 4 * (l & 1) + kg, u = 64 * (p & 1) + l;
    u32x4 pick;
    u32 po = O_PG + 128 * g + 16 * (l & 7) + 16384 * (l >> 3), p3 = l < 32 ? po + 3 * 131072 : 0x80000000u, yf = lds0 + OYP + 4 * min(n16, 12) + 64 * (p >> 1);
    u32 ya = lds0 + OY + CHR * rc, oo = 16 * g + 2 * rc, clo = O_CLR + 16 * min(l, 62);
    u32 yp = lds0 + OY + 512 * (p >> 1) + 4 * (u ^ ((u >> 4) & 6)), yq = yp + 4 * CHR, bp = lds0 + OMISC + 16 * p + 4 * kg, bs = 30 - 2 * n16;
#pragma unroll
    for (int q = 0; q < 4; q++) pick[q] = (n16 >> 2) == q ? 0x3f80u << 16 * (l & 1) : 0u;
    asm volatile("" : "+v"(pick), "+v"(ya), "+v"(oo), "+v"(po), "+v"(p3), "+v"(yf), "+v"(clo), "+v"(yp), "+v"(yq), "+v"(bp), "+v"(bs));
#pragma unroll
    for (int c = 0; c < 8; c++) asm volatile("" : "+v"(y[0][c]), "+v"(y[1][c]));
    if (TE) asm volatile(UNTIL(STR(TE)) :: [st] "s"(st0) : "s8", "s9", "scc");
#pragma unroll
    for (int i = 0; i < 4; i++) {
      if (i == IXI && p == 0) {
        u32 n_ = SPIN;
        asm volatile("s_waitcnt vmcnt(%5)\n1:\n\tv_cmp_ne_u32 vcc, %4, v5\n\ts_sub_u32 %1, %1, 1\n\ts_cbranch_vccz 2f\n\ts_cbranch_scc1 2f\n\t"
                     "buffer_load_dwordx2 v[4:5], %2, %3, 0 offen sc1\n\ts_waitcnt vmcnt(0)\n\ts_branch 1b\n2:"
                     : "+{v[4:5]}"(ix), "+s"(n_) : "v"(ixo), "s"(rs), "s"(tag), "n"(2 * IXI + 1) : "vcc", "scc", "memory");
        const u32 prow = ix.x & 2047u, v = prow >> 1;
        if (l < 16) atomicOr((u32*)(smem + OMISC) + (prow >> 5), 1u << (ix.x & 31u));
        FLAG(OYP + 256 + 32 + 64 * (l & 3), 8u);
        oo += prow * (2 * D);
        asm volatile("v_mov_b32 v162, 0\n\tglobal_load_short_d16_hi v162, %1, %2" : "+{v[160:163]}"(junk) : "v"(oo), "s"(x) : "memory");
        yc = *(const float*)(smem + OSC + 4 * rc); rsel = rbt(sigm(hi2f(ix.x)));
        ya += 4 * (v ^ ((v >> 4) & 6)) + 2 * (prow & 1);
        asm volatile("" : "+v"(yc), "+v"(rsel), "+v"(ya));
        STAMP(11);
      }
      if (i == 2 && p == 0) cl = ld16(rs, clo);
      if (i == 3) {
        asm volatile("s_waitcnt vmcnt(2)" ::: "memory");
        FLAG(OYP + 448 + 36 + 4 * p, 8u);
        if (p == 0) {
#pragma unroll 1
          for (int spin = 0; spin < SPIN; spin++) {
            if (__all(cl.y == tag && cl.w == tag)) break;
            cl = ld16(rs, clo);
          }
          STAMP(12);
        }
        __builtin_amdgcn_s_setprio(LPRIO);
      }
      {
        u32 n_ = SPIN, f_;
        asm volatile("1:\n\tds_read_b32 %0, %2 offset:%3\n\ts_sub_u32 %1, %1, 1\n\ts_waitcnt lgkmcnt(0)\n\tv_cmp_eq_u32 vcc, 0, %0\n\ts_cbranch_scc1 2f\n\ts_and_b32 vcc_lo, vcc_lo, %4\n\t"
                     "s_cbranch_scc0 2f\n\t" YNAP "\n\ts_branch 1b\n2:" : "=&v"(f_), "+s"(n_) : "v"(yf), "n"(128 * i), "n"(i < 2 ? 255 : 511) : "vcc", "scc", "memory");
      }
      if (i == 0) STAMP(5); else if (i == 1) STAMP(6); else if (i == 2) STAMP(7); else STAMP(8);
      u32 bm = 0;
      if (i >= 2) asm volatile("ds_read_b32 %0, %1 offset:%2" : "=v"(bm) : "v"(bp), "n"(64 * i) : "memory");
#pragma unroll
      for (int r = 0; r < 2; r++)
#pragma unroll
        for (int c = 0; c < 8; c++) asm volatile("ds_read_u16_d16_hi %0, %1 offset:%2" : "+v"(y[r][c]) : "v"(c < 4 ? yp : yq), "n"((c & 3) * CHR + 1024 * i + 2 * r) : "memory");
      if (i == 3 && p == 0) {
#define PLD(X) X(132, 133, 134, 135, "%[po]", "0") X(136, 137, 138, 139, "%[po]", "%[k1]") X(140, 141, 142, 143, "%[po]", "%[k2]") X(144, 145, 146, 147, "%[p3]", "0")
#define PL0(a, b_, c, d, o, s_) "buffer_load_dwordx4 v[" #a ":" #d "], " o ", %[rs], " s_ " offen sc1\n\t"
        asm volatile(PLD(PL0) : "=&{v[132:139]}"(pa), "=&{v[140:147]}"(pb) : [po] "v"(po), [p3] "v"(p3), [rs] "s"(rs), [k1] "s"(131072), [k2] "s"(262144) : "memory");
      }
      if (i == 0) {
        st8(rs, t == 512 ? O_ARR + b * 8 : 0x80000000u, u32x2{0u, tag});
        const u32x4 a = *(const u32x4*)(smem + OSC), c = *(const u32x4*)(smem + OSC + 16);
#pragma unroll
        for (int k = 0; k < 4; k++) { ys[k] = (u32)__builtin_amdgcn_readfirstlane((int)a[k]); ys[4 + k] = (u32)__builtin_amdgcn_readfirstlane((int)c[k]); }
      }
      SB();
#pragma unroll
      for (int r = 0; r < 2; r++) {
        asm volatile("s_waitcnt lgkmcnt(%9)" : "+v"(y[r][0]), "+v"(y[r][1]), "+v"(y[r][2]), "+v"(y[r][3]), "+v"(y[r][4]), "+v"(y[r][5]), "+v"(y[r][6]), "+v"(y[r][7]), "+v"(bm) : "n"(8 - 8 * r));
        u32x4 v;
#pragma unroll
        for (int k = 0; k < 4; k++)
          v[k] = pack2(fmas(__uint_as_float(y[r][2 * k]), ys[2 * k], xq[i][2 * k][r]), fmas(__uint_as_float(y[r][2 * k + 1]), ys[2 * k + 1], xq[i][2 * k + 1][r]));
        if (i >= 2 && r == 0) bm <<= bs;
        __builtin_amdgcn_raw_buffer_store_b128(v, ro, i < 2 ? op : op | (bm & 0x40000000u << r), (512 * i + r) * (2 * D), OAUX);
        SB();
        asm volatile("s_nop 1");
        SB();
      }
    }
    asm volatile("" ::"{v[160:163]}"(junk));
    STAMP(13);
    if (p == 0) {
      u32 n_ = SPIN;
#define PL1(a, b_, c, d, o, s_) "v_max3_u32 v150, v" #a ", v" #b_ ", v" #c "\n\tv_max_u32 v150, v150, v" #d "\n\tv_cmpx_eq_u32 vcc, -1, v150\n\t" PL0(a, b_, c, d, o, s_) "s_mov_b64 exec, -1\n\t"
#define PLE(X) X(112, 113, 114, 115, "%[po]", "0") X(116, 117, 118, 119, "%[po]", "%[k1]") X(120, 121, 122, 123, "%[po]", "%[k2]") X(124, 125, 126, 127, "%[p3]", "0")
#define PMX(a, b_) "v_max3_u32 v150, v150, v" a ", v" b_ "\n\t"
#define PMM(d, a, c) "v_mfma_f32_16x16x32_bf16 v[" d "], %[pk], v[" a "], " c "\n\t"
#define PCK(c, p, q) "s_waitcnt vmcnt(" #c ")\n\t" PMM("152:155", p "2:" p "5", "0") "v_max_u32 v150, v" p "2, v" p "3\n\t" PMX(p "4", p "5") PMM("156:159", p "6:" p "9", "0") PMX(p "6", p "7") PMX(p "8", p "9") \
      PMM("152:155", q "0:" q "3", "v[152:155]") PMX(q "0", q "1") PMX(q "2", q "3") PMM("156:159", q "4:" q "7", "v[156:159]") PMX(q "4", q "5") PMX(q "6", q "7") \
      "v_cmp_eq_u32 vcc, -1, v150\n\ts_sub_u32 %[n], %[n], 1\n\t"
#define PCP(a, b_, c, d) "v_mov_b64 v[" #a ":" #b_ "], v[" #c ":" #d "]\n\t"
      asm volatile(PCK(2, "13", "14") "s_cbranch_vccnz 9f\n"
                   "2:\n\t" TS(60, 61) "ds_read_b32 v150, %[yf] offset:448\n\tds_read_u16_d16_hi v151, %[ya]\n\ts_mov_b32 %[n], %[spin]\n\ts_nop 1\n\tv_add_f32 v152, v152, v156\n\tv_add_f32 v153, v153, v157\n\ts_nop 0\n\t"
                   "v_add_f32_dpp v152, v152, v152 row_ror:8 row_mask:0xf bank_mask:0x3\n\tv_add_f32_dpp v152, v153, v153 row_ror:8 row_mask:0xf bank_mask:0xc\n\t"
                   "v_cvt_pk_bf16_f32 v152, 0, v152\n\tv_mul_f32 v152, v152, %[rl]\n\tv_cvt_pk_bf16_f32 v152, 0, v152\n\tv_add_f32 v153, v162, v152\n"
                   "3:\n\ts_waitcnt lgkmcnt(0)\n\tv_cmp_eq_u32 vcc, 0, v150\n\tv_and_b32 v152, 0xffff0000, v151\n\ts_sub_u32 %[n], %[n], 1\n\ts_cbranch_vccz 4f\n\ts_cbranch_scc1 4f\n\t"
                   "ds_read_b32 v150, %[yf] offset:448\n\tds_read_u16_d16_hi v151, %[ya]\n\ts_branch 3b\n"
                   "4:\n\tv_fmac_f32 v153, v152, %[yc]\n\tv_cvt_pk_bf16_f32 v153, v153, v153\n\tbuffer_store_short v153, %[oo], %[ro], 0 offen\n\t" DONE
                   "9:\n\t" PCP(112, 113, 132, 133) PCP(114, 115, 134, 135) PCP(116, 117, 136, 137) PCP(118, 119, 138, 139) PCP(120, 121, 140, 141) PCP(122, 123, 142, 143)
                   PCP(124, 125, 144, 145) PCP(126, 127, 146, 147) PLD(PL1) "s_sleep " STR(PNAP) "\n1:\n\t" PLE(PL1) PCK(4, "13", "14") "s_cbranch_vccz 2b\n\t" PLD(PL1) PCK(4, "11", "12")
                   "s_cbranch_vccz 2b\n\ts_cbranch_scc0 1b\n\ts_branch 2b\n0:"
                   : [n] "+s"(n_), "+{v[132:139]}"(pa), "+{v[140:147]}"(pb)
                   : [po] "v"(po), [p3] "v"(p3), [rs] "s"(rs), [k1] "s"(131072), [k2] "s"(262144), [pk] "v"(pick), [yf] "v"(yf), [ya] "v"(ya), "{v[160:163]}"(junk), [rl] "v"(rsel), [yc] "v"(yc),
                     [oo] "v"(oo), [ro] "s"(ro), [spin] "n"(SPIN)
                   : "vcc", "scc", "memory", "v112", "v113", "v114", "v115", "v116", "v117", "v118", "v119", "v120", "v121", "v122", "v123", "v124", "v125", "v126", "v127", "v150", "v151", "v152", "v153", "v154", "v155", "v156", "v157", "v158", "v159" TSD);
      TSGET(14, 2, 2, 2);
      STAMP(15);
    }
    FLUSH();
    __builtin_amdgcn_endpgm();
  }

  u32 pl, pa, pr, pi, ldtv;
  u32x4 tv, fx, junk;
  ROLE(c, "", , "={v[16:19]}"(tv), "={v[20:23]}"(fx), "={v24}"(ldtv), "={v96}"(pl), "={v97}"(pa), "={v68}"(pr), "={v69}"(pi), "={v[160:163]}"(junk));
  char* W = smem + w * CHR;
  __builtin_amdgcn_s_setprio(2);
  STAMP(3);
  const u32x4 zero4 = {0u, 0u, 0u, 0u};
#pragma unroll
  for (int i = 0; i < 4; i++) *(u32x4*)(W + (i < 3 ? TB + 1024 * i : CHR - 1024) + 16 * l) = zero4;
  SB();
  const u32 wb0 = lds0 + w * CHR, xb0 = lds0 + OX + w * XREG;
  asm volatile("s_mov_b32 m0, %0\n\ts_nop 0\n\tds_write_addtid_b32 %1\n\tds_write_addtid_b32 %1 offset:%2" ::"s"(wb0), "v"(0x3c00u), "n"(PB) : "memory");
  u32 ape = wb0 + (n16 == 12 ? 12 : (n16 ^ 3) - 7 * (n16 >> 3)) * RS + 16 * kg, apo = wb0 + (0x765412b0fedc2301ull >> 4 * n16 & 15) * RS + 16 * kg;
  u32x2 kb = {wb0 + ((15 - n16) & 3) * CPE + 2 * ((15 - n16) & 12) + 16 * kg, wb0 + (((n16 >> 2) & 1) + (n16 & 8) + 2 * (3 - (n16 & 3))) * RS + 16 * kg};
  u32x8 ka = {xb0 + 32 * n16 + 16 * kg, wb0 + OY + 32 * n16 + 8 * (kg ^ ((l >> 2) & 3)),
              lds0 + OXF + 4 * (l & 3), wb0 + TB + ((15 - n16) & 3) * CPO + 2 * ((15 - n16) & 12) + 8 * kg, lds0 + OB + 16 * l,
              l < 32 ? 0x01000100u : 0x03020100u, l < 32 ? 0x03020100u : 0x03020302u, (u32)(l >> 2) & 8u};
  u32 fad = lds0 + OYP + 4 * w, za = wb0 + 16 * kg;
  asm volatile("" : "+{v70}"(ape), "+{v71}"(apo), "+{v[28:35]}"(ka), "+{v[26:27]}"(kb), "+{v25}"(fad), "+{v12}"(za));
  STAMP(4);
  asm volatile("s_waitcnt vmcnt(4)" : "+{v24}"(ldtv), "+{v[160:163]}"(junk));
  float dt = rbt(fexp(hi2f(ldtv)));
  asm volatile("" : "+v"(dt));
  const f32x2 cdt = {rdl(dt * -1.4426950408889634f, 0), rdl(dt * 0.15915494309189535f, 0)};
  const u32 dk = (u32)__builtin_amdgcn_readfirstlane((int)junk.x) << 16, db = max(__float_as_uint(rdl(dt, 0)), 27u << 23);
  u32 em = max(dk >> 23 & 255u, 127u) + 119 - (db >> 23);
  asm("s_max_i32 %0, %0, 8\n\ts_min_u32 %0, %0, 0xfe" : "+s"(em) :: "scc");
  SB();
  STAMP(10);
  STAMPC(11);
  {
    u32 e, tmp, o0, o1, of, os;
    asm volatile(GEN_ASM
                 : [e] "=&s"(e), [t] "=&s"(tmp), "=&{v93}"(o0), "=&{v46}"(o1), "=&{v48}"(of), "=&{v8}"(os), "+{v25}"(fad), "+{v12}"(za), "+{v68}"(pr), "+{v69}"(pi), "+{v70}"(ape),
                   "+{v71}"(apo), "+{v96}"(pl), "+{v97}"(pa), "+{v[28:35]}"(ka), "+{v[26:27]}"(kb), "+{v[16:19]}"(tv), "+{v[20:23]}"(fx)
                 : [w] "s"(wb0), [c] "s"(cdt), [k] "s"(f32x2{1.4426950408889634f, 256.f}), [em] "s"(em)
                 : "memory", "scc", GEN_CLOBBER);
    STAMP(1);
    STAMPC(12);
    const float ys = __uint_as_float((e - 5) << 23) * __uint_as_float(db);
    asm volatile("v_rcp_f32 %1, %2\n\ts_nop 0\n\tv_fma_f32 %1, %3, %1, %1\n\tv_fma_mixhi_f16 %0, %1, 1.0, %0 op_sel:[0,0,1] op_sel_hi:[0,0,1]\n\ts_nop 0"
                 : "+v"(o0), "=&v"(dt) : "v"(ys), "s"(dk));
    const u32 k0 = (u32)__builtin_amdgcn_readlane((int)o0, 3), k256 = (u32)__builtin_amdgcn_readlane((int)o1, 60);
    const u32 k512 = (u32)__builtin_amdgcn_readlane((int)os, 28), k1024 = (u32)__builtin_amdgcn_readlane((int)of, 55);
    *(LDS u16*)(size_t)(wb0 + tv.z) = (u16)__builtin_amdgcn_perm((k512 & 0xffffu) | (k1024 << 16), (k0 >> 16) | (k256 << 16), tv.w);
    *(float*)(smem + OSC + 4 * w) = ys;
    fx &= ((k1024 & 0xffffu) | (k1024 << 16)) ^ 0x80008000u;
  }
  asm volatile(CONV_ASM : "+{v[28:35]}"(ka) : "{v[26:27]}"(kb), "{v25}"(fad), "{v[48:51]}"(fx), [w] "s"(w) : "vcc", "scc", "s6", "memory", CONV_CLOBBER TSC);
  TSGET(2, 5, 6, 7);
  STAMP(13);
  STAMPC(15);
  FLUSH();
  __builtin_amdgcn_endpgm();
}

extern "C" void kernel_launch(void* const* d_in, const int* in_sizes, int n_in, void* d_out, int out_size,
                              void* d_ws, size_t ws_size, hipStream_t stream) {
  const u16* const* in = (const u16* const*)d_in;
  fused<<<256, 768, 0, stream>>>(in[0], in[1], in[2], in[3], in[4], in[5], in[6], in[8], in[9], in[10], in[11],
                                 (u16*)d_out, (char*)d_ws);
}
```

```cpp
#include <hip/hip_runtime.h>
#include <stdint.h>

#define STR2(x) #x
#define STR(x) STR2(x)
#define LA 4
#define WL2 44
#define WL3 100
#define PH 1246
#define ABL 0
#define PPRIO 0
#define CPRIO 1
#define GDELAY 0
#define ROT 1
#define PRE 2
#define GNT 0
#define OAUX 0
#define LNT 1
#define PNT ""
#define LNNT ""
#define PRENT ""
#define PFN 15
#define PFO 512
#define DBAR 2
#define THR 1
#define QD 11
#define HNAP0 4
#define HNAP 4
#define KR 3
#define KI 2
#define QE 11
#define QA 5
#define NA 0
#define CH 0
#define G0E 0
#define DDL 0
#define WBM 0
#define WBW 1
#define HQ 3
#define RSL 5
#define DPF 1
#define HELP 0
#define WARM 0
#define HPI 0
#define Q0 6
#define NP 10
#define HW 28
#define NE 0
#define YNAP "s_sleep 2"
#define QS 7
#define QAS 6
#define RAMP 4
#define GNAP "s_sleep 1"
#define HOLD 8
#define DPRIO 3
#define HAUX 16
#define DPO 128
#define TH 590
#define CLD 245
#define IXI 1
#define LPRIO 0
#define DPI 2
#define PDROP 3
#define TE 0
#define CNAP 2
#define XI 1
#define SCA " sc1"
#define SCB " sc1"
#define SCC " sc1"
#define SCT " sc1"
#define SCU " sc1"
#define SCH " sc1"
#define SCR " sc1"
#define TSG 280
#define SSL 5
#define PNAP 4
#define TW 310
#define TL 4352
#define CODE_KB 24
typedef unsigned short u16;
typedef unsigned int u32;
typedef __attribute__((ext_vector_type(8))) __bf16 bf16x8;
typedef __attribute__((ext_vector_type(2))) __bf16 bf16x2;
typedef __attribute__((ext_vector_type(8))) _Float16 f16x8;
typedef __attribute__((ext_vector_type(2))) _Float16 f16x2;
typedef __attribute__((ext_vector_type(4))) short v4i16;
typedef __attribute__((ext_vector_type(4))) float f32x4;
typedef __attribute__((ext_vector_type(2))) float f32x2;
typedef __attribute__((ext_vector_type(4))) u32 u32x4;
typedef __attribute__((ext_vector_type(2))) u32 u32x2;
typedef __attribute__((ext_vector_type(8))) u32 u32x8;
typedef __attribute__((ext_vector_type(2), aligned(4))) u32 u32x2_a4;
typedef __amdgpu_buffer_rsrc_t rsrc_t;
#define LDS __attribute__((address_space(3)))

#define TLARG
#define TLVAL
#define STAMP(I)
#define STAMPC(I)
#define FLUSH()

constexpr int L = 2048, D = 1024, NM = 128, F = 3584;
constexpr u32 O_ARR = 0x1000, O_CLR = 0x2000, O_RG = 0x4000, O_SG = 0x8000, O_IDX = 0x10000, O_HG = 0x20000,
              O_PG = 0xd0000, O_TOP = 0x140000, TOPS = 8 * TL, WS_END = O_TOP + 2 * TOPS;
constexpr int SPIN = 1 << 16;

__device__ __forceinline__ float lo2f(u32 w) { return __uint_as_float(w << 16); }
__device__ __forceinline__ float hi2f(u32 w) { return __uint_as_float(w & 0xffff0000u); }
__device__ __forceinline__ u32 pack2(float a, float b) {
  bf16x2 r = {(__bf16)a, (__bf16)b};
  return __builtin_bit_cast(u32, r);
}
__device__ __forceinline__ u32 packh(float a, float b) {
  f16x2 r = {(_Float16)a, (_Float16)b};
  return __builtin_bit_cast(u32, r);
}
__device__ __forceinline__ float rbt(float f) { return __uint_as_float(pack2(0.f, f)); }
__device__ __forceinline__ float rb(float f) {
  float r;
  asm("v_cvt_pk_bf16_f32 %0, 0, %1" : "=v"(r) : "v"(f));
  return r;
}
__device__ __forceinline__ u32 hadd(u32 a, u32 b) {
  return __builtin_bit_cast(u32, __builtin_bit_cast(f16x2, a) + __builtin_bit_cast(f16x2, b));
}
__device__ __forceinline__ float hlo(u32 a) { return (float)__builtin_bit_cast(f16x2, a).x; }
__device__ __forceinline__ float hhi(u32 a) { return (float)__builtin_bit_cast(f16x2, a).y; }

template <int CTRL, int RM>
__device__ __forceinline__ float dppf(float v) {
  return __builtin_bit_cast(float, __builtin_amdgcn_update_dpp(0, __builtin_bit_cast(int, v), CTRL, RM, 0xf, true));
}
template <int CTRL, int RM>
__device__ __forceinline__ u32 dppu(u32 v) {
  return (u32)__builtin_amdgcn_update_dpp(0, (int)v, CTRL, RM, 0xf, true);
}
__device__ __forceinline__ float rdl(float v, int i) {
  return __builtin_bit_cast(float, __builtin_amdgcn_readlane(__builtin_bit_cast(int, v), i));
}
__device__ __forceinline__ u32 wmaxu(u32 v) {
  v = max(v, dppu<0x111, 0xf>(v)); v = max(v, dppu<0x112, 0xf>(v)); v = max(v, dppu<0x114, 0xf>(v));
  v = max(v, dppu<0x118, 0xf>(v)); v = max(v, dppu<0x142, 0xa>(v)); v = max(v, dppu<0x143, 0xc>(v));
  return (u32)__builtin_amdgcn_readlane((int)v, 63);
}
__device__ __forceinline__ void swap32(float& a, float& b) {
  auto r = __builtin_amdgcn_permlane32_swap(__float_as_uint(a), __float_as_uint(b), false, false);
  a = __uint_as_float(r[0]); b = __uint_as_float(r[1]);
}
__device__ __forceinline__ void swap16(float& a, float& b) {
  auto r = __builtin_amdgcn_permlane16_swap(__float_as_uint(a), __float_as_uint(b), false, false);
  a = __uint_as_float(r[0]); b = __uint_as_float(r[1]);
}
__device__ __forceinline__ u32x2 trread(const char* p) {
  v4i16 r = __builtin_amdgcn_ds_read_tr16_b64_v4i16((LDS v4i16*)p);
  return __builtin_bit_cast(u32x2, r);
}
__device__ __forceinline__ u32x2 trl(LDS char* p) { return __builtin_bit_cast(u32x2, __builtin_amdgcn_ds_read_tr16_b64_v4i16((LDS v4i16*)p)); }
__device__ __forceinline__ f16x8 mkh(u32x2 a, u32x2 b) {
  u32x4 v = {a.x, a.y, b.x, b.y};
  return __builtin_bit_cast(f16x8, v);
}
__device__ __forceinline__ bf16x8 mkb(u32x2 a, u32x2 b) {
  u32x4 v = {a.x, a.y, b.x, b.y};
  return __builtin_bit_cast(bf16x8, v);
}
__device__ __forceinline__ bf16x8 asb(u32x4 v) { return __builtin_bit_cast(bf16x8, v); }
__device__ __forceinline__ f16x8 ash(u32x4 v) { return __builtin_bit_cast(f16x8, v); }
#define MFMAB(a, b, c) __builtin_amdgcn_mfma_f32_16x16x32_bf16(a, b, c, 0, 0, 0)
#define MFMAH(a, b, c) __builtin_amdgcn_mfma_f32_16x16x32_f16(a, b, c, 0, 0, 0)

__device__ __forceinline__ u32x4 ld16(rsrc_t r, u32 off) { return __builtin_amdgcn_raw_buffer_load_b128(r, off, 0, 16); }
__device__ __forceinline__ u32x2 ld8(rsrc_t r, u32 off) { return __builtin_amdgcn_raw_buffer_load_b64(r, off, 0, 16); }
__device__ __forceinline__ void st16(rsrc_t r, u32 off, u32x4 v) { __builtin_amdgcn_raw_buffer_store_b128(v, r, off, 0, 16); }
__device__ __forceinline__ void st8(rsrc_t r, u32 off, u32x2 v) { __builtin_amdgcn_raw_buffer_store_b64(v, r, off, 0, 16); }
#define NAP() asm volatile("s_sleep 1" ::: "memory")
#define SB() __builtin_amdgcn_sched_barrier(0)
#define LBAR() asm volatile("s_waitcnt lgkmcnt(0)\n\ts_barrier" ::: "memory")

__device__ __forceinline__ float fexp(float v) { return __builtin_amdgcn_exp2f(v * 1.4426950408889634f); }
__device__ __forceinline__ float dot2b(u32 a, u32 b, float c) {
  return __builtin_amdgcn_fdot2_f32_bf16(__builtin_bit_cast(bf16x2, a), __builtin_bit_cast(bf16x2, b), c, false);
}
__device__ __forceinline__ float sigm(float v) { return __builtin_amdgcn_rcpf(1.f + __builtin_amdgcn_exp2f(-1.4426950408889634f * v)); }

constexpr int RS = 288, PB = 16 * RS;
constexpr int CPE = 1216, TB = 2 * PB, CPO = 832;
constexpr int T2B = 4, T3B = 224, C2 = 52, C3 = 84;
constexpr int OY = 0, CHR = TB + 4 * CPO;
constexpr int OX = 8 * CHR, XREG = 4640, XO = 266;
constexpr int OMISC = OX + 8 * XREG;
constexpr int OXF = OMISC + 320, OSC = OMISC + 352, OYP = OMISC + 448;
constexpr int OB = OMISC + 1024, SMEM = 148480;
constexpr int FA = 131072, FH = FA + 8192, HP = 576, FC = FH + 8 * HP;
static_assert(FC + 1024 <= SMEM && OB + 4096 <= SMEM, "lds map");

struct Tab { u32 v[6][64][4]; };
constexpr Tab mktab() {
  Tab T = {};
  for (int l = 0; l < 64; l++) {
    const int n16 = l & 15, kg = l >> 4, pc = ((n16 >> 2) & 1) + (n16 & 8) + 2 * (3 - (n16 & 3)), odd = kg >> 1, s = l & 3;
    const int e0 = 534 - 258 - 128 - 8 * pc - 4 * (kg & 1) - odd;
    const int ez = ((l >> 3) & 3) * CPE + ((l & 4) ? 1056 - 64 : 0) + 16 * (l & 7);
    const int b2 = T2B + 44 + C2 * ((n16 & 1) + 2 * (kg & 1)) - 8 * (n16 & 2), b3 = T3B + 60 + C3 * (kg & 1) - 16 * s;
    const int to = TB + 2 * ((n16 < 8 ? b2 : b3) - 4 * odd - 8) + ((l & 4) ? 0 : 2 * CPO - 4);
    const int q = (l >> 2) & 7, tap = q == 0 ? ((s & 1) ? 0 : 256) : q == 1 ? 257 : q == 7 ? 0 : 509 + q;
    const int sp = q != 6 ? s * (CPE - 2) + 2 * (537 - tap) : TB + s * (CPO - 2) + 2 * (T2B + 15);
    T.v[0][l][0] = (2 * e0 + (2 - odd) * (CPE - 2)) | (2 * e0 + 3 * odd * (CPE - 2)) << 16;
    T.v[0][l][1] = ez | to << 16;
    T.v[0][l][2] = sp;
    T.v[0][l][3] = !tap ? 0x0100u : q == 6 ? 0x0706u : q >= 2 ? 0x0504u : 0x0302u;
    for (int k = 0; k < 8; k++) {
      const int d = 8 * kg + k - n16;
      if (d == 13) T.v[1][l][k >> 1] |= 0xffffu << 16 * (k & 1);
      for (int h = 1; h <= 4; h++)
        if (d >= (h == 1 ? 14 : 10) - h && d <= (h == 1 ? 14 : 10) + h) T.v[1 + h][l][k >> 1] |= 0x3c00u << 16 * (k & 1);
    }
  }
  return T;
}
extern "C" __device__ const __attribute__((aligned(128), used, visibility("hidden"))) Tab lane_tab = mktab();

#define GEN_ASM \
  "s_waitcnt vmcnt(2)\n\tv_lshlrev_b32 v56, 16, v96\n\t" \
  "v_and_b32 v57, 0xffff0000, v96\n\tv_pk_mul_f32 v[56:57], %[k], v[56:57] op_sel_hi:[0,1]\n\t" \
  "v_lshlrev_b32 v49, 16, v97\n\tv_exp_f32 v56, v56\n\t" \
  "v_exp_f32 v57, v57\n\tv_and_b32 v51, 0xffff0000, v97\n\t" \
  "v_cvt_pk_bf16_f32 v56, v56, v57\n\tv_lshlrev_b32 v48, 16, v56\n\t" \
  "v_and_b32 v50, 0xffff0000, v56\n\tv_pk_mul_f32 v[48:49], %[c], v[48:49]\n\t" \
  "v_pk_mul_f32 v[50:51], %[c], v[50:51]\n\tv_pk_mul_f32 v[98:99], %[k], v[48:49] op_sel:[1,0]\n\t" \
  "v_pk_mul_f32 v[100:101], %[k], v[50:51] op_sel:[1,0]\n\tv_fract_f32 v49, v49\n\t" \
  "v_fract_f32 v51, v51\n\tv_exp_f32 v48, v48\n\t" \
  "v_exp_f32 v50, v50\n\tv_cos_f32 v40, v49\n\t" \
  "v_sin_f32 v41, v49\n\tv_cos_f32 v42, v51\n\t" \
  "v_sin_f32 v43, v51\n\tv_pk_mul_f32 v[40:41], v[40:41], v[48:49] op_sel_hi:[1,0]\n\t" \
  "v_pk_mul_f32 v[42:43], v[42:43], v[50:51] op_sel_hi:[1,0]\n\tv_pk_mul_f32 v[48:49], v[40:41], v[40:41] op_sel:[1,1] op_sel_hi:[1,0] neg_lo:[1,0]\n\t" \
  "v_pk_mul_f32 v[50:51], v[42:43], v[42:43] op_sel:[1,1] op_sel_hi:[1,0] neg_lo:[1,0]\n\tv_pk_fma_f32 v[48:49], v[40:41], v[40:41], v[48:49] op_sel:[0,0,0] op_sel_hi:[0,1,1]\n\t" \
  "v_pk_fma_f32 v[50:51], v[42:43], v[42:43], v[50:51] op_sel:[0,0,0] op_sel_hi:[0,1,1]\n\tv_cvt_pk_f16_f32 v72, v40, -v41\n\t" \
  "v_cvt_pk_f16_f32 v73, v42, -v43\n\tv_pk_mul_f32 v[52:53], v[48:49], v[40:41] op_sel:[1,1] op_sel_hi:[1,0] neg_lo:[1,0]\n\t" \
  "v_pk_mul_f32 v[54:55], v[50:51], v[42:43] op_sel:[1,1] op_sel_hi:[1,0] neg_lo:[1,0]\n\tv_pk_fma_f32 v[52:53], v[48:49], v[40:41], v[52:53] op_sel:[0,0,0] op_sel_hi:[0,1,1]\n\t" \
  "v_pk_fma_f32 v[54:55], v[50:51], v[42:43], v[54:55] op_sel:[0,0,0] op_sel_hi:[0,1,1]\n\tv_pk_mul_f32 v[56:57], v[48:49], v[48:49] op_sel:[1,1] op_sel_hi:[1,0] neg_lo:[1,0]\n\t" \
  "v_pk_mul_f32 v[58:59], v[50:51], v[50:51] op_sel:[1,1] op_sel_hi:[1,0] neg_lo:[1,0]\n\tv_pk_fma_f32 v[56:57], v[48:49], v[48:49], v[56:57] op_sel:[0,0,0] op_sel_hi:[0,1,1]\n\t" \
  "v_pk_fma_f32 v[58:59], v[50:51], v[50:51], v[58:59] op_sel:[0,0,0] op_sel_hi:[0,1,1]\n\tv_cvt_pk_f16_f32 v74, v48, -v49\n\t" \
  "v_cvt_pk_f16_f32 v75, v50, -v51\n\tv_cvt_pk_f16_f32 v76, v52, -v53\n\t" \
  "v_cvt_pk_f16_f32 v77, v54, -v55\n\tv_pk_mul_f32 v[52:53], v[56:57], v[48:49] op_sel:[1,1] op_sel_hi:[1,0] neg_lo:[1,0]\n\t" \
  "v_pk_mul_f32 v[54:55], v[58:59], v[50:51] op_sel:[1,1] op_sel_hi:[1,0] neg_lo:[1,0]\n\tv_pk_fma_f32 v[52:53], v[56:57], v[48:49], v[52:53] op_sel:[0,0,0] op_sel_hi:[0,1,1]\n\t" \
  "v_pk_fma_f32 v[54:55], v[58:59], v[50:51], v[54:55] op_sel:[0,0,0] op_sel_hi:[0,1,1]\n\tv_pk_mul_f32 v[60:61], v[56:57], v[56:57] op_sel:[1,1] op_sel_hi:[1,0] neg_lo:[1,0]\n\t" \
  "v_pk_mul_f32 v[62:63], v[58:59], v[58:59] op_sel:[1,1] op_sel_hi:[1,0] neg_lo:[1,0]\n\tv_pk_fma_f32 v[60:61], v[56:57], v[56:57], v[60:61] op_sel:[0,0,0] op_sel_hi:[0,1,1]\n\t" \
  "v_pk_fma_f32 v[62:63], v[58:59], v[58:59], v[62:63] op_sel:[0,0,0] op_sel_hi:[0,1,1]\n\tv_cvt_pk_f16_f32 v78, v56, -v57\n\t" \
  "v_cvt_pk_f16_f32 v79, v58, -v59\n\tv_cvt_pk_f16_f32 v82, v52, -v53\n\t" \
  "v_cvt_pk_f16_f32 v83, v54, -v55\n\tv_pk_mul_f16 v80, v78, v72 op_sel:[1,1] op_sel_hi:[1,0] neg_lo:[1,0]\n\t" \
  "v_pk_mul_f16 v81, v79, v73 op_sel:[1,1] op_sel_hi:[1,0] neg_lo:[1,0]\n\tv_pk_fma_f16 v80, v78, v72, v80 op_sel:[0,0,0] op_sel_hi:[0,1,1]\n\t" \
  "v_pk_fma_f16 v81, v79, v73, v81 op_sel:[0,0,0] op_sel_hi:[0,1,1]\n\tv_pk_mul_f16 v84, v78, v76 op_sel:[1,1] op_sel_hi:[1,0] neg_lo:[1,0]\n\t" \
  "v_pk_mul_f16 v85, v79, v77 op_sel:[1,1] op_sel_hi:[1,0] neg_lo:[1,0]\n\tv_pk_fma_f16 v84, v78, v76, v84 op_sel:[0,0,0] op_sel_hi:[0,1,1]\n\t" \
  "v_pk_fma_f16 v85, v79, v77, v85 op_sel:[0,0,0] op_sel_hi:[0,1,1]\n\tv_cvt_pk_f16_f32 v86, v60, -v61\n\t" \
  "v_cvt_pk_f16_f32 v87, v62, -v63\n\tds_write_addtid_b32 v72 offset:288\n\t" \
  "ds_write_addtid_b32 v73 offset:4896\n\tds_write_addtid_b32 v74 offset:576\n\t" \
  "ds_write_addtid_b32 v75 offset:5184\n\tds_write_addtid_b32 v76 offset:864\n\t" \
  "ds_write_addtid_b32 v77 offset:5472\n\tds_write_addtid_b32 v78 offset:1152\n\t" \
  "ds_write_addtid_b32 v79 offset:5760\n\tds_write_addtid_b32 v80 offset:1440\n\t" \
  "ds_write_addtid_b32 v81 offset:6048\n\tds_write_addtid_b32 v82 offset:1728\n\t" \
  "ds_write_addtid_b32 v83 offset:6336\n\tds_write_addtid_b32 v84 offset:2016\n\t" \
  "ds_write_addtid_b32 v85 offset:6624\n\tds_write_addtid_b32 v86 offset:3456\n\t" \
  "ds_write_addtid_b32 v87 offset:8064\n\tds_read_b128 v[104:107], v70 offset:0\n\t" \
  "ds_read_b128 v[108:111], v70 offset:64\n\tds_read_b128 v[112:115], v70 offset:128\n\t" \
  "ds_read_b128 v[116:119], v70 offset:192\n\tds_read_b128 v[120:123], v70 offset:4608\n\t" \
  "ds_read_b128 v[124:127], v70 offset:4672\n\tds_read_b128 v[128:131], v70 offset:4736\n\t" \
  "ds_read_b128 v[132:135], v70 offset:4800\n\tv_fract_f32 v99, v99\n\t" \
  "v_fract_f32 v101, v101\n\tv_exp_f32 v98, v98\n\t" \
  "v_exp_f32 v100, v100\n\tv_cos_f32 v44, v99\n\t" \
  "v_sin_f32 v45, v99\n\tv_cos_f32 v46, v101\n\t" \
  "v_sin_f32 v47, v101\n\tv_pk_mul_f32 v[44:45], v[44:45], v[98:99] op_sel_hi:[1,0]\n\t" \
  "v_pk_mul_f32 v[46:47], v[46:47], v[100:101] op_sel_hi:[1,0]\n\ts_waitcnt vmcnt(0)\n\t" \
  "v_pk_mul_f32 v[48:49], v[44:45], v[44:45] op_sel:[1,1] op_sel_hi:[1,0] neg_lo:[1,0]\n\tv_pk_mul_f32 v[50:51], v[46:47], v[46:47] op_sel:[1,1] op_sel_hi:[1,0] neg_lo:[1,0]\n\t" \
  "v_pk_fma_f32 v[48:49], v[44:45], v[44:45], v[48:49] op_sel:[0,0,0] op_sel_hi:[0,1,1]\n\tv_pk_fma_f32 v[50:51], v[46:47], v[46:47], v[50:51] op_sel:[0,0,0] op_sel_hi:[0,1,1]\n\t" \
  "v_and_b32 v102, 0x7fff7fff, v68\n\tv_cvt_pk_f16_f32 v88, v44, -v45\n\t" \
  "v_cvt_pk_f16_f32 v89, v46, -v47\n\tv_pk_mul_f32 v[52:53], v[48:49], v[44:45] op_sel:[1,1] op_sel_hi:[1,0] neg_lo:[1,0]\n\t" \
  "v_pk_mul_f32 v[54:55], v[50:51], v[46:47] op_sel:[1,1] op_sel_hi:[1,0] neg_lo:[1,0]\n\tv_and_b32 v103, 0x7fff7fff, v69\n\t" \
  "v_pk_fma_f32 v[52:53], v[48:49], v[44:45], v[52:53] op_sel:[0,0,0] op_sel_hi:[0,1,1]\n\tv_pk_fma_f32 v[54:55], v[50:51], v[46:47], v[54:55] op_sel:[0,0,0] op_sel_hi:[0,1,1]\n\t" \
  "v_pk_mul_f32 v[56:57], v[48:49], v[48:49] op_sel:[1,1] op_sel_hi:[1,0] neg_lo:[1,0]\n\tv_pk_mul_f32 v[58:59], v[50:51], v[50:51] op_sel:[1,1] op_sel_hi:[1,0] neg_lo:[1,0]\n\t" \
  "v_pk_max_u16 v102, v102, v103\n\tv_pk_fma_f32 v[56:57], v[48:49], v[48:49], v[56:57] op_sel:[0,0,0] op_sel_hi:[0,1,1]\n\t" \
  "v_pk_fma_f32 v[58:59], v[50:51], v[50:51], v[58:59] op_sel:[0,0,0] op_sel_hi:[0,1,1]\n\tv_cvt_pk_f16_f32 v90, v48, -v49\n\t" \
  "v_cvt_pk_f16_f32 v91, v50, -v51\n\tv_pk_max_u16 v102, v102, v102 op_sel:[1,0] op_sel_hi:[0,1]\n\t" \
  "v_pk_mul_f32 v[64:65], v[56:57], v[60:61] op_sel:[1,1] op_sel_hi:[1,0] neg_lo:[1,0]\n\tv_pk_mul_f32 v[66:67], v[58:59], v[62:63] op_sel:[1,1] op_sel_hi:[1,0] neg_lo:[1,0]\n\t" \
  "v_pk_fma_f32 v[64:65], v[56:57], v[60:61], v[64:65] op_sel:[0,0,0] op_sel_hi:[0,1,1]\n\tv_pk_fma_f32 v[66:67], v[58:59], v[62:63], v[66:67] op_sel:[0,0,0] op_sel_hi:[0,1,1]\n\t" \
  "v_max_u32_dpp v102, v102, v102 row_shr:1 row_mask:0xf bank_mask:0xf\n\tv_cvt_pk_f16_f32 v92, v52, -v53\n\t" \
  "v_cvt_pk_f16_f32 v93, v54, -v55\n\tv_pk_mul_f16 v136, v88, v74 op_sel:[1,1] op_sel_hi:[1,0] neg_lo:[1,0]\n\t" \
  "v_pk_mul_f16 v137, v89, v75 op_sel:[1,1] op_sel_hi:[1,0] neg_lo:[1,0]\n\tv_max_u32_dpp v102, v102, v102 row_shr:2 row_mask:0xf bank_mask:0xf\n\t" \
  "v_pk_fma_f16 v136, v88, v74, v136 op_sel:[0,0,0] op_sel_hi:[0,1,1]\n\tv_pk_fma_f16 v137, v89, v75, v137 op_sel:[0,0,0] op_sel_hi:[0,1,1]\n\t" \
  "v_pk_mul_f16 v138, v88, v78 op_sel:[1,1] op_sel_hi:[1,0] neg_lo:[1,0]\n\tv_pk_mul_f16 v139, v89, v79 op_sel:[1,1] op_sel_hi:[1,0] neg_lo:[1,0]\n\t" \
  "v_max_u32_dpp v102, v102, v102 row_shr:4 row_mask:0xf bank_mask:0xf\n\tv_pk_fma_f16 v138, v88, v78, v138 op_sel:[0,0,0] op_sel_hi:[0,1,1]\n\t" \
  "v_pk_fma_f16 v139, v89, v79, v139 op_sel:[0,0,0] op_sel_hi:[0,1,1]\n\tv_cvt_pk_f16_f32 v94, v64, -v65\n\t" \
  "v_cvt_pk_f16_f32 v95, v66, -v67\n\tv_max_u32_dpp v102, v102, v102 row_shr:8 row_mask:0xf bank_mask:0xf\n\t" \
  "v_pk_mul_f16 v140, v88, v82 op_sel:[1,1] op_sel_hi:[1,0] neg_lo:[1,0]\n\tv_pk_mul_f16 v141, v89, v83 op_sel:[1,1] op_sel_hi:[1,0] neg_lo:[1,0]\n\t" \
  "v_pk_fma_f16 v140, v88, v82, v140 op_sel:[0,0,0] op_sel_hi:[0,1,1]\n\tv_pk_fma_f16 v141, v89, v83, v141 op_sel:[0,0,0] op_sel_hi:[0,1,1]\n\t" \
  "ds_write_addtid_b32 v88 offset:3168\n\tds_write_addtid_b32 v89 offset:7776\n\t" \
  "ds_write_addtid_b32 v136 offset:0\n\tds_write_addtid_b32 v137 offset:4608\n\t" \
  "ds_write_addtid_b32 v138 offset:288\n\tds_write_addtid_b32 v139 offset:4896\n\t" \
  "ds_write_addtid_b32 v94 offset:1152\n\tds_write_addtid_b32 v95 offset:5760\n\t" \
  "ds_write_addtid_b32 v140 offset:576\n\tds_write_addtid_b32 v141 offset:5184\n\t" \
  "v_max_u32_dpp v102, v102, v102 row_bcast:15 row_mask:0xa bank_mask:0xf\n\tv_pk_mul_f16 v142, v88, v86 op_sel:[1,1] op_sel_hi:[1,0] neg_lo:[1,0]\n\t" \
  "v_pk_mul_f16 v143, v89, v87 op_sel:[1,1] op_sel_hi:[1,0] neg_lo:[1,0]\n\tv_pk_fma_f16 v142, v88, v86, v142 op_sel:[0,0,0] op_sel_hi:[0,1,1]\n\t" \
  "v_pk_fma_f16 v143, v89, v87, v143 op_sel:[0,0,0] op_sel_hi:[0,1,1]\n\tv_max_u32_dpp v102, v102, v102 row_bcast:31 row_mask:0xc bank_mask:0xf\n\t" \
  "v_pk_mul_f16 v144, v90, v78 op_sel:[1,1] op_sel_hi:[1,0] neg_lo:[1,0]\n\tv_pk_mul_f16 v145, v91, v79 op_sel:[1,1] op_sel_hi:[1,0] neg_lo:[1,0]\n\t" \
  "v_pk_fma_f16 v144, v90, v78, v144 op_sel:[0,0,0] op_sel_hi:[0,1,1]\n\tv_pk_fma_f16 v145, v91, v79, v145 op_sel:[0,0,0] op_sel_hi:[0,1,1]\n\t" \
  "v_readlane_b32 %[e], v102, 63\n\tv_pk_mul_f16 v146, v90, v86 op_sel:[1,1] op_sel_hi:[1,0] neg_lo:[1,0]\n\t" \
  "v_pk_mul_f16 v147, v91, v87 op_sel:[1,1] op_sel_hi:[1,0] neg_lo:[1,0]\n\tv_pk_fma_f16 v146, v90, v86, v146 op_sel:[0,0,0] op_sel_hi:[0,1,1]\n\t" \
  "v_pk_fma_f16 v147, v91, v87, v147 op_sel:[0,0,0] op_sel_hi:[0,1,1]\n\tv_pk_mul_f16 v148, v92, v78 op_sel:[1,1] op_sel_hi:[1,0] neg_lo:[1,0]\n\t" \
  "v_pk_mul_f16 v149, v93, v79 op_sel:[1,1] op_sel_hi:[1,0] neg_lo:[1,0]\n\tv_pk_fma_f16 v148, v92, v78, v148 op_sel:[0,0,0] op_sel_hi:[0,1,1]\n\t" \
  "v_pk_fma_f16 v149, v93, v79, v149 op_sel:[0,0,0] op_sel_hi:[0,1,1]\n\tv_pk_mul_f16 v150, v92, v86 op_sel:[1,1] op_sel_hi:[1,0] neg_lo:[1,0]\n\t" \
  "v_pk_mul_f16 v151, v93, v87 op_sel:[1,1] op_sel_hi:[1,0] neg_lo:[1,0]\n\tv_pk_fma_f16 v150, v92, v86, v150 op_sel:[0,0,0] op_sel_hi:[0,1,1]\n\t" \
  "v_pk_fma_f16 v151, v93, v87, v151 op_sel:[0,0,0] op_sel_hi:[0,1,1]\n\ts_bfe_u32 %[e], %[e], 0x80007\n\t" \
  "s_max_u32 %[e], %[e], %[em]\n\ts_sub_u32 %[t], 260, %[e]\n\t" \
  "s_lshl_b32 %[t], %[t], 23\n\tds_write_addtid_b32 v142 offset:864\n\t" \
  "ds_write_addtid_b32 v143 offset:5472\n\tds_write_addtid_b32 v144 offset:3456\n\t" \
  "ds_write_addtid_b32 v145 offset:8064\n\tds_write_addtid_b32 v146 offset:3744\n\t" \
  "ds_write_addtid_b32 v147 offset:8352\n\tds_write_addtid_b32 v148 offset:4032\n\t" \
  "ds_write_addtid_b32 v149 offset:8640\n\tds_write_addtid_b32 v150 offset:4320\n\t" \
  "ds_write_addtid_b32 v151 offset:8928\n\tv_pk_mul_f16 v96, v94, v88 op_sel:[1,1] op_sel_hi:[1,0] neg_lo:[1,0]\n\t" \
  "v_pk_mul_f16 v97, v95, v89 op_sel:[1,1] op_sel_hi:[1,0] neg_lo:[1,0]\n\tv_pk_fma_f16 v96, v94, v88, v96 op_sel:[0,0,0] op_sel_hi:[0,1,1]\n\t" \
  "v_pk_fma_f16 v97, v95, v89, v97 op_sel:[0,0,0] op_sel_hi:[0,1,1]\n\tv_pk_mul_f16 v98, v94, v90 op_sel:[1,1] op_sel_hi:[1,0] neg_lo:[1,0]\n\t" \
  "v_pk_mul_f16 v99, v95, v91 op_sel:[1,1] op_sel_hi:[1,0] neg_lo:[1,0]\n\tv_pk_fma_f16 v98, v94, v90, v98 op_sel:[0,0,0] op_sel_hi:[0,1,1]\n\t" \
  "v_pk_fma_f16 v99, v95, v91, v99 op_sel:[0,0,0] op_sel_hi:[0,1,1]\n\tv_pk_mul_f16 v100, v94, v92 op_sel:[1,1] op_sel_hi:[1,0] neg_lo:[1,0]\n\t" \
  "v_pk_mul_f16 v101, v95, v93 op_sel:[1,1] op_sel_hi:[1,0] neg_lo:[1,0]\n\tv_pk_fma_f16 v100, v94, v92, v100 op_sel:[0,0,0] op_sel_hi:[0,1,1]\n\t" \
  "v_pk_fma_f16 v101, v95, v93, v101 op_sel:[0,0,0] op_sel_hi:[0,1,1]\n\tds_write_addtid_b32 v96 offset:1440\n\t" \
  "ds_write_addtid_b32 v97 offset:6048\n\tds_write_addtid_b32 v98 offset:1728\n\t" \
  "ds_write_addtid_b32 v99 offset:6336\n\tds_write_addtid_b32 v100 offset:2016\n\t" \
  "ds_write_addtid_b32 v101 offset:6624\n\tds_read_b128 v[136:139], v71 offset:0\n\t" \
  "ds_read_b128 v[140:143], v71 offset:64\n\tds_read_b128 v[144:147], v71 offset:128\n\t" \
  "ds_read_b128 v[148:151], v71 offset:192\n\tds_read_b128 v[152:155], v71 offset:4608\n\t" \
  "ds_read_b128 v[156:159], v71 offset:4672\n\tds_read_b128 v[160:163], v71 offset:4736\n\t" \
  "ds_read_b128 v[164:167], v71 offset:4800\n\tv_pk_mul_f32 v[48:49], v[60:61], v[60:61] op_sel:[1,1] op_sel_hi:[1,0] neg_lo:[1,0]\n\t" \
  "v_pk_mul_f32 v[50:51], v[62:63], v[62:63] op_sel:[1,1] op_sel_hi:[1,0] neg_lo:[1,0]\n\tv_pk_fma_f32 v[48:49], v[60:61], v[60:61], v[48:49] op_sel:[0,0,0] op_sel_hi:[0,1,1]\n\t" \
  "s_cmp_lt_u32 %[w], 50176\n\ts_cbranch_scc0 1f\n\t" \
  "s_setprio 1\n\t1:\n\t" \
  "v_pk_fma_f32 v[50:51], v[62:63], v[62:63], v[50:51] op_sel:[0,0,0] op_sel_hi:[0,1,1]\n\tv_cvt_pk_f16_f32 v44, v60, v61\n\t" \
  "v_cvt_pk_f16_f32 v45, v62, v63\n\tv_pk_mul_f32 v[52:53], v[48:49], v[60:61] op_sel:[1,1] op_sel_hi:[1,0] neg_lo:[1,0]\n\t" \
  "v_pk_mul_f32 v[54:55], v[50:51], v[62:63] op_sel:[1,1] op_sel_hi:[1,0] neg_lo:[1,0]\n\tv_pk_fma_f32 v[52:53], v[48:49], v[60:61], v[52:53] op_sel:[0,0,0] op_sel_hi:[0,1,1]\n\t" \
  "v_pk_fma_f32 v[54:55], v[50:51], v[62:63], v[54:55] op_sel:[0,0,0] op_sel_hi:[0,1,1]\n\tv_pk_mul_f32 v[40:41], v[48:49], v[48:49] op_sel:[1,1] op_sel_hi:[1,0] neg_lo:[1,0]\n\t" \
  "v_pk_mul_f32 v[42:43], v[50:51], v[50:51] op_sel:[1,1] op_sel_hi:[1,0] neg_lo:[1,0]\n\tv_pk_fma_f32 v[40:41], v[48:49], v[48:49], v[40:41] op_sel:[0,0,0] op_sel_hi:[0,1,1]\n\t" \
  "v_pk_fma_f32 v[42:43], v[50:51], v[50:51], v[42:43] op_sel:[0,0,0] op_sel_hi:[0,1,1]\n\tv_cvt_pk_f16_f32 v46, v48, v49\n\t" \
  "v_cvt_pk_f16_f32 v47, v50, v51\n\tv_cvt_pk_f16_f32 v56, v52, v53\n\t" \
  "v_cvt_pk_f16_f32 v57, v54, v55\n\tv_lshlrev_b32 v64, 16, v68\n\t" \
  "v_lshlrev_b32 v65, 16, v69\n\tv_and_b32 v66, 0xffff0000, v68\n\t" \
  "v_and_b32 v67, 0xffff0000, v69\n\tv_mul_f32 v64, %[t], v64\n\t" \
  "v_mul_f32 v65, %[t], v65\n\tv_mul_f32 v66, %[t], v66\n\t" \
  "v_mul_f32 v67, %[t], v67\n\tv_cvt_pk_f16_f32 v48, v64, v65\n\t" \
  "v_cvt_pk_f16_f32 v49, v66, v67\n\tv_pk_mul_f32 v[68:69], v[64:65], v[40:41] op_sel:[1,1] op_sel_hi:[1,0] neg_lo:[1,0]\n\t" \
  "v_pk_mul_f32 v[70:71], v[66:67], v[42:43] op_sel:[1,1] op_sel_hi:[1,0] neg_lo:[1,0]\n\tv_pk_fma_f32 v[68:69], v[64:65], v[40:41], v[68:69] op_sel:[0,0,0] op_sel_hi:[0,1,1]\n\t" \
  "v_pk_fma_f32 v[70:71], v[66:67], v[42:43], v[70:71] op_sel:[0,0,0] op_sel_hi:[0,1,1]\n\tv_pk_mul_f16 v50, v48, v44 op_sel:[1,1] op_sel_hi:[1,0] neg_lo:[1,0]\n\t" \
  "v_pk_mul_f16 v51, v49, v45 op_sel:[1,1] op_sel_hi:[1,0] neg_lo:[1,0]\n\tv_pk_fma_f16 v50, v48, v44, v50 op_sel:[0,0,0] op_sel_hi:[0,1,1]\n\t" \
  "v_pk_fma_f16 v51, v49, v45, v51 op_sel:[0,0,0] op_sel_hi:[0,1,1]\n\tv_pk_mul_f16 v52, v48, v46 op_sel:[1,1] op_sel_hi:[1,0] neg_lo:[1,0]\n\t" \
  "v_pk_mul_f16 v53, v49, v47 op_sel:[1,1] op_sel_hi:[1,0] neg_lo:[1,0]\n\tv_pk_fma_f16 v52, v48, v46, v52 op_sel:[0,0,0] op_sel_hi:[0,1,1]\n\t" \
  "v_pk_fma_f16 v53, v49, v47, v53 op_sel:[0,0,0] op_sel_hi:[0,1,1]\n\tv_pk_mul_f16 v54, v48, v56 op_sel:[1,1] op_sel_hi:[1,0] neg_lo:[1,0]\n\t" \
  "v_pk_mul_f16 v55, v49, v57 op_sel:[1,1] op_sel_hi:[1,0] neg_lo:[1,0]\n\tv_pk_fma_f16 v54, v48, v56, v54 op_sel:[0,0,0] op_sel_hi:[0,1,1]\n\t" \
  "v_pk_fma_f16 v55, v49, v57, v55 op_sel:[0,0,0] op_sel_hi:[0,1,1]\n\tds_write_addtid_b32 v48 offset:0\n\t" \
  "ds_write_addtid_b32 v49 offset:4608\n\tds_write_addtid_b32 v50 offset:288\n\t" \
  "ds_write_addtid_b32 v51 offset:4896\n\tds_write_addtid_b32 v52 offset:576\n\t" \
  "ds_write_addtid_b32 v53 offset:5184\n\tds_write_addtid_b32 v54 offset:864\n\t" \
  "ds_write_addtid_b32 v55 offset:5472\n\tv_pk_mul_f32 v[60:61], v[40:41], v[40:41] op_sel:[1,1] op_sel_hi:[1,0] neg_lo:[1,0]\n\t" \
  "v_pk_mul_f32 v[62:63], v[42:43], v[42:43] op_sel:[1,1] op_sel_hi:[1,0] neg_lo:[1,0]\n\tv_pk_fma_f32 v[60:61], v[40:41], v[40:41], v[60:61] op_sel:[0,0,0] op_sel_hi:[0,1,1]\n\t" \
  "v_pk_fma_f32 v[62:63], v[42:43], v[42:43], v[62:63] op_sel:[0,0,0] op_sel_hi:[0,1,1]\n\tv_cvt_pk_f16_f32 v48, v68, v69\n\t" \
  "v_cvt_pk_f16_f32 v49, v70, v71\n\tv_pk_mul_f32 v[64:65], v[68:69], v[40:41] op_sel:[1,1] op_sel_hi:[1,0] neg_lo:[1,0]\n\t" \
  "v_pk_mul_f32 v[66:67], v[70:71], v[42:43] op_sel:[1,1] op_sel_hi:[1,0] neg_lo:[1,0]\n\tv_pk_fma_f32 v[64:65], v[68:69], v[40:41], v[64:65] op_sel:[0,0,0] op_sel_hi:[0,1,1]\n\t" \
  "v_pk_fma_f32 v[66:67], v[70:71], v[42:43], v[66:67] op_sel:[0,0,0] op_sel_hi:[0,1,1]\n\tv_pk_mul_f16 v50, v48, v44 op_sel:[1,1] op_sel_hi:[1,0] neg_lo:[1,0]\n\t" \
  "v_pk_mul_f16 v51, v49, v45 op_sel:[1,1] op_sel_hi:[1,0] neg_lo:[1,0]\n\tv_pk_fma_f16 v50, v48, v44, v50 op_sel:[0,0,0] op_sel_hi:[0,1,1]\n\t" \
  "v_pk_fma_f16 v51, v49, v45, v51 op_sel:[0,0,0] op_sel_hi:[0,1,1]\n\tv_pk_mul_f16 v52, v48, v46 op_sel:[1,1] op_sel_hi:[1,0] neg_lo:[1,0]\n\t" \
  "v_pk_mul_f16 v53, v49, v47 op_sel:[1,1] op_sel_hi:[1,0] neg_lo:[1,0]\n\tv_pk_fma_f16 v52, v48, v46, v52 op_sel:[0,0,0] op_sel_hi:[0,1,1]\n\t" \
  "v_pk_fma_f16 v53, v49, v47, v53 op_sel:[0,0,0] op_sel_hi:[0,1,1]\n\tv_pk_mul_f16 v54, v48, v56 op_sel:[1,1] op_sel_hi:[1,0] neg_lo:[1,0]\n\t" \
  "v_pk_mul_f16 v55, v49, v57 op_sel:[1,1] op_sel_hi:[1,0] neg_lo:[1,0]\n\tv_pk_fma_f16 v54, v48, v56, v54 op_sel:[0,0,0] op_sel_hi:[0,1,1]\n\t" \
  "v_pk_fma_f16 v55, v49, v57, v55 op_sel:[0,0,0] op_sel_hi:[0,1,1]\n\tds_write_addtid_b32 v48 offset:1152\n\t" \
  "ds_write_addtid_b32 v49 offset:5760\n\tds_write_addtid_b32 v50 offset:1440\n\t" \
  "ds_write_addtid_b32 v51 offset:6048\n\tds_write_addtid_b32 v52 offset:1728\n\t" \
  "ds_write_addtid_b32 v53 offset:6336\n\tds_write_addtid_b32 v54 offset:2016\n\t" \
  "ds_write_addtid_b32 v55 offset:6624\n\tv_pk_mul_f32 v[36:37], v[60:61], v[60:61] op_sel:[1,1] op_sel_hi:[1,0] neg_lo:[1,0]\n\t" \
  "v_pk_mul_f32 v[38:39], v[62:63], v[62:63] op_sel:[1,1] op_sel_hi:[1,0] neg_lo:[1,0]\n\tv_pk_fma_f32 v[36:37], v[60:61], v[60:61], v[36:37] op_sel:[0,0,0] op_sel_hi:[0,1,1]\n\t" \
  "v_pk_fma_f32 v[38:39], v[62:63], v[62:63], v[38:39] op_sel:[0,0,0] op_sel_hi:[0,1,1]\n\tv_cvt_pk_f16_f32 v48, v64, v65\n\t" \
  "v_cvt_pk_f16_f32 v49, v66, v67\n\tv_pk_mul_f32 v[68:69], v[64:65], v[40:41] op_sel:[1,1] op_sel_hi:[1,0] neg_lo:[1,0]\n\t" \
  "v_pk_mul_f32 v[70:71], v[66:67], v[42:43] op_sel:[1,1] op_sel_hi:[1,0] neg_lo:[1,0]\n\tv_pk_fma_f32 v[68:69], v[64:65], v[40:41], v[68:69] op_sel:[0,0,0] op_sel_hi:[0,1,1]\n\t" \
  "v_pk_fma_f32 v[70:71], v[66:67], v[42:43], v[70:71] op_sel:[0,0,0] op_sel_hi:[0,1,1]\n\tv_pk_mul_f16 v50, v48, v44 op_sel:[1,1] op_sel_hi:[1,0] neg_lo:[1,0]\n\t" \
  "v_pk_mul_f16 v51, v49, v45 op_sel:[1,1] op_sel_hi:[1,0] neg_lo:[1,0]\n\tv_pk_fma_f16 v50, v48, v44, v50 op_sel:[0,0,0] op_sel_hi:[0,1,1]\n\t" \
  "v_pk_fma_f16 v51, v49, v45, v51 op_sel:[0,0,0] op_sel_hi:[0,1,1]\n\tv_pk_mul_f16 v52, v48, v46 op_sel:[1,1] op_sel_hi:[1,0] neg_lo:[1,0]\n\t" \
  "v_pk_mul_f16 v53, v49, v47 op_sel:[1,1] op_sel_hi:[1,0] neg_lo:[1,0]\n\tv_pk_fma_f16 v52, v48, v46, v52 op_sel:[0,0,0] op_sel_hi:[0,1,1]\n\t" \
  "v_pk_fma_f16 v53, v49, v47, v53 op_sel:[0,0,0] op_sel_hi:[0,1,1]\n\tv_pk_mul_f16 v54, v48, v56 op_sel:[1,1] op_sel_hi:[1,0] neg_lo:[1,0]\n\t" \
  "v_pk_mul_f16 v55, v49, v57 op_sel:[1,1] op_sel_hi:[1,0] neg_lo:[1,0]\n\tv_pk_fma_f16 v54, v48, v56, v54 op_sel:[0,0,0] op_sel_hi:[0,1,1]\n\t" \
  "v_pk_fma_f16 v55, v49, v57, v55 op_sel:[0,0,0] op_sel_hi:[0,1,1]\n\tds_write_addtid_b32 v48 offset:2304\n\t" \
  "ds_write_addtid_b32 v49 offset:6912\n\tds_write_addtid_b32 v50 offset:2592\n\t" \
  "ds_write_addtid_b32 v51 offset:7200\n\tds_write_addtid_b32 v52 offset:2880\n\t" \
  "ds_write_addtid_b32 v53 offset:7488\n\tds_write_addtid_b32 v54 offset:3168\n\t" \
  "ds_write_addtid_b32 v55 offset:7776\n\tv_cvt_pk_f16_f32 v58, v36, v37\n\t" \
  "v_cvt_pk_f16_f32 v59, v38, v39\n\tv_cvt_pk_f16_f32 v48, v68, v69\n\t" \
  "v_cvt_pk_f16_f32 v49, v70, v71\n\tv_pk_mul_f16 v50, v48, v44 op_sel:[1,1] op_sel_hi:[1,0] neg_lo:[1,0]\n\t" \
  "v_pk_mul_f16 v51, v49, v45 op_sel:[1,1] op_sel_hi:[1,0] neg_lo:[1,0]\n\tv_pk_fma_f16 v50, v48, v44, v50 op_sel:[0,0,0] op_sel_hi:[0,1,1]\n\t" \
  "v_pk_fma_f16 v51, v49, v45, v51 op_sel:[0,0,0] op_sel_hi:[0,1,1]\n\tv_pk_mul_f16 v52, v48, v46 op_sel:[1,1] op_sel_hi:[1,0] neg_lo:[1,0]\n\t" \
  "v_pk_mul_f16 v53, v49, v47 op_sel:[1,1] op_sel_hi:[1,0] neg_lo:[1,0]\n\tv_pk_fma_f16 v52, v48, v46, v52 op_sel:[0,0,0] op_sel_hi:[0,1,1]\n\t" \
  "v_pk_fma_f16 v53, v49, v47, v53 op_sel:[0,0,0] op_sel_hi:[0,1,1]\n\tv_pk_mul_f16 v54, v48, v56 op_sel:[1,1] op_sel_hi:[1,0] neg_lo:[1,0]\n\t" \
  "v_pk_mul_f16 v55, v49, v57 op_sel:[1,1] op_sel_hi:[1,0] neg_lo:[1,0]\n\tv_pk_fma_f16 v54, v48, v56, v54 op_sel:[0,0,0] op_sel_hi:[0,1,1]\n\t" \
  "v_pk_fma_f16 v55, v49, v57, v55 op_sel:[0,0,0] op_sel_hi:[0,1,1]\n\tds_write_addtid_b32 v48 offset:3456\n\t" \
  "ds_write_addtid_b32 v49 offset:8064\n\tds_write_addtid_b32 v50 offset:3744\n\t" \
  "ds_write_addtid_b32 v51 offset:8352\n\tds_write_addtid_b32 v52 offset:4032\n\t" \
  "ds_write_addtid_b32 v53 offset:8640\n\tds_write_addtid_b32 v54 offset:4320\n\t" \
  "ds_write_addtid_b32 v55 offset:8928\n\tds_read_b128 v[72:75], v27 offset:0\n\t" \
  "ds_read_b128 v[76:79], v27 offset:64\n\tds_read_b128 v[80:83], v27 offset:128\n\t" \
  "ds_read_b128 v[84:87], v27 offset:192\n\tds_read_b128 v[88:91], v27 offset:4608\n\t" \
  "ds_read_b128 v[92:95], v27 offset:4672\n\tds_read_b128 v[96:99], v27 offset:4736\n\t" \
  "ds_read_b128 v[100:103], v27 offset:4800\n\tds_write_addtid_b32 v58 offset:0\n\t" \
  "ds_write_addtid_b32 v59 offset:4608\n\tds_read_b128 v[40:43], v12 offset:0\n\t" \
  "ds_read_b128 v[44:47], v12 offset:64\n\tds_read_b128 v[48:51], v12 offset:128\n\t" \
  "v_add_u32_sdwa v56, %[w], v16 dst_sel:DWORD dst_unused:UNUSED_PAD src0_sel:DWORD src1_sel:WORD_0\n\tv_add_u32_sdwa v57, %[w], v16 dst_sel:DWORD dst_unused:UNUSED_PAD src0_sel:DWORD src1_sel:WORD_1\n\t" \
  "v_add_u32_sdwa v58, %[w], v17 dst_sel:DWORD dst_unused:UNUSED_PAD src0_sel:DWORD src1_sel:WORD_0\n\tv_add_u32_sdwa v59, %[w], v17 dst_sel:DWORD dst_unused:UNUSED_PAD src0_sel:DWORD src1_sel:WORD_1\n\t" \
  "s_cmp_lt_u32 %[w], 50176\n\ts_cbranch_scc1 1f\n\t" \
  "s_setprio 1\n\t1:\n\t" \
  "v_add_u32 v14, v56, v35\n\tv_add_u32 v15, v57, v35\n\t" \
  "s_waitcnt lgkmcnt(12)\n\tv_mfma_f32_16x16x32_f16 v[60:63], v[104:107], v[72:75], 0\n\t" \
  "v_mfma_f32_16x16x32_f16 v[36:39], v[72:75], v[136:139], 0\n\tv_mfma_f32_16x16x32_f16 v[4:7], v[136:139], v[72:75], 0\n\t" \
  "s_waitcnt lgkmcnt(2)\n\tv_mfma_f32_16x16x32_f16 v[60:63], v[108:111], v[76:79], v[60:63]\n\t" \
  "v_pk_mul_f16 v52, v40, v72 op_sel:[1,1] op_sel_hi:[1,0] neg_lo:[1,0]\n\tv_pk_mul_f16 v53, v41, v73 op_sel:[1,1] op_sel_hi:[1,0] neg_lo:[1,0]\n\t" \
  "v_pk_mul_f16 v54, v42, v74 op_sel:[1,1] op_sel_hi:[1,0] neg_lo:[1,0]\n\tv_mfma_f32_16x16x32_f16 v[36:39], v[76:79], v[140:143], v[36:39]\n\t" \
  "v_pk_mul_f16 v55, v43, v75 op_sel:[1,1] op_sel_hi:[1,0] neg_lo:[1,0]\n\tv_pk_fma_f16 v40, v40, v72, v52 op_sel:[0,0,0] op_sel_hi:[0,1,1]\n\t" \
  "v_pk_fma_f16 v41, v41, v73, v53 op_sel:[0,0,0] op_sel_hi:[0,1,1]\n\tv_mfma_f32_16x16x32_f16 v[4:7], v[140:143], v[76:79], v[4:7]\n\t" \
  "v_pk_fma_f16 v42, v42, v74, v54 op_sel:[0,0,0] op_sel_hi:[0,1,1]\n\tv_pk_fma_f16 v43, v43, v75, v55 op_sel:[0,0,0] op_sel_hi:[0,1,1]\n\t" \
  "s_waitcnt lgkmcnt(1)\n\tv_mfma_f32_16x16x32_f16 v[60:63], v[112:115], v[80:83], v[60:63]\n\t" \
  "ds_read_b128 v[72:75], v12 offset:192\n\tv_pk_mul_f16 v52, v44, v76 op_sel:[1,1] op_sel_hi:[1,0] neg_lo:[1,0]\n\t" \
  "v_pk_mul_f16 v53, v45, v77 op_sel:[1,1] op_sel_hi:[1,0] neg_lo:[1,0]\n\tv_pk_mul_f16 v54, v46, v78 op_sel:[1,1] op_sel_hi:[1,0] neg_lo:[1,0]\n\t" \
  "v_mfma_f32_16x16x32_f16 v[36:39], v[80:83], v[144:147], v[36:39]\n\tv_pk_mul_f16 v55, v47, v79 op_sel:[1,1] op_sel_hi:[1,0] neg_lo:[1,0]\n\t" \
  "v_pk_fma_f16 v44, v44, v76, v52 op_sel:[0,0,0] op_sel_hi:[0,1,1]\n\tv_pk_fma_f16 v45, v45, v77, v53 op_sel:[0,0,0] op_sel_hi:[0,1,1]\n\t" \
  "v_mfma_f32_16x16x32_f16 v[4:7], v[144:147], v[80:83], v[4:7]\n\tv_pk_fma_f16 v46, v46, v78, v54 op_sel:[0,0,0] op_sel_hi:[0,1,1]\n\t" \
  "v_pk_fma_f16 v47, v47, v79, v55 op_sel:[0,0,0] op_sel_hi:[0,1,1]\n\ts_waitcnt lgkmcnt(1)\n\t" \
  "v_mfma_f32_16x16x32_f16 v[60:63], v[116:119], v[84:87], v[60:63]\n\tds_read_b128 v[76:79], v12 offset:4608\n\t" \
  "v_pk_mul_f16 v52, v48, v80 op_sel:[1,1] op_sel_hi:[1,0] neg_lo:[1,0]\n\tv_pk_mul_f16 v53, v49, v81 op_sel:[1,1] op_sel_hi:[1,0] neg_lo:[1,0]\n\t" \
  "v_pk_mul_f16 v54, v50, v82 op_sel:[1,1] op_sel_hi:[1,0] neg_lo:[1,0]\n\tv_mfma_f32_16x16x32_f16 v[36:39], v[84:87], v[148:151], v[36:39]\n\t" \
  "v_pk_mul_f16 v55, v51, v83 op_sel:[1,1] op_sel_hi:[1,0] neg_lo:[1,0]\n\tv_pk_fma_f16 v48, v48, v80, v52 op_sel:[0,0,0] op_sel_hi:[0,1,1]\n\t" \
  "v_pk_fma_f16 v49, v49, v81, v53 op_sel:[0,0,0] op_sel_hi:[0,1,1]\n\tv_mfma_f32_16x16x32_f16 v[4:7], v[148:151], v[84:87], v[4:7]\n\t" \
  "v_pk_fma_f16 v50, v50, v82, v54 op_sel:[0,0,0] op_sel_hi:[0,1,1]\n\tv_pk_fma_f16 v51, v51, v83, v55 op_sel:[0,0,0] op_sel_hi:[0,1,1]\n\t" \
  "s_waitcnt lgkmcnt(1)\n\tv_mfma_f32_16x16x32_f16 v[60:63], v[120:123], v[88:91], v[60:63]\n\t" \
  "ds_read_b128 v[80:83], v12 offset:4672\n\tv_pk_mul_f16 v52, v72, v84 op_sel:[1,1] op_sel_hi:[1,0] neg_lo:[1,0]\n\t" \
  "v_pk_mul_f16 v53, v73, v85 op_sel:[1,1] op_sel_hi:[1,0] neg_lo:[1,0]\n\tv_pk_mul_f16 v54, v74, v86 op_sel:[1,1] op_sel_hi:[1,0] neg_lo:[1,0]\n\t" \
  "v_mfma_f32_16x16x32_f16 v[36:39], v[88:91], v[152:155], v[36:39]\n\tv_pk_mul_f16 v55, v75, v87 op_sel:[1,1] op_sel_hi:[1,0] neg_lo:[1,0]\n\t" \
  "v_pk_fma_f16 v72, v72, v84, v52 op_sel:[0,0,0] op_sel_hi:[0,1,1]\n\tv_pk_fma_f16 v73, v73, v85, v53 op_sel:[0,0,0] op_sel_hi:[0,1,1]\n\t" \
  "v_mfma_f32_16x16x32_f16 v[4:7], v[152:155], v[88:91], v[4:7]\n\tv_pk_fma_f16 v74, v74, v86, v54 op_sel:[0,0,0] op_sel_hi:[0,1,1]\n\t" \
  "v_pk_fma_f16 v75, v75, v87, v55 op_sel:[0,0,0] op_sel_hi:[0,1,1]\n\ts_waitcnt lgkmcnt(1)\n\t" \
  "v_mfma_f32_16x16x32_f16 v[60:63], v[124:127], v[92:95], v[60:63]\n\tds_read_b128 v[84:87], v12 offset:4736\n\t" \
  "v_pk_mul_f16 v52, v76, v88 op_sel:[1,1] op_sel_hi:[1,0] neg_lo:[1,0]\n\tv_pk_mul_f16 v53, v77, v89 op_sel:[1,1] op_sel_hi:[1,0] neg_lo:[1,0]\n\t" \
  "v_pk_mul_f16 v54, v78, v90 op_sel:[1,1] op_sel_hi:[1,0] neg_lo:[1,0]\n\tv_mfma_f32_16x16x32_f16 v[36:39], v[92:95], v[156:159], v[36:39]\n\t" \
  "v_pk_mul_f16 v55, v79, v91 op_sel:[1,1] op_sel_hi:[1,0] neg_lo:[1,0]\n\tv_pk_fma_f16 v76, v76, v88, v52 op_sel:[0,0,0] op_sel_hi:[0,1,1]\n\t" \
  "v_pk_fma_f16 v77, v77, v89, v53 op_sel:[0,0,0] op_sel_hi:[0,1,1]\n\tv_mfma_f32_16x16x32_f16 v[4:7], v[156:159], v[92:95], v[4:7]\n\t" \
  "v_pk_fma_f16 v78, v78, v90, v54 op_sel:[0,0,0] op_sel_hi:[0,1,1]\n\tv_pk_fma_f16 v79, v79, v91, v55 op_sel:[0,0,0] op_sel_hi:[0,1,1]\n\t" \
  "s_waitcnt lgkmcnt(1)\n\tv_mfma_f32_16x16x32_f16 v[60:63], v[128:131], v[96:99], v[60:63]\n\t" \
  "ds_read_b128 v[88:91], v12 offset:4800\n\tv_pk_mul_f16 v52, v80, v92 op_sel:[1,1] op_sel_hi:[1,0] neg_lo:[1,0]\n\t" \
  "v_pk_mul_f16 v53, v81, v93 op_sel:[1,1] op_sel_hi:[1,0] neg_lo:[1,0]\n\tv_pk_mul_f16 v54, v82, v94 op_sel:[1,1] op_sel_hi:[1,0] neg_lo:[1,0]\n\t" \
  "v_mfma_f32_16x16x32_f16 v[36:39], v[96:99], v[160:163], v[36:39]\n\tv_pk_mul_f16 v55, v83, v95 op_sel:[1,1] op_sel_hi:[1,0] neg_lo:[1,0]\n\t" \
  "v_pk_fma_f16 v80, v80, v92, v52 op_sel:[0,0,0] op_sel_hi:[0,1,1]\n\tv_pk_fma_f16 v81, v81, v93, v53 op_sel:[0,0,0] op_sel_hi:[0,1,1]\n\t" \
  "v_mfma_f32_16x16x32_f16 v[4:7], v[160:163], v[96:99], v[4:7]\n\tv_pk_fma_f16 v82, v82, v94, v54 op_sel:[0,0,0] op_sel_hi:[0,1,1]\n\t" \
  "v_pk_fma_f16 v83, v83, v95, v55 op_sel:[0,0,0] op_sel_hi:[0,1,1]\n\ts_waitcnt lgkmcnt(1)\n\t" \
  "v_mfma_f32_16x16x32_f16 v[60:63], v[132:135], v[100:103], v[60:63]\n\tv_pk_mul_f16 v52, v84, v96 op_sel:[1,1] op_sel_hi:[1,0] neg_lo:[1,0]\n\t" \
  "v_pk_mul_f16 v53, v85, v97 op_sel:[1,1] op_sel_hi:[1,0] neg_lo:[1,0]\n\tv_pk_mul_f16 v54, v86, v98 op_sel:[1,1] op_sel_hi:[1,0] neg_lo:[1,0]\n\t" \
  "v_mfma_f32_16x16x32_f16 v[36:39], v[100:103], v[164:167], v[36:39]\n\tv_pk_mul_f16 v55, v87, v99 op_sel:[1,1] op_sel_hi:[1,0] neg_lo:[1,0]\n\t" \
  "v_pk_fma_f16 v84, v84, v96, v52 op_sel:[0,0,0] op_sel_hi:[0,1,1]\n\tv_pk_fma_f16 v85, v85, v97, v53 op_sel:[0,0,0] op_sel_hi:[0,1,1]\n\t" \
  "v_mfma_f32_16x16x32_f16 v[4:7], v[164:167], v[100:103], v[4:7]\n\tv_pk_fma_f16 v86, v86, v98, v54 op_sel:[0,0,0] op_sel_hi:[0,1,1]\n\t" \
  "v_pk_fma_f16 v87, v87, v99, v55 op_sel:[0,0,0] op_sel_hi:[0,1,1]\n\ts_waitcnt lgkmcnt(0)\n\t" \
  "v_mfma_f32_16x16x32_f16 v[64:67], v[104:107], v[40:43], 0\n\tv_pk_mul_f16 v52, v88, v100 op_sel:[1,1] op_sel_hi:[1,0] neg_lo:[1,0]\n\t" \
  "v_pk_mul_f16 v53, v89, v101 op_sel:[1,1] op_sel_hi:[1,0] neg_lo:[1,0]\n\tv_pk_mul_f16 v54, v90, v102 op_sel:[1,1] op_sel_hi:[1,0] neg_lo:[1,0]\n\t" \
  "v_mfma_f32_16x16x32_f16 v[68:71], v[40:43], v[136:139], 0\n\tv_pk_mul_f16 v55, v91, v103 op_sel:[1,1] op_sel_hi:[1,0] neg_lo:[1,0]\n\t" \
  "v_pk_fma_f16 v88, v88, v100, v52 op_sel:[0,0,0] op_sel_hi:[0,1,1]\n\tv_pk_fma_f16 v89, v89, v101, v53 op_sel:[0,0,0] op_sel_hi:[0,1,1]\n\t" \
  "v_mfma_f32_16x16x32_f16 v[8:11], v[136:139], v[40:43], 0\n\tv_pk_fma_f16 v90, v90, v102, v54 op_sel:[0,0,0] op_sel_hi:[0,1,1]\n\t" \
  "v_pk_fma_f16 v91, v91, v103, v55 op_sel:[0,0,0] op_sel_hi:[0,1,1]\n\tv_mov_b64 v[100:101], 0\n\t" \
  "v_mfma_f32_16x16x32_f16 v[64:67], v[108:111], v[44:47], v[64:67]\n\tv_mov_b64 v[102:103], 0\n\t" \
  "v_cvt_pk_f16_f32 v94, v36, v37\n\tv_cvt_pk_f16_f32 v95, v38, v39\n\t" \
  "v_mfma_f32_16x16x32_f16 v[68:71], v[44:47], v[140:143], v[68:71]\n\tv_cvt_pk_f16_f32 v4, v4, v5\n\t" \
  "v_cvt_pk_f16_f32 v5, v6, v7\n\tv_mov_b32_dpp v94, v94 row_shl:4 row_mask:0xf bank_mask:0x5\n\t" \
  "v_mfma_f32_16x16x32_f16 v[8:11], v[140:143], v[44:47], v[8:11]\n\tv_mov_b32_dpp v95, v95 row_shl:4 row_mask:0xf bank_mask:0x5\n\t" \
  "v_permlane16_swap_b32 v4, v5\n\tv_cvt_pk_f16_f32 v92, v60, v61\n\t" \
  "v_mfma_f32_16x16x32_f16 v[64:67], v[112:115], v[48:51], v[64:67]\n\tv_cvt_pk_f16_f32 v93, v62, v63\n\t" \
  "v_alignbit_b32 v96, v95, v94, 16\n\tv_perm_b32 v6, v4, v4, v33\n\t" \
  "v_mfma_f32_16x16x32_f16 v[68:71], v[48:51], v[144:147], v[68:71]\n\tv_perm_b32 v7, v4, v4, v34\n\t" \
  "v_mfma_f32_16x16x32_f16 v[8:11], v[144:147], v[48:51], v[8:11]\n\tds_write_b128 v58, v[100:103]\n\t" \
  "v_mfma_f32_16x16x32_f16 v[64:67], v[116:119], v[72:75], v[64:67]\n\tds_write2_b32 v56, v92, v93 offset0:193 offset1:194\n\t" \
  "v_mfma_f32_16x16x32_f16 v[68:71], v[72:75], v[148:151], v[68:71]\n\tds_write2_b32 v57, v92, v93 offset0:193 offset1:194\n\t" \
  "v_mfma_f32_16x16x32_f16 v[8:11], v[148:151], v[72:75], v[8:11]\n\tds_write2_b32 v14, v6, v7 offset0:64 offset1:65\n\t" \
  "v_mfma_f32_16x16x32_f16 v[64:67], v[120:123], v[76:79], v[64:67]\n\tds_write2_b32 v15, v6, v7 offset0:64 offset1:65\n\t" \
  "v_mfma_f32_16x16x32_f16 v[68:71], v[76:79], v[152:155], v[68:71]\n\tds_write2_b32 v59, v94, v95 offset0:4 offset1:5\n\t" \
  "v_mfma_f32_16x16x32_f16 v[8:11], v[152:155], v[76:79], v[8:11]\n\tds_write_b16 v59, v94 offset:846\n\t" \
  "v_mfma_f32_16x16x32_f16 v[64:67], v[124:127], v[80:83], v[64:67]\n\tds_write_b32 v59, v96 offset:848\n\t" \
  "v_mfma_f32_16x16x32_f16 v[68:71], v[80:83], v[156:159], v[68:71]\n\tds_write_b16_d16_hi v59, v95 offset:852\n\t" \
  "v_mfma_f32_16x16x32_f16 v[8:11], v[156:159], v[80:83], v[8:11]\n\tv_mfma_f32_16x16x32_f16 v[64:67], v[128:131], v[84:87], v[64:67]\n\t" \
  "v_mfma_f32_16x16x32_f16 v[68:71], v[84:87], v[160:163], v[68:71]\n\tv_mfma_f32_16x16x32_f16 v[8:11], v[160:163], v[84:87], v[8:11]\n\t" \
  "v_mfma_f32_16x16x32_f16 v[68:71], v[88:91], v[164:167], v[68:71]\n\tv_mfma_f32_16x16x32_f16 v[8:11], v[164:167], v[88:91], v[8:11]\n\t" \
  "v_mfma_f32_16x16x32_f16 v[64:67], v[132:135], v[88:91], v[64:67]\n\ts_nop 5\n\t" \
  "v_cvt_pk_f16_f32 v48, v68, v69\n\tv_cvt_pk_f16_f32 v49, v70, v71\n\t" \
  "v_cvt_pk_f16_f32 v8, v8, v9\n\tv_cvt_pk_f16_f32 v9, v10, v11\n\t" \
  "v_mov_b32_dpp v48, v48 row_shl:4 row_mask:0xf bank_mask:0x5\n\tv_mov_b32_dpp v49, v49 row_shl:4 row_mask:0xf bank_mask:0x5\n\t" \
  "v_permlane16_swap_b32 v8, v9\n\tv_cvt_pk_f16_f32 v46, v64, v65\n\t" \
  "v_cvt_pk_f16_f32 v47, v66, v67\n\tv_alignbit_b32 v50, v49, v48, 16\n\t" \
  "v_perm_b32 v10, v8, v8, v33\n\tv_perm_b32 v11, v8, v8, v34\n\t" \
  "ds_write2_b32 v56, v46, v47 offset0:129 offset1:130\n\tds_write2_b32 v57, v46, v47 offset0:129 offset1:130\n\t" \
  "ds_write2_b32 v14, v10, v11 offset0:0 offset1:1\n\tds_write2_b32 v15, v10, v11 offset0:0 offset1:1\n\t" \
  "ds_write2_b32 v59, v48, v49 offset0:0 offset1:1\n\tds_write_b16 v59, v48 offset:830\n\t" \
  "ds_write_b32 v59, v50 offset:832\n\tds_write_b16_d16_hi v59, v49 offset:836\n\t"
#define GEN_CLOBBER "v4", "v5", "v6", "v7", "v9", "v10", "v11", "v13", "v14", "v15", "v36", "v37", "v38", "v39", "v40", "v41", "v42", "v43", "v44", "v45", "v47", "v49", "v50", "v51", "v52", "v53", "v54", "v55", "v56", "v57", "v58", "v59", "v60", "v61", "v62", "v63", "v64", "v65", "v66", "v67", "v72", "v73", "v74", "v75", "v76", "v77", "v78", "v79", "v80", "v81", "v82", "v83", "v84", "v85", "v86", "v87", "v88", "v89", "v90", "v91", "v92", "v94", "v95", "v98", "v99", "v100", "v101", "v102", "v103", "v104", "v105", "v106", "v107", "v108", "v109", "v110", "v111", "v112", "v113", "v114", "v115", "v116", "v117", "v118", "v119", "v120", "v121", "v122", "v123", "v124", "v125", "v126", "v127", "v128", "v129", "v130", "v131", "v132", "v133", "v134", "v135", "v136", "v137", "v138", "v139", "v140", "v141", "v142", "v143", "v144", "v145", "v146", "v147", "v148", "v149", "v150", "v151", "v152", "v153", "v154", "v155", "v156", "v157", "v158", "v159", "v160", "v161", "v162", "v163", "v164", "v165", "v166", "v167"
#define CONV_ASM \
  "ds_read_b32 v34, v30\n\tds_read_b64 v[132:133], v26 offset:512\n\tds_read_b64 v[134:135], v26 offset:520\n\t" \
  "ds_read_b64 v[100:101], v26 offset:0\n\tds_read_b64 v[102:103], v26 offset:8\n\tds_read_b64 v[136:137], v26 offset:576\n\t" \
  "ds_read_b64 v[138:139], v26 offset:584\n\tds_read_b64 v[104:105], v26 offset:64\n\tds_read_b64 v[106:107], v26 offset:72\n\t" \
  "ds_read_b64 v[140:141], v26 offset:640\n\tds_read_b64 v[142:143], v26 offset:648\n\tds_read_b64 v[108:109], v26 offset:128\n\t" \
  "ds_read_b64 v[110:111], v26 offset:136\n\tds_read_b64 v[144:145], v26 offset:704\n\tds_read_b64 v[146:147], v26 offset:712\n\t" \
  "ds_read_b64 v[112:113], v26 offset:192\n\tds_read_b64 v[114:115], v26 offset:200\n\tds_read_b64 v[148:149], v26 offset:768\n\t" \
  "ds_read_b64 v[150:151], v26 offset:776\n\tds_read_b64 v[116:117], v26 offset:256\n\tds_read_b64 v[118:119], v26 offset:264\n\t" \
  TS(60, 61) "s_waitcnt lgkmcnt(15)\n\tv_cmp_gt_u32 vcc, 1, v34\n\t" \
  "s_cbranch_vccz 2f\n\ts_mov_b32 s6, 0x10000\n\t1:\n\t" \
  "s_sleep 1\n\tds_read_b32 v34, v30\n\ts_sub_u32 s6, s6, 1\n\t" \
  "s_waitcnt lgkmcnt(0)\n\tv_cmp_gt_u32 vcc, 1, v34\n\ts_cbranch_scc1 2f\n\t" \
  "s_cbranch_vccnz 1b\n\t2:\n\t" TS(62, 63) \
  "s_cmp_lt_u32 %[w], 4\n\ts_setprio 1\n\ts_cbranch_scc1 1f\n\t" \
  "s_setprio 2\n\t1:\n\tds_read_b128 v[0:3], v28 offset:0\n\t" \
  "ds_read_b128 v[4:7], v28 offset:64\n\tds_read_b128 v[8:11], v28 offset:128\n\tds_read_b128 v[12:15], v28 offset:192\n\t" \
  "ds_read_b128 v[16:19], v28 offset:256\n\tds_read_b128 v[20:23], v28 offset:320\n\ts_waitcnt lgkmcnt(5)\n\t" \
  "v_mfma_f32_16x16x32_f16 v[52:55], v[132:135], v[0:3], 0\n\tds_read_b64 v[152:153], v26 offset:832\n\tds_read_b64 v[154:155], v26 offset:840\n\t" \
  "v_mfma_f32_16x16x32_f16 v[56:59], v[100:103], v[0:3], 0\n\tds_read_b64 v[120:121], v26 offset:320\n\tds_read_b64 v[122:123], v26 offset:328\n\t" \
  "ds_read_b128 v[0:3], v28 offset:384\n\ts_waitcnt lgkmcnt(9)\n\tv_mfma_f32_16x16x32_f16 v[52:55], v[136:139], v[4:7], v[52:55]\n\t" \
  "ds_read_b64 v[156:157], v26 offset:896\n\tds_read_b64 v[158:159], v26 offset:904\n\tv_mfma_f32_16x16x32_f16 v[56:59], v[104:107], v[4:7], v[56:59]\n\t" \
  "ds_read_b64 v[124:125], v26 offset:384\n\tds_read_b64 v[126:127], v26 offset:392\n\tds_read_b128 v[4:7], v28 offset:448\n\t" \
  "s_waitcnt lgkmcnt(13)\n\tv_mfma_f32_16x16x32_f16 v[52:55], v[140:143], v[8:11], v[52:55]\n\tds_read_b64 v[160:161], v26 offset:960\n\t" \
  "ds_read_b64 v[162:163], v26 offset:968\n\tv_mfma_f32_16x16x32_f16 v[56:59], v[108:111], v[8:11], v[56:59]\n\tds_read_b64 v[128:129], v26 offset:448\n\t" \
  "ds_read_b64 v[130:131], v26 offset:456\n\tds_read_b128 v[8:11], v28 offset:512\n\ts_waitcnt lgkmcnt(15)\n\t" \
  "v_mfma_f32_16x16x32_f16 v[52:55], v[144:147], v[12:15], v[52:55]\n\tds_read_b64 v[164:165], v26 offset:1024\n\tds_read_b64 v[166:167], v26 offset:1032\n\t" \
  "v_mfma_f32_16x16x32_f16 v[56:59], v[112:115], v[12:15], v[56:59]\n\tds_read_b64 v[76:77], v31 offset:72\n\tds_read_b64 v[78:79], v31 offset:176\n\t" \
  "ds_read_b128 v[12:15], v28 offset:576\n\tv_mfma_f32_16x16x32_f16 v[52:55], v[148:151], v[16:19], v[52:55]\n\tds_read_b64 v[80:81], v31 offset:40\n\t" \
  "ds_read_b64 v[82:83], v31 offset:144\n\tv_mfma_f32_16x16x32_f16 v[56:59], v[116:119], v[16:19], v[56:59]\n\tds_read_b64 v[84:85], v31 offset:8\n\t" \
  "ds_read_b64 v[86:87], v31 offset:112\n\tds_read_b128 v[16:19], v28 offset:640\n\ts_waitcnt lgkmcnt(15)\n\t" \
  "v_mfma_f32_16x16x32_f16 v[52:55], v[152:155], v[20:23], v[52:55]\n\tds_read_b64 v[88:89], v31 offset:280\n\tds_read_b64 v[90:91], v31 offset:384\n\t" \
  "v_mfma_f32_16x16x32_f16 v[56:59], v[120:123], v[20:23], v[56:59]\n\tds_read_b64 v[92:93], v31 offset:248\n\tds_read_b64 v[94:95], v31 offset:352\n\t" \
  "ds_read_b128 v[20:23], v28 offset:704\n\tv_mfma_f32_16x16x32_f16 v[52:55], v[156:159], v[0:3], v[52:55]\n\tds_read_b64 v[96:97], v31 offset:216\n\t" \
  "ds_read_b64 v[98:99], v31 offset:320\n\tv_mfma_f32_16x16x32_f16 v[56:59], v[124:127], v[0:3], v[56:59]\n\tds_read_b128 v[0:3], v28 offset:768\n\t" \
  "s_waitcnt lgkmcnt(15)\n\tv_mfma_f32_16x16x32_f16 v[52:55], v[160:163], v[4:7], v[52:55]\n\tv_mfma_f32_16x16x32_f16 v[56:59], v[128:131], v[4:7], v[56:59]\n\t" \
  "ds_read_b128 v[4:7], v28 offset:832\n\tv_mfma_f32_16x16x32_f16 v[52:55], v[164:167], v[8:11], v[52:55]\n\tv_mfma_f32_16x16x32_f16 v[56:59], v[132:135], v[8:11], v[56:59]\n\t" \
  "v_mfma_f32_16x16x32_f16 v[60:63], v[100:103], v[8:11], 0\n\tds_read_b128 v[8:11], v28 offset:896\n\ts_waitcnt lgkmcnt(15)\n\t" \
  "v_mfma_f32_16x16x32_f16 v[56:59], v[136:139], v[12:15], v[56:59]\n\tds_read_b32 v34, v30\n\tv_mfma_f32_16x16x32_f16 v[60:63], v[104:107], v[12:15], v[60:63]\n\t" \
  "ds_read_b128 v[12:15], v28 offset:960\n\tv_cvt_pk_bf16_f32 v52, v52, v53\n\tv_cvt_pk_bf16_f32 v53, v54, v55\n\t" \
  TS(64, 65) "s_waitcnt lgkmcnt(12)\n\tv_mfma_f32_16x16x32_f16 v[56:59], v[140:143], v[16:19], v[56:59]\n\t" \
  "ds_write_b64 v29, v[52:53] offset:0\n\tds_write_b32 v25, v25 offset:0\n\tv_mfma_f32_16x16x32_f16 v[60:63], v[108:111], v[16:19], v[60:63]\n\t" \
  "s_waitcnt lgkmcnt(3)\n\tv_cmp_gt_u32 vcc, 2, v34\n\ts_cbranch_vccz 2f\n\t" \
  "s_mov_b32 s6, 0x10000\n\t1:\n\ts_sleep 1\n\t" \
  "ds_read_b32 v34, v30\n\ts_sub_u32 s6, s6, 1\n\ts_waitcnt lgkmcnt(0)\n\t" \
  "v_cmp_gt_u32 vcc, 2, v34\n\ts_cbranch_scc1 2f\n\ts_cbranch_vccnz 1b\n\t" \
  "2:\n\tds_read_b128 v[16:19], v28 offset:1024\n\tv_mfma_f32_16x16x32_f16 v[56:59], v[144:147], v[20:23], v[56:59]\n\t" \
  "v_mfma_f32_16x16x32_f16 v[60:63], v[112:115], v[20:23], v[60:63]\n\tds_read_b128 v[68:71], v32\n\tds_read_b128 v[72:75], v32 offset:1024\n\t" \
  "ds_read_b128 v[20:23], v28 offset:1088\n\tv_mfma_f32_16x16x32_f16 v[56:59], v[148:151], v[0:3], v[56:59]\n\tv_mfma_f32_16x16x32_f16 v[60:63], v[116:119], v[0:3], v[60:63]\n\t" \
  "ds_read_b128 v[0:3], v28 offset:496\n\tv_mfma_f32_16x16x32_f16 v[56:59], v[152:155], v[4:7], v[56:59]\n\tv_mfma_f32_16x16x32_f16 v[60:63], v[120:123], v[4:7], v[60:63]\n\t" \
  "ds_read_b128 v[4:7], v28 offset:1152\n\tv_mfma_f32_16x16x32_f16 v[56:59], v[156:159], v[8:11], v[56:59]\n\tv_mfma_f32_16x16x32_f16 v[60:63], v[124:127], v[8:11], v[60:63]\n\t" \
  "ds_read_b128 v[8:11], v28 offset:1216\n\ts_waitcnt lgkmcnt(9)\n\tv_mfma_f32_16x16x32_f16 v[56:59], v[160:163], v[12:15], v[56:59]\n\t" \
  "v_mfma_f32_16x16x32_f16 v[60:63], v[128:131], v[12:15], v[60:63]\n\tds_read_b128 v[12:15], v28 offset:480\n\ts_waitcnt lgkmcnt(7)\n\t" \
  "v_mfma_f32_16x16x32_f16 v[56:59], v[164:167], v[16:19], v[56:59]\n\tv_mfma_f32_16x16x32_f16 v[60:63], v[132:135], v[16:19], v[60:63]\n\tv_mfma_f32_16x16x32_f16 v[64:67], v[100:103], v[16:19], 0\n\t" \
  "ds_read_b128 v[16:19], v28 offset:1280\n\ts_waitcnt lgkmcnt(5)\n\tv_mfma_f32_16x16x32_f16 v[60:63], v[136:139], v[20:23], v[60:63]\n\t" \
  "v_mfma_f32_16x16x32_f16 v[64:67], v[104:107], v[20:23], v[64:67]\n\tds_read_b128 v[20:23], v28 offset:1344\n\ts_waitcnt lgkmcnt(5)\n\t" \
  "v_mfma_f32_16x16x32_f16 v[36:39], v[0:3], v[68:71], 0\n\tv_cvt_pk_bf16_f32 v56, v56, v57\n\tv_cvt_pk_bf16_f32 v57, v58, v59\n\t" \
  "v_mfma_f32_16x16x32_f16 v[40:43], v[0:3], v[72:75], 0\n\tds_write_b64 v29, v[56:57] offset:512\n\tds_write_b32 v25, v25 offset:64\n\t" \
  "ds_read_b128 v[0:3], v28 offset:1408\n\ts_waitcnt lgkmcnt(7)\n\tv_mfma_f32_16x16x32_f16 v[60:63], v[140:143], v[4:7], v[60:63]\n\t" \
  "v_cvt_pk_f16_f32 v36, v36, v37\n\tv_cvt_pk_f16_f32 v37, v38, v39\n\tv_mfma_f32_16x16x32_f16 v[64:67], v[108:111], v[4:7], v[64:67]\n\t" \
  "ds_read_b128 v[4:7], v28 offset:1472\n\tv_cvt_pk_f16_f32 v38, v40, v41\n\tv_cvt_pk_f16_f32 v39, v42, v43\n\t" \
  "s_waitcnt lgkmcnt(7)\n\tv_mfma_f32_16x16x32_f16 v[60:63], v[144:147], v[8:11], v[60:63]\n\tv_mfma_f32_16x16x32_f16 v[64:67], v[112:115], v[8:11], v[64:67]\n\t" \
  "ds_read_b128 v[8:11], v28 offset:1536\n\tv_mfma_f32_16x16x32_f16 v[60:63], v[36:39], v[76:79], v[60:63]\n\tv_mfma_f32_16x16x32_f16 v[64:67], v[36:39], v[80:83], v[64:67]\n\t" \
  "v_mfma_f32_16x16x32_f16 v[52:55], v[36:39], v[84:87], 0\n\ts_waitcnt lgkmcnt(7)\n\tv_mfma_f32_16x16x32_f16 v[44:47], v[12:15], v[68:71], 0\n\t" \
  "v_mfma_f32_16x16x32_f16 v[40:43], v[12:15], v[72:75], 0\n\tds_read_b128 v[12:15], v28 offset:1600\n\ts_waitcnt lgkmcnt(7)\n\t" \
  "v_mfma_f32_16x16x32_f16 v[60:63], v[148:151], v[16:19], v[60:63]\n\tv_mfma_f32_16x16x32_f16 v[64:67], v[116:119], v[16:19], v[64:67]\n\tds_read_b128 v[16:19], v28 offset:1008\n\t" \
  "s_waitcnt lgkmcnt(7)\n\tv_mfma_f32_16x16x32_f16 v[60:63], v[152:155], v[20:23], v[60:63]\n\tv_cvt_pk_f16_f32 v44, v44, v45\n\t" \
  "v_cvt_pk_f16_f32 v45, v46, v47\n\tv_cvt_pk_f16_f32 v46, v40, v41\n\tv_cvt_pk_f16_f32 v47, v42, v43\n\t" \
  "v_mfma_f32_16x16x32_f16 v[64:67], v[120:123], v[20:23], v[64:67]\n\tds_read_b128 v[20:23], v28 offset:1664\n\tv_mfma_f32_16x16x32_f16 v[60:63], v[44:47], v[88:91], v[60:63]\n\t" \
  "v_mfma_f32_16x16x32_f16 v[64:67], v[44:47], v[92:95], v[64:67]\n\tv_mfma_f32_16x16x32_f16 v[52:55], v[44:47], v[96:99], v[52:55]\n\ts_waitcnt lgkmcnt(5)\n\t" \
  "v_mfma_f32_16x16x32_f16 v[60:63], v[156:159], v[0:3], v[60:63]\n\tv_mfma_f32_16x16x32_f16 v[64:67], v[124:127], v[0:3], v[64:67]\n\tds_read_b128 v[0:3], v28 offset:1728\n\t" \
  "s_waitcnt lgkmcnt(5)\n\tv_mfma_f32_16x16x32_f16 v[60:63], v[160:163], v[4:7], v[60:63]\n\tv_mfma_f32_16x16x32_f16 v[64:67], v[128:131], v[4:7], v[64:67]\n\t" \
  "ds_read_b128 v[4:7], v28 offset:992\n\ts_waitcnt lgkmcnt(5)\n\tv_mfma_f32_16x16x32_f16 v[60:63], v[164:167], v[8:11], v[60:63]\n\t" \
  "v_mfma_f32_16x16x32_f16 v[64:67], v[132:135], v[8:11], v[64:67]\n\tv_mfma_f32_16x16x32_f16 v[52:55], v[100:103], v[8:11], v[52:55]\n\tds_read_b128 v[8:11], v28 offset:1792\n\t" \
  "s_waitcnt lgkmcnt(5)\n\tv_mfma_f32_16x16x32_f16 v[64:67], v[136:139], v[12:15], v[64:67]\n\tv_mfma_f32_16x16x32_f16 v[52:55], v[104:107], v[12:15], v[52:55]\n\t" \
  "ds_read_b128 v[12:15], v28 offset:1856\n\ts_waitcnt lgkmcnt(5)\n\tv_mfma_f32_16x16x32_f16 v[36:39], v[16:19], v[68:71], 0\n\t" \
  "v_cvt_pk_bf16_f32 v60, v60, v61\n\tv_cvt_pk_bf16_f32 v61, v62, v63\n\tv_mfma_f32_16x16x32_f16 v[40:43], v[16:19], v[72:75], 0\n\t" \
  "ds_write_b64 v29, v[60:61] offset:1024\n\tds_write_b32 v25, v25 offset:128\n\tds_read_b128 v[16:19], v28 offset:1920\n\t" \
  "s_waitcnt lgkmcnt(7)\n\tv_mfma_f32_16x16x32_f16 v[64:67], v[140:143], v[20:23], v[64:67]\n\tds_read_b32 v34, v30\n\t" \
  "v_cvt_pk_f16_f32 v36, v36, v37\n\tv_cvt_pk_f16_f32 v37, v38, v39\n\tv_mfma_f32_16x16x32_f16 v[52:55], v[108:111], v[20:23], v[52:55]\n\t" \
  "ds_read_b128 v[20:23], v28 offset:1984\n\tv_cvt_pk_f16_f32 v38, v40, v41\n\tv_cvt_pk_f16_f32 v39, v42, v43\n\t" \
  "s_waitcnt lgkmcnt(8)\n\tv_mfma_f32_16x16x32_f16 v[64:67], v[144:147], v[0:3], v[64:67]\n\tv_mfma_f32_16x16x32_f16 v[52:55], v[112:115], v[0:3], v[52:55]\n\t" \
  "s_waitcnt lgkmcnt(1)\n\tv_cmp_gt_u32 vcc, 3, v34\n\ts_cbranch_vccz 2f\n\t" \
  "s_mov_b32 s6, 0x10000\n\t1:\n\ts_sleep 1\n\t" \
  "ds_read_b32 v34, v30\n\ts_sub_u32 s6, s6, 1\n\ts_waitcnt lgkmcnt(0)\n\t" \
  "v_cmp_gt_u32 vcc, 3, v34\n\ts_cbranch_scc1 2f\n\ts_cbranch_vccnz 1b\n\t" \
  "2:\n\tds_read_b128 v[0:3], v28 offset:2048\n\tv_mfma_f32_16x16x32_f16 v[64:67], v[36:39], v[76:79], v[64:67]\n\t" \
  "v_mfma_f32_16x16x32_f16 v[52:55], v[36:39], v[80:83], v[52:55]\n\tv_mfma_f32_16x16x32_f16 v[56:59], v[36:39], v[84:87], 0\n\tv_mfma_f32_16x16x32_f16 v[44:47], v[4:7], v[68:71], 0\n\t" \
  "v_mfma_f32_16x16x32_f16 v[40:43], v[4:7], v[72:75], 0\n\tds_read_b128 v[4:7], v28 offset:2112\n\tv_mfma_f32_16x16x32_f16 v[64:67], v[148:151], v[8:11], v[64:67]\n\t" \
  "v_mfma_f32_16x16x32_f16 v[52:55], v[116:119], v[8:11], v[52:55]\n\tds_read_b128 v[8:11], v28 offset:1520\n\tv_mfma_f32_16x16x32_f16 v[64:67], v[152:155], v[12:15], v[64:67]\n\t" \
  "v_mfma_f32_16x16x32_f16 v[52:55], v[120:123], v[12:15], v[52:55]\n\tds_read_b128 v[12:15], v28 offset:2176\n\tv_cvt_pk_f16_f32 v44, v44, v45\n\t" \
  "v_cvt_pk_f16_f32 v45, v46, v47\n\tv_cvt_pk_f16_f32 v46, v40, v41\n\tv_cvt_pk_f16_f32 v47, v42, v43\n\t" \
  "s_nop 1\n\tv_mfma_f32_16x16x32_f16 v[64:67], v[44:47], v[88:91], v[64:67]\n\tv_mfma_f32_16x16x32_f16 v[52:55], v[44:47], v[92:95], v[52:55]\n\t" \
  "v_mfma_f32_16x16x32_f16 v[56:59], v[44:47], v[96:99], v[56:59]\n\tv_mfma_f32_16x16x32_f16 v[64:67], v[156:159], v[16:19], v[64:67]\n\tv_mfma_f32_16x16x32_f16 v[52:55], v[124:127], v[16:19], v[52:55]\n\t" \
  "ds_read_b128 v[16:19], v28 offset:2240\n\ts_waitcnt lgkmcnt(5)\n\tv_mfma_f32_16x16x32_f16 v[64:67], v[160:163], v[20:23], v[64:67]\n\t" \
  "v_mfma_f32_16x16x32_f16 v[52:55], v[128:131], v[20:23], v[52:55]\n\tds_read_b128 v[20:23], v28 offset:1504\n\ts_waitcnt lgkmcnt(5)\n\t" \
  "v_mfma_f32_16x16x32_f16 v[64:67], v[164:167], v[0:3], v[64:67]\n\tv_mfma_f32_16x16x32_f16 v[52:55], v[132:135], v[0:3], v[52:55]\n\tv_mfma_f32_16x16x32_f16 v[56:59], v[100:103], v[0:3], v[56:59]\n\t" \
  "ds_read_b128 v[0:3], v28 offset:2304\n\ts_waitcnt lgkmcnt(5)\n\tv_mfma_f32_16x16x32_f16 v[52:55], v[136:139], v[4:7], v[52:55]\n\t" \
  "v_mfma_f32_16x16x32_f16 v[56:59], v[104:107], v[4:7], v[56:59]\n\tds_read_b128 v[4:7], v28 offset:2368\n\ts_waitcnt lgkmcnt(5)\n\t" \
  "v_mfma_f32_16x16x32_f16 v[36:39], v[8:11], v[68:71], 0\n\tv_cvt_pk_bf16_f32 v64, v64, v65\n\tv_cvt_pk_bf16_f32 v65, v66, v67\n\t" \
  TS(66, 67) "s_cmp_lt_u32 %[w], 4\n\ts_cbranch_scc1 1f\n\t" \
  "s_setprio 1\n\t1:\n\tv_mfma_f32_16x16x32_f16 v[40:43], v[8:11], v[72:75], 0\n\t" \
  "ds_write_b64 v29, v[64:65] offset:1536\n\tds_write_b32 v25, v25 offset:192\n\tds_read_b128 v[8:11], v28 offset:480\n\t" \
  "s_waitcnt lgkmcnt(7)\n\tv_mfma_f32_16x16x32_f16 v[52:55], v[140:143], v[12:15], v[52:55]\n\tv_cvt_pk_f16_f32 v36, v36, v37\n\t" \
  "v_cvt_pk_f16_f32 v37, v38, v39\n\tv_mfma_f32_16x16x32_f16 v[56:59], v[108:111], v[12:15], v[56:59]\n\tds_read_b128 v[12:15], v32 offset:3072\n\t" \
  "v_cvt_pk_f16_f32 v38, v40, v41\n\tv_cvt_pk_f16_f32 v39, v42, v43\n\ts_waitcnt lgkmcnt(7)\n\t" \
  "v_mfma_f32_16x16x32_f16 v[52:55], v[144:147], v[16:19], v[52:55]\n\tv_mfma_f32_16x16x32_f16 v[56:59], v[112:115], v[16:19], v[56:59]\n\tds_read_b128 v[16:19], v28 offset:496\n\t" \
  "v_mfma_f32_16x16x32_f16 v[52:55], v[36:39], v[76:79], v[52:55]\n\tv_mfma_f32_16x16x32_f16 v[56:59], v[36:39], v[80:83], v[56:59]\n\tv_mfma_f32_16x16x32_f16 v[60:63], v[36:39], v[84:87], 0\n\t" \
  "s_waitcnt lgkmcnt(7)\n\tv_mfma_f32_16x16x32_f16 v[44:47], v[20:23], v[68:71], 0\n\tv_mfma_f32_16x16x32_f16 v[40:43], v[20:23], v[72:75], 0\n\t" \
  "ds_read_b128 v[20:23], v32 offset:2048\n\ts_waitcnt lgkmcnt(7)\n\tv_mfma_f32_16x16x32_f16 v[52:55], v[148:151], v[0:3], v[52:55]\n\t" \
  "v_mfma_f32_16x16x32_f16 v[56:59], v[116:119], v[0:3], v[56:59]\n\tds_read_b128 v[0:3], v28 offset:2432\n\ts_waitcnt lgkmcnt(7)\n\t" \
  "v_mfma_f32_16x16x32_f16 v[52:55], v[152:155], v[4:7], v[52:55]\n\tv_cvt_pk_f16_f32 v44, v44, v45\n\tv_cvt_pk_f16_f32 v45, v46, v47\n\t" \
  "v_cvt_pk_f16_f32 v46, v40, v41\n\tv_cvt_pk_f16_f32 v47, v42, v43\n\tv_mfma_f32_16x16x32_f16 v[56:59], v[120:123], v[4:7], v[56:59]\n\t" \
  "ds_read_b128 v[4:7], v28 offset:2496\n\tv_mfma_f32_16x16x32_f16 v[52:55], v[44:47], v[88:91], v[52:55]\n\tv_mfma_f32_16x16x32_f16 v[56:59], v[44:47], v[92:95], v[56:59]\n\t" \
  "v_mfma_f32_16x16x32_f16 v[60:63], v[44:47], v[96:99], v[60:63]\n\ts_waitcnt lgkmcnt(4)\n\tv_mfma_f32_16x16x32_f16 v[40:43], v[8:11], v[12:15], 0\n\t" \
  "ds_read_b64 v[8:9], v31 offset:544\n\tds_read_b64 v[10:11], v31 offset:712\n\tds_read_b64 v[12:13], v31 offset:512\n\t" \
  "ds_read_b64 v[14:15], v31 offset:680\n\ts_waitcnt lgkmcnt(7)\n\tv_mfma_f32_16x16x32_f16 v[52:55], v[48:51], v[16:19], v[52:55]\n\t" \
  "s_waitcnt lgkmcnt(6)\n\tv_mfma_f32_16x16x32_f16 v[36:39], v[16:19], v[20:23], 0\n\tds_read_b64 v[16:17], v31 offset:480\n\t" \
  "ds_read_b64 v[18:19], v31 offset:648\n\tds_read_b64 v[20:21], v31 offset:448\n\tds_read_b64 v[22:23], v31 offset:616\n\t" \
  "s_waitcnt lgkmcnt(9)\n\tv_mfma_f32_16x16x32_f16 v[52:55], v[156:159], v[0:3], v[52:55]\n\tv_mfma_f32_16x16x32_f16 v[56:59], v[124:127], v[0:3], v[56:59]\n\t" \
  "ds_read_b128 v[0:3], v28 offset:2560\n\tv_cvt_pk_f16_f32 v36, v36, v37\n\tv_cvt_pk_f16_f32 v37, v38, v39\n\t" \
  "s_waitcnt lgkmcnt(9)\n\tv_mfma_f32_16x16x32_f16 v[52:55], v[160:163], v[4:7], v[52:55]\n\tv_cvt_pk_f16_f32 v38, v40, v41\n\t" \
  "v_cvt_pk_f16_f32 v39, v42, v43\n\tv_mfma_f32_16x16x32_f16 v[56:59], v[128:131], v[4:7], v[56:59]\n\tds_read_b128 v[4:7], v28 offset:2624\n\t" \
  "s_waitcnt lgkmcnt(8)\n\tv_mfma_f32_16x16x32_f16 v[52:55], v[36:39], v[8:11], v[52:55]\n\tds_read_b128 v[8:11], v28 offset:2032\n\t" \
  "s_waitcnt lgkmcnt(7)\n\tv_mfma_f32_16x16x32_f16 v[56:59], v[36:39], v[12:15], v[56:59]\n\tds_read_b128 v[12:15], v28 offset:2688\n\t" \
  "s_waitcnt lgkmcnt(6)\n\tv_mfma_f32_16x16x32_f16 v[60:63], v[36:39], v[16:19], v[60:63]\n\tds_read_b128 v[16:19], v28 offset:2752\n\t" \
  "s_waitcnt lgkmcnt(5)\n\tv_mfma_f32_16x16x32_f16 v[64:67], v[36:39], v[20:23], 0\n\tds_read_b128 v[20:23], v28 offset:2016\n\t" \
  "s_waitcnt lgkmcnt(5)\n\tv_mfma_f32_16x16x32_f16 v[52:55], v[164:167], v[0:3], v[52:55]\n\tv_mfma_f32_16x16x32_f16 v[56:59], v[132:135], v[0:3], v[56:59]\n\t" \
  "v_mfma_f32_16x16x32_f16 v[60:63], v[100:103], v[0:3], v[60:63]\n\tds_read_b128 v[0:3], v28 offset:2816\n\ts_waitcnt lgkmcnt(5)\n\t" \
  "v_mfma_f32_16x16x32_f16 v[56:59], v[136:139], v[4:7], v[56:59]\n\tv_mfma_f32_16x16x32_f16 v[60:63], v[104:107], v[4:7], v[60:63]\n\tds_read_b128 v[4:7], v28 offset:2880\n\t" \
  "s_waitcnt lgkmcnt(5)\n\tv_mfma_f32_16x16x32_f16 v[44:47], v[8:11], v[68:71], 0\n\tv_cvt_pk_bf16_f32 v52, v52, v53\n\t" \
  "v_cvt_pk_bf16_f32 v53, v54, v55\n\ts_cmp_lt_u32 %[w], 4\n\ts_cbranch_scc0 1f\n\t" \
  "s_setprio 0\n\t1:\n\tv_mfma_f32_16x16x32_f16 v[40:43], v[8:11], v[72:75], 0\n\t" \
  "ds_write_b64 v29, v[52:53] offset:2048\n\tds_write_b32 v25, v25 offset:256\n\tds_read_b128 v[8:11], v28 offset:992\n\t" \
  "s_waitcnt lgkmcnt(7)\n\tv_mfma_f32_16x16x32_f16 v[56:59], v[140:143], v[12:15], v[56:59]\n\tv_cvt_pk_f16_f32 v44, v44, v45\n\t" \
  "v_cvt_pk_f16_f32 v45, v46, v47\n\tv_mfma_f32_16x16x32_f16 v[60:63], v[108:111], v[12:15], v[60:63]\n\tds_read_b128 v[12:15], v32 offset:3072\n\t" \
  "v_cvt_pk_f16_f32 v46, v40, v41\n\tv_cvt_pk_f16_f32 v47, v42, v43\n\ts_waitcnt lgkmcnt(7)\n\t" \
  "v_mfma_f32_16x16x32_f16 v[56:59], v[144:147], v[16:19], v[56:59]\n\tv_mfma_f32_16x16x32_f16 v[60:63], v[112:115], v[16:19], v[60:63]\n\tds_read_b128 v[16:19], v28 offset:1008\n\t" \
  "v_mfma_f32_16x16x32_f16 v[56:59], v[44:47], v[76:79], v[56:59]\n\tv_mfma_f32_16x16x32_f16 v[60:63], v[44:47], v[80:83], v[60:63]\n\tv_mfma_f32_16x16x32_f16 v[64:67], v[44:47], v[84:87], v[64:67]\n\t" \
  "s_waitcnt lgkmcnt(7)\n\tv_mfma_f32_16x16x32_f16 v[36:39], v[20:23], v[68:71], 0\n\tv_mfma_f32_16x16x32_f16 v[40:43], v[20:23], v[72:75], 0\n\t" \
  "ds_read_b128 v[20:23], v32 offset:2048\n\ts_waitcnt lgkmcnt(7)\n\tv_mfma_f32_16x16x32_f16 v[56:59], v[148:151], v[0:3], v[56:59]\n\t" \
  "v_mfma_f32_16x16x32_f16 v[60:63], v[116:119], v[0:3], v[60:63]\n\tds_read_b128 v[0:3], v28 offset:2944\n\ts_waitcnt lgkmcnt(7)\n\t" \
  "v_mfma_f32_16x16x32_f16 v[56:59], v[152:155], v[4:7], v[56:59]\n\tv_cvt_pk_f16_f32 v36, v36, v37\n\tv_cvt_pk_f16_f32 v37, v38, v39\n\t" \
  "v_cvt_pk_f16_f32 v38, v40, v41\n\tv_cvt_pk_f16_f32 v39, v42, v43\n\tv_mfma_f32_16x16x32_f16 v[60:63], v[120:123], v[4:7], v[60:63]\n\t" \
  "ds_read_b128 v[4:7], v28 offset:3008\n\tv_mfma_f32_16x16x32_f16 v[56:59], v[36:39], v[88:91], v[56:59]\n\tv_mfma_f32_16x16x32_f16 v[60:63], v[36:39], v[92:95], v[60:63]\n\t" \
  "v_mfma_f32_16x16x32_f16 v[64:67], v[36:39], v[96:99], v[64:67]\n\tds_read_b32 v34, v30\n\ts_waitcnt lgkmcnt(5)\n\t" \
  "v_mfma_f32_16x16x32_f16 v[40:43], v[8:11], v[12:15], 0\n\tds_read_b64 v[8:9], v31 offset:544\n\tds_read_b64 v[10:11], v31 offset:712\n\t" \
  "ds_read_b64 v[12:13], v31 offset:512\n\tds_read_b64 v[14:15], v31 offset:680\n\ts_waitcnt lgkmcnt(8)\n\t" \
  "v_mfma_f32_16x16x32_f16 v[56:59], v[48:51], v[16:19], v[56:59]\n\ts_waitcnt lgkmcnt(7)\n\tv_mfma_f32_16x16x32_f16 v[44:47], v[16:19], v[20:23], 0\n\t" \
  "ds_read_b64 v[16:17], v31 offset:480\n\tds_read_b64 v[18:19], v31 offset:648\n\ts_waitcnt lgkmcnt(6)\n\t" \
  "v_cmp_gt_u32 vcc, 4, v34\n\ts_cbranch_vccz 2f\n\ts_mov_b32 s6, 0x10000\n\t" \
  "1:\n\ts_sleep 1\n\tds_read_b32 v34, v30\n\t" \
  "s_sub_u32 s6, s6, 1\n\ts_waitcnt lgkmcnt(0)\n\tv_cmp_gt_u32 vcc, 4, v34\n\t" \
  "s_cbranch_scc1 2f\n\ts_cbranch_vccnz 1b\n\t2:\n\t" \
  "ds_read_b128 v[20:23], v28 offset:3072\n\tv_mfma_f32_16x16x32_f16 v[56:59], v[156:159], v[0:3], v[56:59]\n\tv_mfma_f32_16x16x32_f16 v[60:63], v[124:127], v[0:3], v[60:63]\n\t" \
  "ds_read_b128 v[0:3], v28 offset:3136\n\tv_cvt_pk_f16_f32 v44, v44, v45\n\tv_cvt_pk_f16_f32 v45, v46, v47\n\t" \
  "v_mfma_f32_16x16x32_f16 v[56:59], v[160:163], v[4:7], v[56:59]\n\tv_cvt_pk_f16_f32 v46, v40, v41\n\tv_cvt_pk_f16_f32 v47, v42, v43\n\t" \
  "v_mfma_f32_16x16x32_f16 v[60:63], v[128:131], v[4:7], v[60:63]\n\tds_read_b128 v[4:7], v28 offset:2544\n\ts_waitcnt lgkmcnt(7)\n\t" \
  "v_mfma_f32_16x16x32_f16 v[56:59], v[44:47], v[8:11], v[56:59]\n\tds_read_b128 v[8:11], v28 offset:3200\n\ts_waitcnt lgkmcnt(6)\n\t" \
  "v_mfma_f32_16x16x32_f16 v[60:63], v[44:47], v[12:15], v[60:63]\n\tds_read_b128 v[12:15], v28 offset:3264\n\ts_waitcnt lgkmcnt(5)\n\t" \
  "v_mfma_f32_16x16x32_f16 v[64:67], v[44:47], v[16:19], v[64:67]\n\tds_read_b128 v[16:19], v28 offset:2528\n\ts_waitcnt lgkmcnt(5)\n\t" \
  "v_mfma_f32_16x16x32_f16 v[56:59], v[164:167], v[20:23], v[56:59]\n\tv_mfma_f32_16x16x32_f16 v[60:63], v[132:135], v[20:23], v[60:63]\n\tv_mfma_f32_16x16x32_f16 v[64:67], v[100:103], v[20:23], v[64:67]\n\t" \
  "ds_read_b128 v[20:23], v28 offset:3328\n\ts_waitcnt lgkmcnt(5)\n\tv_mfma_f32_16x16x32_f16 v[60:63], v[136:139], v[0:3], v[60:63]\n\t" \
  "v_mfma_f32_16x16x32_f16 v[64:67], v[104:107], v[0:3], v[64:67]\n\tds_read_b128 v[0:3], v28 offset:3392\n\ts_waitcnt lgkmcnt(5)\n\t" \
  "v_mfma_f32_16x16x32_f16 v[36:39], v[4:7], v[68:71], 0\n\tv_cvt_pk_bf16_f32 v56, v56, v57\n\tv_cvt_pk_bf16_f32 v57, v58, v59\n\t" \
  "s_cmp_lt_u32 %[w], 4\n\ts_cbranch_scc1 1f\n\ts_setprio 0\n\t" \
  "1:\n\tv_mfma_f32_16x16x32_f16 v[40:43], v[4:7], v[72:75], 0\n\tds_write_b64 v29, v[56:57] offset:2560\n\t" \
  "ds_write_b32 v25, v25 offset:320\n\tds_read_b128 v[4:7], v28 offset:1504\n\ts_waitcnt lgkmcnt(7)\n\t" \
  "v_mfma_f32_16x16x32_f16 v[60:63], v[140:143], v[8:11], v[60:63]\n\tv_cvt_pk_f16_f32 v36, v36, v37\n\tv_cvt_pk_f16_f32 v37, v38, v39\n\t" \
  "v_mfma_f32_16x16x32_f16 v[64:67], v[108:111], v[8:11], v[64:67]\n\tds_read_b128 v[8:11], v32 offset:3072\n\tv_cvt_pk_f16_f32 v38, v40, v41\n\t" \
  "v_cvt_pk_f16_f32 v39, v42, v43\n\ts_waitcnt lgkmcnt(7)\n\tv_mfma_f32_16x16x32_f16 v[60:63], v[144:147], v[12:15], v[60:63]\n\t" \
  "v_mfma_f32_16x16x32_f16 v[64:67], v[112:115], v[12:15], v[64:67]\n\tds_read_b128 v[12:15], v28 offset:1520\n\tv_mfma_f32_16x16x32_f16 v[60:63], v[36:39], v[76:79], v[60:63]\n\t" \
  "v_mfma_f32_16x16x32_f16 v[64:67], v[36:39], v[80:83], v[64:67]\n\ts_waitcnt lgkmcnt(7)\n\tv_mfma_f32_16x16x32_f16 v[44:47], v[16:19], v[68:71], 0\n\t" \
  "v_mfma_f32_16x16x32_f16 v[40:43], v[16:19], v[72:75], 0\n\tds_read_b128 v[16:19], v32 offset:2048\n\ts_waitcnt lgkmcnt(7)\n\t" \
  "v_mfma_f32_16x16x32_f16 v[60:63], v[148:151], v[20:23], v[60:63]\n\tv_mfma_f32_16x16x32_f16 v[64:67], v[116:119], v[20:23], v[64:67]\n\tds_read_b128 v[20:23], v28 offset:3456\n\t" \
  "s_waitcnt lgkmcnt(7)\n\tv_mfma_f32_16x16x32_f16 v[60:63], v[152:155], v[0:3], v[60:63]\n\tv_cvt_pk_f16_f32 v44, v44, v45\n\t" \
  "v_cvt_pk_f16_f32 v45, v46, v47\n\tv_cvt_pk_f16_f32 v46, v40, v41\n\tv_cvt_pk_f16_f32 v47, v42, v43\n\t" \
  "v_mfma_f32_16x16x32_f16 v[64:67], v[120:123], v[0:3], v[64:67]\n\tds_read_b128 v[0:3], v28 offset:3520\n\tv_mfma_f32_16x16x32_f16 v[60:63], v[44:47], v[88:91], v[60:63]\n\t" \
  "v_mfma_f32_16x16x32_f16 v[64:67], v[44:47], v[92:95], v[64:67]\n\ts_waitcnt lgkmcnt(4)\n\tv_mfma_f32_16x16x32_f16 v[40:43], v[4:7], v[8:11], 0\n\t" \
  "ds_read_b64 v[4:5], v31 offset:544\n\tds_read_b64 v[6:7], v31 offset:712\n\tds_read_b64 v[8:9], v31 offset:512\n\t" \
  "ds_read_b64 v[10:11], v31 offset:680\n\ts_waitcnt lgkmcnt(7)\n\tv_mfma_f32_16x16x32_f16 v[60:63], v[48:51], v[12:15], v[60:63]\n\t" \
  "s_waitcnt lgkmcnt(6)\n\tv_mfma_f32_16x16x32_f16 v[36:39], v[12:15], v[16:19], 0\n\tds_read_b128 v[12:15], v28 offset:3584\n\t" \
  "ds_read_b128 v[16:19], v28 offset:3648\n\ts_waitcnt lgkmcnt(7)\n\tv_mfma_f32_16x16x32_f16 v[60:63], v[156:159], v[20:23], v[60:63]\n\t" \
  "v_mfma_f32_16x16x32_f16 v[64:67], v[124:127], v[20:23], v[64:67]\n\tds_read_b128 v[20:23], v28 offset:3056\n\ts_waitcnt lgkmcnt(7)\n\t" \
  "v_mfma_f32_16x16x32_f16 v[60:63], v[160:163], v[0:3], v[60:63]\n\tv_cvt_pk_f16_f32 v36, v36, v37\n\tv_cvt_pk_f16_f32 v37, v38, v39\n\t" \
  "v_cvt_pk_f16_f32 v38, v40, v41\n\tv_cvt_pk_f16_f32 v39, v42, v43\n\tv_mfma_f32_16x16x32_f16 v[64:67], v[128:131], v[0:3], v[64:67]\n\t" \
  "ds_read_b128 v[0:3], v28 offset:3712\n\ts_waitcnt lgkmcnt(6)\n\tv_mfma_f32_16x16x32_f16 v[60:63], v[36:39], v[4:7], v[60:63]\n\t" \
  "ds_read_b128 v[4:7], v28 offset:3776\n\ts_waitcnt lgkmcnt(5)\n\tv_mfma_f32_16x16x32_f16 v[64:67], v[36:39], v[8:11], v[64:67]\n\t" \
  "ds_read_b128 v[8:11], v28 offset:3040\n\ts_waitcnt lgkmcnt(5)\n\tv_mfma_f32_16x16x32_f16 v[60:63], v[164:167], v[12:15], v[60:63]\n\t" \
  "v_mfma_f32_16x16x32_f16 v[64:67], v[132:135], v[12:15], v[64:67]\n\tds_read_b128 v[12:15], v28 offset:3840\n\ts_waitcnt lgkmcnt(5)\n\t" \
  "v_mfma_f32_16x16x32_f16 v[64:67], v[136:139], v[16:19], v[64:67]\n\tds_read_b128 v[16:19], v28 offset:3904\n\ts_waitcnt lgkmcnt(5)\n\t" \
  "v_mfma_f32_16x16x32_f16 v[44:47], v[20:23], v[68:71], 0\n\tv_mfma_f32_16x16x32_f16 v[40:43], v[20:23], v[72:75], 0\n\tds_read_b128 v[20:23], v28 offset:2016\n\t" \
  "v_cvt_pk_bf16_f32 v60, v60, v61\n\tv_cvt_pk_bf16_f32 v61, v62, v63\n\ts_waitcnt lgkmcnt(5)\n\t" \
  "v_mfma_f32_16x16x32_f16 v[64:67], v[140:143], v[0:3], v[64:67]\n\tds_write_b64 v29, v[60:61] offset:3072\n\tds_write_b32 v25, v25 offset:384\n\t" \
  "ds_read_b128 v[0:3], v32 offset:3072\n\tv_cvt_pk_f16_f32 v44, v44, v45\n\tv_cvt_pk_f16_f32 v45, v46, v47\n\t" \
  "v_cvt_pk_f16_f32 v46, v40, v41\n\tv_cvt_pk_f16_f32 v47, v42, v43\n\ts_waitcnt lgkmcnt(7)\n\t" \
  "v_mfma_f32_16x16x32_f16 v[64:67], v[144:147], v[4:7], v[64:67]\n\tds_read_b128 v[4:7], v28 offset:2032\n\tv_mfma_f32_16x16x32_f16 v[64:67], v[44:47], v[76:79], v[64:67]\n\t" \
  "s_waitcnt lgkmcnt(7)\n\tv_mfma_f32_16x16x32_f16 v[36:39], v[8:11], v[68:71], 0\n\tv_mfma_f32_16x16x32_f16 v[40:43], v[8:11], v[72:75], 0\n\t" \
  "ds_read_b128 v[8:11], v32 offset:2048\n\ts_waitcnt lgkmcnt(7)\n\tv_mfma_f32_16x16x32_f16 v[64:67], v[148:151], v[12:15], v[64:67]\n\t" \
  "ds_read_b128 v[12:15], v28 offset:3968\n\ts_waitcnt lgkmcnt(7)\n\tv_mfma_f32_16x16x32_f16 v[64:67], v[152:155], v[16:19], v[64:67]\n\t" \
  "ds_read_b128 v[16:19], v28 offset:4032\n\tv_cvt_pk_f16_f32 v36, v36, v37\n\tv_cvt_pk_f16_f32 v37, v38, v39\n\t" \
  "v_cvt_pk_f16_f32 v38, v40, v41\n\tv_cvt_pk_f16_f32 v39, v42, v43\n\ts_nop 1\n\t" \
  "v_mfma_f32_16x16x32_f16 v[64:67], v[36:39], v[88:91], v[64:67]\n\ts_waitcnt lgkmcnt(4)\n\tv_mfma_f32_16x16x32_f16 v[40:43], v[20:23], v[0:3], 0\n\t" \
  "ds_read_b64 v[0:1], v31 offset:544\n\tds_read_b64 v[2:3], v31 offset:712\n\tds_read_b128 v[20:23], v28 offset:4096\n\t" \
  "s_waitcnt lgkmcnt(6)\n\tv_mfma_f32_16x16x32_f16 v[64:67], v[48:51], v[4:7], v[64:67]\n\ts_waitcnt lgkmcnt(5)\n\t" \
  "v_mfma_f32_16x16x32_f16 v[44:47], v[4:7], v[8:11], 0\n\ts_waitcnt lgkmcnt(4)\n\tv_mfma_f32_16x16x32_f16 v[64:67], v[156:159], v[12:15], v[64:67]\n\t" \
  "s_waitcnt lgkmcnt(3)\n\tv_mfma_f32_16x16x32_f16 v[64:67], v[160:163], v[16:19], v[64:67]\n\ts_nop 3\n\t" \
  "v_cvt_pk_f16_f32 v44, v44, v45\n\tv_cvt_pk_f16_f32 v45, v46, v47\n\tv_cvt_pk_f16_f32 v46, v40, v41\n\t" \
  "v_cvt_pk_f16_f32 v47, v42, v43\n\ts_waitcnt lgkmcnt(1)\n\ts_nop 0\n\t" \
  "v_mfma_f32_16x16x32_f16 v[64:67], v[44:47], v[0:3], v[64:67]\n\ts_waitcnt lgkmcnt(0)\n\tv_mfma_f32_16x16x32_f16 v[64:67], v[164:167], v[20:23], v[64:67]\n\t" \
  "s_nop 7\n\tv_cvt_pk_bf16_f32 v64, v64, v65\n\tv_cvt_pk_bf16_f32 v65, v66, v67\n\t" \
  "ds_write_b64 v29, v[64:65] offset:3584\n\tds_write_b32 v25, v25 offset:448\n\t"
#define CONV_CLOBBER "v0", "v1", "v2", "v3", "v4", "v5", "v6", "v7", "v8", "v9", "v10", "v11", "v12", "v13", "v14", "v15", "v16", "v17", "v18", "v19", "v20", "v21", "v22", "v23", "v36", "v37", "v38", "v39", "v40", "v41", "v42", "v43", "v44", "v45", "v46", "v47", "v52", "v53", "v54", "v55", "v56", "v57", "v58", "v59", "v60", "v61", "v62", "v63", "v64", "v65", "v66", "v67", "v68", "v69", "v70", "v71", "v72", "v73", "v74", "v75", "v76", "v77", "v78", "v79", "v80", "v81", "v82", "v83", "v84", "v85", "v86", "v87", "v88", "v89", "v90", "v91", "v92", "v93", "v94", "v95", "v96", "v97", "v98", "v99", "v100", "v101", "v102", "v103", "v104", "v105", "v106", "v107", "v108", "v109", "v110", "v111", "v112", "v113", "v114", "v115", "v116", "v117", "v118", "v119", "v120", "v121", "v122", "v123", "v124", "v125", "v126", "v127", "v128", "v129", "v130", "v131", "v132", "v133", "v134", "v135", "v136", "v137", "v138", "v139", "v140", "v141", "v142", "v143", "v144", "v145", "v146", "v147", "v148", "v149", "v150", "v151", "v152", "v153", "v154", "v155", "v156", "v157", "v158", "v159", "v160", "v161", "v162", "v163", "v164", "v165", "v166", "v167"
#define G_CLOBBER "v8", "v9", "v10", "v11", "v12", "v13", "v14", "v15", "v16", "v17", "v18", "v19", "v20", "v21", "v22", "v23", "v24", "v25", "v26", "v27", "v28", "v29", "v30", "v32", "v33", "v34", "v35", "v36", "v37", "v38", "v39", "v40", "v41", "v42", "v43", "v44", "v45", "v46", "v47", "v48", "v49", "v50", "v51", "v52", "v53", "v54", "v55", "v56", "v57", "v58", "v59", "v60", "v61", "v62", "v63", "v64", "v65", "v66", "v67", "v68", "v69", "v70", "v71", "v72", "v73", "v74", "v75", "v76", "v77", "v78", "v79", "v80", "v81", "v82", "v83", "v84", "v85", "v86", "v87", "v88", "v89", "v90", "v91", "v92", "v93", "v94", "v95", "v96", "v97", "v98", "v99", "v100", "v101", "v102", "v103", "v104", "v105", "v106", "v107", "v108", "v109", "v110", "v111", "v112", "v113", "v114", "v115", "v116", "v117", "v118", "v119", "v120", "v121", "v122", "v123", "v124", "v125", "v126", "v127"
#define D_CLOBBER "v8", "v9", "v10", "v11", "v12", "v13", "v14", "v15", "v16", "v17", "v18", "v19", "v20", "v21", "v22", "v23", "v24", "v25", "v26", "v27", "v28", "v29", "v30", "v31"
__device__ __forceinline__ f32x2 pkfma(f32x2 a, f32x2 b, f32x2 c) {
  f32x2 r;
  asm("v_pk_fma_f32 %0, %1, %2, %3" : "=v"(r) : "v"(a), "v"(b), "v"(c));
  return r;
}
__device__ __forceinline__ float fmav(float a, float b, float c) {
  float r;
  asm("v_fma_f32 %0, %1, %2, %3" : "=v"(r) : "v"(a), "v"(b), "v"(c));
  return r;
}
__device__ __forceinline__ float fmac(float a, float b, float c) {
  asm("v_fmac_f32 %0, %1, %2" : "+v"(c) : "v"(a), "v"(b));
  return c;
}
__device__ __forceinline__ float fmas(float a, u32 s, float c) {
  asm("v_fmac_f32 %0, %2, %1" : "+v"(c) : "v"(a), "s"(s));
  return c;
}
template <int HI>
__device__ __forceinline__ f32x2 pkfmas(f32x2 a, f32x2 s, f32x2 c) {
  f32x2 r;
  if (HI) asm("v_pk_fma_f32 %0, %2, %1, %3 op_sel:[1,0,0] op_sel_hi:[1,1,1]" : "=v"(r) : "v"(a), "v"(s), "v"(c));
  else asm("v_pk_fma_f32 %0, %2, %1, %3 op_sel:[0,0,0] op_sel_hi:[0,1,1]" : "=v"(r) : "v"(a), "v"(s), "v"(c));
  return r;
}
#define RANK8(d0, d1, d2, d3, c0, c1, i0, f0, f1) "v_perm_b32 " d0 ", " d0 ", " d1 ", %[sg]\n\tv_perm_b32 " d2 ", " d2 ", " d3 ", %[sg]\n\t" \
  "v_bcnt_u32_b32 " d0 ", " d0 ", " i0 "\n\tv_bcnt_u32_b32 " c0 ", " d2 ", " d0 "\n\tv_bcnt_u32_b32 " c1 ", " d2 ", " d0 "\n\t" f0 "\n\t" \
  "v_permlane16_swap_b32 " c0 ", " c1 "\n\tv_add_u32 " d0 ", " c0 ", " c1 "\n\tv_add_u32 " d1 ", " c0 ", " c1 "\n\t" f1 "\n\t" \
  "v_permlane32_swap_b32 " d0 ", " d1 "\n\tv_add_u32 " c0 ", " d0 ", " d1 "\n\t"
__device__ __forceinline__ void ln_rows(u32x4 (&xv)[4], u32x8 wv, rsrc_t rs, u32& tag, int b, int w, int l, u32 st, u32 sg TLARG) {
  float s[2][3] = {}, c[2], sw = 0.f;
  u32 os = l < 16 ? O_SG + 128 * (b - 128) + 8 * l : 0x80000000u;
  u32 ot = l < 16 ? O_TOP + 8 * (b & 15) + TL * ((b >> 4) & 7) : l < 32 ? O_RG - 128 + 128 * (b - 128) + 8 * l : 0x80000000u, om = l < 16 ? TOPS / 8 : 0u, fm = l < 16 ? 0xff0u : 0u;
  u32 aw = (l & 16) ? st + 8 * w + (l >> 5) * 4 : st + 136, ac = st + 128, ar = st + 4 * (l & 15), aq = st + (l & 48), one = 1u;
  u32 kl = (2047 - (16 * (b - 128) + 2 * w + (l >> 5))) << 1;
  asm volatile("" : "+v"(os), "+v"(aw), "+v"(ac), "+v"(ar), "+v"(aq), "+v"(one), "+v"(kl), "+v"(ot), "+v"(om), "+v"(fm));
  SB();
  asm volatile("s_waitcnt vmcnt(4) lgkmcnt(0)" : "+{s44}"(tag)::"memory");
  asm volatile("" : "+{v[116:123]}"(wv));
  tag += 1u;
  *(volatile LDS u32*)(size_t)(st - 104) = tag;
  STAMP(12);
#pragma unroll
  for (int i = 0; i < 8; i++) sw = dot2b(wv[i], 0x3f803f80u, sw);
  sw += dppf<0x121, 0xf>(sw); sw += dppf<0x122, 0xf>(sw); sw += dppf<0x124, 0xf>(sw); sw += dppf<0x128, 0xf>(sw);
  sw = (rdl(sw, 0) + rdl(sw, 16)) + (rdl(sw, 32) + rdl(sw, 48));
  asm volatile("" : "+v"(sw));
  SB();
  float cv = 0.f;
#pragma unroll
  for (int r = 0; r < 2; r++) {
#pragma unroll
    for (int i = 0; i < 8; i++) {
      if (r == 0 && i == 0) { asm volatile("s_waitcnt vmcnt(3)" : "+{v[100:103]}"(xv[0])); STAMP(13); }
      if (r == 0 && i == 4) asm volatile("s_waitcnt vmcnt(2)" : "+{v[104:107]}"(xv[1]));
      if (r == 1 && i == 0) asm volatile("s_waitcnt vmcnt(1)" : "+{v[108:111]}"(xv[2]));
      if (r == 1 && i == 4) asm volatile("s_waitcnt vmcnt(0)" : "+{v[112:115]}"(xv[3]));
      const u32 a = xv[2 * r + (i >> 2)][i & 3];
      s[r][0] = dot2b(a, 0x3f803f80u, s[r][0]); s[r][1] = dot2b(a, a, s[r][1]); s[r][2] = dot2b(a, wv[i], s[r][2]);
      if (i == 3) {
        c[r] = rdl(s[r][0] * 0.125f, 0);
        if (r) { cv = l < 32 ? c[0] : c[1]; asm volatile("" : "+v"(cv)); }
        SB();
      }
    }
    const float ml = s[r][0] * 0.0625f, dl = __builtin_fmaf(s[r][0], 0.25f, -4.f * c[r]);
    s[r][1] = __builtin_fmaf(dl, dl, __builtin_fmaf(-s[r][0], ml, s[r][1]));
    SB();
  }
#pragma unroll
  for (int k = 0; k < 3; k++) swap32(s[0][k ? 3 - k : 0], s[1][k ? 3 - k : 0]);
  float q[3];
#pragma unroll
  for (int k = 0; k < 3; k++) q[k] = s[0][k] + s[1][k];
#define RED3(m) "v_add_f32_dpp %0, %0, %0 " m " bank_mask:0xf\n\tv_add_f32_dpp %1, %1, %1 " m " bank_mask:0xf\n\tv_add_f32_dpp %2, %2, %2 " m " bank_mask:0xf\n\t"
  asm volatile("s_nop 1\n\t" RED3("row_ror:1 row_mask:0xf") RED3("row_ror:2 row_mask:0xf") RED3("row_ror:4 row_mask:0xf") RED3("row_ror:8 row_mask:0xf")
               RED3("row_bcast:15 row_mask:0xa") : "+v"(q[0]), "+v"(q[1]), "+v"(q[2]));
  const float mu = q[0] * (1.f / D), dv = mu - cv;
  const float var = fmaxf(__builtin_fmaf(-dv, dv, q[1] * (1.f / D)), 0.f);
  const float m = rb(mu);
  float sd = __builtin_amdgcn_sqrtf(rb(rb(var) + 1e-5f)), num = __builtin_fmaf(-m, sw, q[2]);
  asm volatile("s_nop 0\n\tv_cvt_pk_bf16_f32 %0, 0, %0" : "+v"(sd), "+v"(num));
  u32 rv = __float_as_uint(rb(num * __builtin_amdgcn_rcpf(sd)));
  rv ^= (u32)((int)rv >> 15) | 0x80000000u;
  rv = __builtin_amdgcn_perm(rv, kl, 0x07060100u) >> 1;
  asm volatile("ds_write2_b32 %[aw], %[sv], %[rv] offset1:16\n\ts_mov_b64 exec, 1\n\tds_add_rtn_u32 v104, %[ac], %[one]\n\ts_mov_b64 exec, -1\n\t"
               "ds_read_b32 v106, %[ar]\n\tds_read_b32 v105, %[ar] offset:64\n\tds_read_b128 v[100:103], %[aq] offset:64\n\tv_mov_b32 v107, %[t]\n\tv_mov_b32 v109, %[t]\n\t"
               "s_waitcnt lgkmcnt(3)\n\tv_cmp_eq_u32 vcc, 7, v104\n\ts_bitcmp1_b32 vcc_lo, 0\n\ts_cbranch_scc0 1f\n\t"
               "s_waitcnt lgkmcnt(2)\n\tbuffer_store_dwordx2 v[106:107], %[os], %[rs], 0 offen sc1\n\ts_waitcnt lgkmcnt(0)\n\t"
               "v_sub_u32 v100, v105, v100\n\tv_sub_u32 v101, v105, v101\n\tv_sub_u32 v102, v105, v102\n\tv_sub_u32 v103, v105, v103\n\t"
               RANK8("v100", "v101", "v102", "v103", "v104", "v103", "0", "s_nop 1", "s_nop 1") "v_cmp_eq_u32 vcc, 16, v104\n\t"
               "v_sub_u32 v108, 19, v104\n\ts_ff1_i32_b32 s6, vcc_lo\n\tv_readlane_b32 s6, v105, s6\n\t"
               "v_mad_u32_u24 v104, v104, %[om], %[ot]\n\ts_nop 0\n\tv_lshrrev_b32 v108, v108, s6\n\tv_bfi_b32 v108, %[fm], v108, v105\n\t"
               "buffer_store_dwordx2 v[108:109], v104, %[rs], 0 offen sc1\n1:\n\ts_waitcnt lgkmcnt(0)"
               :: [aw] "v"(aw), [sv] "v"(__builtin_amdgcn_perm(__float_as_uint(sd), __float_as_uint(m), 0x07060302u)), [rv] "v"(rv), [ac] "v"(ac), [one] "v"(one),
                  [ar] "v"(ar), [aq] "v"(aq), [os] "v"(os), [om] "v"(om), [ot] "v"(ot), [fm] "v"(fm), [sg] "s"(sg), [t] "s"(tag), [rs] "s"(rs)
               : "vcc", "scc", "s6", "v100", "v101", "v102", "v103", "v104", "v105", "v106", "v107", "v108", "v109", "memory");
  SB();
}

#define GL __attribute__((address_space(1)))
typedef const GL u16* gp;

__global__ __launch_bounds__(768, 3) void fused(const u16*, const u16*, const u16*, const u16*, const u16*, const u16*, const u16*, const u16*,
    const u16*, const u16*, const u16*, u16*, char*) {
  __shared__ __attribute__((aligned(256))) char smem[SMEM];
  const u32 lds0 = (u32)(size_t)(LDS char*)smem;
  int w0;
#define GLD(r0, r1, r2, r3, o_) "global_load_dwordx4 v[" #r0 ":" #r1 "], " #o_ ", s[16:17]\n\tglobal_load_dwordx4 v[" #r2 ":" #r3 "], " #o_ ", s[16:17] offset:2048\n\t"
  asm volatile(
      "s_load_dwordx16 s[16:31], s[0:1], 0\n\ts_load_dwordx8 s[32:39], s[0:1], 64\n\ts_load_dwordx2 s[40:41], s[0:1], 96\n\t"
      "s_memrealtime s[46:47]\n\t"
      "s_getpc_b64 s[4:5]\n2:\n\ts_lshr_b32 s7, s2, 3\n\ts_and_b32 s10, s4, 0xffffff00\n\ts_mov_b32 s11, s5\n\ts_mul_i32 s8, s7, 0x600\n\t"
      "v_lshl_add_u32 v3, v0, 1, s8\n\ts_cmpk_lt_u32 s2, %[pn]\n\ts_cbranch_scc0 1f\n\t"
      "global_load_ushort v163, v3, s[10:11] offset:%[pf]\n"
      "1:\n\ts_cmpk_lt_u32 s2, 0x80\n\tv_readfirstlane_b32 s3, v0\n\tv_and_b32 v1, 63, v0\n\tv_mov_b64 v[4:5], 0\n\tv_mov_b64 v[6:7], 0\n\tv_lshlrev_b32 v2, 4, v1\n\tv_lshlrev_b32 v8, 2, v0\n\t"
      "s_mov_b32 s42, %[we]\n\ts_mov_b32 s43, 0x20000\n\ts_cbranch_scc0 3f\n\t"
      "s_lshr_b32 s3, s3, 6\n\ts_mul_i32 s9, s3, %[xr]\n\ts_min_u32 s9, s9, %[zm]\n\tv_add_u32 v3, s9, v2\n\tv_add_u32 v3, %[zo], v3\n\t"
      "s_and_b32 s7, s7, 7\n\ts_and_b32 s6, s2, 7\n\ts_and_b32 s8, s2, 64\n\ts_lshl3_add_u32 s8, s6, s8\n\ts_add_u32 s45, s8, s7\n\t"
      "s_add_u32 s4, s4, lane_tab@rel32@lo+(6f-2b-4)\n6:\n\ts_addc_u32 s5, s5, lane_tab@rel32@hi+(6b-2b+4)\n\t"
      "ds_write_b128 v3, v[4:7]\n\ts_cmp_lt_u32 s3, 8\n\ts_cbranch_scc1 5f\n\t"
      "s_lshl_b32 s8, s45, 4\n\ts_lshl_b32 s9, s3, 18\n\ts_add_u32 s9, s9, s8\n\ts_bitset0_b32 s9, 21\n\tv_lshl_add_u32 v148, v1, 12, s9\n\t"
      "s_lshl3_add_u32 s9, s3, s7\n\ts_lshl_b32 s9, s9, 17\n\ts_add_u32 s9, s9, s8\n\ts_bitset0_b32 s9, 23\n\tv_lshl_add_u32 v9, v1, 11, s9\n\t"
      "v_add_u32 v11, 0x100000, v148\n\tv_add_u32 v12, 0x200000, v148\n\t"
      "v_lshlrev_b32 v149, 4, v0\n\ts_lshl_b32 s9, s3, 10\n\ts_add_u32 m0, s9, %[ob]\n\tv_add_u32 v10, 0xffffe800, v149\n\tglobal_load_lds_dwordx4 v10, s[4:5]\n\t"
      "v_add_u32 v149, %[sg], v149\n\t"
      "s_waitcnt lgkmcnt(0)\n\ts_load_dword s44, s[40:41], 0\n\t"
      "s_barrier\n\t"
      "global_load_dword v160, v9, s[16:17]" PRENT "\n\t"
      GLD(100, 103, 104, 107, v148)
      GLD(108, 111, 112, 115, v11)
      "s_cmp_eq_u32 s6, 3\n\ts_cbranch_scc1 .Lrole_p\n\t"
      GLD(116, 119, 120, 123, v12)
      "s_branch .Lrole_p\n"
      "3:\n\ts_lshr_b32 s3, s3, 6\n\ts_cmp_lt_u32 s3, 8\n\ts_cbranch_scc1 4f\n\ts_barrier\n\ts_branch .Lrole_d\n4:\n\t"
      "v_add_u32 v3, %[fc], v2\n\ts_add_u32 s8, s3, s2\n\ts_and_b32 s45, s8, 7\n\ts_lshl3_add_u32 s8, s2, s45\n\t"
      "s_lshl_b32 s8, s8, 12\n\ts_bitset0_b32 s8, 22\n\tds_write_b128 v3, v[4:7]\n\tv_add_u32 v3, s8, v2\n\t"
      "s_waitcnt lgkmcnt(0)\n\tglobal_load_dwordx4 v[116:119], v2, s[30:31]\n\tglobal_load_dwordx4 v[120:123], v2, s[30:31] offset:1024\n\t"
      "s_load_dword s44, s[40:41], 0\n\ts_barrier\n\t"
      "global_load_dwordx4 v[100:103], v3, s[16:17]" LNNT "\n\tglobal_load_dwordx4 v[104:107], v3, s[16:17] offset:1024" LNNT "\n\t"
      "global_load_dwordx4 v[108:111], v3, s[16:17] offset:2048" LNNT "\n\tglobal_load_dwordx4 v[112:115], v3, s[16:17] offset:3072" LNNT "\n\t"
      "s_branch .Lrole_l\n"
      "5:\n\t"
      "global_load_dwordx4 v[16:19], v2, s[4:5]\n\tglobal_load_dwordx4 v[20:23], v2, s[4:5] offset:1024\n\t"
      "s_lshl3_add_u32 s8, s45, s3\n\ts_lshl_b32 s9, s8, 1\n\tv_mov_b32 v9, s9\n\ts_lshl_b32 s9, s45, 11\n\tv_add_u32 v3, s9, v8\n\t"
      "s_waitcnt lgkmcnt(0)\n\tglobal_load_short_d16_hi v24, v9, s[26:27]\n\tglobal_load_ushort v160, v9, s[28:29]\n\t"
      "global_load_dword v96, v3, s[18:19]" PNT "\n\tglobal_load_dword v97, v3, s[20:21]" PNT "\n\t"
      "global_load_dword v68, v3, s[22:23]" PNT "\n\tglobal_load_dword v69, v3, s[24:25]" PNT "\n\ts_barrier\n\ts_branch .Lrole_c"
      : "={s3}"(w0)
      : "{s[0:1]}"(__builtin_amdgcn_kernarg_segment_ptr()), "{s2}"(blockIdx.x), "{v0}"(threadIdx.x), [pf] "n"(PFO), [pn] "n"(8 * PFN), [we] "n"(WS_END), [xr] "n"(XREG),
        [zm] "n"(8 * XREG), [zo] "n"(OX - 16), [sg] "n"(O_SG - 8192), [fc] "n"(FC), [ob] "n"(OB - 8192)
      : "memory");
#define TLREGS()
#define ROLE(name, text, ...) \
  gp x, wg, wu, wd; GL u16* out; rsrc_t rs; int b, w, t, l, g; u32 tag, st0; \
  asm volatile(".Lrole_" #name ":" : "={v0}"(t), "={v1}"(l) __VA_ARGS__ :: "memory"); \
  asm volatile(text : "={s2}"(b), "={s3}"(w), "={s[16:17]}"(x), "={s[32:33]}"(wg), "={s[34:35]}"(wu), "={s[36:37]}"(wd), \
               "={s[38:39]}"(out), "={s[40:43]}"(rs), "={s44}"(tag), "={s45}"(g), "={s46}"(st0) :: "memory"); \
  SB(); \
  __builtin_assume((u32)l < 64u); __builtin_assume((u32)t < 768u); __builtin_assume((u32)w < 12u); __builtin_assume((u32)b < 256u); __builtin_assume((u32)g < 128u); \
  TLREGS(); \
  const int xk = b & 7, j = (b >> 3) & 15, n16 = l & 15, kg = l >> 4
#define TAGWAIT() do { asm volatile("s_waitcnt lgkmcnt(0)" : "+{s44}"(tag)::"memory"); tag += 1u; } while (0)
#define BAR() do { SB(); asm volatile("s_barrier" ::: "memory"); SB(); } while (0)
#define FLAG(o, v) do { asm volatile("" ::: "memory"); SB(); *(volatile LDS u32*)(LDS char*)(smem + (o)) = (v); SB(); } while (0)
#define ARRIVE(o) do { SB(); if (l == 0) asm volatile("ds_add_u32 %0, %1" ::"v"(lds0 + (o)), "v"(1u) : "memory"); SB(); } while (0)
#define CWAIT(o, n, nap) do { _Pragma("unroll 1") for (int spin = 0; spin < SPIN; spin++) { \
      if (__builtin_amdgcn_readfirstlane((int)*(const volatile LDS u32*)(LDS char*)(smem + (o))) >= (int)(n)) break; \
      asm volatile(nap ::: "memory"); } SB(); asm volatile("" ::: "memory"); } while (0)
#define AWAIT(o, n, nap) do { _Pragma("unroll 1") for (int spin = 0; spin < SPIN; spin++) { \
      if (__all(*(const volatile LDS u32*)(LDS char*)(smem + (o)) >= (u32)(n))) break; \
      asm volatile(nap ::: "memory"); } SB(); asm volatile("" ::: "memory"); } while (0)
#define POLL8(res_, off_) do { u32 n_ = SPIN; \
    asm volatile("1:\n\tbuffer_load_dwordx2 v[4:5], %2, %3, 0 offen sc1\n\ts_sub_u32 %1, %1, 1\n\ts_waitcnt vmcnt(0)\n\t" \
                 "v_cmp_ne_u32 vcc, %4, v5\n\ts_cbranch_scc1 2f\n\ts_cbranch_vccnz 1b\n2:" \
                 : "=&{v[4:5]}"(res_), "+s"(n_) : "v"(off_), "s"(rs), "s"(tag) : "vcc", "scc", "memory"); } while (0)
#define UNTIL(t_) "2:\n\ts_sleep 1\n\ts_memrealtime s[8:9]\n\ts_waitcnt lgkmcnt(0)\n\ts_sub_u32 s8, s8, %[st]\n\ts_cmp_lt_u32 s8, " t_ "\n\ts_cbranch_scc1 2b\n\t"
#define LANE(v_) int v_; asm volatile("v_mbcnt_lo_u32_b32 %0, -1, 0\n\tv_mbcnt_hi_u32_b32 %0, -1, %0" : "=v"(v_))
#define IXO(l_) (O_IDX + (2 * xk + (j & 1)) * 128 + ((l_) & 7) * 8)
#define IDS() const int id = 16 * xk + j, sb = id * 57 >> 10, mat = id & 1, u = (id - 18 * sb) >> 1; \
    const bool slow = u >= 6; \
    const int part = slow ? u - 6 : u & 1, cb = 4 * sb + (slow ? 1 : (u >> 1) + (u > 1)), r0 = part * (slow ? 320 : 512), ns = slow ? 5 + (u >> 3) : 8; \
    const int kp = 4 * sb + ((u >> 1) & 3), ct = 2 * (u & 1) + mat
#define TS(a, b_)
#define TSC
#define DONE "s_endpgm\n"
#define TSD
#define TSGET(i0, i1, i2, i3)
  if (blockIdx.x >= 128) {
    if (w0 >= 8) {
      ROLE(d, "s_waitcnt lgkmcnt(0)");
      IDS();
      const int dw = w - 8;
      const u32 sw = (((n16 >> 1) ^ (kg & 1)) << 5) + ((l & 1) << 4);
      gp base = (mat ? wu : wg) + (size_t)(r0 + 4 * dw) * F + cb * 128;
      u32 v = kg * (2 * F) + sw, fa = lds0 + FC + 4 * dw, vp = 96 + dw, t_;
      asm volatile("" : "+v"(v), "+v"(fa), "+v"(vp));
      asm volatile("s_mov_b32 m0, %0" ::"s"(lds0 + 1024 * dw));
#define VMC "s_getreg_b32 s6, hwreg(HW_REG_IB_STS, 0, 4)\n\t"
#define RUN(k, q, lim, v_, b_, st_) asm volatile("1:\n\ts_lshr_b32 s6, m0, 4\n\ts_and_b32 s6, s6, 0xc0\n\tv_xor_b32 %[t], s6, %[v]\n\ts_waitcnt vmcnt(%[c])\n\t" VMC \
        "global_load_lds_dwordx4 %[t], %[b] nt\n\ts_add_u32 m0, m0, %[m]\n\tv_add_u32 %[v], %[d], %[v]\n\t.if %[m] == 3072\n\tv_mad_i32_i24 %[t], s6, -3, %[p]\n\tv_add_u32 %[p], 3, %[p]\n\t" \
        "s_mov_b64 exec, 1\n\tds_max_u32 %[a], %[t]\n\ts_mov_b64 exec, -1\n\t.endif\n\ts_cmp_lt_u32 m0, %[l]\n\ts_cbranch_scc1 1b" \
        : [v] "+v"(v_), [p] "+v"(vp), [t] "=&v"(t_) : [b] "s"(b_), [c] "n"((q) - 1), [m] "n"(1024 * (k)), [d] "n"((k) * (st_)), \
          [a] "v"(fa), [l] "s"(lim) : "scc", "s6", "memory")
#define MARK(c, val) "s_waitcnt vmcnt(" #c ")\n\t" val "\n\ts_mov_b64 exec, 1\n\tds_max_u32 %[a], %[t]\n\ts_mov_b64 exec, -1\n\t"
      __builtin_amdgcn_s_setprio(DPRIO);
      STAMP(8);
      if (id < 126) { if (slow) RUN(4, QAS, 32768, v, base, 8 * F); else RUN(4, 8, 32768, v, base, 8 * F); }
      if (dw < 3) {
        if (id >= 126) __builtin_amdgcn_endpgm();
        gp bd = wd + (size_t)(128 * kp + 2 * dw) * D + ct * 256;
        u32 vd = (l >> 5) * (2 * D + 32) ^ 16 * (l & 31);
        asm volatile("" : "+v"(vd));
        if (slow) RUN(3, QS, 16384 * ns, v, base, 8 * F);
        else { RUN(3, 8, 32768 + 3072 * RAMP, v, base, 8 * F); RUN(3, QD, 131072, v, base, 8 * F); }
        STAMP(1);
        {
          u32 n_ = SPIN;
          asm volatile("1:\n\t" VMC MARK(63, "v_mad_i32_i24 %[t], s6, -3, %[p]") "ds_read_b32 %[t], %[f]\n\ts_sub_u32 %[n], %[n], 1\n\ts_waitcnt lgkmcnt(0)\n\t"
                       "v_cmp_lt_u32 vcc, %[k], %[t]\n\ts_cbranch_scc1 2f\n\ts_cbranch_vccz 1b\n2:\n\ts_mov_b32 m0, %[m]"
                       : [t] "=&v"(t_), [n] "+s"(n_) : [a] "v"(fa), [p] "v"(vp), [f] "v"(u == 8 ? fa : lds0 + FC + 20), [k] "s"(u == 8 ? 159 : 16 * HOLD - 105), [m] "s"(lds0 + 1024 * dw)
                       : "vcc", "scc", "s6", "memory");
        }
        STAMP(3);
        if (u < 8) {
          RUN(3, QE, 65536, vd, bd, 4 * D);
          STAMP(6);
          asm volatile(MARK(0, "v_mov_b32 %[t], -1") : [t] "=&v"(t_) : [a] "v"(fa) : "memory");
          STAMP(4);
        }
        FLUSH();
        __builtin_amdgcn_endpgm();
      }
      asm volatile(MARK(0, "v_mov_b32 %[t], -1") : [t] "=&v"(t_) : [a] "v"(fa) : "memory");
      u32 km, rk;
      u32 hi, lo, k2, k3, gd, thr, sg = 0x0c0c0b09u;
      u32 ka = lds0 + FC + 384 + 8 * (l >> 3), kc = 2047 * 2048 + 2 * r0 + 16 * ((l & 7) ^ ((l >> 2) & 4)), kq = lds0 + FC + 512 + (l & 48), kr = lds0 + FC + 512 + 4 * n16;
      u32 c1 = 896, c2 = 1792, c3 = 2688, cm = (u32)-2048, cs = O_SG + 8 * 2047, ca = lds0 + FC + 512;
      rsrc_t rx = __builtin_amdgcn_make_buffer_rsrc((void*)(x - 1344), 0, 2 * L * D + 4096, 0x00020000);
      u32 co = O_CLR + 8 * (18 * sb + (0x84c0 >> 4 * (kp & 3) & 15) + min(l, (kp & 3) == 1 ? 5 : 3));
      asm volatile("" : "+v"(ka), "+v"(kc), "+v"(kq), "+v"(kr), "+v"(co));
      asm volatile("" : "+s"(c1), "+s"(c2), "+s"(c3), "+s"(cm), "+s"(cs), "+s"(ca), "+s"(sg), "+s"(rx));
      *(u32x4*)(smem + FA + 6144 + 16 * l) = u32x4{0u, 0u, 0u, 0u};
      *(u32x4*)(smem + FA + 7168 + 16 * l) = u32x4{0u, 0u, 0u, 0u};
      CWAIT(FC + 256, 1, "s_sleep 4");
      tag = (u32)__builtin_amdgcn_readfirstlane((int)*(const volatile LDS u32*)(LDS char*)(smem + FC + 24));
      {
        u32 n_ = SPIN, t0, t1, t2, t3, se;
#define TLX(a, b_, c, d, sc) "buffer_load_dwordx4 v[" #a ":" #b_ "], %[o], %[r], 0 offen" sc "\n\tbuffer_load_dwordx4 v[" #c ":" #d "], %[o], %[r], %[ts] offen" sc "\n\t"
#define TLD(a, b_, c, d) TLX(a, b_, c, d, " sc1")
#define TCK(r0, r1, r2, r3, r4, r5, r6, r7) "s_waitcnt vmcnt(4)\n\tv_xor_b32 %[t0], %[t], v" #r1 "\n\tv_bitop3_b32 %[t0], %[t0], %[t], v" #r3 " bitop3:0xf6\n\t" \
          "v_bitop3_b32 %[t0], %[t0], %[t], v" #r5 " bitop3:0xf6\n\tv_bitop3_b32 %[t0], %[t0], %[t], v" #r7 " bitop3:0xf6\n\t" \
          "v_cmp_ne_u32 vcc, 0, %[t0]\n\tv_mov_b32 %[hi], v" #r0 "\n\tv_mov_b32 %[lo], v" #r2 "\n\tv_mov_b32 %[k2], v" #r4 "\n\tv_mov_b32 %[k3], v" #r6 "\n\t" \
          "s_cbranch_vccz 5f\n\t" TLD(r0, r3, r4, r7)
        asm volatile("2:\n\ts_sleep 1\n\ts_memrealtime s[8:9]\n\ts_waitcnt lgkmcnt(0)\n\ts_sub_u32 s8, s8, %[st]\n\ts_cmp_lt_u32 s8, %[tw]\n\ts_cbranch_scc1 2b\n\t"
                     TLX(128, 131, 132, 135, SCT) "s_sleep " STR(RSL) "\n\t" TLD(136, 139, 140, 143) "s_sleep " STR(RSL) "\n\t" TLX(144, 147, 148, 151, SCU)
                     "1:\n\t" TCK(128, 129, 130, 131, 132, 133, 134, 135) TCK(136, 137, 138, 139, 140, 141, 142, 143) TCK(144, 145, 146, 147, 148, 149, 150, 151)
                     "s_sub_u32 %[n], %[n], 1\n\ts_cbranch_scc0 1b\n5:\n\t" TS(60, 61)
                     "v_and_or_b32 %[t0], %[hi], %[mk], %[pa]\n\tv_and_or_b32 %[t1], %[lo], %[mk], %[pb]\n\t"
                     "v_max_u32 %[t2], %[t0], %[t1]\n\tv_max_u32 %[t3], %[t0], %[t1]\n\tv_perm_b32 %[gd], %[hi], %[lo], %[p0]\n\tv_perm_b32 %[se], %[k2], %[k3], %[p0]\n\t"
                     "v_permlane16_swap_b32 %[t2], %[t3]\n\tv_max_u32 %[km], %[t2], %[t3]\n\tv_max_u32 %[rk], %[t2], %[t3]\n\t"
                     "v_max_u32 %[hi], %[t0], %[t1]\n\tv_min_u32 %[lo], %[t0], %[t1]\n\t"
                     "v_permlane32_swap_b32 %[km], %[rk]\n\tv_max_u32 %[km], %[km], %[rk]\n\tv_not_b32 %[t2], %[km]\n\t"
                     "v_and_or_b32 %[k2], %[k2], %[mk], %[pa]\n\t"
                     "v_not_b32_dpp %[t2], %[km] row_ror:4 row_mask:0x2 bank_mask:0xf\n\tv_not_b32_dpp %[t2], %[km] row_ror:8 row_mask:0x4 bank_mask:0xf\n\t"
                     "v_not_b32_dpp %[t2], %[km] row_ror:12 row_mask:0x8 bank_mask:0xf\n\t"
                     "v_pk_lshlrev_b16 %[gd], 4, %[gd] op_sel_hi:[0,1]\n\tv_add_u32 %[t0], %[km], %[t2]\n\t"
                     "v_add_u32_dpp %[t1], %[t2], %[km] row_ror:1 row_mask:0xf bank_mask:0xf\n\tv_add_u32_dpp %[t3], %[t2], %[km] row_ror:2 row_mask:0xf bank_mask:0xf\n\t"
                     "v_add_u32_dpp %[t2], %[t2], %[km] row_ror:3 row_mask:0xf bank_mask:0xf\n\t"
                     RANK8("%[t0]", "%[t1]", "%[t2]", "%[t3]", "%[rk]", "%[t3]", "-2", "v_lshrrev_b32 %[se], 4, %[se]\n\tv_perm_b32 %[gd], %[gd], %[se], %[p1]",
                           "v_pk_max_u16 %[gd], %[gd], %[gd] op_sel:[0,1] op_sel_hi:[1,0]\n\tv_and_or_b32 %[k3], %[k3], %[mk], %[pb]")
                     "v_cmp_eq_u32 vcc, 56, %[rk]\n\tv_max3_u32 %[se], %[lo], %[k2], %[k3]\n\tv_lshrrev_b32 %[gd], 1, %[gd]\n\ts_ff1_i32_b32 s6, vcc_lo\n\t"
                     "v_readlane_b32 s6, %[km], s6\n\tv_or_b32 %[gd], 0x7fff, %[gd]\n\tv_med3_u32 %[t2], %[lo], %[k2], %[k3]\n\t"
                     "v_cmp_le_u32 s[8:9], s6, %[hi]\n\tv_cmp_le_u32 s[10:11], s6, %[se]\n\tv_max_u32 %[t2], %[t2], %[gd]\n\t"
                     "v_mbcnt_lo_u32_b32 %[t0], s8, 0\n\tv_mbcnt_lo_u32_b32 %[t1], s10, -15\n\tv_mbcnt_hi_u32_b32 %[t0], s9, %[t0]\n\tv_mbcnt_hi_u32_b32 %[t1], s11, %[t1]\n\t"
                     "v_lshl_add_u32 %[t0], %[t0], 2, %[ca]\n\tv_mad_i32_i24 %[t1], %[t1], -4, %[ca]\n\tv_cmp_le_u32 vcc, s6, %[t2]\n\t"
                     "s_mov_b64 exec, s[8:9]\n\tds_write_b32 %[t0], %[hi]\n\ts_mov_b64 exec, s[10:11]\n\tds_write_b32 %[t1], %[se]\n\ts_mov_b64 exec, -1\n\t"
                     "ds_read_b32 %[se], %[kr]\n\tds_read_b128 v[152:155], %[kq]\n\t"
                     "s_bcnt1_i32_b64 s8, s[8:9]\n\ts_bcnt1_i32_b64 s10, s[10:11]\n\ts_add_u32 s8, s8, s10\n\ts_cmp_gt_u32 s8, 16\n\ts_cselect_b64 s[8:9], -1, vcc\n\t"
                     "s_cmp_lg_u64 s[8:9], 0\n\ts_waitcnt lgkmcnt(0)\n\ts_cbranch_scc1 6f\n\t"
                     "v_sub_u32 %[t0], %[se], v152\n\tv_sub_u32 %[t1], %[se], v153\n\tv_sub_u32 %[t2], %[se], v154\n\tv_sub_u32 %[t3], %[se], v155\n\t"
                     RANK8("%[t0]", "%[t1]", "%[t2]", "%[t3]", "%[rk]", "%[t3]", "0", "v_mov_b32 %[km], %[se]\n\ts_nop 0", "s_nop 1") "6:\n\t" TS(62, 63)
                     : [hi] "=&v"(hi), [lo] "=&v"(lo), [k2] "=&v"(k2), [k3] "=&v"(k3), [gd] "=&v"(gd), [se] "=&v"(se), [km] "=&v"(km), [rk] "=&v"(rk),
                       [t0] "=&v"(t0), [t1] "=&v"(t1), [t2] "=&v"(t2), [t3] "=&v"(t3), [n] "+s"(n_)
                     : [o] "v"(O_TOP + 16 * (l & 7) + TL * (l >> 3)), [r] "s"(rs), [t] "s"(tag), [ts] "s"(TOPS), [st] "s"(st0), [tw] "n"(TW), [mk] "s"(0x7fff800fu),
                       [pa] "v"((127 - 2 * l) << 4), [pb] "v"((126 - 2 * l) << 4), [sg] "s"(sg), [p0] "s"(0x05040100u), [p1] "s"(0x07020500u), [ca] "s"(ca),
                       [kr] "v"(kr), [kq] "v"(kq)
                     : "vcc", "scc", "s6", "s8", "s9", "s10", "s11", "memory" TSC);
      }
      unsigned long long more;
      asm volatile("" : "={s6}"(thr), "={s[8:9]}"(more));
      if (__builtin_expect(more != 0, 0)) {
        const u32 m0 = km;
        unsigned long long all = __ballot(gd >= thr);
        if (gd >= thr) hi = lo = k2 = k3 = 0u;
#pragma unroll 1
        for (int i = 0; i < 4;) {
          u32 k, m = m0;
          if (all) {
            const int a = __builtin_ctzll(all);
            all &= all - 1;
            u32x2 r;
            const u32 o = O_RG + 256 * a + 8 * (l & 31);
            POLL8(r, o);
            k = l < 32 ? r.x : 0u;
            m = (u32)__builtin_amdgcn_readlane((int)m0, a);
          } else { k = hi; hi = lo; lo = k2; k2 = k3; i++; }
          asm volatile("v_cmp_le_u32 s[8:9], %[t], %[k]\n\tv_cmp_ne_u32 vcc, %[k], %[m0]\n\ts_and_b64 s[8:9], s[8:9], vcc\n\ts_cbranch_scc0 2f\n"
                       "1:\n\ts_ff1_i32_b64 s7, s[8:9]\n\tv_readlane_b32 s12, %[k], s7\n\ts_lshl_b64 s[10:11], 1, s7\n\ts_nop 0\n\t"
                       "v_cmpx_gt_u32 vcc, s12, %[km]\n\tv_add_u32 %[rk], 8, %[rk]\n\ts_bcnt0_i32_b32 s7, vcc_lo\n\tv_cmpx_eq_u32 vcc, 0x80, %[rk]\n\t"
                       "v_mov_b32 %[km], s12\n\tv_lshlrev_b32 %[rk], 2, s7\n\ts_mov_b64 exec, -1\n\ts_andn2_b64 s[8:9], s[8:9], s[10:11]\n\ts_cbranch_scc1 1b\n2:"
                       : [km] "+v"(km), [rk] "+v"(rk) : [k] "v"(k), [m0] "v"(m), [t] "s"(thr) : "vcc", "scc", "s7", "s8", "s9", "s10", "s11", "s12");
        }
      }
      if (id < 126) {
        u32 a_, v_, n_ = SPIN;
        u32x2 cl;
#define RLD(o, s_) "buffer_load_dwordx4 %[v], %[rx], " s_ " offen offset:" #o " lds\n\t"
        asm volatile("v_add_u32 %[a], %[kb], %[rk]\n\tv_and_b32 %[v], 0x7ff, %[km]\n\ts_mov_b32 m0, %[fa]\n\tds_write_b32 %[a], %[v]\n\tds_read_b32 %[a], %[ka]\n\t"
                     "s_cmp_eq_u32 %[ns], 8\n\ts_waitcnt lgkmcnt(0)\n\tv_mad_i32_i24 %[v], %[a], %[cm], %[kc]\n\t"
                     RLD(0, "%[c3]") RLD(1024, "%[c2]") RLD(2048, "%[c1]") RLD(3072, "0")
                     "s_mov_b32 m0, %[fb]\n\tv_add_u32 %[v], 0x200, %[v]\n\t"
                     RLD(0, "%[c3]") RLD(1024, "%[c2]") "s_cbranch_scc0 3f\n\t" RLD(2048, "%[c1]") RLD(3072, "0")
                     "3:\n\tv_mad_i32_i24 %[a], %[a], -8, %[cs]\n\tbuffer_load_dwordx2 v[160:161], %[a], %[rs], 0 offen" SCR "\n\tbuffer_load_dwordx2 %[cl], %[co], %[rs], 0 offen sc1\n\t"
                     "v_mov_b32 %[v], 16 * " STR(HOLD) " - 112\n\tv_subrev_u32 %[ka], %[kd], %[ka]\n\ts_mov_b64 exec, 1\n\tds_add_u32 %[fc], %[v] offset:8\n\ts_mov_b64 exec, -1\n\tv_mov_b32 %[v], -1\n\t" TS(64, 65)
                     "s_waitcnt vmcnt(1)\n1:\n\tv_cmp_ne_u32 vcc, %[t], v161\n\tv_and_b32 v156, 0xffff0000, v160\n\tv_lshlrev_b32 v157, 16, v160\n\tv_rcp_f32 v156, v156\n\t"
                     "s_sub_u32 %[n], %[n], 1\n\ts_cbranch_vccz 2f\n\ts_cbranch_scc1 2f\n\t"
                     "buffer_load_dwordx2 v[160:161], %[a], %[rs], 0 offen sc1\n\ts_waitcnt vmcnt(0)\n\ts_branch 1b\n2:\n\tv_mul_f32 v157, -v157, v156\n\t"
                     "ds_write_b64 %[ka], v[156:157]\n\tds_write_b32 %[fc], %[v] offset:4\n\t" TS(66, 67) "s_waitcnt vmcnt(0)"
                     : [a] "=&v"(a_), [v] "=&v"(v_), [ka] "+v"(ka), [n] "+s"(n_), [cl] "=&v"(cl)
                     : [co] "v"(co), [rk] "v"(rk), [km] "v"(km), [kc] "v"(kc), [kb] "n"(FC + 384), [kd] "n"(384 - 32), [fa] "n"(FA), [fb] "n"(FA + 4096), [rx] "s"(rx), [rs] "s"(rs),
                       [c1] "s"(c1), [c2] "s"(c2), [c3] "s"(c3), [cm] "s"(cm), [cs] "s"(cs), [ns] "s"(ns), [fc] "v"(fa), [t] "s"(tag) : "vcc", "scc", "memory" TSC);
        TSGET(12, 13, 14, 15);
#pragma unroll 1
        for (int spin = 0; spin < SPIN; spin++) {
          if (__all(cl.y == tag)) break;
          asm volatile("s_sleep " STR(CNAP) ::: "memory");
          cl = ld8(rs, co);
        }
        FLAG(FC + 28, 1u);
        STAMP(10);
      } else {
        const u32 cd = km << 1;
        const u32x2 r = {(2047u - (km & 0x7ffu)) | ((cd ^ ((u32)((int)~cd >> 15) | 0x80000000u)) & 0xffff0000u), tag};
        const u32 o = rk < 64u ? O_IDX + 1024 * mat + 128 * kg + rk : 0x80000000u;
        st8(rs, o, r);
        st8(rs, o + 512, r);
        TSGET(12, 13, 14, 15);
        STAMP(14);
        STAMP(15);
        if (mat) {
#pragma unroll 1
          for (int spin = 0; spin < SPIN; spin++) {
            const u32x4 a = ld16(rs, O_ARR + 16 * l);
            if (__all(a.y == tag && a.w == tag)) break;
            asm volatile("s_sleep 16" ::: "memory");
          }
          if (l == 0) __builtin_amdgcn_raw_buffer_store_b32(tag, rs, 0, 0, 16);
        }
      }
      FLUSH();
      __builtin_amdgcn_endpgm();
    }
    u32x4 xv[4];
    u32x8 wv;
    ROLE(l, "", , "={v[100:103]}"(xv[0]), "={v[104:107]}"(xv[1]), "={v[108:111]}"(xv[2]), "={v[112:115]}"(xv[3]), "={v[116:123]}"(wv));
    STAMP(11);
    u32 sg = 0x0c0c0b09u;
    asm volatile("" : "+s"(sg));
    ln_rows(xv, wv, rs, tag, b, g, l, lds0 + FC + 128, sg TLVAL);
    STAMP(10);
    IDS();
    if (id >= 126) {
      FLUSH();
      __builtin_amdgcn_endpgm();
    }
    const int wr = 16 * (kg >> 1) + 4 * (kg & 1) + (n16 & 8) + ((n16 >> 2) & 1);
    u32 wa = lds0 + 256 * wr + ((w ^ ((wr & 1) | ((wr >> 1) & 6))) << 5) + 8 * (l & 3), sel = (l & 8) ? 0xeeeeu : 0x4444u;
    u32 so = 4 * t;
    asm volatile("" : "+v"(wa), "+v"(sel), "+v"(so));
    asm volatile(UNTIL(STR(CLD)) :: [st] "s"(st0) : "s8", "s9", "scc");
    __builtin_amdgcn_raw_buffer_store_b32(~0u, rs, so, O_HG + (6 * cb + 3 * mat + part) * 2048, 16);
    st8(rs, u < 8 ? O_PG + (4 * kp + ct) * 4096 + 2 * so : 0x80000000u, u32x2{~0u, ~0u});
#define SWP(a0, a1, a2, a3) "v_permlane32_swap_b32 " a0 ", " a2 "\n\tv_permlane32_swap_b32 " a1 ", " a3 "\n\t"
#define OUT(a0, a1, o) "v_cvt_pk_bf16_f32 " a0 ", " a0 ", " a1 "\n\tv_min_u32 " a0 ", -2, " a0 "\n\tbuffer_store_dword " a0 ", %[so], %[rs], " o " sc1\n\t"
#define TR4(a, b_, c, d, e, f, g_, h, adr, o) "ds_read_b64_tr_b16 v[" #a ":" #b_ "], " adr " offset:" #o "\n\tds_read_b64_tr_b16 v[" #c ":" #d "], " adr " offset:" #o "+512\n\t" \
      "ds_read_b64_tr_b16 v[" #e ":" #f "], " adr " offset:" #o "+8192\n\tds_read_b64_tr_b16 v[" #g_ ":" #h "], " adr " offset:" #o "+8704\n\t"
#define SM(d, a, b_) "v_smfmac_f32_16x16x64_bf16 v[" d "], v[" a "], v[" b_ "], %[sel]\n\t"
#define CALL "s_call_b64 s[10:11], 1f\n\t"
#define USE(c, xa, wf) "s_add_u32 s6, s6, 16\n\ts_waitcnt lgkmcnt(" #c ")\n\t" SM("16:19", "20:23", wf) SM("8:11", xa, wf)
#define XR(xa, o) "ds_read_b128 v[" xa "], %[xa] offset:" #o "\n\t"
#define XU(c, xa, wf) "s_waitcnt lgkmcnt(" #c ")\n\t" SM("8:11", xa, wf)
#define ZERO(r) "v_mfma_f32_32x32x16_bf16 v[" r "], v[8:11], v[8:11], 0\n\t"
    asm volatile("v_mov_b64 v[8:9], 0\n\tv_mov_b64 v[10:11], 0\n\tv_mov_b64 v[16:17], 0\n\tv_mov_b64 v[18:19], 0\n\t"
                 ZERO("32:47") ZERO("48:63") ZERO("64:79") ZERO("80:95") ZERO("96:111") ZERO("112:127")
                 "v_mov_b32 v20, 0x3f803f80\n\tv_mov_b32 v21, v20\n\tv_mov_b64 v[22:23], v[20:21]\n\tv_mov_b32 v13, %[t]\n\tv_mov_b32 v28, 1\n\t"
                 "v_add_u32 v29, 0x10000, %[wa]\n\ts_movk_i32 s6, 96\n\ts_mov_b32 s7, 1\n\ts_mov_b32 s8, 15\n\ts_mov_b32 s9, %[spin]\n\ts_nop 7\n\t"
                 CALL TR4(32, 33, 34, 35, 36, 37, 38, 39, "%[wa]", 0) TR4(40, 41, 42, 43, 44, 45, 46, 47, "%[wa]", 16384)
                 USE(4, "96:99", "32:39") "s_waitcnt lgkmcnt(0)\n\t" SM("16:19", "20:23", "40:47") SM("8:11", "100:103", "40:47")
                 CALL TR4(48, 49, 50, 51, 52, 53, 54, 55, "%[wa]", 32768) USE(0, "104:107", "48:55")
                 CALL TR4(56, 57, 58, 59, 60, 61, 62, 63, "%[wa]", 49152) "s_waitcnt vmcnt(0)\n\ts_mov_b64 exec, 1\n\tds_add_rtn_u32 v28, %[fl], v28 offset:20\n\ts_mov_b64 exec, -1\n\t" TS(60, 61)
                 USE(0, "108:111", "56:63") "v_and_b32 v28, 15, v28\n\tv_cmpx_eq_u32 vcc, 7, v28\n\tbuffer_store_dwordx2 v[12:13], off, %[rs], %[co] sc1\n\ts_mov_b64 exec, -1\n\t"
                 CALL TR4(64, 65, 66, 67, 68, 69, 70, 71, "v29", 0) USE(0, "112:115", "64:71") "s_cmp_eq_u32 %[ns], 5\n\ts_cbranch_scc1 5f\n\t"
                 CALL TR4(72, 73, 74, 75, 76, 77, 78, 79, "v29", 16384) USE(0, "116:119", "72:79") "s_cmp_eq_u32 %[ns], 6\n\ts_cbranch_scc1 5f\n\t"
                 CALL TR4(80, 81, 82, 83, 84, 85, 86, 87, "v29", 32768) USE(0, "120:123", "80:87")
                 CALL TR4(88, 89, 90, 91, 92, 93, 94, 95, "v29", 49152) USE(0, "124:127", "88:95")
                 "5:\n\t" TS(62, 63) "s_cmp_eq_u32 s7, 0\n\ts_cbranch_scc1 4f\n\ts_mov_b32 s8, 16\n"
                 "1:\n\tds_read_b32 v30, %[fl]\n\ts_waitcnt lgkmcnt(0)\n\tv_cmp_le_u32 vcc, s6, v30\n\ts_and_b32 s12, vcc_hi, s7\n\ts_cbranch_scc1 3f\n"
                 "2:\n\ts_andn2_b32 s12, s8, vcc_lo\n\ts_cbranch_scc1 6f\n\ts_setpc_b64 s[10:11]\n"
                 "6:\n\ts_sub_u32 s9, s9, 1\n\ts_cbranch_scc1 4f\n\t" GNAP "\n\ts_branch 1b\n"
                 "3:\n\t" XR("96:99", 0) XR("100:103", 1024) XR("104:107", 2048) XR("108:111", 3072) XR("112:115", 4096) XR("116:119", 5120) XR("120:123", 6144) XR("124:127", 7168)
                 "ds_read_b128 v[24:27], %[pa]\n\t" TS(64, 65) "s_mov_b32 s7, 0\n\t"
                 XU(8, "96:99", "32:39") XU(7, "100:103", "40:47") XU(6, "104:107", "48:55") XU(5, "108:111", "56:63")
                 XU(4, "112:115", "64:71") XU(3, "116:119", "72:79") XU(2, "120:123", "80:87") XU(0, "124:127", "88:95")
                 "s_cmp_eq_u32 s8, 16\n\ts_cbranch_scc0 2b\n"
                 "4:\n\ts_nop 4\n\tv_permlane32_swap_b32 v16, v17\n\tv_add_f32 v16, v16, v17\n\tv_mul_f32 v25, v25, v16\n\tv_mul_f32 v27, v27, v16\n\t"
                 SWP("v8", "v9", "v10", "v11") "v_fmac_f32 v25, v10, v24\n\tv_fmac_f32 v27, v11, v26\n\tv_fma_f32 v8, v8, v24, v25\n\tv_fma_f32 v9, v9, v26, v27\n\t"
                 OUT("v8", "v9", "%[ho] offen") TS(66, 67)
                 :: [wa] "v"(wa), [sel] "v"(sel), [so] "v"(so), [fl] "v"(lds0 + FC + 4 * min(l, 4)), [xa] "v"(lds0 + FA + 128 * (l & 7) + 16 * ((2 * kg + (n16 >> 3)) ^ (2 * l & 4))),
                    [pa] "v"(lds0 + FC + 32 + 32 * (kg & 1) + 16 * (kg >> 1)), [ns] "s"(ns), [t] "s"(tag), [co] "s"(O_CLR + 8 * id), [rs] "s"(rs), [ho] "s"(O_HG + (6 * cb + 3 * mat + part) * 2048), [spin] "n"(SPIN)
                 : "vcc", "scc", "s6", "s7", "s8", "s9", "s10", "s11", "s12", "memory", G_CLOBBER TSC);
    TSGET(1, 4, 3, 5);
    if (u < 8) {
      const int tp = 2 * (kg & 1) + (kg >> 1), r7 = 4 * (kg & 1) + (n16 >> 2);
      u32 o3 = (kp & 3) == 1 ? so + 4096 : 0x80000000u, h0 = O_HG + kp * 12288, h1 = h0 + 6144;
      u32 hw = lds0 + FH + 2 * tp * HP + 32 * w + 8 * (l & 2) + (l & 4) * 2 + ((l & 8) >> 1) + 2 * (l & 1);
      u32 hr = lds0 + FH + (l & 7) * HP + 32 * kg + 2 * (l & 8);
      u32 da = lds0 + 512 * (16 * (kg >> 1) + r7) + ((2 * w ^ r7) << 5) + 8 * (l & 3), db = da ^ 32, fl = lds0 + FC + 4 * (l & 3);
      u32 po = 512 * w + 16 * (l & 8) + 32 * tp + 4 * (l & 7);
      u32 e0 = 0x3f80u, e1 = 0x3f800000u;
      asm volatile("" : "+v"(hw), "+v"(hr), "+v"(da), "+v"(db), "+v"(fl), "+v"(po), "+v"(o3));
      asm volatile("" : "+s"(e0), "+s"(e1), "+s"(h0), "+s"(h1));
#define TRD(a, b_, c, d, e, f, g_, h, adr, o) "ds_read_b64_tr_b16 v[" #a ":" #b_ "], " adr " offset:" #o "\n\tds_read_b64_tr_b16 v[" #c ":" #d "], " adr " offset:" #o "+4096\n\t" \
      "ds_read_b64_tr_b16 v[" #e ":" #f "], " adr " offset:" #o "+16384\n\tds_read_b64_tr_b16 v[" #g_ ":" #h "], " adr " offset:" #o "+20480\n\t"
#define HLD(X, r) X(r "0", "%[s1]", "%[h0] offen") X(r "3", "%[s1]", "%[h1] offen") X(r "1", "%[s1]", "%[h0] offen offset:2048") X(r "4", "%[s1]", "%[h1] offen offset:2048") \
      X(r "2", "%[o3]", "%[h0] offen") X(r "5", "%[o3]", "%[h1] offen")
#define HL0(a, o, s_) "buffer_load_dword " a ", " o ", %[rs], " s_ " sc1\n\t"
#define HLF(a, o, s_) "buffer_load_dword " a ", " o ", %[rs], " s_ SCH "\n\t"
#define HL1(a, o, s_) "v_cmpx_eq_u32 vcc, -1, " a "\n\t" HL0(a, o, s_) "s_mov_b64 exec, -1\n\t"
#define DOT(d, e, a) "v_dot2c_f32_bf16 " d ", %[" e "], " a "\n\t"
#define HCK(c, r) "s_cmp_eq_u32 s7, 0\n\ts_cbranch_scc1 5f\n\ts_call_b64 s[10:11], 4f\n5:\n\ts_waitcnt vmcnt(" #c ")\n\tv_max3_u32 v28, " r "0, " r "1, " r "2\n\tv_lshlrev_b32 v24, 16, " r "0\n\tv_max3_u32 v28, v28, " r "3, " r "4\n\t" \
      "v_and_b32 v25, %[hm], " r "0\n\tv_max_u32 v28, v28, " r "5\n\t" DOT("v24", "e0", r "1") "v_cmp_eq_u32 vcc, -1, v28\n\t" DOT("v25", "e1", r "1") DOT("v24", "e0", r "2") DOT("v25", "e1", r "2") \
      "v_lshlrev_b32 v26, 16, " r "3\n\tv_and_b32 v27, %[hm], " r "3\n\t" DOT("v26", "e0", r "4") DOT("v27", "e1", r "4") DOT("v26", "e0", r "5") DOT("v27", "e1", r "5")
#define RB2(a, b_) "v_cvt_pk_bf16_f32 " a ", 0, " a "\n\tv_cvt_pk_bf16_f32 " b_ ", 0, " b_ "\n\t"
      asm volatile("s_mov_b32 s7, 1\n\ts_mov_b32 s12, %[spin]\n\tv_mov_b64 v[8:9], 0\n\tv_mov_b64 v[10:11], 0\n\tv_mov_b64 v[12:13], 0\n\tv_mov_b64 v[14:15], 0\n"
                   "2:\n\ts_sleep 1\n\tds_read_b32 v28, %[fl] offset:16\n\ts_memrealtime s[8:9]\n\ts_waitcnt lgkmcnt(0)\n\tv_cmp_eq_u32 vcc, 0, v28\n\ts_sub_u32 s8, s8, %[st]\n\t"
                   "s_cmp_lt_u32 s8, %[th]\n\ts_cbranch_scc1 2b\n\ts_bitcmp1_b32 vcc_lo, 3\n\ts_cbranch_scc1 2b\n\t" TS(64, 65)
                   HLD(HLF, "v7") HCK(0, "v7") "s_cbranch_vccnz 9f\n"
                   "3:\n\t" TS(66, 67) RB2("v24", "v25") "v_mul_f32 v28, %[le], v24\n\tv_mul_f32 v29, %[le], v25\n\tv_exp_f32 v28, v28\n\tv_exp_f32 v29, v29\n\t" RB2("v26", "v27")
                   "v_add_f32 v28, 1.0, v28\n\tv_add_f32 v29, 1.0, v29\n\tv_rcp_f32 v28, v28\n\tv_rcp_f32 v29, v29\n\ts_cmp_eq_u32 s7, 0\n\tv_mul_f32 v24, v24, v28\n\tv_mul_f32 v25, v25, v29\n\t"
                   RB2("v24", "v25") "v_mul_f32 v24, v24, v26\n\tv_mul_f32 v25, v25, v27\n\tv_cvt_pk_bf16_f32 v24, v24, v25\n\ts_mov_b32 s12, %[spin]\n\t"
                   "ds_write_b16 %[hw], v24\n\tds_write_b16_d16_hi %[hw], v24 offset:%[hp]\n\ts_cbranch_scc1 8f\n"
                   "7:\n\ts_call_b64 s[10:11], 4f\n\ts_mul_i32 s12, s12, s7\n\ts_sub_u32 s12, s12, 1\n\ts_cbranch_scc0 7b\n"
                   "8:\n\ts_waitcnt lgkmcnt(0)\n\ts_barrier\n\t" TS(60, 61)
                   XR("16:19", 0) XR("20:23", 128)
                   XU(1, "16:19", "32:39") SM("12:15", "16:19", "40:47") XU(0, "20:23", "48:55") SM("12:15", "20:23", "56:63") "s_nop 6\n\t"
                   SWP("v8", "v9", "v10", "v11") "v_add_f32 v8, v8, v10\n\tv_add_f32 v9, v9, v11\n\t" SWP("v12", "v13", "v14", "v15") OUT("v8", "v9", "%[po] offen")
                   "v_add_f32 v12, v12, v14\n\tv_add_f32 v13, v13, v15\n\t" OUT("v12", "v13", "%[po] offen offset:256") TS(62, 63) DONE
                   "9:\n\tv_mov_b64 v[80:81], v[70:71]\n\tv_mov_b64 v[82:83], v[72:73]\n\tv_mov_b64 v[84:85], v[74:75]\n\t" HLD(HL1, "v7") "s_sleep " STR(HNAP) "\n"
                   "1:\n\t" HLD(HL1, "v8") HCK(6, "v7") "s_cbranch_vccz 3b\n\t" HLD(HL1, "v7") HCK(6, "v8") "s_sub_u32 s12, s12, 1\n\ts_cbranch_vccz 3b\n\ts_cbranch_scc0 1b\n\ts_branch 3b\n"
                   "4:\n\tds_read_b32 v28, %[fl]\n\ts_waitcnt lgkmcnt(0)\n\tv_cmp_ne_u32 vcc, -1, v28\n\ts_cbranch_vccnz 6f\n\t"
                   TRD(32, 33, 34, 35, 36, 37, 38, 39, "%[da]", 0) TRD(40, 41, 42, 43, 44, 45, 46, 47, "%[db]", 0)
                   TRD(48, 49, 50, 51, 52, 53, 54, 55, "%[da]", 32768) TRD(56, 57, 58, 59, 60, 61, 62, 63, "%[db]", 32768) "s_mov_b32 s7, 0\n"
                   "6:\n\ts_setpc_b64 s[10:11]\n0:"
                   :: [s1] "v"(so), [o3] "v"(o3), [fl] "v"(fl), [da] "v"(da), [db] "v"(db), [hw] "v"(hw), [xa] "v"(hr), [sel] "v"(sel), [so] "v"(po), [rs] "s"(rs), [h0] "s"(h0), [h1] "s"(h1),
                      [st] "s"(st0), [th] "n"(TH), [spin] "n"(SPIN), [hp] "n"(HP), [po] "s"(O_PG + (4 * kp + ct) * 4096), [e0] "s"(e0), [e1] "s"(e1), [hm] "s"(0xffff0000u),
                      [le] "s"(-1.4426950408889634f)
                   : "vcc", "scc", "s7", "s8", "s9", "s10", "s11", "s12", "memory", G_CLOBBER TSC);
      TSGET(8, 9, 6, 7);
    }
    FLUSH();
    __builtin_amdgcn_endpgm();
  }

  if (w0 >= 8) {
    u32x8 xa0, xa1, xa2, xa3, sa1;
    u32x4 junk;
    u32 op, so;
    ROLE(p, "", , "={v[100:107]}"(xa0), "={v[108:115]}"(xa1), "={v[116:123]}"(xa2),
         "={v148}"(op), "={v149}"(so), "={v[160:163]}"(junk));
    const int p = w - 8;
    __builtin_amdgcn_s_setprio(3);
    asm volatile("s_waitcnt lgkmcnt(0)" : "+{s44}"(tag)::"memory");
    tag += 1u;
    STAMP(9);
    f32x2 xq[4][8];
    u32x8 pa;
    {
#define SCK(r1, r3, r5, r7) "v_xor_b32 %[d], %[t], v" #r1 "\n\tv_bitop3_b32 %[d], %[d], %[t], v" #r3 " bitop3:0xf6\n\t" \
      "v_bitop3_b32 %[d], %[d], %[t], v" #r5 " bitop3:0xf6\n\tv_bitop3_b32 %[d], %[d], %[t], v" #r7 " bitop3:0xf6\n\t"
#define SLX(r0, r3, r4, r7, k, k1, sc) "buffer_load_dwordx4 v[" #r0 ":" #r3 "], %[so], %[rs], " k " offen" sc "\n\tbuffer_load_dwordx4 v[" #r4 ":" #r7 "], %[so], %[rs], " k1 " offen" sc "\n\t"
#define SLD(r0, r3, r4, r7, k, k1) SLX(r0, r3, r4, r7, k, k1, " sc1")
#define SLOOP(r0, r1, r3, r4, r5, r7, k, k1) "1:\n\ts_waitcnt vmcnt(0)\n4:\n\t" SCK(r1, r3, r5, r7) "s_sub_u32 %[n], %[n], 1\n\tv_cmp_ne_u32 vcc, 0, %[d]\n\ts_cbranch_scc1 2f\n\ts_cbranch_vccz 2f\n\t" \
      SLD(r0, r3, r4, r7, k, k1) "s_branch 1b\n2:\n\t"
      u32 d_, n_ = SPIN, k1 = 4096, k2 = 8192, k3 = 12288, s0[4];
      asm volatile("s_mov_b32 m0, %[m]\n\ts_cmp_eq_u32 %[k], 3\n\ts_waitcnt vmcnt(2)\n\ts_cbranch_scc0 1f\n\ts_waitcnt vmcnt(0)\n1:"
                   : "+{v[100:107]}"(xa0), "+{v[108:115]}"(xa1), "+{v[160:163]}"(junk) : [m] "s"(lds0 + OX + 2 * XO + 256 * p), [k] "s"(xk) : "scc", "memory");
      SB();
#define UNPACK(i, xs) _Pragma("unroll") for (int c = 0; c < 8; c++) { \
        xq[i][c] = (c & 1) ? f32x2{hi2f(xs[c >> 1]), hi2f(xs[4 + (c >> 1)])} : f32x2{lo2f(xs[c >> 1]), lo2f(xs[4 + (c >> 1)])}; asm volatile("" : "+v"(xq[i][c].x), "+v"(xq[i][c].y)); }
      UNPACK(0, xa0);
      UNPACK(1, xa1);
      u32 fa = lds0 + OXF + 4 * p, fv = 1;
      asm volatile("" : "+v"(fa), "+v"(fv));
      asm volatile("" : "+s"(k1), "+s"(k2), "+s"(k3), "+s"(n_));
      SB();
      STAMP(1);
#define SCP(r0, r1, r2, r3, r4, r5, r6, r7) "s_waitcnt vmcnt(2)\n\t" SCK(r1, r3, r5, r7) "v_cmp_ne_u32 vcc, 0, %[d]\n\tv_mov_b32 %[a0], v" #r0 "\n\tv_mov_b32 %[a1], v" #r2 "\n\t" \
      "v_mov_b32 %[a2], v" #r4 "\n\tv_mov_b32 %[a3], v" #r6 "\n\ts_cbranch_vccz 3f\n\t" SLD(r0, r3, r4, r7, "0", "%[k1]")
      asm volatile(UNTIL(STR(TSG)) SLX(132, 135, 136, 139, "0", "%[k1]", SCA) "s_sleep " STR(SSL) "\n\t" SLX(140, 143, 144, 147, "0", "%[k1]", SCC)
                   "1:\n\t" SCP(132, 133, 134, 135, 136, 137, 138, 139) SCP(140, 141, 142, 143, 144, 145, 146, 147) "s_sub_u32 %[n], %[n], 1\n\ts_cbranch_scc0 1b\n"
                   "3:\n\t" SLX(140, 143, 144, 147, "%[k2]", "%[k3]", SCB)
                   : [d] "=&v"(d_), [n] "+s"(n_), [a0] "=&v"(s0[0]), [a1] "=&v"(s0[1]), [a2] "=&v"(s0[2]), [a3] "=&v"(s0[3]), "=&{v[132:139]}"(pa), "=&{v[140:147]}"(sa1)
                   : [st] "s"(st0), [t] "s"(tag), [so] "v"(so), [rs] "s"(rs), [k1] "s"(k1), [k2] "s"(k2), [k3] "s"(k3) : "vcc", "scc", "s8", "s9", "memory");
#pragma unroll
      for (int i = 0; i < 5; i++) {
#define XLD(r0, r1, r2, r3, o_) "v_add_u32 %[d], " #o_ ", %[o]\n\tglobal_load_dwordx4 v[" #r0 ":" #r1 "], %[d], %[x]\n\tglobal_load_dwordx4 v[" #r2 ":" #r3 "], %[d], %[x] offset:2048\n"
        if (i == XI) asm volatile("s_cmp_eq_u32 %[k], 3\n\ts_cbranch_scc0 1f\n\t" XLD(116, 119, 120, 123, 0x200000) "1:\n\t" XLD(124, 127, 128, 131, 0x300000)
                                 : [d] "=&v"(d_), "+{v[116:123]}"(xa2), "=&{v[124:131]}"(xa3) : [k] "s"(xk), [o] "v"(op), [x] "s"(x) : "scc", "memory");
        if (i == 2) {
          asm volatile("s_waitcnt vmcnt(2)\n\ts_branch 4f\n" SLOOP(140, 141, 143, 144, 145, 147, "%[k2]", "%[k3]") : [d] "=&v"(d_), [n] "+s"(n_), "+{v[140:147]}"(sa1), "+{v[116:123]}"(xa2), "+{v[132:139]}"(pa)
                       : [t] "s"(tag), [so] "v"(so), [rs] "s"(rs), [k2] "s"(k2), [k3] "s"(k3) : "vcc", "scc", "memory");
          UNPACK(2, xa2);
        }
        if (i == 3) asm volatile("s_waitcnt vmcnt(%1)" : "+{v[124:131]}"(xa3) : "n"(DPI == 2 ? DPF : 0));
        if (i == DPI && DPF) {
          u32 lx = l;
          asm volatile("" : "+v"(lx));
          const u32 ti = min(16 * xk + 4 * p + (lx >> 4), 125u), tsb = __umul24(ti, 57) >> 10, tu = min(ti - __umul24(18, tsb), 15u);
          const u32 o = (128 * (4 * tsb + (tu >> 2)) + DPO / 2 + 4 * j + ((lx >> 2) & 3)) * (2 * D) + 512 * (tu & 3) + 128 * (lx & 3);
          if (DPI == 4) AWAIT(OXF + 4 * (l & 3), 3, "s_sleep 1");
          if (DPF == 1) asm volatile("global_load_dword v160, %1, %2" : "=&{v[160:163]}"(junk) : "v"(o), "s"(wd) : "memory");
          else asm volatile("global_load_dword v160, %1, %2\n\tglobal_load_dword v161, %1, %3" : "=&{v[160:163]}"(junk) : "v"(o), "s"(wd), "s"(wd + 64 * D) : "memory");
        }
        if (i == 4) break;
        if (i == 3) UNPACK(3, xa3);
        const u32 sa = i < 2 ? s0[2 * i] : sa1[4 * i - 8], sb = i < 2 ? s0[2 * i + 1] : sa1[4 * i - 6];
        if (i == 0) STAMP(3);
        if (i == 2) STAMP(10);
        const f32x2 r2 = {__builtin_amdgcn_rcpf(hi2f(sa)), __builtin_amdgcn_rcpf(hi2f(sb))};
        const f32x2 m2 = {-lo2f(sa) * r2.x, -lo2f(sb) * r2.y};
        f32x2 n2[8];
        u32 o[8];
        SB();
#pragma unroll
        for (int c = 0; c < 8; c++) n2[c] = f32x2{fmav(xq[i][c].x, r2.x, m2.x), fmav(xq[i][c].y, r2.y, m2.y)};
        SB();
#pragma unroll
        for (int c = 0; c < 8; c++) o[c] = packh(n2[c].x, n2[c].y);
        SB();
#pragma unroll
        for (int c = 0; c < 8; c++) asm volatile("ds_write_addtid_b32 %0 offset:%1" ::"v"(o[c]), "n"(c * XREG + 1024 * i) : "memory");
        asm volatile("ds_write_b32 %1, %0\n\tv_add_u32 %0, 1, %0" : "+v"(fv) : "v"(fa) : "memory");
        if (i == PDROP) __builtin_amdgcn_s_setprio(PPRIO);
        SB();
      }
    }
    STAMP(4);
    const rsrc_t ro = __builtin_amdgcn_make_buffer_rsrc((void*)out, 0, 2 * L * D, 0x00020000);
    u32 ys[8], y[2][8] = {};
    u32x2 ix;
    u32x4 cl;
    u32x8 pb;
    float yc, rsel;
    const u32 ixo = IXO((l & 6) + (n16 >> 3));
    if (p == 0) asm volatile("buffer_load_dwordx2 v[4:5], %1, %2, 0 offen sc1" : "=&{v[4:5]}"(ix) : "v"(ixo), "s"(rs) : "memory");
    const int rc = 4 * (l & 1) + kg, u = 64 * (p & 1) + l;
    u32x4 pick;
    u32 po = O_PG + 128 * g + 16 * (l & 7) + 16384 * (l >> 3), p3 = l < 32 ? po + 3 * 131072 : 0x80000000u, yf = lds0 + OYP + 4 * min(n16, 12) + 64 * (p >> 1);
    u32 ya = lds0 + OY + CHR * rc, oo = 16 * g + 2 * rc, clo = O_CLR + 16 * min(l, 62);
    u32 yp = lds0 + OY + 512 * (p >> 1) + 4 * (u ^ ((u >> 4) & 6)), yq = yp + 4 * CHR, bp = lds0 + OMISC + 16 * p + 4 * kg, bs = 30 - 2 * n16;
#pragma unroll
    for (int q = 0; q < 4; q++) pick[q] = (n16 >> 2) == q ? 0x3f80u << 16 * (l & 1) : 0u;
    asm volatile("" : "+v"(pick), "+v"(ya), "+v"(oo), "+v"(po), "+v"(p3), "+v"(yf), "+v"(clo), "+v"(yp), "+v"(yq), "+v"(bp), "+v"(bs));
#pragma unroll
    for (int c = 0; c < 8; c++) asm volatile("" : "+v"(y[0][c]), "+v"(y[1][c]));
    if (TE) asm volatile(UNTIL(STR(TE)) :: [st] "s"(st0) : "s8", "s9", "scc");
#pragma unroll
    for (int i = 0; i < 4; i++) {
      if (i == IXI && p == 0) {
        u32 n_ = SPIN;
        asm volatile("s_waitcnt vmcnt(%5)\n1:\n\tv_cmp_ne_u32 vcc, %4, v5\n\ts_sub_u32 %1, %1, 1\n\ts_cbranch_vccz 2f\n\ts_cbranch_scc1 2f\n\t"
                     "buffer_load_dwordx2 v[4:5], %2, %3, 0 offen sc1\n\ts_waitcnt vmcnt(0)\n\ts_branch 1b\n2:"
                     : "+{v[4:5]}"(ix), "+s"(n_) : "v"(ixo), "s"(rs), "s"(tag), "n"(2 * IXI + 1) : "vcc", "scc", "memory");
        const u32 prow = ix.x & 2047u, v = prow >> 1;
        if (l < 16) atomicOr((u32*)(smem + OMISC) + (prow >> 5), 1u << (ix.x & 31u));
        FLAG(OYP + 256 + 32 + 64 * (l & 3), 8u);
        oo += prow * (2 * D);
        asm volatile("v_mov_b32 v162, 0\n\tglobal_load_short_d16_hi v162, %1, %2" : "+{v[160:163]}"(junk) : "v"(oo), "s"(x) : "memory");
        yc = *(const float*)(smem + OSC + 4 * rc); rsel = rbt(sigm(hi2f(ix.x)));
        ya += 4 * (v ^ ((v >> 4) & 6)) + 2 * (prow & 1);
        asm volatile("" : "+v"(yc), "+v"(rsel), "+v"(ya));
        STAMP(11);
      }
      if (i == 2 && p == 0) cl = ld16(rs, clo);
      if (i == 3) {
        asm volatile("s_waitcnt vmcnt(2)" ::: "memory");
        FLAG(OYP + 448 + 36 + 4 * p, 8u);
        if (p == 0) {
#pragma unroll 1
          for (int spin = 0; spin < SPIN; spin++) {
            if (__all(cl.y == tag && cl.w == tag)) break;
            cl = ld16(rs, clo);
          }
          STAMP(12);
        }
        __builtin_amdgcn_s_setprio(LPRIO);
      }
      {
        u32 n_ = SPIN, f_;
        asm volatile("1:\n\tds_read_b32 %0, %2 offset:%3\n\ts_sub_u32 %1, %1, 1\n\ts_waitcnt lgkmcnt(0)\n\tv_cmp_eq_u32 vcc, 0, %0\n\ts_cbranch_scc1 2f\n\ts_and_b32 vcc_lo, vcc_lo, %4\n\t"
                     "s_cbranch_scc0 2f\n\t" YNAP "\n\ts_branch 1b\n2:" : "=&v"(f_), "+s"(n_) : "v"(yf), "n"(128 * i), "n"(i < 2 ? 255 : 511) : "vcc", "scc", "memory");
      }
      if (i == 0) STAMP(5); else if (i == 1) STAMP(6); else if (i == 2) STAMP(7); else STAMP(8);
      u32 bm = 0;
      if (i >= 2) asm volatile("ds_read_b32 %0, %1 offset:%2" : "=v"(bm) : "v"(bp), "n"(64 * i) : "memory");
#pragma unroll
      for (int r = 0; r < 2; r++)
#pragma unroll
        for (int c = 0; c < 8; c++) asm volatile("ds_read_u16_d16_hi %0, %1 offset:%2" : "+v"(y[r][c]) : "v"(c < 4 ? yp : yq), "n"((c & 3) * CHR + 1024 * i + 2 * r) : "memory");
      if (i == 3 && p == 0) {
#define PLD(X) X(132, 133, 134, 135, "%[po]", "0") X(136, 137, 138, 139, "%[po]", "%[k1]") X(140, 141, 142, 143, "%[po]", "%[k2]") X(144, 145, 146, 147, "%[p3]", "0")
#define PL0(a, b_, c, d, o, s_) "buffer_load_dwordx4 v[" #a ":" #d "], " o ", %[rs], " s_ " offen sc1\n\t"
        asm volatile(PLD(PL0) : "=&{v[132:139]}"(pa), "=&{v[140:147]}"(pb) : [po] "v"(po), [p3] "v"(p3), [rs] "s"(rs), [k1] "s"(131072), [k2] "s"(262144) : "memory");
      }
      if (i == 0) {
        st8(rs, t == 512 ? O_ARR + b * 8 : 0x80000000u, u32x2{0u, tag});
        const u32x4 a = *(const u32x4*)(smem + OSC), c = *(const u32x4*)(smem + OSC + 16);
#pragma unroll
        for (int k = 0; k < 4; k++) { ys[k] = (u32)__builtin_amdgcn_readfirstlane((int)a[k]); ys[4 + k] = (u32)__builtin_amdgcn_readfirstlane((int)c[k]); }
      }
      SB();
#pragma unroll
      for (int r = 0; r < 2; r++) {
        asm volatile("s_waitcnt lgkmcnt(%9)" : "+v"(y[r][0]), "+v"(y[r][1]), "+v"(y[r][2]), "+v"(y[r][3]), "+v"(y[r][4]), "+v"(y[r][5]), "+v"(y[r][6]), "+v"(y[r][7]), "+v"(bm) : "n"(8 - 8 * r));
        u32x4 v;
#pragma unroll
        for (int k = 0; k < 4; k++)
          v[k] = pack2(fmas(__uint_as_float(y[r][2 * k]), ys[2 * k], xq[i][2 * k][r]), fmas(__uint_as_float(y[r][2 * k + 1]), ys[2 * k + 1], xq[i][2 * k + 1][r]));
        if (i >= 2 && r == 0) bm <<= bs;
        __builtin_amdgcn_raw_buffer_store_b128(v, ro, i < 2 ? op : op | (bm & 0x40000000u << r), (512 * i + r) * (2 * D), OAUX);
        SB();
        asm volatile("s_nop 1");
        SB();
      }
    }
    asm volatile("" ::"{v[160:163]}"(junk));
    STAMP(13);
    if (p == 0) {
      u32 n_ = SPIN;
#define PL1(a, b_, c, d, o, s_) "v_max3_u32 v150, v" #a ", v" #b_ ", v" #c "\n\tv_max_u32 v150, v150, v" #d "\n\tv_cmpx_eq_u32 vcc, -1, v150\n\t" PL0(a, b_, c, d, o, s_) "s_mov_b64 exec, -1\n\t"
#define PLE(X) X(112, 113, 114, 115, "%[po]", "0") X(116, 117, 118, 119, "%[po]", "%[k1]") X(120, 121, 122, 123, "%[po]", "%[k2]") X(124, 125, 126, 127, "%[p3]", "0")
#define PMX(a, b_) "v_max3_u32 v150, v150, v" a ", v" b_ "\n\t"
#define PMM(d, a, c) "v_mfma_f32_16x16x32_bf16 v[" d "], %[pk], v[" a "], " c "\n\t"
#define PCK(c, p, q) "s_waitcnt vmcnt(" #c ")\n\t" PMM("152:155", p "2:" p "5", "0") "v_max_u32 v150, v" p "2, v" p "3\n\t" PMX(p "4", p "5") PMM("156:159", p "6:" p "9", "0") PMX(p "6", p "7") PMX(p "8", p "9") \
      PMM("152:155", q "0:" q "3", "v[152:155]") PMX(q "0", q "1") PMX(q "2", q "3") PMM("156:159", q "4:" q "7", "v[156:159]") PMX(q "4", q "5") PMX(q "6", q "7") \
      "v_cmp_eq_u32 vcc, -1, v150\n\ts_sub_u32 %[n], %[n], 1\n\t"
#define PCP(a, b_, c, d) "v_mov_b64 v[" #a ":" #b_ "], v[" #c ":" #d "]\n\t"
      asm volatile(PCK(2, "13", "14") "s_cbranch_vccnz 9f\n"
                   "2:\n\t" TS(60, 61) "ds_read_b32 v150, %[yf] offset:448\n\tds_read_u16_d16_hi v151, %[ya]\n\ts_mov_b32 %[n], %[spin]\n\ts_nop 1\n\tv_add_f32 v152, v152, v156\n\tv_add_f32 v153, v153, v157\n\ts_nop 0\n\t"
                   "v_add_f32_dpp v152, v152, v152 row_ror:8 row_mask:0xf bank_mask:0x3\n\tv_add_f32_dpp v152, v153, v153 row_ror:8 row_mask:0xf bank_mask:0xc\n\t"
                   "v_cvt_pk_bf16_f32 v152, 0, v152\n\tv_mul_f32 v152, v152, %[rl]\n\tv_cvt_pk_bf16_f32 v152, 0, v152\n\tv_add_f32 v153, v162, v152\n"
                   "3:\n\ts_waitcnt lgkmcnt(0)\n\tv_cmp_eq_u32 vcc, 0, v150\n\tv_and_b32 v152, 0xffff0000, v151\n\ts_sub_u32 %[n], %[n], 1\n\ts_cbranch_vccz 4f\n\ts_cbranch_scc1 4f\n\t"
                   "ds_read_b32 v150, %[yf] offset:448\n\tds_read_u16_d16_hi v151, %[ya]\n\ts_branch 3b\n"
                   "4:\n\tv_fmac_f32 v153, v152, %[yc]\n\tv_cvt_pk_bf16_f32 v153, v153, v153\n\tbuffer_store_short v153, %[oo], %[ro], 0 offen\n\t" DONE
                   "9:\n\t" PCP(112, 113, 132, 133) PCP(114, 115, 134, 135) PCP(116, 117, 136, 137) PCP(118, 119, 138, 139) PCP(120, 121, 140, 141) PCP(122, 123, 142, 143)
                   PCP(124, 125, 144, 145) PCP(126, 127, 146, 147) PLD(PL1) "s_sleep " STR(PNAP) "\n1:\n\t" PLE(PL1) PCK(4, "13", "14") "s_cbranch_vccz 2b\n\t" PLD(PL1) PCK(4, "11", "12")
                   "s_cbranch_vccz 2b\n\ts_cbranch_scc0 1b\n\ts_branch 2b\n0:"
                   : [n] "+s"(n_), "+{v[132:139]}"(pa), "+{v[140:147]}"(pb)
                   : [po] "v"(po), [p3] "v"(p3), [rs] "s"(rs), [k1] "s"(131072), [k2] "s"(262144), [pk] "v"(pick), [yf] "v"(yf), [ya] "v"(ya), "{v[160:163]}"(junk), [rl] "v"(rsel), [yc] "v"(yc),
                     [oo] "v"(oo), [ro] "s"(ro), [spin] "n"(SPIN)
                   : "vcc", "scc", "memory", "v112", "v113", "v114", "v115", "v116", "v117", "v118", "v119", "v120", "v121", "v122", "v123", "v124", "v125", "v126", "v127", "v150", "v151", "v152", "v153", "v154", "v155", "v156", "v157", "v158", "v159" TSD);
      TSGET(14, 2, 2, 2);
      STAMP(15);
    }
    FLUSH();
    __builtin_amdgcn_endpgm();
  }

  u32 pl, pa, pr, pi, ldtv;
  u32x4 tv, fx, junk;
  ROLE(c, "", , "={v[16:19]}"(tv), "={v[20:23]}"(fx), "={v24}"(ldtv), "={v96}"(pl), "={v97}"(pa), "={v68}"(pr), "={v69}"(pi), "={v[160:163]}"(junk));
  char* W = smem + w * CHR;
  __builtin_amdgcn_s_setprio(2);
  STAMP(3);
  const u32x4 zero4 = {0u, 0u, 0u, 0u};
#pragma unroll
  for (int i = 0; i < 4; i++) *(u32x4*)(W + (i < 3 ? TB + 1024 * i : CHR - 1024) + 16 * l) = zero4;
  SB();
  const u32 wb0 = lds0 + w * CHR, xb0 = lds0 + OX + w * XREG;
  asm volatile("s_mov_b32 m0, %0\n\ts_nop 0\n\tds_write_addtid_b32 %1\n\tds_write_addtid_b32 %1 offset:%2" ::"s"(wb0), "v"(0x3c00u), "n"(PB) : "memory");
  u32 ape = wb0 + (n16 == 12 ? 12 : (n16 ^ 3) - 7 * (n16 >> 3)) * RS + 16 * kg, apo = wb0 + (0x765412b0fedc2301ull >> 4 * n16 & 15) * RS + 16 * kg;
  u32x2 kb = {wb0 + ((15 - n16) & 3) * CPE + 2 * ((15 - n16) & 12) + 16 * kg, wb0 + (((n16 >> 2) & 1) + (n16 & 8) + 2 * (3 - (n16 & 3))) * RS + 16 * kg};
  u32x8 ka = {xb0 + 32 * n16 + 16 * kg, wb0 + OY + 32 * n16 + 8 * (kg ^ ((l >> 2) & 3)),
              lds0 + OXF + 4 * (l & 3), wb0 + TB + ((15 - n16) & 3) * CPO + 2 * ((15 - n16) & 12) + 8 * kg, lds0 + OB + 16 * l,
              l < 32 ? 0x01000100u : 0x03020100u, l < 32 ? 0x03020100u : 0x03020302u, (u32)(l >> 2) & 8u};
  u32 fad = lds0 + OYP + 4 * w, za = wb0 + 16 * kg;
  asm volatile("" : "+{v70}"(ape), "+{v71}"(apo), "+{v[28:35]}"(ka), "+{v[26:27]}"(kb), "+{v25}"(fad), "+{v12}"(za));
  STAMP(4);
  asm volatile("s_waitcnt vmcnt(4)" : "+{v24}"(ldtv), "+{v[160:163]}"(junk));
  float dt = rbt(fexp(hi2f(ldtv)));
  asm volatile("" : "+v"(dt));
  const f32x2 cdt = {rdl(dt * -1.4426950408889634f, 0), rdl(dt * 0.15915494309189535f, 0)};
  const u32 dk = (u32)__builtin_amdgcn_readfirstlane((int)junk.x) << 16, db = max(__float_as_uint(rdl(dt, 0)), 27u << 23);
  u32 em = max(dk >> 23 & 255u, 127u) + 119 - (db >> 23);
  asm("s_max_i32 %0, %0, 8\n\ts_min_u32 %0, %0, 0xfe" : "+s"(em) :: "scc");
  SB();
  STAMP(10);
  STAMPC(11);
  {
    u32 e, tmp, o0, o1, of, os;
    asm volatile(GEN_ASM
                 : [e] "=&s"(e), [t] "=&s"(tmp), "=&{v93}"(o0), "=&{v46}"(o1), "=&{v48}"(of), "=&{v8}"(os), "+{v25}"(fad), "+{v12}"(za), "+{v68}"(pr), "+{v69}"(pi), "+{v70}"(ape),
                   "+{v71}"(apo), "+{v96}"(pl), "+{v97}"(pa), "+{v[28:35]}"(ka), "+{v[26:27]}"(kb), "+{v[16:19]}"(tv), "+{v[20:23]}"(fx)
                 : [w] "s"(wb0), [c] "s"(cdt), [k] "s"(f32x2{1.4426950408889634f, 256.f}), [em] "s"(em)
                 : "memory", "scc", GEN_CLOBBER);
    STAMP(1);
    STAMPC(12);
    const float ys = __uint_as_float((e - 5) << 23) * __uint_as_float(db);
    asm volatile("v_rcp_f32 %1, %2\n\ts_nop 0\n\tv_fma_f32 %1, %3, %1, %1\n\tv_fma_mixhi_f16 %0, %1, 1.0, %0 op_sel:[0,0,1] op_sel_hi:[0,0,1]\n\ts_nop 0"
                 : "+v"(o0), "=&v"(dt) : "v"(ys), "s"(dk));
    const u32 k0 = (u32)__builtin_amdgcn_readlane((int)o0, 3), k256 = (u32)__builtin_amdgcn_readlane((int)o1, 60);
    const u32 k512 = (u32)__builtin_amdgcn_readlane((int)os, 28), k1024 = (u32)__builtin_amdgcn_readlane((int)of, 55);
    *(LDS u16*)(size_t)(wb0 + tv.z) = (u16)__builtin_amdgcn_perm((k512 & 0xffffu) | (k1024 << 16), (k0 >> 16) | (k256 << 16), tv.w);
    *(float*)(smem + OSC + 4 * w) = ys;
    fx &= ((k1024 & 0xffffu) | (k1024 << 16)) ^ 0x80008000u;
  }
  asm volatile(CONV_ASM : "+{v[28:35]}"(ka) : "{v[26:27]}"(kb), "{v25}"(fad), "{v[48:51]}"(fx), [w] "s"(w) : "vcc", "scc", "s6", "memory", CONV_CLOBBER TSC);
  TSGET(2, 5, 6, 7);
  STAMP(13);
  STAMPC(15);
  FLUSH();
  __builtin_amdgcn_endpgm();
}

extern "C" void kernel_launch(void* const* d_in, const int* in_sizes, int n_in, void* d_out, int out_size,
                              void* d_ws, size_t ws_size, hipStream_t stream) {
  const u16* const* in = (const u16* const*)d_in;
  fused<<<256, 768, 0, stream>>>(in[0], in[1], in[2], in[3], in[4], in[5], in[6], in[8], in[9], in[10], in[11],
                                 (u16*)d_out, (char*)d_ws);
}
```

```cpp
#include <hip/hip_runtime.h>
#include <stdint.h>

#define STR2(x) #x
#define STR(x) STR2(x)
#define LA 4
#define WL2 44
#define WL3 100
#define PH 1246
#define ABL 0
#define PPRIO 0
#define CPRIO 1
#define GDELAY 0
#define ROT 1
#define PRE 2
#define GNT 0
#define OAUX 0
#define LNT 1
#define PNT ""
#define LNNT ""
#define PRENT ""
#define PFN 15
#define PFO 512
#define DBAR 2
#define THR 1
#define QD 11
#define HNAP0 4
#define HNAP 4
#define KR 3
#define KI 2
#define QE 11
#define QA 5
#define NA 0
#define CH 0
#define G0E 0
#define DDL 0
#define WBM 0
#define WBW 1
#define HQ 3
#define RSL 5
#define DPF 1
#define HELP 0
#define WARM 0
#define HPI 0
#define Q0 6
#define NP 10
#define HW 28
#define NE 0
#define YNAP "s_sleep 2"
#define QS 7
#define QAS 6
#define RAMP 4
#define GNAP "s_sleep 1"
#define RNAP "s_sleep 1"
#define CLP 0
#define CSL 8
#define XSP 0
#define SPEC 0
#define HOLD 8
#define DPRIO 3
#define HAUX 16
#define DPO 128
#define TH 590
#define CLD 245
#define IXI 1
#define LPRIO 0
#define DPI 2
#define PDROP 3
#define TE 0
#define CNAP 2
#define XI 1
#define SCA " sc1"
#define SCB " sc1"
#define SCC " sc1"
#define SCT " sc1"
#define SCU " sc1"
#define SCH " sc1"
#define SCR " sc1"
#define TSG 280
#define SSL 5
#define PNAP 4
#define TW 310
#define TL 4352
#define CODE_KB 24
typedef unsigned short u16;
typedef unsigned int u32;
typedef __attribute__((ext_vector_type(8))) __bf16 bf16x8;
typedef __attribute__((ext_vector_type(2))) __bf16 bf16x2;
typedef __attribute__((ext_vector_type(8))) _Float16 f16x8;
typedef __attribute__((ext_vector_type(2))) _Float16 f16x2;
typedef __attribute__((ext_vector_type(4))) short v4i16;
typedef __attribute__((ext_vector_type(4))) float f32x4;
typedef __attribute__((ext_vector_type(2))) float f32x2;
typedef __attribute__((ext_vector_type(4))) u32 u32x4;
typedef __attribute__((ext_vector_type(2))) u32 u32x2;
typedef __attribute__((ext_vector_type(8))) u32 u32x8;
typedef __attribute__((ext_vector_type(2), aligned(4))) u32 u32x2_a4;
typedef __amdgpu_buffer_rsrc_t rsrc_t;
#define LDS __attribute__((address_space(3)))

#define TLARG
#define TLVAL
#define STAMP(I)
#define STAMPC(I)
#define FLUSH()

constexpr int L = 2048, D = 1024, NM = 128, F = 3584;
constexpr u32 O_ARR = 0x1000, O_CLR = 0x2000, O_RG = 0x4000, O_SG = 0x8000, O_IDX = 0x10000, O_HG = 0x20000,
              O_PG = 0xd0000, O_TOP = 0x140000, TOPS = 8 * TL, WS_END = O_TOP + 2 * TOPS;
constexpr int SPIN = 1 << 16;

__device__ __forceinline__ float lo2f(u32 w) { return __uint_as_float(w << 16); }
__device__ __forceinline__ float hi2f(u32 w) { return __uint_as_float(w & 0xffff0000u); }
__device__ __forceinline__ u32 pack2(float a, float b) {
  bf16x2 r = {(__bf16)a, (__bf16)b};
  return __builtin_bit_cast(u32, r);
}
__device__ __forceinline__ u32 packh(float a, float b) {
  f16x2 r = {(_Float16)a, (_Float16)b};
  return __builtin_bit_cast(u32, r);
}
__device__ __forceinline__ float rbt(float f) { return __uint_as_float(pack2(0.f, f)); }
__device__ __forceinline__ float rb(float f) {
  float r;
  asm("v_cvt_pk_bf16_f32 %0, 0, %1" : "=v"(r) : "v"(f));
  return r;
}
__device__ __forceinline__ u32 hadd(u32 a, u32 b) {
  return __builtin_bit_cast(u32, __builtin_bit_cast(f16x2, a) + __builtin_bit_cast(f16x2, b));
}
__device__ __forceinline__ float hlo(u32 a) { return (float)__builtin_bit_cast(f16x2, a).x; }
__device__ __forceinline__ float hhi(u32 a) { return (float)__builtin_bit_cast(f16x2, a).y; }

template <int CTRL, int RM>
__device__ __forceinline__ float dppf(float v) {
  return __builtin_bit_cast(float, __builtin_amdgcn_update_dpp(0, __builtin_bit_cast(int, v), CTRL, RM, 0xf, true));
}
template <int CTRL, int RM>
__device__ __forceinline__ u32 dppu(u32 v) {
  return (u32)__builtin_amdgcn_update_dpp(0, (int)v, CTRL, RM, 0xf, true);
}
__device__ __forceinline__ float rdl(float v, int i) {
  return __builtin_bit_cast(float, __builtin_amdgcn_readlane(__builtin_bit_cast(int, v), i));
}
__device__ __forceinline__ u32 wmaxu(u32 v) {
  v = max(v, dppu<0x111, 0xf>(v)); v = max(v, dppu<0x112, 0xf>(v)); v = max(v, dppu<0x114, 0xf>(v));
  v = max(v, dppu<0x118, 0xf>(v)); v = max(v, dppu<0x142, 0xa>(v)); v = max(v, dppu<0x143, 0xc>(v));
  return (u32)__builtin_amdgcn_readlane((int)v, 63);
}
__device__ __forceinline__ void swap32(float& a, float& b) {
  auto r = __builtin_amdgcn_permlane32_swap(__float_as_uint(a), __float_as_uint(b), false, false);
  a = __uint_as_float(r[0]); b = __uint_as_float(r[1]);
}
__device__ __forceinline__ void swap16(float& a, float& b) {
  auto r = __builtin_amdgcn_permlane16_swap(__float_as_uint(a), __float_as_uint(b), false, false);
  a = __uint_as_float(r[0]); b = __uint_as_float(r[1]);
}
__device__ __forceinline__ u32x2 trread(const char* p) {
  v4i16 r = __builtin_amdgcn_ds_read_tr16_b64_v4i16((LDS v4i16*)p);
  return __builtin_bit_cast(u32x2, r);
}
__device__ __forceinline__ u32x2 trl(LDS char* p) { return __builtin_bit_cast(u32x2, __builtin_amdgcn_ds_read_tr16_b64_v4i16((LDS v4i16*)p)); }
__device__ __forceinline__ f16x8 mkh(u32x2 a, u32x2 b) {
  u32x4 v = {a.x, a.y, b.x, b.y};
  return __builtin_bit_cast(f16x8, v);
}
__device__ __forceinline__ bf16x8 mkb(u32x2 a, u32x2 b) {
  u32x4 v = {a.x, a.y, b.x, b.y};
  return __builtin_bit_cast(bf16x8, v);
}
__device__ __forceinline__ bf16x8 asb(u32x4 v) { return __builtin_bit_cast(bf16x8, v); }
__device__ __forceinline__ f16x8 ash(u32x4 v) { return __builtin_bit_cast(f16x8, v); }
#define MFMAB(a, b, c) __builtin_amdgcn_mfma_f32_16x16x32_bf16(a, b, c, 0, 0, 0)
#define MFMAH(a, b, c) __builtin_amdgcn_mfma_f32_16x16x32_f16(a, b, c, 0, 0, 0)

__device__ __forceinline__ u32x4 ld16(rsrc_t r, u32 off) { return __builtin_amdgcn_raw_buffer_load_b128(r, off, 0, 16); }
__device__ __forceinline__ u32x2 ld8(rsrc_t r, u32 off) { return __builtin_amdgcn_raw_buffer_load_b64(r, off, 0, 16); }
__device__ __forceinline__ void st16(rsrc_t r, u32 off, u32x4 v) { __builtin_amdgcn_raw_buffer_store_b128(v, r, off, 0, 16); }
__device__ __forceinline__ void st8(rsrc_t r, u32 off, u32x2 v) { __builtin_amdgcn_raw_buffer_store_b64(v, r, off, 0, 16); }
#define NAP() asm volatile("s_sleep 1" ::: "memory")
#define SB() __builtin_amdgcn_sched_barrier(0)
#define LBAR() asm volatile("s_waitcnt lgkmcnt(0)\n\ts_barrier" ::: "memory")

__device__ __forceinline__ float fexp(float v) { return __builtin_amdgcn_exp2f(v * 1.4426950408889634f); }
__device__ __forceinline__ float dot2b(u32 a, u32 b, float c) {
  return __builtin_amdgcn_fdot2_f32_bf16(__builtin_bit_cast(bf16x2, a), __builtin_bit_cast(bf16x2, b), c, false);
}
__device__ __forceinline__ float sigm(float v) { return __builtin_amdgcn_rcpf(1.f + __builtin_amdgcn_exp2f(-1.4426950408889634f * v)); }

constexpr int RS = 288, PB = 16 * RS;
constexpr int CPE = 1216, TB = 2 * PB, CPO = 832;
constexpr int T2B = 4, T3B = 224, C2 = 52, C3 = 84;
constexpr int OY = 0, CHR = TB + 4 * CPO;
constexpr int OX = 8 * CHR, XREG = 4640, XO = 266;
constexpr int OMISC = OX + 8 * XREG;
constexpr int OXF = OMISC + 320, OSC = OMISC + 352, OYP = OMISC + 448;
constexpr int OB = OMISC + 1024, SMEM = 148480;
constexpr int FA = 131072, FH = FA + 8192, HP = 576, FC = FH + 8 * HP;
static_assert(FC + 1024 <= SMEM && OB + 4096 <= SMEM, "lds map");

struct Tab { u32 v[6][64][4]; };
constexpr Tab mktab() {
  Tab T = {};
  for (int l = 0; l < 64; l++) {
    const int n16 = l & 15, kg = l >> 4, pc = ((n16 >> 2) & 1) + (n16 & 8) + 2 * (3 - (n16 & 3)), odd = kg >> 1, s = l & 3;
    const int e0 = 534 - 258 - 128 - 8 * pc - 4 * (kg & 1) - odd;
    const int ez = ((l >> 3) & 3) * CPE + ((l & 4) ? 1056 - 64 : 0) + 16 * (l & 7);
    const int b2 = T2B + 44 + C2 * ((n16 & 1) + 2 * (kg & 1)) - 8 * (n16 & 2), b3 = T3B + 60 + C3 * (kg & 1) - 16 * s;
    const int to = TB + 2 * ((n16 < 8 ? b2 : b3) - 4 * odd - 8) + ((l & 4) ? 0 : 2 * CPO - 4);
    const int q = (l >> 2) & 7, tap = q == 0 ? ((s & 1) ? 0 : 256) : q == 1 ? 257 : q == 7 ? 0 : 509 + q;
    const int sp = q != 6 ? s * (CPE - 2) + 2 * (537 - tap) : TB + s * (CPO - 2) + 2 * (T2B + 15);
    T.v[0][l][0] = (2 * e0 + (2 - odd) * (CPE - 2)) | (2 * e0 + 3 * odd * (CPE - 2)) << 16;
    T.v[0][l][1] = ez | to << 16;
    T.v[0][l][2] = sp;
    T.v[0][l][3] = !tap ? 0x0100u : q == 6 ? 0x0706u : q >= 2 ? 0x0504u : 0x0302u;
    for (int k = 0; k < 8; k++) {
      const int d = 8 * kg + k - n16;
      if (d == 13) T.v[1][l][k >> 1] |= 0xffffu << 16 * (k & 1);
      for (int h = 1; h <= 4; h++)
        if (d >= (h == 1 ? 14 : 10) - h && d <= (h == 1 ? 14 : 10) + h) T.v[1 + h][l][k >> 1] |= 0x3c00u << 16 * (k & 1);
    }
  }
  return T;
}
extern "C" __device__ const __attribute__((aligned(128), used, visibility("hidden"))) Tab lane_tab = mktab();

#define GEN_ASM \
  "s_waitcnt vmcnt(2)\n\tv_lshlrev_b32 v56, 16, v96\n\t" \
  "v_and_b32 v57, 0xffff0000, v96\n\tv_pk_mul_f32 v[56:57], %[k], v[56:57] op_sel_hi:[0,1]\n\t" \
  "v_lshlrev_b32 v49, 16, v97\n\tv_exp_f32 v56, v56\n\t" \
  "v_exp_f32 v57, v57\n\tv_and_b32 v51, 0xffff0000, v97\n\t" \
  "v_cvt_pk_bf16_f32 v56, v56, v57\n\tv_lshlrev_b32 v48, 16, v56\n\t" \
  "v_and_b32 v50, 0xffff0000, v56\n\tv_pk_mul_f32 v[48:49], %[c], v[48:49]\n\t" \
  "v_pk_mul_f32 v[50:51], %[c], v[50:51]\n\tv_pk_mul_f32 v[98:99], %[k], v[48:49] op_sel:[1,0]\n\t" \
  "v_pk_mul_f32 v[100:101], %[k], v[50:51] op_sel:[1,0]\n\tv_fract_f32 v49, v49\n\t" \
  "v_fract_f32 v51, v51\n\tv_exp_f32 v48, v48\n\t" \
  "v_exp_f32 v50, v50\n\tv_cos_f32 v40, v49\n\t" \
  "v_sin_f32 v41, v49\n\tv_cos_f32 v42, v51\n\t" \
  "v_sin_f32 v43, v51\n\tv_pk_mul_f32 v[40:41], v[40:41], v[48:49] op_sel_hi:[1,0]\n\t" \
  "v_pk_mul_f32 v[42:43], v[42:43], v[50:51] op_sel_hi:[1,0]\n\tv_pk_mul_f32 v[48:49], v[40:41], v[40:41] op_sel:[1,1] op_sel_hi:[1,0] neg_lo:[1,0]\n\t" \
  "v_pk_mul_f32 v[50:51], v[42:43], v[42:43] op_sel:[1,1] op_sel_hi:[1,0] neg_lo:[1,0]\n\tv_pk_fma_f32 v[48:49], v[40:41], v[40:41], v[48:49] op_sel:[0,0,0] op_sel_hi:[0,1,1]\n\t" \
  "v_pk_fma_f32 v[50:51], v[42:43], v[42:43], v[50:51] op_sel:[0,0,0] op_sel_hi:[0,1,1]\n\tv_cvt_pk_f16_f32 v72, v40, -v41\n\t" \
  "v_cvt_pk_f16_f32 v73, v42, -v43\n\tv_pk_mul_f32 v[52:53], v[48:49], v[40:41] op_sel:[1,1] op_sel_hi:[1,0] neg_lo:[1,0]\n\t" \
  "v_pk_mul_f32 v[54:55], v[50:51], v[42:43] op_sel:[1,1] op_sel_hi:[1,0] neg_lo:[1,0]\n\tv_pk_fma_f32 v[52:53], v[48:49], v[40:41], v[52:53] op_sel:[0,0,0] op_sel_hi:[0,1,1]\n\t" \
  "v_pk_fma_f32 v[54:55], v[50:51], v[42:43], v[54:55] op_sel:[0,0,0] op_sel_hi:[0,1,1]\n\tv_pk_mul_f32 v[56:57], v[48:49], v[48:49] op_sel:[1,1] op_sel_hi:[1,0] neg_lo:[1,0]\n\t" \
  "v_pk_mul_f32 v[58:59], v[50:51], v[50:51] op_sel:[1,1] op_sel_hi:[1,0] neg_lo:[1,0]\n\tv_pk_fma_f32 v[56:57], v[48:49], v[48:49], v[56:57] op_sel:[0,0,0] op_sel_hi:[0,1,1]\n\t" \
  "v_pk_fma_f32 v[58:59], v[50:51], v[50:51], v[58:59] op_sel:[0,0,0] op_sel_hi:[0,1,1]\n\tv_cvt_pk_f16_f32 v74, v48, -v49\n\t" \
  "v_cvt_pk_f16_f32 v75, v50, -v51\n\tv_cvt_pk_f16_f32 v76, v52, -v53\n\t" \
  "v_cvt_pk_f16_f32 v77, v54, -v55\n\tv_pk_mul_f32 v[52:53], v[56:57], v[48:49] op_sel:[1,1] op_sel_hi:[1,0] neg_lo:[1,0]\n\t" \
  "v_pk_mul_f32 v[54:55], v[58:59], v[50:51] op_sel:[1,1] op_sel_hi:[1,0] neg_lo:[1,0]\n\tv_pk_fma_f32 v[52:53], v[56:57], v[48:49], v[52:53] op_sel:[0,0,0] op_sel_hi:[0,1,1]\n\t" \
  "v_pk_fma_f32 v[54:55], v[58:59], v[50:51], v[54:55] op_sel:[0,0,0] op_sel_hi:[0,1,1]\n\tv_pk_mul_f32 v[60:61], v[56:57], v[56:57] op_sel:[1,1] op_sel_hi:[1,0] neg_lo:[1,0]\n\t" \
  "v_pk_mul_f32 v[62:63], v[58:59], v[58:59] op_sel:[1,1] op_sel_hi:[1,0] neg_lo:[1,0]\n\tv_pk_fma_f32 v[60:61], v[56:57], v[56:57], v[60:61] op_sel:[0,0,0] op_sel_hi:[0,1,1]\n\t" \
  "v_pk_fma_f32 v[62:63], v[58:59], v[58:59], v[62:63] op_sel:[0,0,0] op_sel_hi:[0,1,1]\n\tv_cvt_pk_f16_f32 v78, v56, -v57\n\t" \
  "v_cvt_pk_f16_f32 v79, v58, -v59\n\tv_cvt_pk_f16_f32 v82, v52, -v53\n\t" \
  "v_cvt_pk_f16_f32 v83, v54, -v55\n\tv_pk_mul_f16 v80, v78, v72 op_sel:[1,1] op_sel_hi:[1,0] neg_lo:[1,0]\n\t" \
  "v_pk_mul_f16 v81, v79, v73 op_sel:[1,1] op_sel_hi:[1,0] neg_lo:[1,0]\n\tv_pk_fma_f16 v80, v78, v72, v80 op_sel:[0,0,0] op_sel_hi:[0,1,1]\n\t" \
  "v_pk_fma_f16 v81, v79, v73, v81 op_sel:[0,0,0] op_sel_hi:[0,1,1]\n\tv_pk_mul_f16 v84, v78, v76 op_sel:[1,1] op_sel_hi:[1,0] neg_lo:[1,0]\n\t" \
  "v_pk_mul_f16 v85, v79, v77 op_sel:[1,1] op_sel_hi:[1,0] neg_lo:[1,0]\n\tv_pk_fma_f16 v84, v78, v76, v84 op_sel:[0,0,0] op_sel_hi:[0,1,1]\n\t" \
  "v_pk_fma_f16 v85, v79, v77, v85 op_sel:[0,0,0] op_sel_hi:[0,1,1]\n\tv_cvt_pk_f16_f32 v86, v60, -v61\n\t" \
  "v_cvt_pk_f16_f32 v87, v62, -v63\n\tds_write_addtid_b32 v72 offset:288\n\t" \
  "ds_write_addtid_b32 v73 offset:4896\n\tds_write_addtid_b32 v74 offset:576\n\t" \
  "ds_write_addtid_b32 v75 offset:5184\n\tds_write_addtid_b32 v76 offset:864\n\t" \
  "ds_write_addtid_b32 v77 offset:5472\n\tds_write_addtid_b32 v78 offset:1152\n\t" \
  "ds_write_addtid_b32 v79 offset:5760\n\tds_write_addtid_b32 v80 offset:1440\n\t" \
  "ds_write_addtid_b32 v81 offset:6048\n\tds_write_addtid_b32 v82 offset:1728\n\t" \
  "ds_write_addtid_b32 v83 offset:6336\n\tds_write_addtid_b32 v84 offset:2016\n\t" \
  "ds_write_addtid_b32 v85 offset:6624\n\tds_write_addtid_b32 v86 offset:3456\n\t" \
  "ds_write_addtid_b32 v87 offset:8064\n\tds_read_b128 v[104:107], v70 offset:0\n\t" \
  "ds_read_b128 v[108:111], v70 offset:64\n\tds_read_b128 v[112:115], v70 offset:128\n\t" \
  "ds_read_b128 v[116:119], v70 offset:192\n\tds_read_b128 v[120:123], v70 offset:4608\n\t" \
  "ds_read_b128 v[124:127], v70 offset:4672\n\tds_read_b128 v[128:131], v70 offset:4736\n\t" \
  "ds_read_b128 v[132:135], v70 offset:4800\n\tv_fract_f32 v99, v99\n\t" \
  "v_fract_f32 v101, v101\n\tv_exp_f32 v98, v98\n\t" \
  "v_exp_f32 v100, v100\n\tv_cos_f32 v44, v99\n\t" \
  "v_sin_f32 v45, v99\n\tv_cos_f32 v46, v101\n\t" \
  "v_sin_f32 v47, v101\n\tv_pk_mul_f32 v[44:45], v[44:45], v[98:99] op_sel_hi:[1,0]\n\t" \
  "v_pk_mul_f32 v[46:47], v[46:47], v[100:101] op_sel_hi:[1,0]\n\ts_waitcnt vmcnt(0)\n\t" \
  "v_pk_mul_f32 v[48:49], v[44:45], v[44:45] op_sel:[1,1] op_sel_hi:[1,0] neg_lo:[1,0]\n\tv_pk_mul_f32 v[50:51], v[46:47], v[46:47] op_sel:[1,1] op_sel_hi:[1,0] neg_lo:[1,0]\n\t" \
  "v_pk_fma_f32 v[48:49], v[44:45], v[44:45], v[48:49] op_sel:[0,0,0] op_sel_hi:[0,1,1]\n\tv_pk_fma_f32 v[50:51], v[46:47], v[46:47], v[50:51] op_sel:[0,0,0] op_sel_hi:[0,1,1]\n\t" \
  "v_and_b32 v102, 0x7fff7fff, v68\n\tv_cvt_pk_f16_f32 v88, v44, -v45\n\t" \
  "v_cvt_pk_f16_f32 v89, v46, -v47\n\tv_pk_mul_f32 v[52:53], v[48:49], v[44:45] op_sel:[1,1] op_sel_hi:[1,0] neg_lo:[1,0]\n\t" \
  "v_pk_mul_f32 v[54:55], v[50:51], v[46:47] op_sel:[1,1] op_sel_hi:[1,0] neg_lo:[1,0]\n\tv_and_b32 v103, 0x7fff7fff, v69\n\t" \
  "v_pk_fma_f32 v[52:53], v[48:49], v[44:45], v[52:53] op_sel:[0,0,0] op_sel_hi:[0,1,1]\n\tv_pk_fma_f32 v[54:55], v[50:51], v[46:47], v[54:55] op_sel:[0,0,0] op_sel_hi:[0,1,1]\n\t" \
  "v_pk_mul_f32 v[56:57], v[48:49], v[48:49] op_sel:[1,1] op_sel_hi:[1,0] neg_lo:[1,0]\n\tv_pk_mul_f32 v[58:59], v[50:51], v[50:51] op_sel:[1,1] op_sel_hi:[1,0] neg_lo:[1,0]\n\t" \
  "v_pk_max_u16 v102, v102, v103\n\tv_pk_fma_f32 v[56:57], v[48:49], v[48:49], v[56:57] op_sel:[0,0,0] op_sel_hi:[0,1,1]\n\t" \
  "v_pk_fma_f32 v[58:59], v[50:51], v[50:51], v[58:59] op_sel:[0,0,0] op_sel_hi:[0,1,1]\n\tv_cvt_pk_f16_f32 v90, v48, -v49\n\t" \
  "v_cvt_pk_f16_f32 v91, v50, -v51\n\tv_pk_max_u16 v102, v102, v102 op_sel:[1,0] op_sel_hi:[0,1]\n\t" \
  "v_pk_mul_f32 v[64:65], v[56:57], v[60:61] op_sel:[1,1] op_sel_hi:[1,0] neg_lo:[1,0]\n\tv_pk_mul_f32 v[66:67], v[58:59], v[62:63] op_sel:[1,1] op_sel_hi:[1,0] neg_lo:[1,0]\n\t" \
  "v_pk_fma_f32 v[64:65], v[56:57], v[60:61], v[64:65] op_sel:[0,0,0] op_sel_hi:[0,1,1]\n\tv_pk_fma_f32 v[66:67], v[58:59], v[62:63], v[66:67] op_sel:[0,0,0] op_sel_hi:[0,1,1]\n\t" \
  "v_max_u32_dpp v102, v102, v102 row_shr:1 row_mask:0xf bank_mask:0xf\n\tv_cvt_pk_f16_f32 v92, v52, -v53\n\t" \
  "v_cvt_pk_f16_f32 v93, v54, -v55\n\tv_pk_mul_f16 v136, v88, v74 op_sel:[1,1] op_sel_hi:[1,0] neg_lo:[1,0]\n\t" \
  "v_pk_mul_f16 v137, v89, v75 op_sel:[1,1] op_sel_hi:[1,0] neg_lo:[1,0]\n\tv_max_u32_dpp v102, v102, v102 row_shr:2 row_mask:0xf bank_mask:0xf\n\t" \
  "v_pk_fma_f16 v136, v88, v74, v136 op_sel:[0,0,0] op_sel_hi:[0,1,1]\n\tv_pk_fma_f16 v137, v89, v75, v137 op_sel:[0,0,0] op_sel_hi:[0,1,1]\n\t" \
  "v_pk_mul_f16 v138, v88, v78 op_sel:[1,1] op_sel_hi:[1,0] neg_lo:[1,0]\n\tv_pk_mul_f16 v139, v89, v79 op_sel:[1,1] op_sel_hi:[1,0] neg_lo:[1,0]\n\t" \
  "v_max_u32_dpp v102, v102, v102 row_shr:4 row_mask:0xf bank_mask:0xf\n\tv_pk_fma_f16 v138, v88, v78, v138 op_sel:[0,0,0] op_sel_hi:[0,1,1]\n\t" \
  "v_pk_fma_f16 v139, v89, v79, v139 op_sel:[0,0,0] op_sel_hi:[0,1,1]\n\tv_cvt_pk_f16_f32 v94, v64, -v65\n\t" \
  "v_cvt_pk_f16_f32 v95, v66, -v67\n\tv_max_u32_dpp v102, v102, v102 row_shr:8 row_mask:0xf bank_mask:0xf\n\t" \
  "v_pk_mul_f16 v140, v88, v82 op_sel:[1,1] op_sel_hi:[1,0] neg_lo:[1,0]\n\tv_pk_mul_f16 v141, v89, v83 op_sel:[1,1] op_sel_hi:[1,0] neg_lo:[1,0]\n\t" \
  "v_pk_fma_f16 v140, v88, v82, v140 op_sel:[0,0,0] op_sel_hi:[0,1,1]\n\tv_pk_fma_f16 v141, v89, v83, v141 op_sel:[0,0,0] op_sel_hi:[0,1,1]\n\t" \
  "ds_write_addtid_b32 v88 offset:3168\n\tds_write_addtid_b32 v89 offset:7776\n\t" \
  "ds_write_addtid_b32 v136 offset:0\n\tds_write_addtid_b32 v137 offset:4608\n\t" \
  "ds_write_addtid_b32 v138 offset:288\n\tds_write_addtid_b32 v139 offset:4896\n\t" \
  "ds_write_addtid_b32 v94 offset:1152\n\tds_write_addtid_b32 v95 offset:5760\n\t" \
  "ds_write_addtid_b32 v140 offset:576\n\tds_write_addtid_b32 v141 offset:5184\n\t" \
  "v_max_u32_dpp v102, v102, v102 row_bcast:15 row_mask:0xa bank_mask:0xf\n\tv_pk_mul_f16 v142, v88, v86 op_sel:[1,1] op_sel_hi:[1,0] neg_lo:[1,0]\n\t" \
  "v_pk_mul_f16 v143, v89, v87 op_sel:[1,1] op_sel_hi:[1,0] neg_lo:[1,0]\n\tv_pk_fma_f16 v142, v88, v86, v142 op_sel:[0,0,0] op_sel_hi:[0,1,1]\n\t" \
  "v_pk_fma_f16 v143, v89, v87, v143 op_sel:[0,0,0] op_sel_hi:[0,1,1]\n\tv_max_u32_dpp v102, v102, v102 row_bcast:31 row_mask:0xc bank_mask:0xf\n\t" \
  "v_pk_mul_f16 v144, v90, v78 op_sel:[1,1] op_sel_hi:[1,0] neg_lo:[1,0]\n\tv_pk_mul_f16 v145, v91, v79 op_sel:[1,1] op_sel_hi:[1,0] neg_lo:[1,0]\n\t" \
  "v_pk_fma_f16 v144, v90, v78, v144 op_sel:[0,0,0] op_sel_hi:[0,1,1]\n\tv_pk_fma_f16 v145, v91, v79, v145 op_sel:[0,0,0] op_sel_hi:[0,1,1]\n\t" \
  "v_readlane_b32 %[e], v102, 63\n\tv_pk_mul_f16 v146, v90, v86 op_sel:[1,1] op_sel_hi:[1,0] neg_lo:[1,0]\n\t" \
  "v_pk_mul_f16 v147, v91, v87 op_sel:[1,1] op_sel_hi:[1,0] neg_lo:[1,0]\n\tv_pk_fma_f16 v146, v90, v86, v146 op_sel:[0,0,0] op_sel_hi:[0,1,1]\n\t" \
  "v_pk_fma_f16 v147, v91, v87, v147 op_sel:[0,0,0] op_sel_hi:[0,1,1]\n\tv_pk_mul_f16 v148, v92, v78 op_sel:[1,1] op_sel_hi:[1,0] neg_lo:[1,0]\n\t" \
  "v_pk_mul_f16 v149, v93, v79 op_sel:[1,1] op_sel_hi:[1,0] neg_lo:[1,0]\n\tv_pk_fma_f16 v148, v92, v78, v148 op_sel:[0,0,0] op_sel_hi:[0,1,1]\n\t" \
  "v_pk_fma_f16 v149, v93, v79, v149 op_sel:[0,0,0] op_sel_hi:[0,1,1]\n\tv_pk_mul_f16 v150, v92, v86 op_sel:[1,1] op_sel_hi:[1,0] neg_lo:[1,0]\n\t" \
  "v_pk_mul_f16 v151, v93, v87 op_sel:[1,1] op_sel_hi:[1,0] neg_lo:[1,0]\n\tv_pk_fma_f16 v150, v92, v86, v150 op_sel:[0,0,0] op_sel_hi:[0,1,1]\n\t" \
  "v_pk_fma_f16 v151, v93, v87, v151 op_sel:[0,0,0] op_sel_hi:[0,1,1]\n\ts_bfe_u32 %[e], %[e], 0x80007\n\t" \
  "s_max_u32 %[e], %[e], %[em]\n\ts_sub_u32 %[t], 260, %[e]\n\t" \
  "s_lshl_b32 %[t], %[t], 23\n\tds_write_addtid_b32 v142 offset:864\n\t" \
  "ds_write_addtid_b32 v143 offset:5472\n\tds_write_addtid_b32 v144 offset:3456\n\t" \
  "ds_write_addtid_b32 v145 offset:8064\n\tds_write_addtid_b32 v146 offset:3744\n\t" \
  "ds_write_addtid_b32 v147 offset:8352\n\tds_write_addtid_b32 v148 offset:4032\n\t" \
  "ds_write_addtid_b32 v149 offset:8640\n\tds_write_addtid_b32 v150 offset:4320\n\t" \
  "ds_write_addtid_b32 v151 offset:8928\n\tv_pk_mul_f16 v96, v94, v88 op_sel:[1,1] op_sel_hi:[1,0] neg_lo:[1,0]\n\t" \
  "v_pk_mul_f16 v97, v95, v89 op_sel:[1,1] op_sel_hi:[1,0] neg_lo:[1,0]\n\tv_pk_fma_f16 v96, v94, v88, v96 op_sel:[0,0,0] op_sel_hi:[0,1,1]\n\t" \
  "v_pk_fma_f16 v97, v95, v89, v97 op_sel:[0,0,0] op_sel_hi:[0,1,1]\n\tv_pk_mul_f16 v98, v94, v90 op_sel:[1,1] op_sel_hi:[1,0] neg_lo:[1,0]\n\t" \
  "v_pk_mul_f16 v99, v95, v91 op_sel:[1,1] op_sel_hi:[1,0] neg_lo:[1,0]\n\tv_pk_fma_f16 v98, v94, v90, v98 op_sel:[0,0,0] op_sel_hi:[0,1,1]\n\t" \
  "v_pk_fma_f16 v99, v95, v91, v99 op_sel:[0,0,0] op_sel_hi:[0,1,1]\n\tv_pk_mul_f16 v100, v94, v92 op_sel:[1,1] op_sel_hi:[1,0] neg_lo:[1,0]\n\t" \
  "v_pk_mul_f16 v101, v95, v93 op_sel:[1,1] op_sel_hi:[1,0] neg_lo:[1,0]\n\tv_pk_fma_f16 v100, v94, v92, v100 op_sel:[0,0,0] op_sel_hi:[0,1,1]\n\t" \
  "v_pk_fma_f16 v101, v95, v93, v101 op_sel:[0,0,0] op_sel_hi:[0,1,1]\n\tds_write_addtid_b32 v96 offset:1440\n\t" \
  "ds_write_addtid_b32 v97 offset:6048\n\tds_write_addtid_b32 v98 offset:1728\n\t" \
  "ds_write_addtid_b32 v99 offset:6336\n\tds_write_addtid_b32 v100 offset:2016\n\t" \
  "ds_write_addtid_b32 v101 offset:6624\n\tds_read_b128 v[136:139], v71 offset:0\n\t" \
  "ds_read_b128 v[140:143], v71 offset:64\n\tds_read_b128 v[144:147], v71 offset:128\n\t" \
  "ds_read_b128 v[148:151], v71 offset:192\n\tds_read_b128 v[152:155], v71 offset:4608\n\t" \
  "ds_read_b128 v[156:159], v71 offset:4672\n\tds_read_b128 v[160:163], v71 offset:4736\n\t" \
  "ds_read_b128 v[164:167], v71 offset:4800\n\tv_pk_mul_f32 v[48:49], v[60:61], v[60:61] op_sel:[1,1] op_sel_hi:[1,0] neg_lo:[1,0]\n\t" \
  "v_pk_mul_f32 v[50:51], v[62:63], v[62:63] op_sel:[1,1] op_sel_hi:[1,0] neg_lo:[1,0]\n\tv_pk_fma_f32 v[48:49], v[60:61], v[60:61], v[48:49] op_sel:[0,0,0] op_sel_hi:[0,1,1]\n\t" \
  "s_cmp_lt_u32 %[w], 50176\n\ts_cbranch_scc0 1f\n\t" \
  "s_setprio 1\n\t1:\n\t" \
  "v_pk_fma_f32 v[50:51], v[62:63], v[62:63], v[50:51] op_sel:[0,0,0] op_sel_hi:[0,1,1]\n\tv_cvt_pk_f16_f32 v44, v60, v61\n\t" \
  "v_cvt_pk_f16_f32 v45, v62, v63\n\tv_pk_mul_f32 v[52:53], v[48:49], v[60:61] op_sel:[1,1] op_sel_hi:[1,0] neg_lo:[1,0]\n\t" \
  "v_pk_mul_f32 v[54:55], v[50:51], v[62:63] op_sel:[1,1] op_sel_hi:[1,0] neg_lo:[1,0]\n\tv_pk_fma_f32 v[52:53], v[48:49], v[60:61], v[52:53] op_sel:[0,0,0] op_sel_hi:[0,1,1]\n\t" \
  "v_pk_fma_f32 v[54:55], v[50:51], v[62:63], v[54:55] op_sel:[0,0,0] op_sel_hi:[0,1,1]\n\tv_pk_mul_f32 v[40:41], v[48:49], v[48:49] op_sel:[1,1] op_sel_hi:[1,0] neg_lo:[1,0]\n\t" \
  "v_pk_mul_f32 v[42:43], v[50:51], v[50:51] op_sel:[1,1] op_sel_hi:[1,0] neg_lo:[1,0]\n\tv_pk_fma_f32 v[40:41], v[48:49], v[48:49], v[40:41] op_sel:[0,0,0] op_sel_hi:[0,1,1]\n\t" \
  "v_pk_fma_f32 v[42:43], v[50:51], v[50:51], v[42:43] op_sel:[0,0,0] op_sel_hi:[0,1,1]\n\tv_cvt_pk_f16_f32 v46, v48, v49\n\t" \
  "v_cvt_pk_f16_f32 v47, v50, v51\n\tv_cvt_pk_f16_f32 v56, v52, v53\n\t" \
  "v_cvt_pk_f16_f32 v57, v54, v55\n\tv_lshlrev_b32 v64, 16, v68\n\t" \
  "v_lshlrev_b32 v65, 16, v69\n\tv_and_b32 v66, 0xffff0000, v68\n\t" \
  "v_and_b32 v67, 0xffff0000, v69\n\tv_mul_f32 v64, %[t], v64\n\t" \
  "v_mul_f32 v65, %[t], v65\n\tv_mul_f32 v66, %[t], v66\n\t" \
  "v_mul_f32 v67, %[t], v67\n\tv_cvt_pk_f16_f32 v48, v64, v65\n\t" \
  "v_cvt_pk_f16_f32 v49, v66, v67\n\tv_pk_mul_f32 v[68:69], v[64:65], v[40:41] op_sel:[1,1] op_sel_hi:[1,0] neg_lo:[1,0]\n\t" \
  "v_pk_mul_f32 v[70:71], v[66:67], v[42:43] op_sel:[1,1] op_sel_hi:[1,0] neg_lo:[1,0]\n\tv_pk_fma_f32 v[68:69], v[64:65], v[40:41], v[68:69] op_sel:[0,0,0] op_sel_hi:[0,1,1]\n\t" \
  "v_pk_fma_f32 v[70:71], v[66:67], v[42:43], v[70:71] op_sel:[0,0,0] op_sel_hi:[0,1,1]\n\tv_pk_mul_f16 v50, v48, v44 op_sel:[1,1] op_sel_hi:[1,0] neg_lo:[1,0]\n\t" \
  "v_pk_mul_f16 v51, v49, v45 op_sel:[1,1] op_sel_hi:[1,0] neg_lo:[1,0]\n\tv_pk_fma_f16 v50, v48, v44, v50 op_sel:[0,0,0] op_sel_hi:[0,1,1]\n\t" \
  "v_pk_fma_f16 v51, v49, v45, v51 op_sel:[0,0,0] op_sel_hi:[0,1,1]\n\tv_pk_mul_f16 v52, v48, v46 op_sel:[1,1] op_sel_hi:[1,0] neg_lo:[1,0]\n\t" \
  "v_pk_mul_f16 v53, v49, v47 op_sel:[1,1] op_sel_hi:[1,0] neg_lo:[1,0]\n\tv_pk_fma_f16 v52, v48, v46, v52 op_sel:[0,0,0] op_sel_hi:[0,1,1]\n\t" \
  "v_pk_fma_f16 v53, v49, v47, v53 op_sel:[0,0,0] op_sel_hi:[0,1,1]\n\tv_pk_mul_f16 v54, v48, v56 op_sel:[1,1] op_sel_hi:[1,0] neg_lo:[1,0]\n\t" \
  "v_pk_mul_f16 v55, v49, v57 op_sel:[1,1] op_sel_hi:[1,0] neg_lo:[1,0]\n\tv_pk_fma_f16 v54, v48, v56, v54 op_sel:[0,0,0] op_sel_hi:[0,1,1]\n\t" \
  "v_pk_fma_f16 v55, v49, v57, v55 op_sel:[0,0,0] op_sel_hi:[0,1,1]\n\tds_write_addtid_b32 v48 offset:0\n\t" \
  "ds_write_addtid_b32 v49 offset:4608\n\tds_write_addtid_b32 v50 offset:288\n\t" \
  "ds_write_addtid_b32 v51 offset:4896\n\tds_write_addtid_b32 v52 offset:576\n\t" \
  "ds_write_addtid_b32 v53 offset:5184\n\tds_write_addtid_b32 v54 offset:864\n\t" \
  "ds_write_addtid_b32 v55 offset:5472\n\tv_pk_mul_f32 v[60:61], v[40:41], v[40:41] op_sel:[1,1] op_sel_hi:[1,0] neg_lo:[1,0]\n\t" \
  "v_pk_mul_f32 v[62:63], v[42:43], v[42:43] op_sel:[1,1] op_sel_hi:[1,0] neg_lo:[1,0]\n\tv_pk_fma_f32 v[60:61], v[40:41], v[40:41], v[60:61] op_sel:[0,0,0] op_sel_hi:[0,1,1]\n\t" \
  "v_pk_fma_f32 v[62:63], v[42:43], v[42:43], v[62:63] op_sel:[0,0,0] op_sel_hi:[0,1,1]\n\tv_cvt_pk_f16_f32 v48, v68, v69\n\t" \
  "v_cvt_pk_f16_f32 v49, v70, v71\n\tv_pk_mul_f32 v[64:65], v[68:69], v[40:41] op_sel:[1,1] op_sel_hi:[1,0] neg_lo:[1,0]\n\t" \
  "v_pk_mul_f32 v[66:67], v[70:71], v[42:43] op_sel:[1,1] op_sel_hi:[1,0] neg_lo:[1,0]\n\tv_pk_fma_f32 v[64:65], v[68:69], v[40:41], v[64:65] op_sel:[0,0,0] op_sel_hi:[0,1,1]\n\t" \
  "v_pk_fma_f32 v[66:67], v[70:71], v[42:43], v[66:67] op_sel:[0,0,0] op_sel_hi:[0,1,1]\n\tv_pk_mul_f16 v50, v48, v44 op_sel:[1,1] op_sel_hi:[1,0] neg_lo:[1,0]\n\t" \
  "v_pk_mul_f16 v51, v49, v45 op_sel:[1,1] op_sel_hi:[1,0] neg_lo:[1,0]\n\tv_pk_fma_f16 v50, v48, v44, v50 op_sel:[0,0,0] op_sel_hi:[0,1,1]\n\t" \
  "v_pk_fma_f16 v51, v49, v45, v51 op_sel:[0,0,0] op_sel_hi:[0,1,1]\n\tv_pk_mul_f16 v52, v48, v46 op_sel:[1,1] op_sel_hi:[1,0] neg_lo:[1,0]\n\t" \
  "v_pk_mul_f16 v53, v49, v47 op_sel:[1,1] op_sel_hi:[1,0] neg_lo:[1,0]\n\tv_pk_fma_f16 v52, v48, v46, v52 op_sel:[0,0,0] op_sel_hi:[0,1,1]\n\t" \
  "v_pk_fma_f16 v53, v49, v47, v53 op_sel:[0,0,0] op_sel_hi:[0,1,1]\n\tv_pk_mul_f16 v54, v48, v56 op_sel:[1,1] op_sel_hi:[1,0] neg_lo:[1,0]\n\t" \
  "v_pk_mul_f16 v55, v49, v57 op_sel:[1,1] op_sel_hi:[1,0] neg_lo:[1,0]\n\tv_pk_fma_f16 v54, v48, v56, v54 op_sel:[0,0,0] op_sel_hi:[0,1,1]\n\t" \
  "v_pk_fma_f16 v55, v49, v57, v55 op_sel:[0,0,0] op_sel_hi:[0,1,1]\n\tds_write_addtid_b32 v48 offset:1152\n\t" \
  "ds_write_addtid_b32 v49 offset:5760\n\tds_write_addtid_b32 v50 offset:1440\n\t" \
  "ds_write_addtid_b32 v51 offset:6048\n\tds_write_addtid_b32 v52 offset:1728\n\t" \
  "ds_write_addtid_b32 v53 offset:6336\n\tds_write_addtid_b32 v54 offset:2016\n\t" \
  "ds_write_addtid_b32 v55 offset:6624\n\tv_pk_mul_f32 v[36:37], v[60:61], v[60:61] op_sel:[1,1] op_sel_hi:[1,0] neg_lo:[1,0]\n\t" \
  "v_pk_mul_f32 v[38:39], v[62:63], v[62:63] op_sel:[1,1] op_sel_hi:[1,0] neg_lo:[1,0]\n\tv_pk_fma_f32 v[36:37], v[60:61], v[60:61], v[36:37] op_sel:[0,0,0] op_sel_hi:[0,1,1]\n\t" \
  "v_pk_fma_f32 v[38:39], v[62:63], v[62:63], v[38:39] op_sel:[0,0,0] op_sel_hi:[0,1,1]\n\tv_cvt_pk_f16_f32 v48, v64, v65\n\t" \
  "v_cvt_pk_f16_f32 v49, v66, v67\n\tv_pk_mul_f32 v[68:69], v[64:65], v[40:41] op_sel:[1,1] op_sel_hi:[1,0] neg_lo:[1,0]\n\t" \
  "v_pk_mul_f32 v[70:71], v[66:67], v[42:43] op_sel:[1,1] op_sel_hi:[1,0] neg_lo:[1,0]\n\tv_pk_fma_f32 v[68:69], v[64:65], v[40:41], v[68:69] op_sel:[0,0,0] op_sel_hi:[0,1,1]\n\t" \
  "v_pk_fma_f32 v[70:71], v[66:67], v[42:43], v[70:71] op_sel:[0,0,0] op_sel_hi:[0,1,1]\n\tv_pk_mul_f16 v50, v48, v44 op_sel:[1,1] op_sel_hi:[1,0] neg_lo:[1,0]\n\t" \
  "v_pk_mul_f16 v51, v49, v45 op_sel:[1,1] op_sel_hi:[1,0] neg_lo:[1,0]\n\tv_pk_fma_f16 v50, v48, v44, v50 op_sel:[0,0,0] op_sel_hi:[0,1,1]\n\t" \
  "v_pk_fma_f16 v51, v49, v45, v51 op_sel:[0,0,0] op_sel_hi:[0,1,1]\n\tv_pk_mul_f16 v52, v48, v46 op_sel:[1,1] op_sel_hi:[1,0] neg_lo:[1,0]\n\t" \
  "v_pk_mul_f16 v53, v49, v47 op_sel:[1,1] op_sel_hi:[1,0] neg_lo:[1,0]\n\tv_pk_fma_f16 v52, v48, v46, v52 op_sel:[0,0,0] op_sel_hi:[0,1,1]\n\t" \
  "v_pk_fma_f16 v53, v49, v47, v53 op_sel:[0,0,0] op_sel_hi:[0,1,1]\n\tv_pk_mul_f16 v54, v48, v56 op_sel:[1,1] op_sel_hi:[1,0] neg_lo:[1,0]\n\t" \
  "v_pk_mul_f16 v55, v49, v57 op_sel:[1,1] op_sel_hi:[1,0] neg_lo:[1,0]\n\tv_pk_fma_f16 v54, v48, v56, v54 op_sel:[0,0,0] op_sel_hi:[0,1,1]\n\t" \
  "v_pk_fma_f16 v55, v49, v57, v55 op_sel:[0,0,0] op_sel_hi:[0,1,1]\n\tds_write_addtid_b32 v48 offset:2304\n\t" \
  "ds_write_addtid_b32 v49 offset:6912\n\tds_write_addtid_b32 v50 offset:2592\n\t" \
  "ds_write_addtid_b32 v51 offset:7200\n\tds_write_addtid_b32 v52 offset:2880\n\t" \
  "ds_write_addtid_b32 v53 offset:7488\n\tds_write_addtid_b32 v54 offset:3168\n\t" \
  "ds_write_addtid_b32 v55 offset:7776\n\tv_cvt_pk_f16_f32 v58, v36, v37\n\t" \
  "v_cvt_pk_f16_f32 v59, v38, v39\n\tv_cvt_pk_f16_f32 v48, v68, v69\n\t" \
  "v_cvt_pk_f16_f32 v49, v70, v71\n\tv_pk_mul_f16 v50, v48, v44 op_sel:[1,1] op_sel_hi:[1,0] neg_lo:[1,0]\n\t" \
  "v_pk_mul_f16 v51, v49, v45 op_sel:[1,1] op_sel_hi:[1,0] neg_lo:[1,0]\n\tv_pk_fma_f16 v50, v48, v44, v50 op_sel:[0,0,0] op_sel_hi:[0,1,1]\n\t" \
  "v_pk_fma_f16 v51, v49, v45, v51 op_sel:[0,0,0] op_sel_hi:[0,1,1]\n\tv_pk_mul_f16 v52, v48, v46 op_sel:[1,1] op_sel_hi:[1,0] neg_lo:[1,0]\n\t" \
  "v_pk_mul_f16 v53, v49, v47 op_sel:[1,1] op_sel_hi:[1,0] neg_lo:[1,0]\n\tv_pk_fma_f16 v52, v48, v46, v52 op_sel:[0,0,0] op_sel_hi:[0,1,1]\n\t" \
  "v_pk_fma_f16 v53, v49, v47, v53 op_sel:[0,0,0] op_sel_hi:[0,1,1]\n\tv_pk_mul_f16 v54, v48, v56 op_sel:[1,1] op_sel_hi:[1,0] neg_lo:[1,0]\n\t" \
  "v_pk_mul_f16 v55, v49, v57 op_sel:[1,1] op_sel_hi:[1,0] neg_lo:[1,0]\n\tv_pk_fma_f16 v54, v48, v56, v54 op_sel:[0,0,0] op_sel_hi:[0,1,1]\n\t" \
  "v_pk_fma_f16 v55, v49, v57, v55 op_sel:[0,0,0] op_sel_hi:[0,1,1]\n\tds_write_addtid_b32 v48 offset:3456\n\t" \
  "ds_write_addtid_b32 v49 offset:8064\n\tds_write_addtid_b32 v50 offset:3744\n\t" \
  "ds_write_addtid_b32 v51 offset:8352\n\tds_write_addtid_b32 v52 offset:4032\n\t" \
  "ds_write_addtid_b32 v53 offset:8640\n\tds_write_addtid_b32 v54 offset:4320\n\t" \
  "ds_write_addtid_b32 v55 offset:8928\n\tds_read_b128 v[72:75], v27 offset:0\n\t" \
  "ds_read_b128 v[76:79], v27 offset:64\n\tds_read_b128 v[80:83], v27 offset:128\n\t" \
  "ds_read_b128 v[84:87], v27 offset:192\n\tds_read_b128 v[88:91], v27 offset:4608\n\t" \
  "ds_read_b128 v[92:95], v27 offset:4672\n\tds_read_b128 v[96:99], v27 offset:4736\n\t" \
  "ds_read_b128 v[100:103], v27 offset:4800\n\tds_write_addtid_b32 v58 offset:0\n\t" \
  "ds_write_addtid_b32 v59 offset:4608\n\tds_read_b128 v[40:43], v12 offset:0\n\t" \
  "ds_read_b128 v[44:47], v12 offset:64\n\tds_read_b128 v[48:51], v12 offset:128\n\t" \
  "v_add_u32_sdwa v56, %[w], v16 dst_sel:DWORD dst_unused:UNUSED_PAD src0_sel:DWORD src1_sel:WORD_0\n\tv_add_u32_sdwa v57, %[w], v16 dst_sel:DWORD dst_unused:UNUSED_PAD src0_sel:DWORD src1_sel:WORD_1\n\t" \
  "v_add_u32_sdwa v58, %[w], v17 dst_sel:DWORD dst_unused:UNUSED_PAD src0_sel:DWORD src1_sel:WORD_0\n\tv_add_u32_sdwa v59, %[w], v17 dst_sel:DWORD dst_unused:UNUSED_PAD src0_sel:DWORD src1_sel:WORD_1\n\t" \
  "s_cmp_lt_u32 %[w], 50176\n\ts_cbranch_scc1 1f\n\t" \
  "s_setprio 1\n\t1:\n\t" \
  "v_add_u32 v14, v56, v35\n\tv_add_u32 v15, v57, v35\n\t" \
  "s_waitcnt lgkmcnt(12)\n\tv_mfma_f32_16x16x32_f16 v[60:63], v[104:107], v[72:75], 0\n\t" \
  "v_mfma_f32_16x16x32_f16 v[36:39], v[72:75], v[136:139], 0\n\tv_mfma_f32_16x16x32_f16 v[4:7], v[136:139], v[72:75], 0\n\t" \
  "s_waitcnt lgkmcnt(2)\n\tv_mfma_f32_16x16x32_f16 v[60:63], v[108:111], v[76:79], v[60:63]\n\t" \
  "v_pk_mul_f16 v52, v40, v72 op_sel:[1,1] op_sel_hi:[1,0] neg_lo:[1,0]\n\tv_pk_mul_f16 v53, v41, v73 op_sel:[1,1] op_sel_hi:[1,0] neg_lo:[1,0]\n\t" \
  "v_pk_mul_f16 v54, v42, v74 op_sel:[1,1] op_sel_hi:[1,0] neg_lo:[1,0]\n\tv_mfma_f32_16x16x32_f16 v[36:39], v[76:79], v[140:143], v[36:39]\n\t" \
  "v_pk_mul_f16 v55, v43, v75 op_sel:[1,1] op_sel_hi:[1,0] neg_lo:[1,0]\n\tv_pk_fma_f16 v40, v40, v72, v52 op_sel:[0,0,0] op_sel_hi:[0,1,1]\n\t" \
  "v_pk_fma_f16 v41, v41, v73, v53 op_sel:[0,0,0] op_sel_hi:[0,1,1]\n\tv_mfma_f32_16x16x32_f16 v[4:7], v[140:143], v[76:79], v[4:7]\n\t" \
  "v_pk_fma_f16 v42, v42, v74, v54 op_sel:[0,0,0] op_sel_hi:[0,1,1]\n\tv_pk_fma_f16 v43, v43, v75, v55 op_sel:[0,0,0] op_sel_hi:[0,1,1]\n\t" \
  "s_waitcnt lgkmcnt(1)\n\tv_mfma_f32_16x16x32_f16 v[60:63], v[112:115], v[80:83], v[60:63]\n\t" \
  "ds_read_b128 v[72:75], v12 offset:192\n\tv_pk_mul_f16 v52, v44, v76 op_sel:[1,1] op_sel_hi:[1,0] neg_lo:[1,0]\n\t" \
  "v_pk_mul_f16 v53, v45, v77 op_sel:[1,1] op_sel_hi:[1,0] neg_lo:[1,0]\n\tv_pk_mul_f16 v54, v46, v78 op_sel:[1,1] op_sel_hi:[1,0] neg_lo:[1,0]\n\t" \
  "v_mfma_f32_16x16x32_f16 v[36:39], v[80:83], v[144:147], v[36:39]\n\tv_pk_mul_f16 v55, v47, v79 op_sel:[1,1] op_sel_hi:[1,0] neg_lo:[1,0]\n\t" \
  "v_pk_fma_f16 v44, v44, v76, v52 op_sel:[0,0,0] op_sel_hi:[0,1,1]\n\tv_pk_fma_f16 v45, v45, v77, v53 op_sel:[0,0,0] op_sel_hi:[0,1,1]\n\t" \
  "v_mfma_f32_16x16x32_f16 v[4:7], v[144:147], v[80:83], v[4:7]\n\tv_pk_fma_f16 v46, v46, v78, v54 op_sel:[0,0,0] op_sel_hi:[0,1,1]\n\t" \
  "v_pk_fma_f16 v47, v47, v79, v55 op_sel:[0,0,0] op_sel_hi:[0,1,1]\n\ts_waitcnt lgkmcnt(1)\n\t" \
  "v_mfma_f32_16x16x32_f16 v[60:63], v[116:119], v[84:87], v[60:63]\n\tds_read_b128 v[76:79], v12 offset:4608\n\t" \
  "v_pk_mul_f16 v52, v48, v80 op_sel:[1,1] op_sel_hi:[1,0] neg_lo:[1,0]\n\tv_pk_mul_f16 v53, v49, v81 op_sel:[1,1] op_sel_hi:[1,0] neg_lo:[1,0]\n\t" \
  "v_pk_mul_f16 v54, v50, v82 op_sel:[1,1] op_sel_hi:[1,0] neg_lo:[1,0]\n\tv_mfma_f32_16x16x32_f16 v[36:39], v[84:87], v[148:151], v[36:39]\n\t" \
  "v_pk_mul_f16 v55, v51, v83 op_sel:[1,1] op_sel_hi:[1,0] neg_lo:[1,0]\n\tv_pk_fma_f16 v48, v48, v80, v52 op_sel:[0,0,0] op_sel_hi:[0,1,1]\n\t" \
  "v_pk_fma_f16 v49, v49, v81, v53 op_sel:[0,0,0] op_sel_hi:[0,1,1]\n\tv_mfma_f32_16x16x32_f16 v[4:7], v[148:151], v[84:87], v[4:7]\n\t" \
  "v_pk_fma_f16 v50, v50, v82, v54 op_sel:[0,0,0] op_sel_hi:[0,1,1]\n\tv_pk_fma_f16 v51, v51, v83, v55 op_sel:[0,0,0] op_sel_hi:[0,1,1]\n\t" \
  "s_waitcnt lgkmcnt(1)\n\tv_mfma_f32_16x16x32_f16 v[60:63], v[120:123], v[88:91], v[60:63]\n\t" \
  "ds_read_b128 v[80:83], v12 offset:4672\n\tv_pk_mul_f16 v52, v72, v84 op_sel:[1,1] op_sel_hi:[1,0] neg_lo:[1,0]\n\t" \
  "v_pk_mul_f16 v53, v73, v85 op_sel:[1,1] op_sel_hi:[1,0] neg_lo:[1,0]\n\tv_pk_mul_f16 v54, v74, v86 op_sel:[1,1] op_sel_hi:[1,0] neg_lo:[1,0]\n\t" \
  "v_mfma_f32_16x16x32_f16 v[36:39], v[88:91], v[152:155], v[36:39]\n\tv_pk_mul_f16 v55, v75, v87 op_sel:[1,1] op_sel_hi:[1,0] neg_lo:[1,0]\n\t" \
  "v_pk_fma_f16 v72, v72, v84, v52 op_sel:[0,0,0] op_sel_hi:[0,1,1]\n\tv_pk_fma_f16 v73, v73, v85, v53 op_sel:[0,0,0] op_sel_hi:[0,1,1]\n\t" \
  "v_mfma_f32_16x16x32_f16 v[4:7], v[152:155], v[88:91], v[4:7]\n\tv_pk_fma_f16 v74, v74, v86, v54 op_sel:[0,0,0] op_sel_hi:[0,1,1]\n\t" \
  "v_pk_fma_f16 v75, v75, v87, v55 op_sel:[0,0,0] op_sel_hi:[0,1,1]\n\ts_waitcnt lgkmcnt(1)\n\t" \
  "v_mfma_f32_16x16x32_f16 v[60:63], v[124:127], v[92:95], v[60:63]\n\tds_read_b128 v[84:87], v12 offset:4736\n\t" \
  "v_pk_mul_f16 v52, v76, v88 op_sel:[1,1] op_sel_hi:[1,0] neg_lo:[1,0]\n\tv_pk_mul_f16 v53, v77, v89 op_sel:[1,1] op_sel_hi:[1,0] neg_lo:[1,0]\n\t" \
  "v_pk_mul_f16 v54, v78, v90 op_sel:[1,1] op_sel_hi:[1,0] neg_lo:[1,0]\n\tv_mfma_f32_16x16x32_f16 v[36:39], v[92:95], v[156:159], v[36:39]\n\t" \
  "v_pk_mul_f16 v55, v79, v91 op_sel:[1,1] op_sel_hi:[1,0] neg_lo:[1,0]\n\tv_pk_fma_f16 v76, v76, v88, v52 op_sel:[0,0,0] op_sel_hi:[0,1,1]\n\t" \
  "v_pk_fma_f16 v77, v77, v89, v53 op_sel:[0,0,0] op_sel_hi:[0,1,1]\n\tv_mfma_f32_16x16x32_f16 v[4:7], v[156:159], v[92:95], v[4:7]\n\t" \
  "v_pk_fma_f16 v78, v78, v90, v54 op_sel:[0,0,0] op_sel_hi:[0,1,1]\n\tv_pk_fma_f16 v79, v79, v91, v55 op_sel:[0,0,0] op_sel_hi:[0,1,1]\n\t" \
  "s_waitcnt lgkmcnt(1)\n\tv_mfma_f32_16x16x32_f16 v[60:63], v[128:131], v[96:99], v[60:63]\n\t" \
  "ds_read_b128 v[88:91], v12 offset:4800\n\tv_pk_mul_f16 v52, v80, v92 op_sel:[1,1] op_sel_hi:[1,0] neg_lo:[1,0]\n\t" \
  "v_pk_mul_f16 v53, v81, v93 op_sel:[1,1] op_sel_hi:[1,0] neg_lo:[1,0]\n\tv_pk_mul_f16 v54, v82, v94 op_sel:[1,1] op_sel_hi:[1,0] neg_lo:[1,0]\n\t" \
  "v_mfma_f32_16x16x32_f16 v[36:39], v[96:99], v[160:163], v[36:39]\n\tv_pk_mul_f16 v55, v83, v95 op_sel:[1,1] op_sel_hi:[1,0] neg_lo:[1,0]\n\t" \
  "v_pk_fma_f16 v80, v80, v92, v52 op_sel:[0,0,0] op_sel_hi:[0,1,1]\n\tv_pk_fma_f16 v81, v81, v93, v53 op_sel:[0,0,0] op_sel_hi:[0,1,1]\n\t" \
  "v_mfma_f32_16x16x32_f16 v[4:7], v[160:163], v[96:99], v[4:7]\n\tv_pk_fma_f16 v82, v82, v94, v54 op_sel:[0,0,0] op_sel_hi:[0,1,1]\n\t" \
  "v_pk_fma_f16 v83, v83, v95, v55 op_sel:[0,0,0] op_sel_hi:[0,1,1]\n\ts_waitcnt lgkmcnt(1)\n\t" \
  "v_mfma_f32_16x16x32_f16 v[60:63], v[132:135], v[100:103], v[60:63]\n\tv_pk_mul_f16 v52, v84, v96 op_sel:[1,1] op_sel_hi:[1,0] neg_lo:[1,0]\n\t" \
  "v_pk_mul_f16 v53, v85, v97 op_sel:[1,1] op_sel_hi:[1,0] neg_lo:[1,0]\n\tv_pk_mul_f16 v54, v86, v98 op_sel:[1,1] op_sel_hi:[1,0] neg_lo:[1,0]\n\t" \
  "v_mfma_f32_16x16x32_f16 v[36:39], v[100:103], v[164:167], v[36:39]\n\tv_pk_mul_f16 v55, v87, v99 op_sel:[1,1] op_sel_hi:[1,0] neg_lo:[1,0]\n\t" \
  "v_pk_fma_f16 v84, v84, v96, v52 op_sel:[0,0,0] op_sel_hi:[0,1,1]\n\tv_pk_fma_f16 v85, v85, v97, v53 op_sel:[0,0,0] op_sel_hi:[0,1,1]\n\t" \
  "v_mfma_f32_16x16x32_f16 v[4:7], v[164:167], v[100:103], v[4:7]\n\tv_pk_fma_f16 v86, v86, v98, v54 op_sel:[0,0,0] op_sel_hi:[0,1,1]\n\t" \
  "v_pk_fma_f16 v87, v87, v99, v55 op_sel:[0,0,0] op_sel_hi:[0,1,1]\n\ts_waitcnt lgkmcnt(0)\n\t" \
  "v_mfma_f32_16x16x32_f16 v[64:67], v[104:107], v[40:43], 0\n\tv_pk_mul_f16 v52, v88, v100 op_sel:[1,1] op_sel_hi:[1,0] neg_lo:[1,0]\n\t" \
  "v_pk_mul_f16 v53, v89, v101 op_sel:[1,1] op_sel_hi:[1,0] neg_lo:[1,0]\n\tv_pk_mul_f16 v54, v90, v102 op_sel:[1,1] op_sel_hi:[1,0] neg_lo:[1,0]\n\t" \
  "v_mfma_f32_16x16x32_f16 v[68:71], v[40:43], v[136:139], 0\n\tv_pk_mul_f16 v55, v91, v103 op_sel:[1,1] op_sel_hi:[1,0] neg_lo:[1,0]\n\t" \
  "v_pk_fma_f16 v88, v88, v100, v52 op_sel:[0,0,0] op_sel_hi:[0,1,1]\n\tv_pk_fma_f16 v89, v89, v101, v53 op_sel:[0,0,0] op_sel_hi:[0,1,1]\n\t" \
  "v_mfma_f32_16x16x32_f16 v[8:11], v[136:139], v[40:43], 0\n\tv_pk_fma_f16 v90, v90, v102, v54 op_sel:[0,0,0] op_sel_hi:[0,1,1]\n\t" \
  "v_pk_fma_f16 v91, v91, v103, v55 op_sel:[0,0,0] op_sel_hi:[0,1,1]\n\tv_mov_b64 v[100:101], 0\n\t" \
  "v_mfma_f32_16x16x32_f16 v[64:67], v[108:111], v[44:47], v[64:67]\n\tv_mov_b64 v[102:103], 0\n\t" \
  "v_cvt_pk_f16_f32 v94, v36, v37\n\tv_cvt_pk_f16_f32 v95, v38, v39\n\t" \
  "v_mfma_f32_16x16x32_f16 v[68:71], v[44:47], v[140:143], v[68:71]\n\tv_cvt_pk_f16_f32 v4, v4, v5\n\t" \
  "v_cvt_pk_f16_f32 v5, v6, v7\n\tv_mov_b32_dpp v94, v94 row_shl:4 row_mask:0xf bank_mask:0x5\n\t" \
  "v_mfma_f32_16x16x32_f16 v[8:11], v[140:143], v[44:47], v[8:11]\n\tv_mov_b32_dpp v95, v95 row_shl:4 row_mask:0xf bank_mask:0x5\n\t" \
  "v_permlane16_swap_b32 v4, v5\n\tv_cvt_pk_f16_f32 v92, v60, v61\n\t" \
  "v_mfma_f32_16x16x32_f16 v[64:67], v[112:115], v[48:51], v[64:67]\n\tv_cvt_pk_f16_f32 v93, v62, v63\n\t" \
  "v_alignbit_b32 v96, v95, v94, 16\n\tv_perm_b32 v6, v4, v4, v33\n\t" \
  "v_mfma_f32_16x16x32_f16 v[68:71], v[48:51], v[144:147], v[68:71]\n\tv_perm_b32 v7, v4, v4, v34\n\t" \
  "v_mfma_f32_16x16x32_f16 v[8:11], v[144:147], v[48:51], v[8:11]\n\tds_write_b128 v58, v[100:103]\n\t" \
  "v_mfma_f32_16x16x32_f16 v[64:67], v[116:119], v[72:75], v[64:67]\n\tds_write2_b32 v56, v92, v93 offset0:193 offset1:194\n\t" \
  "v_mfma_f32_16x16x32_f16 v[68:71], v[72:75], v[148:151], v[68:71]\n\tds_write2_b32 v57, v92, v93 offset0:193 offset1:194\n\t" \
  "v_mfma_f32_16x16x32_f16 v[8:11], v[148:151], v[72:75], v[8:11]\n\tds_write2_b32 v14, v6, v7 offset0:64 offset1:65\n\t" \
  "v_mfma_f32_16x16x32_f16 v[64:67], v[120:123], v[76:79], v[64:67]\n\tds_write2_b32 v15, v6, v7 offset0:64 offset1:65\n\t" \
  "v_mfma_f32_16x16x32_f16 v[68:71], v[76:79], v[152:155], v[68:71]\n\tds_write2_b32 v59, v94, v95 offset0:4 offset1:5\n\t" \
  "v_mfma_f32_16x16x32_f16 v[8:11], v[152:155], v[76:79], v[8:11]\n\tds_write_b16 v59, v94 offset:846\n\t" \
  "v_mfma_f32_16x16x32_f16 v[64:67], v[124:127], v[80:83], v[64:67]\n\tds_write_b32 v59, v96 offset:848\n\t" \
  "v_mfma_f32_16x16x32_f16 v[68:71], v[80:83], v[156:159], v[68:71]\n\tds_write_b16_d16_hi v59, v95 offset:852\n\t" \
  "v_mfma_f32_16x16x32_f16 v[8:11], v[156:159], v[80:83], v[8:11]\n\tv_mfma_f32_16x16x32_f16 v[64:67], v[128:131], v[84:87], v[64:67]\n\t" \
  "v_mfma_f32_16x16x32_f16 v[68:71], v[84:87], v[160:163], v[68:71]\n\tv_mfma_f32_16x16x32_f16 v[8:11], v[160:163], v[84:87], v[8:11]\n\t" \
  "v_mfma_f32_16x16x32_f16 v[68:71], v[88:91], v[164:167], v[68:71]\n\tv_mfma_f32_16x16x32_f16 v[8:11], v[164:167], v[88:91], v[8:11]\n\t" \
  "v_mfma_f32_16x16x32_f16 v[64:67], v[132:135], v[88:91], v[64:67]\n\ts_nop 5\n\t" \
  "v_cvt_pk_f16_f32 v48, v68, v69\n\tv_cvt_pk_f16_f32 v49, v70, v71\n\t" \
  "v_cvt_pk_f16_f32 v8, v8, v9\n\tv_cvt_pk_f16_f32 v9, v10, v11\n\t" \
  "v_mov_b32_dpp v48, v48 row_shl:4 row_mask:0xf bank_mask:0x5\n\tv_mov_b32_dpp v49, v49 row_shl:4 row_mask:0xf bank_mask:0x5\n\t" \
  "v_permlane16_swap_b32 v8, v9\n\tv_cvt_pk_f16_f32 v46, v64, v65\n\t" \
  "v_cvt_pk_f16_f32 v47, v66, v67\n\tv_alignbit_b32 v50, v49, v48, 16\n\t" \
  "v_perm_b32 v10, v8, v8, v33\n\tv_perm_b32 v11, v8, v8, v34\n\t" \
  "ds_write2_b32 v56, v46, v47 offset0:129 offset1:130\n\tds_write2_b32 v57, v46, v47 offset0:129 offset1:130\n\t" \
  "ds_write2_b32 v14, v10, v11 offset0:0 offset1:1\n\tds_write2_b32 v15, v10, v11 offset0:0 offset1:1\n\t" \
  "ds_write2_b32 v59, v48, v49 offset0:0 offset1:1\n\tds_write_b16 v59, v48 offset:830\n\t" \
  "ds_write_b32 v59, v50 offset:832\n\tds_write_b16_d16_hi v59, v49 offset:836\n\t"
#define GEN_CLOBBER "v4", "v5", "v6", "v7", "v9", "v10", "v11", "v13", "v14", "v15", "v36", "v37", "v38", "v39", "v40", "v41", "v42", "v43", "v44", "v45", "v47", "v49", "v50", "v51", "v52", "v53", "v54", "v55", "v56", "v57", "v58", "v59", "v60", "v61", "v62", "v63", "v64", "v65", "v66", "v67", "v72", "v73", "v74", "v75", "v76", "v77", "v78", "v79", "v80", "v81", "v82", "v83", "v84", "v85", "v86", "v87", "v88", "v89", "v90", "v91", "v92", "v94", "v95", "v98", "v99", "v100", "v101", "v102", "v103", "v104", "v105", "v106", "v107", "v108", "v109", "v110", "v111", "v112", "v113", "v114", "v115", "v116", "v117", "v118", "v119", "v120", "v121", "v122", "v123", "v124", "v125", "v126", "v127", "v128", "v129", "v130", "v131", "v132", "v133", "v134", "v135", "v136", "v137", "v138", "v139", "v140", "v141", "v142", "v143", "v144", "v145", "v146", "v147", "v148", "v149", "v150", "v151", "v152", "v153", "v154", "v155", "v156", "v157", "v158", "v159", "v160", "v161", "v162", "v163", "v164", "v165", "v166", "v167"
#define CONV_ASM \
  "ds_read_b32 v34, v30\n\tds_read_b64 v[132:133], v26 offset:512\n\tds_read_b64 v[134:135], v26 offset:520\n\t" \
  "ds_read_b64 v[100:101], v26 offset:0\n\tds_read_b64 v[102:103], v26 offset:8\n\tds_read_b64 v[136:137], v26 offset:576\n\t" \
  "ds_read_b64 v[138:139], v26 offset:584\n\tds_read_b64 v[104:105], v26 offset:64\n\tds_read_b64 v[106:107], v26 offset:72\n\t" \
  "ds_read_b64 v[140:141], v26 offset:640\n\tds_read_b64 v[142:143], v26 offset:648\n\tds_read_b64 v[108:109], v26 offset:128\n\t" \
  "ds_read_b64 v[110:111], v26 offset:136\n\tds_read_b64 v[144:145], v26 offset:704\n\tds_read_b64 v[146:147], v26 offset:712\n\t" \
  "ds_read_b64 v[112:113], v26 offset:192\n\tds_read_b64 v[114:115], v26 offset:200\n\tds_read_b64 v[148:149], v26 offset:768\n\t" \
  "ds_read_b64 v[150:151], v26 offset:776\n\tds_read_b64 v[116:117], v26 offset:256\n\tds_read_b64 v[118:119], v26 offset:264\n\t" \
  TS(60, 61) "s_waitcnt lgkmcnt(15)\n\tv_cmp_gt_u32 vcc, 1, v34\n\t" \
  "s_cbranch_vccz 2f\n\ts_mov_b32 s6, 0x10000\n\t1:\n\t" \
  "s_sleep 1\n\tds_read_b32 v34, v30\n\ts_sub_u32 s6, s6, 1\n\t" \
  "s_waitcnt lgkmcnt(0)\n\tv_cmp_gt_u32 vcc, 1, v34\n\ts_cbranch_scc1 2f\n\t" \
  "s_cbranch_vccnz 1b\n\t2:\n\t" TS(62, 63) \
  "s_cmp_lt_u32 %[w], 4\n\ts_setprio 1\n\ts_cbranch_scc1 1f\n\t" \
  "s_setprio 2\n\t1:\n\tds_read_b128 v[0:3], v28 offset:0\n\t" \
  "ds_read_b128 v[4:7], v28 offset:64\n\tds_read_b128 v[8:11], v28 offset:128\n\tds_read_b128 v[12:15], v28 offset:192\n\t" \
  "ds_read_b128 v[16:19], v28 offset:256\n\tds_read_b128 v[20:23], v28 offset:320\n\ts_waitcnt lgkmcnt(5)\n\t" \
  "v_mfma_f32_16x16x32_f16 v[52:55], v[132:135], v[0:3], 0\n\tds_read_b64 v[152:153], v26 offset:832\n\tds_read_b64 v[154:155], v26 offset:840\n\t" \
  "v_mfma_f32_16x16x32_f16 v[56:59], v[100:103], v[0:3], 0\n\tds_read_b64 v[120:121], v26 offset:320\n\tds_read_b64 v[122:123], v26 offset:328\n\t" \
  "ds_read_b128 v[0:3], v28 offset:384\n\ts_waitcnt lgkmcnt(9)\n\tv_mfma_f32_16x16x32_f16 v[52:55], v[136:139], v[4:7], v[52:55]\n\t" \
  "ds_read_b64 v[156:157], v26 offset:896\n\tds_read_b64 v[158:159], v26 offset:904\n\tv_mfma_f32_16x16x32_f16 v[56:59], v[104:107], v[4:7], v[56:59]\n\t" \
  "ds_read_b64 v[124:125], v26 offset:384\n\tds_read_b64 v[126:127], v26 offset:392\n\tds_read_b128 v[4:7], v28 offset:448\n\t" \
  "s_waitcnt lgkmcnt(13)\n\tv_mfma_f32_16x16x32_f16 v[52:55], v[140:143], v[8:11], v[52:55]\n\tds_read_b64 v[160:161], v26 offset:960\n\t" \
  "ds_read_b64 v[162:163], v26 offset:968\n\tv_mfma_f32_16x16x32_f16 v[56:59], v[108:111], v[8:11], v[56:59]\n\tds_read_b64 v[128:129], v26 offset:448\n\t" \
  "ds_read_b64 v[130:131], v26 offset:456\n\tds_read_b128 v[8:11], v28 offset:512\n\ts_waitcnt lgkmcnt(15)\n\t" \
  "v_mfma_f32_16x16x32_f16 v[52:55], v[144:147], v[12:15], v[52:55]\n\tds_read_b64 v[164:165], v26 offset:1024\n\tds_read_b64 v[166:167], v26 offset:1032\n\t" \
  "v_mfma_f32_16x16x32_f16 v[56:59], v[112:115], v[12:15], v[56:59]\n\tds_read_b64 v[76:77], v31 offset:72\n\tds_read_b64 v[78:79], v31 offset:176\n\t" \
  "ds_read_b128 v[12:15], v28 offset:576\n\tv_mfma_f32_16x16x32_f16 v[52:55], v[148:151], v[16:19], v[52:55]\n\tds_read_b64 v[80:81], v31 offset:40\n\t" \
  "ds_read_b64 v[82:83], v31 offset:144\n\tv_mfma_f32_16x16x32_f16 v[56:59], v[116:119], v[16:19], v[56:59]\n\tds_read_b64 v[84:85], v31 offset:8\n\t" \
  "ds_read_b64 v[86:87], v31 offset:112\n\tds_read_b128 v[16:19], v28 offset:640\n\ts_waitcnt lgkmcnt(15)\n\t" \
  "v_mfma_f32_16x16x32_f16 v[52:55], v[152:155], v[20:23], v[52:55]\n\tds_read_b64 v[88:89], v31 offset:280\n\tds_read_b64 v[90:91], v31 offset:384\n\t" \
  "v_mfma_f32_16x16x32_f16 v[56:59], v[120:123], v[20:23], v[56:59]\n\tds_read_b64 v[92:93], v31 offset:248\n\tds_read_b64 v[94:95], v31 offset:352\n\t" \
  "ds_read_b128 v[20:23], v28 offset:704\n\tv_mfma_f32_16x16x32_f16 v[52:55], v[156:159], v[0:3], v[52:55]\n\tds_read_b64 v[96:97], v31 offset:216\n\t" \
  "ds_read_b64 v[98:99], v31 offset:320\n\tv_mfma_f32_16x16x32_f16 v[56:59], v[124:127], v[0:3], v[56:59]\n\tds_read_b128 v[0:3], v28 offset:768\n\t" \
  "s_waitcnt lgkmcnt(15)\n\tv_mfma_f32_16x16x32_f16 v[52:55], v[160:163], v[4:7], v[52:55]\n\tv_mfma_f32_16x16x32_f16 v[56:59], v[128:131], v[4:7], v[56:59]\n\t" \
  "ds_read_b128 v[4:7], v28 offset:832\n\tv_mfma_f32_16x16x32_f16 v[52:55], v[164:167], v[8:11], v[52:55]\n\tv_mfma_f32_16x16x32_f16 v[56:59], v[132:135], v[8:11], v[56:59]\n\t" \
  "v_mfma_f32_16x16x32_f16 v[60:63], v[100:103], v[8:11], 0\n\tds_read_b128 v[8:11], v28 offset:896\n\ts_waitcnt lgkmcnt(15)\n\t" \
  "v_mfma_f32_16x16x32_f16 v[56:59], v[136:139], v[12:15], v[56:59]\n\tds_read_b32 v34, v30\n\tv_mfma_f32_16x16x32_f16 v[60:63], v[104:107], v[12:15], v[60:63]\n\t" \
  "ds_read_b128 v[12:15], v28 offset:960\n\tv_cvt_pk_bf16_f32 v52, v52, v53\n\tv_cvt_pk_bf16_f32 v53, v54, v55\n\t" \
  TS(64, 65) "s_waitcnt lgkmcnt(12)\n\tv_mfma_f32_16x16x32_f16 v[56:59], v[140:143], v[16:19], v[56:59]\n\t" \
  "ds_write_b64 v29, v[52:53] offset:0\n\tds_write_b32 v25, v25 offset:0\n\tv_mfma_f32_16x16x32_f16 v[60:63], v[108:111], v[16:19], v[60:63]\n\t" \
  "s_waitcnt lgkmcnt(3)\n\tv_cmp_gt_u32 vcc, 2, v34\n\ts_cbranch_vccz 2f\n\t" \
  "s_mov_b32 s6, 0x10000\n\t1:\n\ts_sleep 1\n\t" \
  "ds_read_b32 v34, v30\n\ts_sub_u32 s6, s6, 1\n\ts_waitcnt lgkmcnt(0)\n\t" \
  "v_cmp_gt_u32 vcc, 2, v34\n\ts_cbranch_scc1 2f\n\ts_cbranch_vccnz 1b\n\t" \
  "2:\n\tds_read_b128 v[16:19], v28 offset:1024\n\tv_mfma_f32_16x16x32_f16 v[56:59], v[144:147], v[20:23], v[56:59]\n\t" \
  "v_mfma_f32_16x16x32_f16 v[60:63], v[112:115], v[20:23], v[60:63]\n\tds_read_b128 v[68:71], v32\n\tds_read_b128 v[72:75], v32 offset:1024\n\t" \
  "ds_read_b128 v[20:23], v28 offset:1088\n\tv_mfma_f32_16x16x32_f16 v[56:59], v[148:151], v[0:3], v[56:59]\n\tv_mfma_f32_16x16x32_f16 v[60:63], v[116:119], v[0:3], v[60:63]\n\t" \
  "ds_read_b128 v[0:3], v28 offset:496\n\tv_mfma_f32_16x16x32_f16 v[56:59], v[152:155], v[4:7], v[56:59]\n\tv_mfma_f32_16x16x32_f16 v[60:63], v[120:123], v[4:7], v[60:63]\n\t" \
  "ds_read_b128 v[4:7], v28 offset:1152\n\tv_mfma_f32_16x16x32_f16 v[56:59], v[156:159], v[8:11], v[56:59]\n\tv_mfma_f32_16x16x32_f16 v[60:63], v[124:127], v[8:11], v[60:63]\n\t" \
  "ds_read_b128 v[8:11], v28 offset:1216\n\ts_waitcnt lgkmcnt(9)\n\tv_mfma_f32_16x16x32_f16 v[56:59], v[160:163], v[12:15], v[56:59]\n\t" \
  "v_mfma_f32_16x16x32_f16 v[60:63], v[128:131], v[12:15], v[60:63]\n\tds_read_b128 v[12:15], v28 offset:480\n\ts_waitcnt lgkmcnt(7)\n\t" \
  "v_mfma_f32_16x16x32_f16 v[56:59], v[164:167], v[16:19], v[56:59]\n\tv_mfma_f32_16x16x32_f16 v[60:63], v[132:135], v[16:19], v[60:63]\n\tv_mfma_f32_16x16x32_f16 v[64:67], v[100:103], v[16:19], 0\n\t" \
  "ds_read_b128 v[16:19], v28 offset:1280\n\ts_waitcnt lgkmcnt(5)\n\tv_mfma_f32_16x16x32_f16 v[60:63], v[136:139], v[20:23], v[60:63]\n\t" \
  "v_mfma_f32_16x16x32_f16 v[64:67], v[104:107], v[20:23], v[64:67]\n\tds_read_b128 v[20:23], v28 offset:1344\n\ts_waitcnt lgkmcnt(5)\n\t" \
  "v_mfma_f32_16x16x32_f16 v[36:39], v[0:3], v[68:71], 0\n\tv_cvt_pk_bf16_f32 v56, v56, v57\n\tv_cvt_pk_bf16_f32 v57, v58, v59\n\t" \
  "v_mfma_f32_16x16x32_f16 v[40:43], v[0:3], v[72:75], 0\n\tds_write_b64 v29, v[56:57] offset:512\n\tds_write_b32 v25, v25 offset:64\n\t" \
  "ds_read_b128 v[0:3], v28 offset:1408\n\ts_waitcnt lgkmcnt(7)\n\tv_mfma_f32_16x16x32_f16 v[60:63], v[140:143], v[4:7], v[60:63]\n\t" \
  "v_cvt_pk_f16_f32 v36, v36, v37\n\tv_cvt_pk_f16_f32 v37, v38, v39\n\tv_mfma_f32_16x16x32_f16 v[64:67], v[108:111], v[4:7], v[64:67]\n\t" \
  "ds_read_b128 v[4:7], v28 offset:1472\n\tv_cvt_pk_f16_f32 v38, v40, v41\n\tv_cvt_pk_f16_f32 v39, v42, v43\n\t" \
  "s_waitcnt lgkmcnt(7)\n\tv_mfma_f32_16x16x32_f16 v[60:63], v[144:147], v[8:11], v[60:63]\n\tv_mfma_f32_16x16x32_f16 v[64:67], v[112:115], v[8:11], v[64:67]\n\t" \
  "ds_read_b128 v[8:11], v28 offset:1536\n\tv_mfma_f32_16x16x32_f16 v[60:63], v[36:39], v[76:79], v[60:63]\n\tv_mfma_f32_16x16x32_f16 v[64:67], v[36:39], v[80:83], v[64:67]\n\t" \
  "v_mfma_f32_16x16x32_f16 v[52:55], v[36:39], v[84:87], 0\n\ts_waitcnt lgkmcnt(7)\n\tv_mfma_f32_16x16x32_f16 v[44:47], v[12:15], v[68:71], 0\n\t" \
  "v_mfma_f32_16x16x32_f16 v[40:43], v[12:15], v[72:75], 0\n\tds_read_b128 v[12:15], v28 offset:1600\n\ts_waitcnt lgkmcnt(7)\n\t" \
  "v_mfma_f32_16x16x32_f16 v[60:63], v[148:151], v[16:19], v[60:63]\n\tv_mfma_f32_16x16x32_f16 v[64:67], v[116:119], v[16:19], v[64:67]\n\tds_read_b128 v[16:19], v28 offset:1008\n\t" \
  "s_waitcnt lgkmcnt(7)\n\tv_mfma_f32_16x16x32_f16 v[60:63], v[152:155], v[20:23], v[60:63]\n\tv_cvt_pk_f16_f32 v44, v44, v45\n\t" \
  "v_cvt_pk_f16_f32 v45, v46, v47\n\tv_cvt_pk_f16_f32 v46, v40, v41\n\tv_cvt_pk_f16_f32 v47, v42, v43\n\t" \
  "v_mfma_f32_16x16x32_f16 v[64:67], v[120:123], v[20:23], v[64:67]\n\tds_read_b128 v[20:23], v28 offset:1664\n\tv_mfma_f32_16x16x32_f16 v[60:63], v[44:47], v[88:91], v[60:63]\n\t" \
  "v_mfma_f32_16x16x32_f16 v[64:67], v[44:47], v[92:95], v[64:67]\n\tv_mfma_f32_16x16x32_f16 v[52:55], v[44:47], v[96:99], v[52:55]\n\ts_waitcnt lgkmcnt(5)\n\t" \
  "v_mfma_f32_16x16x32_f16 v[60:63], v[156:159], v[0:3], v[60:63]\n\tv_mfma_f32_16x16x32_f16 v[64:67], v[124:127], v[0:3], v[64:67]\n\tds_read_b128 v[0:3], v28 offset:1728\n\t" \
  "s_waitcnt lgkmcnt(5)\n\tv_mfma_f32_16x16x32_f16 v[60:63], v[160:163], v[4:7], v[60:63]\n\tv_mfma_f32_16x16x32_f16 v[64:67], v[128:131], v[4:7], v[64:67]\n\t" \
  "ds_read_b128 v[4:7], v28 offset:992\n\ts_waitcnt lgkmcnt(5)\n\tv_mfma_f32_16x16x32_f16 v[60:63], v[164:167], v[8:11], v[60:63]\n\t" \
  "v_mfma_f32_16x16x32_f16 v[64:67], v[132:135], v[8:11], v[64:67]\n\tv_mfma_f32_16x16x32_f16 v[52:55], v[100:103], v[8:11], v[52:55]\n\tds_read_b128 v[8:11], v28 offset:1792\n\t" \
  "s_waitcnt lgkmcnt(5)\n\tv_mfma_f32_16x16x32_f16 v[64:67], v[136:139], v[12:15], v[64:67]\n\tv_mfma_f32_16x16x32_f16 v[52:55], v[104:107], v[12:15], v[52:55]\n\t" \
  "ds_read_b128 v[12:15], v28 offset:1856\n\ts_waitcnt lgkmcnt(5)\n\tv_mfma_f32_16x16x32_f16 v[36:39], v[16:19], v[68:71], 0\n\t" \
  "v_cvt_pk_bf16_f32 v60, v60, v61\n\tv_cvt_pk_bf16_f32 v61, v62, v63\n\tv_mfma_f32_16x16x32_f16 v[40:43], v[16:19], v[72:75], 0\n\t" \
  "ds_write_b64 v29, v[60:61] offset:1024\n\tds_write_b32 v25, v25 offset:128\n\tds_read_b128 v[16:19], v28 offset:1920\n\t" \
  "s_waitcnt lgkmcnt(7)\n\tv_mfma_f32_16x16x32_f16 v[64:67], v[140:143], v[20:23], v[64:67]\n\tds_read_b32 v34, v30\n\t" \
  "v_cvt_pk_f16_f32 v36, v36, v37\n\tv_cvt_pk_f16_f32 v37, v38, v39\n\tv_mfma_f32_16x16x32_f16 v[52:55], v[108:111], v[20:23], v[52:55]\n\t" \
  "ds_read_b128 v[20:23], v28 offset:1984\n\tv_cvt_pk_f16_f32 v38, v40, v41\n\tv_cvt_pk_f16_f32 v39, v42, v43\n\t" \
  "s_waitcnt lgkmcnt(8)\n\tv_mfma_f32_16x16x32_f16 v[64:67], v[144:147], v[0:3], v[64:67]\n\tv_mfma_f32_16x16x32_f16 v[52:55], v[112:115], v[0:3], v[52:55]\n\t" \
  "s_waitcnt lgkmcnt(1)\n\tv_cmp_gt_u32 vcc, 3, v34\n\ts_cbranch_vccz 2f\n\t" \
  "s_mov_b32 s6, 0x10000\n\t1:\n\ts_sleep 1\n\t" \
  "ds_read_b32 v34, v30\n\ts_sub_u32 s6, s6, 1\n\ts_waitcnt lgkmcnt(0)\n\t" \
  "v_cmp_gt_u32 vcc, 3, v34\n\ts_cbranch_scc1 2f\n\ts_cbranch_vccnz 1b\n\t" \
  "2:\n\tds_read_b128 v[0:3], v28 offset:2048\n\tv_mfma_f32_16x16x32_f16 v[64:67], v[36:39], v[76:79], v[64:67]\n\t" \
  "v_mfma_f32_16x16x32_f16 v[52:55], v[36:39], v[80:83], v[52:55]\n\tv_mfma_f32_16x16x32_f16 v[56:59], v[36:39], v[84:87], 0\n\tv_mfma_f32_16x16x32_f16 v[44:47], v[4:7], v[68:71], 0\n\t" \
  "v_mfma_f32_16x16x32_f16 v[40:43], v[4:7], v[72:75], 0\n\tds_read_b128 v[4:7], v28 offset:2112\n\tv_mfma_f32_16x16x32_f16 v[64:67], v[148:151], v[8:11], v[64:67]\n\t" \
  "v_mfma_f32_16x16x32_f16 v[52:55], v[116:119], v[8:11], v[52:55]\n\tds_read_b128 v[8:11], v28 offset:1520\n\tv_mfma_f32_16x16x32_f16 v[64:67], v[152:155], v[12:15], v[64:67]\n\t" \
  "v_mfma_f32_16x16x32_f16 v[52:55], v[120:123], v[12:15], v[52:55]\n\tds_read_b128 v[12:15], v28 offset:2176\n\tv_cvt_pk_f16_f32 v44, v44, v45\n\t" \
  "v_cvt_pk_f16_f32 v45, v46, v47\n\tv_cvt_pk_f16_f32 v46, v40, v41\n\tv_cvt_pk_f16_f32 v47, v42, v43\n\t" \
  "s_nop 1\n\tv_mfma_f32_16x16x32_f16 v[64:67], v[44:47], v[88:91], v[64:67]\n\tv_mfma_f32_16x16x32_f16 v[52:55], v[44:47], v[92:95], v[52:55]\n\t" \
  "v_mfma_f32_16x16x32_f16 v[56:59], v[44:47], v[96:99], v[56:59]\n\tv_mfma_f32_16x16x32_f16 v[64:67], v[156:159], v[16:19], v[64:67]\n\tv_mfma_f32_16x16x32_f16 v[52:55], v[124:127], v[16:19], v[52:55]\n\t" \
  "ds_read_b128 v[16:19], v28 offset:2240\n\ts_waitcnt lgkmcnt(5)\n\tv_mfma_f32_16x16x32_f16 v[64:67], v[160:163], v[20:23], v[64:67]\n\t" \
  "v_mfma_f32_16x16x32_f16 v[52:55], v[128:131], v[20:23], v[52:55]\n\tds_read_b128 v[20:23], v28 offset:1504\n\ts_waitcnt lgkmcnt(5)\n\t" \
  "v_mfma_f32_16x16x32_f16 v[64:67], v[164:167], v[0:3], v[64:67]\n\tv_mfma_f32_16x16x32_f16 v[52:55], v[132:135], v[0:3], v[52:55]\n\tv_mfma_f32_16x16x32_f16 v[56:59], v[100:103], v[0:3], v[56:59]\n\t" \
  "ds_read_b128 v[0:3], v28 offset:2304\n\ts_waitcnt lgkmcnt(5)\n\tv_mfma_f32_16x16x32_f16 v[52:55], v[136:139], v[4:7], v[52:55]\n\t" \
  "v_mfma_f32_16x16x32_f16 v[56:59], v[104:107], v[4:7], v[56:59]\n\tds_read_b128 v[4:7], v28 offset:2368\n\ts_waitcnt lgkmcnt(5)\n\t" \
  "v_mfma_f32_16x16x32_f16 v[36:39], v[8:11], v[68:71], 0\n\tv_cvt_pk_bf16_f32 v64, v64, v65\n\tv_cvt_pk_bf16_f32 v65, v66, v67\n\t" \
  TS(66, 67) "s_cmp_lt_u32 %[w], 4\n\ts_cbranch_scc1 1f\n\t" \
  "s_setprio 1\n\t1:\n\tv_mfma_f32_16x16x32_f16 v[40:43], v[8:11], v[72:75], 0\n\t" \
  "ds_write_b64 v29, v[64:65] offset:1536\n\tds_write_b32 v25, v25 offset:192\n\tds_read_b128 v[8:11], v28 offset:480\n\t" \
  "s_waitcnt lgkmcnt(7)\n\tv_mfma_f32_16x16x32_f16 v[52:55], v[140:143], v[12:15], v[52:55]\n\tv_cvt_pk_f16_f32 v36, v36, v37\n\t" \
  "v_cvt_pk_f16_f32 v37, v38, v39\n\tv_mfma_f32_16x16x32_f16 v[56:59], v[108:111], v[12:15], v[56:59]\n\tds_read_b128 v[12:15], v32 offset:3072\n\t" \
  "v_cvt_pk_f16_f32 v38, v40, v41\n\tv_cvt_pk_f16_f32 v39, v42, v43\n\ts_waitcnt lgkmcnt(7)\n\t" \
  "v_mfma_f32_16x16x32_f16 v[52:55], v[144:147], v[16:19], v[52:55]\n\tv_mfma_f32_16x16x32_f16 v[56:59], v[112:115], v[16:19], v[56:59]\n\tds_read_b128 v[16:19], v28 offset:496\n\t" \
  "v_mfma_f32_16x16x32_f16 v[52:55], v[36:39], v[76:79], v[52:55]\n\tv_mfma_f32_16x16x32_f16 v[56:59], v[36:39], v[80:83], v[56:59]\n\tv_mfma_f32_16x16x32_f16 v[60:63], v[36:39], v[84:87], 0\n\t" \
  "s_waitcnt lgkmcnt(7)\n\tv_mfma_f32_16x16x32_f16 v[44:47], v[20:23], v[68:71], 0\n\tv_mfma_f32_16x16x32_f16 v[40:43], v[20:23], v[72:75], 0\n\t" \
  "ds_read_b128 v[20:23], v32 offset:2048\n\ts_waitcnt lgkmcnt(7)\n\tv_mfma_f32_16x16x32_f16 v[52:55], v[148:151], v[0:3], v[52:55]\n\t" \
  "v_mfma_f32_16x16x32_f16 v[56:59], v[116:119], v[0:3], v[56:59]\n\tds_read_b128 v[0:3], v28 offset:2432\n\ts_waitcnt lgkmcnt(7)\n\t" \
  "v_mfma_f32_16x16x32_f16 v[52:55], v[152:155], v[4:7], v[52:55]\n\tv_cvt_pk_f16_f32 v44, v44, v45\n\tv_cvt_pk_f16_f32 v45, v46, v47\n\t" \
  "v_cvt_pk_f16_f32 v46, v40, v41\n\tv_cvt_pk_f16_f32 v47, v42, v43\n\tv_mfma_f32_16x16x32_f16 v[56:59], v[120:123], v[4:7], v[56:59]\n\t" \
  "ds_read_b128 v[4:7], v28 offset:2496\n\tv_mfma_f32_16x16x32_f16 v[52:55], v[44:47], v[88:91], v[52:55]\n\tv_mfma_f32_16x16x32_f16 v[56:59], v[44:47], v[92:95], v[56:59]\n\t" \
  "v_mfma_f32_16x16x32_f16 v[60:63], v[44:47], v[96:99], v[60:63]\n\ts_waitcnt lgkmcnt(4)\n\tv_mfma_f32_16x16x32_f16 v[40:43], v[8:11], v[12:15], 0\n\t" \
  "ds_read_b64 v[8:9], v31 offset:544\n\tds_read_b64 v[10:11], v31 offset:712\n\tds_read_b64 v[12:13], v31 offset:512\n\t" \
  "ds_read_b64 v[14:15], v31 offset:680\n\ts_waitcnt lgkmcnt(7)\n\tv_mfma_f32_16x16x32_f16 v[52:55], v[48:51], v[16:19], v[52:55]\n\t" \
  "s_waitcnt lgkmcnt(6)\n\tv_mfma_f32_16x16x32_f16 v[36:39], v[16:19], v[20:23], 0\n\tds_read_b64 v[16:17], v31 offset:480\n\t" \
  "ds_read_b64 v[18:19], v31 offset:648\n\tds_read_b64 v[20:21], v31 offset:448\n\tds_read_b64 v[22:23], v31 offset:616\n\t" \
  "s_waitcnt lgkmcnt(9)\n\tv_mfma_f32_16x16x32_f16 v[52:55], v[156:159], v[0:3], v[52:55]\n\tv_mfma_f32_16x16x32_f16 v[56:59], v[124:127], v[0:3], v[56:59]\n\t" \
  "ds_read_b128 v[0:3], v28 offset:2560\n\tv_cvt_pk_f16_f32 v36, v36, v37\n\tv_cvt_pk_f16_f32 v37, v38, v39\n\t" \
  "s_waitcnt lgkmcnt(9)\n\tv_mfma_f32_16x16x32_f16 v[52:55], v[160:163], v[4:7], v[52:55]\n\tv_cvt_pk_f16_f32 v38, v40, v41\n\t" \
  "v_cvt_pk_f16_f32 v39, v42, v43\n\tv_mfma_f32_16x16x32_f16 v[56:59], v[128:131], v[4:7], v[56:59]\n\tds_read_b128 v[4:7], v28 offset:2624\n\t" \
  "s_waitcnt lgkmcnt(8)\n\tv_mfma_f32_16x16x32_f16 v[52:55], v[36:39], v[8:11], v[52:55]\n\tds_read_b128 v[8:11], v28 offset:2032\n\t" \
  "s_waitcnt lgkmcnt(7)\n\tv_mfma_f32_16x16x32_f16 v[56:59], v[36:39], v[12:15], v[56:59]\n\tds_read_b128 v[12:15], v28 offset:2688\n\t" \
  "s_waitcnt lgkmcnt(6)\n\tv_mfma_f32_16x16x32_f16 v[60:63], v[36:39], v[16:19], v[60:63]\n\tds_read_b128 v[16:19], v28 offset:2752\n\t" \
  "s_waitcnt lgkmcnt(5)\n\tv_mfma_f32_16x16x32_f16 v[64:67], v[36:39], v[20:23], 0\n\tds_read_b128 v[20:23], v28 offset:2016\n\t" \
  "s_waitcnt lgkmcnt(5)\n\tv_mfma_f32_16x16x32_f16 v[52:55], v[164:167], v[0:3], v[52:55]\n\tv_mfma_f32_16x16x32_f16 v[56:59], v[132:135], v[0:3], v[56:59]\n\t" \
  "v_mfma_f32_16x16x32_f16 v[60:63], v[100:103], v[0:3], v[60:63]\n\tds_read_b128 v[0:3], v28 offset:2816\n\ts_waitcnt lgkmcnt(5)\n\t" \
  "v_mfma_f32_16x16x32_f16 v[56:59], v[136:139], v[4:7], v[56:59]\n\tv_mfma_f32_16x16x32_f16 v[60:63], v[104:107], v[4:7], v[60:63]\n\tds_read_b128 v[4:7], v28 offset:2880\n\t" \
  "s_waitcnt lgkmcnt(5)\n\tv_mfma_f32_16x16x32_f16 v[44:47], v[8:11], v[68:71], 0\n\tv_cvt_pk_bf16_f32 v52, v52, v53\n\t" \
  "v_cvt_pk_bf16_f32 v53, v54, v55\n\ts_cmp_lt_u32 %[w], 4\n\ts_cbranch_scc0 1f\n\t" \
  "s_setprio 0\n\t1:\n\tv_mfma_f32_16x16x32_f16 v[40:43], v[8:11], v[72:75], 0\n\t" \
  "ds_write_b64 v29, v[52:53] offset:2048\n\tds_write_b32 v25, v25 offset:256\n\tds_read_b128 v[8:11], v28 offset:992\n\t" \
  "s_waitcnt lgkmcnt(7)\n\tv_mfma_f32_16x16x32_f16 v[56:59], v[140:143], v[12:15], v[56:59]\n\tv_cvt_pk_f16_f32 v44, v44, v45\n\t" \
  "v_cvt_pk_f16_f32 v45, v46, v47\n\tv_mfma_f32_16x16x32_f16 v[60:63], v[108:111], v[12:15], v[60:63]\n\tds_read_b128 v[12:15], v32 offset:3072\n\t" \
  "v_cvt_pk_f16_f32 v46, v40, v41\n\tv_cvt_pk_f16_f32 v47, v42, v43\n\ts_waitcnt lgkmcnt(7)\n\t" \
  "v_mfma_f32_16x16x32_f16 v[56:59], v[144:147], v[16:19], v[56:59]\n\tv_mfma_f32_16x16x32_f16 v[60:63], v[112:115], v[16:19], v[60:63]\n\tds_read_b128 v[16:19], v28 offset:1008\n\t" \
  "v_mfma_f32_16x16x32_f16 v[56:59], v[44:47], v[76:79], v[56:59]\n\tv_mfma_f32_16x16x32_f16 v[60:63], v[44:47], v[80:83], v[60:63]\n\tv_mfma_f32_16x16x32_f16 v[64:67], v[44:47], v[84:87], v[64:67]\n\t" \
  "s_waitcnt lgkmcnt(7)\n\tv_mfma_f32_16x16x32_f16 v[36:39], v[20:23], v[68:71], 0\n\tv_mfma_f32_16x16x32_f16 v[40:43], v[20:23], v[72:75], 0\n\t" \
  "ds_read_b128 v[20:23], v32 offset:2048\n\ts_waitcnt lgkmcnt(7)\n\tv_mfma_f32_16x16x32_f16 v[56:59], v[148:151], v[0:3], v[56:59]\n\t" \
  "v_mfma_f32_16x16x32_f16 v[60:63], v[116:119], v[0:3], v[60:63]\n\tds_read_b128 v[0:3], v28 offset:2944\n\ts_waitcnt lgkmcnt(7)\n\t" \
  "v_mfma_f32_16x16x32_f16 v[56:59], v[152:155], v[4:7], v[56:59]\n\tv_cvt_pk_f16_f32 v36, v36, v37\n\tv_cvt_pk_f16_f32 v37, v38, v39\n\t" \
  "v_cvt_pk_f16_f32 v38, v40, v41\n\tv_cvt_pk_f16_f32 v39, v42, v43\n\tv_mfma_f32_16x16x32_f16 v[60:63], v[120:123], v[4:7], v[60:63]\n\t" \
  "ds_read_b128 v[4:7], v28 offset:3008\n\tv_mfma_f32_16x16x32_f16 v[56:59], v[36:39], v[88:91], v[56:59]\n\tv_mfma_f32_16x16x32_f16 v[60:63], v[36:39], v[92:95], v[60:63]\n\t" \
  "v_mfma_f32_16x16x32_f16 v[64:67], v[36:39], v[96:99], v[64:67]\n\tds_read_b32 v34, v30\n\ts_waitcnt lgkmcnt(5)\n\t" \
  "v_mfma_f32_16x16x32_f16 v[40:43], v[8:11], v[12:15], 0\n\tds_read_b64 v[8:9], v31 offset:544\n\tds_read_b64 v[10:11], v31 offset:712\n\t" \
  "ds_read_b64 v[12:13], v31 offset:512\n\tds_read_b64 v[14:15], v31 offset:680\n\ts_waitcnt lgkmcnt(8)\n\t" \
  "v_mfma_f32_16x16x32_f16 v[56:59], v[48:51], v[16:19], v[56:59]\n\ts_waitcnt lgkmcnt(7)\n\tv_mfma_f32_16x16x32_f16 v[44:47], v[16:19], v[20:23], 0\n\t" \
  "ds_read_b64 v[16:17], v31 offset:480\n\tds_read_b64 v[18:19], v31 offset:648\n\ts_waitcnt lgkmcnt(6)\n\t" \
  "v_cmp_gt_u32 vcc, 4, v34\n\ts_cbranch_vccz 2f\n\ts_mov_b32 s6, 0x10000\n\t" \
  "1:\n\ts_sleep 1\n\tds_read_b32 v34, v30\n\t" \
  "s_sub_u32 s6, s6, 1\n\ts_waitcnt lgkmcnt(0)\n\tv_cmp_gt_u32 vcc, 4, v34\n\t" \
  "s_cbranch_scc1 2f\n\ts_cbranch_vccnz 1b\n\t2:\n\t" \
  "ds_read_b128 v[20:23], v28 offset:3072\n\tv_mfma_f32_16x16x32_f16 v[56:59], v[156:159], v[0:3], v[56:59]\n\tv_mfma_f32_16x16x32_f16 v[60:63], v[124:127], v[0:3], v[60:63]\n\t" \
  "ds_read_b128 v[0:3], v28 offset:3136\n\tv_cvt_pk_f16_f32 v44, v44, v45\n\tv_cvt_pk_f16_f32 v45, v46, v47\n\t" \
  "v_mfma_f32_16x16x32_f16 v[56:59], v[160:163], v[4:7], v[56:59]\n\tv_cvt_pk_f16_f32 v46, v40, v41\n\tv_cvt_pk_f16_f32 v47, v42, v43\n\t" \
  "v_mfma_f32_16x16x32_f16 v[60:63], v[128:131], v[4:7], v[60:63]\n\tds_read_b128 v[4:7], v28 offset:2544\n\ts_waitcnt lgkmcnt(7)\n\t" \
  "v_mfma_f32_16x16x32_f16 v[56:59], v[44:47], v[8:11], v[56:59]\n\tds_read_b128 v[8:11], v28 offset:3200\n\ts_waitcnt lgkmcnt(6)\n\t" \
  "v_mfma_f32_16x16x32_f16 v[60:63], v[44:47], v[12:15], v[60:63]\n\tds_read_b128 v[12:15], v28 offset:3264\n\ts_waitcnt lgkmcnt(5)\n\t" \
  "v_mfma_f32_16x16x32_f16 v[64:67], v[44:47], v[16:19], v[64:67]\n\tds_read_b128 v[16:19], v28 offset:2528\n\ts_waitcnt lgkmcnt(5)\n\t" \
  "v_mfma_f32_16x16x32_f16 v[56:59], v[164:167], v[20:23], v[56:59]\n\tv_mfma_f32_16x16x32_f16 v[60:63], v[132:135], v[20:23], v[60:63]\n\tv_mfma_f32_16x16x32_f16 v[64:67], v[100:103], v[20:23], v[64:67]\n\t" \
  "ds_read_b128 v[20:23], v28 offset:3328\n\ts_waitcnt lgkmcnt(5)\n\tv_mfma_f32_16x16x32_f16 v[60:63], v[136:139], v[0:3], v[60:63]\n\t" \
  "v_mfma_f32_16x16x32_f16 v[64:67], v[104:107], v[0:3], v[64:67]\n\tds_read_b128 v[0:3], v28 offset:3392\n\ts_waitcnt lgkmcnt(5)\n\t" \
  "v_mfma_f32_16x16x32_f16 v[36:39], v[4:7], v[68:71], 0\n\tv_cvt_pk_bf16_f32 v56, v56, v57\n\tv_cvt_pk_bf16_f32 v57, v58, v59\n\t" \
  "s_cmp_lt_u32 %[w], 4\n\ts_cbranch_scc1 1f\n\ts_setprio 0\n\t" \
  "1:\n\tv_mfma_f32_16x16x32_f16 v[40:43], v[4:7], v[72:75], 0\n\tds_write_b64 v29, v[56:57] offset:2560\n\t" \
  "ds_write_b32 v25, v25 offset:320\n\tds_read_b128 v[4:7], v28 offset:1504\n\ts_waitcnt lgkmcnt(7)\n\t" \
  "v_mfma_f32_16x16x32_f16 v[60:63], v[140:143], v[8:11], v[60:63]\n\tv_cvt_pk_f16_f32 v36, v36, v37\n\tv_cvt_pk_f16_f32 v37, v38, v39\n\t" \
  "v_mfma_f32_16x16x32_f16 v[64:67], v[108:111], v[8:11], v[64:67]\n\tds_read_b128 v[8:11], v32 offset:3072\n\tv_cvt_pk_f16_f32 v38, v40, v41\n\t" \
  "v_cvt_pk_f16_f32 v39, v42, v43\n\ts_waitcnt lgkmcnt(7)\n\tv_mfma_f32_16x16x32_f16 v[60:63], v[144:147], v[12:15], v[60:63]\n\t" \
  "v_mfma_f32_16x16x32_f16 v[64:67], v[112:115], v[12:15], v[64:67]\n\tds_read_b128 v[12:15], v28 offset:1520\n\tv_mfma_f32_16x16x32_f16 v[60:63], v[36:39], v[76:79], v[60:63]\n\t" \
  "v_mfma_f32_16x16x32_f16 v[64:67], v[36:39], v[80:83], v[64:67]\n\ts_waitcnt lgkmcnt(7)\n\tv_mfma_f32_16x16x32_f16 v[44:47], v[16:19], v[68:71], 0\n\t" \
  "v_mfma_f32_16x16x32_f16 v[40:43], v[16:19], v[72:75], 0\n\tds_read_b128 v[16:19], v32 offset:2048\n\ts_waitcnt lgkmcnt(7)\n\t" \
  "v_mfma_f32_16x16x32_f16 v[60:63], v[148:151], v[20:23], v[60:63]\n\tv_mfma_f32_16x16x32_f16 v[64:67], v[116:119], v[20:23], v[64:67]\n\tds_read_b128 v[20:23], v28 offset:3456\n\t" \
  "s_waitcnt lgkmcnt(7)\n\tv_mfma_f32_16x16x32_f16 v[60:63], v[152:155], v[0:3], v[60:63]\n\tv_cvt_pk_f16_f32 v44, v44, v45\n\t" \
  "v_cvt_pk_f16_f32 v45, v46, v47\n\tv_cvt_pk_f16_f32 v46, v40, v41\n\tv_cvt_pk_f16_f32 v47, v42, v43\n\t" \
  "v_mfma_f32_16x16x32_f16 v[64:67], v[120:123], v[0:3], v[64:67]\n\tds_read_b128 v[0:3], v28 offset:3520\n\tv_mfma_f32_16x16x32_f16 v[60:63], v[44:47], v[88:91], v[60:63]\n\t" \
  "v_mfma_f32_16x16x32_f16 v[64:67], v[44:47], v[92:95], v[64:67]\n\ts_waitcnt lgkmcnt(4)\n\tv_mfma_f32_16x16x32_f16 v[40:43], v[4:7], v[8:11], 0\n\t" \
  "ds_read_b64 v[4:5], v31 offset:544\n\tds_read_b64 v[6:7], v31 offset:712\n\tds_read_b64 v[8:9], v31 offset:512\n\t" \
  "ds_read_b64 v[10:11], v31 offset:680\n\ts_waitcnt lgkmcnt(7)\n\tv_mfma_f32_16x16x32_f16 v[60:63], v[48:51], v[12:15], v[60:63]\n\t" \
  "s_waitcnt lgkmcnt(6)\n\tv_mfma_f32_16x16x32_f16 v[36:39], v[12:15], v[16:19], 0\n\tds_read_b128 v[12:15], v28 offset:3584\n\t" \
  "ds_read_b128 v[16:19], v28 offset:3648\n\ts_waitcnt lgkmcnt(7)\n\tv_mfma_f32_16x16x32_f16 v[60:63], v[156:159], v[20:23], v[60:63]\n\t" \
  "v_mfma_f32_16x16x32_f16 v[64:67], v[124:127], v[20:23], v[64:67]\n\tds_read_b128 v[20:23], v28 offset:3056\n\ts_waitcnt lgkmcnt(7)\n\t" \
  "v_mfma_f32_16x16x32_f16 v[60:63], v[160:163], v[0:3], v[60:63]\n\tv_cvt_pk_f16_f32 v36, v36, v37\n\tv_cvt_pk_f16_f32 v37, v38, v39\n\t" \
  "v_cvt_pk_f16_f32 v38, v40, v41\n\tv_cvt_pk_f16_f32 v39, v42, v43\n\tv_mfma_f32_16x16x32_f16 v[64:67], v[128:131], v[0:3], v[64:67]\n\t" \
  "ds_read_b128 v[0:3], v28 offset:3712\n\ts_waitcnt lgkmcnt(6)\n\tv_mfma_f32_16x16x32_f16 v[60:63], v[36:39], v[4:7], v[60:63]\n\t" \
  "ds_read_b128 v[4:7], v28 offset:3776\n\ts_waitcnt lgkmcnt(5)\n\tv_mfma_f32_16x16x32_f16 v[64:67], v[36:39], v[8:11], v[64:67]\n\t" \
  "ds_read_b128 v[8:11], v28 offset:3040\n\ts_waitcnt lgkmcnt(5)\n\tv_mfma_f32_16x16x32_f16 v[60:63], v[164:167], v[12:15], v[60:63]\n\t" \
  "v_mfma_f32_16x16x32_f16 v[64:67], v[132:135], v[12:15], v[64:67]\n\tds_read_b128 v[12:15], v28 offset:3840\n\ts_waitcnt lgkmcnt(5)\n\t" \
  "v_mfma_f32_16x16x32_f16 v[64:67], v[136:139], v[16:19], v[64:67]\n\tds_read_b128 v[16:19], v28 offset:3904\n\ts_waitcnt lgkmcnt(5)\n\t" \
  "v_mfma_f32_16x16x32_f16 v[44:47], v[20:23], v[68:71], 0\n\tv_mfma_f32_16x16x32_f16 v[40:43], v[20:23], v[72:75], 0\n\tds_read_b128 v[20:23], v28 offset:2016\n\t" \
  "v_cvt_pk_bf16_f32 v60, v60, v61\n\tv_cvt_pk_bf16_f32 v61, v62, v63\n\ts_waitcnt lgkmcnt(5)\n\t" \
  "v_mfma_f32_16x16x32_f16 v[64:67], v[140:143], v[0:3], v[64:67]\n\tds_write_b64 v29, v[60:61] offset:3072\n\tds_write_b32 v25, v25 offset:384\n\t" \
  "ds_read_b128 v[0:3], v32 offset:3072\n\tv_cvt_pk_f16_f32 v44, v44, v45\n\tv_cvt_pk_f16_f32 v45, v46, v47\n\t" \
  "v_cvt_pk_f16_f32 v46, v40, v41\n\tv_cvt_pk_f16_f32 v47, v42, v43\n\ts_waitcnt lgkmcnt(7)\n\t" \
  "v_mfma_f32_16x16x32_f16 v[64:67], v[144:147], v[4:7], v[64:67]\n\tds_read_b128 v[4:7], v28 offset:2032\n\tv_mfma_f32_16x16x32_f16 v[64:67], v[44:47], v[76:79], v[64:67]\n\t" \
  "s_waitcnt lgkmcnt(7)\n\tv_mfma_f32_16x16x32_f16 v[36:39], v[8:11], v[68:71], 0\n\tv_mfma_f32_16x16x32_f16 v[40:43], v[8:11], v[72:75], 0\n\t" \
  "ds_read_b128 v[8:11], v32 offset:2048\n\ts_waitcnt lgkmcnt(7)\n\tv_mfma_f32_16x16x32_f16 v[64:67], v[148:151], v[12:15], v[64:67]\n\t" \
  "ds_read_b128 v[12:15], v28 offset:3968\n\ts_waitcnt lgkmcnt(7)\n\tv_mfma_f32_16x16x32_f16 v[64:67], v[152:155], v[16:19], v[64:67]\n\t" \
  "ds_read_b128 v[16:19], v28 offset:4032\n\tv_cvt_pk_f16_f32 v36, v36, v37\n\tv_cvt_pk_f16_f32 v37, v38, v39\n\t" \
  "v_cvt_pk_f16_f32 v38, v40, v41\n\tv_cvt_pk_f16_f32 v39, v42, v43\n\ts_nop 1\n\t" \
  "v_mfma_f32_16x16x32_f16 v[64:67], v[36:39], v[88:91], v[64:67]\n\ts_waitcnt lgkmcnt(4)\n\tv_mfma_f32_16x16x32_f16 v[40:43], v[20:23], v[0:3], 0\n\t" \
  "ds_read_b64 v[0:1], v31 offset:544\n\tds_read_b64 v[2:3], v31 offset:712\n\tds_read_b128 v[20:23], v28 offset:4096\n\t" \
  "s_waitcnt lgkmcnt(6)\n\tv_mfma_f32_16x16x32_f16 v[64:67], v[48:51], v[4:7], v[64:67]\n\ts_waitcnt lgkmcnt(5)\n\t" \
  "v_mfma_f32_16x16x32_f16 v[44:47], v[4:7], v[8:11], 0\n\ts_waitcnt lgkmcnt(4)\n\tv_mfma_f32_16x16x32_f16 v[64:67], v[156:159], v[12:15], v[64:67]\n\t" \
  "s_waitcnt lgkmcnt(3)\n\tv_mfma_f32_16x16x32_f16 v[64:67], v[160:163], v[16:19], v[64:67]\n\ts_nop 3\n\t" \
  "v_cvt_pk_f16_f32 v44, v44, v45\n\tv_cvt_pk_f16_f32 v45, v46, v47\n\tv_cvt_pk_f16_f32 v46, v40, v41\n\t" \
  "v_cvt_pk_f16_f32 v47, v42, v43\n\ts_waitcnt lgkmcnt(1)\n\ts_nop 0\n\t" \
  "v_mfma_f32_16x16x32_f16 v[64:67], v[44:47], v[0:3], v[64:67]\n\ts_waitcnt lgkmcnt(0)\n\tv_mfma_f32_16x16x32_f16 v[64:67], v[164:167], v[20:23], v[64:67]\n\t" \
  "s_nop 7\n\tv_cvt_pk_bf16_f32 v64, v64, v65\n\tv_cvt_pk_bf16_f32 v65, v66, v67\n\t" \
  "ds_write_b64 v29, v[64:65] offset:3584\n\tds_write_b32 v25, v25 offset:448\n\t"
#define CONV_CLOBBER "v0", "v1", "v2", "v3", "v4", "v5", "v6", "v7", "v8", "v9", "v10", "v11", "v12", "v13", "v14", "v15", "v16", "v17", "v18", "v19", "v20", "v21", "v22", "v23", "v36", "v37", "v38", "v39", "v40", "v41", "v42", "v43", "v44", "v45", "v46", "v47", "v52", "v53", "v54", "v55", "v56", "v57", "v58", "v59", "v60", "v61", "v62", "v63", "v64", "v65", "v66", "v67", "v68", "v69", "v70", "v71", "v72", "v73", "v74", "v75", "v76", "v77", "v78", "v79", "v80", "v81", "v82", "v83", "v84", "v85", "v86", "v87", "v88", "v89", "v90", "v91", "v92", "v93", "v94", "v95", "v96", "v97", "v98", "v99", "v100", "v101", "v102", "v103", "v104", "v105", "v106", "v107", "v108", "v109", "v110", "v111", "v112", "v113", "v114", "v115", "v116", "v117", "v118", "v119", "v120", "v121", "v122", "v123", "v124", "v125", "v126", "v127", "v128", "v129", "v130", "v131", "v132", "v133", "v134", "v135", "v136", "v137", "v138", "v139", "v140", "v141", "v142", "v143", "v144", "v145", "v146", "v147", "v148", "v149", "v150", "v151", "v152", "v153", "v154", "v155", "v156", "v157", "v158", "v159", "v160", "v161", "v162", "v163", "v164", "v165", "v166", "v167"
#define G_CLOBBER "v8", "v9", "v10", "v11", "v12", "v13", "v14", "v15", "v16", "v17", "v18", "v19", "v20", "v21", "v22", "v23", "v24", "v25", "v26", "v27", "v28", "v29", "v30", "v32", "v33", "v34", "v35", "v36", "v37", "v38", "v39", "v40", "v41", "v42", "v43", "v44", "v45", "v46", "v47", "v48", "v49", "v50", "v51", "v52", "v53", "v54", "v55", "v56", "v57", "v58", "v59", "v60", "v61", "v62", "v63", "v64", "v65", "v66", "v67", "v68", "v69", "v70", "v71", "v72", "v73", "v74", "v75", "v76", "v77", "v78", "v79", "v80", "v81", "v82", "v83", "v84", "v85", "v86", "v87", "v88", "v89", "v90", "v91", "v92", "v93", "v94", "v95", "v96", "v97", "v98", "v99", "v100", "v101", "v102", "v103", "v104", "v105", "v106", "v107", "v108", "v109", "v110", "v111", "v112", "v113", "v114", "v115", "v116", "v117", "v118", "v119", "v120", "v121", "v122", "v123", "v124", "v125", "v126", "v127"
#define D_CLOBBER "v8", "v9", "v10", "v11", "v12", "v13", "v14", "v15", "v16", "v17", "v18", "v19", "v20", "v21", "v22", "v23", "v24", "v25", "v26", "v27", "v28", "v29", "v30", "v31"
__device__ __forceinline__ f32x2 pkfma(f32x2 a, f32x2 b, f32x2 c) {
  f32x2 r;
  asm("v_pk_fma_f32 %0, %1, %2, %3" : "=v"(r) : "v"(a), "v"(b), "v"(c));
  return r;
}
__device__ __forceinline__ float fmav(float a, float b, float c) {
  float r;
  asm("v_fma_f32 %0, %1, %2, %3" : "=v"(r) : "v"(a), "v"(b), "v"(c));
  return r;
}
__device__ __forceinline__ float fmac(float a, float b, float c) {
  asm("v_fmac_f32 %0, %1, %2" : "+v"(c) : "v"(a), "v"(b));
  return c;
}
__device__ __forceinline__ float fmas(float a, u32 s, float c) {
  asm("v_fmac_f32 %0, %2, %1" : "+v"(c) : "v"(a), "s"(s));
  return c;
}
template <int HI>
__device__ __forceinline__ f32x2 pkfmas(f32x2 a, f32x2 s, f32x2 c) {
  f32x2 r;
  if (HI) asm("v_pk_fma_f32 %0, %2, %1, %3 op_sel:[1,0,0] op_sel_hi:[1,1,1]" : "=v"(r) : "v"(a), "v"(s), "v"(c));
  else asm("v_pk_fma_f32 %0, %2, %1, %3 op_sel:[0,0,0] op_sel_hi:[0,1,1]" : "=v"(r) : "v"(a), "v"(s), "v"(c));
  return r;
}
#define RANK8(d0, d1, d2, d3, c0, c1, i0, f0, f1) "v_perm_b32 " d0 ", " d0 ", " d1 ", %[sg]\n\tv_perm_b32 " d2 ", " d2 ", " d3 ", %[sg]\n\t" \
  "v_bcnt_u32_b32 " d0 ", " d0 ", " i0 "\n\tv_bcnt_u32_b32 " c0 ", " d2 ", " d0 "\n\tv_bcnt_u32_b32 " c1 ", " d2 ", " d0 "\n\t" f0 "\n\t" \
  "v_permlane16_swap_b32 " c0 ", " c1 "\n\tv_add_u32 " d0 ", " c0 ", " c1 "\n\tv_add_u32 " d1 ", " c0 ", " c1 "\n\t" f1 "\n\t" \
  "v_permlane32_swap_b32 " d0 ", " d1 "\n\tv_add_u32 " c0 ", " d0 ", " d1 "\n\t"
__device__ __forceinline__ void ln_rows(u32x4 (&xv)[4], u32x8 wv, rsrc_t rs, u32& tag, int b, int w, int l, u32 st, u32 sg TLARG) {
  float s[2][3] = {}, c[2], sw = 0.f;
  u32 os = l < 16 ? O_SG + 128 * (b - 128) + 8 * l : 0x80000000u;
  u32 ot = l < 16 ? O_TOP + 8 * (b & 15) + TL * ((b >> 4) & 7) : l < 32 ? O_RG - 128 + 128 * (b - 128) + 8 * l : 0x80000000u, om = l < 16 ? TOPS / 8 : 0u, fm = l < 16 ? 0xff0u : 0u;
  u32 aw = (l & 16) ? st + 8 * w + (l >> 5) * 4 : st + 136, ac = st + 128, ar = st + 4 * (l & 15), aq = st + (l & 48), one = 1u;
  u32 kl = (2047 - (16 * (b - 128) + 2 * w + (l >> 5))) << 1;
  asm volatile("" : "+v"(os), "+v"(aw), "+v"(ac), "+v"(ar), "+v"(aq), "+v"(one), "+v"(kl), "+v"(ot), "+v"(om), "+v"(fm));
  SB();
  asm volatile("s_waitcnt vmcnt(4) lgkmcnt(0)" : "+{s44}"(tag)::"memory");
  asm volatile("" : "+{v[116:123]}"(wv));
  tag += 1u;
  *(volatile LDS u32*)(size_t)(st - 104) = tag;
  STAMP(12);
#pragma unroll
  for (int i = 0; i < 8; i++) sw = dot2b(wv[i], 0x3f803f80u, sw);
  sw += dppf<0x121, 0xf>(sw); sw += dppf<0x122, 0xf>(sw); sw += dppf<0x124, 0xf>(sw); sw += dppf<0x128, 0xf>(sw);
  sw = (rdl(sw, 0) + rdl(sw, 16)) + (rdl(sw, 32) + rdl(sw, 48));
  asm volatile("" : "+v"(sw));
  SB();
  float cv = 0.f;
#pragma unroll
  for (int r = 0; r < 2; r++) {
#pragma unroll
    for (int i = 0; i < 8; i++) {
      if (r == 0 && i == 0) { asm volatile("s_waitcnt vmcnt(3)" : "+{v[100:103]}"(xv[0])); STAMP(13); }
      if (r == 0 && i == 4) asm volatile("s_waitcnt vmcnt(2)" : "+{v[104:107]}"(xv[1]));
      if (r == 1 && i == 0) asm volatile("s_waitcnt vmcnt(1)" : "+{v[108:111]}"(xv[2]));
      if (r == 1 && i == 4) asm volatile("s_waitcnt vmcnt(0)" : "+{v[112:115]}"(xv[3]));
      const u32 a = xv[2 * r + (i >> 2)][i & 3];
      s[r][0] = dot2b(a, 0x3f803f80u, s[r][0]); s[r][1] = dot2b(a, a, s[r][1]); s[r][2] = dot2b(a, wv[i], s[r][2]);
      if (i == 3) {
        c[r] = rdl(s[r][0] * 0.125f, 0);
        if (r) { cv = l < 32 ? c[0] : c[1]; asm volatile("" : "+v"(cv)); }
        SB();
      }
    }
    const float ml = s[r][0] * 0.0625f, dl = __builtin_fmaf(s[r][0], 0.25f, -4.f * c[r]);
    s[r][1] = __builtin_fmaf(dl, dl, __builtin_fmaf(-s[r][0], ml, s[r][1]));
    SB();
  }
#pragma unroll
  for (int k = 0; k < 3; k++) swap32(s[0][k ? 3 - k : 0], s[1][k ? 3 - k : 0]);
  float q[3];
#pragma unroll
  for (int k = 0; k < 3; k++) q[k] = s[0][k] + s[1][k];
#define RED3(m) "v_add_f32_dpp %0, %0, %0 " m " bank_mask:0xf\n\tv_add_f32_dpp %1, %1, %1 " m " bank_mask:0xf\n\tv_add_f32_dpp %2, %2, %2 " m " bank_mask:0xf\n\t"
  asm volatile("s_nop 1\n\t" RED3("row_ror:1 row_mask:0xf") RED3("row_ror:2 row_mask:0xf") RED3("row_ror:4 row_mask:0xf") RED3("row_ror:8 row_mask:0xf")
               RED3("row_bcast:15 row_mask:0xa") : "+v"(q[0]), "+v"(q[1]), "+v"(q[2]));
  const float mu = q[0] * (1.f / D), dv = mu - cv;
  const float var = fmaxf(__builtin_fmaf(-dv, dv, q[1] * (1.f / D)), 0.f);
  const float m = rb(mu);
  float sd = __builtin_amdgcn_sqrtf(rb(rb(var) + 1e-5f)), num = __builtin_fmaf(-m, sw, q[2]);
  asm volatile("s_nop 0\n\tv_cvt_pk_bf16_f32 %0, 0, %0" : "+v"(sd), "+v"(num));
  u32 rv = __float_as_uint(rb(num * __builtin_amdgcn_rcpf(sd)));
  rv ^= (u32)((int)rv >> 15) | 0x80000000u;
  rv = __builtin_amdgcn_perm(rv, kl, 0x07060100u) >> 1;
  asm volatile("ds_write2_b32 %[aw], %[sv], %[rv] offset1:16\n\ts_mov_b64 exec, 1\n\tds_add_rtn_u32 v104, %[ac], %[one]\n\ts_mov_b64 exec, -1\n\t"
               "ds_read_b32 v106, %[ar]\n\tds_read_b32 v105, %[ar] offset:64\n\tds_read_b128 v[100:103], %[aq] offset:64\n\tv_mov_b32 v107, %[t]\n\tv_mov_b32 v109, %[t]\n\t"
               "s_waitcnt lgkmcnt(3)\n\tv_cmp_eq_u32 vcc, 7, v104\n\ts_bitcmp1_b32 vcc_lo, 0\n\ts_cbranch_scc0 1f\n\t"
               "s_waitcnt lgkmcnt(2)\n\tbuffer_store_dwordx2 v[106:107], %[os], %[rs], 0 offen sc1\n\ts_waitcnt lgkmcnt(0)\n\t"
               "v_sub_u32 v100, v105, v100\n\tv_sub_u32 v101, v105, v101\n\tv_sub_u32 v102, v105, v102\n\tv_sub_u32 v103, v105, v103\n\t"
               RANK8("v100", "v101", "v102", "v103", "v104", "v103", "0", "s_nop 1", "s_nop 1") "v_cmp_eq_u32 vcc, 16, v104\n\t"
               "v_sub_u32 v108, 19, v104\n\ts_ff1_i32_b32 s6, vcc_lo\n\tv_readlane_b32 s6, v105, s6\n\t"
               "v_mad_u32_u24 v104, v104, %[om], %[ot]\n\ts_nop 0\n\tv_lshrrev_b32 v108, v108, s6\n\tv_bfi_b32 v108, %[fm], v108, v105\n\t"
               "buffer_store_dwordx2 v[108:109], v104, %[rs], 0 offen sc1\n1:\n\ts_waitcnt lgkmcnt(0)"
               :: [aw] "v"(aw), [sv] "v"(__builtin_amdgcn_perm(__float_as_uint(sd), __float_as_uint(m), 0x07060302u)), [rv] "v"(rv), [ac] "v"(ac), [one] "v"(one),
                  [ar] "v"(ar), [aq] "v"(aq), [os] "v"(os), [om] "v"(om), [ot] "v"(ot), [fm] "v"(fm), [sg] "s"(sg), [t] "s"(tag), [rs] "s"(rs)
               : "vcc", "scc", "s6", "v100", "v101", "v102", "v103", "v104", "v105", "v106", "v107", "v108", "v109", "memory");
  SB();
}

#define GL __attribute__((address_space(1)))
typedef const GL u16* gp;

__global__ __launch_bounds__(768, 3) void fused(const u16*, const u16*, const u16*, const u16*, const u16*, const u16*, const u16*, const u16*,
    const u16*, const u16*, const u16*, u16*, char*) {
  __shared__ __attribute__((aligned(256))) char smem[SMEM];
  const u32 lds0 = (u32)(size_t)(LDS char*)smem;
  int w0;
#define GLD(r0, r1, r2, r3, o_) "global_load_dwordx4 v[" #r0 ":" #r1 "], " #o_ ", s[16:17]\n\tglobal_load_dwordx4 v[" #r2 ":" #r3 "], " #o_ ", s[16:17] offset:2048\n\t"
  asm volatile(
      "s_load_dwordx16 s[16:31], s[0:1], 0\n\ts_load_dwordx8 s[32:39], s[0:1], 64\n\ts_load_dwordx2 s[40:41], s[0:1], 96\n\t"
      "s_memrealtime s[46:47]\n\t"
      "s_getpc_b64 s[4:5]\n2:\n\ts_lshr_b32 s7, s2, 3\n\ts_and_b32 s10, s4, 0xffffff00\n\ts_mov_b32 s11, s5\n\ts_mul_i32 s8, s7, 0x600\n\t"
      "v_lshl_add_u32 v3, v0, 1, s8\n\ts_cmpk_lt_u32 s2, %[pn]\n\ts_cbranch_scc0 1f\n\t"
      "global_load_ushort v163, v3, s[10:11] offset:%[pf]\n"
      "1:\n\ts_cmpk_lt_u32 s2, 0x80\n\tv_readfirstlane_b32 s3, v0\n\tv_and_b32 v1, 63, v0\n\tv_mov_b64 v[4:5], 0\n\tv_mov_b64 v[6:7], 0\n\tv_lshlrev_b32 v2, 4, v1\n\tv_lshlrev_b32 v8, 2, v0\n\t"
      "s_mov_b32 s42, %[we]\n\ts_mov_b32 s43, 0x20000\n\ts_cbranch_scc0 3f\n\t"
      "s_lshr_b32 s3, s3, 6\n\ts_mul_i32 s9, s3, %[xr]\n\ts_min_u32 s9, s9, %[zm]\n\tv_add_u32 v3, s9, v2\n\tv_add_u32 v3, %[zo], v3\n\t"
      "s_and_b32 s7, s7, 7\n\ts_and_b32 s6, s2, 7\n\ts_and_b32 s8, s2, 64\n\ts_lshl3_add_u32 s8, s6, s8\n\ts_add_u32 s45, s8, s7\n\t"
      "s_add_u32 s4, s4, lane_tab@rel32@lo+(6f-2b-4)\n6:\n\ts_addc_u32 s5, s5, lane_tab@rel32@hi+(6b-2b+4)\n\t"
      "ds_write_b128 v3, v[4:7]\n\ts_cmp_lt_u32 s3, 8\n\ts_cbranch_scc1 5f\n\t"
      "s_lshl_b32 s8, s45, 4\n\ts_lshl_b32 s9, s3, 18\n\ts_add_u32 s9, s9, s8\n\ts_bitset0_b32 s9, 21\n\tv_lshl_add_u32 v148, v1, 12, s9\n\t"
      "s_lshl3_add_u32 s9, s3, s7\n\ts_lshl_b32 s9, s9, 17\n\ts_add_u32 s9, s9, s8\n\ts_bitset0_b32 s9, 23\n\tv_lshl_add_u32 v9, v1, 11, s9\n\t"
      "v_add_u32 v11, 0x100000, v148\n\tv_add_u32 v12, 0x200000, v148\n\t"
      "v_lshlrev_b32 v149, 4, v0\n\ts_lshl_b32 s9, s3, 10\n\ts_add_u32 m0, s9, %[ob]\n\tv_add_u32 v10, 0xffffe800, v149\n\tglobal_load_lds_dwordx4 v10, s[4:5]\n\t"
      "v_add_u32 v149, %[sg], v149\n\t"
      "s_waitcnt lgkmcnt(0)\n\ts_load_dword s44, s[40:41], 0\n\t"
      "s_barrier\n\t"
      "global_load_dword v160, v9, s[16:17]" PRENT "\n\t"
      GLD(100, 103, 104, 107, v148)
      GLD(108, 111, 112, 115, v11)
      "s_cmp_eq_u32 s6, 3\n\ts_cbranch_scc1 .Lrole_p\n\t"
      GLD(116, 119, 120, 123, v12)
      "s_branch .Lrole_p\n"
      "3:\n\ts_lshr_b32 s3, s3, 6\n\ts_cmp_lt_u32 s3, 8\n\ts_cbranch_scc1 4f\n\ts_barrier\n\ts_branch .Lrole_d\n4:\n\t"
      "v_add_u32 v3, %[fc], v2\n\ts_add_u32 s8, s3, s2\n\ts_and_b32 s45, s8, 7\n\ts_lshl3_add_u32 s8, s2, s45\n\t"
      "s_lshl_b32 s8, s8, 12\n\ts_bitset0_b32 s8, 22\n\tds_write_b128 v3, v[4:7]\n\tv_add_u32 v3, s8, v2\n\t"
      "s_waitcnt lgkmcnt(0)\n\tglobal_load_dwordx4 v[116:119], v2, s[30:31]\n\tglobal_load_dwordx4 v[120:123], v2, s[30:31] offset:1024\n\t"
      "s_load_dword s44, s[40:41], 0\n\ts_barrier\n\t"
      "global_load_dwordx4 v[100:103], v3, s[16:17]" LNNT "\n\tglobal_load_dwordx4 v[104:107], v3, s[16:17] offset:1024" LNNT "\n\t"
      "global_load_dwordx4 v[108:111], v3, s[16:17] offset:2048" LNNT "\n\tglobal_load_dwordx4 v[112:115], v3, s[16:17] offset:3072" LNNT "\n\t"
      "s_branch .Lrole_l\n"
      "5:\n\t"
      "global_load_dwordx4 v[16:19], v2, s[4:5]\n\tglobal_load_dwordx4 v[20:23], v2, s[4:5] offset:1024\n\t"
      "s_lshl3_add_u32 s8, s45, s3\n\ts_lshl_b32 s9, s8, 1\n\tv_mov_b32 v9, s9\n\ts_lshl_b32 s9, s45, 11\n\tv_add_u32 v3, s9, v8\n\t"
      "s_waitcnt lgkmcnt(0)\n\tglobal_load_short_d16_hi v24, v9, s[26:27]\n\tglobal_load_ushort v160, v9, s[28:29]\n\t"
      "global_load_dword v96, v3, s[18:19]" PNT "\n\tglobal_load_dword v97, v3, s[20:21]" PNT "\n\t"
      "global_load_dword v68, v3, s[22:23]" PNT "\n\tglobal_load_dword v69, v3, s[24:25]" PNT "\n\ts_barrier\n\ts_branch .Lrole_c"
      : "={s3}"(w0)
      : "{s[0:1]}"(__builtin_amdgcn_kernarg_segment_ptr()), "{s2}"(blockIdx.x), "{v0}"(threadIdx.x), [pf] "n"(PFO), [pn] "n"(8 * PFN), [we] "n"(WS_END), [xr] "n"(XREG),
        [zm] "n"(8 * XREG), [zo] "n"(OX - 16), [sg] "n"(O_SG - 8192), [fc] "n"(FC), [ob] "n"(OB - 8192)
      : "memory");
#define TLREGS()
#define ROLE(name, text, ...) \
  gp x, wg, wu, wd; GL u16* out; rsrc_t rs; int b, w, t, l, g; u32 tag, st0; \
  asm volatile(".Lrole_" #name ":" : "={v0}"(t), "={v1}"(l) __VA_ARGS__ :: "memory"); \
  asm volatile(text : "={s2}"(b), "={s3}"(w), "={s[16:17]}"(x), "={s[32:33]}"(wg), "={s[34:35]}"(wu), "={s[36:37]}"(wd), \
               "={s[38:39]}"(out), "={s[40:43]}"(rs), "={s44}"(tag), "={s45}"(g), "={s46}"(st0) :: "memory"); \
  SB(); \
  __builtin_assume((u32)l < 64u); __builtin_assume((u32)t < 768u); __builtin_assume((u32)w < 12u); __builtin_assume((u32)b < 256u); __builtin_assume((u32)g < 128u); \
  TLREGS(); \
  const int xk = b & 7, j = (b >> 3) & 15, n16 = l & 15, kg = l >> 4
#define TAGWAIT() do { asm volatile("s_waitcnt lgkmcnt(0)" : "+{s44}"(tag)::"memory"); tag += 1u; } while (0)
#define BAR() do { SB(); asm volatile("s_barrier" ::: "memory"); SB(); } while (0)
#define FLAG(o, v) do { asm volatile("" ::: "memory"); SB(); *(volatile LDS u32*)(LDS char*)(smem + (o)) = (v); SB(); } while (0)
#define ARRIVE(o) do { SB(); if (l == 0) asm volatile("ds_add_u32 %0, %1" ::"v"(lds0 + (o)), "v"(1u) : "memory"); SB(); } while (0)
#define CWAIT(o, n, nap) do { _Pragma("unroll 1") for (int spin = 0; spin < SPIN; spin++) { \
      if (__builtin_amdgcn_readfirstlane((int)*(const volatile LDS u32*)(LDS char*)(smem + (o))) >= (int)(n)) break; \
      asm volatile(nap ::: "memory"); } SB(); asm volatile("" ::: "memory"); } while (0)
#define AWAIT(o, n, nap) do { _Pragma("unroll 1") for (int spin = 0; spin < SPIN; spin++) { \
      if (__all(*(const volatile LDS u32*)(LDS char*)(smem + (o)) >= (u32)(n))) break; \
      asm volatile(nap ::: "memory"); } SB(); asm volatile("" ::: "memory"); } while (0)
#define POLL8(res_, off_) do { u32 n_ = SPIN; \
    asm volatile("1:\n\tbuffer_load_dwordx2 v[4:5], %2, %3, 0 offen sc1\n\ts_sub_u32 %1, %1, 1\n\ts_waitcnt vmcnt(0)\n\t" \
                 "v_cmp_ne_u32 vcc, %4, v5\n\ts_cbranch_scc1 2f\n\ts_cbranch_vccnz 1b\n2:" \
                 : "=&{v[4:5]}"(res_), "+s"(n_) : "v"(off_), "s"(rs), "s"(tag) : "vcc", "scc", "memory"); } while (0)
#define UNTIL(t_) "2:\n\ts_sleep 1\n\ts_memrealtime s[8:9]\n\ts_waitcnt lgkmcnt(0)\n\ts_sub_u32 s8, s8, %[st]\n\ts_cmp_lt_u32 s8, " t_ "\n\ts_cbranch_scc1 2b\n\t"
#define LANE(v_) int v_; asm volatile("v_mbcnt_lo_u32_b32 %0, -1, 0\n\tv_mbcnt_hi_u32_b32 %0, -1, %0" : "=v"(v_))
#define IXO(l_) (O_IDX + (2 * xk + (j & 1)) * 128 + ((l_) & 7) * 8)
#define IDS() const int id = 16 * xk + j, sb = id * 57 >> 10, mat = id & 1, u = (id - 18 * sb) >> 1; \
    const bool slow = u >= 6; \
    const int part = slow ? u - 6 : u & 1, cb = 4 * sb + (slow ? 1 : (u >> 1) + (u > 1)), r0 = part * (slow ? 320 : 512), ns = slow ? 5 + (u >> 3) : 8; \
    const int kp = 4 * sb + ((u >> 1) & 3), ct = 2 * (u & 1) + mat
#define TS(a, b_)
#define TSC
#define DONE "s_endpgm\n"
#define TSD
#define TSGET(i0, i1, i2, i3)
  if (blockIdx.x >= 128) {
    if (w0 >= 8) {
      ROLE(d, "s_waitcnt lgkmcnt(0)");
      IDS();
      const int dw = w - 8;
      const u32 sw = (((n16 >> 1) ^ (kg & 1)) << 5) + ((l & 1) << 4);
      gp base = (mat ? wu : wg) + (size_t)(r0 + 4 * dw) * F + cb * 128;
      u32 v = kg * (2 * F) + sw, fa = lds0 + FC + 4 * dw, vp = 96 + dw, t_;
      asm volatile("" : "+v"(v), "+v"(fa), "+v"(vp));
      asm volatile("s_mov_b32 m0, %0" ::"s"(lds0 + 1024 * dw));
#define VMC "s_getreg_b32 s6, hwreg(HW_REG_IB_STS, 0, 4)\n\t"
#define RUN(k, q, lim, v_, b_, st_) asm volatile("1:\n\ts_lshr_b32 s6, m0, 4\n\ts_and_b32 s6, s6, 0xc0\n\tv_xor_b32 %[t], s6, %[v]\n\ts_waitcnt vmcnt(%[c])\n\t" VMC \
        "global_load_lds_dwordx4 %[t], %[b] nt\n\ts_add_u32 m0, m0, %[m]\n\tv_add_u32 %[v], %[d], %[v]\n\t.if %[m] == 3072\n\tv_mad_i32_i24 %[t], s6, -3, %[p]\n\tv_add_u32 %[p], 3, %[p]\n\t" \
        "s_mov_b64 exec, 1\n\tds_max_u32 %[a], %[t]\n\ts_mov_b64 exec, -1\n\t.endif\n\ts_cmp_lt_u32 m0, %[l]\n\ts_cbranch_scc1 1b" \
        : [v] "+v"(v_), [p] "+v"(vp), [t] "=&v"(t_) : [b] "s"(b_), [c] "n"((q) - 1), [m] "n"(1024 * (k)), [d] "n"((k) * (st_)), \
          [a] "v"(fa), [l] "s"(lim) : "scc", "s6", "memory")
#define MARK(c, val) "s_waitcnt vmcnt(" #c ")\n\t" val "\n\ts_mov_b64 exec, 1\n\tds_max_u32 %[a], %[t]\n\ts_mov_b64 exec, -1\n\t"
      __builtin_amdgcn_s_setprio(DPRIO);
      STAMP(8);
      if (id < 126) { if (slow) RUN(4, QAS, 32768, v, base, 8 * F); else RUN(4, 8, 32768, v, base, 8 * F); }
      if (dw < 3) {
        if (id >= 126) __builtin_amdgcn_endpgm();
        gp bd = wd + (size_t)(128 * kp + 2 * dw) * D + ct * 256;
        u32 vd = (l >> 5) * (2 * D + 32) ^ 16 * (l & 31);
        asm volatile("" : "+v"(vd));
        if (slow) RUN(3, QS, 16384 * ns, v, base, 8 * F);
        else { RUN(3, 8, 32768 + 3072 * RAMP, v, base, 8 * F); RUN(3, QD, 131072, v, base, 8 * F); }
        STAMP(1);
        {
          u32 n_ = SPIN;
          asm volatile("1:\n\t" VMC MARK(63, "v_mad_i32_i24 %[t], s6, -3, %[p]") "ds_read_b32 %[t], %[f]\n\ts_sub_u32 %[n], %[n], 1\n\ts_waitcnt lgkmcnt(0)\n\t"
                       "v_cmp_lt_u32 vcc, %[k], %[t]\n\ts_cbranch_scc1 2f\n\ts_cbranch_vccz 1b\n2:\n\ts_mov_b32 m0, %[m]"
                       : [t] "=&v"(t_), [n] "+s"(n_) : [a] "v"(fa), [p] "v"(vp), [f] "v"(u == 8 ? fa : lds0 + FC + 20), [k] "s"(u == 8 ? 159 : 16 * HOLD - 105), [m] "s"(lds0 + 1024 * dw)
                       : "vcc", "scc", "s6", "memory");
        }
        STAMP(3);
        if (u < 8) {
          RUN(3, QE, 65536, vd, bd, 4 * D);
          STAMP(6);
          asm volatile(MARK(0, "v_mov_b32 %[t], -1") : [t] "=&v"(t_) : [a] "v"(fa) : "memory");
          STAMP(4);
        }
        FLUSH();
        __builtin_amdgcn_endpgm();
      }
      asm volatile(MARK(0, "v_mov_b32 %[t], -1") : [t] "=&v"(t_) : [a] "v"(fa) : "memory");
      u32 km, rk;
      u32 hi, lo, k2, k3, gd, thr, sg = 0x0c0c0b09u;
      u32 ka = lds0 + FC + 384 + 8 * (l >> 3), kc = 2047 * 2048 + 2 * r0 + 16 * ((l & 7) ^ ((l >> 2) & 4)), kq = lds0 + FC + 512 + (l & 48), kr = lds0 + FC + 512 + 4 * n16;
      u32 c1 = 896, c2 = 1792, c3 = 2688, cm = (u32)-2048, cs = O_SG + 8 * 2047, ca = lds0 + FC + 512;
      rsrc_t rx = __builtin_amdgcn_make_buffer_rsrc((void*)(x - 1344), 0, 2 * L * D + 4096, 0x00020000);
      u32 co = O_CLR + 8 * (18 * sb + (0x84c0 >> 4 * (kp & 3) & 15) + min(l, (kp & 3) == 1 ? 5 : 3));
      asm volatile("" : "+v"(ka), "+v"(kc), "+v"(kq), "+v"(kr), "+v"(co));
      asm volatile("" : "+s"(c1), "+s"(c2), "+s"(c3), "+s"(cm), "+s"(cs), "+s"(ca), "+s"(sg), "+s"(rx));
      *(u32x4*)(smem + FA + 6144 + 16 * l) = u32x4{0u, 0u, 0u, 0u};
      *(u32x4*)(smem + FA + 7168 + 16 * l) = u32x4{0u, 0u, 0u, 0u};
      CWAIT(FC + 256, 1, "s_sleep 4");
      tag = (u32)__builtin_amdgcn_readfirstlane((int)*(const volatile LDS u32*)(LDS char*)(smem + FC + 24));
      {
        u32 n_ = SPIN, t0, t1, t2, t3, se;
#define TLX(a, b_, c, d, sc) "buffer_load_dwordx4 v[" #a ":" #b_ "], %[o], %[r], 0 offen" sc "\n\tbuffer_load_dwordx4 v[" #c ":" #d "], %[o], %[r], %[ts] offen" sc "\n\t"
#define TLD(a, b_, c, d) TLX(a, b_, c, d, " sc1")
#define TCK(r0, r1, r2, r3, r4, r5, r6, r7) "s_waitcnt vmcnt(4)\n\tv_xor_b32 %[t0], %[t], v" #r1 "\n\tv_bitop3_b32 %[t0], %[t0], %[t], v" #r3 " bitop3:0xf6\n\t" \
          "v_bitop3_b32 %[t0], %[t0], %[t], v" #r5 " bitop3:0xf6\n\tv_bitop3_b32 %[t0], %[t0], %[t], v" #r7 " bitop3:0xf6\n\t" \
          "v_cmp_ne_u32 vcc, 0, %[t0]\n\tv_mov_b32 %[hi], v" #r0 "\n\tv_mov_b32 %[lo], v" #r2 "\n\tv_mov_b32 %[k2], v" #r4 "\n\tv_mov_b32 %[k3], v" #r6 "\n\t" \
          "s_cbranch_vccz 5f\n\t" TLD(r0, r3, r4, r7)
        asm volatile("2:\n\ts_sleep 1\n\ts_memrealtime s[8:9]\n\ts_waitcnt lgkmcnt(0)\n\ts_sub_u32 s8, s8, %[st]\n\ts_cmp_lt_u32 s8, %[tw]\n\ts_cbranch_scc1 2b\n\t"
                     TLX(128, 131, 132, 135, SCT) "s_sleep " STR(RSL) "\n\t" TLD(136, 139, 140, 143) "s_sleep " STR(RSL) "\n\t" TLX(144, 147, 148, 151, SCU)
                     "1:\n\t" TCK(128, 129, 130, 131, 132, 133, 134, 135) TCK(136, 137, 138, 139, 140, 141, 142, 143) TCK(144, 145, 146, 147, 148, 149, 150, 151)
                     "s_sub_u32 %[n], %[n], 1\n\ts_cbranch_scc0 1b\n5:\n\t" TS(60, 61)
                     "v_and_or_b32 %[t0], %[hi], %[mk], %[pa]\n\tv_and_or_b32 %[t1], %[lo], %[mk], %[pb]\n\t"
                     "v_max_u32 %[t2], %[t0], %[t1]\n\tv_max_u32 %[t3], %[t0], %[t1]\n\tv_perm_b32 %[gd], %[hi], %[lo], %[p0]\n\tv_perm_b32 %[se], %[k2], %[k3], %[p0]\n\t"
                     "v_permlane16_swap_b32 %[t2], %[t3]\n\tv_max_u32 %[km], %[t2], %[t3]\n\tv_max_u32 %[rk], %[t2], %[t3]\n\t"
                     "v_max_u32 %[hi], %[t0], %[t1]\n\tv_min_u32 %[lo], %[t0], %[t1]\n\t"
                     "v_permlane32_swap_b32 %[km], %[rk]\n\tv_max_u32 %[km], %[km], %[rk]\n\tv_not_b32 %[t2], %[km]\n\t"
                     "v_and_or_b32 %[k2], %[k2], %[mk], %[pa]\n\t"
                     "v_not_b32_dpp %[t2], %[km] row_ror:4 row_mask:0x2 bank_mask:0xf\n\tv_not_b32_dpp %[t2], %[km] row_ror:8 row_mask:0x4 bank_mask:0xf\n\t"
                     "v_not_b32_dpp %[t2], %[km] row_ror:12 row_mask:0x8 bank_mask:0xf\n\t"
                     "v_pk_lshlrev_b16 %[gd], 4, %[gd] op_sel_hi:[0,1]\n\tv_add_u32 %[t0], %[km], %[t2]\n\t"
                     "v_add_u32_dpp %[t1], %[t2], %[km] row_ror:1 row_mask:0xf bank_mask:0xf\n\tv_add_u32_dpp %[t3], %[t2], %[km] row_ror:2 row_mask:0xf bank_mask:0xf\n\t"
                     "v_add_u32_dpp %[t2], %[t2], %[km] row_ror:3 row_mask:0xf bank_mask:0xf\n\t"
                     RANK8("%[t0]", "%[t1]", "%[t2]", "%[t3]", "%[rk]", "%[t3]", "-2", "v_lshrrev_b32 %[se], 4, %[se]\n\tv_perm_b32 %[gd], %[gd], %[se], %[p1]",
                           "v_pk_max_u16 %[gd], %[gd], %[gd] op_sel:[0,1] op_sel_hi:[1,0]\n\tv_and_or_b32 %[k3], %[k3], %[mk], %[pb]")
                     "v_cmp_eq_u32 vcc, 56, %[rk]\n\tv_max3_u32 %[se], %[lo], %[k2], %[k3]\n\tv_lshrrev_b32 %[gd], 1, %[gd]\n\ts_ff1_i32_b32 s6, vcc_lo\n\t"
                     "v_readlane_b32 s6, %[km], s6\n\tv_or_b32 %[gd], 0x7fff, %[gd]\n\tv_med3_u32 %[t2], %[lo], %[k2], %[k3]\n\t"
                     "v_cmp_le_u32 s[8:9], s6, %[hi]\n\tv_cmp_le_u32 s[10:11], s6, %[se]\n\tv_max_u32 %[t2], %[t2], %[gd]\n\t"
                     "v_mbcnt_lo_u32_b32 %[t0], s8, 0\n\tv_mbcnt_lo_u32_b32 %[t1], s10, -15\n\tv_mbcnt_hi_u32_b32 %[t0], s9, %[t0]\n\tv_mbcnt_hi_u32_b32 %[t1], s11, %[t1]\n\t"
                     "v_lshl_add_u32 %[t0], %[t0], 2, %[ca]\n\tv_mad_i32_i24 %[t1], %[t1], -4, %[ca]\n\tv_cmp_le_u32 vcc, s6, %[t2]\n\t"
                     "s_mov_b64 exec, s[8:9]\n\tds_write_b32 %[t0], %[hi]\n\ts_mov_b64 exec, s[10:11]\n\tds_write_b32 %[t1], %[se]\n\ts_mov_b64 exec, -1\n\t"
                     "ds_read_b32 %[se], %[kr]\n\tds_read_b128 v[152:155], %[kq]\n\t"
                     "s_bcnt1_i32_b64 s8, s[8:9]\n\ts_bcnt1_i32_b64 s10, s[10:11]\n\ts_add_u32 s8, s8, s10\n\ts_cmp_gt_u32 s8, 16\n\ts_cselect_b64 s[8:9], -1, vcc\n\t"
                     "s_cmp_lg_u64 s[8:9], 0\n\ts_waitcnt lgkmcnt(0)\n\ts_cbranch_scc1 6f\n\t"
                     "v_sub_u32 %[t0], %[se], v152\n\tv_sub_u32 %[t1], %[se], v153\n\tv_sub_u32 %[t2], %[se], v154\n\tv_sub_u32 %[t3], %[se], v155\n\t"
                     RANK8("%[t0]", "%[t1]", "%[t2]", "%[t3]", "%[rk]", "%[t3]", "0", "v_mov_b32 %[km], %[se]\n\ts_nop 0", "s_nop 1") "6:\n\t" TS(62, 63)
                     : [hi] "=&v"(hi), [lo] "=&v"(lo), [k2] "=&v"(k2), [k3] "=&v"(k3), [gd] "=&v"(gd), [se] "=&v"(se), [km] "=&v"(km), [rk] "=&v"(rk),
                       [t0] "=&v"(t0), [t1] "=&v"(t1), [t2] "=&v"(t2), [t3] "=&v"(t3), [n] "+s"(n_)
                     : [o] "v"(O_TOP + 16 * (l & 7) + TL * (l >> 3)), [r] "s"(rs), [t] "s"(tag), [ts] "s"(TOPS), [st] "s"(st0), [tw] "n"(TW), [mk] "s"(0x7fff800fu),
                       [pa] "v"((127 - 2 * l) << 4), [pb] "v"((126 - 2 * l) << 4), [sg] "s"(sg), [p0] "s"(0x05040100u), [p1] "s"(0x07020500u), [ca] "s"(ca),
                       [kr] "v"(kr), [kq] "v"(kq)
                     : "vcc", "scc", "s6", "s8", "s9", "s10", "s11", "memory" TSC);
      }
      unsigned long long more;
      asm volatile("" : "={s6}"(thr), "={s[8:9]}"(more));
      if (__builtin_expect(more != 0, 0)) {
        const u32 m0 = km;
        unsigned long long all = __ballot(gd >= thr);
        if (gd >= thr) hi = lo = k2 = k3 = 0u;
#pragma unroll 1
        for (int i = 0; i < 4;) {
          u32 k, m = m0;
          if (all) {
            const int a = __builtin_ctzll(all);
            all &= all - 1;
            u32x2 r;
            const u32 o = O_RG + 256 * a + 8 * (l & 31);
            POLL8(r, o);
            k = l < 32 ? r.x : 0u;
            m = (u32)__builtin_amdgcn_readlane((int)m0, a);
          } else { k = hi; hi = lo; lo = k2; k2 = k3; i++; }
          asm volatile("v_cmp_le_u32 s[8:9], %[t], %[k]\n\tv_cmp_ne_u32 vcc, %[k], %[m0]\n\ts_and_b64 s[8:9], s[8:9], vcc\n\ts_cbranch_scc0 2f\n"
                       "1:\n\ts_ff1_i32_b64 s7, s[8:9]\n\tv_readlane_b32 s12, %[k], s7\n\ts_lshl_b64 s[10:11], 1, s7\n\ts_nop 0\n\t"
                       "v_cmpx_gt_u32 vcc, s12, %[km]\n\tv_add_u32 %[rk], 8, %[rk]\n\ts_bcnt0_i32_b32 s7, vcc_lo\n\tv_cmpx_eq_u32 vcc, 0x80, %[rk]\n\t"
                       "v_mov_b32 %[km], s12\n\tv_lshlrev_b32 %[rk], 2, s7\n\ts_mov_b64 exec, -1\n\ts_andn2_b64 s[8:9], s[8:9], s[10:11]\n\ts_cbranch_scc1 1b\n2:"
                       : [km] "+v"(km), [rk] "+v"(rk) : [k] "v"(k), [m0] "v"(m), [t] "s"(thr) : "vcc", "scc", "s7", "s8", "s9", "s10", "s11", "s12");
        }
      }
      if (id < 126) {
        u32 a_, v_, n_ = SPIN;
#define RLD(o, s_) "buffer_load_dwordx4 %[v], %[rx], " s_ " offen offset:" #o " lds\n\t"
        asm volatile("v_add_u32 %[a], %[kb], %[rk]\n\tv_and_b32 %[v], 0x7ff, %[km]\n\ts_mov_b32 m0, %[fa]\n\tds_write_b32 %[a], %[v]\n\tds_read_b32 %[a], %[ka]\n\t"
                     "s_cmp_eq_u32 %[ns], 8\n\ts_waitcnt lgkmcnt(0)\n\tv_mad_i32_i24 %[v], %[a], %[cm], %[kc]\n\t"
                     RLD(0, "%[c3]") RLD(1024, "%[c2]") RLD(2048, "%[c1]") RLD(3072, "0")
                     "s_mov_b32 m0, %[fb]\n\tv_add_u32 %[v], 0x200, %[v]\n\tv_mad_i32_i24 %[a], %[a], -8, %[cs]\n\t"
                     "s_cbranch_scc0 3f\n\t" RLD(2048, "%[c1]") RLD(3072, "0")
                     "3:\n\tbuffer_load_dwordx2 v[160:161], %[a], %[rs], 0 offen sc1\n\t" RLD(0, "%[c3]") RLD(1024, "%[c2]") "buffer_load_dwordx2 v[158:159], %[co], %[rs], 0 offen sc1\n\t"
                     "v_mov_b32 %[v], 16 * " STR(HOLD) " - 112\n\tv_subrev_u32 %[ka], %[kd], %[ka]\n\ts_mov_b64 exec, 1\n\tds_add_u32 %[fc], %[v] offset:8\n\ts_mov_b64 exec, -1\n\tv_mov_b32 %[v], -1\n\t" TS(64, 65)
                     "s_sleep " STR(CSL) "\n\tbuffer_load_dwordx2 v[154:155], %[co], %[rs], 0 offen sc1\n\t"
                     "s_waitcnt vmcnt(4)\n1:\n\tv_cmp_ne_u32 vcc, %[t], v161\n\tv_and_b32 v156, 0xffff0000, v160\n\tv_lshlrev_b32 v157, 16, v160\n\tv_rcp_f32 v156, v156\n\t"
                     "s_sub_u32 %[n], %[n], 1\n\ts_cbranch_vccz 2f\n\ts_cbranch_scc1 2f\n\t"
                     "buffer_load_dwordx2 v[160:161], %[a], %[rs], 0 offen sc1\n\ts_waitcnt vmcnt(0)\n\ts_branch 1b\n2:\n\tv_mul_f32 v157, -v157, v156\n\t"
                     "ds_write_b64 %[ka], v[156:157]\n\ts_waitcnt vmcnt(2)\n\tds_write_b32 %[fc], %[v] offset:4\n\t" TS(66, 67)
                     "s_mov_b32 %[n], %[spin]\n\ts_waitcnt vmcnt(1)\n\tv_cmp_ne_u32 vcc, %[t], v159\n\ts_cbranch_vccz 5f\n"
                     "4:\n\ts_waitcnt vmcnt(0)\n\tv_cmp_ne_u32 vcc, %[t], v155\n\ts_sub_u32 %[n], %[n], 1\n\ts_cbranch_vccz 5f\n\t"
                     "buffer_load_dwordx2 v[154:155], %[co], %[rs], 0 offen sc1\n\ts_cbranch_scc0 4b\n5:"
                     : [a] "=&v"(a_), [v] "=&v"(v_), [ka] "+v"(ka), [n] "+s"(n_)
                     : [co] "v"(co), [rk] "v"(rk), [km] "v"(km), [kc] "v"(kc), [kb] "n"(FC + 384), [kd] "n"(384 - 32), [fa] "n"(FA), [fb] "n"(FA + 4096), [rx] "s"(rx), [rs] "s"(rs),
                       [c1] "s"(c1), [c2] "s"(c2), [c3] "s"(c3), [cm] "s"(cm), [cs] "s"(cs), [ns] "s"(ns), [fc] "v"(fa), [t] "s"(tag), [spin] "n"(SPIN) : "vcc", "scc", "memory" TSC);
        TSGET(12, 13, 14, 15);
        FLAG(FC + 28, 1u);
        STAMP(10);
      } else {
        const u32 cd = km << 1;
        const u32x2 r = {(2047u - (km & 0x7ffu)) | ((cd ^ ((u32)((int)~cd >> 15) | 0x80000000u)) & 0xffff0000u), tag};
        const u32 o = rk < 64u ? O_IDX + 1024 * mat + 128 * kg + rk : 0x80000000u;
        st8(rs, o, r);
        st8(rs, o + 512, r);
        TSGET(12, 13, 14, 15);
        STAMP(14);
        STAMP(15);
        if (mat) {
#pragma unroll 1
          for (int spin = 0; spin < SPIN; spin++) {
            const u32x4 a = ld16(rs, O_ARR + 16 * l);
            if (__all(a.y == tag && a.w == tag)) break;
            asm volatile("s_sleep 16" ::: "memory");
          }
          if (l == 0) __builtin_amdgcn_raw_buffer_store_b32(tag, rs, 0, 0, 16);
        }
      }
      FLUSH();
      __builtin_amdgcn_endpgm();
    }
    u32x4 xv[4];
    u32x8 wv;
    ROLE(l, "", , "={v[100:103]}"(xv[0]), "={v[104:107]}"(xv[1]), "={v[108:111]}"(xv[2]), "={v[112:115]}"(xv[3]), "={v[116:123]}"(wv));
    STAMP(11);
    u32 sg = 0x0c0c0b09u;
    asm volatile("" : "+s"(sg));
    ln_rows(xv, wv, rs, tag, b, g, l, lds0 + FC + 128, sg TLVAL);
    STAMP(10);
    IDS();
    if (id >= 126) {
      FLUSH();
      __builtin_amdgcn_endpgm();
    }
    const int wr = 16 * (kg >> 1) + 4 * (kg & 1) + (n16 & 8) + ((n16 >> 2) & 1);
    u32 wa = lds0 + 256 * wr + ((w ^ ((wr & 1) | ((wr >> 1) & 6))) << 5) + 8 * (l & 3), sel = (l & 8) ? 0xeeeeu : 0x4444u;
    u32 so = 4 * t;
    asm volatile("" : "+v"(wa), "+v"(sel), "+v"(so));
    asm volatile(UNTIL(STR(CLD)) :: [st] "s"(st0) : "s8", "s9", "scc");
    __builtin_amdgcn_raw_buffer_store_b32(~0u, rs, so, O_HG + (6 * cb + 3 * mat + part) * 2048, 16);
    st8(rs, u < 8 ? O_PG + (4 * kp + ct) * 4096 + 2 * so : 0x80000000u, u32x2{~0u, ~0u});
#define SWP(a0, a1, a2, a3) "v_permlane32_swap_b32 " a0 ", " a2 "\n\tv_permlane32_swap_b32 " a1 ", " a3 "\n\t"
#define OUT(a0, a1, o) "v_cvt_pk_bf16_f32 " a0 ", " a0 ", " a1 "\n\tv_min_u32 " a0 ", -2, " a0 "\n\tbuffer_store_dword " a0 ", %[so], %[rs], " o " sc1\n\t"
#define TR4(a, b_, c, d, e, f, g_, h, adr, o) "ds_read_b64_tr_b16 v[" #a ":" #b_ "], " adr " offset:" #o "\n\tds_read_b64_tr_b16 v[" #c ":" #d "], " adr " offset:" #o "+512\n\t" \
      "ds_read_b64_tr_b16 v[" #e ":" #f "], " adr " offset:" #o "+8192\n\tds_read_b64_tr_b16 v[" #g_ ":" #h "], " adr " offset:" #o "+8704\n\t"
#define SM(d, a, b_) "v_smfmac_f32_16x16x64_bf16 v[" d "], v[" a "], v[" b_ "], %[sel]\n\t"
#define USE(c, xa, wf) "s_add_u32 s6, s6, 16\n\ts_waitcnt lgkmcnt(" #c ")\n\t" SM("16:19", "20:23", wf) SM("8:11", xa, wf)
#define XR(xa, o) "ds_read_b128 v[" xa "], %[xa] offset:" #o "\n\t"
#define XU(c, xa, wf) "s_waitcnt lgkmcnt(" #c ")\n\t" SM("8:11", xa, wf)
#define ZERO(r) "v_mfma_f32_32x32x16_bf16 v[" r "], v[8:11], v[8:11], 0\n\t"
#define POLL(c, rd) "7:\n\tds_read_b32 v30, %[fl]\n\ts_waitcnt lgkmcnt(0)\n\ts_call_b64 s[10:11], 1f\n\ts_cbranch_scc1 7b\n\t" rd
#define CLR "s_waitcnt vmcnt(0)\n\ts_mov_b64 exec, 1\n\tds_add_rtn_u32 v14, %[fl], v28 offset:96\n\ts_waitcnt lgkmcnt(0)\n\tv_cmpx_eq_u32 vcc, 7, v14\n\t" \
      "buffer_store_dwordx2 v[12:13], off, %[rs], %[co] sc1\n\ts_mov_b64 exec, -1\n\t"
#define XE(k) "s_cmpk_eq_u32 s6, " #k "\n\ts_cbranch_scc1 9f\n\t"
    asm volatile("v_mov_b64 v[8:9], 0\n\tv_mov_b64 v[10:11], 0\n\tv_mov_b64 v[16:17], 0\n\tv_mov_b64 v[18:19], 0\n\t"
                 ZERO("96:111") ZERO("112:127")
                 "v_mov_b32 v20, 0x3f803f80\n\tv_mov_b32 v21, v20\n\tv_mov_b64 v[22:23], v[20:21]\n\tv_mov_b32 v13, %[t]\n\tv_mov_b32 v28, 1\n\tv_mov_b32 v30, 0\n\t"
                 "v_add_u32 v29, 0x10000, %[wa]\n\ts_movk_i32 s6, 96\n\ts_mov_b32 s7, 1\n\ts_mov_b32 s8, 15\n\ts_mov_b32 s9, %[spin]\n\ts_nop 7\n\t"
                 POLL(8, TR4(32, 33, 34, 35, 36, 37, 38, 39, "%[wa]", 0) TR4(40, 41, 42, 43, 44, 45, 46, 47, "%[wa]", 16384))
                 USE(4, "96:99", "32:39") "s_waitcnt lgkmcnt(0)\n\t" SM("16:19", "20:23", "40:47") SM("8:11", "100:103", "40:47")
                 CLR
                 POLL(4, TR4(48, 49, 50, 51, 52, 53, 54, 55, "%[wa]", 32768)) USE(0, "104:107", "48:55")
                 POLL(4, TR4(56, 57, 58, 59, 60, 61, 62, 63, "%[wa]", 49152)) "s_mov_b64 exec, 1\n\tds_add_u32 %[fl], v28 offset:20\n\ts_mov_b64 exec, -1\n\t" TS(60, 61)
                 USE(0, "108:111", "56:63")
                 POLL(4, TR4(64, 65, 66, 67, 68, 69, 70, 71, "v29", 0)) USE(0, "112:115", "64:71") "s_cmp_eq_u32 %[ns], 5\n\ts_cbranch_scc1 5f\n\t"
                 POLL(4, TR4(72, 73, 74, 75, 76, 77, 78, 79, "v29", 16384)) USE(0, "116:119", "72:79") "s_cmp_eq_u32 %[ns], 6\n\ts_cbranch_scc1 5f\n\t"
                 POLL(4, TR4(80, 81, 82, 83, 84, 85, 86, 87, "v29", 32768)) USE(0, "120:123", "80:87")
                 POLL(4, TR4(88, 89, 90, 91, 92, 93, 94, 95, "v29", 49152)) USE(0, "124:127", "88:95")
                 "5:\n\t" TS(62, 63) "s_cmp_eq_u32 s7, 0\n\ts_cbranch_scc1 4f\n\ts_mov_b32 s8, 16\n\t"
                 "s_getpc_b64 s[10:11]\n\tds_read_b32 v30, %[fl]\n\ts_waitcnt lgkmcnt(0)\n"

                 "1:\n\tv_cmp_le_u32 vcc, s6, v30\n\ts_and_b32 s12, vcc_hi, s7\n\ts_cbranch_scc1 3f\n"
                 "2:\n\ts_andn2_b32 s12, s8, vcc_lo\n\ts_cbranch_scc1 6f\n\ts_setpc_b64 s[10:11]\n"
                 "6:\n\ts_sub_u32 s9, s9, 1\n\ts_cbranch_scc1 4f\n\t" GNAP "\n\ts_cmp_eq_u32 s6, s6\n\ts_setpc_b64 s[10:11]\n"
                 "3:\n\t"
                 XR("96:99", 0) XR("100:103", 1024) XR("104:107", 2048) XR("108:111", 3072) XR("112:115", 4096) XR("116:119", 5120) XR("120:123", 6144) XR("124:127", 7168)
                 "ds_read_b128 v[24:27], %[pa]\n\ts_mov_b32 s7, 0\n\t"
                 TS(64, 65)
                 XE(96) XU(8, "96:99", "32:39") XU(7, "100:103", "40:47") XE(112) XU(6, "104:107", "48:55") XE(128) XU(5, "108:111", "56:63") XE(144)
                 XU(4, "112:115", "64:71") XE(160) XU(3, "116:119", "72:79") XE(176) XU(2, "120:123", "80:87") XE(192) XU(1, "124:127", "88:95")
                 "9:\n\ts_cmp_eq_u32 s8, 16\n\ts_waitcnt lgkmcnt(0)\n\ts_cbranch_scc0 2b\n"
                 "4:\n\ts_nop 4\n\tv_permlane32_swap_b32 v16, v17\n\tv_add_f32 v16, v16, v17\n\tv_mul_f32 v25, v25, v16\n\tv_mul_f32 v27, v27, v16\n\t"
                 SWP("v8", "v9", "v10", "v11") "v_fmac_f32 v25, v10, v24\n\tv_fmac_f32 v27, v11, v26\n\tv_fma_f32 v8, v8, v24, v25\n\tv_fma_f32 v9, v9, v26, v27\n\t"
                 OUT("v8", "v9", "%[ho] offen") TS(66, 67)
                 :: [wa] "v"(wa), [sel] "v"(sel), [so] "v"(so), [fl] "v"(lds0 + FC + 4 * min(l, 4)), [xa] "v"(lds0 + FA + 128 * (l & 7) + 16 * ((2 * kg + (n16 >> 3)) ^ (2 * l & 4))),
                    [pa] "v"(lds0 + FC + 32 + 32 * (kg & 1) + 16 * (kg >> 1)), [ns] "s"(ns), [t] "s"(tag), [co] "s"(O_CLR + 8 * id), [rs] "s"(rs), [ho] "s"(O_HG + (6 * cb + 3 * mat + part) * 2048), [spin] "n"(SPIN)
                 : "vcc", "scc", "s6", "s7", "s8", "s9", "s10", "s11", "s12", "s13", "memory", G_CLOBBER TSC);
    TSGET(1, 4, 3, 5);
    if (u < 8) {
      const int tp = 2 * (kg & 1) + (kg >> 1), r7 = 4 * (kg & 1) + (n16 >> 2);
      u32 o3 = (kp & 3) == 1 ? so + 4096 : 0x80000000u, h0 = O_HG + kp * 12288, h1 = h0 + 6144;
      u32 hw = lds0 + FH + 2 * tp * HP + 32 * w + 8 * (l & 2) + (l & 4) * 2 + ((l & 8) >> 1) + 2 * (l & 1);
      u32 hr = lds0 + FH + (l & 7) * HP + 32 * kg + 2 * (l & 8);
      u32 da = lds0 + 512 * (16 * (kg >> 1) + r7) + ((2 * w ^ r7) << 5) + 8 * (l & 3), db = da ^ 32, fl = lds0 + FC + 4 * (l & 3);
      u32 po = 512 * w + 16 * (l & 8) + 32 * tp + 4 * (l & 7);
      u32 e0 = 0x3f80u, e1 = 0x3f800000u;
      asm volatile("" : "+v"(hw), "+v"(hr), "+v"(da), "+v"(db), "+v"(fl), "+v"(po), "+v"(o3));
      asm volatile("" : "+s"(e0), "+s"(e1), "+s"(h0), "+s"(h1));
#define TRD(a, b_, c, d, e, f, g_, h, adr, o) "ds_read_b64_tr_b16 v[" #a ":" #b_ "], " adr " offset:" #o "\n\tds_read_b64_tr_b16 v[" #c ":" #d "], " adr " offset:" #o "+4096\n\t" \
      "ds_read_b64_tr_b16 v[" #e ":" #f "], " adr " offset:" #o "+16384\n\tds_read_b64_tr_b16 v[" #g_ ":" #h "], " adr " offset:" #o "+20480\n\t"
#define HLD(X, r) X(r "0", "%[s1]", "%[h0] offen") X(r "3", "%[s1]", "%[h1] offen") X(r "1", "%[s1]", "%[h0] offen offset:2048") X(r "4", "%[s1]", "%[h1] offen offset:2048") \
      X(r "2", "%[o3]", "%[h0] offen") X(r "5", "%[o3]", "%[h1] offen")
#define HL0(a, o, s_) "buffer_load_dword " a ", " o ", %[rs], " s_ " sc1\n\t"
#define HLF(a, o, s_) "buffer_load_dword " a ", " o ", %[rs], " s_ SCH "\n\t"
#define HL1(a, o, s_) "v_cmpx_eq_u32 vcc, -1, " a "\n\t" HL0(a, o, s_) "s_mov_b64 exec, -1\n\t"
#define DOT(d, e, a) "v_dot2c_f32_bf16 " d ", %[" e "], " a "\n\t"
#define HCK(c, r) "s_cmp_eq_u32 s7, 0\n\ts_cbranch_scc1 5f\n\ts_call_b64 s[10:11], 4f\n5:\n\ts_waitcnt vmcnt(" #c ")\n\tv_max3_u32 v28, " r "0, " r "1, " r "2\n\tv_lshlrev_b32 v24, 16, " r "0\n\tv_max3_u32 v28, v28, " r "3, " r "4\n\t" \
      "v_and_b32 v25, %[hm], " r "0\n\tv_max_u32 v28, v28, " r "5\n\t" DOT("v24", "e0", r "1") "v_cmp_eq_u32 vcc, -1, v28\n\t" DOT("v25", "e1", r "1") DOT("v24", "e0", r "2") DOT("v25", "e1", r "2") \
      "v_lshlrev_b32 v26, 16, " r "3\n\tv_and_b32 v27, %[hm], " r "3\n\t" DOT("v26", "e0", r "4") DOT("v27", "e1", r "4") DOT("v26", "e0", r "5") DOT("v27", "e1", r "5")
#define RB2(a, b_) "v_cvt_pk_bf16_f32 " a ", 0, " a "\n\tv_cvt_pk_bf16_f32 " b_ ", 0, " b_ "\n\t"
      asm volatile("s_mov_b32 s7, 1\n\ts_mov_b32 s12, %[spin]\n\tv_mov_b64 v[8:9], 0\n\tv_mov_b64 v[10:11], 0\n\tv_mov_b64 v[12:13], 0\n\tv_mov_b64 v[14:15], 0\n"
                   "2:\n\ts_sleep 1\n\tds_read_b32 v28, %[fl] offset:16\n\ts_memrealtime s[8:9]\n\ts_waitcnt lgkmcnt(0)\n\tv_cmp_eq_u32 vcc, 0, v28\n\ts_sub_u32 s8, s8, %[st]\n\t"
                   "s_cmp_lt_u32 s8, %[th]\n\ts_cbranch_scc1 2b\n\ts_bitcmp1_b32 vcc_lo, 3\n\ts_cbranch_scc1 2b\n\t" TS(64, 65)
                   HLD(HLF, "v7") HCK(0, "v7") "s_cbranch_vccnz 9f\n"
                   "3:\n\t" TS(66, 67) RB2("v24", "v25") "v_mul_f32 v28, %[le], v24\n\tv_mul_f32 v29, %[le], v25\n\tv_exp_f32 v28, v28\n\tv_exp_f32 v29, v29\n\t" RB2("v26", "v27")
                   "v_add_f32 v28, 1.0, v28\n\tv_add_f32 v29, 1.0, v29\n\tv_rcp_f32 v28, v28\n\tv_rcp_f32 v29, v29\n\ts_cmp_eq_u32 s7, 0\n\tv_mul_f32 v24, v24, v28\n\tv_mul_f32 v25, v25, v29\n\t"
                   RB2("v24", "v25") "v_mul_f32 v24, v24, v26\n\tv_mul_f32 v25, v25, v27\n\tv_cvt_pk_bf16_f32 v24, v24, v25\n\ts_mov_b32 s12, %[spin]\n\t"
                   "ds_write_b16 %[hw], v24\n\tds_write_b16_d16_hi %[hw], v24 offset:%[hp]\n\ts_cbranch_scc1 8f\n"
                   "7:\n\ts_call_b64 s[10:11], 4f\n\ts_mul_i32 s12, s12, s7\n\ts_sub_u32 s12, s12, 1\n\ts_cbranch_scc0 7b\n"
                   "8:\n\ts_waitcnt lgkmcnt(0)\n\ts_barrier\n\t" TS(60, 61)
                   XR("16:19", 0) XR("20:23", 128)
                   XU(1, "16:19", "32:39") SM("12:15", "16:19", "40:47") XU(0, "20:23", "48:55") SM("12:15", "20:23", "56:63") "s_nop 6\n\t"
                   SWP("v8", "v9", "v10", "v11") "v_add_f32 v8, v8, v10\n\tv_add_f32 v9, v9, v11\n\t" SWP("v12", "v13", "v14", "v15") OUT("v8", "v9", "%[po] offen")
                   "v_add_f32 v12, v12, v14\n\tv_add_f32 v13, v13, v15\n\t" OUT("v12", "v13", "%[po] offen offset:256") TS(62, 63) DONE
                   "9:\n\tv_mov_b64 v[80:81], v[70:71]\n\tv_mov_b64 v[82:83], v[72:73]\n\tv_mov_b64 v[84:85], v[74:75]\n\t" HLD(HL1, "v7") "s_sleep " STR(HNAP) "\n"
                   "1:\n\t" HLD(HL1, "v8") HCK(6, "v7") "s_cbranch_vccz 3b\n\t" HLD(HL1, "v7") HCK(6, "v8") "s_sub_u32 s12, s12, 1\n\ts_cbranch_vccz 3b\n\ts_cbranch_scc0 1b\n\ts_branch 3b\n"
                   "4:\n\tds_read_b32 v28, %[fl]\n\ts_waitcnt lgkmcnt(0)\n\tv_cmp_ne_u32 vcc, -1, v28\n\ts_cbranch_vccnz 6f\n\t"
                   TRD(32, 33, 34, 35, 36, 37, 38, 39, "%[da]", 0) TRD(40, 41, 42, 43, 44, 45, 46, 47, "%[db]", 0)
                   TRD(48, 49, 50, 51, 52, 53, 54, 55, "%[da]", 32768) TRD(56, 57, 58, 59, 60, 61, 62, 63, "%[db]", 32768) "s_mov_b32 s7, 0\n"
                   "6:\n\ts_setpc_b64 s[10:11]\n0:"
                   :: [s1] "v"(so), [o3] "v"(o3), [fl] "v"(fl), [da] "v"(da), [db] "v"(db), [hw] "v"(hw), [xa] "v"(hr), [sel] "v"(sel), [so] "v"(po), [rs] "s"(rs), [h0] "s"(h0), [h1] "s"(h1),
                      [st] "s"(st0), [th] "n"(TH), [spin] "n"(SPIN), [hp] "n"(HP), [po] "s"(O_PG + (4 * kp + ct) * 4096), [e0] "s"(e0), [e1] "s"(e1), [hm] "s"(0xffff0000u),
                      [le] "s"(-1.4426950408889634f)
                   : "vcc", "scc", "s7", "s8", "s9", "s10", "s11", "s12", "memory", G_CLOBBER TSC);
      TSGET(8, 9, 6, 7);
    }
    FLUSH();
    __builtin_amdgcn_endpgm();
  }

  if (w0 >= 8) {
    u32x8 xa0, xa1, xa2, xa3, sa1;
    u32x4 junk;
    u32 op, so;
    ROLE(p, "", , "={v[100:107]}"(xa0), "={v[108:115]}"(xa1), "={v[116:123]}"(xa2),
         "={v148}"(op), "={v149}"(so), "={v[160:163]}"(junk));
    const int p = w - 8;
    __builtin_amdgcn_s_setprio(3);
    asm volatile("s_waitcnt lgkmcnt(0)" : "+{s44}"(tag)::"memory");
    tag += 1u;
    STAMP(9);
    f32x2 xq[4][8];
    u32x8 pa;
    {
#define SCK(r1, r3, r5, r7) "v_xor_b32 %[d], %[t], v" #r1 "\n\tv_bitop3_b32 %[d], %[d], %[t], v" #r3 " bitop3:0xf6\n\t" \
      "v_bitop3_b32 %[d], %[d], %[t], v" #r5 " bitop3:0xf6\n\tv_bitop3_b32 %[d], %[d], %[t], v" #r7 " bitop3:0xf6\n\t"
#define SLX(r0, r3, r4, r7, k, k1, sc) "buffer_load_dwordx4 v[" #r0 ":" #r3 "], %[so], %[rs], " k " offen" sc "\n\tbuffer_load_dwordx4 v[" #r4 ":" #r7 "], %[so], %[rs], " k1 " offen" sc "\n\t"
#define SLD(r0, r3, r4, r7, k, k1) SLX(r0, r3, r4, r7, k, k1, " sc1")
#define SLOOP(r0, r1, r3, r4, r5, r7, k, k1) "1:\n\ts_waitcnt vmcnt(0)\n4:\n\t" SCK(r1, r3, r5, r7) "s_sub_u32 %[n], %[n], 1\n\tv_cmp_ne_u32 vcc, 0, %[d]\n\ts_cbranch_scc1 2f\n\ts_cbranch_vccz 2f\n\t" \
      SLD(r0, r3, r4, r7, k, k1) "s_branch 1b\n2:\n\t"
      u32 d_, n_ = SPIN, k1 = 4096, k2 = 8192, k3 = 12288, s0[4];
      asm volatile("s_mov_b32 m0, %[m]\n\ts_cmp_eq_u32 %[k], 3\n\ts_waitcnt vmcnt(2)\n\ts_cbranch_scc0 1f\n\ts_waitcnt vmcnt(0)\n1:"
                   : "+{v[100:107]}"(xa0), "+{v[108:115]}"(xa1), "+{v[160:163]}"(junk) : [m] "s"(lds0 + OX + 2 * XO + 256 * p), [k] "s"(xk) : "scc", "memory");
      SB();
#define UNPACK(i, xs) _Pragma("unroll") for (int c = 0; c < 8; c++) { \
        xq[i][c] = (c & 1) ? f32x2{hi2f(xs[c >> 1]), hi2f(xs[4 + (c >> 1)])} : f32x2{lo2f(xs[c >> 1]), lo2f(xs[4 + (c >> 1)])}; asm volatile("" : "+v"(xq[i][c].x), "+v"(xq[i][c].y)); }
      UNPACK(0, xa0);
      UNPACK(1, xa1);
      u32 fa = lds0 + OXF + 4 * p, fv = 1;
      asm volatile("" : "+v"(fa), "+v"(fv));
      asm volatile("" : "+s"(k1), "+s"(k2), "+s"(k3), "+s"(n_));
      SB();
      STAMP(1);
#define SCP(r0, r1, r2, r3, r4, r5, r6, r7) "s_waitcnt vmcnt(2)\n\t" SCK(r1, r3, r5, r7) "v_cmp_ne_u32 vcc, 0, %[d]\n\tv_mov_b32 %[a0], v" #r0 "\n\tv_mov_b32 %[a1], v" #r2 "\n\t" \
      "v_mov_b32 %[a2], v" #r4 "\n\tv_mov_b32 %[a3], v" #r6 "\n\ts_cbranch_vccz 3f\n\t" SLD(r0, r3, r4, r7, "0", "%[k1]")
      asm volatile(UNTIL(STR(TSG)) SLX(132, 135, 136, 139, "0", "%[k1]", SCA) "s_sleep " STR(SSL) "\n\t" SLX(140, 143, 144, 147, "0", "%[k1]", SCC)
                   "1:\n\t" SCP(132, 133, 134, 135, 136, 137, 138, 139) SCP(140, 141, 142, 143, 144, 145, 146, 147) "s_sub_u32 %[n], %[n], 1\n\ts_cbranch_scc0 1b\n"
                   "3:\n\t" SLX(140, 143, 144, 147, "%[k2]", "%[k3]", SCB)
                   : [d] "=&v"(d_), [n] "+s"(n_), [a0] "=&v"(s0[0]), [a1] "=&v"(s0[1]), [a2] "=&v"(s0[2]), [a3] "=&v"(s0[3]), "=&{v[132:139]}"(pa), "=&{v[140:147]}"(sa1)
                   : [st] "s"(st0), [t] "s"(tag), [so] "v"(so), [rs] "s"(rs), [k1] "s"(k1), [k2] "s"(k2), [k3] "s"(k3) : "vcc", "scc", "s8", "s9", "memory");
#pragma unroll
      for (int i = 0; i < 5; i++) {
#define XLD(r0, r1, r2, r3, o_) "v_add_u32 %[d], " #o_ ", %[o]\n\tglobal_load_dwordx4 v[" #r0 ":" #r1 "], %[d], %[x]\n\tglobal_load_dwordx4 v[" #r2 ":" #r3 "], %[d], %[x] offset:2048\n"
        if (i == XI) asm volatile("s_cmp_eq_u32 %[k], 3\n\ts_cbranch_scc0 1f\n\t" XLD(116, 119, 120, 123, 0x200000) "1:\n\t" XLD(124, 127, 128, 131, 0x300000)
                                 : [d] "=&v"(d_), "+{v[116:123]}"(xa2), "=&{v[124:131]}"(xa3) : [k] "s"(xk), [o] "v"(op), [x] "s"(x) : "scc", "memory");
        if (i == 2) {
          asm volatile("s_waitcnt vmcnt(2)\n\ts_branch 4f\n" SLOOP(140, 141, 143, 144, 145, 147, "%[k2]", "%[k3]") : [d] "=&v"(d_), [n] "+s"(n_), "+{v[140:147]}"(sa1), "+{v[116:123]}"(xa2), "+{v[132:139]}"(pa)
                       : [t] "s"(tag), [so] "v"(so), [rs] "s"(rs), [k2] "s"(k2), [k3] "s"(k3) : "vcc", "scc", "memory");
          UNPACK(2, xa2);
        }
        if (i == 3) asm volatile("s_waitcnt vmcnt(%1)" : "+{v[124:131]}"(xa3) : "n"(DPI == 2 ? DPF : 0));
        if (i == DPI && DPF) {
          u32 lx = l;
          asm volatile("" : "+v"(lx));
          const u32 ti = min(16 * xk + 4 * p + (lx >> 4), 125u), tsb = __umul24(ti, 57) >> 10, tu = min(ti - __umul24(18, tsb), 15u);
          const u32 o = (128 * (4 * tsb + (tu >> 2)) + DPO / 2 + 4 * j + ((lx >> 2) & 3)) * (2 * D) + 512 * (tu & 3) + 128 * (lx & 3);
          if (DPI == 4) AWAIT(OXF + 4 * (l & 3), 3, "s_sleep 1");
          if (DPF == 1) asm volatile("global_load_dword v160, %1, %2" : "=&{v[160:163]}"(junk) : "v"(o), "s"(wd) : "memory");
          else asm volatile("global_load_dword v160, %1, %2\n\tglobal_load_dword v161, %1, %3" : "=&{v[160:163]}"(junk) : "v"(o), "s"(wd), "s"(wd + 64 * D) : "memory");
        }
        if (i == 4) break;
        if (i == 3) UNPACK(3, xa3);
        const u32 sa = i < 2 ? s0[2 * i] : sa1[4 * i - 8], sb = i < 2 ? s0[2 * i + 1] : sa1[4 * i - 6];
        if (i == 0) STAMP(3);
        if (i == 2) STAMP(10);
        const f32x2 r2 = {__builtin_amdgcn_rcpf(hi2f(sa)), __builtin_amdgcn_rcpf(hi2f(sb))};
        const f32x2 m2 = {-lo2f(sa) * r2.x, -lo2f(sb) * r2.y};
        f32x2 n2[8];
        u32 o[8];
        SB();
#pragma unroll
        for (int c = 0; c < 8; c++) n2[c] = f32x2{fmav(xq[i][c].x, r2.x, m2.x), fmav(xq[i][c].y, r2.y, m2.y)};
        SB();
#pragma unroll
        for (int c = 0; c < 8; c++) o[c] = packh(n2[c].x, n2[c].y);
        SB();
#pragma unroll
        for (int c = 0; c < 8; c++) asm volatile("ds_write_addtid_b32 %0 offset:%1" ::"v"(o[c]), "n"(c * XREG + 1024 * i) : "memory");
        asm volatile("ds_write_b32 %1, %0\n\tv_add_u32 %0, 1, %0" : "+v"(fv) : "v"(fa) : "memory");
        if (i == PDROP) __builtin_amdgcn_s_setprio(PPRIO);
        SB();
      }
    }
    STAMP(4);
    const rsrc_t ro = __builtin_amdgcn_make_buffer_rsrc((void*)out, 0, 2 * L * D, 0x00020000);
    u32 ys[8], y[2][8] = {};
    u32x2 ix;
    u32x4 cl;
    u32x8 pb;
    float yc, rsel;
    const u32 ixo = IXO((l & 6) + (n16 >> 3));
    if (p == 0) asm volatile("buffer_load_dwordx2 v[4:5], %1, %2, 0 offen sc1" : "=&{v[4:5]}"(ix) : "v"(ixo), "s"(rs) : "memory");
    const int rc = 4 * (l & 1) + kg, u = 64 * (p & 1) + l;
    u32x4 pick;
    u32 po = O_PG + 128 * g + 16 * (l & 7) + 16384 * (l >> 3), p3 = l < 32 ? po + 3 * 131072 : 0x80000000u, yf = lds0 + OYP + 4 * min(n16, 12) + 64 * (p >> 1);
    u32 ya = lds0 + OY + CHR * rc, oo = 16 * g + 2 * rc, clo = O_CLR + 16 * min(l, 62);
    u32 yp = lds0 + OY + 512 * (p >> 1) + 4 * (u ^ ((u >> 4) & 6)), yq = yp + 4 * CHR, bp = lds0 + OMISC + 16 * p + 4 * kg, bs = 30 - 2 * n16;
#pragma unroll
    for (int q = 0; q < 4; q++) pick[q] = (n16 >> 2) == q ? 0x3f80u << 16 * (l & 1) : 0u;
    asm volatile("" : "+v"(pick), "+v"(ya), "+v"(oo), "+v"(po), "+v"(p3), "+v"(yf), "+v"(clo), "+v"(yp), "+v"(yq), "+v"(bp), "+v"(bs));
#pragma unroll
    for (int c = 0; c < 8; c++) asm volatile("" : "+v"(y[0][c]), "+v"(y[1][c]));
    if (TE) asm volatile(UNTIL(STR(TE)) :: [st] "s"(st0) : "s8", "s9", "scc");
#pragma unroll
    for (int i = 0; i < 4; i++) {
      if (i == IXI && p == 0) {
        u32 n_ = SPIN;
        asm volatile("s_waitcnt vmcnt(%5)\n1:\n\tv_cmp_ne_u32 vcc, %4, v5\n\ts_sub_u32 %1, %1, 1\n\ts_cbranch_vccz 2f\n\ts_cbranch_scc1 2f\n\t"
                     "buffer_load_dwordx2 v[4:5], %2, %3, 0 offen sc1\n\ts_waitcnt vmcnt(0)\n\ts_branch 1b\n2:"
                     : "+{v[4:5]}"(ix), "+s"(n_) : "v"(ixo), "s"(rs), "s"(tag), "n"(2 * IXI + 1) : "vcc", "scc", "memory");
        const u32 prow = ix.x & 2047u, v = prow >> 1;
        if (l < 16) atomicOr((u32*)(smem + OMISC) + (prow >> 5), 1u << (ix.x & 31u));
        FLAG(OYP + 256 + 32 + 64 * (l & 3), 8u);
        oo += prow * (2 * D);
        asm volatile("v_mov_b32 v162, 0\n\tglobal_load_short_d16_hi v162, %1, %2" : "+{v[160:163]}"(junk) : "v"(oo), "s"(x) : "memory");
        yc = *(const float*)(smem + OSC + 4 * rc); rsel = rbt(sigm(hi2f(ix.x)));
        ya += 4 * (v ^ ((v >> 4) & 6)) + 2 * (prow & 1);
        asm volatile("" : "+v"(yc), "+v"(rsel), "+v"(ya));
        STAMP(11);
      }
      if (i == 2 && p == 0) cl = ld16(rs, clo);
      if (i == 3) {
        asm volatile("s_waitcnt vmcnt(2)" ::: "memory");
        FLAG(OYP + 448 + 36 + 4 * p, 8u);
        if (p == 0) {
#pragma unroll 1
          for (int spin = 0; spin < SPIN; spin++) {
            if (__all(cl.y == tag && cl.w == tag)) break;
            cl = ld16(rs, clo);
          }
          STAMP(12);
        }
        __builtin_amdgcn_s_setprio(LPRIO);
      }
      {
        u32 n_ = SPIN, f_;
        asm volatile("1:\n\tds_read_b32 %0, %2 offset:%3\n\ts_sub_u32 %1, %1, 1\n\ts_waitcnt lgkmcnt(0)\n\tv_cmp_eq_u32 vcc, 0, %0\n\ts_cbranch_scc1 2f\n\ts_and_b32 vcc_lo, vcc_lo, %4\n\t"
                     "s_cbranch_scc0 2f\n\t" YNAP "\n\ts_branch 1b\n2:" : "=&v"(f_), "+s"(n_) : "v"(yf), "n"(128 * i), "n"(i < 2 ? 255 : 511) : "vcc", "scc", "memory");
      }
      if (i == 0) STAMP(5); else if (i == 1) STAMP(6); else if (i == 2) STAMP(7); else STAMP(8);
      u32 bm = 0;
      if (i >= 2) asm volatile("ds_read_b32 %0, %1 offset:%2" : "=v"(bm) : "v"(bp), "n"(64 * i) : "memory");
#pragma unroll
      for (int r = 0; r < 2; r++)
#pragma unroll
        for (int c = 0; c < 8; c++) asm volatile("ds_read_u16_d16_hi %0, %1 offset:%2" : "+v"(y[r][c]) : "v"(c < 4 ? yp : yq), "n"((c & 3) * CHR + 1024 * i + 2 * r) : "memory");
      if (i == 3 && p == 0) {
#define PLD(X) X(132, 133, 134, 135, "%[po]", "0") X(136, 137, 138, 139, "%[po]", "%[k1]") X(140, 141, 142, 143, "%[po]", "%[k2]") X(144, 145, 146, 147, "%[p3]", "0")
#define PL0(a, b_, c, d, o, s_) "buffer_load_dwordx4 v[" #a ":" #d "], " o ", %[rs], " s_ " offen sc1\n\t"
        asm volatile(PLD(PL0) : "=&{v[132:139]}"(pa), "=&{v[140:147]}"(pb) : [po] "v"(po), [p3] "v"(p3), [rs] "s"(rs), [k1] "s"(131072), [k2] "s"(262144) : "memory");
      }
      if (i == 0) {
        st8(rs, t == 512 ? O_ARR + b * 8 : 0x80000000u, u32x2{0u, tag});
        const u32x4 a = *(const u32x4*)(smem + OSC), c = *(const u32x4*)(smem + OSC + 16);
#pragma unroll
        for (int k = 0; k < 4; k++) { ys[k] = (u32)__builtin_amdgcn_readfirstlane((int)a[k]); ys[4 + k] = (u32)__builtin_amdgcn_readfirstlane((int)c[k]); }
      }
      SB();
#pragma unroll
      for (int r = 0; r < 2; r++) {
        asm volatile("s_waitcnt lgkmcnt(%9)" : "+v"(y[r][0]), "+v"(y[r][1]), "+v"(y[r][2]), "+v"(y[r][3]), "+v"(y[r][4]), "+v"(y[r][5]), "+v"(y[r][6]), "+v"(y[r][7]), "+v"(bm) : "n"(8 - 8 * r));
        u32x4 v;
#pragma unroll
        for (int k = 0; k < 4; k++)
          v[k] = pack2(fmas(__uint_as_float(y[r][2 * k]), ys[2 * k], xq[i][2 * k][r]), fmas(__uint_as_float(y[r][2 * k + 1]), ys[2 * k + 1], xq[i][2 * k + 1][r]));
        if (i >= 2 && r == 0) bm <<= bs;
        __builtin_amdgcn_raw_buffer_store_b128(v, ro, i < 2 ? op : op | (bm & 0x40000000u << r), (512 * i + r) * (2 * D), OAUX);
        SB();
        asm volatile("s_nop 1");
        SB();
      }
    }
    asm volatile("" ::"{v[160:163]}"(junk));
    STAMP(13);
    if (p == 0) {
      u32 n_ = SPIN;
#define PL1(a, b_, c, d, o, s_) "v_max3_u32 v150, v" #a ", v" #b_ ", v" #c "\n\tv_max_u32 v150, v150, v" #d "\n\tv_cmpx_eq_u32 vcc, -1, v150\n\t" PL0(a, b_, c, d, o, s_) "s_mov_b64 exec, -1\n\t"
#define PLE(X) X(112, 113, 114, 115, "%[po]", "0") X(116, 117, 118, 119, "%[po]", "%[k1]") X(120, 121, 122, 123, "%[po]", "%[k2]") X(124, 125, 126, 127, "%[p3]", "0")
#define PMX(a, b_) "v_max3_u32 v150, v150, v" a ", v" b_ "\n\t"
#define PMM(d, a, c) "v_mfma_f32_16x16x32_bf16 v[" d "], %[pk], v[" a "], " c "\n\t"
#define PCK(c, p, q) "s_waitcnt vmcnt(" #c ")\n\t" PMM("152:155", p "2:" p "5", "0") "v_max_u32 v150, v" p "2, v" p "3\n\t" PMX(p "4", p "5") PMM("156:159", p "6:" p "9", "0") PMX(p "6", p "7") PMX(p "8", p "9") \
      PMM("152:155", q "0:" q "3", "v[152:155]") PMX(q "0", q "1") PMX(q "2", q "3") PMM("156:159", q "4:" q "7", "v[156:159]") PMX(q "4", q "5") PMX(q "6", q "7") \
      "v_cmp_eq_u32 vcc, -1, v150\n\ts_sub_u32 %[n], %[n], 1\n\t"
#define PCP(a, b_, c, d) "v_mov_b64 v[" #a ":" #b_ "], v[" #c ":" #d "]\n\t"
      asm volatile(PCK(2, "13", "14") "s_cbranch_vccnz 9f\n"
                   "2:\n\t" TS(60, 61) "ds_read_b32 v150, %[yf] offset:448\n\tds_read_u16_d16_hi v151, %[ya]\n\ts_mov_b32 %[n], %[spin]\n\ts_nop 1\n\tv_add_f32 v152, v152, v156\n\tv_add_f32 v153, v153, v157\n\ts_nop 0\n\t"
                   "v_add_f32_dpp v152, v152, v152 row_ror:8 row_mask:0xf bank_mask:0x3\n\tv_add_f32_dpp v152, v153, v153 row_ror:8 row_mask:0xf bank_mask:0xc\n\t"
                   "v_cvt_pk_bf16_f32 v152, 0, v152\n\tv_mul_f32 v152, v152, %[rl]\n\tv_cvt_pk_bf16_f32 v152, 0, v152\n\tv_add_f32 v153, v162, v152\n"
                   "3:\n\ts_waitcnt lgkmcnt(0)\n\tv_cmp_eq_u32 vcc, 0, v150\n\tv_and_b32 v152, 0xffff0000, v151\n\ts_sub_u32 %[n], %[n], 1\n\ts_cbranch_vccz 4f\n\ts_cbranch_scc1 4f\n\t"
                   "ds_read_b32 v150, %[yf] offset:448\n\tds_read_u16_d16_hi v151, %[ya]\n\ts_branch 3b\n"
                   "4:\n\tv_fmac_f32 v153, v152, %[yc]\n\tv_cvt_pk_bf16_f32 v153, v153, v153\n\tbuffer_store_short v153, %[oo], %[ro], 0 offen\n\t" DONE
                   "9:\n\t" PCP(112, 113, 132, 133) PCP(114, 115, 134, 135) PCP(116, 117, 136, 137) PCP(118, 119, 138, 139) PCP(120, 121, 140, 141) PCP(122, 123, 142, 143)
                   PCP(124, 125, 144, 145) PCP(126, 127, 146, 147) PLD(PL1) "s_sleep " STR(PNAP) "\n1:\n\t" PLE(PL1) PCK(4, "13", "14") "s_cbranch_vccz 2b\n\t" PLD(PL1) PCK(4, "11", "12")
                   "s_cbranch_vccz 2b\n\ts_cbranch_scc0 1b\n\ts_branch 2b\n0:"
                   : [n] "+s"(n_), "+{v[132:139]}"(pa), "+{v[140:147]}"(pb)
                   : [po] "v"(po), [p3] "v"(p3), [rs] "s"(rs), [k1] "s"(131072), [k2] "s"(262144), [pk] "v"(pick), [yf] "v"(yf), [ya] "v"(ya), "{v[160:163]}"(junk), [rl] "v"(rsel), [yc] "v"(yc),
                     [oo] "v"(oo), [ro] "s"(ro), [spin] "n"(SPIN)
                   : "vcc", "scc", "memory", "v112", "v113", "v114", "v115", "v116", "v117", "v118", "v119", "v120", "v121", "v122", "v123", "v124", "v125", "v126", "v127", "v150", "v151", "v152", "v153", "v154", "v155", "v156", "v157", "v158", "v159" TSD);
      TSGET(14, 2, 2, 2);
      STAMP(15);
    }
    FLUSH();
    __builtin_amdgcn_endpgm();
  }

  u32 pl, pa, pr, pi, ldtv;
  u32x4 tv, fx, junk;
  ROLE(c, "", , "={v[16:19]}"(tv), "={v[20:23]}"(fx), "={v24}"(ldtv), "={v96}"(pl), "={v97}"(pa), "={v68}"(pr), "={v69}"(pi), "={v[160:163]}"(junk));
  char* W = smem + w * CHR;
  __builtin_amdgcn_s_setprio(2);
  STAMP(3);
  const u32x4 zero4 = {0u, 0u, 0u, 0u};
#pragma unroll
  for (int i = 0; i < 4; i++) *(u32x4*)(W + (i < 3 ? TB + 1024 * i : CHR - 1024) + 16 * l) = zero4;
  SB();
  const u32 wb0 = lds0 + w * CHR, xb0 = lds0 + OX + w * XREG;
  asm volatile("s_mov_b32 m0, %0\n\ts_nop 0\n\tds_write_addtid_b32 %1\n\tds_write_addtid_b32 %1 offset:%2" ::"s"(wb0), "v"(0x3c00u), "n"(PB) : "memory");
  u32 ape = wb0 + (n16 == 12 ? 12 : (n16 ^ 3) - 7 * (n16 >> 3)) * RS + 16 * kg, apo = wb0 + (0x765412b0fedc2301ull >> 4 * n16 & 15) * RS + 16 * kg;
  u32x2 kb = {wb0 + ((15 - n16) & 3) * CPE + 2 * ((15 - n16) & 12) + 16 * kg, wb0 + (((n16 >> 2) & 1) + (n16 & 8) + 2 * (3 - (n16 & 3))) * RS + 16 * kg};
  u32x8 ka = {xb0 + 32 * n16 + 16 * kg, wb0 + OY + 32 * n16 + 8 * (kg ^ ((l >> 2) & 3)),
              lds0 + OXF + 4 * (l & 3), wb0 + TB + ((15 - n16) & 3) * CPO + 2 * ((15 - n16) & 12) + 8 * kg, lds0 + OB + 16 * l,
              l < 32 ? 0x01000100u : 0x03020100u, l < 32 ? 0x03020100u : 0x03020302u, (u32)(l >> 2) & 8u};
  u32 fad = lds0 + OYP + 4 * w, za = wb0 + 16 * kg;
  asm volatile("" : "+{v70}"(ape), "+{v71}"(apo), "+{v[28:35]}"(ka), "+{v[26:27]}"(kb), "+{v25}"(fad), "+{v12}"(za));
  STAMP(4);
  asm volatile("s_waitcnt vmcnt(4)" : "+{v24}"(ldtv), "+{v[160:163]}"(junk));
  float dt = rbt(fexp(hi2f(ldtv)));
  asm volatile("" : "+v"(dt));
  const f32x2 cdt = {rdl(dt * -1.4426950408889634f, 0), rdl(dt * 0.15915494309189535f, 0)};
  const u32 dk = (u32)__builtin_amdgcn_readfirstlane((int)junk.x) << 16, db = max(__float_as_uint(rdl(dt, 0)), 27u << 23);
  u32 em = max(dk >> 23 & 255u, 127u) + 119 - (db >> 23);
  asm("s_max_i32 %0, %0, 8\n\ts_min_u32 %0, %0, 0xfe" : "+s"(em) :: "scc");
  SB();
  STAMP(10);
  STAMPC(11);
  {
    u32 e, tmp, o0, o1, of, os;
    asm volatile(GEN_ASM
                 : [e] "=&s"(e), [t] "=&s"(tmp), "=&{v93}"(o0), "=&{v46}"(o1), "=&{v48}"(of), "=&{v8}"(os), "+{v25}"(fad), "+{v12}"(za), "+{v68}"(pr), "+{v69}"(pi), "+{v70}"(ape),
                   "+{v71}"(apo), "+{v96}"(pl), "+{v97}"(pa), "+{v[28:35]}"(ka), "+{v[26:27]}"(kb), "+{v[16:19]}"(tv), "+{v[20:23]}"(fx)
                 : [w] "s"(wb0), [c] "s"(cdt), [k] "s"(f32x2{1.4426950408889634f, 256.f}), [em] "s"(em)
                 : "memory", "scc", GEN_CLOBBER);
    STAMP(1);
    STAMPC(12);
    const float ys = __uint_as_float((e - 5) << 23) * __uint_as_float(db);
    asm volatile("v_rcp_f32 %1, %2\n\ts_nop 0\n\tv_fma_f32 %1, %3, %1, %1\n\tv_fma_mixhi_f16 %0, %1, 1.0, %0 op_sel:[0,0,1] op_sel_hi:[0,0,1]\n\ts_nop 0"
                 : "+v"(o0), "=&v"(dt) : "v"(ys), "s"(dk));
    const u32 k0 = (u32)__builtin_amdgcn_readlane((int)o0, 3), k256 = (u32)__builtin_amdgcn_readlane((int)o1, 60);
    const u32 k512 = (u32)__builtin_amdgcn_readlane((int)os, 28), k1024 = (u32)__builtin_amdgcn_readlane((int)of, 55);
    *(LDS u16*)(size_t)(wb0 + tv.z) = (u16)__builtin_amdgcn_perm((k512 & 0xffffu) | (k1024 << 16), (k0 >> 16) | (k256 << 16), tv.w);
    *(float*)(smem + OSC + 4 * w) = ys;
    fx &= ((k1024 & 0xffffu) | (k1024 << 16)) ^ 0x80008000u;
  }
  asm volatile(CONV_ASM : "+{v[28:35]}"(ka) : "{v[26:27]}"(kb), "{v25}"(fad), "{v[48:51]}"(fx), [w] "s"(w) : "vcc", "scc", "s6", "memory", CONV_CLOBBER TSC);
  asm volatile(UNTIL("1500") :: [st] "s"(st0) : "s8", "s9", "scc");
  TSGET(2, 5, 6, 7);
  STAMP(13);
  STAMPC(15);
  FLUSH();
  __builtin_amdgcn_endpgm();
}

extern "C" void kernel_launch(void* const* d_in, const int* in_sizes, int n_in, void* d_out, int out_size,
                              void* d_ws, size_t ws_size, hipStream_t stream) {
  const u16* const* in = (const u16* const*)d_in;
  fused<<<256, 768, 0, stream>>>(in[0], in[1], in[2], in[3], in[4], in[5], in[6], in[8], in[9], in[10], in[11],
                                 (u16*)d_out, (char*)d_ws);
}
```

```cpp
#include <hip/hip_runtime.h>
#include <stdint.h>

#define STR2(x) #x
#define STR(x) STR2(x)
#define LA 4
#define WL2 44
#define WL3 100
#define PH 1246
#define ABL 0
#define PPRIO 0
#define CPRIO 1
#define GDELAY 0
#define ROT 1
#define PRE 2
#define GNT 0
#define OAUX 0
#define LNT 1
#define PNT ""
#define LNNT ""
#define PRENT ""
#define PFN 15
#define PFO 512
#define DBAR 2
#define THR 1
#define QD 11
#define HNAP0 4
#define HNAP 4
#define KR 3
#define KI 2
#define QE 11
#define QA 5
#define NA 0
#define CH 0
#define G0E 0
#define DDL 0
#define WBM 0
#define WBW 1
#define HQ 3
#define RSL 5
#define DPF 1
#define HELP 0
#define WARM 0
#define HPI 0
#define Q0 6
#define NP 10
#define HW 28
#define NE 0
#define YN0 2
#define YN3 0
#define QS 7
#define QAS 6
#define RAMP 4
#define GNAP "s_sleep 1"
#define RNAP "s_sleep 1"
#define CLP 0
#define CSL 8
#define XSP 0
#define SPEC 0
#define HOLD 8
#define DPRIO 3
#define HAUX 16
#define DPO 128
#define TH 590
#define CLD 245
#define IXI 1
#define LPRIO 0
#define DPI 2
#define PDROP 2
#define TE 0
#define CNAP 2
#define XI 1
#define SCA " sc1"
#define SCB " sc1"
#define SCC " sc1"
#define SCT " sc1"
#define SCU " sc1"
#define SCH " sc1"
#define SCR " sc1"
#define TSG 280
#define SSL 5
#define PNAP 4
#define TW 310
#define TL 4352
#define CODE_KB 24
typedef unsigned short u16;
typedef unsigned int u32;
typedef __attribute__((ext_vector_type(8))) __bf16 bf16x8;
typedef __attribute__((ext_vector_type(2))) __bf16 bf16x2;
typedef __attribute__((ext_vector_type(8))) _Float16 f16x8;
typedef __attribute__((ext_vector_type(2))) _Float16 f16x2;
typedef __attribute__((ext_vector_type(4))) short v4i16;
typedef __attribute__((ext_vector_type(4))) float f32x4;
typedef __attribute__((ext_vector_type(2))) float f32x2;
typedef __attribute__((ext_vector_type(4))) u32 u32x4;
typedef __attribute__((ext_vector_type(2))) u32 u32x2;
typedef __attribute__((ext_vector_type(8))) u32 u32x8;
typedef __attribute__((ext_vector_type(2), aligned(4))) u32 u32x2_a4;
typedef __amdgpu_buffer_rsrc_t rsrc_t;
#define LDS __attribute__((address_space(3)))

#define TLARG
#define TLVAL
#define STAMP(I)
#define STAMPC(I)
#define FLUSH()

constexpr int L = 2048, D = 1024, NM = 128, F = 3584;
constexpr u32 O_ARR = 0x1000, O_CLR = 0x2000, O_RG = 0x4000, O_SG = 0x8000, O_IDX = 0x10000, O_HG = 0x20000,
              O_PG = 0xd0000, O_TOP = 0x140000, TOPS = 8 * TL, WS_END = O_TOP + 2 * TOPS;
constexpr int SPIN = 1 << 16;

__device__ __forceinline__ float lo2f(u32 w) { return __uint_as_float(w << 16); }
__device__ __forceinline__ float hi2f(u32 w) { return __uint_as_float(w & 0xffff0000u); }
__device__ __forceinline__ u32 pack2(float a, float b) {
  bf16x2 r = {(__bf16)a, (__bf16)b};
  return __builtin_bit_cast(u32, r);
}
__device__ __forceinline__ u32 packh(float a, float b) {
  f16x2 r = {(_Float16)a, (_Float16)b};
  return __builtin_bit_cast(u32, r);
}
__device__ __forceinline__ float rbt(float f) { return __uint_as_float(pack2(0.f, f)); }
__device__ __forceinline__ float rb(float f) {
  float r;
  asm("v_cvt_pk_bf16_f32 %0, 0, %1" : "=v"(r) : "v"(f));
  return r;
}
__device__ __forceinline__ u32 hadd(u32 a, u32 b) {
  return __builtin_bit_cast(u32, __builtin_bit_cast(f16x2, a) + __builtin_bit_cast(f16x2, b));
}
__device__ __forceinline__ float hlo(u32 a) { return (float)__builtin_bit_cast(f16x2, a).x; }
__device__ __forceinline__ float hhi(u32 a) { return (float)__builtin_bit_cast(f16x2, a).y; }

template <int CTRL, int RM>
__device__ __forceinline__ float dppf(float v) {
  return __builtin_bit_cast(float, __builtin_amdgcn_update_dpp(0, __builtin_bit_cast(int, v), CTRL, RM, 0xf, true));
}
template <int CTRL, int RM>
__device__ __forceinline__ u32 dppu(u32 v) {
  return (u32)__builtin_amdgcn_update_dpp(0, (int)v, CTRL, RM, 0xf, true);
}
__device__ __forceinline__ float rdl(float v, int i) {
  return __builtin_bit_cast(float, __builtin_amdgcn_readlane(__builtin_bit_cast(int, v), i));
}
__device__ __forceinline__ u32 wmaxu(u32 v) {
  v = max(v, dppu<0x111, 0xf>(v)); v = max(v, dppu<0x112, 0xf>(v)); v = max(v, dppu<0x114, 0xf>(v));
  v = max(v, dppu<0x118, 0xf>(v)); v = max(v, dppu<0x142, 0xa>(v)); v = max(v, dppu<0x143, 0xc>(v));
  return (u32)__builtin_amdgcn_readlane((int)v, 63);
}
__device__ __forceinline__ void swap32(float& a, float& b) {
  auto r = __builtin_amdgcn_permlane32_swap(__float_as_uint(a), __float_as_uint(b), false, false);
  a = __uint_as_float(r[0]); b = __uint_as_float(r[1]);
}
__device__ __forceinline__ void swap16(float& a, float& b) {
  auto r = __builtin_amdgcn_permlane16_swap(__float_as_uint(a), __float_as_uint(b), false, false);
  a = __uint_as_float(r[0]); b = __uint_as_float(r[1]);
}
__device__ __forceinline__ u32x2 trread(const char* p) {
  v4i16 r = __builtin_amdgcn_ds_read_tr16_b64_v4i16((LDS v4i16*)p);
  return __builtin_bit_cast(u32x2, r);
}
__device__ __forceinline__ u32x2 trl(LDS char* p) { return __builtin_bit_cast(u32x2, __builtin_amdgcn_ds_read_tr16_b64_v4i16((LDS v4i16*)p)); }
__device__ __forceinline__ f16x8 mkh(u32x2 a, u32x2 b) {
  u32x4 v = {a.x, a.y, b.x, b.y};
  return __builtin_bit_cast(f16x8, v);
}
__device__ __forceinline__ bf16x8 mkb(u32x2 a, u32x2 b) {
  u32x4 v = {a.x, a.y, b.x, b.y};
  return __builtin_bit_cast(bf16x8, v);
}
__device__ __forceinline__ bf16x8 asb(u32x4 v) { return __builtin_bit_cast(bf16x8, v); }
__device__ __forceinline__ f16x8 ash(u32x4 v) { return __builtin_bit_cast(f16x8, v); }
#define MFMAB(a, b, c) __builtin_amdgcn_mfma_f32_16x16x32_bf16(a, b, c, 0, 0, 0)
#define MFMAH(a, b, c) __builtin_amdgcn_mfma_f32_16x16x32_f16(a, b, c, 0, 0, 0)

__device__ __forceinline__ u32x4 ld16(rsrc_t r, u32 off) { return __builtin_amdgcn_raw_buffer_load_b128(r, off, 0, 16); }
__device__ __forceinline__ u32x2 ld8(rsrc_t r, u32 off) { return __builtin_amdgcn_raw_buffer_load_b64(r, off, 0, 16); }
__device__ __forceinline__ void st16(rsrc_t r, u32 off, u32x4 v) { __builtin_amdgcn_raw_buffer_store_b128(v, r, off, 0, 16); }
__device__ __forceinline__ void st8(rsrc_t r, u32 off, u32x2 v) { __builtin_amdgcn_raw_buffer_store_b64(v, r, off, 0, 16); }
#define NAP() asm volatile("s_sleep 1" ::: "memory")
#define SB() __builtin_amdgcn_sched_barrier(0)
#define LBAR() asm volatile("s_waitcnt lgkmcnt(0)\n\ts_barrier" ::: "memory")

__device__ __forceinline__ float fexp(float v) { return __builtin_amdgcn_exp2f(v * 1.4426950408889634f); }
__device__ __forceinline__ float dot2b(u32 a, u32 b, float c) {
  return __builtin_amdgcn_fdot2_f32_bf16(__builtin_bit_cast(bf16x2, a), __builtin_bit_cast(bf16x2, b), c, false);
}
__device__ __forceinline__ float sigm(float v) { return __builtin_amdgcn_rcpf(1.f + __builtin_amdgcn_exp2f(-1.4426950408889634f * v)); }

constexpr int RS = 288, PB = 16 * RS;
constexpr int CPE = 1216, TB = 2 * PB, CPO = 832;
constexpr int T2B = 4, T3B = 224, C2 = 52, C3 = 84;
constexpr int OY = 0, CHR = TB + 4 * CPO;
constexpr int OX = 8 * CHR, XREG = 4640, XO = 266;
constexpr int OMISC = OX + 8 * XREG;
constexpr int OXF = OMISC + 320, OSC = OMISC + 352, OYP = OMISC + 448;
constexpr int OB = OMISC + 1024, SMEM = 148480;
constexpr int FA = 131072, FH = FA + 8192, HP = 576, FC = FH + 8 * HP;
static_assert(FC + 1024 <= SMEM && OB + 4096 <= SMEM, "lds map");

struct Tab { u32 v[6][64][4]; };
constexpr Tab mktab() {
  Tab T = {};
  for (int l = 0; l < 64; l++) {
    const int n16 = l & 15, kg = l >> 4, pc = ((n16 >> 2) & 1) + (n16 & 8) + 2 * (3 - (n16 & 3)), odd = kg >> 1, s = l & 3;
    const int e0 = 534 - 258 - 128 - 8 * pc - 4 * (kg & 1) - odd;
    const int ez = ((l >> 3) & 3) * CPE + ((l & 4) ? 1056 - 64 : 0) + 16 * (l & 7);
    const int b2 = T2B + 44 + C2 * ((kg & 1) + 2 * (pc & 1)), b3 = T3B + 60 + C3 * (pc & 1) - 32 * (kg & 1);
    const int to = TB + 2 * ((odd ? b3 : b2) + 3 - (pc >> 1) - 8 - 16);
    const int q = (l >> 2) & 7, tap = q == 0 ? ((s & 1) ? 0 : 256) : q == 1 ? 257 : q == 7 ? 0 : 509 + q;
    const int sp = q != 6 ? s * (CPE - 2) + 2 * (537 - tap) : TB + s * (CPO - 2) + 2 * (T2B + 15);
    T.v[0][l][0] = (2 * e0 + (2 - odd) * (CPE - 2)) | (2 * e0 + 3 * odd * (CPE - 2)) << 16;
    T.v[0][l][1] = ez | to << 16;
    T.v[0][l][2] = sp;
    T.v[0][l][3] = !tap ? 0x0100u : q == 6 ? 0x0706u : q >= 2 ? 0x0504u : 0x0302u;
    for (int k = 0; k < 8; k++) {
      const int d = 8 * kg + k - n16;
      if (d == 13) T.v[1][l][k >> 1] |= 0xffffu << 16 * (k & 1);
      for (int h = 1; h <= 4; h++)
        if (d >= (h == 1 ? 14 : 10) - h && d <= (h == 1 ? 14 : 10) + h) T.v[1 + h][l][k >> 1] |= 0x3c00u << 16 * (k & 1);
    }
  }
  return T;
}
extern "C" __device__ const __attribute__((aligned(128), used, visibility("hidden"))) Tab lane_tab = mktab();

#define GEN_ASM \
  "s_waitcnt vmcnt(2)\n\tv_lshlrev_b32 v56, 16, v96\n\t" \
  "v_and_b32 v57, 0xffff0000, v96\n\tv_pk_mul_f32 v[56:57], %[k], v[56:57] op_sel_hi:[0,1]\n\t" \
  "v_lshlrev_b32 v49, 16, v97\n\tv_exp_f32 v56, v56\n\t" \
  "v_exp_f32 v57, v57\n\tv_and_b32 v51, 0xffff0000, v97\n\t" \
  "v_cvt_pk_bf16_f32 v56, v56, v57\n\tv_lshlrev_b32 v48, 16, v56\n\t" \
  "v_and_b32 v50, 0xffff0000, v56\n\tv_pk_mul_f32 v[48:49], %[c], v[48:49]\n\t" \
  "v_pk_mul_f32 v[50:51], %[c], v[50:51]\n\tv_pk_mul_f32 v[98:99], %[k], v[48:49] op_sel:[1,0]\n\t" \
  "v_pk_mul_f32 v[100:101], %[k], v[50:51] op_sel:[1,0]\n\tv_fract_f32 v49, v49\n\t" \
  "v_fract_f32 v51, v51\n\tv_exp_f32 v48, v48\n\t" \
  "v_exp_f32 v50, v50\n\tv_cos_f32 v40, v49\n\t" \
  "v_sin_f32 v41, v49\n\tv_cos_f32 v42, v51\n\t" \
  "v_sin_f32 v43, v51\n\tv_pk_mul_f32 v[40:41], v[40:41], v[48:49] op_sel_hi:[1,0]\n\t" \
  "v_pk_mul_f32 v[42:43], v[42:43], v[50:51] op_sel_hi:[1,0]\n\tv_pk_mul_f32 v[48:49], v[40:41], v[40:41] op_sel:[1,1] op_sel_hi:[1,0] neg_lo:[1,0]\n\t" \
  "v_pk_mul_f32 v[50:51], v[42:43], v[42:43] op_sel:[1,1] op_sel_hi:[1,0] neg_lo:[1,0]\n\tv_pk_fma_f32 v[48:49], v[40:41], v[40:41], v[48:49] op_sel:[0,0,0] op_sel_hi:[0,1,1]\n\t" \
  "v_pk_fma_f32 v[50:51], v[42:43], v[42:43], v[50:51] op_sel:[0,0,0] op_sel_hi:[0,1,1]\n\tv_cvt_pk_f16_f32 v72, v40, -v41\n\t" \
  "v_cvt_pk_f16_f32 v73, v42, -v43\n\tv_pk_mul_f32 v[52:53], v[48:49], v[40:41] op_sel:[1,1] op_sel_hi:[1,0] neg_lo:[1,0]\n\t" \
  "v_pk_mul_f32 v[54:55], v[50:51], v[42:43] op_sel:[1,1] op_sel_hi:[1,0] neg_lo:[1,0]\n\tv_pk_fma_f32 v[52:53], v[48:49], v[40:41], v[52:53] op_sel:[0,0,0] op_sel_hi:[0,1,1]\n\t" \
  "v_pk_fma_f32 v[54:55], v[50:51], v[42:43], v[54:55] op_sel:[0,0,0] op_sel_hi:[0,1,1]\n\tv_pk_mul_f32 v[56:57], v[48:49], v[48:49] op_sel:[1,1] op_sel_hi:[1,0] neg_lo:[1,0]\n\t" \
  "v_pk_mul_f32 v[58:59], v[50:51], v[50:51] op_sel:[1,1] op_sel_hi:[1,0] neg_lo:[1,0]\n\tv_pk_fma_f32 v[56:57], v[48:49], v[48:49], v[56:57] op_sel:[0,0,0] op_sel_hi:[0,1,1]\n\t" \
  "v_pk_fma_f32 v[58:59], v[50:51], v[50:51], v[58:59] op_sel:[0,0,0] op_sel_hi:[0,1,1]\n\tv_cvt_pk_f16_f32 v74, v48, -v49\n\t" \
  "v_cvt_pk_f16_f32 v75, v50, -v51\n\tv_cvt_pk_f16_f32 v76, v52, -v53\n\t" \
  "v_cvt_pk_f16_f32 v77, v54, -v55\n\tv_pk_mul_f32 v[52:53], v[56:57], v[48:49] op_sel:[1,1] op_sel_hi:[1,0] neg_lo:[1,0]\n\t" \
  "v_pk_mul_f32 v[54:55], v[58:59], v[50:51] op_sel:[1,1] op_sel_hi:[1,0] neg_lo:[1,0]\n\tv_pk_fma_f32 v[52:53], v[56:57], v[48:49], v[52:53] op_sel:[0,0,0] op_sel_hi:[0,1,1]\n\t" \
  "v_pk_fma_f32 v[54:55], v[58:59], v[50:51], v[54:55] op_sel:[0,0,0] op_sel_hi:[0,1,1]\n\tv_pk_mul_f32 v[60:61], v[56:57], v[56:57] op_sel:[1,1] op_sel_hi:[1,0] neg_lo:[1,0]\n\t" \
  "v_pk_mul_f32 v[62:63], v[58:59], v[58:59] op_sel:[1,1] op_sel_hi:[1,0] neg_lo:[1,0]\n\tv_pk_fma_f32 v[60:61], v[56:57], v[56:57], v[60:61] op_sel:[0,0,0] op_sel_hi:[0,1,1]\n\t" \
  "v_pk_fma_f32 v[62:63], v[58:59], v[58:59], v[62:63] op_sel:[0,0,0] op_sel_hi:[0,1,1]\n\tv_cvt_pk_f16_f32 v78, v56, -v57\n\t" \
  "v_cvt_pk_f16_f32 v79, v58, -v59\n\tv_cvt_pk_f16_f32 v82, v52, -v53\n\t" \
  "v_cvt_pk_f16_f32 v83, v54, -v55\n\tv_pk_mul_f16 v80, v78, v72 op_sel:[1,1] op_sel_hi:[1,0] neg_lo:[1,0]\n\t" \
  "v_pk_mul_f16 v81, v79, v73 op_sel:[1,1] op_sel_hi:[1,0] neg_lo:[1,0]\n\tv_pk_fma_f16 v80, v78, v72, v80 op_sel:[0,0,0] op_sel_hi:[0,1,1]\n\t" \
  "v_pk_fma_f16 v81, v79, v73, v81 op_sel:[0,0,0] op_sel_hi:[0,1,1]\n\tv_pk_mul_f16 v84, v78, v76 op_sel:[1,1] op_sel_hi:[1,0] neg_lo:[1,0]\n\t" \
  "v_pk_mul_f16 v85, v79, v77 op_sel:[1,1] op_sel_hi:[1,0] neg_lo:[1,0]\n\tv_pk_fma_f16 v84, v78, v76, v84 op_sel:[0,0,0] op_sel_hi:[0,1,1]\n\t" \
  "v_pk_fma_f16 v85, v79, v77, v85 op_sel:[0,0,0] op_sel_hi:[0,1,1]\n\tv_cvt_pk_f16_f32 v86, v60, -v61\n\t" \
  "v_cvt_pk_f16_f32 v87, v62, -v63\n\tds_write_addtid_b32 v72 offset:288\n\t" \
  "ds_write_addtid_b32 v73 offset:4896\n\tds_write_addtid_b32 v74 offset:576\n\t" \
  "ds_write_addtid_b32 v75 offset:5184\n\tds_write_addtid_b32 v76 offset:864\n\t" \
  "ds_write_addtid_b32 v77 offset:5472\n\tds_write_addtid_b32 v78 offset:1152\n\t" \
  "ds_write_addtid_b32 v79 offset:5760\n\tds_write_addtid_b32 v80 offset:1440\n\t" \
  "ds_write_addtid_b32 v81 offset:6048\n\tds_write_addtid_b32 v82 offset:1728\n\t" \
  "ds_write_addtid_b32 v83 offset:6336\n\tds_write_addtid_b32 v84 offset:2016\n\t" \
  "ds_write_addtid_b32 v85 offset:6624\n\tds_write_addtid_b32 v86 offset:3456\n\t" \
  "ds_write_addtid_b32 v87 offset:8064\n\tds_read_b128 v[104:107], v70 offset:0\n\t" \
  "ds_read_b128 v[108:111], v70 offset:64\n\tds_read_b128 v[112:115], v70 offset:128\n\t" \
  "ds_read_b128 v[116:119], v70 offset:192\n\tds_read_b128 v[120:123], v70 offset:4608\n\t" \
  "ds_read_b128 v[124:127], v70 offset:4672\n\tds_read_b128 v[128:131], v70 offset:4736\n\t" \
  "ds_read_b128 v[132:135], v70 offset:4800\n\tv_fract_f32 v99, v99\n\t" \
  "v_fract_f32 v101, v101\n\tv_exp_f32 v98, v98\n\t" \
  "v_exp_f32 v100, v100\n\tv_cos_f32 v40, v99\n\t" \
  "v_sin_f32 v41, v99\n\tv_cos_f32 v42, v101\n\t" \
  "v_sin_f32 v43, v101\n\tv_pk_mul_f32 v[40:41], v[40:41], v[98:99] op_sel_hi:[1,0]\n\t" \
  "v_pk_mul_f32 v[42:43], v[42:43], v[100:101] op_sel_hi:[1,0]\n\tv_pk_mul_f32 v[44:45], v[40:41], v[40:41] op_sel:[1,1] op_sel_hi:[1,0] neg_lo:[1,0]\n\t" \
  "v_pk_mul_f32 v[46:47], v[42:43], v[42:43] op_sel:[1,1] op_sel_hi:[1,0] neg_lo:[1,0]\n\tv_pk_fma_f32 v[44:45], v[40:41], v[40:41], v[44:45] op_sel:[0,0,0] op_sel_hi:[0,1,1]\n\t" \
  "v_pk_fma_f32 v[46:47], v[42:43], v[42:43], v[46:47] op_sel:[0,0,0] op_sel_hi:[0,1,1]\n\tv_cvt_pk_f16_f32 v36, v40, v41\n\t" \
  "v_cvt_pk_f16_f32 v37, v42, v43\n\ts_waitcnt vmcnt(0)\n\t" \
  "v_pk_mul_f32 v[48:49], v[44:45], v[44:45] op_sel:[1,1] op_sel_hi:[1,0] neg_lo:[1,0]\n\tv_pk_mul_f32 v[50:51], v[46:47], v[46:47] op_sel:[1,1] op_sel_hi:[1,0] neg_lo:[1,0]\n\t" \
  "v_pk_fma_f32 v[48:49], v[44:45], v[44:45], v[48:49] op_sel:[0,0,0] op_sel_hi:[0,1,1]\n\tv_pk_fma_f32 v[50:51], v[46:47], v[46:47], v[50:51] op_sel:[0,0,0] op_sel_hi:[0,1,1]\n\t" \
  "v_lshlrev_b32 v4, 16, v68\n\tv_cvt_pk_f16_f32 v88, v44, -v45\n\t" \
  "v_cvt_pk_f16_f32 v89, v46, -v47\n\tv_pk_mul_f32 v[52:53], v[48:49], v[44:45] op_sel:[1,1] op_sel_hi:[1,0] neg_lo:[1,0]\n\t" \
  "v_pk_mul_f32 v[54:55], v[50:51], v[46:47] op_sel:[1,1] op_sel_hi:[1,0] neg_lo:[1,0]\n\tv_lshlrev_b32 v5, 16, v69\n\t" \
  "v_pk_fma_f32 v[52:53], v[48:49], v[44:45], v[52:53] op_sel:[0,0,0] op_sel_hi:[0,1,1]\n\tv_pk_fma_f32 v[54:55], v[50:51], v[46:47], v[54:55] op_sel:[0,0,0] op_sel_hi:[0,1,1]\n\t" \
  "v_pk_mul_f32 v[56:57], v[48:49], v[48:49] op_sel:[1,1] op_sel_hi:[1,0] neg_lo:[1,0]\n\tv_pk_mul_f32 v[58:59], v[50:51], v[50:51] op_sel:[1,1] op_sel_hi:[1,0] neg_lo:[1,0]\n\t" \
  "v_and_b32 v6, 0xffff0000, v68\n\tv_pk_fma_f32 v[56:57], v[48:49], v[48:49], v[56:57] op_sel:[0,0,0] op_sel_hi:[0,1,1]\n\t" \
  "v_pk_fma_f32 v[58:59], v[50:51], v[50:51], v[58:59] op_sel:[0,0,0] op_sel_hi:[0,1,1]\n\tv_cvt_pk_f16_f32 v90, v48, -v49\n\t" \
  "v_cvt_pk_f16_f32 v91, v50, -v51\n\tv_and_b32 v7, 0xffff0000, v69\n\t" \
  "v_pk_mul_f32 v[64:65], v[56:57], v[60:61] op_sel:[1,1] op_sel_hi:[1,0] neg_lo:[1,0]\n\tv_pk_mul_f32 v[66:67], v[58:59], v[62:63] op_sel:[1,1] op_sel_hi:[1,0] neg_lo:[1,0]\n\t" \
  "v_pk_fma_f32 v[64:65], v[56:57], v[60:61], v[64:65] op_sel:[0,0,0] op_sel_hi:[0,1,1]\n\tv_pk_fma_f32 v[66:67], v[58:59], v[62:63], v[66:67] op_sel:[0,0,0] op_sel_hi:[0,1,1]\n\t" \
  "v_max3_f32 v102, |v4|, |v5|, |v6|\n\tv_cvt_pk_f16_f32 v92, v52, -v53\n\t" \
  "v_cvt_pk_f16_f32 v93, v54, -v55\n\tv_pk_mul_f16 v136, v88, v74 op_sel:[1,1] op_sel_hi:[1,0] neg_lo:[1,0]\n\t" \
  "v_pk_mul_f16 v137, v89, v75 op_sel:[1,1] op_sel_hi:[1,0] neg_lo:[1,0]\n\tv_max_f32 v102, v102, |v7|\n\t" \
  "v_pk_fma_f16 v136, v88, v74, v136 op_sel:[0,0,0] op_sel_hi:[0,1,1]\n\tv_pk_fma_f16 v137, v89, v75, v137 op_sel:[0,0,0] op_sel_hi:[0,1,1]\n\t" \
  "v_pk_mul_f16 v138, v88, v78 op_sel:[1,1] op_sel_hi:[1,0] neg_lo:[1,0]\n\tv_pk_mul_f16 v139, v89, v79 op_sel:[1,1] op_sel_hi:[1,0] neg_lo:[1,0]\n\t" \
  "v_max_u32_dpp v102, v102, v102 row_shr:1 row_mask:0xf bank_mask:0xf\n\tv_pk_fma_f16 v138, v88, v78, v138 op_sel:[0,0,0] op_sel_hi:[0,1,1]\n\t" \
  "v_pk_fma_f16 v139, v89, v79, v139 op_sel:[0,0,0] op_sel_hi:[0,1,1]\n\tv_cvt_pk_f16_f32 v94, v64, -v65\n\t" \
  "v_cvt_pk_f16_f32 v95, v66, -v67\n\tv_max_u32_dpp v102, v102, v102 row_shr:2 row_mask:0xf bank_mask:0xf\n\t" \
  "v_pk_mul_f16 v140, v88, v82 op_sel:[1,1] op_sel_hi:[1,0] neg_lo:[1,0]\n\tv_pk_mul_f16 v141, v89, v83 op_sel:[1,1] op_sel_hi:[1,0] neg_lo:[1,0]\n\t" \
  "v_pk_fma_f16 v140, v88, v82, v140 op_sel:[0,0,0] op_sel_hi:[0,1,1]\n\tv_pk_fma_f16 v141, v89, v83, v141 op_sel:[0,0,0] op_sel_hi:[0,1,1]\n\t" \
  "ds_write_addtid_b32 v88 offset:3168\n\tds_write_addtid_b32 v89 offset:7776\n\t" \
  "ds_write_addtid_b32 v136 offset:0\n\tds_write_addtid_b32 v137 offset:4608\n\t" \
  "ds_write_addtid_b32 v138 offset:288\n\tds_write_addtid_b32 v139 offset:4896\n\t" \
  "ds_write_addtid_b32 v94 offset:1152\n\tds_write_addtid_b32 v95 offset:5760\n\t" \
  "ds_write_addtid_b32 v140 offset:576\n\tds_write_addtid_b32 v141 offset:5184\n\t" \
  "v_max_u32_dpp v102, v102, v102 row_shr:4 row_mask:0xf bank_mask:0xf\n\tv_pk_mul_f16 v142, v88, v86 op_sel:[1,1] op_sel_hi:[1,0] neg_lo:[1,0]\n\t" \
  "v_pk_mul_f16 v143, v89, v87 op_sel:[1,1] op_sel_hi:[1,0] neg_lo:[1,0]\n\tv_pk_fma_f16 v142, v88, v86, v142 op_sel:[0,0,0] op_sel_hi:[0,1,1]\n\t" \
  "v_pk_fma_f16 v143, v89, v87, v143 op_sel:[0,0,0] op_sel_hi:[0,1,1]\n\tv_max_u32_dpp v102, v102, v102 row_shr:8 row_mask:0xf bank_mask:0xf\n\t" \
  "v_pk_mul_f16 v144, v90, v78 op_sel:[1,1] op_sel_hi:[1,0] neg_lo:[1,0]\n\tv_pk_mul_f16 v145, v91, v79 op_sel:[1,1] op_sel_hi:[1,0] neg_lo:[1,0]\n\t" \
  "v_pk_fma_f16 v144, v90, v78, v144 op_sel:[0,0,0] op_sel_hi:[0,1,1]\n\tv_pk_fma_f16 v145, v91, v79, v145 op_sel:[0,0,0] op_sel_hi:[0,1,1]\n\t" \
  "v_max_u32_dpp v102, v102, v102 row_bcast:15 row_mask:0xa bank_mask:0xf\n\tv_pk_mul_f16 v146, v90, v86 op_sel:[1,1] op_sel_hi:[1,0] neg_lo:[1,0]\n\t" \
  "v_pk_mul_f16 v147, v91, v87 op_sel:[1,1] op_sel_hi:[1,0] neg_lo:[1,0]\n\tv_pk_fma_f16 v146, v90, v86, v146 op_sel:[0,0,0] op_sel_hi:[0,1,1]\n\t" \
  "v_pk_fma_f16 v147, v91, v87, v147 op_sel:[0,0,0] op_sel_hi:[0,1,1]\n\tv_max_u32_dpp v102, v102, v102 row_bcast:31 row_mask:0xc bank_mask:0xf\n\t" \
  "v_pk_mul_f16 v148, v92, v78 op_sel:[1,1] op_sel_hi:[1,0] neg_lo:[1,0]\n\tv_pk_mul_f16 v149, v93, v79 op_sel:[1,1] op_sel_hi:[1,0] neg_lo:[1,0]\n\t" \
  "v_pk_fma_f16 v148, v92, v78, v148 op_sel:[0,0,0] op_sel_hi:[0,1,1]\n\tv_pk_fma_f16 v149, v93, v79, v149 op_sel:[0,0,0] op_sel_hi:[0,1,1]\n\t" \
  "v_readlane_b32 %[e], v102, 63\n\tv_pk_mul_f16 v150, v92, v86 op_sel:[1,1] op_sel_hi:[1,0] neg_lo:[1,0]\n\t" \
  "v_pk_mul_f16 v151, v93, v87 op_sel:[1,1] op_sel_hi:[1,0] neg_lo:[1,0]\n\tv_pk_fma_f16 v150, v92, v86, v150 op_sel:[0,0,0] op_sel_hi:[0,1,1]\n\t" \
  "v_pk_fma_f16 v151, v93, v87, v151 op_sel:[0,0,0] op_sel_hi:[0,1,1]\n\ts_lshr_b32 %[e], %[e], 23\n\t" \
  "s_max_u32 %[e], %[e], %[em]\n\ts_sub_u32 s6, 260, %[e]\n\t" \
  "s_lshl_b32 s6, s6, 23\n\tds_write_addtid_b32 v142 offset:864\n\t" \
  "ds_write_addtid_b32 v143 offset:5472\n\tds_write_addtid_b32 v144 offset:3456\n\t" \
  "ds_write_addtid_b32 v145 offset:8064\n\tds_write_addtid_b32 v146 offset:3744\n\t" \
  "ds_write_addtid_b32 v147 offset:8352\n\tds_write_addtid_b32 v148 offset:4032\n\t" \
  "ds_write_addtid_b32 v149 offset:8640\n\tds_write_addtid_b32 v150 offset:4320\n\t" \
  "ds_write_addtid_b32 v151 offset:8928\n\tv_pk_mul_f16 v96, v94, v88 op_sel:[1,1] op_sel_hi:[1,0] neg_lo:[1,0]\n\t" \
  "v_pk_mul_f16 v97, v95, v89 op_sel:[1,1] op_sel_hi:[1,0] neg_lo:[1,0]\n\tv_pk_fma_f16 v96, v94, v88, v96 op_sel:[0,0,0] op_sel_hi:[0,1,1]\n\t" \
  "v_pk_fma_f16 v97, v95, v89, v97 op_sel:[0,0,0] op_sel_hi:[0,1,1]\n\tv_pk_mul_f16 v98, v94, v90 op_sel:[1,1] op_sel_hi:[1,0] neg_lo:[1,0]\n\t" \
  "v_pk_mul_f16 v99, v95, v91 op_sel:[1,1] op_sel_hi:[1,0] neg_lo:[1,0]\n\tv_pk_fma_f16 v98, v94, v90, v98 op_sel:[0,0,0] op_sel_hi:[0,1,1]\n\t" \
  "v_pk_fma_f16 v99, v95, v91, v99 op_sel:[0,0,0] op_sel_hi:[0,1,1]\n\tv_pk_mul_f16 v100, v94, v92 op_sel:[1,1] op_sel_hi:[1,0] neg_lo:[1,0]\n\t" \
  "v_pk_mul_f16 v101, v95, v93 op_sel:[1,1] op_sel_hi:[1,0] neg_lo:[1,0]\n\tv_pk_fma_f16 v100, v94, v92, v100 op_sel:[0,0,0] op_sel_hi:[0,1,1]\n\t" \
  "v_pk_fma_f16 v101, v95, v93, v101 op_sel:[0,0,0] op_sel_hi:[0,1,1]\n\tds_write_addtid_b32 v96 offset:1440\n\t" \
  "ds_write_addtid_b32 v97 offset:6048\n\tds_write_addtid_b32 v98 offset:1728\n\t" \
  "ds_write_addtid_b32 v99 offset:6336\n\tds_write_addtid_b32 v100 offset:2016\n\t" \
  "ds_write_addtid_b32 v101 offset:6624\n\tds_read_b128 v[136:139], v71 offset:0\n\t" \
  "ds_read_b128 v[140:143], v71 offset:64\n\tds_read_b128 v[144:147], v71 offset:128\n\t" \
  "s_cmp_lt_u32 %[w], 50176\n\ts_cbranch_scc0 1f\n\t" \
  "s_setprio 1\n\t1:\n\t" \
  "ds_read_b128 v[148:151], v71 offset:192\n\tds_read_b128 v[152:155], v71 offset:4608\n\t" \
  "ds_read_b128 v[156:159], v71 offset:4672\n\tds_read_b128 v[160:163], v71 offset:4736\n\t" \
  "ds_read_b128 v[164:167], v71 offset:4800\n\tv_pk_mul_f32 v[48:49], v[60:61], v[60:61] op_sel:[1,1] op_sel_hi:[1,0] neg_lo:[1,0]\n\t" \
  "v_pk_mul_f32 v[50:51], v[62:63], v[62:63] op_sel:[1,1] op_sel_hi:[1,0] neg_lo:[1,0]\n\tv_pk_fma_f32 v[48:49], v[60:61], v[60:61], v[48:49] op_sel:[0,0,0] op_sel_hi:[0,1,1]\n\t" \
  "v_pk_fma_f32 v[50:51], v[62:63], v[62:63], v[50:51] op_sel:[0,0,0] op_sel_hi:[0,1,1]\n\tv_pk_mul_f32 v[52:53], v[48:49], v[60:61] op_sel:[1,1] op_sel_hi:[1,0] neg_lo:[1,0]\n\t" \
  "v_pk_mul_f32 v[54:55], v[50:51], v[62:63] op_sel:[1,1] op_sel_hi:[1,0] neg_lo:[1,0]\n\tv_pk_fma_f32 v[52:53], v[48:49], v[60:61], v[52:53] op_sel:[0,0,0] op_sel_hi:[0,1,1]\n\t" \
  "v_pk_fma_f32 v[54:55], v[50:51], v[62:63], v[54:55] op_sel:[0,0,0] op_sel_hi:[0,1,1]\n\tv_pk_mul_f32 v[40:41], v[48:49], v[48:49] op_sel:[1,1] op_sel_hi:[1,0] neg_lo:[1,0]\n\t" \
  "v_pk_mul_f32 v[42:43], v[50:51], v[50:51] op_sel:[1,1] op_sel_hi:[1,0] neg_lo:[1,0]\n\tv_pk_fma_f32 v[40:41], v[48:49], v[48:49], v[40:41] op_sel:[0,0,0] op_sel_hi:[0,1,1]\n\t" \
  "v_pk_fma_f32 v[42:43], v[50:51], v[50:51], v[42:43] op_sel:[0,0,0] op_sel_hi:[0,1,1]\n\tv_cvt_pk_f16_f32 v46, v48, v49\n\t" \
  "v_cvt_pk_f16_f32 v47, v50, v51\n\tv_cvt_pk_f16_f32 v56, v52, v53\n\t" \
  "v_cvt_pk_f16_f32 v57, v54, v55\n\tv_pk_mul_f32 v[64:65], s[6:7], v[4:5] op_sel_hi:[0,1]\n\t" \
  "v_pk_mul_f32 v[66:67], s[6:7], v[6:7] op_sel_hi:[0,1]\n\tv_cvt_pk_f16_f32 v48, v64, v65\n\t" \
  "v_cvt_pk_f16_f32 v49, v66, v67\n\tv_pk_mul_f32 v[68:69], v[64:65], v[40:41] op_sel:[1,1] op_sel_hi:[1,0] neg_lo:[1,0]\n\t" \
  "v_pk_mul_f32 v[70:71], v[66:67], v[42:43] op_sel:[1,1] op_sel_hi:[1,0] neg_lo:[1,0]\n\tv_pk_fma_f32 v[68:69], v[64:65], v[40:41], v[68:69] op_sel:[0,0,0] op_sel_hi:[0,1,1]\n\t" \
  "v_pk_fma_f32 v[70:71], v[66:67], v[42:43], v[70:71] op_sel:[0,0,0] op_sel_hi:[0,1,1]\n\tv_pk_mul_f16 v50, v48, v86 op_sel:[1,1] op_sel_hi:[1,0]\n\t" \
  "v_pk_mul_f16 v51, v49, v87 op_sel:[1,1] op_sel_hi:[1,0]\n\tv_pk_fma_f16 v50, v48, v86, v50 op_sel:[0,0,0] op_sel_hi:[0,1,1] neg_hi:[0,1,0]\n\t" \
  "v_pk_fma_f16 v51, v49, v87, v51 op_sel:[0,0,0] op_sel_hi:[0,1,1] neg_hi:[0,1,0]\n\tv_pk_mul_f16 v52, v48, v46 op_sel:[1,1] op_sel_hi:[1,0] neg_lo:[1,0]\n\t" \
  "v_pk_mul_f16 v53, v49, v47 op_sel:[1,1] op_sel_hi:[1,0] neg_lo:[1,0]\n\tv_pk_fma_f16 v52, v48, v46, v52 op_sel:[0,0,0] op_sel_hi:[0,1,1]\n\t" \
  "v_pk_fma_f16 v53, v49, v47, v53 op_sel:[0,0,0] op_sel_hi:[0,1,1]\n\tv_pk_mul_f16 v54, v48, v56 op_sel:[1,1] op_sel_hi:[1,0] neg_lo:[1,0]\n\t" \
  "v_pk_mul_f16 v55, v49, v57 op_sel:[1,1] op_sel_hi:[1,0] neg_lo:[1,0]\n\tv_pk_fma_f16 v54, v48, v56, v54 op_sel:[0,0,0] op_sel_hi:[0,1,1]\n\t" \
  "v_pk_fma_f16 v55, v49, v57, v55 op_sel:[0,0,0] op_sel_hi:[0,1,1]\n\tds_write_addtid_b32 v48 offset:0\n\t" \
  "ds_write_addtid_b32 v49 offset:4608\n\tds_write_addtid_b32 v50 offset:288\n\t" \
  "ds_write_addtid_b32 v51 offset:4896\n\tds_write_addtid_b32 v52 offset:576\n\t" \
  "ds_write_addtid_b32 v53 offset:5184\n\tds_write_addtid_b32 v54 offset:864\n\t" \
  "ds_write_addtid_b32 v55 offset:5472\n\tv_cvt_pk_f16_f32 v48, v68, v69\n\t" \
  "v_cvt_pk_f16_f32 v49, v70, v71\n\tv_pk_mul_f32 v[64:65], v[68:69], v[40:41] op_sel:[1,1] op_sel_hi:[1,0] neg_lo:[1,0]\n\t" \
  "v_pk_mul_f32 v[66:67], v[70:71], v[42:43] op_sel:[1,1] op_sel_hi:[1,0] neg_lo:[1,0]\n\tv_pk_fma_f32 v[64:65], v[68:69], v[40:41], v[64:65] op_sel:[0,0,0] op_sel_hi:[0,1,1]\n\t" \
  "v_pk_fma_f32 v[66:67], v[70:71], v[42:43], v[66:67] op_sel:[0,0,0] op_sel_hi:[0,1,1]\n\tv_pk_mul_f16 v50, v48, v86 op_sel:[1,1] op_sel_hi:[1,0]\n\t" \
  "v_pk_mul_f16 v51, v49, v87 op_sel:[1,1] op_sel_hi:[1,0]\n\tv_pk_fma_f16 v50, v48, v86, v50 op_sel:[0,0,0] op_sel_hi:[0,1,1] neg_hi:[0,1,0]\n\t" \
  "v_pk_fma_f16 v51, v49, v87, v51 op_sel:[0,0,0] op_sel_hi:[0,1,1] neg_hi:[0,1,0]\n\tv_pk_mul_f16 v52, v48, v46 op_sel:[1,1] op_sel_hi:[1,0] neg_lo:[1,0]\n\t" \
  "v_pk_mul_f16 v53, v49, v47 op_sel:[1,1] op_sel_hi:[1,0] neg_lo:[1,0]\n\tv_pk_fma_f16 v52, v48, v46, v52 op_sel:[0,0,0] op_sel_hi:[0,1,1]\n\t" \
  "v_pk_fma_f16 v53, v49, v47, v53 op_sel:[0,0,0] op_sel_hi:[0,1,1]\n\tv_pk_mul_f16 v54, v48, v56 op_sel:[1,1] op_sel_hi:[1,0] neg_lo:[1,0]\n\t" \
  "v_pk_mul_f16 v55, v49, v57 op_sel:[1,1] op_sel_hi:[1,0] neg_lo:[1,0]\n\tv_pk_fma_f16 v54, v48, v56, v54 op_sel:[0,0,0] op_sel_hi:[0,1,1]\n\t" \
  "v_pk_fma_f16 v55, v49, v57, v55 op_sel:[0,0,0] op_sel_hi:[0,1,1]\n\tds_write_addtid_b32 v48 offset:1152\n\t" \
  "ds_write_addtid_b32 v49 offset:5760\n\tds_write_addtid_b32 v50 offset:1440\n\t" \
  "ds_write_addtid_b32 v51 offset:6048\n\tds_write_addtid_b32 v52 offset:1728\n\t" \
  "ds_write_addtid_b32 v53 offset:6336\n\tds_write_addtid_b32 v54 offset:2016\n\t" \
  "ds_write_addtid_b32 v55 offset:6624\n\tv_cvt_pk_f16_f32 v48, v64, v65\n\t" \
  "v_cvt_pk_f16_f32 v49, v66, v67\n\tv_pk_mul_f32 v[68:69], v[64:65], v[40:41] op_sel:[1,1] op_sel_hi:[1,0] neg_lo:[1,0]\n\t" \
  "v_pk_mul_f32 v[70:71], v[66:67], v[42:43] op_sel:[1,1] op_sel_hi:[1,0] neg_lo:[1,0]\n\tv_pk_fma_f32 v[68:69], v[64:65], v[40:41], v[68:69] op_sel:[0,0,0] op_sel_hi:[0,1,1]\n\t" \
  "v_pk_fma_f32 v[70:71], v[66:67], v[42:43], v[70:71] op_sel:[0,0,0] op_sel_hi:[0,1,1]\n\tv_pk_mul_f16 v50, v48, v86 op_sel:[1,1] op_sel_hi:[1,0]\n\t" \
  "v_pk_mul_f16 v51, v49, v87 op_sel:[1,1] op_sel_hi:[1,0]\n\tv_pk_fma_f16 v50, v48, v86, v50 op_sel:[0,0,0] op_sel_hi:[0,1,1] neg_hi:[0,1,0]\n\t" \
  "v_pk_fma_f16 v51, v49, v87, v51 op_sel:[0,0,0] op_sel_hi:[0,1,1] neg_hi:[0,1,0]\n\tv_pk_mul_f16 v52, v48, v46 op_sel:[1,1] op_sel_hi:[1,0] neg_lo:[1,0]\n\t" \
  "v_pk_mul_f16 v53, v49, v47 op_sel:[1,1] op_sel_hi:[1,0] neg_lo:[1,0]\n\tv_pk_fma_f16 v52, v48, v46, v52 op_sel:[0,0,0] op_sel_hi:[0,1,1]\n\t" \
  "v_pk_fma_f16 v53, v49, v47, v53 op_sel:[0,0,0] op_sel_hi:[0,1,1]\n\tv_pk_mul_f16 v54, v48, v56 op_sel:[1,1] op_sel_hi:[1,0] neg_lo:[1,0]\n\t" \
  "v_pk_mul_f16 v55, v49, v57 op_sel:[1,1] op_sel_hi:[1,0] neg_lo:[1,0]\n\tv_pk_fma_f16 v54, v48, v56, v54 op_sel:[0,0,0] op_sel_hi:[0,1,1]\n\t" \
  "v_pk_fma_f16 v55, v49, v57, v55 op_sel:[0,0,0] op_sel_hi:[0,1,1]\n\tds_write_addtid_b32 v48 offset:2304\n\t" \
  "ds_write_addtid_b32 v49 offset:6912\n\tds_write_addtid_b32 v50 offset:2592\n\t" \
  "ds_write_addtid_b32 v51 offset:7200\n\tds_write_addtid_b32 v52 offset:2880\n\t" \
  "ds_write_addtid_b32 v53 offset:7488\n\tds_write_addtid_b32 v54 offset:3168\n\t" \
  "ds_write_addtid_b32 v55 offset:7776\n\tv_cvt_pk_f16_f32 v48, v68, v69\n\t" \
  "v_cvt_pk_f16_f32 v49, v70, v71\n\tv_pk_mul_f16 v50, v48, v86 op_sel:[1,1] op_sel_hi:[1,0]\n\t" \
  "v_pk_mul_f16 v51, v49, v87 op_sel:[1,1] op_sel_hi:[1,0]\n\tv_pk_fma_f16 v50, v48, v86, v50 op_sel:[0,0,0] op_sel_hi:[0,1,1] neg_hi:[0,1,0]\n\t" \
  "v_pk_fma_f16 v51, v49, v87, v51 op_sel:[0,0,0] op_sel_hi:[0,1,1] neg_hi:[0,1,0]\n\tv_pk_mul_f16 v52, v48, v46 op_sel:[1,1] op_sel_hi:[1,0] neg_lo:[1,0]\n\t" \
  "v_pk_mul_f16 v53, v49, v47 op_sel:[1,1] op_sel_hi:[1,0] neg_lo:[1,0]\n\tv_pk_fma_f16 v52, v48, v46, v52 op_sel:[0,0,0] op_sel_hi:[0,1,1]\n\t" \
  "v_pk_fma_f16 v53, v49, v47, v53 op_sel:[0,0,0] op_sel_hi:[0,1,1]\n\tv_pk_mul_f16 v54, v48, v56 op_sel:[1,1] op_sel_hi:[1,0] neg_lo:[1,0]\n\t" \
  "v_pk_mul_f16 v55, v49, v57 op_sel:[1,1] op_sel_hi:[1,0] neg_lo:[1,0]\n\tv_pk_fma_f16 v54, v48, v56, v54 op_sel:[0,0,0] op_sel_hi:[0,1,1]\n\t" \
  "v_pk_fma_f16 v55, v49, v57, v55 op_sel:[0,0,0] op_sel_hi:[0,1,1]\n\tds_write_addtid_b32 v48 offset:3456\n\t" \
  "ds_write_addtid_b32 v49 offset:8064\n\tds_write_addtid_b32 v50 offset:3744\n\t" \
  "ds_write_addtid_b32 v51 offset:8352\n\tds_write_addtid_b32 v52 offset:4032\n\t" \
  "ds_write_addtid_b32 v53 offset:8640\n\tds_write_addtid_b32 v54 offset:4320\n\t" \
  "ds_write_addtid_b32 v55 offset:8928\n\tds_read_b128 v[72:75], v27 offset:0\n\t" \
  "ds_read_b128 v[76:79], v27 offset:64\n\tds_read_b128 v[80:83], v27 offset:128\n\t" \
  "ds_read_b128 v[84:87], v27 offset:192\n\tds_read_b128 v[88:91], v27 offset:4608\n\t" \
  "ds_read_b128 v[92:95], v27 offset:4672\n\tds_read_b128 v[96:99], v27 offset:4736\n\t" \
  "ds_read_b128 v[100:103], v27 offset:4800\n\tds_write_addtid_b32 v36 offset:0\n\t" \
  "ds_write_addtid_b32 v37 offset:4608\n\tds_read_b128 v[40:43], v12 offset:0\n\t" \
  "ds_read_b128 v[44:47], v12 offset:64\n\tds_read_b128 v[48:51], v12 offset:128\n\t" \
  "v_add_u32_sdwa v56, %[w], v16 dst_sel:DWORD dst_unused:UNUSED_PAD src0_sel:DWORD src1_sel:WORD_0\n\tv_add_u32_sdwa v57, %[w], v16 dst_sel:DWORD dst_unused:UNUSED_PAD src0_sel:DWORD src1_sel:WORD_1\n\t" \
  "v_add_u32_sdwa v58, %[w], v17 dst_sel:DWORD dst_unused:UNUSED_PAD src0_sel:DWORD src1_sel:WORD_0\n\tv_add_u32_sdwa v59, %[w], v17 dst_sel:DWORD dst_unused:UNUSED_PAD src0_sel:DWORD src1_sel:WORD_1\n\t" \
  "v_add_u32 v14, v56, v35\n\tv_add_u32 v15, v57, v35\n\t" \
  "s_waitcnt lgkmcnt(12)\n\tv_mfma_f32_16x16x32_f16 v[60:63], v[104:107], v[72:75], 0\n\t" \
  "v_mfma_f32_16x16x32_f16 v[4:7], v[136:139], v[72:75], 0\n\ts_cmp_lt_u32 %[w], 50176\n\t" \
  "s_cbranch_scc1 1f\n\ts_setprio 1\n\t" \
  "1:\n\ts_waitcnt lgkmcnt(2)\n\t" \
  "v_mfma_f32_16x16x32_f16 v[60:63], v[108:111], v[76:79], v[60:63]\n\tv_pk_mul_f16 v52, v40, v72 op_sel:[1,1] op_sel_hi:[1,0] neg_lo:[1,0]\n\t" \
  "v_pk_mul_f16 v53, v41, v73 op_sel:[1,1] op_sel_hi:[1,0] neg_lo:[1,0]\n\tv_pk_mul_f16 v54, v42, v74 op_sel:[1,1] op_sel_hi:[1,0] neg_lo:[1,0]\n\t" \
  "v_pk_mul_f16 v55, v43, v75 op_sel:[1,1] op_sel_hi:[1,0] neg_lo:[1,0]\n\tv_mfma_f32_16x16x32_f16 v[4:7], v[140:143], v[76:79], v[4:7]\n\t" \
  "v_pk_fma_f16 v40, v40, v72, v52 op_sel:[0,0,0] op_sel_hi:[0,1,1]\n\tv_pk_fma_f16 v41, v41, v73, v53 op_sel:[0,0,0] op_sel_hi:[0,1,1]\n\t" \
  "v_pk_fma_f16 v42, v42, v74, v54 op_sel:[0,0,0] op_sel_hi:[0,1,1]\n\tv_pk_fma_f16 v43, v43, v75, v55 op_sel:[0,0,0] op_sel_hi:[0,1,1]\n\t" \
  "s_waitcnt lgkmcnt(1)\n\tv_mfma_f32_16x16x32_f16 v[60:63], v[112:115], v[80:83], v[60:63]\n\t" \
  "ds_read_b128 v[72:75], v12 offset:192\n\tv_pk_mul_f16 v52, v44, v76 op_sel:[1,1] op_sel_hi:[1,0] neg_lo:[1,0]\n\t" \
  "v_pk_mul_f16 v53, v45, v77 op_sel:[1,1] op_sel_hi:[1,0] neg_lo:[1,0]\n\tv_pk_mul_f16 v54, v46, v78 op_sel:[1,1] op_sel_hi:[1,0] neg_lo:[1,0]\n\t" \
  "v_pk_mul_f16 v55, v47, v79 op_sel:[1,1] op_sel_hi:[1,0] neg_lo:[1,0]\n\tv_mfma_f32_16x16x32_f16 v[4:7], v[144:147], v[80:83], v[4:7]\n\t" \
  "v_pk_fma_f16 v44, v44, v76, v52 op_sel:[0,0,0] op_sel_hi:[0,1,1]\n\tv_pk_fma_f16 v45, v45, v77, v53 op_sel:[0,0,0] op_sel_hi:[0,1,1]\n\t" \
  "v_pk_fma_f16 v46, v46, v78, v54 op_sel:[0,0,0] op_sel_hi:[0,1,1]\n\tv_pk_fma_f16 v47, v47, v79, v55 op_sel:[0,0,0] op_sel_hi:[0,1,1]\n\t" \
  "s_waitcnt lgkmcnt(1)\n\tv_mfma_f32_16x16x32_f16 v[60:63], v[116:119], v[84:87], v[60:63]\n\t" \
  "ds_read_b128 v[76:79], v12 offset:4608\n\tv_pk_mul_f16 v52, v48, v80 op_sel:[1,1] op_sel_hi:[1,0] neg_lo:[1,0]\n\t" \
  "v_pk_mul_f16 v53, v49, v81 op_sel:[1,1] op_sel_hi:[1,0] neg_lo:[1,0]\n\tv_pk_mul_f16 v54, v50, v82 op_sel:[1,1] op_sel_hi:[1,0] neg_lo:[1,0]\n\t" \
  "v_pk_mul_f16 v55, v51, v83 op_sel:[1,1] op_sel_hi:[1,0] neg_lo:[1,0]\n\tv_mfma_f32_16x16x32_f16 v[4:7], v[148:151], v[84:87], v[4:7]\n\t" \
  "v_pk_fma_f16 v48, v48, v80, v52 op_sel:[0,0,0] op_sel_hi:[0,1,1]\n\tv_pk_fma_f16 v49, v49, v81, v53 op_sel:[0,0,0] op_sel_hi:[0,1,1]\n\t" \
  "v_pk_fma_f16 v50, v50, v82, v54 op_sel:[0,0,0] op_sel_hi:[0,1,1]\n\tv_pk_fma_f16 v51, v51, v83, v55 op_sel:[0,0,0] op_sel_hi:[0,1,1]\n\t" \
  "s_waitcnt lgkmcnt(1)\n\tv_mfma_f32_16x16x32_f16 v[60:63], v[120:123], v[88:91], v[60:63]\n\t" \
  "ds_read_b128 v[80:83], v12 offset:4672\n\tv_pk_mul_f16 v52, v72, v84 op_sel:[1,1] op_sel_hi:[1,0] neg_lo:[1,0]\n\t" \
  "v_pk_mul_f16 v53, v73, v85 op_sel:[1,1] op_sel_hi:[1,0] neg_lo:[1,0]\n\tv_pk_mul_f16 v54, v74, v86 op_sel:[1,1] op_sel_hi:[1,0] neg_lo:[1,0]\n\t" \
  "v_pk_mul_f16 v55, v75, v87 op_sel:[1,1] op_sel_hi:[1,0] neg_lo:[1,0]\n\tv_mfma_f32_16x16x32_f16 v[4:7], v[152:155], v[88:91], v[4:7]\n\t" \
  "v_pk_fma_f16 v72, v72, v84, v52 op_sel:[0,0,0] op_sel_hi:[0,1,1]\n\tv_pk_fma_f16 v73, v73, v85, v53 op_sel:[0,0,0] op_sel_hi:[0,1,1]\n\t" \
  "v_pk_fma_f16 v74, v74, v86, v54 op_sel:[0,0,0] op_sel_hi:[0,1,1]\n\tv_pk_fma_f16 v75, v75, v87, v55 op_sel:[0,0,0] op_sel_hi:[0,1,1]\n\t" \
  "s_waitcnt lgkmcnt(1)\n\tv_mfma_f32_16x16x32_f16 v[60:63], v[124:127], v[92:95], v[60:63]\n\t" \
  "ds_read_b128 v[84:87], v12 offset:4736\n\tv_pk_mul_f16 v52, v76, v88 op_sel:[1,1] op_sel_hi:[1,0] neg_lo:[1,0]\n\t" \
  "v_pk_mul_f16 v53, v77, v89 op_sel:[1,1] op_sel_hi:[1,0] neg_lo:[1,0]\n\tv_pk_mul_f16 v54, v78, v90 op_sel:[1,1] op_sel_hi:[1,0] neg_lo:[1,0]\n\t" \
  "v_pk_mul_f16 v55, v79, v91 op_sel:[1,1] op_sel_hi:[1,0] neg_lo:[1,0]\n\tv_mfma_f32_16x16x32_f16 v[4:7], v[156:159], v[92:95], v[4:7]\n\t" \
  "v_pk_fma_f16 v76, v76, v88, v52 op_sel:[0,0,0] op_sel_hi:[0,1,1]\n\tv_pk_fma_f16 v77, v77, v89, v53 op_sel:[0,0,0] op_sel_hi:[0,1,1]\n\t" \
  "v_pk_fma_f16 v78, v78, v90, v54 op_sel:[0,0,0] op_sel_hi:[0,1,1]\n\tv_pk_fma_f16 v79, v79, v91, v55 op_sel:[0,0,0] op_sel_hi:[0,1,1]\n\t" \
  "s_waitcnt lgkmcnt(1)\n\tv_mfma_f32_16x16x32_f16 v[60:63], v[128:131], v[96:99], v[60:63]\n\t" \
  "ds_read_b128 v[88:91], v12 offset:4800\n\tv_pk_mul_f16 v52, v80, v92 op_sel:[1,1] op_sel_hi:[1,0] neg_lo:[1,0]\n\t" \
  "v_pk_mul_f16 v53, v81, v93 op_sel:[1,1] op_sel_hi:[1,0] neg_lo:[1,0]\n\tv_pk_mul_f16 v54, v82, v94 op_sel:[1,1] op_sel_hi:[1,0] neg_lo:[1,0]\n\t" \
  "v_pk_mul_f16 v55, v83, v95 op_sel:[1,1] op_sel_hi:[1,0] neg_lo:[1,0]\n\tv_mfma_f32_16x16x32_f16 v[4:7], v[160:163], v[96:99], v[4:7]\n\t" \
  "v_pk_fma_f16 v80, v80, v92, v52 op_sel:[0,0,0] op_sel_hi:[0,1,1]\n\tv_pk_fma_f16 v81, v81, v93, v53 op_sel:[0,0,0] op_sel_hi:[0,1,1]\n\t" \
  "v_pk_fma_f16 v82, v82, v94, v54 op_sel:[0,0,0] op_sel_hi:[0,1,1]\n\tv_pk_fma_f16 v83, v83, v95, v55 op_sel:[0,0,0] op_sel_hi:[0,1,1]\n\t" \
  "s_waitcnt lgkmcnt(1)\n\tv_mfma_f32_16x16x32_f16 v[60:63], v[132:135], v[100:103], v[60:63]\n\t" \
  "v_pk_mul_f16 v52, v84, v96 op_sel:[1,1] op_sel_hi:[1,0] neg_lo:[1,0]\n\tv_pk_mul_f16 v53, v85, v97 op_sel:[1,1] op_sel_hi:[1,0] neg_lo:[1,0]\n\t" \
  "v_pk_mul_f16 v54, v86, v98 op_sel:[1,1] op_sel_hi:[1,0] neg_lo:[1,0]\n\tv_pk_mul_f16 v55, v87, v99 op_sel:[1,1] op_sel_hi:[1,0] neg_lo:[1,0]\n\t" \
  "v_mfma_f32_16x16x32_f16 v[4:7], v[164:167], v[100:103], v[4:7]\n\tv_pk_fma_f16 v84, v84, v96, v52 op_sel:[0,0,0] op_sel_hi:[0,1,1]\n\t" \
  "v_pk_fma_f16 v85, v85, v97, v53 op_sel:[0,0,0] op_sel_hi:[0,1,1]\n\tv_pk_fma_f16 v86, v86, v98, v54 op_sel:[0,0,0] op_sel_hi:[0,1,1]\n\t" \
  "v_pk_fma_f16 v87, v87, v99, v55 op_sel:[0,0,0] op_sel_hi:[0,1,1]\n\ts_waitcnt lgkmcnt(0)\n\t" \
  "v_mfma_f32_16x16x32_f16 v[8:11], v[136:139], v[40:43], 0\n\tv_pk_mul_f16 v52, v88, v100 op_sel:[1,1] op_sel_hi:[1,0] neg_lo:[1,0]\n\t" \
  "v_pk_mul_f16 v53, v89, v101 op_sel:[1,1] op_sel_hi:[1,0] neg_lo:[1,0]\n\tv_pk_mul_f16 v54, v90, v102 op_sel:[1,1] op_sel_hi:[1,0] neg_lo:[1,0]\n\t" \
  "v_mfma_f32_16x16x32_f16 v[64:67], v[104:107], v[40:43], 0\n\tv_pk_mul_f16 v55, v91, v103 op_sel:[1,1] op_sel_hi:[1,0] neg_lo:[1,0]\n\t" \
  "v_pk_fma_f16 v88, v88, v100, v52 op_sel:[0,0,0] op_sel_hi:[0,1,1]\n\tv_pk_fma_f16 v89, v89, v101, v53 op_sel:[0,0,0] op_sel_hi:[0,1,1]\n\t" \
  "v_mfma_f32_16x16x32_f16 v[8:11], v[140:143], v[44:47], v[8:11]\n\tv_pk_fma_f16 v90, v90, v102, v54 op_sel:[0,0,0] op_sel_hi:[0,1,1]\n\t" \
  "v_pk_fma_f16 v91, v91, v103, v55 op_sel:[0,0,0] op_sel_hi:[0,1,1]\n\tv_mov_b64 v[100:101], 0\n\t" \
  "v_mfma_f32_16x16x32_f16 v[64:67], v[108:111], v[44:47], v[64:67]\n\tv_mov_b64 v[102:103], 0\n\t" \
  "v_cvt_pk_f16_f32 v4, v4, v5\n\tv_cvt_pk_f16_f32 v5, v6, v7\n\t" \
  "v_mfma_f32_16x16x32_f16 v[8:11], v[144:147], v[48:51], v[8:11]\n\tv_cvt_pk_f16_f32 v92, v60, v61\n\t" \
  "v_cvt_pk_f16_f32 v93, v62, v63\n\tv_permlane16_swap_b32 v4, v5\n\t" \
  "v_mfma_f32_16x16x32_f16 v[64:67], v[112:115], v[48:51], v[64:67]\n\tv_perm_b32 v6, v4, v4, v33\n\t" \
  "v_perm_b32 v7, v4, v4, v34\n\tv_mfma_f32_16x16x32_f16 v[8:11], v[148:151], v[72:75], v[8:11]\n\t" \
  "ds_write_b128 v58, v[100:103]\n\tv_mfma_f32_16x16x32_f16 v[64:67], v[116:119], v[72:75], v[64:67]\n\t" \
  "ds_write2_b32 v56, v92, v93 offset0:193 offset1:194\n\tv_mfma_f32_16x16x32_f16 v[8:11], v[152:155], v[76:79], v[8:11]\n\t" \
  "ds_write2_b32 v57, v92, v93 offset0:193 offset1:194\n\tv_mfma_f32_16x16x32_f16 v[64:67], v[120:123], v[76:79], v[64:67]\n\t" \
  "ds_write2_b32 v14, v6, v7 offset0:64 offset1:65\n\tv_mfma_f32_16x16x32_f16 v[8:11], v[156:159], v[80:83], v[8:11]\n\t" \
  "ds_write2_b32 v15, v6, v7 offset0:64 offset1:65\n\tv_mfma_f32_16x16x32_f16 v[64:67], v[124:127], v[80:83], v[64:67]\n\t" \
  "ds_write_b16 v59, v5 offset:48\n\tv_mfma_f32_16x16x32_f16 v[8:11], v[160:163], v[84:87], v[8:11]\n\t" \
  "ds_write_b16_d16_hi v59, v5 offset:16\n\tv_mfma_f32_16x16x32_f16 v[64:67], v[128:131], v[84:87], v[64:67]\n\t" \
  "ds_write_b16 v59, v5 offset:878\n\tv_mfma_f32_16x16x32_f16 v[8:11], v[164:167], v[88:91], v[8:11]\n\t" \
  "ds_write_b16_d16_hi v59, v5 offset:846\n\tv_mfma_f32_16x16x32_f16 v[64:67], v[132:135], v[88:91], v[64:67]\n\t" \
  "ds_write_b16 v59, v5 offset:1708\n\tds_write_b16_d16_hi v59, v5 offset:1676\n\t" \
  "ds_write_b16 v59, v5 offset:2538\n\tds_write_b16_d16_hi v59, v5 offset:2506\n\t" \
  "s_nop 1\n\tv_cvt_pk_f16_f32 v8, v8, v9\n\t" \
  "v_cvt_pk_f16_f32 v9, v10, v11\n\tv_cvt_pk_f16_f32 v46, v64, v65\n\t" \
  "v_cvt_pk_f16_f32 v47, v66, v67\n\tv_permlane16_swap_b32 v8, v9\n\t" \
  "v_perm_b32 v10, v8, v8, v33\n\tv_perm_b32 v11, v8, v8, v34\n\t" \
  "ds_write2_b32 v56, v46, v47 offset0:129 offset1:130\n\tds_write2_b32 v57, v46, v47 offset0:129 offset1:130\n\t" \
  "ds_write2_b32 v14, v10, v11 offset0:0 offset1:1\n\tds_write2_b32 v15, v10, v11 offset0:0 offset1:1\n\t" \
  "ds_write_b16 v59, v9 offset:32\n\tds_write_b16_d16_hi v59, v9 offset:0\n\t" \
  "ds_write_b16 v59, v9 offset:862\n\tds_write_b16_d16_hi v59, v9 offset:830\n\t" \
  "ds_write_b16 v59, v9 offset:1692\n\tds_write_b16_d16_hi v59, v9 offset:1660\n\t" \
  "ds_write_b16 v59, v9 offset:2522\n\tds_write_b16_d16_hi v59, v9 offset:2490\n\t"
#define GEN_CLOBBER "v4", "v5", "v6", "v7", "v10", "v11", "v13", "v14", "v15", "v36", "v37", "v38", "v39", "v40", "v41", "v42", "v43", "v44", "v45", "v47", "v48", "v49", "v50", "v51", "v52", "v53", "v54", "v55", "v56", "v57", "v58", "v59", "v60", "v61", "v62", "v63", "v64", "v65", "v66", "v67", "v72", "v73", "v74", "v75", "v76", "v77", "v78", "v79", "v80", "v81", "v82", "v83", "v84", "v85", "v86", "v87", "v88", "v89", "v90", "v91", "v92", "v94", "v95", "v98", "v99", "v100", "v101", "v102", "v103", "v104", "v105", "v106", "v107", "v108", "v109", "v110", "v111", "v112", "v113", "v114", "v115", "v116", "v117", "v118", "v119", "v120", "v121", "v122", "v123", "v124", "v125", "v126", "v127", "v128", "v129", "v130", "v131", "v132", "v133", "v134", "v135", "v136", "v137", "v138", "v139", "v140", "v141", "v142", "v143", "v144", "v145", "v146", "v147", "v148", "v149", "v150", "v151", "v152", "v153", "v154", "v155", "v156", "v157", "v158", "v159", "v160", "v161", "v162", "v163", "v164", "v165", "v166", "v167"
#define CONV_ASM \
  "ds_read_b32 v34, v30\n\tds_read_b64 v[132:133], v26 offset:512\n\tds_read_b64 v[134:135], v26 offset:520\n\t" \
  "ds_read_b64 v[100:101], v26 offset:0\n\tds_read_b64 v[102:103], v26 offset:8\n\tds_read_b64 v[136:137], v26 offset:576\n\t" \
  "ds_read_b64 v[138:139], v26 offset:584\n\tds_read_b64 v[104:105], v26 offset:64\n\tds_read_b64 v[106:107], v26 offset:72\n\t" \
  "ds_read_b64 v[140:141], v26 offset:640\n\tds_read_b64 v[142:143], v26 offset:648\n\tds_read_b64 v[108:109], v26 offset:128\n\t" \
  "ds_read_b64 v[110:111], v26 offset:136\n\tds_read_b64 v[144:145], v26 offset:704\n\tds_read_b64 v[146:147], v26 offset:712\n\t" \
  "ds_read_b64 v[112:113], v26 offset:192\n\tds_read_b64 v[114:115], v26 offset:200\n\tds_read_b64 v[148:149], v26 offset:768\n\t" \
  "ds_read_b64 v[150:151], v26 offset:776\n\tds_read_b64 v[116:117], v26 offset:256\n\tds_read_b64 v[118:119], v26 offset:264\n\t" \
  TS(60, 61) "s_waitcnt lgkmcnt(15)\n\tv_cmp_gt_u32 vcc, 1, v34\n\t" \
  "s_cbranch_vccz 2f\n\ts_mov_b32 s6, 0x10000\n\t1:\n\t" \
  "s_sleep 1\n\tds_read_b32 v34, v30\n\ts_sub_u32 s6, s6, 1\n\t" \
  "s_waitcnt lgkmcnt(0)\n\tv_cmp_gt_u32 vcc, 1, v34\n\ts_cbranch_scc1 2f\n\t" \
  "s_cbranch_vccnz 1b\n\t2:\n\t" TS(62, 63) \
  "s_cmp_lt_u32 %[w], 4\n\ts_setprio 1\n\ts_cbranch_scc1 1f\n\t" \
  "s_setprio 2\n\t1:\n\tds_read_b128 v[0:3], v28 offset:0\n\t" \
  "ds_read_b128 v[4:7], v28 offset:64\n\tds_read_b128 v[8:11], v28 offset:128\n\tds_read_b128 v[12:15], v28 offset:192\n\t" \
  "ds_read_b128 v[16:19], v28 offset:256\n\tds_read_b128 v[20:23], v28 offset:320\n\ts_waitcnt lgkmcnt(5)\n\t" \
  "v_mfma_f32_16x16x32_f16 v[52:55], v[132:135], v[0:3], 0\n\tds_read_b64 v[152:153], v26 offset:832\n\tds_read_b64 v[154:155], v26 offset:840\n\t" \
  "v_mfma_f32_16x16x32_f16 v[56:59], v[100:103], v[0:3], 0\n\tds_read_b64 v[120:121], v26 offset:320\n\tds_read_b64 v[122:123], v26 offset:328\n\t" \
  "ds_read_b128 v[0:3], v28 offset:384\n\ts_waitcnt lgkmcnt(9)\n\tv_mfma_f32_16x16x32_f16 v[52:55], v[136:139], v[4:7], v[52:55]\n\t" \
  "ds_read_b64 v[156:157], v26 offset:896\n\tds_read_b64 v[158:159], v26 offset:904\n\tv_mfma_f32_16x16x32_f16 v[56:59], v[104:107], v[4:7], v[56:59]\n\t" \
  "ds_read_b64 v[124:125], v26 offset:384\n\tds_read_b64 v[126:127], v26 offset:392\n\tds_read_b128 v[4:7], v28 offset:448\n\t" \
  "s_waitcnt lgkmcnt(13)\n\tv_mfma_f32_16x16x32_f16 v[52:55], v[140:143], v[8:11], v[52:55]\n\tds_read_b64 v[160:161], v26 offset:960\n\t" \
  "ds_read_b64 v[162:163], v26 offset:968\n\tv_mfma_f32_16x16x32_f16 v[56:59], v[108:111], v[8:11], v[56:59]\n\tds_read_b64 v[128:129], v26 offset:448\n\t" \
  "ds_read_b64 v[130:131], v26 offset:456\n\tds_read_b128 v[8:11], v28 offset:512\n\ts_waitcnt lgkmcnt(15)\n\t" \
  "v_mfma_f32_16x16x32_f16 v[52:55], v[144:147], v[12:15], v[52:55]\n\tds_read_b64 v[164:165], v26 offset:1024\n\tds_read_b64 v[166:167], v26 offset:1032\n\t" \
  "v_mfma_f32_16x16x32_f16 v[56:59], v[112:115], v[12:15], v[56:59]\n\tds_read_b64 v[76:77], v31 offset:72\n\tds_read_b64 v[78:79], v31 offset:176\n\t" \
  "ds_read_b128 v[12:15], v28 offset:576\n\tv_mfma_f32_16x16x32_f16 v[52:55], v[148:151], v[16:19], v[52:55]\n\tds_read_b64 v[80:81], v31 offset:40\n\t" \
  "ds_read_b64 v[82:83], v31 offset:144\n\tv_mfma_f32_16x16x32_f16 v[56:59], v[116:119], v[16:19], v[56:59]\n\tds_read_b64 v[84:85], v31 offset:8\n\t" \
  "ds_read_b64 v[86:87], v31 offset:112\n\tds_read_b128 v[16:19], v28 offset:640\n\ts_waitcnt lgkmcnt(15)\n\t" \
  "v_mfma_f32_16x16x32_f16 v[52:55], v[152:155], v[20:23], v[52:55]\n\tds_read_b64 v[88:89], v31 offset:280\n\tds_read_b64 v[90:91], v31 offset:384\n\t" \
  "v_mfma_f32_16x16x32_f16 v[56:59], v[120:123], v[20:23], v[56:59]\n\tds_read_b64 v[92:93], v31 offset:248\n\tds_read_b64 v[94:95], v31 offset:352\n\t" \
  "ds_read_b128 v[20:23], v28 offset:704\n\tv_mfma_f32_16x16x32_f16 v[52:55], v[156:159], v[0:3], v[52:55]\n\tds_read_b64 v[96:97], v31 offset:216\n\t" \
  "ds_read_b64 v[98:99], v31 offset:320\n\tv_mfma_f32_16x16x32_f16 v[56:59], v[124:127], v[0:3], v[56:59]\n\tds_read_b128 v[0:3], v28 offset:768\n\t" \
  "s_waitcnt lgkmcnt(15)\n\tv_mfma_f32_16x16x32_f16 v[52:55], v[160:163], v[4:7], v[52:55]\n\tv_mfma_f32_16x16x32_f16 v[56:59], v[128:131], v[4:7], v[56:59]\n\t" \
  "ds_read_b128 v[4:7], v28 offset:832\n\tv_mfma_f32_16x16x32_f16 v[52:55], v[164:167], v[8:11], v[52:55]\n\tv_mfma_f32_16x16x32_f16 v[56:59], v[132:135], v[8:11], v[56:59]\n\t" \
  "v_mfma_f32_16x16x32_f16 v[60:63], v[100:103], v[8:11], 0\n\tds_read_b128 v[8:11], v28 offset:896\n\ts_waitcnt lgkmcnt(15)\n\t" \
  "v_mfma_f32_16x16x32_f16 v[56:59], v[136:139], v[12:15], v[56:59]\n\tds_read_b32 v34, v30\n\tv_mfma_f32_16x16x32_f16 v[60:63], v[104:107], v[12:15], v[60:63]\n\t" \
  "ds_read_b128 v[12:15], v28 offset:960\n\tv_cvt_pk_bf16_f32 v52, v52, v53\n\tv_cvt_pk_bf16_f32 v53, v54, v55\n\t" \
  TS(64, 65) "s_waitcnt lgkmcnt(12)\n\tv_mfma_f32_16x16x32_f16 v[56:59], v[140:143], v[16:19], v[56:59]\n\t" \
  "ds_write_b64 v29, v[52:53] offset:0\n\tds_write_b32 v25, v25 offset:0\n\tv_mfma_f32_16x16x32_f16 v[60:63], v[108:111], v[16:19], v[60:63]\n\t" \
  "s_waitcnt lgkmcnt(3)\n\tv_cmp_gt_u32 vcc, 2, v34\n\ts_cbranch_vccz 2f\n\t" \
  "s_mov_b32 s6, 0x10000\n\t1:\n\ts_sleep 1\n\t" \
  "ds_read_b32 v34, v30\n\ts_sub_u32 s6, s6, 1\n\ts_waitcnt lgkmcnt(0)\n\t" \
  "v_cmp_gt_u32 vcc, 2, v34\n\ts_cbranch_scc1 2f\n\ts_cbranch_vccnz 1b\n\t" \
  "2:\n\tds_read_b128 v[16:19], v28 offset:1024\n\tv_mfma_f32_16x16x32_f16 v[56:59], v[144:147], v[20:23], v[56:59]\n\t" \
  "v_mfma_f32_16x16x32_f16 v[60:63], v[112:115], v[20:23], v[60:63]\n\tds_read_b128 v[68:71], v32\n\tds_read_b128 v[72:75], v32 offset:1024\n\t" \
  "ds_read_b128 v[20:23], v28 offset:1088\n\tv_mfma_f32_16x16x32_f16 v[56:59], v[148:151], v[0:3], v[56:59]\n\tv_mfma_f32_16x16x32_f16 v[60:63], v[116:119], v[0:3], v[60:63]\n\t" \
  "ds_read_b128 v[0:3], v28 offset:496\n\tv_mfma_f32_16x16x32_f16 v[56:59], v[152:155], v[4:7], v[56:59]\n\tv_mfma_f32_16x16x32_f16 v[60:63], v[120:123], v[4:7], v[60:63]\n\t" \
  "ds_read_b128 v[4:7], v28 offset:1152\n\tv_mfma_f32_16x16x32_f16 v[56:59], v[156:159], v[8:11], v[56:59]\n\tv_mfma_f32_16x16x32_f16 v[60:63], v[124:127], v[8:11], v[60:63]\n\t" \
  "ds_read_b128 v[8:11], v28 offset:1216\n\ts_waitcnt lgkmcnt(9)\n\tv_mfma_f32_16x16x32_f16 v[56:59], v[160:163], v[12:15], v[56:59]\n\t" \
  "v_mfma_f32_16x16x32_f16 v[60:63], v[128:131], v[12:15], v[60:63]\n\tds_read_b128 v[12:15], v28 offset:480\n\ts_waitcnt lgkmcnt(7)\n\t" \
  "v_mfma_f32_16x16x32_f16 v[56:59], v[164:167], v[16:19], v[56:59]\n\tv_mfma_f32_16x16x32_f16 v[60:63], v[132:135], v[16:19], v[60:63]\n\tv_mfma_f32_16x16x32_f16 v[64:67], v[100:103], v[16:19], 0\n\t" \
  "ds_read_b128 v[16:19], v28 offset:1280\n\ts_waitcnt lgkmcnt(5)\n\tv_mfma_f32_16x16x32_f16 v[60:63], v[136:139], v[20:23], v[60:63]\n\t" \
  "v_mfma_f32_16x16x32_f16 v[64:67], v[104:107], v[20:23], v[64:67]\n\tds_read_b128 v[20:23], v28 offset:1344\n\ts_waitcnt lgkmcnt(5)\n\t" \
  "v_mfma_f32_16x16x32_f16 v[36:39], v[0:3], v[68:71], 0\n\tv_cvt_pk_bf16_f32 v56, v56, v57\n\tv_cvt_pk_bf16_f32 v57, v58, v59\n\t" \
  "v_mfma_f32_16x16x32_f16 v[40:43], v[0:3], v[72:75], 0\n\tds_write_b64 v29, v[56:57] offset:512\n\tds_write_b32 v25, v25 offset:64\n\t" \
  "ds_read_b128 v[0:3], v28 offset:1408\n\ts_waitcnt lgkmcnt(7)\n\tv_mfma_f32_16x16x32_f16 v[60:63], v[140:143], v[4:7], v[60:63]\n\t" \
  "v_cvt_pk_f16_f32 v36, v36, v37\n\tv_cvt_pk_f16_f32 v37, v38, v39\n\tv_mfma_f32_16x16x32_f16 v[64:67], v[108:111], v[4:7], v[64:67]\n\t" \
  "ds_read_b128 v[4:7], v28 offset:1472\n\tv_cvt_pk_f16_f32 v38, v40, v41\n\tv_cvt_pk_f16_f32 v39, v42, v43\n\t" \
  "s_waitcnt lgkmcnt(7)\n\tv_mfma_f32_16x16x32_f16 v[60:63], v[144:147], v[8:11], v[60:63]\n\tv_mfma_f32_16x16x32_f16 v[64:67], v[112:115], v[8:11], v[64:67]\n\t" \
  "ds_read_b128 v[8:11], v28 offset:1536\n\tv_mfma_f32_16x16x32_f16 v[60:63], v[36:39], v[76:79], v[60:63]\n\tv_mfma_f32_16x16x32_f16 v[64:67], v[36:39], v[80:83], v[64:67]\n\t" \
  "v_mfma_f32_16x16x32_f16 v[52:55], v[36:39], v[84:87], 0\n\ts_waitcnt lgkmcnt(7)\n\tv_mfma_f32_16x16x32_f16 v[44:47], v[12:15], v[68:71], 0\n\t" \
  "v_mfma_f32_16x16x32_f16 v[40:43], v[12:15], v[72:75], 0\n\tds_read_b128 v[12:15], v28 offset:1600\n\ts_waitcnt lgkmcnt(7)\n\t" \
  "v_mfma_f32_16x16x32_f16 v[60:63], v[148:151], v[16:19], v[60:63]\n\tv_mfma_f32_16x16x32_f16 v[64:67], v[116:119], v[16:19], v[64:67]\n\tds_read_b128 v[16:19], v28 offset:1008\n\t" \
  "s_waitcnt lgkmcnt(7)\n\tv_mfma_f32_16x16x32_f16 v[60:63], v[152:155], v[20:23], v[60:63]\n\tv_cvt_pk_f16_f32 v44, v44, v45\n\t" \
  "v_cvt_pk_f16_f32 v45, v46, v47\n\tv_cvt_pk_f16_f32 v46, v40, v41\n\tv_cvt_pk_f16_f32 v47, v42, v43\n\t" \
  "v_mfma_f32_16x16x32_f16 v[64:67], v[120:123], v[20:23], v[64:67]\n\tds_read_b128 v[20:23], v28 offset:1664\n\tv_mfma_f32_16x16x32_f16 v[60:63], v[44:47], v[88:91], v[60:63]\n\t" \
  "v_mfma_f32_16x16x32_f16 v[64:67], v[44:47], v[92:95], v[64:67]\n\tv_mfma_f32_16x16x32_f16 v[52:55], v[44:47], v[96:99], v[52:55]\n\ts_waitcnt lgkmcnt(5)\n\t" \
  "v_mfma_f32_16x16x32_f16 v[60:63], v[156:159], v[0:3], v[60:63]\n\tv_mfma_f32_16x16x32_f16 v[64:67], v[124:127], v[0:3], v[64:67]\n\tds_read_b128 v[0:3], v28 offset:1728\n\t" \
  "s_waitcnt lgkmcnt(5)\n\tv_mfma_f32_16x16x32_f16 v[60:63], v[160:163], v[4:7], v[60:63]\n\tv_mfma_f32_16x16x32_f16 v[64:67], v[128:131], v[4:7], v[64:67]\n\t" \
  "ds_read_b128 v[4:7], v28 offset:992\n\ts_waitcnt lgkmcnt(5)\n\tv_mfma_f32_16x16x32_f16 v[60:63], v[164:167], v[8:11], v[60:63]\n\t" \
  "v_mfma_f32_16x16x32_f16 v[64:67], v[132:135], v[8:11], v[64:67]\n\tv_mfma_f32_16x16x32_f16 v[52:55], v[100:103], v[8:11], v[52:55]\n\tds_read_b128 v[8:11], v28 offset:1792\n\t" \
  "s_waitcnt lgkmcnt(5)\n\tv_mfma_f32_16x16x32_f16 v[64:67], v[136:139], v[12:15], v[64:67]\n\tv_mfma_f32_16x16x32_f16 v[52:55], v[104:107], v[12:15], v[52:55]\n\t" \
  "ds_read_b128 v[12:15], v28 offset:1856\n\ts_waitcnt lgkmcnt(5)\n\tv_mfma_f32_16x16x32_f16 v[36:39], v[16:19], v[68:71], 0\n\t" \
  "v_cvt_pk_bf16_f32 v60, v60, v61\n\tv_cvt_pk_bf16_f32 v61, v62, v63\n\tv_mfma_f32_16x16x32_f16 v[40:43], v[16:19], v[72:75], 0\n\t" \
  "ds_write_b64 v29, v[60:61] offset:1024\n\tds_write_b32 v25, v25 offset:128\n\tds_read_b128 v[16:19], v28 offset:1920\n\t" \
  "s_waitcnt lgkmcnt(7)\n\tv_mfma_f32_16x16x32_f16 v[64:67], v[140:143], v[20:23], v[64:67]\n\tds_read_b32 v34, v30\n\t" \
  "v_cvt_pk_f16_f32 v36, v36, v37\n\tv_cvt_pk_f16_f32 v37, v38, v39\n\tv_mfma_f32_16x16x32_f16 v[52:55], v[108:111], v[20:23], v[52:55]\n\t" \
  "ds_read_b128 v[20:23], v28 offset:1984\n\tv_cvt_pk_f16_f32 v38, v40, v41\n\tv_cvt_pk_f16_f32 v39, v42, v43\n\t" \
  "s_waitcnt lgkmcnt(8)\n\tv_mfma_f32_16x16x32_f16 v[64:67], v[144:147], v[0:3], v[64:67]\n\tv_mfma_f32_16x16x32_f16 v[52:55], v[112:115], v[0:3], v[52:55]\n\t" \
  "s_waitcnt lgkmcnt(1)\n\tv_cmp_gt_u32 vcc, 3, v34\n\ts_cbranch_vccz 2f\n\t" \
  "s_mov_b32 s6, 0x10000\n\t1:\n\ts_sleep 1\n\t" \
  "ds_read_b32 v34, v30\n\ts_sub_u32 s6, s6, 1\n\ts_waitcnt lgkmcnt(0)\n\t" \
  "v_cmp_gt_u32 vcc, 3, v34\n\ts_cbranch_scc1 2f\n\ts_cbranch_vccnz 1b\n\t" \
  "2:\n\tds_read_b128 v[0:3], v28 offset:2048\n\tv_mfma_f32_16x16x32_f16 v[64:67], v[36:39], v[76:79], v[64:67]\n\t" \
  "v_mfma_f32_16x16x32_f16 v[52:55], v[36:39], v[80:83], v[52:55]\n\tv_mfma_f32_16x16x32_f16 v[56:59], v[36:39], v[84:87], 0\n\tv_mfma_f32_16x16x32_f16 v[44:47], v[4:7], v[68:71], 0\n\t" \
  "v_mfma_f32_16x16x32_f16 v[40:43], v[4:7], v[72:75], 0\n\tds_read_b128 v[4:7], v28 offset:2112\n\tv_mfma_f32_16x16x32_f16 v[64:67], v[148:151], v[8:11], v[64:67]\n\t" \
  "v_mfma_f32_16x16x32_f16 v[52:55], v[116:119], v[8:11], v[52:55]\n\tds_read_b128 v[8:11], v28 offset:1520\n\tv_mfma_f32_16x16x32_f16 v[64:67], v[152:155], v[12:15], v[64:67]\n\t" \
  "v_mfma_f32_16x16x32_f16 v[52:55], v[120:123], v[12:15], v[52:55]\n\tds_read_b128 v[12:15], v28 offset:2176\n\tv_cvt_pk_f16_f32 v44, v44, v45\n\t" \
  "v_cvt_pk_f16_f32 v45, v46, v47\n\tv_cvt_pk_f16_f32 v46, v40, v41\n\tv_cvt_pk_f16_f32 v47, v42, v43\n\t" \
  "s_nop 1\n\tv_mfma_f32_16x16x32_f16 v[64:67], v[44:47], v[88:91], v[64:67]\n\tv_mfma_f32_16x16x32_f16 v[52:55], v[44:47], v[92:95], v[52:55]\n\t" \
  "v_mfma_f32_16x16x32_f16 v[56:59], v[44:47], v[96:99], v[56:59]\n\tv_mfma_f32_16x16x32_f16 v[64:67], v[156:159], v[16:19], v[64:67]\n\tv_mfma_f32_16x16x32_f16 v[52:55], v[124:127], v[16:19], v[52:55]\n\t" \
  "ds_read_b128 v[16:19], v28 offset:2240\n\ts_waitcnt lgkmcnt(5)\n\tv_mfma_f32_16x16x32_f16 v[64:67], v[160:163], v[20:23], v[64:67]\n\t" \
  "v_mfma_f32_16x16x32_f16 v[52:55], v[128:131], v[20:23], v[52:55]\n\tds_read_b128 v[20:23], v28 offset:1504\n\ts_waitcnt lgkmcnt(5)\n\t" \
  "v_mfma_f32_16x16x32_f16 v[64:67], v[164:167], v[0:3], v[64:67]\n\tv_mfma_f32_16x16x32_f16 v[52:55], v[132:135], v[0:3], v[52:55]\n\tv_mfma_f32_16x16x32_f16 v[56:59], v[100:103], v[0:3], v[56:59]\n\t" \
  "ds_read_b128 v[0:3], v28 offset:2304\n\ts_waitcnt lgkmcnt(5)\n\tv_mfma_f32_16x16x32_f16 v[52:55], v[136:139], v[4:7], v[52:55]\n\t" \
  "v_mfma_f32_16x16x32_f16 v[56:59], v[104:107], v[4:7], v[56:59]\n\tds_read_b128 v[4:7], v28 offset:2368\n\ts_waitcnt lgkmcnt(5)\n\t" \
  "v_mfma_f32_16x16x32_f16 v[36:39], v[8:11], v[68:71], 0\n\tv_cvt_pk_bf16_f32 v64, v64, v65\n\tv_cvt_pk_bf16_f32 v65, v66, v67\n\t" \
  TS(66, 67) "s_cmp_lt_u32 %[w], 4\n\ts_cbranch_scc1 1f\n\t" \
  "s_setprio 1\n\t1:\n\tv_mfma_f32_16x16x32_f16 v[40:43], v[8:11], v[72:75], 0\n\t" \
  "ds_write_b64 v29, v[64:65] offset:1536\n\tds_write_b32 v25, v25 offset:192\n\tds_read_b128 v[8:11], v28 offset:480\n\t" \
  "s_waitcnt lgkmcnt(7)\n\tv_mfma_f32_16x16x32_f16 v[52:55], v[140:143], v[12:15], v[52:55]\n\tv_cvt_pk_f16_f32 v36, v36, v37\n\t" \
  "v_cvt_pk_f16_f32 v37, v38, v39\n\tv_mfma_f32_16x16x32_f16 v[56:59], v[108:111], v[12:15], v[56:59]\n\tds_read_b128 v[12:15], v32 offset:3072\n\t" \
  "v_cvt_pk_f16_f32 v38, v40, v41\n\tv_cvt_pk_f16_f32 v39, v42, v43\n\ts_waitcnt lgkmcnt(7)\n\t" \
  "v_mfma_f32_16x16x32_f16 v[52:55], v[144:147], v[16:19], v[52:55]\n\tv_mfma_f32_16x16x32_f16 v[56:59], v[112:115], v[16:19], v[56:59]\n\tds_read_b128 v[16:19], v28 offset:496\n\t" \
  "v_mfma_f32_16x16x32_f16 v[52:55], v[36:39], v[76:79], v[52:55]\n\tv_mfma_f32_16x16x32_f16 v[56:59], v[36:39], v[80:83], v[56:59]\n\tv_mfma_f32_16x16x32_f16 v[60:63], v[36:39], v[84:87], 0\n\t" \
  "s_waitcnt lgkmcnt(7)\n\tv_mfma_f32_16x16x32_f16 v[44:47], v[20:23], v[68:71], 0\n\tv_mfma_f32_16x16x32_f16 v[40:43], v[20:23], v[72:75], 0\n\t" \
  "ds_read_b128 v[20:23], v32 offset:2048\n\ts_waitcnt lgkmcnt(7)\n\tv_mfma_f32_16x16x32_f16 v[52:55], v[148:151], v[0:3], v[52:55]\n\t" \
  "v_mfma_f32_16x16x32_f16 v[56:59], v[116:119], v[0:3], v[56:59]\n\tds_read_b128 v[0:3], v28 offset:2432\n\ts_waitcnt lgkmcnt(7)\n\t" \
  "v_mfma_f32_16x16x32_f16 v[52:55], v[152:155], v[4:7], v[52:55]\n\tv_cvt_pk_f16_f32 v44, v44, v45\n\tv_cvt_pk_f16_f32 v45, v46, v47\n\t" \
  "v_cvt_pk_f16_f32 v46, v40, v41\n\tv_cvt_pk_f16_f32 v47, v42, v43\n\tv_mfma_f32_16x16x32_f16 v[56:59], v[120:123], v[4:7], v[56:59]\n\t" \
  "ds_read_b128 v[4:7], v28 offset:2496\n\tv_mfma_f32_16x16x32_f16 v[52:55], v[44:47], v[88:91], v[52:55]\n\tv_mfma_f32_16x16x32_f16 v[56:59], v[44:47], v[92:95], v[56:59]\n\t" \
  "v_mfma_f32_16x16x32_f16 v[60:63], v[44:47], v[96:99], v[60:63]\n\ts_waitcnt lgkmcnt(4)\n\tv_mfma_f32_16x16x32_f16 v[40:43], v[8:11], v[12:15], 0\n\t" \
  "ds_read_b64 v[8:9], v31 offset:544\n\tds_read_b64 v[10:11], v31 offset:712\n\tds_read_b64 v[12:13], v31 offset:512\n\t" \
  "ds_read_b64 v[14:15], v31 offset:680\n\ts_waitcnt lgkmcnt(7)\n\tv_mfma_f32_16x16x32_f16 v[52:55], v[48:51], v[16:19], v[52:55]\n\t" \
  "s_waitcnt lgkmcnt(6)\n\tv_mfma_f32_16x16x32_f16 v[36:39], v[16:19], v[20:23], 0\n\tds_read_b64 v[16:17], v31 offset:480\n\t" \
  "ds_read_b64 v[18:19], v31 offset:648\n\tds_read_b64 v[20:21], v31 offset:448\n\tds_read_b64 v[22:23], v31 offset:616\n\t" \
  "s_waitcnt lgkmcnt(9)\n\tv_mfma_f32_16x16x32_f16 v[52:55], v[156:159], v[0:3], v[52:55]\n\tv_mfma_f32_16x16x32_f16 v[56:59], v[124:127], v[0:3], v[56:59]\n\t" \
  "ds_read_b128 v[0:3], v28 offset:2560\n\tv_cvt_pk_f16_f32 v36, v36, v37\n\tv_cvt_pk_f16_f32 v37, v38, v39\n\t" \
  "s_waitcnt lgkmcnt(9)\n\tv_mfma_f32_16x16x32_f16 v[52:55], v[160:163], v[4:7], v[52:55]\n\tv_cvt_pk_f16_f32 v38, v40, v41\n\t" \
  "v_cvt_pk_f16_f32 v39, v42, v43\n\tv_mfma_f32_16x16x32_f16 v[56:59], v[128:131], v[4:7], v[56:59]\n\tds_read_b128 v[4:7], v28 offset:2624\n\t" \
  "s_waitcnt lgkmcnt(8)\n\tv_mfma_f32_16x16x32_f16 v[52:55], v[36:39], v[8:11], v[52:55]\n\tds_read_b128 v[8:11], v28 offset:2032\n\t" \
  "s_waitcnt lgkmcnt(7)\n\tv_mfma_f32_16x16x32_f16 v[56:59], v[36:39], v[12:15], v[56:59]\n\tds_read_b128 v[12:15], v28 offset:2688\n\t" \
  "s_waitcnt lgkmcnt(6)\n\tv_mfma_f32_16x16x32_f16 v[60:63], v[36:39], v[16:19], v[60:63]\n\tds_read_b128 v[16:19], v28 offset:2752\n\t" \
  "s_waitcnt lgkmcnt(5)\n\tv_mfma_f32_16x16x32_f16 v[64:67], v[36:39], v[20:23], 0\n\tds_read_b128 v[20:23], v28 offset:2016\n\t" \
  "s_waitcnt lgkmcnt(5)\n\tv_mfma_f32_16x16x32_f16 v[52:55], v[164:167], v[0:3], v[52:55]\n\tv_mfma_f32_16x16x32_f16 v[56:59], v[132:135], v[0:3], v[56:59]\n\t" \
  "v_mfma_f32_16x16x32_f16 v[60:63], v[100:103], v[0:3], v[60:63]\n\tds_read_b128 v[0:3], v28 offset:2816\n\ts_waitcnt lgkmcnt(5)\n\t" \
  "v_mfma_f32_16x16x32_f16 v[56:59], v[136:139], v[4:7], v[56:59]\n\tv_mfma_f32_16x16x32_f16 v[60:63], v[104:107], v[4:7], v[60:63]\n\tds_read_b128 v[4:7], v28 offset:2880\n\t" \
  "s_waitcnt lgkmcnt(5)\n\tv_mfma_f32_16x16x32_f16 v[44:47], v[8:11], v[68:71], 0\n\tv_cvt_pk_bf16_f32 v52, v52, v53\n\t" \
  "v_cvt_pk_bf16_f32 v53, v54, v55\n\ts_cmp_lt_u32 %[w], 4\n\ts_cbranch_scc0 1f\n\t" \
  "s_setprio 0\n\t1:\n\tv_mfma_f32_16x16x32_f16 v[40:43], v[8:11], v[72:75], 0\n\t" \
  "ds_write_b64 v29, v[52:53] offset:2048\n\tds_write_b32 v25, v25 offset:256\n\tds_read_b128 v[8:11], v28 offset:992\n\t" \
  "s_waitcnt lgkmcnt(7)\n\tv_mfma_f32_16x16x32_f16 v[56:59], v[140:143], v[12:15], v[56:59]\n\tv_cvt_pk_f16_f32 v44, v44, v45\n\t" \
  "v_cvt_pk_f16_f32 v45, v46, v47\n\tv_mfma_f32_16x16x32_f16 v[60:63], v[108:111], v[12:15], v[60:63]\n\tds_read_b128 v[12:15], v32 offset:3072\n\t" \
  "v_cvt_pk_f16_f32 v46, v40, v41\n\tv_cvt_pk_f16_f32 v47, v42, v43\n\ts_waitcnt lgkmcnt(7)\n\t" \
  "v_mfma_f32_16x16x32_f16 v[56:59], v[144:147], v[16:19], v[56:59]\n\tv_mfma_f32_16x16x32_f16 v[60:63], v[112:115], v[16:19], v[60:63]\n\tds_read_b128 v[16:19], v28 offset:1008\n\t" \
  "v_mfma_f32_16x16x32_f16 v[56:59], v[44:47], v[76:79], v[56:59]\n\tv_mfma_f32_16x16x32_f16 v[60:63], v[44:47], v[80:83], v[60:63]\n\tv_mfma_f32_16x16x32_f16 v[64:67], v[44:47], v[84:87], v[64:67]\n\t" \
  "s_waitcnt lgkmcnt(7)\n\tv_mfma_f32_16x16x32_f16 v[36:39], v[20:23], v[68:71], 0\n\tv_mfma_f32_16x16x32_f16 v[40:43], v[20:23], v[72:75], 0\n\t" \
  "ds_read_b128 v[20:23], v32 offset:2048\n\ts_waitcnt lgkmcnt(7)\n\tv_mfma_f32_16x16x32_f16 v[56:59], v[148:151], v[0:3], v[56:59]\n\t" \
  "v_mfma_f32_16x16x32_f16 v[60:63], v[116:119], v[0:3], v[60:63]\n\tds_read_b128 v[0:3], v28 offset:2944\n\ts_waitcnt lgkmcnt(7)\n\t" \
  "v_mfma_f32_16x16x32_f16 v[56:59], v[152:155], v[4:7], v[56:59]\n\tv_cvt_pk_f16_f32 v36, v36, v37\n\tv_cvt_pk_f16_f32 v37, v38, v39\n\t" \
  "v_cvt_pk_f16_f32 v38, v40, v41\n\tv_cvt_pk_f16_f32 v39, v42, v43\n\tv_mfma_f32_16x16x32_f16 v[60:63], v[120:123], v[4:7], v[60:63]\n\t" \
  "ds_read_b128 v[4:7], v28 offset:3008\n\tv_mfma_f32_16x16x32_f16 v[56:59], v[36:39], v[88:91], v[56:59]\n\tv_mfma_f32_16x16x32_f16 v[60:63], v[36:39], v[92:95], v[60:63]\n\t" \
  "v_mfma_f32_16x16x32_f16 v[64:67], v[36:39], v[96:99], v[64:67]\n\tds_read_b32 v34, v30\n\ts_waitcnt lgkmcnt(5)\n\t" \
  "v_mfma_f32_16x16x32_f16 v[40:43], v[8:11], v[12:15], 0\n\tds_read_b64 v[8:9], v31 offset:544\n\tds_read_b64 v[10:11], v31 offset:712\n\t" \
  "ds_read_b64 v[12:13], v31 offset:512\n\tds_read_b64 v[14:15], v31 offset:680\n\ts_waitcnt lgkmcnt(8)\n\t" \
  "v_mfma_f32_16x16x32_f16 v[56:59], v[48:51], v[16:19], v[56:59]\n\ts_waitcnt lgkmcnt(7)\n\tv_mfma_f32_16x16x32_f16 v[44:47], v[16:19], v[20:23], 0\n\t" \
  "ds_read_b64 v[16:17], v31 offset:480\n\tds_read_b64 v[18:19], v31 offset:648\n\ts_waitcnt lgkmcnt(6)\n\t" \
  "v_cmp_gt_u32 vcc, 4, v34\n\ts_cbranch_vccz 2f\n\ts_mov_b32 s6, 0x10000\n\t" \
  "1:\n\ts_sleep 1\n\tds_read_b32 v34, v30\n\t" \
  "s_sub_u32 s6, s6, 1\n\ts_waitcnt lgkmcnt(0)\n\tv_cmp_gt_u32 vcc, 4, v34\n\t" \
  "s_cbranch_scc1 2f\n\ts_cbranch_vccnz 1b\n\t2:\n\t" \
  "ds_read_b128 v[20:23], v28 offset:3072\n\tv_mfma_f32_16x16x32_f16 v[56:59], v[156:159], v[0:3], v[56:59]\n\tv_mfma_f32_16x16x32_f16 v[60:63], v[124:127], v[0:3], v[60:63]\n\t" \
  "ds_read_b128 v[0:3], v28 offset:3136\n\tv_cvt_pk_f16_f32 v44, v44, v45\n\tv_cvt_pk_f16_f32 v45, v46, v47\n\t" \
  "v_mfma_f32_16x16x32_f16 v[56:59], v[160:163], v[4:7], v[56:59]\n\tv_cvt_pk_f16_f32 v46, v40, v41\n\tv_cvt_pk_f16_f32 v47, v42, v43\n\t" \
  "v_mfma_f32_16x16x32_f16 v[60:63], v[128:131], v[4:7], v[60:63]\n\tds_read_b128 v[4:7], v28 offset:2544\n\ts_waitcnt lgkmcnt(7)\n\t" \
  "v_mfma_f32_16x16x32_f16 v[56:59], v[44:47], v[8:11], v[56:59]\n\tds_read_b128 v[8:11], v28 offset:3200\n\ts_waitcnt lgkmcnt(6)\n\t" \
  "v_mfma_f32_16x16x32_f16 v[60:63], v[44:47], v[12:15], v[60:63]\n\tds_read_b128 v[12:15], v28 offset:3264\n\ts_waitcnt lgkmcnt(5)\n\t" \
  "v_mfma_f32_16x16x32_f16 v[64:67], v[44:47], v[16:19], v[64:67]\n\tds_read_b128 v[16:19], v28 offset:2528\n\ts_waitcnt lgkmcnt(5)\n\t" \
  "v_mfma_f32_16x16x32_f16 v[56:59], v[164:167], v[20:23], v[56:59]\n\tv_mfma_f32_16x16x32_f16 v[60:63], v[132:135], v[20:23], v[60:63]\n\tv_mfma_f32_16x16x32_f16 v[64:67], v[100:103], v[20:23], v[64:67]\n\t" \
  "ds_read_b128 v[20:23], v28 offset:3328\n\ts_waitcnt lgkmcnt(5)\n\tv_mfma_f32_16x16x32_f16 v[60:63], v[136:139], v[0:3], v[60:63]\n\t" \
  "v_mfma_f32_16x16x32_f16 v[64:67], v[104:107], v[0:3], v[64:67]\n\tds_read_b128 v[0:3], v28 offset:3392\n\ts_waitcnt lgkmcnt(5)\n\t" \
  "v_mfma_f32_16x16x32_f16 v[36:39], v[4:7], v[68:71], 0\n\tv_cvt_pk_bf16_f32 v56, v56, v57\n\tv_cvt_pk_bf16_f32 v57, v58, v59\n\t" \
  "s_cmp_lt_u32 %[w], 4\n\ts_cbranch_scc1 1f\n\ts_setprio 0\n\t" \
  "1:\n\tv_mfma_f32_16x16x32_f16 v[40:43], v[4:7], v[72:75], 0\n\tds_write_b64 v29, v[56:57] offset:2560\n\t" \
  "ds_write_b32 v25, v25 offset:320\n\tds_read_b128 v[4:7], v28 offset:1504\n\ts_waitcnt lgkmcnt(7)\n\t" \
  "v_mfma_f32_16x16x32_f16 v[60:63], v[140:143], v[8:11], v[60:63]\n\tv_cvt_pk_f16_f32 v36, v36, v37\n\tv_cvt_pk_f16_f32 v37, v38, v39\n\t" \
  "v_mfma_f32_16x16x32_f16 v[64:67], v[108:111], v[8:11], v[64:67]\n\tds_read_b128 v[8:11], v32 offset:3072\n\tv_cvt_pk_f16_f32 v38, v40, v41\n\t" \
  "v_cvt_pk_f16_f32 v39, v42, v43\n\ts_waitcnt lgkmcnt(7)\n\tv_mfma_f32_16x16x32_f16 v[60:63], v[144:147], v[12:15], v[60:63]\n\t" \
  "v_mfma_f32_16x16x32_f16 v[64:67], v[112:115], v[12:15], v[64:67]\n\tds_read_b128 v[12:15], v28 offset:1520\n\tv_mfma_f32_16x16x32_f16 v[60:63], v[36:39], v[76:79], v[60:63]\n\t" \
  "v_mfma_f32_16x16x32_f16 v[64:67], v[36:39], v[80:83], v[64:67]\n\ts_waitcnt lgkmcnt(7)\n\tv_mfma_f32_16x16x32_f16 v[44:47], v[16:19], v[68:71], 0\n\t" \
  "v_mfma_f32_16x16x32_f16 v[40:43], v[16:19], v[72:75], 0\n\tds_read_b128 v[16:19], v32 offset:2048\n\ts_waitcnt lgkmcnt(7)\n\t" \
  "v_mfma_f32_16x16x32_f16 v[60:63], v[148:151], v[20:23], v[60:63]\n\tv_mfma_f32_16x16x32_f16 v[64:67], v[116:119], v[20:23], v[64:67]\n\tds_read_b128 v[20:23], v28 offset:3456\n\t" \
  "s_waitcnt lgkmcnt(7)\n\tv_mfma_f32_16x16x32_f16 v[60:63], v[152:155], v[0:3], v[60:63]\n\tv_cvt_pk_f16_f32 v44, v44, v45\n\t" \
  "v_cvt_pk_f16_f32 v45, v46, v47\n\tv_cvt_pk_f16_f32 v46, v40, v41\n\tv_cvt_pk_f16_f32 v47, v42, v43\n\t" \
  "v_mfma_f32_16x16x32_f16 v[64:67], v[120:123], v[0:3], v[64:67]\n\tds_read_b128 v[0:3], v28 offset:3520\n\tv_mfma_f32_16x16x32_f16 v[60:63], v[44:47], v[88:91], v[60:63]\n\t" \
  "v_mfma_f32_16x16x32_f16 v[64:67], v[44:47], v[92:95], v[64:67]\n\ts_waitcnt lgkmcnt(4)\n\tv_mfma_f32_16x16x32_f16 v[40:43], v[4:7], v[8:11], 0\n\t" \
  "ds_read_b64 v[4:5], v31 offset:544\n\tds_read_b64 v[6:7], v31 offset:712\n\tds_read_b64 v[8:9], v31 offset:512\n\t" \
  "ds_read_b64 v[10:11], v31 offset:680\n\ts_waitcnt lgkmcnt(7)\n\tv_mfma_f32_16x16x32_f16 v[60:63], v[48:51], v[12:15], v[60:63]\n\t" \
  "s_waitcnt lgkmcnt(6)\n\tv_mfma_f32_16x16x32_f16 v[36:39], v[12:15], v[16:19], 0\n\tds_read_b128 v[12:15], v28 offset:3584\n\t" \
  "ds_read_b128 v[16:19], v28 offset:3648\n\ts_waitcnt lgkmcnt(7)\n\tv_mfma_f32_16x16x32_f16 v[60:63], v[156:159], v[20:23], v[60:63]\n\t" \
  "v_mfma_f32_16x16x32_f16 v[64:67], v[124:127], v[20:23], v[64:67]\n\tds_read_b128 v[20:23], v28 offset:3056\n\ts_waitcnt lgkmcnt(7)\n\t" \
  "v_mfma_f32_16x16x32_f16 v[60:63], v[160:163], v[0:3], v[60:63]\n\tv_cvt_pk_f16_f32 v36, v36, v37\n\tv_cvt_pk_f16_f32 v37, v38, v39\n\t" \
  "v_cvt_pk_f16_f32 v38, v40, v41\n\tv_cvt_pk_f16_f32 v39, v42, v43\n\tv_mfma_f32_16x16x32_f16 v[64:67], v[128:131], v[0:3], v[64:67]\n\t" \
  "ds_read_b128 v[0:3], v28 offset:3712\n\ts_waitcnt lgkmcnt(6)\n\tv_mfma_f32_16x16x32_f16 v[60:63], v[36:39], v[4:7], v[60:63]\n\t" \
  "ds_read_b128 v[4:7], v28 offset:3776\n\ts_waitcnt lgkmcnt(5)\n\tv_mfma_f32_16x16x32_f16 v[64:67], v[36:39], v[8:11], v[64:67]\n\t" \
  "ds_read_b128 v[8:11], v28 offset:3040\n\ts_waitcnt lgkmcnt(5)\n\tv_mfma_f32_16x16x32_f16 v[60:63], v[164:167], v[12:15], v[60:63]\n\t" \
  "v_mfma_f32_16x16x32_f16 v[64:67], v[132:135], v[12:15], v[64:67]\n\tds_read_b128 v[12:15], v28 offset:3840\n\ts_waitcnt lgkmcnt(5)\n\t" \
  "v_mfma_f32_16x16x32_f16 v[64:67], v[136:139], v[16:19], v[64:67]\n\tds_read_b128 v[16:19], v28 offset:3904\n\ts_waitcnt lgkmcnt(5)\n\t" \
  "v_mfma_f32_16x16x32_f16 v[44:47], v[20:23], v[68:71], 0\n\tv_mfma_f32_16x16x32_f16 v[40:43], v[20:23], v[72:75], 0\n\tds_read_b128 v[20:23], v28 offset:2016\n\t" \
  "v_cvt_pk_bf16_f32 v60, v60, v61\n\tv_cvt_pk_bf16_f32 v61, v62, v63\n\ts_waitcnt lgkmcnt(5)\n\t" \
  "v_mfma_f32_16x16x32_f16 v[64:67], v[140:143], v[0:3], v[64:67]\n\tds_write_b64 v29, v[60:61] offset:3072\n\tds_write_b32 v25, v25 offset:384\n\t" \
  "ds_read_b128 v[0:3], v32 offset:3072\n\tv_cvt_pk_f16_f32 v44, v44, v45\n\tv_cvt_pk_f16_f32 v45, v46, v47\n\t" \
  "v_cvt_pk_f16_f32 v46, v40, v41\n\tv_cvt_pk_f16_f32 v47, v42, v43\n\ts_waitcnt lgkmcnt(7)\n\t" \
  "v_mfma_f32_16x16x32_f16 v[64:67], v[144:147], v[4:7], v[64:67]\n\tds_read_b128 v[4:7], v28 offset:2032\n\tv_mfma_f32_16x16x32_f16 v[64:67], v[44:47], v[76:79], v[64:67]\n\t" \
  "s_waitcnt lgkmcnt(7)\n\tv_mfma_f32_16x16x32_f16 v[36:39], v[8:11], v[68:71], 0\n\tv_mfma_f32_16x16x32_f16 v[40:43], v[8:11], v[72:75], 0\n\t" \
  "ds_read_b128 v[8:11], v32 offset:2048\n\ts_waitcnt lgkmcnt(7)\n\tv_mfma_f32_16x16x32_f16 v[64:67], v[148:151], v[12:15], v[64:67]\n\t" \
  "ds_read_b128 v[12:15], v28 offset:3968\n\ts_waitcnt lgkmcnt(7)\n\tv_mfma_f32_16x16x32_f16 v[64:67], v[152:155], v[16:19], v[64:67]\n\t" \
  "ds_read_b128 v[16:19], v28 offset:4032\n\tv_cvt_pk_f16_f32 v36, v36, v37\n\tv_cvt_pk_f16_f32 v37, v38, v39\n\t" \
  "v_cvt_pk_f16_f32 v38, v40, v41\n\tv_cvt_pk_f16_f32 v39, v42, v43\n\ts_nop 1\n\t" \
  "v_mfma_f32_16x16x32_f16 v[64:67], v[36:39], v[88:91], v[64:67]\n\ts_waitcnt lgkmcnt(4)\n\tv_mfma_f32_16x16x32_f16 v[40:43], v[20:23], v[0:3], 0\n\t" \
  "ds_read_b64 v[0:1], v31 offset:544\n\tds_read_b64 v[2:3], v31 offset:712\n\tds_read_b128 v[20:23], v28 offset:4096\n\t" \
  "s_waitcnt lgkmcnt(6)\n\tv_mfma_f32_16x16x32_f16 v[64:67], v[48:51], v[4:7], v[64:67]\n\ts_waitcnt lgkmcnt(5)\n\t" \
  "v_mfma_f32_16x16x32_f16 v[44:47], v[4:7], v[8:11], 0\n\ts_waitcnt lgkmcnt(4)\n\tv_mfma_f32_16x16x32_f16 v[64:67], v[156:159], v[12:15], v[64:67]\n\t" \
  "s_waitcnt lgkmcnt(3)\n\tv_mfma_f32_16x16x32_f16 v[64:67], v[160:163], v[16:19], v[64:67]\n\ts_nop 3\n\t" \
  "v_cvt_pk_f16_f32 v44, v44, v45\n\tv_cvt_pk_f16_f32 v45, v46, v47\n\tv_cvt_pk_f16_f32 v46, v40, v41\n\t" \
  "v_cvt_pk_f16_f32 v47, v42, v43\n\ts_waitcnt lgkmcnt(1)\n\ts_nop 0\n\t" \
  "v_mfma_f32_16x16x32_f16 v[64:67], v[44:47], v[0:3], v[64:67]\n\ts_waitcnt lgkmcnt(0)\n\tv_mfma_f32_16x16x32_f16 v[64:67], v[164:167], v[20:23], v[64:67]\n\t" \
  "s_nop 7\n\tv_cvt_pk_bf16_f32 v64, v64, v65\n\tv_cvt_pk_bf16_f32 v65, v66, v67\n\t" \
  "ds_write_b64 v29, v[64:65] offset:3584\n\tds_write_b32 v25, v25 offset:448\n\t"
#define CONV_CLOBBER "v0", "v1", "v2", "v3", "v4", "v5", "v6", "v7", "v8", "v9", "v10", "v11", "v12", "v13", "v14", "v15", "v16", "v17", "v18", "v19", "v20", "v21", "v22", "v23", "v36", "v37", "v38", "v39", "v40", "v41", "v42", "v43", "v44", "v45", "v46", "v47", "v52", "v53", "v54", "v55", "v56", "v57", "v58", "v59", "v60", "v61", "v62", "v63", "v64", "v65", "v66", "v67", "v68", "v69", "v70", "v71", "v72", "v73", "v74", "v75", "v76", "v77", "v78", "v79", "v80", "v81", "v82", "v83", "v84", "v85", "v86", "v87", "v88", "v89", "v90", "v91", "v92", "v93", "v94", "v95", "v96", "v97", "v98", "v99", "v100", "v101", "v102", "v103", "v104", "v105", "v106", "v107", "v108", "v109", "v110", "v111", "v112", "v113", "v114", "v115", "v116", "v117", "v118", "v119", "v120", "v121", "v122", "v123", "v124", "v125", "v126", "v127", "v128", "v129", "v130", "v131", "v132", "v133", "v134", "v135", "v136", "v137", "v138", "v139", "v140", "v141", "v142", "v143", "v144", "v145", "v146", "v147", "v148", "v149", "v150", "v151", "v152", "v153", "v154", "v155", "v156", "v157", "v158", "v159", "v160", "v161", "v162", "v163", "v164", "v165", "v166", "v167"
#define G_CLOBBER "v8", "v9", "v10", "v11", "v12", "v13", "v14", "v15", "v16", "v17", "v18", "v19", "v20", "v21", "v22", "v23", "v24", "v25", "v26", "v27", "v28", "v29", "v30", "v32", "v33", "v34", "v35", "v36", "v37", "v38", "v39", "v40", "v41", "v42", "v43", "v44", "v45", "v46", "v47", "v48", "v49", "v50", "v51", "v52", "v53", "v54", "v55", "v56", "v57", "v58", "v59", "v60", "v61", "v62", "v63", "v64", "v65", "v66", "v67", "v68", "v69", "v70", "v71", "v72", "v73", "v74", "v75", "v76", "v77", "v78", "v79", "v80", "v81", "v82", "v83", "v84", "v85", "v86", "v87", "v88", "v89", "v90", "v91", "v92", "v93", "v94", "v95", "v96", "v97", "v98", "v99", "v100", "v101", "v102", "v103", "v104", "v105", "v106", "v107", "v108", "v109", "v110", "v111", "v112", "v113", "v114", "v115", "v116", "v117", "v118", "v119", "v120", "v121", "v122", "v123", "v124", "v125", "v126", "v127"
#define D_CLOBBER "v8", "v9", "v10", "v11", "v12", "v13", "v14", "v15", "v16", "v17", "v18", "v19", "v20", "v21", "v22", "v23", "v24", "v25", "v26", "v27", "v28", "v29", "v30", "v31"
__device__ __forceinline__ f32x2 pkfma(f32x2 a, f32x2 b, f32x2 c) {
  f32x2 r;
  asm("v_pk_fma_f32 %0, %1, %2, %3" : "=v"(r) : "v"(a), "v"(b), "v"(c));
  return r;
}
__device__ __forceinline__ float fmav(float a, float b, float c) {
  float r;
  asm("v_fma_f32 %0, %1, %2, %3" : "=v"(r) : "v"(a), "v"(b), "v"(c));
  return r;
}
__device__ __forceinline__ float fmac(float a, float b, float c) {
  asm("v_fmac_f32 %0, %1, %2" : "+v"(c) : "v"(a), "v"(b));
  return c;
}
__device__ __forceinline__ float fmas(float a, u32 s, float c) {
  asm("v_fmac_f32 %0, %2, %1" : "+v"(c) : "v"(a), "s"(s));
  return c;
}
template <int HI>
__device__ __forceinline__ f32x2 pkfmas(f32x2 a, f32x2 s, f32x2 c) {
  f32x2 r;
  if (HI) asm("v_pk_fma_f32 %0, %2, %1, %3 op_sel:[1,0,0] op_sel_hi:[1,1,1]" : "=v"(r) : "v"(a), "v"(s), "v"(c));
  else asm("v_pk_fma_f32 %0, %2, %1, %3 op_sel:[0,0,0] op_sel_hi:[0,1,1]" : "=v"(r) : "v"(a), "v"(s), "v"(c));
  return r;
}
#define RANK8(d0, d1, d2, d3, c0, c1, i0, f0, f1) "v_perm_b32 " d0 ", " d0 ", " d1 ", %[sg]\n\tv_perm_b32 " d2 ", " d2 ", " d3 ", %[sg]\n\t" \
  "v_bcnt_u32_b32 " d0 ", " d0 ", " i0 "\n\tv_bcnt_u32_b32 " c0 ", " d2 ", " d0 "\n\tv_bcnt_u32_b32 " c1 ", " d2 ", " d0 "\n\t" f0 "\n\t" \
  "v_permlane16_swap_b32 " c0 ", " c1 "\n\tv_add_u32 " d0 ", " c0 ", " c1 "\n\tv_add_u32 " d1 ", " c0 ", " c1 "\n\t" f1 "\n\t" \
  "v_permlane32_swap_b32 " d0 ", " d1 "\n\tv_add_u32 " c0 ", " d0 ", " d1 "\n\t"
__device__ __forceinline__ void ln_rows(u32x4 (&xv)[4], u32x8 wv, rsrc_t rs, u32& tag, int b, int w, int l, u32 st, u32 sg TLARG) {
  float s[2][3] = {}, c[2], sw = 0.f;
  u32 os = l < 16 ? O_SG + 128 * (b - 128) + 8 * l : 0x80000000u;
  u32 ot = l < 16 ? O_TOP + 8 * (b & 15) + TL * ((b >> 4) & 7) : l < 32 ? O_RG - 128 + 128 * (b - 128) + 8 * l : 0x80000000u, om = l < 16 ? TOPS / 8 : 0u, fm = l < 16 ? 0xff0u : 0u;
  u32 aw = (l & 16) ? st + 8 * w + (l >> 5) * 4 : st + 136, ac = st + 128, ar = st + 4 * (l & 15), aq = st + (l & 48), one = 1u;
  u32 kl = (2047 - (16 * (b - 128) + 2 * w + (l >> 5))) << 1;
  asm volatile("" : "+v"(os), "+v"(aw), "+v"(ac), "+v"(ar), "+v"(aq), "+v"(one), "+v"(kl), "+v"(ot), "+v"(om), "+v"(fm));
  SB();
  asm volatile("s_waitcnt vmcnt(4) lgkmcnt(0)" : "+{s44}"(tag)::"memory");
  asm volatile("" : "+{v[116:123]}"(wv));
  tag += 1u;
  *(volatile LDS u32*)(size_t)(st - 104) = tag;
  STAMP(12);
#pragma unroll
  for (int i = 0; i < 8; i++) sw = dot2b(wv[i], 0x3f803f80u, sw);
  sw += dppf<0x121, 0xf>(sw); sw += dppf<0x122, 0xf>(sw); sw += dppf<0x124, 0xf>(sw); sw += dppf<0x128, 0xf>(sw);
  sw = (rdl(sw, 0) + rdl(sw, 16)) + (rdl(sw, 32) + rdl(sw, 48));
  asm volatile("" : "+v"(sw));
  SB();
  float cv = 0.f;
#pragma unroll
  for (int r = 0; r < 2; r++) {
#pragma unroll
    for (int i = 0; i < 8; i++) {
      if (r == 0 && i == 0) { asm volatile("s_waitcnt vmcnt(3)" : "+{v[100:103]}"(xv[0])); STAMP(13); }
      if (r == 0 && i == 4) asm volatile("s_waitcnt vmcnt(2)" : "+{v[104:107]}"(xv[1]));
      if (r == 1 && i == 0) asm volatile("s_waitcnt vmcnt(1)" : "+{v[108:111]}"(xv[2]));
      if (r == 1 && i == 4) asm volatile("s_waitcnt vmcnt(0)" : "+{v[112:115]}"(xv[3]));
      const u32 a = xv[2 * r + (i >> 2)][i & 3];
      s[r][0] = dot2b(a, 0x3f803f80u, s[r][0]); s[r][1] = dot2b(a, a, s[r][1]); s[r][2] = dot2b(a, wv[i], s[r][2]);
      if (i == 3) {
        c[r] = rdl(s[r][0] * 0.125f, 0);
        if (r) { cv = l < 32 ? c[0] : c[1]; asm volatile("" : "+v"(cv)); }
        SB();
      }
    }
    const float ml = s[r][0] * 0.0625f, dl = __builtin_fmaf(s[r][0], 0.25f, -4.f * c[r]);
    s[r][1] = __builtin_fmaf(dl, dl, __builtin_fmaf(-s[r][0], ml, s[r][1]));
    SB();
  }
#pragma unroll
  for (int k = 0; k < 3; k++) swap32(s[0][k ? 3 - k : 0], s[1][k ? 3 - k : 0]);
  float q[3];
#pragma unroll
  for (int k = 0; k < 3; k++) q[k] = s[0][k] + s[1][k];
#define RED3(m) "v_add_f32_dpp %0, %0, %0 " m " bank_mask:0xf\n\tv_add_f32_dpp %1, %1, %1 " m " bank_mask:0xf\n\tv_add_f32_dpp %2, %2, %2 " m " bank_mask:0xf\n\t"
  asm volatile("s_nop 1\n\t" RED3("row_ror:1 row_mask:0xf") RED3("row_ror:2 row_mask:0xf") RED3("row_ror:4 row_mask:0xf") RED3("row_ror:8 row_mask:0xf")
               RED3("row_bcast:15 row_mask:0xa") : "+v"(q[0]), "+v"(q[1]), "+v"(q[2]));
  const float mu = q[0] * (1.f / D), dv = mu - cv;
  const float var = fmaxf(__builtin_fmaf(-dv, dv, q[1] * (1.f / D)), 0.f);
  const float m = rb(mu);
  float sd = __builtin_amdgcn_sqrtf(rb(rb(var) + 1e-5f)), num = __builtin_fmaf(-m, sw, q[2]);
  asm volatile("s_nop 0\n\tv_cvt_pk_bf16_f32 %0, 0, %0" : "+v"(sd), "+v"(num));
  u32 rv = __float_as_uint(rb(num * __builtin_amdgcn_rcpf(sd)));
  rv ^= (u32)((int)rv >> 15) | 0x80000000u;
  rv = __builtin_amdgcn_perm(rv, kl, 0x07060100u) >> 1;
  asm volatile("ds_write2_b32 %[aw], %[sv], %[rv] offset1:16\n\ts_mov_b64 exec, 1\n\tds_add_rtn_u32 v104, %[ac], %[one]\n\ts_mov_b64 exec, -1\n\t"
               "ds_read_b32 v106, %[ar]\n\tds_read_b32 v105, %[ar] offset:64\n\tds_read_b128 v[100:103], %[aq] offset:64\n\tv_mov_b32 v107, %[t]\n\tv_mov_b32 v109, %[t]\n\t"
               "s_waitcnt lgkmcnt(3)\n\tv_cmp_eq_u32 vcc, 7, v104\n\ts_bitcmp1_b32 vcc_lo, 0\n\ts_cbranch_scc0 1f\n\t"
               "s_waitcnt lgkmcnt(2)\n\tbuffer_store_dwordx2 v[106:107], %[os], %[rs], 0 offen sc1\n\ts_waitcnt lgkmcnt(0)\n\t"
               "v_sub_u32 v100, v105, v100\n\tv_sub_u32 v101, v105, v101\n\tv_sub_u32 v102, v105, v102\n\tv_sub_u32 v103, v105, v103\n\t"
               RANK8("v100", "v101", "v102", "v103", "v104", "v103", "0", "s_nop 1", "s_nop 1") "v_cmp_eq_u32 vcc, 16, v104\n\t"
               "v_sub_u32 v108, 19, v104\n\ts_ff1_i32_b32 s6, vcc_lo\n\tv_readlane_b32 s6, v105, s6\n\t"
               "v_mad_u32_u24 v104, v104, %[om], %[ot]\n\ts_nop 0\n\tv_lshrrev_b32 v108, v108, s6\n\tv_bfi_b32 v108, %[fm], v108, v105\n\t"
               "buffer_store_dwordx2 v[108:109], v104, %[rs], 0 offen sc1\n1:\n\ts_waitcnt lgkmcnt(0)"
               :: [aw] "v"(aw), [sv] "v"(__builtin_amdgcn_perm(__float_as_uint(sd), __float_as_uint(m), 0x07060302u)), [rv] "v"(rv), [ac] "v"(ac), [one] "v"(one),
                  [ar] "v"(ar), [aq] "v"(aq), [os] "v"(os), [om] "v"(om), [ot] "v"(ot), [fm] "v"(fm), [sg] "s"(sg), [t] "s"(tag), [rs] "s"(rs)
               : "vcc", "scc", "s6", "v100", "v101", "v102", "v103", "v104", "v105", "v106", "v107", "v108", "v109", "memory");
  SB();
}

#define GL __attribute__((address_space(1)))
typedef const GL u16* gp;

__global__ __launch_bounds__(768, 3) void fused(const u16*, const u16*, const u16*, const u16*, const u16*, const u16*, const u16*, const u16*,
    const u16*, const u16*, const u16*, u16*, char*) {
  __shared__ __attribute__((aligned(256))) char smem[SMEM];
  const u32 lds0 = (u32)(size_t)(LDS char*)smem;
  int w0;
#define GLD(r0, r1, r2, r3, o_) "global_load_dwordx4 v[" #r0 ":" #r1 "], " #o_ ", s[16:17]\n\tglobal_load_dwordx4 v[" #r2 ":" #r3 "], " #o_ ", s[16:17] offset:2048\n\t"
  asm volatile(
      "s_load_dwordx16 s[16:31], s[0:1], 0\n\ts_load_dwordx8 s[32:39], s[0:1], 64\n\ts_load_dwordx2 s[40:41], s[0:1], 96\n\t"
      "s_memrealtime s[46:47]\n\t"
      "s_getpc_b64 s[4:5]\n2:\n\ts_lshr_b32 s7, s2, 3\n\ts_and_b32 s10, s4, 0xffffff00\n\ts_mov_b32 s11, s5\n\ts_mul_i32 s8, s7, 0x600\n\t"
      "v_lshl_add_u32 v3, v0, 1, s8\n\ts_cmpk_lt_u32 s2, %[pn]\n\ts_cbranch_scc0 1f\n\t"
      "global_load_ushort v163, v3, s[10:11] offset:%[pf]\n"
      "1:\n\ts_cmpk_lt_u32 s2, 0x80\n\tv_readfirstlane_b32 s3, v0\n\tv_and_b32 v1, 63, v0\n\tv_mov_b64 v[4:5], 0\n\tv_mov_b64 v[6:7], 0\n\tv_lshlrev_b32 v2, 4, v1\n\tv_lshlrev_b32 v8, 2, v0\n\t"
      "s_mov_b32 s42, %[we]\n\ts_mov_b32 s43, 0x20000\n\ts_cbranch_scc0 3f\n\t"
      "s_lshr_b32 s3, s3, 6\n\ts_mul_i32 s9, s3, %[xr]\n\ts_min_u32 s9, s9, %[zm]\n\tv_add_u32 v3, s9, v2\n\tv_add_u32 v3, %[zo], v3\n\t"
      "s_and_b32 s7, s7, 7\n\ts_and_b32 s6, s2, 7\n\ts_and_b32 s8, s2, 64\n\ts_lshl3_add_u32 s8, s6, s8\n\ts_add_u32 s45, s8, s7\n\t"
      "s_add_u32 s4, s4, lane_tab@rel32@lo+(6f-2b-4)\n6:\n\ts_addc_u32 s5, s5, lane_tab@rel32@hi+(6b-2b+4)\n\t"
      "ds_write_b128 v3, v[4:7]\n\ts_cmp_lt_u32 s3, 8\n\ts_cbranch_scc1 5f\n\t"
      "s_lshl_b32 s8, s45, 4\n\ts_lshl_b32 s9, s3, 18\n\ts_add_u32 s9, s9, s8\n\ts_bitset0_b32 s9, 21\n\tv_lshl_add_u32 v148, v1, 12, s9\n\t"
      "s_lshl3_add_u32 s9, s3, s7\n\ts_lshl_b32 s9, s9, 17\n\ts_add_u32 s9, s9, s8\n\ts_bitset0_b32 s9, 23\n\tv_lshl_add_u32 v9, v1, 11, s9\n\t"
      "v_add_u32 v11, 0x100000, v148\n\tv_add_u32 v12, 0x200000, v148\n\t"
      "v_lshlrev_b32 v149, 4, v0\n\ts_lshl_b32 s9, s3, 10\n\ts_add_u32 m0, s9, %[ob]\n\tv_add_u32 v10, 0xffffe800, v149\n\tglobal_load_lds_dwordx4 v10, s[4:5]\n\t"
      "v_add_u32 v149, %[sg], v149\n\t"
      "s_waitcnt lgkmcnt(0)\n\ts_load_dword s44, s[40:41], 0\n\t"
      "s_barrier\n\t"
      "global_load_dword v160, v9, s[16:17]" PRENT "\n\t"
      GLD(100, 103, 104, 107, v148)
      GLD(108, 111, 112, 115, v11)
      "s_cmp_eq_u32 s6, 3\n\ts_cbranch_scc1 .Lrole_p\n\t"
      GLD(116, 119, 120, 123, v12)
      "s_branch .Lrole_p\n"
      "3:\n\ts_lshr_b32 s3, s3, 6\n\ts_cmp_lt_u32 s3, 8\n\ts_cbranch_scc1 4f\n\ts_barrier\n\ts_branch .Lrole_d\n4:\n\t"
      "v_add_u32 v3, %[fc], v2\n\ts_add_u32 s8, s3, s2\n\ts_and_b32 s45, s8, 7\n\ts_lshl3_add_u32 s8, s2, s45\n\t"
      "s_lshl_b32 s8, s8, 12\n\ts_bitset0_b32 s8, 22\n\tds_write_b128 v3, v[4:7]\n\tv_add_u32 v3, s8, v2\n\t"
      "s_waitcnt lgkmcnt(0)\n\tglobal_load_dwordx4 v[116:119], v2, s[30:31]\n\tglobal_load_dwordx4 v[120:123], v2, s[30:31] offset:1024\n\t"
      "s_load_dword s44, s[40:41], 0\n\ts_barrier\n\t"
      "global_load_dwordx4 v[100:103], v3, s[16:17]" LNNT "\n\tglobal_load_dwordx4 v[104:107], v3, s[16:17] offset:1024" LNNT "\n\t"
      "global_load_dwordx4 v[108:111], v3, s[16:17] offset:2048" LNNT "\n\tglobal_load_dwordx4 v[112:115], v3, s[16:17] offset:3072" LNNT "\n\t"
      "s_branch .Lrole_l\n"
      "5:\n\t"
      "global_load_dwordx4 v[16:19], v2, s[4:5]\n\tglobal_load_dwordx4 v[20:23], v2, s[4:5] offset:1024\n\t"
      "s_lshl3_add_u32 s8, s45, s3\n\ts_lshl_b32 s9, s8, 1\n\tv_mov_b32 v9, s9\n\ts_lshl_b32 s9, s45, 11\n\tv_add_u32 v3, s9, v8\n\t"
      "s_waitcnt lgkmcnt(0)\n\tglobal_load_short_d16_hi v24, v9, s[26:27]\n\tglobal_load_ushort v160, v9, s[28:29]\n\t"
      "global_load_dword v96, v3, s[18:19]" PNT "\n\tglobal_load_dword v97, v3, s[20:21]" PNT "\n\t"
      "global_load_dword v68, v3, s[22:23]" PNT "\n\tglobal_load_dword v69, v3, s[24:25]" PNT "\n\ts_barrier\n\ts_branch .Lrole_c"
      : "={s3}"(w0)
      : "{s[0:1]}"(__builtin_amdgcn_kernarg_segment_ptr()), "{s2}"(blockIdx.x), "{v0}"(threadIdx.x), [pf] "n"(PFO), [pn] "n"(8 * PFN), [we] "n"(WS_END), [xr] "n"(XREG),
        [zm] "n"(8 * XREG), [zo] "n"(OX - 16), [sg] "n"(O_SG - 8192), [fc] "n"(FC), [ob] "n"(OB - 8192)
      : "memory");
#define TLREGS()
#define ROLE(name, text, ...) \
  gp x, wg, wu, wd; GL u16* out; rsrc_t rs; int b, w, t, l, g; u32 tag, st0; \
  asm volatile(".Lrole_" #name ":" : "={v0}"(t), "={v1}"(l) __VA_ARGS__ :: "memory"); \
  asm volatile(text : "={s2}"(b), "={s3}"(w), "={s[16:17]}"(x), "={s[32:33]}"(wg), "={s[34:35]}"(wu), "={s[36:37]}"(wd), \
               "={s[38:39]}"(out), "={s[40:43]}"(rs), "={s44}"(tag), "={s45}"(g), "={s46}"(st0) :: "memory"); \
  SB(); \
  __builtin_assume((u32)l < 64u); __builtin_assume((u32)t < 768u); __builtin_assume((u32)w < 12u); __builtin_assume((u32)b < 256u); __builtin_assume((u32)g < 128u); \
  TLREGS(); \
  const int xk = b & 7, j = (b >> 3) & 15, n16 = l & 15, kg = l >> 4
#define TAGWAIT() do { asm volatile("s_waitcnt lgkmcnt(0)" : "+{s44}"(tag)::"memory"); tag += 1u; } while (0)
#define BAR() do { SB(); asm volatile("s_barrier" ::: "memory"); SB(); } while (0)
#define FLAG(o, v) do { asm volatile("" ::: "memory"); SB(); *(volatile LDS u32*)(LDS char*)(smem + (o)) = (v); SB(); } while (0)
#define ARRIVE(o) do { SB(); if (l == 0) asm volatile("ds_add_u32 %0, %1" ::"v"(lds0 + (o)), "v"(1u) : "memory"); SB(); } while (0)
#define CWAIT(o, n, nap) do { _Pragma("unroll 1") for (int spin = 0; spin < SPIN; spin++) { \
      if (__builtin_amdgcn_readfirstlane((int)*(const volatile LDS u32*)(LDS char*)(smem + (o))) >= (int)(n)) break; \
      asm volatile(nap ::: "memory"); } SB(); asm volatile("" ::: "memory"); } while (0)
#define AWAIT(o, n, nap) do { _Pragma("unroll 1") for (int spin = 0; spin < SPIN; spin++) { \
      if (__all(*(const volatile LDS u32*)(LDS char*)(smem + (o)) >= (u32)(n))) break; \
      asm volatile(nap ::: "memory"); } SB(); asm volatile("" ::: "memory"); } while (0)
#define POLL8(res_, off_) do { u32 n_ = SPIN; \
    asm volatile("1:\n\tbuffer_load_dwordx2 v[4:5], %2, %3, 0 offen sc1\n\ts_sub_u32 %1, %1, 1\n\ts_waitcnt vmcnt(0)\n\t" \
                 "v_cmp_ne_u32 vcc, %4, v5\n\ts_cbranch_scc1 2f\n\ts_cbranch_vccnz 1b\n2:" \
                 : "=&{v[4:5]}"(res_), "+s"(n_) : "v"(off_), "s"(rs), "s"(tag) : "vcc", "scc", "memory"); } while (0)
#define UNTIL(t_) "2:\n\ts_sleep 1\n\ts_memrealtime s[8:9]\n\ts_waitcnt lgkmcnt(0)\n\ts_sub_u32 s8, s8, %[st]\n\ts_cmp_lt_u32 s8, " t_ "\n\ts_cbranch_scc1 2b\n\t"
#define LANE(v_) int v_; asm volatile("v_mbcnt_lo_u32_b32 %0, -1, 0\n\tv_mbcnt_hi_u32_b32 %0, -1, %0" : "=v"(v_))
#define IXO(l_) (O_IDX + (2 * xk + (j & 1)) * 128 + ((l_) & 7) * 8)
#define IDS() const int id = 16 * xk + j, sb = id * 57 >> 10, mat = id & 1, u = (id - 18 * sb) >> 1; \
    const bool slow = u >= 6; \
    const int part = slow ? u - 6 : u & 1, cb = 4 * sb + (slow ? 1 : (u >> 1) + (u > 1)), r0 = part * (slow ? 320 : 512), ns = slow ? 5 + (u >> 3) : 8; \
    const int kp = 4 * sb + ((u >> 1) & 3), ct = 2 * (u & 1) + mat
#define TS(a, b_)
#define TSC
#define DONE "s_endpgm\n"
#define TSD
#define TSGET(i0, i1, i2, i3)
  if (blockIdx.x >= 128) {
    if (w0 >= 8) {
      ROLE(d, "s_waitcnt lgkmcnt(0)");
      IDS();
      const int dw = w - 8;
      const u32 sw = (((n16 >> 1) ^ (kg & 1)) << 5) + ((l & 1) << 4);
      gp base = (mat ? wu : wg) + (size_t)(r0 + 4 * dw) * F + cb * 128;
      u32 v = kg * (2 * F) + sw, fa = lds0 + FC + 4 * dw, vp = 96 + dw, t_;
      asm volatile("" : "+v"(v), "+v"(fa), "+v"(vp));
      asm volatile("s_mov_b32 m0, %0" ::"s"(lds0 + 1024 * dw));
#define VMC "s_getreg_b32 s6, hwreg(HW_REG_IB_STS, 0, 4)\n\t"
#define RUN(k, q, lim, v_, b_, st_) asm volatile("1:\n\ts_lshr_b32 s6, m0, 4\n\ts_and_b32 s6, s6, 0xc0\n\tv_xor_b32 %[t], s6, %[v]\n\ts_waitcnt vmcnt(%[c])\n\t" VMC \
        "global_load_lds_dwordx4 %[t], %[b] nt\n\ts_add_u32 m0, m0, %[m]\n\tv_add_u32 %[v], %[d], %[v]\n\t.if %[m] == 3072\n\tv_mad_i32_i24 %[t], s6, -3, %[p]\n\tv_add_u32 %[p], 3, %[p]\n\t" \
        "s_mov_b64 exec, 1\n\tds_max_u32 %[a], %[t]\n\ts_mov_b64 exec, -1\n\t.endif\n\ts_cmp_lt_u32 m0, %[l]\n\ts_cbranch_scc1 1b" \
        : [v] "+v"(v_), [p] "+v"(vp), [t] "=&v"(t_) : [b] "s"(b_), [c] "n"((q) - 1), [m] "n"(1024 * (k)), [d] "n"((k) * (st_)), \
          [a] "v"(fa), [l] "s"(lim) : "scc", "s6", "memory")
#define MARK(c, val) "s_waitcnt vmcnt(" #c ")\n\t" val "\n\ts_mov_b64 exec, 1\n\tds_max_u32 %[a], %[t]\n\ts_mov_b64 exec, -1\n\t"
      __builtin_amdgcn_s_setprio(DPRIO);
      STAMP(8);
      if (id < 126) { if (slow) RUN(4, QAS, 32768, v, base, 8 * F); else RUN(4, 8, 32768, v, base, 8 * F); }
      if (dw < 3) {
        if (id >= 126) __builtin_amdgcn_endpgm();
        gp bd = wd + (size_t)(128 * kp + 2 * dw) * D + ct * 256;
        u32 vd = (l >> 5) * (2 * D + 32) ^ 16 * (l & 31);
        asm volatile("" : "+v"(vd));
        if (slow) RUN(3, QS, 16384 * ns, v, base, 8 * F);
        else { RUN(3, 8, 32768 + 3072 * RAMP, v, base, 8 * F); RUN(3, QD, 131072, v, base, 8 * F); }
        STAMP(1);
        {
          u32 n_ = SPIN;
          asm volatile("1:\n\t" VMC MARK(63, "v_mad_i32_i24 %[t], s6, -3, %[p]") "ds_read_b32 %[t], %[f]\n\ts_sub_u32 %[n], %[n], 1\n\ts_waitcnt lgkmcnt(0)\n\t"
                       "v_cmp_lt_u32 vcc, %[k], %[t]\n\ts_cbranch_scc1 2f\n\ts_cbranch_vccz 1b\n2:\n\ts_mov_b32 m0, %[m]"
                       : [t] "=&v"(t_), [n] "+s"(n_) : [a] "v"(fa), [p] "v"(vp), [f] "v"(u == 8 ? fa : lds0 + FC + 20), [k] "s"(u == 8 ? 159 : 16 * HOLD - 105), [m] "s"(lds0 + 1024 * dw)
                       : "vcc", "scc", "s6", "memory");
        }
        STAMP(3);
        if (u < 8) {
          RUN(3, QE, 65536, vd, bd, 4 * D);
          STAMP(6);
          asm volatile(MARK(0, "v_mov_b32 %[t], -1") : [t] "=&v"(t_) : [a] "v"(fa) : "memory");
          STAMP(4);
        }
        FLUSH();
        __builtin_amdgcn_endpgm();
      }
      asm volatile(MARK(0, "v_mov_b32 %[t], -1") : [t] "=&v"(t_) : [a] "v"(fa) : "memory");
      u32 km, rk;
      u32 hi, lo, k2, k3, gd, thr, sg = 0x0c0c0b09u;
      u32 ka = lds0 + FC + 384 + 8 * (l >> 3), kc = 2047 * 2048 + 2 * r0 + 16 * ((l & 7) ^ ((l >> 2) & 4)), kq = lds0 + FC + 512 + (l & 48), kr = lds0 + FC + 512 + 4 * n16;
      u32 c1 = 896, c2 = 1792, c3 = 2688, cm = (u32)-2048, cs = O_SG + 8 * 2047, ca = lds0 + FC + 512;
      rsrc_t rx = __builtin_amdgcn_make_buffer_rsrc((void*)(x - 1344), 0, 2 * L * D + 4096, 0x00020000);
      u32 co = O_CLR + 8 * (18 * sb + (0x84c0 >> 4 * (kp & 3) & 15) + min(l, (kp & 3) == 1 ? 5 : 3));
      asm volatile("" : "+v"(ka), "+v"(kc), "+v"(kq), "+v"(kr), "+v"(co));
      asm volatile("" : "+s"(c1), "+s"(c2), "+s"(c3), "+s"(cm), "+s"(cs), "+s"(ca), "+s"(sg), "+s"(rx));
      *(u32x4*)(smem + FA + 6144 + 16 * l) = u32x4{0u, 0u, 0u, 0u};
      *(u32x4*)(smem + FA + 7168 + 16 * l) = u32x4{0u, 0u, 0u, 0u};
      CWAIT(FC + 256, 1, "s_sleep 4");
      tag = (u32)__builtin_amdgcn_readfirstlane((int)*(const volatile LDS u32*)(LDS char*)(smem + FC + 24));
      {
        u32 n_ = SPIN, t0, t1, t2, t3, se;
#define TLX(a, b_, c, d, sc) "buffer_load_dwordx4 v[" #a ":" #b_ "], %[o], %[r], 0 offen" sc "\n\tbuffer_load_dwordx4 v[" #c ":" #d "], %[o], %[r], %[ts] offen" sc "\n\t"
#define TLD(a, b_, c, d) TLX(a, b_, c, d, " sc1")
#define TCK(r0, r1, r2, r3, r4, r5, r6, r7) "s_waitcnt vmcnt(4)\n\tv_xor_b32 %[t0], %[t], v" #r1 "\n\tv_bitop3_b32 %[t0], %[t0], %[t], v" #r3 " bitop3:0xf6\n\t" \
          "v_bitop3_b32 %[t0], %[t0], %[t], v" #r5 " bitop3:0xf6\n\tv_bitop3_b32 %[t0], %[t0], %[t], v" #r7 " bitop3:0xf6\n\t" \
          "v_cmp_ne_u32 vcc, 0, %[t0]\n\tv_mov_b32 %[hi], v" #r0 "\n\tv_mov_b32 %[lo], v" #r2 "\n\tv_mov_b32 %[k2], v" #r4 "\n\tv_mov_b32 %[k3], v" #r6 "\n\t" \
          "s_cbranch_vccz 5f\n\t" TLD(r0, r3, r4, r7)
        asm volatile("2:\n\ts_sleep 1\n\ts_memrealtime s[8:9]\n\ts_waitcnt lgkmcnt(0)\n\ts_sub_u32 s8, s8, %[st]\n\ts_cmp_lt_u32 s8, %[tw]\n\ts_cbranch_scc1 2b\n\t"
                     TLX(128, 131, 132, 135, SCT) "s_sleep " STR(RSL) "\n\t" TLD(136, 139, 140, 143) "s_sleep " STR(RSL) "\n\t" TLX(144, 147, 148, 151, SCU)
                     "1:\n\t" TCK(128, 129, 130, 131, 132, 133, 134, 135) TCK(136, 137, 138, 139, 140, 141, 142, 143) TCK(144, 145, 146, 147, 148, 149, 150, 151)
                     "s_sub_u32 %[n], %[n], 1\n\ts_cbranch_scc0 1b\n5:\n\t" TS(60, 61)
                     "v_and_or_b32 %[t0], %[hi], %[mk], %[pa]\n\tv_and_or_b32 %[t1], %[lo], %[mk], %[pb]\n\t"
                     "v_max_u32 %[t2], %[t0], %[t1]\n\tv_max_u32 %[t3], %[t0], %[t1]\n\tv_perm_b32 %[gd], %[hi], %[lo], %[p0]\n\tv_perm_b32 %[se], %[k2], %[k3], %[p0]\n\t"
                     "v_permlane16_swap_b32 %[t2], %[t3]\n\tv_max_u32 %[km], %[t2], %[t3]\n\tv_max_u32 %[rk], %[t2], %[t3]\n\t"
                     "v_max_u32 %[hi], %[t0], %[t1]\n\tv_min_u32 %[lo], %[t0], %[t1]\n\t"
                     "v_permlane32_swap_b32 %[km], %[rk]\n\tv_max_u32 %[km], %[km], %[rk]\n\tv_not_b32 %[t2], %[km]\n\t"
                     "v_and_or_b32 %[k2], %[k2], %[mk], %[pa]\n\t"
                     "v_not_b32_dpp %[t2], %[km] row_ror:4 row_mask:0x2 bank_mask:0xf\n\tv_not_b32_dpp %[t2], %[km] row_ror:8 row_mask:0x4 bank_mask:0xf\n\t"
                     "v_not_b32_dpp %[t2], %[km] row_ror:12 row_mask:0x8 bank_mask:0xf\n\t"
                     "v_pk_lshlrev_b16 %[gd], 4, %[gd] op_sel_hi:[0,1]\n\tv_add_u32 %[t0], %[km], %[t2]\n\t"
                     "v_add_u32_dpp %[t1], %[t2], %[km] row_ror:1 row_mask:0xf bank_mask:0xf\n\tv_add_u32_dpp %[t3], %[t2], %[km] row_ror:2 row_mask:0xf bank_mask:0xf\n\t"
                     "v_add_u32_dpp %[t2], %[t2], %[km] row_ror:3 row_mask:0xf bank_mask:0xf\n\t"
                     RANK8("%[t0]", "%[t1]", "%[t2]", "%[t3]", "%[rk]", "%[t3]", "-2", "v_lshrrev_b32 %[se], 4, %[se]\n\tv_perm_b32 %[gd], %[gd], %[se], %[p1]",
                           "v_pk_max_u16 %[gd], %[gd], %[gd] op_sel:[0,1] op_sel_hi:[1,0]\n\tv_and_or_b32 %[k3], %[k3], %[mk], %[pb]")
                     "v_cmp_eq_u32 vcc, 56, %[rk]\n\tv_max3_u32 %[se], %[lo], %[k2], %[k3]\n\tv_lshrrev_b32 %[gd], 1, %[gd]\n\ts_ff1_i32_b32 s6, vcc_lo\n\t"
                     "v_readlane_b32 s6, %[km], s6\n\tv_or_b32 %[gd], 0x7fff, %[gd]\n\tv_med3_u32 %[t2], %[lo], %[k2], %[k3]\n\t"
                     "v_cmp_le_u32 s[8:9], s6, %[hi]\n\tv_cmp_le_u32 s[10:11], s6, %[se]\n\tv_max_u32 %[t2], %[t2], %[gd]\n\t"
                     "v_mbcnt_lo_u32_b32 %[t0], s8, 0\n\tv_mbcnt_lo_u32_b32 %[t1], s10, -15\n\tv_mbcnt_hi_u32_b32 %[t0], s9, %[t0]\n\tv_mbcnt_hi_u32_b32 %[t1], s11, %[t1]\n\t"
                     "v_lshl_add_u32 %[t0], %[t0], 2, %[ca]\n\tv_mad_i32_i24 %[t1], %[t1], -4, %[ca]\n\tv_cmp_le_u32 vcc, s6, %[t2]\n\t"
                     "s_mov_b64 exec, s[8:9]\n\tds_write_b32 %[t0], %[hi]\n\ts_mov_b64 exec, s[10:11]\n\tds_write_b32 %[t1], %[se]\n\ts_mov_b64 exec, -1\n\t"
                     "ds_read_b32 %[se], %[kr]\n\tds_read_b128 v[152:155], %[kq]\n\t"
                     "s_bcnt1_i32_b64 s8, s[8:9]\n\ts_bcnt1_i32_b64 s10, s[10:11]\n\ts_add_u32 s8, s8, s10\n\ts_cmp_gt_u32 s8, 16\n\ts_cselect_b64 s[8:9], -1, vcc\n\t"
                     "s_cmp_lg_u64 s[8:9], 0\n\ts_waitcnt lgkmcnt(0)\n\ts_cbranch_scc1 6f\n\t"
                     "v_sub_u32 %[t0], %[se], v152\n\tv_sub_u32 %[t1], %[se], v153\n\tv_sub_u32 %[t2], %[se], v154\n\tv_sub_u32 %[t3], %[se], v155\n\t"
                     RANK8("%[t0]", "%[t1]", "%[t2]", "%[t3]", "%[rk]", "%[t3]", "0", "v_mov_b32 %[km], %[se]\n\ts_nop 0", "s_nop 1") "6:\n\t" TS(62, 63)
                     : [hi] "=&v"(hi), [lo] "=&v"(lo), [k2] "=&v"(k2), [k3] "=&v"(k3), [gd] "=&v"(gd), [se] "=&v"(se), [km] "=&v"(km), [rk] "=&v"(rk),
                       [t0] "=&v"(t0), [t1] "=&v"(t1), [t2] "=&v"(t2), [t3] "=&v"(t3), [n] "+s"(n_)
                     : [o] "v"(O_TOP + 16 * (l & 7) + TL * (l >> 3)), [r] "s"(rs), [t] "s"(tag), [ts] "s"(TOPS), [st] "s"(st0), [tw] "n"(TW), [mk] "s"(0x7fff800fu),
                       [pa] "v"((127 - 2 * l) << 4), [pb] "v"((126 - 2 * l) << 4), [sg] "s"(sg), [p0] "s"(0x05040100u), [p1] "s"(0x07020500u), [ca] "s"(ca),
                       [kr] "v"(kr), [kq] "v"(kq)
                     : "vcc", "scc", "s6", "s8", "s9", "s10", "s11", "memory" TSC);
      }
      unsigned long long more;
      asm volatile("" : "={s6}"(thr), "={s[8:9]}"(more));
      if (__builtin_expect(more != 0, 0)) {
        const u32 m0 = km;
        unsigned long long all = __ballot(gd >= thr);
        if (gd >= thr) hi = lo = k2 = k3 = 0u;
#pragma unroll 1
        for (int i = 0; i < 4;) {
          u32 k, m = m0;
          if (all) {
            const int a = __builtin_ctzll(all);
            all &= all - 1;
            u32x2 r;
            const u32 o = O_RG + 256 * a + 8 * (l & 31);
            POLL8(r, o);
            k = l < 32 ? r.x : 0u;
            m = (u32)__builtin_amdgcn_readlane((int)m0, a);
          } else { k = hi; hi = lo; lo = k2; k2 = k3; i++; }
          asm volatile("v_cmp_le_u32 s[8:9], %[t], %[k]\n\tv_cmp_ne_u32 vcc, %[k], %[m0]\n\ts_and_b64 s[8:9], s[8:9], vcc\n\ts_cbranch_scc0 2f\n"
                       "1:\n\ts_ff1_i32_b64 s7, s[8:9]\n\tv_readlane_b32 s12, %[k], s7\n\ts_lshl_b64 s[10:11], 1, s7\n\ts_nop 0\n\t"
                       "v_cmpx_gt_u32 vcc, s12, %[km]\n\tv_add_u32 %[rk], 8, %[rk]\n\ts_bcnt0_i32_b32 s7, vcc_lo\n\tv_cmpx_eq_u32 vcc, 0x80, %[rk]\n\t"
                       "v_mov_b32 %[km], s12\n\tv_lshlrev_b32 %[rk], 2, s7\n\ts_mov_b64 exec, -1\n\ts_andn2_b64 s[8:9], s[8:9], s[10:11]\n\ts_cbranch_scc1 1b\n2:"
                       : [km] "+v"(km), [rk] "+v"(rk) : [k] "v"(k), [m0] "v"(m), [t] "s"(thr) : "vcc", "scc", "s7", "s8", "s9", "s10", "s11", "s12");
        }
      }
      if (id < 126) {
        u32 a_, v_, n_ = SPIN;
#define RLD(o, s_) "buffer_load_dwordx4 %[v], %[rx], " s_ " offen offset:" #o " lds\n\t"
        asm volatile("v_add_u32 %[a], %[kb], %[rk]\n\tv_and_b32 %[v], 0x7ff, %[km]\n\ts_mov_b32 m0, %[fa]\n\tds_write_b32 %[a], %[v]\n\tds_read_b32 %[a], %[ka]\n\t"
                     "s_cmp_eq_u32 %[ns], 8\n\ts_waitcnt lgkmcnt(0)\n\tv_mad_i32_i24 %[v], %[a], %[cm], %[kc]\n\t"
                     RLD(0, "%[c3]") RLD(1024, "%[c2]") RLD(2048, "%[c1]") RLD(3072, "0")
                     "s_mov_b32 m0, %[fb]\n\tv_add_u32 %[v], 0x200, %[v]\n\tv_mad_i32_i24 %[a], %[a], -8, %[cs]\n\t"
                     "s_cbranch_scc0 3f\n\t" RLD(2048, "%[c1]") RLD(3072, "0")
                     "3:\n\tbuffer_load_dwordx2 v[160:161], %[a], %[rs], 0 offen sc1\n\t" RLD(0, "%[c3]") RLD(1024, "%[c2]") "buffer_load_dwordx2 v[158:159], %[co], %[rs], 0 offen sc1\n\t"
                     "v_mov_b32 %[v], 16 * " STR(HOLD) " - 112\n\tv_subrev_u32 %[ka], %[kd], %[ka]\n\ts_mov_b64 exec, 1\n\tds_add_u32 %[fc], %[v] offset:8\n\ts_mov_b64 exec, -1\n\tv_mov_b32 %[v], -1\n\t" TS(64, 65)
                     "s_sleep " STR(CSL) "\n\tbuffer_load_dwordx2 v[154:155], %[co], %[rs], 0 offen sc1\n\t"
                     "s_waitcnt vmcnt(4)\n1:\n\tv_cmp_ne_u32 vcc, %[t], v161\n\tv_and_b32 v156, 0xffff0000, v160\n\tv_lshlrev_b32 v157, 16, v160\n\tv_rcp_f32 v156, v156\n\t"
                     "s_sub_u32 %[n], %[n], 1\n\ts_cbranch_vccz 2f\n\ts_cbranch_scc1 2f\n\t"
                     "buffer_load_dwordx2 v[160:161], %[a], %[rs], 0 offen sc1\n\ts_waitcnt vmcnt(0)\n\ts_branch 1b\n2:\n\tv_mul_f32 v157, -v157, v156\n\t"
                     "ds_write_b64 %[ka], v[156:157]\n\ts_waitcnt vmcnt(2)\n\tds_write_b32 %[fc], %[v] offset:4\n\t" TS(66, 67)
                     "s_mov_b32 %[n], %[spin]\n\ts_waitcnt vmcnt(1)\n\tv_cmp_ne_u32 vcc, %[t], v159\n\ts_cbranch_vccz 5f\n"
                     "4:\n\ts_waitcnt vmcnt(0)\n\tv_cmp_ne_u32 vcc, %[t], v155\n\ts_sub_u32 %[n], %[n], 1\n\ts_cbranch_vccz 5f\n\t"
                     "buffer_load_dwordx2 v[154:155], %[co], %[rs], 0 offen sc1\n\ts_cbranch_scc0 4b\n5:"
                     : [a] "=&v"(a_), [v] "=&v"(v_), [ka] "+v"(ka), [n] "+s"(n_)
                     : [co] "v"(co), [rk] "v"(rk), [km] "v"(km), [kc] "v"(kc), [kb] "n"(FC + 384), [kd] "n"(384 - 32), [fa] "n"(FA), [fb] "n"(FA + 4096), [rx] "s"(rx), [rs] "s"(rs),
                       [c1] "s"(c1), [c2] "s"(c2), [c3] "s"(c3), [cm] "s"(cm), [cs] "s"(cs), [ns] "s"(ns), [fc] "v"(fa), [t] "s"(tag), [spin] "n"(SPIN) : "vcc", "scc", "memory" TSC);
        TSGET(12, 13, 14, 15);
        FLAG(FC + 28, 1u);
        STAMP(10);
      } else {
        const u32 cd = km << 1;
        const u32x2 r = {(2047u - (km & 0x7ffu)) | ((cd ^ ((u32)((int)~cd >> 15) | 0x80000000u)) & 0xffff0000u), tag};
        const u32 o = rk < 64u ? O_IDX + 1024 * mat + 128 * kg + rk : 0x80000000u;
        st8(rs, o, r);
        st8(rs, o + 512, r);
        TSGET(12, 13, 14, 15);
        STAMP(14);
        STAMP(15);
        if (mat) {
#pragma unroll 1
          for (int spin = 0; spin < SPIN; spin++) {
            const u32x4 a = ld16(rs, O_ARR + 16 * l);
            if (__all(a.y == tag && a.w == tag)) break;
            asm volatile("s_sleep 16" ::: "memory");
          }
          if (l == 0) __builtin_amdgcn_raw_buffer_store_b32(tag, rs, 0, 0, 16);
        }
      }
      FLUSH();
      __builtin_amdgcn_endpgm();
    }
    u32x4 xv[4];
    u32x8 wv;
    ROLE(l, "", , "={v[100:103]}"(xv[0]), "={v[104:107]}"(xv[1]), "={v[108:111]}"(xv[2]), "={v[112:115]}"(xv[3]), "={v[116:123]}"(wv));
    STAMP(11);
    u32 sg = 0x0c0c0b09u;
    asm volatile("" : "+s"(sg));
    ln_rows(xv, wv, rs, tag, b, g, l, lds0 + FC + 128, sg TLVAL);
    STAMP(10);
    IDS();
    if (id >= 126) {
      FLUSH();
      __builtin_amdgcn_endpgm();
    }
    const int wr = 16 * (kg >> 1) + 4 * (kg & 1) + (n16 & 8) + ((n16 >> 2) & 1);
    u32 wa = lds0 + 256 * wr + ((w ^ ((wr & 1) | ((wr >> 1) & 6))) << 5) + 8 * (l & 3), sel = (l & 8) ? 0xeeeeu : 0x4444u;
    u32 so = 4 * t;
    asm volatile("" : "+v"(wa), "+v"(sel), "+v"(so));
    asm volatile(UNTIL(STR(CLD)) :: [st] "s"(st0) : "s8", "s9", "scc");
    __builtin_amdgcn_raw_buffer_store_b32(~0u, rs, so, O_HG + (6 * cb + 3 * mat + part) * 2048, 16);
    st8(rs, u < 8 ? O_PG + (4 * kp + ct) * 4096 + 2 * so : 0x80000000u, u32x2{~0u, ~0u});
#define SWP(a0, a1, a2, a3) "v_permlane32_swap_b32 " a0 ", " a2 "\n\tv_permlane32_swap_b32 " a1 ", " a3 "\n\t"
#define OUT(a0, a1, o) "v_cvt_pk_bf16_f32 " a0 ", " a0 ", " a1 "\n\tv_min_u32 " a0 ", -2, " a0 "\n\tbuffer_store_dword " a0 ", %[so], %[rs], " o " sc1\n\t"
#define TR4(a, b_, c, d, e, f, g_, h, adr, o) "ds_read_b64_tr_b16 v[" #a ":" #b_ "], " adr " offset:" #o "\n\tds_read_b64_tr_b16 v[" #c ":" #d "], " adr " offset:" #o "+512\n\t" \
      "ds_read_b64_tr_b16 v[" #e ":" #f "], " adr " offset:" #o "+8192\n\tds_read_b64_tr_b16 v[" #g_ ":" #h "], " adr " offset:" #o "+8704\n\t"
#define SM(d, a, b_) "v_smfmac_f32_16x16x64_bf16 v[" d "], v[" a "], v[" b_ "], %[sel]\n\t"
#define USE(c, xa, wf) "s_add_u32 s6, s6, 16\n\ts_waitcnt lgkmcnt(" #c ")\n\t" SM("16:19", "20:23", wf) SM("8:11", xa, wf)
#define XR(xa, o) "ds_read_b128 v[" xa "], %[xa] offset:" #o "\n\t"
#define XU(c, xa, wf) "s_waitcnt lgkmcnt(" #c ")\n\t" SM("8:11", xa, wf)
#define ZERO(r) "v_mfma_f32_32x32x16_bf16 v[" r "], v[8:11], v[8:11], 0\n\t"
#define POLL(c, rd) "7:\n\tds_read_b32 v30, %[fl]\n\ts_waitcnt lgkmcnt(0)\n\ts_call_b64 s[10:11], 1f\n\ts_cbranch_scc1 7b\n\t" rd
#define CLR "s_waitcnt vmcnt(0)\n\ts_mov_b64 exec, 1\n\tds_add_rtn_u32 v14, %[fl], v28 offset:96\n\ts_waitcnt lgkmcnt(0)\n\tv_cmpx_eq_u32 vcc, 7, v14\n\t" \
      "buffer_store_dwordx2 v[12:13], off, %[rs], %[co] sc1\n\ts_mov_b64 exec, -1\n\t"
#define XE(k) "s_cmpk_eq_u32 s6, " #k "\n\ts_cbranch_scc1 9f\n\t"
    asm volatile("v_mov_b64 v[8:9], 0\n\tv_mov_b64 v[10:11], 0\n\tv_mov_b64 v[16:17], 0\n\tv_mov_b64 v[18:19], 0\n\t"
                 ZERO("96:111") ZERO("112:127")
                 "v_mov_b32 v20, 0x3f803f80\n\tv_mov_b32 v21, v20\n\tv_mov_b64 v[22:23], v[20:21]\n\tv_mov_b32 v13, %[t]\n\tv_mov_b32 v28, 1\n\tv_mov_b32 v30, 0\n\t"
                 "v_add_u32 v29, 0x10000, %[wa]\n\ts_movk_i32 s6, 96\n\ts_mov_b32 s7, 1\n\ts_mov_b32 s8, 15\n\ts_mov_b32 s9, %[spin]\n\ts_nop 7\n\t"
                 POLL(8, TR4(32, 33, 34, 35, 36, 37, 38, 39, "%[wa]", 0) TR4(40, 41, 42, 43, 44, 45, 46, 47, "%[wa]", 16384))
                 USE(4, "96:99", "32:39") "s_waitcnt lgkmcnt(0)\n\t" SM("16:19", "20:23", "40:47") SM("8:11", "100:103", "40:47")
                 CLR
                 POLL(4, TR4(48, 49, 50, 51, 52, 53, 54, 55, "%[wa]", 32768)) USE(0, "104:107", "48:55")
                 POLL(4, TR4(56, 57, 58, 59, 60, 61, 62, 63, "%[wa]", 49152)) "s_mov_b64 exec, 1\n\tds_add_u32 %[fl], v28 offset:20\n\ts_mov_b64 exec, -1\n\t" TS(60, 61)
                 USE(0, "108:111", "56:63")
                 POLL(4, TR4(64, 65, 66, 67, 68, 69, 70, 71, "v29", 0)) USE(0, "112:115", "64:71") "s_cmp_eq_u32 %[ns], 5\n\ts_cbranch_scc1 5f\n\t"
                 POLL(4, TR4(72, 73, 74, 75, 76, 77, 78, 79, "v29", 16384)) USE(0, "116:119", "72:79") "s_cmp_eq_u32 %[ns], 6\n\ts_cbranch_scc1 5f\n\t"
                 POLL(4, TR4(80, 81, 82, 83, 84, 85, 86, 87, "v29", 32768)) USE(0, "120:123", "80:87")
                 POLL(4, TR4(88, 89, 90, 91, 92, 93, 94, 95, "v29", 49152)) USE(0, "124:127", "88:95")
                 "5:\n\t" TS(62, 63) "s_cmp_eq_u32 s7, 0\n\ts_cbranch_scc1 4f\n\ts_mov_b32 s8, 16\n\t"
                 "s_getpc_b64 s[10:11]\n\tds_read_b32 v30, %[fl]\n\ts_waitcnt lgkmcnt(0)\n"

                 "1:\n\tv_cmp_le_u32 vcc, s6, v30\n\ts_and_b32 s12, vcc_hi, s7\n\ts_cbranch_scc1 3f\n"
                 "2:\n\ts_andn2_b32 s12, s8, vcc_lo\n\ts_cbranch_scc1 6f\n\ts_setpc_b64 s[10:11]\n"
                 "6:\n\ts_sub_u32 s9, s9, 1\n\ts_cbranch_scc1 4f\n\t" GNAP "\n\ts_cmp_eq_u32 s6, s6\n\ts_setpc_b64 s[10:11]\n"
                 "3:\n\t"
                 XR("96:99", 0) XR("100:103", 1024) XR("104:107", 2048) XR("108:111", 3072) XR("112:115", 4096) XR("116:119", 5120) XR("120:123", 6144) XR("124:127", 7168)
                 "ds_read_b128 v[24:27], %[pa]\n\ts_mov_b32 s7, 0\n\t"
                 TS(64, 65)
                 XE(96) XU(8, "96:99", "32:39") XU(7, "100:103", "40:47") XE(112) XU(6, "104:107", "48:55") XE(128) XU(5, "108:111", "56:63") XE(144)
                 XU(4, "112:115", "64:71") XE(160) XU(3, "116:119", "72:79") XE(176) XU(2, "120:123", "80:87") XE(192) XU(1, "124:127", "88:95")
                 "9:\n\ts_cmp_eq_u32 s8, 16\n\ts_waitcnt lgkmcnt(0)\n\ts_cbranch_scc0 2b\n"
                 "4:\n\ts_nop 4\n\tv_permlane32_swap_b32 v16, v17\n\tv_add_f32 v16, v16, v17\n\tv_mul_f32 v25, v25, v16\n\tv_mul_f32 v27, v27, v16\n\t"
                 SWP("v8", "v9", "v10", "v11") "v_fmac_f32 v25, v10, v24\n\tv_fmac_f32 v27, v11, v26\n\tv_fma_f32 v8, v8, v24, v25\n\tv_fma_f32 v9, v9, v26, v27\n\t"
                 OUT("v8", "v9", "%[ho] offen") TS(66, 67)
                 :: [wa] "v"(wa), [sel] "v"(sel), [so] "v"(so), [fl] "v"(lds0 + FC + 4 * min(l, 4)), [xa] "v"(lds0 + FA + 128 * (l & 7) + 16 * ((2 * kg + (n16 >> 3)) ^ (2 * l & 4))),
                    [pa] "v"(lds0 + FC + 32 + 32 * (kg & 1) + 16 * (kg >> 1)), [ns] "s"(ns), [t] "s"(tag), [co] "s"(O_CLR + 8 * id), [rs] "s"(rs), [ho] "s"(O_HG + (6 * cb + 3 * mat + part) * 2048), [spin] "n"(SPIN)
                 : "vcc", "scc", "s6", "s7", "s8", "s9", "s10", "s11", "s12", "s13", "memory", G_CLOBBER TSC);
    TSGET(1, 4, 3, 5);
    if (u < 8) {
      const int tp = 2 * (kg & 1) + (kg >> 1), r7 = 4 * (kg & 1) + (n16 >> 2);
      u32 o3 = (kp & 3) == 1 ? so + 4096 : 0x80000000u, h0 = O_HG + kp * 12288, h1 = h0 + 6144;
      u32 hw = lds0 + FH + 2 * tp * HP + 32 * w + 8 * (l & 2) + (l & 4) * 2 + ((l & 8) >> 1) + 2 * (l & 1);
      u32 hr = lds0 + FH + (l & 7) * HP + 32 * kg + 2 * (l & 8);
      u32 da = lds0 + 512 * (16 * (kg >> 1) + r7) + ((2 * w ^ r7) << 5) + 8 * (l & 3), db = da ^ 32, fl = lds0 + FC + 4 * (l & 3);
      u32 po = 512 * w + 16 * (l & 8) + 32 * tp + 4 * (l & 7);
      u32 e0 = 0x3f80u, e1 = 0x3f800000u;
      asm volatile("" : "+v"(hw), "+v"(hr), "+v"(da), "+v"(db), "+v"(fl), "+v"(po), "+v"(o3));
      asm volatile("" : "+s"(e0), "+s"(e1), "+s"(h0), "+s"(h1));
#define TRD(a, b_, c, d, e, f, g_, h, adr, o) "ds_read_b64_tr_b16 v[" #a ":" #b_ "], " adr " offset:" #o "\n\tds_read_b64_tr_b16 v[" #c ":" #d "], " adr " offset:" #o "+4096\n\t" \
      "ds_read_b64_tr_b16 v[" #e ":" #f "], " adr " offset:" #o "+16384\n\tds_read_b64_tr_b16 v[" #g_ ":" #h "], " adr " offset:" #o "+20480\n\t"
#define HLD(X, r) X(r "0", "%[s1]", "%[h0] offen") X(r "3", "%[s1]", "%[h1] offen") X(r "1", "%[s1]", "%[h0] offen offset:2048") X(r "4", "%[s1]", "%[h1] offen offset:2048") \
      X(r "2", "%[o3]", "%[h0] offen") X(r "5", "%[o3]", "%[h1] offen")
#define HL0(a, o, s_) "buffer_load_dword " a ", " o ", %[rs], " s_ " sc1\n\t"
#define HLF(a, o, s_) "buffer_load_dword " a ", " o ", %[rs], " s_ SCH "\n\t"
#define HL1(a, o, s_) "v_cmpx_eq_u32 vcc, -1, " a "\n\t" HL0(a, o, s_) "s_mov_b64 exec, -1\n\t"
#define DOT(d, e, a) "v_dot2c_f32_bf16 " d ", %[" e "], " a "\n\t"
#define HCK(c, r) "s_cmp_eq_u32 s7, 0\n\ts_cbranch_scc1 5f\n\ts_call_b64 s[10:11], 4f\n5:\n\ts_waitcnt vmcnt(" #c ")\n\tv_max3_u32 v28, " r "0, " r "1, " r "2\n\tv_lshlrev_b32 v24, 16, " r "0\n\tv_max3_u32 v28, v28, " r "3, " r "4\n\t" \
      "v_and_b32 v25, %[hm], " r "0\n\tv_max_u32 v28, v28, " r "5\n\t" DOT("v24", "e0", r "1") "v_cmp_eq_u32 vcc, -1, v28\n\t" DOT("v25", "e1", r "1") DOT("v24", "e0", r "2") DOT("v25", "e1", r "2") \
      "v_lshlrev_b32 v26, 16, " r "3\n\tv_and_b32 v27, %[hm], " r "3\n\t" DOT("v26", "e0", r "4") DOT("v27", "e1", r "4") DOT("v26", "e0", r "5") DOT("v27", "e1", r "5")
#define RB2(a, b_) "v_cvt_pk_bf16_f32 " a ", 0, " a "\n\tv_cvt_pk_bf16_f32 " b_ ", 0, " b_ "\n\t"
      asm volatile("s_mov_b32 s7, 1\n\ts_mov_b32 s12, %[spin]\n\tv_mov_b64 v[8:9], 0\n\tv_mov_b64 v[10:11], 0\n\tv_mov_b64 v[12:13], 0\n\tv_mov_b64 v[14:15], 0\n"
                   "2:\n\ts_sleep 1\n\tds_read_b32 v28, %[fl] offset:16\n\ts_memrealtime s[8:9]\n\ts_waitcnt lgkmcnt(0)\n\tv_cmp_eq_u32 vcc, 0, v28\n\ts_sub_u32 s8, s8, %[st]\n\t"
                   "s_cmp_lt_u32 s8, %[th]\n\ts_cbranch_scc1 2b\n\ts_bitcmp1_b32 vcc_lo, 3\n\ts_cbranch_scc1 2b\n\t" TS(64, 65)
                   HLD(HLF, "v7") HCK(0, "v7") "s_cbranch_vccnz 9f\n"
                   "3:\n\t" TS(66, 67) RB2("v24", "v25") "v_mul_f32 v28, %[le], v24\n\tv_mul_f32 v29, %[le], v25\n\tv_exp_f32 v28, v28\n\tv_exp_f32 v29, v29\n\t" RB2("v26", "v27")
                   "v_add_f32 v28, 1.0, v28\n\tv_add_f32 v29, 1.0, v29\n\tv_rcp_f32 v28, v28\n\tv_rcp_f32 v29, v29\n\ts_cmp_eq_u32 s7, 0\n\tv_mul_f32 v24, v24, v28\n\tv_mul_f32 v25, v25, v29\n\t"
                   RB2("v24", "v25") "v_mul_f32 v24, v24, v26\n\tv_mul_f32 v25, v25, v27\n\tv_cvt_pk_bf16_f32 v24, v24, v25\n\ts_mov_b32 s12, %[spin]\n\t"
                   "ds_write_b16 %[hw], v24\n\tds_write_b16_d16_hi %[hw], v24 offset:%[hp]\n\ts_cbranch_scc1 8f\n"
                   "7:\n\ts_call_b64 s[10:11], 4f\n\ts_mul_i32 s12, s12, s7\n\ts_sub_u32 s12, s12, 1\n\ts_cbranch_scc0 7b\n"
                   "8:\n\ts_waitcnt lgkmcnt(0)\n\ts_barrier\n\t" TS(60, 61)
                   XR("16:19", 0) XR("20:23", 128)
                   XU(1, "16:19", "32:39") SM("12:15", "16:19", "40:47") XU(0, "20:23", "48:55") SM("12:15", "20:23", "56:63") "s_nop 6\n\t"
                   SWP("v8", "v9", "v10", "v11") "v_add_f32 v8, v8, v10\n\tv_add_f32 v9, v9, v11\n\t" SWP("v12", "v13", "v14", "v15") OUT("v8", "v9", "%[po] offen")
                   "v_add_f32 v12, v12, v14\n\tv_add_f32 v13, v13, v15\n\t" OUT("v12", "v13", "%[po] offen offset:256") TS(62, 63) DONE
                   "9:\n\tv_mov_b64 v[80:81], v[70:71]\n\tv_mov_b64 v[82:83], v[72:73]\n\tv_mov_b64 v[84:85], v[74:75]\n\t" HLD(HL1, "v7") "s_sleep " STR(HNAP) "\n"
                   "1:\n\t" HLD(HL1, "v8") HCK(6, "v7") "s_cbranch_vccz 3b\n\t" HLD(HL1, "v7") HCK(6, "v8") "s_sub_u32 s12, s12, 1\n\ts_cbranch_vccz 3b\n\ts_cbranch_scc0 1b\n\ts_branch 3b\n"
                   "4:\n\tds_read_b32 v28, %[fl]\n\ts_waitcnt lgkmcnt(0)\n\tv_cmp_ne_u32 vcc, -1, v28\n\ts_cbranch_vccnz 6f\n\t"
                   TRD(32, 33, 34, 35, 36, 37, 38, 39, "%[da]", 0) TRD(40, 41, 42, 43, 44, 45, 46, 47, "%[db]", 0)
                   TRD(48, 49, 50, 51, 52, 53, 54, 55, "%[da]", 32768) TRD(56, 57, 58, 59, 60, 61, 62, 63, "%[db]", 32768) "s_mov_b32 s7, 0\n"
                   "6:\n\ts_setpc_b64 s[10:11]\n0:"
                   :: [s1] "v"(so), [o3] "v"(o3), [fl] "v"(fl), [da] "v"(da), [db] "v"(db), [hw] "v"(hw), [xa] "v"(hr), [sel] "v"(sel), [so] "v"(po), [rs] "s"(rs), [h0] "s"(h0), [h1] "s"(h1),
                      [st] "s"(st0), [th] "n"(TH), [spin] "n"(SPIN), [hp] "n"(HP), [po] "s"(O_PG + (4 * kp + ct) * 4096), [e0] "s"(e0), [e1] "s"(e1), [hm] "s"(0xffff0000u),
                      [le] "s"(-1.4426950408889634f)
                   : "vcc", "scc", "s7", "s8", "s9", "s10", "s11", "s12", "memory", G_CLOBBER TSC);
      TSGET(8, 9, 6, 7);
    }
    FLUSH();
    __builtin_amdgcn_endpgm();
  }

  if (w0 >= 8) {
    u32x8 xa0, xa1, xa2, xa3, sa1;
    u32x4 junk;
    u32 op, so;
    ROLE(p, "", , "={v[100:107]}"(xa0), "={v[108:115]}"(xa1), "={v[116:123]}"(xa2),
         "={v148}"(op), "={v149}"(so), "={v[160:163]}"(junk));
    const int p = w - 8;
    __builtin_amdgcn_s_setprio(3);
    asm volatile("s_waitcnt lgkmcnt(0)" : "+{s44}"(tag)::"memory");
    tag += 1u;
    STAMP(9);
    f32x2 xq[4][8];
    u32x8 pa;
    {
#define SCK(r1, r3, r5, r7) "v_xor_b32 %[d], %[t], v" #r1 "\n\tv_bitop3_b32 %[d], %[d], %[t], v" #r3 " bitop3:0xf6\n\t" \
      "v_bitop3_b32 %[d], %[d], %[t], v" #r5 " bitop3:0xf6\n\tv_bitop3_b32 %[d], %[d], %[t], v" #r7 " bitop3:0xf6\n\t"
#define SLX(r0, r3, r4, r7, k, k1, sc) "buffer_load_dwordx4 v[" #r0 ":" #r3 "], %[so], %[rs], " k " offen" sc "\n\tbuffer_load_dwordx4 v[" #r4 ":" #r7 "], %[so], %[rs], " k1 " offen" sc "\n\t"
#define SLD(r0, r3, r4, r7, k, k1) SLX(r0, r3, r4, r7, k, k1, " sc1")
#define SLOOP(r0, r1, r3, r4, r5, r7, k, k1) "1:\n\ts_waitcnt vmcnt(0)\n4:\n\t" SCK(r1, r3, r5, r7) "s_sub_u32 %[n], %[n], 1\n\tv_cmp_ne_u32 vcc, 0, %[d]\n\ts_cbranch_scc1 2f\n\ts_cbranch_vccz 2f\n\t" \
      SLD(r0, r3, r4, r7, k, k1) "s_branch 1b\n2:\n\t"
      u32 d_, n_ = SPIN, k1 = 4096, k2 = 8192, k3 = 12288, s0[4];
      asm volatile("s_mov_b32 m0, %[m]\n\ts_cmp_eq_u32 %[k], 3\n\ts_waitcnt vmcnt(2)\n\ts_cbranch_scc0 1f\n\ts_waitcnt vmcnt(0)\n1:"
                   : "+{v[100:107]}"(xa0), "+{v[108:115]}"(xa1), "+{v[160:163]}"(junk) : [m] "s"(lds0 + OX + 2 * XO + 256 * p), [k] "s"(xk) : "scc", "memory");
      SB();
#define UNPACK(i, xs) _Pragma("unroll") for (int c = 0; c < 8; c++) { \
        xq[i][c] = (c & 1) ? f32x2{hi2f(xs[c >> 1]), hi2f(xs[4 + (c >> 1)])} : f32x2{lo2f(xs[c >> 1]), lo2f(xs[4 + (c >> 1)])}; asm volatile("" : "+v"(xq[i][c].x), "+v"(xq[i][c].y)); }
      UNPACK(0, xa0);
      UNPACK(1, xa1);
      u32 fa = lds0 + OXF + 4 * p, fv = 1;
      asm volatile("" : "+v"(fa), "+v"(fv));
      asm volatile("" : "+s"(k1), "+s"(k2), "+s"(k3), "+s"(n_));
      SB();
      STAMP(1);
#define SCP(r0, r1, r2, r3, r4, r5, r6, r7) "s_waitcnt vmcnt(2)\n\t" SCK(r1, r3, r5, r7) "v_cmp_ne_u32 vcc, 0, %[d]\n\tv_mov_b32 %[a0], v" #r0 "\n\tv_mov_b32 %[a1], v" #r2 "\n\t" \
      "v_mov_b32 %[a2], v" #r4 "\n\tv_mov_b32 %[a3], v" #r6 "\n\ts_cbranch_vccz 3f\n\t" SLD(r0, r3, r4, r7, "0", "%[k1]")
      asm volatile(UNTIL(STR(TSG)) SLD(132, 135, 136, 139, "0", "%[k1]") "s_sleep " STR(SSL) "\n\t" SLD(140, 143, 144, 147, "0", "%[k1]") "s_waitcnt vmcnt(4)"
                   : "=&{v[132:139]}"(pa), "=&{v[140:147]}"(sa1), "+{v[116:123]}"(xa2)
                   : [st] "s"(st0), [so] "v"(so), [rs] "s"(rs), [k1] "s"(k1) : "scc", "s8", "s9", "memory");
      UNPACK(2, xa2);
      SB();
      asm volatile("1:\n\t" SCP(132, 133, 134, 135, 136, 137, 138, 139) SCP(140, 141, 142, 143, 144, 145, 146, 147) "s_sub_u32 %[n], %[n], 1\n\ts_cbranch_scc0 1b\n"
                   "3:\n\t" SLD(140, 143, 144, 147, "%[k2]", "%[k3]")
                   : [d] "=&v"(d_), [n] "+s"(n_), [a0] "=&v"(s0[0]), [a1] "=&v"(s0[1]), [a2] "=&v"(s0[2]), [a3] "=&v"(s0[3]), "+{v[132:139]}"(pa), "+{v[140:147]}"(sa1)
                   : [t] "s"(tag), [so] "v"(so), [rs] "s"(rs), [k1] "s"(k1), [k2] "s"(k2), [k3] "s"(k3) : "vcc", "scc", "memory");
#pragma unroll
      for (int i = 0; i < 5; i++) {
#define XLD(r0, r1, r2, r3, o_) "v_add_u32 %[d], " #o_ ", %[o]\n\tglobal_load_dwordx4 v[" #r0 ":" #r1 "], %[d], %[x]\n\tglobal_load_dwordx4 v[" #r2 ":" #r3 "], %[d], %[x] offset:2048\n"
        if (i == XI) asm volatile("s_cmp_eq_u32 %[k], 3\n\ts_cbranch_scc0 1f\n\t" XLD(116, 119, 120, 123, 0x200000) "1:\n\t" XLD(124, 127, 128, 131, 0x300000)
                                 : [d] "=&v"(d_), "+{v[116:123]}"(xa2), "=&{v[124:131]}"(xa3) : [k] "s"(xk), [o] "v"(op), [x] "s"(x) : "scc", "memory");
        if (i == 2) {
          asm volatile("s_waitcnt vmcnt(2)\n\ts_branch 4f\n" SLOOP(140, 141, 143, 144, 145, 147, "%[k2]", "%[k3]") : [d] "=&v"(d_), [n] "+s"(n_), "+{v[140:147]}"(sa1), "+{v[116:123]}"(xa2), "+{v[132:139]}"(pa)
                       : [t] "s"(tag), [so] "v"(so), [rs] "s"(rs), [k2] "s"(k2), [k3] "s"(k3) : "vcc", "scc", "memory");
          if (xk == 3) { UNPACK(2, xa2); }
        }
        if (i == 3) asm volatile("s_waitcnt vmcnt(%1)" : "+{v[124:131]}"(xa3) : "n"(DPI == 2 ? DPF : 0));
        if (i == DPI && DPF) {
          u32 lx = l;
          asm volatile("" : "+v"(lx));
          const u32 ti = min(16 * xk + 4 * p + (lx >> 4), 125u), tsb = __umul24(ti, 57) >> 10, tu = min(ti - __umul24(18, tsb), 15u);
          const u32 o = (128 * (4 * tsb + (tu >> 2)) + DPO / 2 + 4 * j + ((lx >> 2) & 3)) * (2 * D) + 512 * (tu & 3) + 128 * (lx & 3);
          if (DPI == 4) AWAIT(OXF + 4 * (l & 3), 3, "s_sleep 1");
          if (DPF == 1) asm volatile("global_load_dword v160, %1, %2" : "=&{v[160:163]}"(junk) : "v"(o), "s"(wd) : "memory");
          else asm volatile("global_load_dword v160, %1, %2\n\tglobal_load_dword v161, %1, %3" : "=&{v[160:163]}"(junk) : "v"(o), "s"(wd), "s"(wd + 64 * D) : "memory");
        }
        if (i == 4) break;
        if (i == 3) UNPACK(3, xa3);
        const u32 sa = i < 2 ? s0[2 * i] : sa1[4 * i - 8], sb = i < 2 ? s0[2 * i + 1] : sa1[4 * i - 6];
        if (i == 0) STAMP(3);
        if (i == 2) STAMP(10);
        const f32x2 r2 = {__builtin_amdgcn_rcpf(hi2f(sa)), __builtin_amdgcn_rcpf(hi2f(sb))};
        const f32x2 m2 = {-lo2f(sa) * r2.x, -lo2f(sb) * r2.y};
        f32x2 n2[8];
        u32 o[8];
        SB();
#pragma unroll
        for (int c = 0; c < 8; c++) n2[c] = f32x2{fmav(xq[i][c].x, r2.x, m2.x), fmav(xq[i][c].y, r2.y, m2.y)};
        SB();
#pragma unroll
        for (int c = 0; c < 8; c++) o[c] = packh(n2[c].x, n2[c].y);
        SB();
#pragma unroll
        for (int c = 0; c < 8; c++) asm volatile("ds_write_addtid_b32 %0 offset:%1" ::"v"(o[c]), "n"(c * XREG + 1024 * i) : "memory");
        asm volatile("ds_write_b32 %1, %0\n\tv_add_u32 %0, 1, %0" : "+v"(fv) : "v"(fa) : "memory");
        if (i == PDROP) __builtin_amdgcn_s_setprio(PPRIO);
        SB();
      }
    }
    STAMP(4);
    const rsrc_t ro = __builtin_amdgcn_make_buffer_rsrc((void*)out, 0, 2 * L * D, 0x00020000);
    u32 ys[8], y[2][8] = {};
    u32x2 ix;
    u32x4 cl;
    u32x8 pb;
    float yc, rsel;
    const u32 ixo = IXO((l & 6) + (n16 >> 3));
    if (p == 0) asm volatile("buffer_load_dwordx2 v[4:5], %1, %2, 0 offen sc1" : "=&{v[4:5]}"(ix) : "v"(ixo), "s"(rs) : "memory");
    const int rc = 4 * (l & 1) + kg, u = 64 * (p & 1) + l;
    u32x4 pick;
    u32 po = O_PG + 128 * g + 16 * (l & 7) + 16384 * (l >> 3), p3 = l < 32 ? po + 3 * 131072 : 0x80000000u, yf = lds0 + OYP + 4 * min(n16, 12) + 64 * (p >> 1);
    u32 ya = lds0 + OY + CHR * rc, oo = 16 * g + 2 * rc, clo = O_CLR + 16 * min(l, 62);
    u32 yp = lds0 + OY + 512 * (p >> 1) + 4 * (u ^ ((u >> 4) & 6)), yq = yp + 4 * CHR, bp = lds0 + OMISC + 16 * p + 4 * kg, bs = 30 - 2 * n16;
#pragma unroll
    for (int q = 0; q < 4; q++) pick[q] = (n16 >> 2) == q ? 0x3f80u << 16 * (l & 1) : 0u;
    asm volatile("" : "+v"(pick), "+v"(ya), "+v"(oo), "+v"(po), "+v"(p3), "+v"(yf), "+v"(clo), "+v"(yp), "+v"(yq), "+v"(bp), "+v"(bs));
#pragma unroll
    for (int c = 0; c < 8; c++) asm volatile("" : "+v"(y[0][c]), "+v"(y[1][c]));
    if (TE) asm volatile(UNTIL(STR(TE)) :: [st] "s"(st0) : "s8", "s9", "scc");
#pragma unroll
    for (int i = 0; i < 4; i++) {
      if (i == IXI && p == 0) {
        u32 n_ = SPIN;
        asm volatile("s_waitcnt vmcnt(%5)\n1:\n\tv_cmp_ne_u32 vcc, %4, v5\n\ts_sub_u32 %1, %1, 1\n\ts_cbranch_vccz 2f\n\ts_cbranch_scc1 2f\n\t"
                     "buffer_load_dwordx2 v[4:5], %2, %3, 0 offen sc1\n\ts_waitcnt vmcnt(0)\n\ts_branch 1b\n2:"
                     : "+{v[4:5]}"(ix), "+s"(n_) : "v"(ixo), "s"(rs), "s"(tag), "n"(2 * IXI + 1) : "vcc", "scc", "memory");
        const u32 prow = ix.x & 2047u, v = prow >> 1;
        if (l < 16) atomicOr((u32*)(smem + OMISC) + (prow >> 5), 1u << (ix.x & 31u));
        FLAG(OYP + 256 + 32 + 64 * (l & 3), 8u);
        oo += prow * (2 * D);
        asm volatile("v_mov_b32 v162, 0\n\tglobal_load_short_d16_hi v162, %1, %2" : "+{v[160:163]}"(junk) : "v"(oo), "s"(x) : "memory");
        yc = *(const float*)(smem + OSC + 4 * rc); rsel = rbt(sigm(hi2f(ix.x)));
        ya += 4 * (v ^ ((v >> 4) & 6)) + 2 * (prow & 1);
        asm volatile("" : "+v"(yc), "+v"(rsel), "+v"(ya));
        STAMP(11);
      }
      if (i == 2 && p == 0) cl = ld16(rs, clo);
      if (i == 3) {
        asm volatile("s_waitcnt vmcnt(2)" ::: "memory");
        FLAG(OYP + 448 + 36 + 4 * p, 8u);
        if (p == 0) {
#pragma unroll 1
          for (int spin = 0; spin < SPIN; spin++) {
            if (__all(cl.y == tag && cl.w == tag)) break;
            cl = ld16(rs, clo);
          }
          STAMP(12);
        }
        __builtin_amdgcn_s_setprio(LPRIO);
      }
      {
        u32 n_ = SPIN, f_;
        asm volatile("1:\n\tds_read_b32 %0, %2 offset:%3\n\ts_sub_u32 %1, %1, 1\n\ts_waitcnt lgkmcnt(0)\n\tv_cmp_eq_u32 vcc, 0, %0\n\ts_cbranch_scc1 2f\n\ts_and_b32 vcc_lo, vcc_lo, %4\n\t"
                     "s_cbranch_scc0 2f\n\ts_sleep %5\n\ts_branch 1b\n2:" : "=&v"(f_), "+s"(n_) : "v"(yf), "n"(128 * i), "n"(i < 2 ? 255 : 511), "n"(i < 3 ? YN0 : YN3) : "vcc", "scc", "memory");
      }
      if (i == 0) STAMP(5); else if (i == 1) STAMP(6); else if (i == 2) STAMP(7); else STAMP(8);
      u32 bm = 0;
      if (i >= 2) asm volatile("ds_read_b32 %0, %1 offset:%2" : "=v"(bm) : "v"(bp), "n"(64 * i) : "memory");
#pragma unroll
      for (int r = 0; r < 2; r++)
#pragma unroll
        for (int c = 0; c < 8; c++) asm volatile("ds_read_u16_d16_hi %0, %1 offset:%2" : "+v"(y[r][c]) : "v"(c < 4 ? yp : yq), "n"((c & 3) * CHR + 1024 * i + 2 * r) : "memory");
      if (i == 3 && p == 0) {
#define PLD(X) X(132, 133, 134, 135, "%[po]", "0") X(136, 137, 138, 139, "%[po]", "%[k1]") X(140, 141, 142, 143, "%[po]", "%[k2]") X(144, 145, 146, 147, "%[p3]", "0")
#define PL0(a, b_, c, d, o, s_) "buffer_load_dwordx4 v[" #a ":" #d "], " o ", %[rs], " s_ " offen sc1\n\t"
        asm volatile(PLD(PL0) : "=&{v[132:139]}"(pa), "=&{v[140:147]}"(pb) : [po] "v"(po), [p3] "v"(p3), [rs] "s"(rs), [k1] "s"(131072), [k2] "s"(262144) : "memory");
      }
      if (i == 0) {
        st8(rs, t == 512 ? O_ARR + b * 8 : 0x80000000u, u32x2{0u, tag});
        const u32x4 a = *(const u32x4*)(smem + OSC), c = *(const u32x4*)(smem + OSC + 16);
#pragma unroll
        for (int k = 0; k < 4; k++) { ys[k] = (u32)__builtin_amdgcn_readfirstlane((int)a[k]); ys[4 + k] = (u32)__builtin_amdgcn_readfirstlane((int)c[k]); }
      }
      SB();
#pragma unroll
      for (int r = 0; r < 2; r++) {
        asm volatile("s_waitcnt lgkmcnt(%9)" : "+v"(y[r][0]), "+v"(y[r][1]), "+v"(y[r][2]), "+v"(y[r][3]), "+v"(y[r][4]), "+v"(y[r][5]), "+v"(y[r][6]), "+v"(y[r][7]), "+v"(bm) : "n"(8 - 8 * r));
        u32x4 v;
#pragma unroll
        for (int k = 0; k < 4; k++)
          v[k] = pack2(fmas(__uint_as_float(y[r][2 * k]), ys[2 * k], xq[i][2 * k][r]), fmas(__uint_as_float(y[r][2 * k + 1]), ys[2 * k + 1], xq[i][2 * k + 1][r]));
        if (i >= 2 && r == 0) bm <<= bs;
        __builtin_amdgcn_raw_buffer_store_b128(v, ro, i < 2 ? op : op | (bm & 0x40000000u << r), (512 * i + r) * (2 * D), OAUX);
        SB();
        asm volatile("s_nop 1");
        SB();
      }
    }
    asm volatile("" ::"{v[160:163]}"(junk));
    STAMP(13);
    if (p == 0) {
      u32 n_ = SPIN;
#define PL1(a, b_, c, d, o, s_) "v_max3_u32 v150, v" #a ", v" #b_ ", v" #c "\n\tv_max_u32 v150, v150, v" #d "\n\tv_cmpx_eq_u32 vcc, -1, v150\n\t" PL0(a, b_, c, d, o, s_) "s_mov_b64 exec, -1\n\t"
#define PLE(X) X(112, 113, 114, 115, "%[po]", "0") X(116, 117, 118, 119, "%[po]", "%[k1]") X(120, 121, 122, 123, "%[po]", "%[k2]") X(124, 125, 126, 127, "%[p3]", "0")
#define PMX(a, b_) "v_max3_u32 v150, v150, v" a ", v" b_ "\n\t"
#define PMM(d, a, c) "v_mfma_f32_16x16x32_bf16 v[" d "], %[pk], v[" a "], " c "\n\t"
#define PCK(c, p, q) "s_waitcnt vmcnt(" #c ")\n\t" PMM("152:155", p "2:" p "5", "0") "v_max_u32 v150, v" p "2, v" p "3\n\t" PMX(p "4", p "5") PMM("156:159", p "6:" p "9", "0") PMX(p "6", p "7") PMX(p "8", p "9") \
      PMM("152:155", q "0:" q "3", "v[152:155]") PMX(q "0", q "1") PMX(q "2", q "3") PMM("156:159", q "4:" q "7", "v[156:159]") PMX(q "4", q "5") PMX(q "6", q "7") \
      "v_cmp_eq_u32 vcc, -1, v150\n\ts_sub_u32 %[n], %[n], 1\n\t"
#define PCP(a, b_, c, d) "v_mov_b64 v[" #a ":" #b_ "], v[" #c ":" #d "]\n\t"
      asm volatile(PCK(2, "13", "14") "s_cbranch_vccnz 9f\n"
                   "2:\n\t" TS(60, 61) "ds_read_b32 v150, %[yf] offset:448\n\tds_read_u16_d16_hi v151, %[ya]\n\ts_mov_b32 %[n], %[spin]\n\ts_nop 1\n\tv_add_f32 v152, v152, v156\n\tv_add_f32 v153, v153, v157\n\ts_nop 0\n\t"
                   "v_add_f32_dpp v152, v152, v152 row_ror:8 row_mask:0xf bank_mask:0x3\n\tv_add_f32_dpp v152, v153, v153 row_ror:8 row_mask:0xf bank_mask:0xc\n\t"
                   "v_cvt_pk_bf16_f32 v152, 0, v152\n\tv_mul_f32 v152, v152, %[rl]\n\tv_cvt_pk_bf16_f32 v152, 0, v152\n\tv_add_f32 v153, v162, v152\n"
                   "3:\n\ts_waitcnt lgkmcnt(0)\n\tv_cmp_eq_u32 vcc, 0, v150\n\tv_and_b32 v152, 0xffff0000, v151\n\ts_sub_u32 %[n], %[n], 1\n\ts_cbranch_vccz 4f\n\ts_cbranch_scc1 4f\n\t"
                   "ds_read_b32 v150, %[yf] offset:448\n\tds_read_u16_d16_hi v151, %[ya]\n\ts_branch 3b\n"
                   "4:\n\tv_fmac_f32 v153, v152, %[yc]\n\tv_cvt_pk_bf16_f32 v153, v153, v153\n\tbuffer_store_short v153, %[oo], %[ro], 0 offen\n\t" DONE
                   "9:\n\t" PCP(112, 113, 132, 133) PCP(114, 115, 134, 135) PCP(116, 117, 136, 137) PCP(118, 119, 138, 139) PCP(120, 121, 140, 141) PCP(122, 123, 142, 143)
                   PCP(124, 125, 144, 145) PCP(126, 127, 146, 147) PLD(PL1) "s_sleep " STR(PNAP) "\n1:\n\t" PLE(PL1) PCK(4, "13", "14") "s_cbranch_vccz 2b\n\t" PLD(PL1) PCK(4, "11", "12")
                   "s_cbranch_vccz 2b\n\ts_cbranch_scc0 1b\n\ts_branch 2b\n0:"
                   : [n] "+s"(n_), "+{v[132:139]}"(pa), "+{v[140:147]}"(pb)
                   : [po] "v"(po), [p3] "v"(p3), [rs] "s"(rs), [k1] "s"(131072), [k2] "s"(262144), [pk] "v"(pick), [yf] "v"(yf), [ya] "v"(ya), "{v[160:163]}"(junk), [rl] "v"(rsel), [yc] "v"(yc),
                     [oo] "v"(oo), [ro] "s"(ro), [spin] "n"(SPIN)
                   : "vcc", "scc", "memory", "v112", "v113", "v114", "v115", "v116", "v117", "v118", "v119", "v120", "v121", "v122", "v123", "v124", "v125", "v126", "v127", "v150", "v151", "v152", "v153", "v154", "v155", "v156", "v157", "v158", "v159" TSD);
      TSGET(14, 2, 2, 2);
      STAMP(15);
    }
    FLUSH();
    __builtin_amdgcn_endpgm();
  }

  u32 pl, pa, pr, pi, ldtv;
  u32x4 tv, fx, junk;
  ROLE(c, "", , "={v[16:19]}"(tv), "={v[20:23]}"(fx), "={v24}"(ldtv), "={v96}"(pl), "={v97}"(pa), "={v68}"(pr), "={v69}"(pi), "={v[160:163]}"(junk));
  char* W = smem + w * CHR;
  __builtin_amdgcn_s_setprio(2);
  STAMP(3);
  const u32x4 zero4 = {0u, 0u, 0u, 0u};
#pragma unroll
  for (int i = 0; i < 4; i++) *(u32x4*)(W + (i < 3 ? TB + 1024 * i : CHR - 1024) + 16 * l) = zero4;
  SB();
  const u32 wb0 = lds0 + w * CHR, xb0 = lds0 + OX + w * XREG;
  asm volatile("s_mov_b32 m0, %0\n\ts_nop 0\n\tds_write_addtid_b32 %1\n\tds_write_addtid_b32 %1 offset:%2" ::"s"(wb0), "v"(0x3c00u), "n"(PB) : "memory");
  u32 ape = wb0 + (n16 == 12 ? 12 : (n16 ^ 3) - 7 * (n16 >> 3)) * RS + 16 * kg, apo = wb0 + (0x765412b0fdec2301ull >> 4 * n16 & 15) * RS + 16 * kg;
  u32x2 kb = {wb0 + ((15 - n16) & 3) * CPE + 2 * ((15 - n16) & 12) + 16 * kg, wb0 + (((n16 >> 2) & 1) + (n16 & 8) + 2 * (3 - (n16 & 3))) * RS + 16 * kg};
  u32x8 ka = {xb0 + 32 * n16 + 16 * kg, wb0 + OY + 32 * n16 + 8 * (kg ^ ((l >> 2) & 3)),
              lds0 + OXF + 4 * (l & 3), wb0 + TB + ((15 - n16) & 3) * CPO + 2 * ((15 - n16) & 12) + 8 * kg, lds0 + OB + 16 * l,
              l < 32 ? 0x01000100u : 0x03020100u, l < 32 ? 0x03020100u : 0x03020302u, (u32)(l >> 2) & 8u};
  u32 fad = lds0 + OYP + 4 * w, za = wb0 + 16 * kg;
  asm volatile("" : "+{v70}"(ape), "+{v71}"(apo), "+{v[28:35]}"(ka), "+{v[26:27]}"(kb), "+{v25}"(fad), "+{v12}"(za));
  STAMP(4);
  asm volatile("s_waitcnt vmcnt(4)" : "+{v24}"(ldtv), "+{v[160:163]}"(junk));
  float dt = rbt(fexp(hi2f(ldtv)));
  asm volatile("" : "+v"(dt));
  const f32x2 cdt = {rdl(dt * -1.4426950408889634f, 0), rdl(dt * 0.15915494309189535f, 0)};
  const u32 dk = (u32)__builtin_amdgcn_readfirstlane((int)junk.x) << 16, db = max(__float_as_uint(rdl(dt, 0)), 27u << 23);
  u32 em = max(dk >> 23 & 255u, 127u) + 119 - (db >> 23);
  asm("s_max_i32 %0, %0, 8\n\ts_min_u32 %0, %0, 0xfe" : "+s"(em) :: "scc");
  SB();
  STAMP(10);
  STAMPC(11);
  {
    u32 e, o0, o1, of, os;
    asm volatile(GEN_ASM
                 : [e] "=&s"(e), "=&{v93}"(o0), "=&{v46}"(o1), "=&{v9}"(of), "=&{v8}"(os), "+{v25}"(fad), "+{v12}"(za), "+{v68}"(pr), "+{v69}"(pi), "+{v70}"(ape),
                   "+{v71}"(apo), "+{v96}"(pl), "+{v97}"(pa), "+{v[28:35]}"(ka), "+{v[26:27]}"(kb), "+{v[16:19]}"(tv), "+{v[20:23]}"(fx)
                 : [w] "s"(wb0), [c] "s"(cdt), [k] "s"(f32x2{1.4426950408889634f, 128.f}), [em] "s"(em)
                 : "memory", "scc", "s6", "s7", GEN_CLOBBER);
    STAMP(1);
    STAMPC(12);
    const float ys = __uint_as_float((e - 5) << 23) * __uint_as_float(db);
    asm volatile("v_rcp_f32 %1, %2\n\ts_nop 0\n\tv_fma_f32 %1, %3, %1, %1\n\tv_fma_mixhi_f16 %0, %1, 1.0, %0 op_sel:[0,0,1] op_sel_hi:[0,0,1]\n\ts_nop 0"
                 : "+v"(o0), "=&v"(dt) : "v"(ys), "s"(dk));
    const u32 k0 = (u32)__builtin_amdgcn_readlane((int)o0, 3), k256 = (u32)__builtin_amdgcn_readlane((int)o1, 60);
    const u32 k512 = (u32)__builtin_amdgcn_readlane((int)os, 28), k1024 = (u32)__builtin_amdgcn_readlane((int)of, 28) >> 16;
    *(LDS u16*)(size_t)(wb0 + tv.z) = (u16)__builtin_amdgcn_perm((k512 & 0xffffu) | (k1024 << 16), (k0 >> 16) | (k256 << 16), tv.w);
    *(float*)(smem + OSC + 4 * w) = ys;
    fx &= (k1024 | (k1024 << 16)) ^ 0x80008000u;
  }
  asm volatile(CONV_ASM : "+{v[28:35]}"(ka) : "{v[26:27]}"(kb), "{v25}"(fad), "{v[48:51]}"(fx), [w] "s"(w) : "vcc", "scc", "s6", "memory", CONV_CLOBBER TSC);
  TSGET(2, 5, 6, 7);
  STAMP(13);
  STAMPC(15);
  FLUSH();
  __builtin_amdgcn_endpgm();
}

extern "C" void kernel_launch(void* const* d_in, const int* in_sizes, int n_in, void* d_out, int out_size,
                              void* d_ws, size_t ws_size, hipStream_t stream) {
  const u16* const* in = (const u16* const*)d_in;
  fused<<<256, 768, 0, stream>>>(in[0], in[1], in[2], in[3], in[4], in[5], in[6], in[8], in[9], in[10], in[11],
                                 (u16*)d_out, (char*)d_ws);
}
```

```cpp
#include <hip/hip_runtime.h>
#include <stdint.h>

#define STR2(x) #x
#define STR(x) STR2(x)
#define LA 4
#define WL2 44
#define WL3 100
#define PH 1246
#define ABL 0
#define PPRIO 0
#define CPRIO 1
#define GDELAY 0
#define ROT 1
#define PRE 2
#define GNT 0
#define OAUX 0
#define LNT 1
#define PNT ""
#define LNNT ""
#define PRENT ""
#define PFN 15
#define PFO 512
#define DBAR 2
#define THR 1
#define QD 11
#define HNAP0 4
#define HNAP 4
#define KR 3
#define KI 2
#define QE 11
#define QA 5
#define NA 0
#define CH 0
#define G0E 0
#define DDL 0
#define WBM 0
#define WBW 1
#define HQ 3
#define RSL 5
#define DPF 1
#define HELP 0
#define WARM 0
#define HPI 0
#define Q0 6
#define NP 10
#define HW 28
#define NE 0
#define YN0 2
#define YN3 0
#define QS 7
#define QAS 6
#define RAMP 4
#define GNAP "s_sleep 1"
#define RNAP "s_sleep 1"
#define CLP 0
#define CSL 8
#define XSP 0
#define SPEC 0
#define KDL ""
#define HOLD 8
#define DPRIO 3
#define HAUX 16
#define DPO 128
#define TH 560
#define CLD 215
#define IXI 1
#define LPRIO 0
#define DPI 2
#define PDROP 2
#define TE 0
#define CNAP 2
#define XI 1
#define SCA " sc1"
#define SCB " sc1"
#define SCC " sc1"
#define SCT " sc1"
#define SCU " sc1"
#define SCH " sc1"
#define SCR " sc1"
#define TSG 255
#define SSL 5
#define PNAP 4
#define TW 280
#define TL 4352
#define CODE_KB 24
typedef unsigned short u16;
typedef unsigned int u32;
typedef __attribute__((ext_vector_type(8))) __bf16 bf16x8;
typedef __attribute__((ext_vector_type(2))) __bf16 bf16x2;
typedef __attribute__((ext_vector_type(8))) _Float16 f16x8;
typedef __attribute__((ext_vector_type(2))) _Float16 f16x2;
typedef __attribute__((ext_vector_type(4))) short v4i16;
typedef __attribute__((ext_vector_type(4))) float f32x4;
typedef __attribute__((ext_vector_type(2))) float f32x2;
typedef __attribute__((ext_vector_type(4))) u32 u32x4;
typedef __attribute__((ext_vector_type(2))) u32 u32x2;
typedef __attribute__((ext_vector_type(8))) u32 u32x8;
typedef __attribute__((ext_vector_type(2), aligned(4))) u32 u32x2_a4;
typedef __amdgpu_buffer_rsrc_t rsrc_t;
#define LDS __attribute__((address_space(3)))

#define TLARG
#define TLVAL
#define STAMP(I)
#define STAMPC(I)
#define FLUSH()

constexpr int L = 2048, D = 1024, NM = 128, F = 3584;
constexpr u32 O_ARR = 0x1000, O_CLR = 0x2000, O_RG = 0x4000, O_SG = 0x8000, O_IDX = 0x10000, O_HG = 0x20000,
              O_PG = 0xd0000, O_TOP = 0x140000, TOPS = 8 * TL, WS_END = O_TOP + 2 * TOPS;
constexpr int SPIN = 1 << 16;

__device__ __forceinline__ float lo2f(u32 w) { return __uint_as_float(w << 16); }
__device__ __forceinline__ float hi2f(u32 w) { return __uint_as_float(w & 0xffff0000u); }
__device__ __forceinline__ u32 pack2(float a, float b) {
  bf16x2 r = {(__bf16)a, (__bf16)b};
  return __builtin_bit_cast(u32, r);
}
__device__ __forceinline__ u32 packh(float a, float b) {
  f16x2 r = {(_Float16)a, (_Float16)b};
  return __builtin_bit_cast(u32, r);
}
__device__ __forceinline__ float rbt(float f) { return __uint_as_float(pack2(0.f, f)); }
__device__ __forceinline__ float rb(float f) {
  float r;
  asm("v_cvt_pk_bf16_f32 %0, 0, %1" : "=v"(r) : "v"(f));
  return r;
}
__device__ __forceinline__ u32 hadd(u32 a, u32 b) {
  return __builtin_bit_cast(u32, __builtin_bit_cast(f16x2, a) + __builtin_bit_cast(f16x2, b));
}
__device__ __forceinline__ float hlo(u32 a) { return (float)__builtin_bit_cast(f16x2, a).x; }
__device__ __forceinline__ float hhi(u32 a) { return (float)__builtin_bit_cast(f16x2, a).y; }

template <int CTRL, int RM>
__device__ __forceinline__ float dppf(float v) {
  return __builtin_bit_cast(float, __builtin_amdgcn_update_dpp(0, __builtin_bit_cast(int, v), CTRL, RM, 0xf, true));
}
template <int CTRL, int RM>
__device__ __forceinline__ u32 dppu(u32 v) {
  return (u32)__builtin_amdgcn_update_dpp(0, (int)v, CTRL, RM, 0xf, true);
}
__device__ __forceinline__ float rdl(float v, int i) {
  return __builtin_bit_cast(float, __builtin_amdgcn_readlane(__builtin_bit_cast(int, v), i));
}
__device__ __forceinline__ u32 wmaxu(u32 v) {
  v = max(v, dppu<0x111, 0xf>(v)); v = max(v, dppu<0x112, 0xf>(v)); v = max(v, dppu<0x114, 0xf>(v));
  v = max(v, dppu<0x118, 0xf>(v)); v = max(v, dppu<0x142, 0xa>(v)); v = max(v, dppu<0x143, 0xc>(v));
  return (u32)__builtin_amdgcn_readlane((int)v, 63);
}
__device__ __forceinline__ void swap32(float& a, float& b) {
  auto r = __builtin_amdgcn_permlane32_swap(__float_as_uint(a), __float_as_uint(b), false, false);
  a = __uint_as_float(r[0]); b = __uint_as_float(r[1]);
}
__device__ __forceinline__ void swap16(float& a, float& b) {
  auto r = __builtin_amdgcn_permlane16_swap(__float_as_uint(a), __float_as_uint(b), false, false);
  a = __uint_as_float(r[0]); b = __uint_as_float(r[1]);
}
__device__ __forceinline__ u32x2 trread(const char* p) {
  v4i16 r = __builtin_amdgcn_ds_read_tr16_b64_v4i16((LDS v4i16*)p);
  return __builtin_bit_cast(u32x2, r);
}
__device__ __forceinline__ u32x2 trl(LDS char* p) { return __builtin_bit_cast(u32x2, __builtin_amdgcn_ds_read_tr16_b64_v4i16((LDS v4i16*)p)); }
__device__ __forceinline__ f16x8 mkh(u32x2 a, u32x2 b) {
  u32x4 v = {a.x, a.y, b.x, b.y};
  return __builtin_bit_cast(f16x8, v);
}
__device__ __forceinline__ bf16x8 mkb(u32x2 a, u32x2 b) {
  u32x4 v = {a.x, a.y, b.x, b.y};
  return __builtin_bit_cast(bf16x8, v);
}
__device__ __forceinline__ bf16x8 asb(u32x4 v) { return __builtin_bit_cast(bf16x8, v); }
__device__ __forceinline__ f16x8 ash(u32x4 v) { return __builtin_bit_cast(f16x8, v); }
#define MFMAB(a, b, c) __builtin_amdgcn_mfma_f32_16x16x32_bf16(a, b, c, 0, 0, 0)
#define MFMAH(a, b, c) __builtin_amdgcn_mfma_f32_16x16x32_f16(a, b, c, 0, 0, 0)

__device__ __forceinline__ u32x4 ld16(rsrc_t r, u32 off) { return __builtin_amdgcn_raw_buffer_load_b128(r, off, 0, 16); }
__device__ __forceinline__ u32x2 ld8(rsrc_t r, u32 off) { return __builtin_amdgcn_raw_buffer_load_b64(r, off, 0, 16); }
__device__ __forceinline__ void st16(rsrc_t r, u32 off, u32x4 v) { __builtin_amdgcn_raw_buffer_store_b128(v, r, off, 0, 16); }
__device__ __forceinline__ void st8(rsrc_t r, u32 off, u32x2 v) { __builtin_amdgcn_raw_buffer_store_b64(v, r, off, 0, 16); }
#define NAP() asm volatile("s_sleep 1" ::: "memory")
#define SB() __builtin_amdgcn_sched_barrier(0)
#define LBAR() asm volatile("s_waitcnt lgkmcnt(0)\n\ts_barrier" ::: "memory")

__device__ __forceinline__ float fexp(float v) { return __builtin_amdgcn_exp2f(v * 1.4426950408889634f); }
__device__ __forceinline__ float dot2b(u32 a, u32 b, float c) {
  return __builtin_amdgcn_fdot2_f32_bf16(__builtin_bit_cast(bf16x2, a), __builtin_bit_cast(bf16x2, b), c, false);
}
__device__ __forceinline__ float sigm(float v) { return __builtin_amdgcn_rcpf(1.f + __builtin_amdgcn_exp2f(-1.4426950408889634f * v)); }

constexpr int RS = 288, PB = 16 * RS;
constexpr int CPE = 1216, TB = 2 * PB, CPO = 832;
constexpr int T2B = 4, T3B = 224, C2 = 52, C3 = 84;
constexpr int OY = 0, CHR = TB + 4 * CPO;
constexpr int OX = 8 * CHR, XREG = 4640, XO = 266;
constexpr int OMISC = OX + 8 * XREG;
constexpr int OXF = OMISC + 320, OSC = OMISC + 352, OYP = OMISC + 448;
constexpr int OB = OMISC + 1024, SMEM = 148480;
constexpr int FA = 131072, FH = FA + 8192, HP = 576, FC = FH + 8 * HP;
static_assert(FC + 1024 <= SMEM && OB + 4096 <= SMEM, "lds map");

struct Tab { u32 v[6][64][4]; };
constexpr Tab mktab() {
  Tab T = {};
  for (int l = 0; l < 64; l++) {
    const int n16 = l & 15, kg = l >> 4, pc = ((n16 >> 2) & 1) + (n16 & 8) + 2 * (3 - (n16 & 3)), odd = kg >> 1, s = l & 3;
    const int e0 = 534 - 258 - 128 - 8 * pc - 4 * (kg & 1) - odd;
    const int ez = ((l >> 3) & 3) * CPE + ((l & 4) ? 1056 - 64 : 0) + 16 * (l & 7);
    const int b2 = T2B + 44 + C2 * ((kg & 1) + 2 * (pc & 1)), b3 = T3B + 60 + C3 * (pc & 1) - 32 * (kg & 1);
    const int to = TB + 2 * ((odd ? b3 : b2) + 3 - (pc >> 1) - 8 - 16);
    const int q = (l >> 2) & 7, tap = q == 0 ? ((s & 1) ? 0 : 256) : q == 1 ? 257 : q == 7 ? 0 : 509 + q;
    const int sp = q != 6 ? s * (CPE - 2) + 2 * (537 - tap) : TB + s * (CPO - 2) + 2 * (T2B + 15);
    T.v[0][l][0] = (2 * e0 + (2 - odd) * (CPE - 2)) | (2 * e0 + 3 * odd * (CPE - 2)) << 16;
    T.v[0][l][1] = ez | to << 16;
    T.v[0][l][2] = sp;
    T.v[0][l][3] = !tap ? 0x0100u : q == 6 ? 0x0706u : q >= 2 ? 0x0504u : 0x0302u;
    for (int k = 0; k < 8; k++) {
      const int d = 8 * kg + k - n16;
      if (d == 13) T.v[1][l][k >> 1] |= 0xffffu << 16 * (k & 1);
      for (int h = 1; h <= 4; h++)
        if (d >= (h == 1 ? 14 : 10) - h && d <= (h == 1 ? 14 : 10) + h) T.v[1 + h][l][k >> 1] |= 0x3c00u << 16 * (k & 1);
    }
  }
  return T;
}
extern "C" __device__ const __attribute__((aligned(128), used, visibility("hidden"))) Tab lane_tab = mktab();

#define GEN_ASM \
  "s_waitcnt vmcnt(2)\n\tv_lshlrev_b32 v56, 16, v96\n\t" \
  "v_and_b32 v57, 0xffff0000, v96\n\tv_pk_mul_f32 v[56:57], %[k], v[56:57] op_sel_hi:[0,1]\n\t" \
  "v_lshlrev_b32 v49, 16, v97\n\tv_exp_f32 v56, v56\n\t" \
  "v_exp_f32 v57, v57\n\tv_and_b32 v51, 0xffff0000, v97\n\t" \
  "v_cvt_pk_bf16_f32 v56, v56, v57\n\tv_lshlrev_b32 v48, 16, v56\n\t" \
  "v_and_b32 v50, 0xffff0000, v56\n\tv_pk_mul_f32 v[48:49], %[c], v[48:49]\n\t" \
  "v_pk_mul_f32 v[50:51], %[c], v[50:51]\n\tv_pk_mul_f32 v[98:99], %[k], v[48:49] op_sel:[1,0]\n\t" \
  "v_pk_mul_f32 v[100:101], %[k], v[50:51] op_sel:[1,0]\n\tv_fract_f32 v49, v49\n\t" \
  "v_fract_f32 v51, v51\n\tv_exp_f32 v48, v48\n\t" \
  "v_exp_f32 v50, v50\n\tv_cos_f32 v40, v49\n\t" \
  "v_sin_f32 v41, v49\n\tv_cos_f32 v42, v51\n\t" \
  "v_sin_f32 v43, v51\n\tv_pk_mul_f32 v[40:41], v[40:41], v[48:49] op_sel_hi:[1,0]\n\t" \
  "v_pk_mul_f32 v[42:43], v[42:43], v[50:51] op_sel_hi:[1,0]\n\tv_pk_mul_f32 v[48:49], v[40:41], v[40:41] op_sel:[1,1] op_sel_hi:[1,0] neg_lo:[1,0]\n\t" \
  "v_pk_mul_f32 v[50:51], v[42:43], v[42:43] op_sel:[1,1] op_sel_hi:[1,0] neg_lo:[1,0]\n\tv_pk_fma_f32 v[48:49], v[40:41], v[40:41], v[48:49] op_sel:[0,0,0] op_sel_hi:[0,1,1]\n\t" \
  "v_pk_fma_f32 v[50:51], v[42:43], v[42:43], v[50:51] op_sel:[0,0,0] op_sel_hi:[0,1,1]\n\tv_cvt_pk_f16_f32 v72, v40, -v41\n\t" \
  "v_cvt_pk_f16_f32 v73, v42, -v43\n\tv_pk_mul_f32 v[52:53], v[48:49], v[40:41] op_sel:[1,1] op_sel_hi:[1,0] neg_lo:[1,0]\n\t" \
  "v_pk_mul_f32 v[54:55], v[50:51], v[42:43] op_sel:[1,1] op_sel_hi:[1,0] neg_lo:[1,0]\n\tv_pk_fma_f32 v[52:53], v[48:49], v[40:41], v[52:53] op_sel:[0,0,0] op_sel_hi:[0,1,1]\n\t" \
  "v_pk_fma_f32 v[54:55], v[50:51], v[42:43], v[54:55] op_sel:[0,0,0] op_sel_hi:[0,1,1]\n\tv_pk_mul_f32 v[56:57], v[48:49], v[48:49] op_sel:[1,1] op_sel_hi:[1,0] neg_lo:[1,0]\n\t" \
  "v_pk_mul_f32 v[58:59], v[50:51], v[50:51] op_sel:[1,1] op_sel_hi:[1,0] neg_lo:[1,0]\n\tv_pk_fma_f32 v[56:57], v[48:49], v[48:49], v[56:57] op_sel:[0,0,0] op_sel_hi:[0,1,1]\n\t" \
  "v_pk_fma_f32 v[58:59], v[50:51], v[50:51], v[58:59] op_sel:[0,0,0] op_sel_hi:[0,1,1]\n\tv_cvt_pk_f16_f32 v74, v48, -v49\n\t" \
  "v_cvt_pk_f16_f32 v75, v50, -v51\n\tv_cvt_pk_f16_f32 v76, v52, -v53\n\t" \
  "v_cvt_pk_f16_f32 v77, v54, -v55\n\tv_pk_mul_f32 v[52:53], v[56:57], v[48:49] op_sel:[1,1] op_sel_hi:[1,0] neg_lo:[1,0]\n\t" \
  "v_pk_mul_f32 v[54:55], v[58:59], v[50:51] op_sel:[1,1] op_sel_hi:[1,0] neg_lo:[1,0]\n\tv_pk_fma_f32 v[52:53], v[56:57], v[48:49], v[52:53] op_sel:[0,0,0] op_sel_hi:[0,1,1]\n\t" \
  "v_pk_fma_f32 v[54:55], v[58:59], v[50:51], v[54:55] op_sel:[0,0,0] op_sel_hi:[0,1,1]\n\tv_pk_mul_f32 v[60:61], v[56:57], v[56:57] op_sel:[1,1] op_sel_hi:[1,0] neg_lo:[1,0]\n\t" \
  "v_pk_mul_f32 v[62:63], v[58:59], v[58:59] op_sel:[1,1] op_sel_hi:[1,0] neg_lo:[1,0]\n\tv_pk_fma_f32 v[60:61], v[56:57], v[56:57], v[60:61] op_sel:[0,0,0] op_sel_hi:[0,1,1]\n\t" \
  "v_pk_fma_f32 v[62:63], v[58:59], v[58:59], v[62:63] op_sel:[0,0,0] op_sel_hi:[0,1,1]\n\tv_cvt_pk_f16_f32 v78, v56, -v57\n\t" \
  "v_cvt_pk_f16_f32 v79, v58, -v59\n\tv_cvt_pk_f16_f32 v82, v52, -v53\n\t" \
  "v_cvt_pk_f16_f32 v83, v54, -v55\n\tv_pk_mul_f16 v80, v78, v72 op_sel:[1,1] op_sel_hi:[1,0] neg_lo:[1,0]\n\t" \
  "v_pk_mul_f16 v81, v79, v73 op_sel:[1,1] op_sel_hi:[1,0] neg_lo:[1,0]\n\tv_pk_fma_f16 v80, v78, v72, v80 op_sel:[0,0,0] op_sel_hi:[0,1,1]\n\t" \
  "v_pk_fma_f16 v81, v79, v73, v81 op_sel:[0,0,0] op_sel_hi:[0,1,1]\n\tv_pk_mul_f16 v84, v78, v76 op_sel:[1,1] op_sel_hi:[1,0] neg_lo:[1,0]\n\t" \
  "v_pk_mul_f16 v85, v79, v77 op_sel:[1,1] op_sel_hi:[1,0] neg_lo:[1,0]\n\tv_pk_fma_f16 v84, v78, v76, v84 op_sel:[0,0,0] op_sel_hi:[0,1,1]\n\t" \
  "v_pk_fma_f16 v85, v79, v77, v85 op_sel:[0,0,0] op_sel_hi:[0,1,1]\n\tv_cvt_pk_f16_f32 v86, v60, -v61\n\t" \
  "v_cvt_pk_f16_f32 v87, v62, -v63\n\tds_write_addtid_b32 v72 offset:288\n\t" \
  "ds_write_addtid_b32 v73 offset:4896\n\tds_write_addtid_b32 v74 offset:576\n\t" \
  "ds_write_addtid_b32 v75 offset:5184\n\tds_write_addtid_b32 v76 offset:864\n\t" \
  "ds_write_addtid_b32 v77 offset:5472\n\tds_write_addtid_b32 v78 offset:1152\n\t" \
  "ds_write_addtid_b32 v79 offset:5760\n\tds_write_addtid_b32 v80 offset:1440\n\t" \
  "ds_write_addtid_b32 v81 offset:6048\n\tds_write_addtid_b32 v82 offset:1728\n\t" \
  "ds_write_addtid_b32 v83 offset:6336\n\tds_write_addtid_b32 v84 offset:2016\n\t" \
  "ds_write_addtid_b32 v85 offset:6624\n\tds_write_addtid_b32 v86 offset:3456\n\t" \
  "ds_write_addtid_b32 v87 offset:8064\n\tds_read_b128 v[104:107], v70 offset:0\n\t" \
  "ds_read_b128 v[108:111], v70 offset:64\n\tds_read_b128 v[112:115], v70 offset:128\n\t" \
  "ds_read_b128 v[116:119], v70 offset:192\n\tds_read_b128 v[120:123], v70 offset:4608\n\t" \
  "ds_read_b128 v[124:127], v70 offset:4672\n\tds_read_b128 v[128:131], v70 offset:4736\n\t" \
  "ds_read_b128 v[132:135], v70 offset:4800\n\tv_fract_f32 v99, v99\n\t" \
  "v_fract_f32 v101, v101\n\tv_exp_f32 v98, v98\n\t" \
  "v_exp_f32 v100, v100\n\tv_cos_f32 v40, v99\n\t" \
  "v_sin_f32 v41, v99\n\tv_cos_f32 v42, v101\n\t" \
  "v_sin_f32 v43, v101\n\tv_pk_mul_f32 v[40:41], v[40:41], v[98:99] op_sel_hi:[1,0]\n\t" \
  "v_pk_mul_f32 v[42:43], v[42:43], v[100:101] op_sel_hi:[1,0]\n\tv_pk_mul_f32 v[44:45], v[40:41], v[40:41] op_sel:[1,1] op_sel_hi:[1,0] neg_lo:[1,0]\n\t" \
  "v_pk_mul_f32 v[46:47], v[42:43], v[42:43] op_sel:[1,1] op_sel_hi:[1,0] neg_lo:[1,0]\n\tv_pk_fma_f32 v[44:45], v[40:41], v[40:41], v[44:45] op_sel:[0,0,0] op_sel_hi:[0,1,1]\n\t" \
  "v_pk_fma_f32 v[46:47], v[42:43], v[42:43], v[46:47] op_sel:[0,0,0] op_sel_hi:[0,1,1]\n\tv_cvt_pk_f16_f32 v36, v40, v41\n\t" \
  "v_cvt_pk_f16_f32 v37, v42, v43\n\ts_waitcnt vmcnt(0)\n\t" \
  "v_pk_mul_f32 v[48:49], v[44:45], v[44:45] op_sel:[1,1] op_sel_hi:[1,0] neg_lo:[1,0]\n\tv_pk_mul_f32 v[50:51], v[46:47], v[46:47] op_sel:[1,1] op_sel_hi:[1,0] neg_lo:[1,0]\n\t" \
  "v_pk_fma_f32 v[48:49], v[44:45], v[44:45], v[48:49] op_sel:[0,0,0] op_sel_hi:[0,1,1]\n\tv_pk_fma_f32 v[50:51], v[46:47], v[46:47], v[50:51] op_sel:[0,0,0] op_sel_hi:[0,1,1]\n\t" \
  "v_lshlrev_b32 v4, 16, v68\n\tv_cvt_pk_f16_f32 v88, v44, -v45\n\t" \
  "v_cvt_pk_f16_f32 v89, v46, -v47\n\tv_pk_mul_f32 v[52:53], v[48:49], v[44:45] op_sel:[1,1] op_sel_hi:[1,0] neg_lo:[1,0]\n\t" \
  "v_pk_mul_f32 v[54:55], v[50:51], v[46:47] op_sel:[1,1] op_sel_hi:[1,0] neg_lo:[1,0]\n\tv_lshlrev_b32 v5, 16, v69\n\t" \
  "v_pk_fma_f32 v[52:53], v[48:49], v[44:45], v[52:53] op_sel:[0,0,0] op_sel_hi:[0,1,1]\n\tv_pk_fma_f32 v[54:55], v[50:51], v[46:47], v[54:55] op_sel:[0,0,0] op_sel_hi:[0,1,1]\n\t" \
  "v_pk_mul_f32 v[56:57], v[48:49], v[48:49] op_sel:[1,1] op_sel_hi:[1,0] neg_lo:[1,0]\n\tv_pk_mul_f32 v[58:59], v[50:51], v[50:51] op_sel:[1,1] op_sel_hi:[1,0] neg_lo:[1,0]\n\t" \
  "v_and_b32 v6, 0xffff0000, v68\n\tv_pk_fma_f32 v[56:57], v[48:49], v[48:49], v[56:57] op_sel:[0,0,0] op_sel_hi:[0,1,1]\n\t" \
  "v_pk_fma_f32 v[58:59], v[50:51], v[50:51], v[58:59] op_sel:[0,0,0] op_sel_hi:[0,1,1]\n\tv_cvt_pk_f16_f32 v90, v48, -v49\n\t" \
  "v_cvt_pk_f16_f32 v91, v50, -v51\n\tv_and_b32 v7, 0xffff0000, v69\n\t" \
  "v_pk_mul_f32 v[64:65], v[56:57], v[60:61] op_sel:[1,1] op_sel_hi:[1,0] neg_lo:[1,0]\n\tv_pk_mul_f32 v[66:67], v[58:59], v[62:63] op_sel:[1,1] op_sel_hi:[1,0] neg_lo:[1,0]\n\t" \
  "v_pk_fma_f32 v[64:65], v[56:57], v[60:61], v[64:65] op_sel:[0,0,0] op_sel_hi:[0,1,1]\n\tv_pk_fma_f32 v[66:67], v[58:59], v[62:63], v[66:67] op_sel:[0,0,0] op_sel_hi:[0,1,1]\n\t" \
  "v_max3_f32 v102, |v4|, |v5|, |v6|\n\tv_cvt_pk_f16_f32 v92, v52, -v53\n\t" \
  "v_cvt_pk_f16_f32 v93, v54, -v55\n\tv_pk_mul_f16 v136, v88, v74 op_sel:[1,1] op_sel_hi:[1,0] neg_lo:[1,0]\n\t" \
  "v_pk_mul_f16 v137, v89, v75 op_sel:[1,1] op_sel_hi:[1,0] neg_lo:[1,0]\n\tv_max_f32 v102, v102, |v7|\n\t" \
  "v_pk_fma_f16 v136, v88, v74, v136 op_sel:[0,0,0] op_sel_hi:[0,1,1]\n\tv_pk_fma_f16 v137, v89, v75, v137 op_sel:[0,0,0] op_sel_hi:[0,1,1]\n\t" \
  "v_pk_mul_f16 v138, v88, v78 op_sel:[1,1] op_sel_hi:[1,0] neg_lo:[1,0]\n\tv_pk_mul_f16 v139, v89, v79 op_sel:[1,1] op_sel_hi:[1,0] neg_lo:[1,0]\n\t" \
  "v_max_u32_dpp v102, v102, v102 row_shr:1 row_mask:0xf bank_mask:0xf\n\tv_pk_fma_f16 v138, v88, v78, v138 op_sel:[0,0,0] op_sel_hi:[0,1,1]\n\t" \
  "v_pk_fma_f16 v139, v89, v79, v139 op_sel:[0,0,0] op_sel_hi:[0,1,1]\n\tv_cvt_pk_f16_f32 v94, v64, -v65\n\t" \
  "v_cvt_pk_f16_f32 v95, v66, -v67\n\tv_max_u32_dpp v102, v102, v102 row_shr:2 row_mask:0xf bank_mask:0xf\n\t" \
  "v_pk_mul_f16 v140, v88, v82 op_sel:[1,1] op_sel_hi:[1,0] neg_lo:[1,0]\n\tv_pk_mul_f16 v141, v89, v83 op_sel:[1,1] op_sel_hi:[1,0] neg_lo:[1,0]\n\t" \
  "v_pk_fma_f16 v140, v88, v82, v140 op_sel:[0,0,0] op_sel_hi:[0,1,1]\n\tv_pk_fma_f16 v141, v89, v83, v141 op_sel:[0,0,0] op_sel_hi:[0,1,1]\n\t" \
  "ds_write_addtid_b32 v88 offset:3168\n\tds_write_addtid_b32 v89 offset:7776\n\t" \
  "ds_write_addtid_b32 v136 offset:0\n\tds_write_addtid_b32 v137 offset:4608\n\t" \
  "ds_write_addtid_b32 v138 offset:288\n\tds_write_addtid_b32 v139 offset:4896\n\t" \
  "ds_write_addtid_b32 v94 offset:1152\n\tds_write_addtid_b32 v95 offset:5760\n\t" \
  "ds_write_addtid_b32 v140 offset:576\n\tds_write_addtid_b32 v141 offset:5184\n\t" \
  "v_max_u32_dpp v102, v102, v102 row_shr:4 row_mask:0xf bank_mask:0xf\n\tv_pk_mul_f16 v142, v88, v86 op_sel:[1,1] op_sel_hi:[1,0] neg_lo:[1,0]\n\t" \
  "v_pk_mul_f16 v143, v89, v87 op_sel:[1,1] op_sel_hi:[1,0] neg_lo:[1,0]\n\tv_pk_fma_f16 v142, v88, v86, v142 op_sel:[0,0,0] op_sel_hi:[0,1,1]\n\t" \
  "v_pk_fma_f16 v143, v89, v87, v143 op_sel:[0,0,0] op_sel_hi:[0,1,1]\n\tv_max_u32_dpp v102, v102, v102 row_shr:8 row_mask:0xf bank_mask:0xf\n\t" \
  "v_pk_mul_f16 v144, v90, v78 op_sel:[1,1] op_sel_hi:[1,0] neg_lo:[1,0]\n\tv_pk_mul_f16 v145, v91, v79 op_sel:[1,1] op_sel_hi:[1,0] neg_lo:[1,0]\n\t" \
  "v_pk_fma_f16 v144, v90, v78, v144 op_sel:[0,0,0] op_sel_hi:[0,1,1]\n\tv_pk_fma_f16 v145, v91, v79, v145 op_sel:[0,0,0] op_sel_hi:[0,1,1]\n\t" \
  "v_max_u32_dpp v102, v102, v102 row_bcast:15 row_mask:0xa bank_mask:0xf\n\tv_pk_mul_f16 v146, v90, v86 op_sel:[1,1] op_sel_hi:[1,0] neg_lo:[1,0]\n\t" \
  "v_pk_mul_f16 v147, v91, v87 op_sel:[1,1] op_sel_hi:[1,0] neg_lo:[1,0]\n\tv_pk_fma_f16 v146, v90, v86, v146 op_sel:[0,0,0] op_sel_hi:[0,1,1]\n\t" \
  "v_pk_fma_f16 v147, v91, v87, v147 op_sel:[0,0,0] op_sel_hi:[0,1,1]\n\tv_max_u32_dpp v102, v102, v102 row_bcast:31 row_mask:0xc bank_mask:0xf\n\t" \
  "v_pk_mul_f16 v148, v92, v78 op_sel:[1,1] op_sel_hi:[1,0] neg_lo:[1,0]\n\tv_pk_mul_f16 v149, v93, v79 op_sel:[1,1] op_sel_hi:[1,0] neg_lo:[1,0]\n\t" \
  "v_pk_fma_f16 v148, v92, v78, v148 op_sel:[0,0,0] op_sel_hi:[0,1,1]\n\tv_pk_fma_f16 v149, v93, v79, v149 op_sel:[0,0,0] op_sel_hi:[0,1,1]\n\t" \
  "v_readlane_b32 %[e], v102, 63\n\tv_pk_mul_f16 v150, v92, v86 op_sel:[1,1] op_sel_hi:[1,0] neg_lo:[1,0]\n\t" \
  "v_pk_mul_f16 v151, v93, v87 op_sel:[1,1] op_sel_hi:[1,0] neg_lo:[1,0]\n\tv_pk_fma_f16 v150, v92, v86, v150 op_sel:[0,0,0] op_sel_hi:[0,1,1]\n\t" \
  "v_pk_fma_f16 v151, v93, v87, v151 op_sel:[0,0,0] op_sel_hi:[0,1,1]\n\ts_lshr_b32 %[e], %[e], 23\n\t" \
  "s_max_u32 %[e], %[e], %[em]\n\ts_sub_u32 s6, 260, %[e]\n\t" \
  "s_lshl_b32 s6, s6, 23\n\tds_write_addtid_b32 v142 offset:864\n\t" \
  "ds_write_addtid_b32 v143 offset:5472\n\tds_write_addtid_b32 v144 offset:3456\n\t" \
  "ds_write_addtid_b32 v145 offset:8064\n\tds_write_addtid_b32 v146 offset:3744\n\t" \
  "ds_write_addtid_b32 v147 offset:8352\n\tds_write_addtid_b32 v148 offset:4032\n\t" \
  "ds_write_addtid_b32 v149 offset:8640\n\tds_write_addtid_b32 v150 offset:4320\n\t" \
  "ds_write_addtid_b32 v151 offset:8928\n\tv_pk_mul_f16 v96, v94, v88 op_sel:[1,1] op_sel_hi:[1,0] neg_lo:[1,0]\n\t" \
  "v_pk_mul_f16 v97, v95, v89 op_sel:[1,1] op_sel_hi:[1,0] neg_lo:[1,0]\n\tv_pk_fma_f16 v96, v94, v88, v96 op_sel:[0,0,0] op_sel_hi:[0,1,1]\n\t" \
  "v_pk_fma_f16 v97, v95, v89, v97 op_sel:[0,0,0] op_sel_hi:[0,1,1]\n\tv_pk_mul_f16 v98, v94, v90 op_sel:[1,1] op_sel_hi:[1,0] neg_lo:[1,0]\n\t" \
  "v_pk_mul_f16 v99, v95, v91 op_sel:[1,1] op_sel_hi:[1,0] neg_lo:[1,0]\n\tv_pk_fma_f16 v98, v94, v90, v98 op_sel:[0,0,0] op_sel_hi:[0,1,1]\n\t" \
  "v_pk_fma_f16 v99, v95, v91, v99 op_sel:[0,0,0] op_sel_hi:[0,1,1]\n\tv_pk_mul_f16 v100, v94, v92 op_sel:[1,1] op_sel_hi:[1,0] neg_lo:[1,0]\n\t" \
  "v_pk_mul_f16 v101, v95, v93 op_sel:[1,1] op_sel_hi:[1,0] neg_lo:[1,0]\n\tv_pk_fma_f16 v100, v94, v92, v100 op_sel:[0,0,0] op_sel_hi:[0,1,1]\n\t" \
  "v_pk_fma_f16 v101, v95, v93, v101 op_sel:[0,0,0] op_sel_hi:[0,1,1]\n\tds_write_addtid_b32 v96 offset:1440\n\t" \
  "ds_write_addtid_b32 v97 offset:6048\n\tds_write_addtid_b32 v98 offset:1728\n\t" \
  "ds_write_addtid_b32 v99 offset:6336\n\tds_write_addtid_b32 v100 offset:2016\n\t" \
  "ds_write_addtid_b32 v101 offset:6624\n\tds_read_b128 v[136:139], v71 offset:0\n\t" \
  "ds_read_b128 v[140:143], v71 offset:64\n\tds_read_b128 v[144:147], v71 offset:128\n\t" \
  "s_cmp_lt_u32 %[w], 50176\n\ts_cbranch_scc0 1f\n\t" \
  "s_setprio 1\n\t1:\n\t" \
  "ds_read_b128 v[148:151], v71 offset:192\n\tds_read_b128 v[152:155], v71 offset:4608\n\t" \
  "ds_read_b128 v[156:159], v71 offset:4672\n\tds_read_b128 v[160:163], v71 offset:4736\n\t" \
  "ds_read_b128 v[164:167], v71 offset:4800\n\tv_pk_mul_f32 v[48:49], v[60:61], v[60:61] op_sel:[1,1] op_sel_hi:[1,0] neg_lo:[1,0]\n\t" \
  "v_pk_mul_f32 v[50:51], v[62:63], v[62:63] op_sel:[1,1] op_sel_hi:[1,0] neg_lo:[1,0]\n\tv_pk_fma_f32 v[48:49], v[60:61], v[60:61], v[48:49] op_sel:[0,0,0] op_sel_hi:[0,1,1]\n\t" \
  "v_pk_fma_f32 v[50:51], v[62:63], v[62:63], v[50:51] op_sel:[0,0,0] op_sel_hi:[0,1,1]\n\tv_pk_mul_f32 v[52:53], v[48:49], v[60:61] op_sel:[1,1] op_sel_hi:[1,0] neg_lo:[1,0]\n\t" \
  "v_pk_mul_f32 v[54:55], v[50:51], v[62:63] op_sel:[1,1] op_sel_hi:[1,0] neg_lo:[1,0]\n\tv_pk_fma_f32 v[52:53], v[48:49], v[60:61], v[52:53] op_sel:[0,0,0] op_sel_hi:[0,1,1]\n\t" \
  "v_pk_fma_f32 v[54:55], v[50:51], v[62:63], v[54:55] op_sel:[0,0,0] op_sel_hi:[0,1,1]\n\tv_pk_mul_f32 v[40:41], v[48:49], v[48:49] op_sel:[1,1] op_sel_hi:[1,0] neg_lo:[1,0]\n\t" \
  "v_pk_mul_f32 v[42:43], v[50:51], v[50:51] op_sel:[1,1] op_sel_hi:[1,0] neg_lo:[1,0]\n\tv_pk_fma_f32 v[40:41], v[48:49], v[48:49], v[40:41] op_sel:[0,0,0] op_sel_hi:[0,1,1]\n\t" \
  "v_pk_fma_f32 v[42:43], v[50:51], v[50:51], v[42:43] op_sel:[0,0,0] op_sel_hi:[0,1,1]\n\tv_cvt_pk_f16_f32 v46, v48, v49\n\t" \
  "v_cvt_pk_f16_f32 v47, v50, v51\n\tv_cvt_pk_f16_f32 v56, v52, v53\n\t" \
  "v_cvt_pk_f16_f32 v57, v54, v55\n\tv_pk_mul_f32 v[64:65], s[6:7], v[4:5] op_sel_hi:[0,1]\n\t" \
  "v_pk_mul_f32 v[66:67], s[6:7], v[6:7] op_sel_hi:[0,1]\n\tv_cvt_pk_f16_f32 v48, v64, v65\n\t" \
  "v_cvt_pk_f16_f32 v49, v66, v67\n\tv_pk_mul_f32 v[68:69], v[64:65], v[40:41] op_sel:[1,1] op_sel_hi:[1,0] neg_lo:[1,0]\n\t" \
  "v_pk_mul_f32 v[70:71], v[66:67], v[42:43] op_sel:[1,1] op_sel_hi:[1,0] neg_lo:[1,0]\n\tv_pk_fma_f32 v[68:69], v[64:65], v[40:41], v[68:69] op_sel:[0,0,0] op_sel_hi:[0,1,1]\n\t" \
  "v_pk_fma_f32 v[70:71], v[66:67], v[42:43], v[70:71] op_sel:[0,0,0] op_sel_hi:[0,1,1]\n\tv_pk_mul_f16 v50, v48, v86 op_sel:[1,1] op_sel_hi:[1,0]\n\t" \
  "v_pk_mul_f16 v51, v49, v87 op_sel:[1,1] op_sel_hi:[1,0]\n\tv_pk_fma_f16 v50, v48, v86, v50 op_sel:[0,0,0] op_sel_hi:[0,1,1] neg_hi:[0,1,0]\n\t" \
  "v_pk_fma_f16 v51, v49, v87, v51 op_sel:[0,0,0] op_sel_hi:[0,1,1] neg_hi:[0,1,0]\n\tv_pk_mul_f16 v52, v48, v46 op_sel:[1,1] op_sel_hi:[1,0] neg_lo:[1,0]\n\t" \
  "v_pk_mul_f16 v53, v49, v47 op_sel:[1,1] op_sel_hi:[1,0] neg_lo:[1,0]\n\tv_pk_fma_f16 v52, v48, v46, v52 op_sel:[0,0,0] op_sel_hi:[0,1,1]\n\t" \
  "v_pk_fma_f16 v53, v49, v47, v53 op_sel:[0,0,0] op_sel_hi:[0,1,1]\n\tv_pk_mul_f16 v54, v48, v56 op_sel:[1,1] op_sel_hi:[1,0] neg_lo:[1,0]\n\t" \
  "v_pk_mul_f16 v55, v49, v57 op_sel:[1,1] op_sel_hi:[1,0] neg_lo:[1,0]\n\tv_pk_fma_f16 v54, v48, v56, v54 op_sel:[0,0,0] op_sel_hi:[0,1,1]\n\t" \
  "v_pk_fma_f16 v55, v49, v57, v55 op_sel:[0,0,0] op_sel_hi:[0,1,1]\n\tds_write_addtid_b32 v48 offset:0\n\t" \
  "ds_write_addtid_b32 v49 offset:4608\n\tds_write_addtid_b32 v50 offset:288\n\t" \
  "ds_write_addtid_b32 v51 offset:4896\n\tds_write_addtid_b32 v52 offset:576\n\t" \
  "ds_write_addtid_b32 v53 offset:5184\n\tds_write_addtid_b32 v54 offset:864\n\t" \
  "ds_write_addtid_b32 v55 offset:5472\n\tv_cvt_pk_f16_f32 v48, v68, v69\n\t" \
  "v_cvt_pk_f16_f32 v49, v70, v71\n\tv_pk_mul_f32 v[64:65], v[68:69], v[40:41] op_sel:[1,1] op_sel_hi:[1,0] neg_lo:[1,0]\n\t" \
  "v_pk_mul_f32 v[66:67], v[70:71], v[42:43] op_sel:[1,1] op_sel_hi:[1,0] neg_lo:[1,0]\n\tv_pk_fma_f32 v[64:65], v[68:69], v[40:41], v[64:65] op_sel:[0,0,0] op_sel_hi:[0,1,1]\n\t" \
  "v_pk_fma_f32 v[66:67], v[70:71], v[42:43], v[66:67] op_sel:[0,0,0] op_sel_hi:[0,1,1]\n\tv_pk_mul_f16 v50, v48, v86 op_sel:[1,1] op_sel_hi:[1,0]\n\t" \
  "v_pk_mul_f16 v51, v49, v87 op_sel:[1,1] op_sel_hi:[1,0]\n\tv_pk_fma_f16 v50, v48, v86, v50 op_sel:[0,0,0] op_sel_hi:[0,1,1] neg_hi:[0,1,0]\n\t" \
  "v_pk_fma_f16 v51, v49, v87, v51 op_sel:[0,0,0] op_sel_hi:[0,1,1] neg_hi:[0,1,0]\n\tv_pk_mul_f16 v52, v48, v46 op_sel:[1,1] op_sel_hi:[1,0] neg_lo:[1,0]\n\t" \
  "v_pk_mul_f16 v53, v49, v47 op_sel:[1,1] op_sel_hi:[1,0] neg_lo:[1,0]\n\tv_pk_fma_f16 v52, v48, v46, v52 op_sel:[0,0,0] op_sel_hi:[0,1,1]\n\t" \
  "v_pk_fma_f16 v53, v49, v47, v53 op_sel:[0,0,0] op_sel_hi:[0,1,1]\n\tv_pk_mul_f16 v54, v48, v56 op_sel:[1,1] op_sel_hi:[1,0] neg_lo:[1,0]\n\t" \
  "v_pk_mul_f16 v55, v49, v57 op_sel:[1,1] op_sel_hi:[1,0] neg_lo:[1,0]\n\tv_pk_fma_f16 v54, v48, v56, v54 op_sel:[0,0,0] op_sel_hi:[0,1,1]\n\t" \
  "v_pk_fma_f16 v55, v49, v57, v55 op_sel:[0,0,0] op_sel_hi:[0,1,1]\n\tds_write_addtid_b32 v48 offset:1152\n\t" \
  "ds_write_addtid_b32 v49 offset:5760\n\tds_write_addtid_b32 v50 offset:1440\n\t" \
  "ds_write_addtid_b32 v51 offset:6048\n\tds_write_addtid_b32 v52 offset:1728\n\t" \
  "ds_write_addtid_b32 v53 offset:6336\n\tds_write_addtid_b32 v54 offset:2016\n\t" \
  "ds_write_addtid_b32 v55 offset:6624\n\tv_cvt_pk_f16_f32 v48, v64, v65\n\t" \
  "v_cvt_pk_f16_f32 v49, v66, v67\n\tv_pk_mul_f32 v[68:69], v[64:65], v[40:41] op_sel:[1,1] op_sel_hi:[1,0] neg_lo:[1,0]\n\t" \
  "v_pk_mul_f32 v[70:71], v[66:67], v[42:43] op_sel:[1,1] op_sel_hi:[1,0] neg_lo:[1,0]\n\tv_pk_fma_f32 v[68:69], v[64:65], v[40:41], v[68:69] op_sel:[0,0,0] op_sel_hi:[0,1,1]\n\t" \
  "v_pk_fma_f32 v[70:71], v[66:67], v[42:43], v[70:71] op_sel:[0,0,0] op_sel_hi:[0,1,1]\n\tv_pk_mul_f16 v50, v48, v86 op_sel:[1,1] op_sel_hi:[1,0]\n\t" \
  "v_pk_mul_f16 v51, v49, v87 op_sel:[1,1] op_sel_hi:[1,0]\n\tv_pk_fma_f16 v50, v48, v86, v50 op_sel:[0,0,0] op_sel_hi:[0,1,1] neg_hi:[0,1,0]\n\t" \
  "v_pk_fma_f16 v51, v49, v87, v51 op_sel:[0,0,0] op_sel_hi:[0,1,1] neg_hi:[0,1,0]\n\tv_pk_mul_f16 v52, v48, v46 op_sel:[1,1] op_sel_hi:[1,0] neg_lo:[1,0]\n\t" \
  "v_pk_mul_f16 v53, v49, v47 op_sel:[1,1] op_sel_hi:[1,0] neg_lo:[1,0]\n\tv_pk_fma_f16 v52, v48, v46, v52 op_sel:[0,0,0] op_sel_hi:[0,1,1]\n\t" \
  "v_pk_fma_f16 v53, v49, v47, v53 op_sel:[0,0,0] op_sel_hi:[0,1,1]\n\tv_pk_mul_f16 v54, v48, v56 op_sel:[1,1] op_sel_hi:[1,0] neg_lo:[1,0]\n\t" \
  "v_pk_mul_f16 v55, v49, v57 op_sel:[1,1] op_sel_hi:[1,0] neg_lo:[1,0]\n\tv_pk_fma_f16 v54, v48, v56, v54 op_sel:[0,0,0] op_sel_hi:[0,1,1]\n\t" \
  "v_pk_fma_f16 v55, v49, v57, v55 op_sel:[0,0,0] op_sel_hi:[0,1,1]\n\tds_write_addtid_b32 v48 offset:2304\n\t" \
  "ds_write_addtid_b32 v49 offset:6912\n\tds_write_addtid_b32 v50 offset:2592\n\t" \
  "ds_write_addtid_b32 v51 offset:7200\n\tds_write_addtid_b32 v52 offset:2880\n\t" \
  "ds_write_addtid_b32 v53 offset:7488\n\tds_write_addtid_b32 v54 offset:3168\n\t" \
  "ds_write_addtid_b32 v55 offset:7776\n\tv_cvt_pk_f16_f32 v48, v68, v69\n\t" \
  "v_cvt_pk_f16_f32 v49, v70, v71\n\tv_pk_mul_f16 v50, v48, v86 op_sel:[1,1] op_sel_hi:[1,0]\n\t" \
  "v_pk_mul_f16 v51, v49, v87 op_sel:[1,1] op_sel_hi:[1,0]\n\tv_pk_fma_f16 v50, v48, v86, v50 op_sel:[0,0,0] op_sel_hi:[0,1,1] neg_hi:[0,1,0]\n\t" \
  "v_pk_fma_f16 v51, v49, v87, v51 op_sel:[0,0,0] op_sel_hi:[0,1,1] neg_hi:[0,1,0]\n\tv_pk_mul_f16 v52, v48, v46 op_sel:[1,1] op_sel_hi:[1,0] neg_lo:[1,0]\n\t" \
  "v_pk_mul_f16 v53, v49, v47 op_sel:[1,1] op_sel_hi:[1,0] neg_lo:[1,0]\n\tv_pk_fma_f16 v52, v48, v46, v52 op_sel:[0,0,0] op_sel_hi:[0,1,1]\n\t" \
  "v_pk_fma_f16 v53, v49, v47, v53 op_sel:[0,0,0] op_sel_hi:[0,1,1]\n\tv_pk_mul_f16 v54, v48, v56 op_sel:[1,1] op_sel_hi:[1,0] neg_lo:[1,0]\n\t" \
  "v_pk_mul_f16 v55, v49, v57 op_sel:[1,1] op_sel_hi:[1,0] neg_lo:[1,0]\n\tv_pk_fma_f16 v54, v48, v56, v54 op_sel:[0,0,0] op_sel_hi:[0,1,1]\n\t" \
  "v_pk_fma_f16 v55, v49, v57, v55 op_sel:[0,0,0] op_sel_hi:[0,1,1]\n\tds_write_addtid_b32 v48 offset:3456\n\t" \
  "ds_write_addtid_b32 v49 offset:8064\n\tds_write_addtid_b32 v50 offset:3744\n\t" \
  "ds_write_addtid_b32 v51 offset:8352\n\tds_write_addtid_b32 v52 offset:4032\n\t" \
  "ds_write_addtid_b32 v53 offset:8640\n\tds_write_addtid_b32 v54 offset:4320\n\t" \
  "ds_write_addtid_b32 v55 offset:8928\n\tds_read_b128 v[72:75], v27 offset:0\n\t" \
  "ds_read_b128 v[76:79], v27 offset:64\n\tds_read_b128 v[80:83], v27 offset:128\n\t" \
  "ds_read_b128 v[84:87], v27 offset:192\n\tds_read_b128 v[88:91], v27 offset:4608\n\t" \
  "ds_read_b128 v[92:95], v27 offset:4672\n\tds_read_b128 v[96:99], v27 offset:4736\n\t" \
  "ds_read_b128 v[100:103], v27 offset:4800\n\tds_write_addtid_b32 v36 offset:0\n\t" \
  "ds_write_addtid_b32 v37 offset:4608\n\tds_read_b128 v[40:43], v12 offset:0\n\t" \
  "ds_read_b128 v[44:47], v12 offset:64\n\tds_read_b128 v[48:51], v12 offset:128\n\t" \
  "v_add_u32_sdwa v56, %[w], v16 dst_sel:DWORD dst_unused:UNUSED_PAD src0_sel:DWORD src1_sel:WORD_0\n\tv_add_u32_sdwa v57, %[w], v16 dst_sel:DWORD dst_unused:UNUSED_PAD src0_sel:DWORD src1_sel:WORD_1\n\t" \
  "v_add_u32_sdwa v58, %[w], v17 dst_sel:DWORD dst_unused:UNUSED_PAD src0_sel:DWORD src1_sel:WORD_0\n\tv_add_u32_sdwa v59, %[w], v17 dst_sel:DWORD dst_unused:UNUSED_PAD src0_sel:DWORD src1_sel:WORD_1\n\t" \
  "v_add_u32 v14, v56, v35\n\tv_add_u32 v15, v57, v35\n\t" \
  "s_waitcnt lgkmcnt(12)\n\tv_mfma_f32_16x16x32_f16 v[60:63], v[104:107], v[72:75], 0\n\t" \
  "v_mfma_f32_16x16x32_f16 v[4:7], v[136:139], v[72:75], 0\n\ts_cmp_lt_u32 %[w], 50176\n\t" \
  "s_cbranch_scc1 1f\n\ts_setprio 1\n\t" \
  "1:\n\ts_waitcnt lgkmcnt(2)\n\t" \
  "v_mfma_f32_16x16x32_f16 v[60:63], v[108:111], v[76:79], v[60:63]\n\tv_pk_mul_f16 v52, v40, v72 op_sel:[1,1] op_sel_hi:[1,0] neg_lo:[1,0]\n\t" \
  "v_pk_mul_f16 v53, v41, v73 op_sel:[1,1] op_sel_hi:[1,0] neg_lo:[1,0]\n\tv_pk_mul_f16 v54, v42, v74 op_sel:[1,1] op_sel_hi:[1,0] neg_lo:[1,0]\n\t" \
  "v_pk_mul_f16 v55, v43, v75 op_sel:[1,1] op_sel_hi:[1,0] neg_lo:[1,0]\n\tv_mfma_f32_16x16x32_f16 v[4:7], v[140:143], v[76:79], v[4:7]\n\t" \
  "v_pk_fma_f16 v40, v40, v72, v52 op_sel:[0,0,0] op_sel_hi:[0,1,1]\n\tv_pk_fma_f16 v41, v41, v73, v53 op_sel:[0,0,0] op_sel_hi:[0,1,1]\n\t" \
  "v_pk_fma_f16 v42, v42, v74, v54 op_sel:[0,0,0] op_sel_hi:[0,1,1]\n\tv_pk_fma_f16 v43, v43, v75, v55 op_sel:[0,0,0] op_sel_hi:[0,1,1]\n\t" \
  "s_waitcnt lgkmcnt(1)\n\tv_mfma_f32_16x16x32_f16 v[60:63], v[112:115], v[80:83], v[60:63]\n\t" \
  "ds_read_b128 v[72:75], v12 offset:192\n\tv_pk_mul_f16 v52, v44, v76 op_sel:[1,1] op_sel_hi:[1,0] neg_lo:[1,0]\n\t" \
  "v_pk_mul_f16 v53, v45, v77 op_sel:[1,1] op_sel_hi:[1,0] neg_lo:[1,0]\n\tv_pk_mul_f16 v54, v46, v78 op_sel:[1,1] op_sel_hi:[1,0] neg_lo:[1,0]\n\t" \
  "v_pk_mul_f16 v55, v47, v79 op_sel:[1,1] op_sel_hi:[1,0] neg_lo:[1,0]\n\tv_mfma_f32_16x16x32_f16 v[4:7], v[144:147], v[80:83], v[4:7]\n\t" \
  "v_pk_fma_f16 v44, v44, v76, v52 op_sel:[0,0,0] op_sel_hi:[0,1,1]\n\tv_pk_fma_f16 v45, v45, v77, v53 op_sel:[0,0,0] op_sel_hi:[0,1,1]\n\t" \
  "v_pk_fma_f16 v46, v46, v78, v54 op_sel:[0,0,0] op_sel_hi:[0,1,1]\n\tv_pk_fma_f16 v47, v47, v79, v55 op_sel:[0,0,0] op_sel_hi:[0,1,1]\n\t" \
  "s_waitcnt lgkmcnt(1)\n\tv_mfma_f32_16x16x32_f16 v[60:63], v[116:119], v[84:87], v[60:63]\n\t" \
  "ds_read_b128 v[76:79], v12 offset:4608\n\tv_pk_mul_f16 v52, v48, v80 op_sel:[1,1] op_sel_hi:[1,0] neg_lo:[1,0]\n\t" \
  "v_pk_mul_f16 v53, v49, v81 op_sel:[1,1] op_sel_hi:[1,0] neg_lo:[1,0]\n\tv_pk_mul_f16 v54, v50, v82 op_sel:[1,1] op_sel_hi:[1,0] neg_lo:[1,0]\n\t" \
  "v_pk_mul_f16 v55, v51, v83 op_sel:[1,1] op_sel_hi:[1,0] neg_lo:[1,0]\n\tv_mfma_f32_16x16x32_f16 v[4:7], v[148:151], v[84:87], v[4:7]\n\t" \
  "v_pk_fma_f16 v48, v48, v80, v52 op_sel:[0,0,0] op_sel_hi:[0,1,1]\n\tv_pk_fma_f16 v49, v49, v81, v53 op_sel:[0,0,0] op_sel_hi:[0,1,1]\n\t" \
  "v_pk_fma_f16 v50, v50, v82, v54 op_sel:[0,0,0] op_sel_hi:[0,1,1]\n\tv_pk_fma_f16 v51, v51, v83, v55 op_sel:[0,0,0] op_sel_hi:[0,1,1]\n\t" \
  "s_waitcnt lgkmcnt(1)\n\tv_mfma_f32_16x16x32_f16 v[60:63], v[120:123], v[88:91], v[60:63]\n\t" \
  "ds_read_b128 v[80:83], v12 offset:4672\n\tv_pk_mul_f16 v52, v72, v84 op_sel:[1,1] op_sel_hi:[1,0] neg_lo:[1,0]\n\t" \
  "v_pk_mul_f16 v53, v73, v85 op_sel:[1,1] op_sel_hi:[1,0] neg_lo:[1,0]\n\tv_pk_mul_f16 v54, v74, v86 op_sel:[1,1] op_sel_hi:[1,0] neg_lo:[1,0]\n\t" \
  "v_pk_mul_f16 v55, v75, v87 op_sel:[1,1] op_sel_hi:[1,0] neg_lo:[1,0]\n\tv_mfma_f32_16x16x32_f16 v[4:7], v[152:155], v[88:91], v[4:7]\n\t" \
  "v_pk_fma_f16 v72, v72, v84, v52 op_sel:[0,0,0] op_sel_hi:[0,1,1]\n\tv_pk_fma_f16 v73, v73, v85, v53 op_sel:[0,0,0] op_sel_hi:[0,1,1]\n\t" \
  "v_pk_fma_f16 v74, v74, v86, v54 op_sel:[0,0,0] op_sel_hi:[0,1,1]\n\tv_pk_fma_f16 v75, v75, v87, v55 op_sel:[0,0,0] op_sel_hi:[0,1,1]\n\t" \
  "s_waitcnt lgkmcnt(1)\n\tv_mfma_f32_16x16x32_f16 v[60:63], v[124:127], v[92:95], v[60:63]\n\t" \
  "ds_read_b128 v[84:87], v12 offset:4736\n\tv_pk_mul_f16 v52, v76, v88 op_sel:[1,1] op_sel_hi:[1,0] neg_lo:[1,0]\n\t" \
  "v_pk_mul_f16 v53, v77, v89 op_sel:[1,1] op_sel_hi:[1,0] neg_lo:[1,0]\n\tv_pk_mul_f16 v54, v78, v90 op_sel:[1,1] op_sel_hi:[1,0] neg_lo:[1,0]\n\t" \
  "v_pk_mul_f16 v55, v79, v91 op_sel:[1,1] op_sel_hi:[1,0] neg_lo:[1,0]\n\tv_mfma_f32_16x16x32_f16 v[4:7], v[156:159], v[92:95], v[4:7]\n\t" \
  "v_pk_fma_f16 v76, v76, v88, v52 op_sel:[0,0,0] op_sel_hi:[0,1,1]\n\tv_pk_fma_f16 v77, v77, v89, v53 op_sel:[0,0,0] op_sel_hi:[0,1,1]\n\t" \
  "v_pk_fma_f16 v78, v78, v90, v54 op_sel:[0,0,0] op_sel_hi:[0,1,1]\n\tv_pk_fma_f16 v79, v79, v91, v55 op_sel:[0,0,0] op_sel_hi:[0,1,1]\n\t" \
  "s_waitcnt lgkmcnt(1)\n\tv_mfma_f32_16x16x32_f16 v[60:63], v[128:131], v[96:99], v[60:63]\n\t" \
  "ds_read_b128 v[88:91], v12 offset:4800\n\tv_pk_mul_f16 v52, v80, v92 op_sel:[1,1] op_sel_hi:[1,0] neg_lo:[1,0]\n\t" \
  "v_pk_mul_f16 v53, v81, v93 op_sel:[1,1] op_sel_hi:[1,0] neg_lo:[1,0]\n\tv_pk_mul_f16 v54, v82, v94 op_sel:[1,1] op_sel_hi:[1,0] neg_lo:[1,0]\n\t" \
  "v_pk_mul_f16 v55, v83, v95 op_sel:[1,1] op_sel_hi:[1,0] neg_lo:[1,0]\n\tv_mfma_f32_16x16x32_f16 v[4:7], v[160:163], v[96:99], v[4:7]\n\t" \
  "v_pk_fma_f16 v80, v80, v92, v52 op_sel:[0,0,0] op_sel_hi:[0,1,1]\n\tv_pk_fma_f16 v81, v81, v93, v53 op_sel:[0,0,0] op_sel_hi:[0,1,1]\n\t" \
  "v_pk_fma_f16 v82, v82, v94, v54 op_sel:[0,0,0] op_sel_hi:[0,1,1]\n\tv_pk_fma_f16 v83, v83, v95, v55 op_sel:[0,0,0] op_sel_hi:[0,1,1]\n\t" \
  "s_waitcnt lgkmcnt(1)\n\tv_mfma_f32_16x16x32_f16 v[60:63], v[132:135], v[100:103], v[60:63]\n\t" \
  "v_pk_mul_f16 v52, v84, v96 op_sel:[1,1] op_sel_hi:[1,0] neg_lo:[1,0]\n\tv_pk_mul_f16 v53, v85, v97 op_sel:[1,1] op_sel_hi:[1,0] neg_lo:[1,0]\n\t" \
  "v_pk_mul_f16 v54, v86, v98 op_sel:[1,1] op_sel_hi:[1,0] neg_lo:[1,0]\n\tv_pk_mul_f16 v55, v87, v99 op_sel:[1,1] op_sel_hi:[1,0] neg_lo:[1,0]\n\t" \
  "v_mfma_f32_16x16x32_f16 v[4:7], v[164:167], v[100:103], v[4:7]\n\tv_pk_fma_f16 v84, v84, v96, v52 op_sel:[0,0,0] op_sel_hi:[0,1,1]\n\t" \
  "v_pk_fma_f16 v85, v85, v97, v53 op_sel:[0,0,0] op_sel_hi:[0,1,1]\n\tv_pk_fma_f16 v86, v86, v98, v54 op_sel:[0,0,0] op_sel_hi:[0,1,1]\n\t" \
  "v_pk_fma_f16 v87, v87, v99, v55 op_sel:[0,0,0] op_sel_hi:[0,1,1]\n\ts_waitcnt lgkmcnt(0)\n\t" \
  "v_mfma_f32_16x16x32_f16 v[8:11], v[136:139], v[40:43], 0\n\tv_pk_mul_f16 v52, v88, v100 op_sel:[1,1] op_sel_hi:[1,0] neg_lo:[1,0]\n\t" \
  "v_pk_mul_f16 v53, v89, v101 op_sel:[1,1] op_sel_hi:[1,0] neg_lo:[1,0]\n\tv_pk_mul_f16 v54, v90, v102 op_sel:[1,1] op_sel_hi:[1,0] neg_lo:[1,0]\n\t" \
  "v_mfma_f32_16x16x32_f16 v[64:67], v[104:107], v[40:43], 0\n\tv_pk_mul_f16 v55, v91, v103 op_sel:[1,1] op_sel_hi:[1,0] neg_lo:[1,0]\n\t" \
  "v_pk_fma_f16 v88, v88, v100, v52 op_sel:[0,0,0] op_sel_hi:[0,1,1]\n\tv_pk_fma_f16 v89, v89, v101, v53 op_sel:[0,0,0] op_sel_hi:[0,1,1]\n\t" \
  "v_mfma_f32_16x16x32_f16 v[8:11], v[140:143], v[44:47], v[8:11]\n\tv_pk_fma_f16 v90, v90, v102, v54 op_sel:[0,0,0] op_sel_hi:[0,1,1]\n\t" \
  "v_pk_fma_f16 v91, v91, v103, v55 op_sel:[0,0,0] op_sel_hi:[0,1,1]\n\tv_mov_b64 v[100:101], 0\n\t" \
  "v_mfma_f32_16x16x32_f16 v[64:67], v[108:111], v[44:47], v[64:67]\n\tv_mov_b64 v[102:103], 0\n\t" \
  "v_cvt_pk_f16_f32 v4, v4, v5\n\tv_cvt_pk_f16_f32 v5, v6, v7\n\t" \
  "v_mfma_f32_16x16x32_f16 v[8:11], v[144:147], v[48:51], v[8:11]\n\tv_cvt_pk_f16_f32 v92, v60, v61\n\t" \
  "v_cvt_pk_f16_f32 v93, v62, v63\n\tv_permlane16_swap_b32 v4, v5\n\t" \
  "v_mfma_f32_16x16x32_f16 v[64:67], v[112:115], v[48:51], v[64:67]\n\tv_perm_b32 v6, v4, v4, v33\n\t" \
  "v_perm_b32 v7, v4, v4, v34\n\tv_mfma_f32_16x16x32_f16 v[8:11], v[148:151], v[72:75], v[8:11]\n\t" \
  "ds_write_b128 v58, v[100:103]\n\tv_mfma_f32_16x16x32_f16 v[64:67], v[116:119], v[72:75], v[64:67]\n\t" \
  "ds_write2_b32 v56, v92, v93 offset0:193 offset1:194\n\tv_mfma_f32_16x16x32_f16 v[8:11], v[152:155], v[76:79], v[8:11]\n\t" \
  "ds_write2_b32 v57, v92, v93 offset0:193 offset1:194\n\tv_mfma_f32_16x16x32_f16 v[64:67], v[120:123], v[76:79], v[64:67]\n\t" \
  "ds_write2_b32 v14, v6, v7 offset0:64 offset1:65\n\tv_mfma_f32_16x16x32_f16 v[8:11], v[156:159], v[80:83], v[8:11]\n\t" \
  "ds_write2_b32 v15, v6, v7 offset0:64 offset1:65\n\tv_mfma_f32_16x16x32_f16 v[64:67], v[124:127], v[80:83], v[64:67]\n\t" \
  "ds_write_b16 v59, v5 offset:48\n\tv_mfma_f32_16x16x32_f16 v[8:11], v[160:163], v[84:87], v[8:11]\n\t" \
  "ds_write_b16_d16_hi v59, v5 offset:16\n\tv_mfma_f32_16x16x32_f16 v[64:67], v[128:131], v[84:87], v[64:67]\n\t" \
  "ds_write_b16 v59, v5 offset:878\n\tv_mfma_f32_16x16x32_f16 v[8:11], v[164:167], v[88:91], v[8:11]\n\t" \
  "ds_write_b16_d16_hi v59, v5 offset:846\n\tv_mfma_f32_16x16x32_f16 v[64:67], v[132:135], v[88:91], v[64:67]\n\t" \
  "ds_write_b16 v59, v5 offset:1708\n\tds_write_b16_d16_hi v59, v5 offset:1676\n\t" \
  "ds_write_b16 v59, v5 offset:2538\n\tds_write_b16_d16_hi v59, v5 offset:2506\n\t" \
  "s_nop 1\n\tv_cvt_pk_f16_f32 v8, v8, v9\n\t" \
  "v_cvt_pk_f16_f32 v9, v10, v11\n\tv_cvt_pk_f16_f32 v46, v64, v65\n\t" \
  "v_cvt_pk_f16_f32 v47, v66, v67\n\tv_permlane16_swap_b32 v8, v9\n\t" \
  "v_perm_b32 v10, v8, v8, v33\n\tv_perm_b32 v11, v8, v8, v34\n\t" \
  "ds_write2_b32 v56, v46, v47 offset0:129 offset1:130\n\tds_write2_b32 v57, v46, v47 offset0:129 offset1:130\n\t" \
  "ds_write2_b32 v14, v10, v11 offset0:0 offset1:1\n\tds_write2_b32 v15, v10, v11 offset0:0 offset1:1\n\t" \
  "ds_write_b16 v59, v9 offset:32\n\tds_write_b16_d16_hi v59, v9 offset:0\n\t" \
  "ds_write_b16 v59, v9 offset:862\n\tds_write_b16_d16_hi v59, v9 offset:830\n\t" \
  "ds_write_b16 v59, v9 offset:1692\n\tds_write_b16_d16_hi v59, v9 offset:1660\n\t" \
  "ds_write_b16 v59, v9 offset:2522\n\tds_write_b16_d16_hi v59, v9 offset:2490\n\t"
#define GEN_CLOBBER "v4", "v5", "v6", "v7", "v10", "v11", "v13", "v14", "v15", "v36", "v37", "v38", "v39", "v40", "v41", "v42", "v43", "v44", "v45", "v47", "v48", "v49", "v50", "v51", "v52", "v53", "v54", "v55", "v56", "v57", "v58", "v59", "v60", "v61", "v62", "v63", "v64", "v65", "v66", "v67", "v72", "v73", "v74", "v75", "v76", "v77", "v78", "v79", "v80", "v81", "v82", "v83", "v84", "v85", "v86", "v87", "v88", "v89", "v90", "v91", "v92", "v94", "v95", "v98", "v99", "v100", "v101", "v102", "v103", "v104", "v105", "v106", "v107", "v108", "v109", "v110", "v111", "v112", "v113", "v114", "v115", "v116", "v117", "v118", "v119", "v120", "v121", "v122", "v123", "v124", "v125", "v126", "v127", "v128", "v129", "v130", "v131", "v132", "v133", "v134", "v135", "v136", "v137", "v138", "v139", "v140", "v141", "v142", "v143", "v144", "v145", "v146", "v147", "v148", "v149", "v150", "v151", "v152", "v153", "v154", "v155", "v156", "v157", "v158", "v159", "v160", "v161", "v162", "v163", "v164", "v165", "v166", "v167"
#define CONV_ASM \
  "ds_read_b32 v34, v30\n\tds_read_b64 v[132:133], v26 offset:512\n\tds_read_b64 v[134:135], v26 offset:520\n\t" \
  "ds_read_b64 v[100:101], v26 offset:0\n\tds_read_b64 v[102:103], v26 offset:8\n\tds_read_b64 v[136:137], v26 offset:576\n\t" \
  "ds_read_b64 v[138:139], v26 offset:584\n\tds_read_b64 v[104:105], v26 offset:64\n\tds_read_b64 v[106:107], v26 offset:72\n\t" \
  "ds_read_b64 v[140:141], v26 offset:640\n\tds_read_b64 v[142:143], v26 offset:648\n\tds_read_b64 v[108:109], v26 offset:128\n\t" \
  "ds_read_b64 v[110:111], v26 offset:136\n\tds_read_b64 v[144:145], v26 offset:704\n\tds_read_b64 v[146:147], v26 offset:712\n\t" \
  "ds_read_b64 v[112:113], v26 offset:192\n\tds_read_b64 v[114:115], v26 offset:200\n\tds_read_b64 v[148:149], v26 offset:768\n\t" \
  "ds_read_b64 v[150:151], v26 offset:776\n\tds_read_b64 v[116:117], v26 offset:256\n\tds_read_b64 v[118:119], v26 offset:264\n\t" \
  TS(60, 61) "s_waitcnt lgkmcnt(15)\n\tv_cmp_gt_u32 vcc, 1, v34\n\t" \
  "s_cbranch_vccz 2f\n\ts_mov_b32 s6, 0x10000\n\t1:\n\t" \
  "s_sleep 1\n\tds_read_b32 v34, v30\n\ts_sub_u32 s6, s6, 1\n\t" \
  "s_waitcnt lgkmcnt(0)\n\tv_cmp_gt_u32 vcc, 1, v34\n\ts_cbranch_scc1 2f\n\t" \
  "s_cbranch_vccnz 1b\n\t2:\n\t" TS(62, 63) \
  "s_cmp_lt_u32 %[w], 4\n\ts_setprio 1\n\ts_cbranch_scc1 1f\n\t" \
  "s_setprio 2\n\t1:\n\tds_read_b128 v[0:3], v28 offset:0\n\t" \
  "ds_read_b128 v[4:7], v28 offset:64\n\tds_read_b128 v[8:11], v28 offset:128\n\tds_read_b128 v[12:15], v28 offset:192\n\t" \
  "ds_read_b128 v[16:19], v28 offset:256\n\tds_read_b128 v[20:23], v28 offset:320\n\ts_waitcnt lgkmcnt(5)\n\t" \
  "v_mfma_f32_16x16x32_f16 v[52:55], v[132:135], v[0:3], 0\n\tds_read_b64 v[152:153], v26 offset:832\n\tds_read_b64 v[154:155], v26 offset:840\n\t" \
  "v_mfma_f32_16x16x32_f16 v[56:59], v[100:103], v[0:3], 0\n\tds_read_b64 v[120:121], v26 offset:320\n\tds_read_b64 v[122:123], v26 offset:328\n\t" \
  "ds_read_b128 v[0:3], v28 offset:384\n\ts_waitcnt lgkmcnt(9)\n\tv_mfma_f32_16x16x32_f16 v[52:55], v[136:139], v[4:7], v[52:55]\n\t" \
  "ds_read_b64 v[156:157], v26 offset:896\n\tds_read_b64 v[158:159], v26 offset:904\n\tv_mfma_f32_16x16x32_f16 v[56:59], v[104:107], v[4:7], v[56:59]\n\t" \
  "ds_read_b64 v[124:125], v26 offset:384\n\tds_read_b64 v[126:127], v26 offset:392\n\tds_read_b128 v[4:7], v28 offset:448\n\t" \
  "s_waitcnt lgkmcnt(13)\n\tv_mfma_f32_16x16x32_f16 v[52:55], v[140:143], v[8:11], v[52:55]\n\tds_read_b64 v[160:161], v26 offset:960\n\t" \
  "ds_read_b64 v[162:163], v26 offset:968\n\tv_mfma_f32_16x16x32_f16 v[56:59], v[108:111], v[8:11], v[56:59]\n\tds_read_b64 v[128:129], v26 offset:448\n\t" \
  "ds_read_b64 v[130:131], v26 offset:456\n\tds_read_b128 v[8:11], v28 offset:512\n\ts_waitcnt lgkmcnt(15)\n\t" \
  "v_mfma_f32_16x16x32_f16 v[52:55], v[144:147], v[12:15], v[52:55]\n\tds_read_b64 v[164:165], v26 offset:1024\n\tds_read_b64 v[166:167], v26 offset:1032\n\t" \
  "v_mfma_f32_16x16x32_f16 v[56:59], v[112:115], v[12:15], v[56:59]\n\tds_read_b64 v[76:77], v31 offset:72\n\tds_read_b64 v[78:79], v31 offset:176\n\t" \
  "ds_read_b128 v[12:15], v28 offset:576\n\tv_mfma_f32_16x16x32_f16 v[52:55], v[148:151], v[16:19], v[52:55]\n\tds_read_b64 v[80:81], v31 offset:40\n\t" \
  "ds_read_b64 v[82:83], v31 offset:144\n\tv_mfma_f32_16x16x32_f16 v[56:59], v[116:119], v[16:19], v[56:59]\n\tds_read_b64 v[84:85], v31 offset:8\n\t" \
  "ds_read_b64 v[86:87], v31 offset:112\n\tds_read_b128 v[16:19], v28 offset:640\n\ts_waitcnt lgkmcnt(15)\n\t" \
  "v_mfma_f32_16x16x32_f16 v[52:55], v[152:155], v[20:23], v[52:55]\n\tds_read_b64 v[88:89], v31 offset:280\n\tds_read_b64 v[90:91], v31 offset:384\n\t" \
  "v_mfma_f32_16x16x32_f16 v[56:59], v[120:123], v[20:23], v[56:59]\n\tds_read_b64 v[92:93], v31 offset:248\n\tds_read_b64 v[94:95], v31 offset:352\n\t" \
  "ds_read_b128 v[20:23], v28 offset:704\n\tv_mfma_f32_16x16x32_f16 v[52:55], v[156:159], v[0:3], v[52:55]\n\tds_read_b64 v[96:97], v31 offset:216\n\t" \
  "ds_read_b64 v[98:99], v31 offset:320\n\tv_mfma_f32_16x16x32_f16 v[56:59], v[124:127], v[0:3], v[56:59]\n\tds_read_b128 v[0:3], v28 offset:768\n\t" \
  "s_waitcnt lgkmcnt(15)\n\tv_mfma_f32_16x16x32_f16 v[52:55], v[160:163], v[4:7], v[52:55]\n\tv_mfma_f32_16x16x32_f16 v[56:59], v[128:131], v[4:7], v[56:59]\n\t" \
  "ds_read_b128 v[4:7], v28 offset:832\n\tv_mfma_f32_16x16x32_f16 v[52:55], v[164:167], v[8:11], v[52:55]\n\tv_mfma_f32_16x16x32_f16 v[56:59], v[132:135], v[8:11], v[56:59]\n\t" \
  "v_mfma_f32_16x16x32_f16 v[60:63], v[100:103], v[8:11], 0\n\tds_read_b128 v[8:11], v28 offset:896\n\ts_waitcnt lgkmcnt(15)\n\t" \
  "v_mfma_f32_16x16x32_f16 v[56:59], v[136:139], v[12:15], v[56:59]\n\tds_read_b32 v34, v30\n\tv_mfma_f32_16x16x32_f16 v[60:63], v[104:107], v[12:15], v[60:63]\n\t" \
  "ds_read_b128 v[12:15], v28 offset:960\n\tv_cvt_pk_bf16_f32 v52, v52, v53\n\tv_cvt_pk_bf16_f32 v53, v54, v55\n\t" \
  TS(64, 65) "s_waitcnt lgkmcnt(12)\n\tv_mfma_f32_16x16x32_f16 v[56:59], v[140:143], v[16:19], v[56:59]\n\t" \
  "ds_write_b64 v29, v[52:53] offset:0\n\tds_write_b32 v25, v25 offset:0\n\tv_mfma_f32_16x16x32_f16 v[60:63], v[108:111], v[16:19], v[60:63]\n\t" \
  "s_waitcnt lgkmcnt(3)\n\tv_cmp_gt_u32 vcc, 2, v34\n\ts_cbranch_vccz 2f\n\t" \
  "s_mov_b32 s6, 0x10000\n\t1:\n\ts_sleep 1\n\t" \
  "ds_read_b32 v34, v30\n\ts_sub_u32 s6, s6, 1\n\ts_waitcnt lgkmcnt(0)\n\t" \
  "v_cmp_gt_u32 vcc, 2, v34\n\ts_cbranch_scc1 2f\n\ts_cbranch_vccnz 1b\n\t" \
  "2:\n\tds_read_b128 v[16:19], v28 offset:1024\n\tv_mfma_f32_16x16x32_f16 v[56:59], v[144:147], v[20:23], v[56:59]\n\t" \
  "v_mfma_f32_16x16x32_f16 v[60:63], v[112:115], v[20:23], v[60:63]\n\tds_read_b128 v[68:71], v32\n\tds_read_b128 v[72:75], v32 offset:1024\n\t" \
  "ds_read_b128 v[20:23], v28 offset:1088\n\tv_mfma_f32_16x16x32_f16 v[56:59], v[148:151], v[0:3], v[56:59]\n\tv_mfma_f32_16x16x32_f16 v[60:63], v[116:119], v[0:3], v[60:63]\n\t" \
  "ds_read_b128 v[0:3], v28 offset:496\n\tv_mfma_f32_16x16x32_f16 v[56:59], v[152:155], v[4:7], v[56:59]\n\tv_mfma_f32_16x16x32_f16 v[60:63], v[120:123], v[4:7], v[60:63]\n\t" \
  "ds_read_b128 v[4:7], v28 offset:1152\n\tv_mfma_f32_16x16x32_f16 v[56:59], v[156:159], v[8:11], v[56:59]\n\tv_mfma_f32_16x16x32_f16 v[60:63], v[124:127], v[8:11], v[60:63]\n\t" \
  "ds_read_b128 v[8:11], v28 offset:1216\n\ts_waitcnt lgkmcnt(9)\n\tv_mfma_f32_16x16x32_f16 v[56:59], v[160:163], v[12:15], v[56:59]\n\t" \
  "v_mfma_f32_16x16x32_f16 v[60:63], v[128:131], v[12:15], v[60:63]\n\tds_read_b128 v[12:15], v28 offset:480\n\ts_waitcnt lgkmcnt(7)\n\t" \
  "v_mfma_f32_16x16x32_f16 v[56:59], v[164:167], v[16:19], v[56:59]\n\tv_mfma_f32_16x16x32_f16 v[60:63], v[132:135], v[16:19], v[60:63]\n\tv_mfma_f32_16x16x32_f16 v[64:67], v[100:103], v[16:19], 0\n\t" \
  "ds_read_b128 v[16:19], v28 offset:1280\n\ts_waitcnt lgkmcnt(5)\n\tv_mfma_f32_16x16x32_f16 v[60:63], v[136:139], v[20:23], v[60:63]\n\t" \
  "v_mfma_f32_16x16x32_f16 v[64:67], v[104:107], v[20:23], v[64:67]\n\tds_read_b128 v[20:23], v28 offset:1344\n\ts_waitcnt lgkmcnt(5)\n\t" \
  "v_mfma_f32_16x16x32_f16 v[36:39], v[0:3], v[68:71], 0\n\tv_cvt_pk_bf16_f32 v56, v56, v57\n\tv_cvt_pk_bf16_f32 v57, v58, v59\n\t" \
  "v_mfma_f32_16x16x32_f16 v[40:43], v[0:3], v[72:75], 0\n\tds_write_b64 v29, v[56:57] offset:512\n\tds_write_b32 v25, v25 offset:64\n\t" \
  "ds_read_b128 v[0:3], v28 offset:1408\n\ts_waitcnt lgkmcnt(7)\n\tv_mfma_f32_16x16x32_f16 v[60:63], v[140:143], v[4:7], v[60:63]\n\t" \
  "v_cvt_pk_f16_f32 v36, v36, v37\n\tv_cvt_pk_f16_f32 v37, v38, v39\n\tv_mfma_f32_16x16x32_f16 v[64:67], v[108:111], v[4:7], v[64:67]\n\t" \
  "ds_read_b128 v[4:7], v28 offset:1472\n\tv_cvt_pk_f16_f32 v38, v40, v41\n\tv_cvt_pk_f16_f32 v39, v42, v43\n\t" \
  "s_waitcnt lgkmcnt(7)\n\tv_mfma_f32_16x16x32_f16 v[60:63], v[144:147], v[8:11], v[60:63]\n\tv_mfma_f32_16x16x32_f16 v[64:67], v[112:115], v[8:11], v[64:67]\n\t" \
  "ds_read_b128 v[8:11], v28 offset:1536\n\tv_mfma_f32_16x16x32_f16 v[60:63], v[36:39], v[76:79], v[60:63]\n\tv_mfma_f32_16x16x32_f16 v[64:67], v[36:39], v[80:83], v[64:67]\n\t" \
  "v_mfma_f32_16x16x32_f16 v[52:55], v[36:39], v[84:87], 0\n\ts_waitcnt lgkmcnt(7)\n\tv_mfma_f32_16x16x32_f16 v[44:47], v[12:15], v[68:71], 0\n\t" \
  "v_mfma_f32_16x16x32_f16 v[40:43], v[12:15], v[72:75], 0\n\tds_read_b128 v[12:15], v28 offset:1600\n\ts_waitcnt lgkmcnt(7)\n\t" \
  "v_mfma_f32_16x16x32_f16 v[60:63], v[148:151], v[16:19], v[60:63]\n\tv_mfma_f32_16x16x32_f16 v[64:67], v[116:119], v[16:19], v[64:67]\n\tds_read_b128 v[16:19], v28 offset:1008\n\t" \
  "s_waitcnt lgkmcnt(7)\n\tv_mfma_f32_16x16x32_f16 v[60:63], v[152:155], v[20:23], v[60:63]\n\tv_cvt_pk_f16_f32 v44, v44, v45\n\t" \
  "v_cvt_pk_f16_f32 v45, v46, v47\n\tv_cvt_pk_f16_f32 v46, v40, v41\n\tv_cvt_pk_f16_f32 v47, v42, v43\n\t" \
  "v_mfma_f32_16x16x32_f16 v[64:67], v[120:123], v[20:23], v[64:67]\n\tds_read_b128 v[20:23], v28 offset:1664\n\tv_mfma_f32_16x16x32_f16 v[60:63], v[44:47], v[88:91], v[60:63]\n\t" \
  "v_mfma_f32_16x16x32_f16 v[64:67], v[44:47], v[92:95], v[64:67]\n\tv_mfma_f32_16x16x32_f16 v[52:55], v[44:47], v[96:99], v[52:55]\n\ts_waitcnt lgkmcnt(5)\n\t" \
  "v_mfma_f32_16x16x32_f16 v[60:63], v[156:159], v[0:3], v[60:63]\n\tv_mfma_f32_16x16x32_f16 v[64:67], v[124:127], v[0:3], v[64:67]\n\tds_read_b128 v[0:3], v28 offset:1728\n\t" \
  "s_waitcnt lgkmcnt(5)\n\tv_mfma_f32_16x16x32_f16 v[60:63], v[160:163], v[4:7], v[60:63]\n\tv_mfma_f32_16x16x32_f16 v[64:67], v[128:131], v[4:7], v[64:67]\n\t" \
  "ds_read_b128 v[4:7], v28 offset:992\n\ts_waitcnt lgkmcnt(5)\n\tv_mfma_f32_16x16x32_f16 v[60:63], v[164:167], v[8:11], v[60:63]\n\t" \
  "v_mfma_f32_16x16x32_f16 v[64:67], v[132:135], v[8:11], v[64:67]\n\tv_mfma_f32_16x16x32_f16 v[52:55], v[100:103], v[8:11], v[52:55]\n\tds_read_b128 v[8:11], v28 offset:1792\n\t" \
  "s_waitcnt lgkmcnt(5)\n\tv_mfma_f32_16x16x32_f16 v[64:67], v[136:139], v[12:15], v[64:67]\n\tv_mfma_f32_16x16x32_f16 v[52:55], v[104:107], v[12:15], v[52:55]\n\t" \
  "ds_read_b128 v[12:15], v28 offset:1856\n\ts_waitcnt lgkmcnt(5)\n\tv_mfma_f32_16x16x32_f16 v[36:39], v[16:19], v[68:71], 0\n\t" \
  "v_cvt_pk_bf16_f32 v60, v60, v61\n\tv_cvt_pk_bf16_f32 v61, v62, v63\n\tv_mfma_f32_16x16x32_f16 v[40:43], v[16:19], v[72:75], 0\n\t" \
  "ds_write_b64 v29, v[60:61] offset:1024\n\tds_write_b32 v25, v25 offset:128\n\tds_read_b128 v[16:19], v28 offset:1920\n\t" \
  "s_waitcnt lgkmcnt(7)\n\tv_mfma_f32_16x16x32_f16 v[64:67], v[140:143], v[20:23], v[64:67]\n\tds_read_b32 v34, v30\n\t" \
  "v_cvt_pk_f16_f32 v36, v36, v37\n\tv_cvt_pk_f16_f32 v37, v38, v39\n\tv_mfma_f32_16x16x32_f16 v[52:55], v[108:111], v[20:23], v[52:55]\n\t" \
  "ds_read_b128 v[20:23], v28 offset:1984\n\tv_cvt_pk_f16_f32 v38, v40, v41\n\tv_cvt_pk_f16_f32 v39, v42, v43\n\t" \
  "s_waitcnt lgkmcnt(8)\n\tv_mfma_f32_16x16x32_f16 v[64:67], v[144:147], v[0:3], v[64:67]\n\tv_mfma_f32_16x16x32_f16 v[52:55], v[112:115], v[0:3], v[52:55]\n\t" \
  "s_waitcnt lgkmcnt(1)\n\tv_cmp_gt_u32 vcc, 3, v34\n\ts_cbranch_vccz 2f\n\t" \
  "s_mov_b32 s6, 0x10000\n\t1:\n\ts_sleep 1\n\t" \
  "ds_read_b32 v34, v30\n\ts_sub_u32 s6, s6, 1\n\ts_waitcnt lgkmcnt(0)\n\t" \
  "v_cmp_gt_u32 vcc, 3, v34\n\ts_cbranch_scc1 2f\n\ts_cbranch_vccnz 1b\n\t" \
  "2:\n\tds_read_b128 v[0:3], v28 offset:2048\n\tv_mfma_f32_16x16x32_f16 v[64:67], v[36:39], v[76:79], v[64:67]\n\t" \
  "v_mfma_f32_16x16x32_f16 v[52:55], v[36:39], v[80:83], v[52:55]\n\tv_mfma_f32_16x16x32_f16 v[56:59], v[36:39], v[84:87], 0\n\tv_mfma_f32_16x16x32_f16 v[44:47], v[4:7], v[68:71], 0\n\t" \
  "v_mfma_f32_16x16x32_f16 v[40:43], v[4:7], v[72:75], 0\n\tds_read_b128 v[4:7], v28 offset:2112\n\tv_mfma_f32_16x16x32_f16 v[64:67], v[148:151], v[8:11], v[64:67]\n\t" \
  "v_mfma_f32_16x16x32_f16 v[52:55], v[116:119], v[8:11], v[52:55]\n\tds_read_b128 v[8:11], v28 offset:1520\n\tv_mfma_f32_16x16x32_f16 v[64:67], v[152:155], v[12:15], v[64:67]\n\t" \
  "v_mfma_f32_16x16x32_f16 v[52:55], v[120:123], v[12:15], v[52:55]\n\tds_read_b128 v[12:15], v28 offset:2176\n\tv_cvt_pk_f16_f32 v44, v44, v45\n\t" \
  "v_cvt_pk_f16_f32 v45, v46, v47\n\tv_cvt_pk_f16_f32 v46, v40, v41\n\tv_cvt_pk_f16_f32 v47, v42, v43\n\t" \
  "s_nop 1\n\tv_mfma_f32_16x16x32_f16 v[64:67], v[44:47], v[88:91], v[64:67]\n\tv_mfma_f32_16x16x32_f16 v[52:55], v[44:47], v[92:95], v[52:55]\n\t" \
  "v_mfma_f32_16x16x32_f16 v[56:59], v[44:47], v[96:99], v[56:59]\n\tv_mfma_f32_16x16x32_f16 v[64:67], v[156:159], v[16:19], v[64:67]\n\tv_mfma_f32_16x16x32_f16 v[52:55], v[124:127], v[16:19], v[52:55]\n\t" \
  "ds_read_b128 v[16:19], v28 offset:2240\n\ts_waitcnt lgkmcnt(5)\n\tv_mfma_f32_16x16x32_f16 v[64:67], v[160:163], v[20:23], v[64:67]\n\t" \
  "v_mfma_f32_16x16x32_f16 v[52:55], v[128:131], v[20:23], v[52:55]\n\tds_read_b128 v[20:23], v28 offset:1504\n\ts_waitcnt lgkmcnt(5)\n\t" \
  "v_mfma_f32_16x16x32_f16 v[64:67], v[164:167], v[0:3], v[64:67]\n\tv_mfma_f32_16x16x32_f16 v[52:55], v[132:135], v[0:3], v[52:55]\n\tv_mfma_f32_16x16x32_f16 v[56:59], v[100:103], v[0:3], v[56:59]\n\t" \
  "ds_read_b128 v[0:3], v28 offset:2304\n\ts_waitcnt lgkmcnt(5)\n\tv_mfma_f32_16x16x32_f16 v[52:55], v[136:139], v[4:7], v[52:55]\n\t" \
  "v_mfma_f32_16x16x32_f16 v[56:59], v[104:107], v[4:7], v[56:59]\n\tds_read_b128 v[4:7], v28 offset:2368\n\ts_waitcnt lgkmcnt(5)\n\t" \
  "v_mfma_f32_16x16x32_f16 v[36:39], v[8:11], v[68:71], 0\n\tv_cvt_pk_bf16_f32 v64, v64, v65\n\tv_cvt_pk_bf16_f32 v65, v66, v67\n\t" \
  TS(66, 67) "s_cmp_lt_u32 %[w], 4\n\ts_cbranch_scc1 1f\n\t" \
  "s_setprio 1\n\t1:\n\tv_mfma_f32_16x16x32_f16 v[40:43], v[8:11], v[72:75], 0\n\t" \
  "ds_write_b64 v29, v[64:65] offset:1536\n\tds_write_b32 v25, v25 offset:192\n\tds_read_b128 v[8:11], v28 offset:480\n\t" \
  "s_waitcnt lgkmcnt(7)\n\tv_mfma_f32_16x16x32_f16 v[52:55], v[140:143], v[12:15], v[52:55]\n\tv_cvt_pk_f16_f32 v36, v36, v37\n\t" \
  "v_cvt_pk_f16_f32 v37, v38, v39\n\tv_mfma_f32_16x16x32_f16 v[56:59], v[108:111], v[12:15], v[56:59]\n\tds_read_b128 v[12:15], v32 offset:3072\n\t" \
  "v_cvt_pk_f16_f32 v38, v40, v41\n\tv_cvt_pk_f16_f32 v39, v42, v43\n\ts_waitcnt lgkmcnt(7)\n\t" \
  "v_mfma_f32_16x16x32_f16 v[52:55], v[144:147], v[16:19], v[52:55]\n\tv_mfma_f32_16x16x32_f16 v[56:59], v[112:115], v[16:19], v[56:59]\n\tds_read_b128 v[16:19], v28 offset:496\n\t" \
  "v_mfma_f32_16x16x32_f16 v[52:55], v[36:39], v[76:79], v[52:55]\n\tv_mfma_f32_16x16x32_f16 v[56:59], v[36:39], v[80:83], v[56:59]\n\tv_mfma_f32_16x16x32_f16 v[60:63], v[36:39], v[84:87], 0\n\t" \
  "s_waitcnt lgkmcnt(7)\n\tv_mfma_f32_16x16x32_f16 v[44:47], v[20:23], v[68:71], 0\n\tv_mfma_f32_16x16x32_f16 v[40:43], v[20:23], v[72:75], 0\n\t" \
  "ds_read_b128 v[20:23], v32 offset:2048\n\ts_waitcnt lgkmcnt(7)\n\tv_mfma_f32_16x16x32_f16 v[52:55], v[148:151], v[0:3], v[52:55]\n\t" \
  "v_mfma_f32_16x16x32_f16 v[56:59], v[116:119], v[0:3], v[56:59]\n\tds_read_b128 v[0:3], v28 offset:2432\n\ts_waitcnt lgkmcnt(7)\n\t" \
  "v_mfma_f32_16x16x32_f16 v[52:55], v[152:155], v[4:7], v[52:55]\n\tv_cvt_pk_f16_f32 v44, v44, v45\n\tv_cvt_pk_f16_f32 v45, v46, v47\n\t" \
  "v_cvt_pk_f16_f32 v46, v40, v41\n\tv_cvt_pk_f16_f32 v47, v42, v43\n\tv_mfma_f32_16x16x32_f16 v[56:59], v[120:123], v[4:7], v[56:59]\n\t" \
  "ds_read_b128 v[4:7], v28 offset:2496\n\tv_mfma_f32_16x16x32_f16 v[52:55], v[44:47], v[88:91], v[52:55]\n\tv_mfma_f32_16x16x32_f16 v[56:59], v[44:47], v[92:95], v[56:59]\n\t" \
  "v_mfma_f32_16x16x32_f16 v[60:63], v[44:47], v[96:99], v[60:63]\n\ts_waitcnt lgkmcnt(4)\n\tv_mfma_f32_16x16x32_f16 v[40:43], v[8:11], v[12:15], 0\n\t" \
  "ds_read_b64 v[8:9], v31 offset:544\n\tds_read_b64 v[10:11], v31 offset:712\n\tds_read_b64 v[12:13], v31 offset:512\n\t" \
  "ds_read_b64 v[14:15], v31 offset:680\n\ts_waitcnt lgkmcnt(7)\n\tv_mfma_f32_16x16x32_f16 v[52:55], v[48:51], v[16:19], v[52:55]\n\t" \
  "s_waitcnt lgkmcnt(6)\n\tv_mfma_f32_16x16x32_f16 v[36:39], v[16:19], v[20:23], 0\n\tds_read_b64 v[16:17], v31 offset:480\n\t" \
  "ds_read_b64 v[18:19], v31 offset:648\n\tds_read_b64 v[20:21], v31 offset:448\n\tds_read_b64 v[22:23], v31 offset:616\n\t" \
  "s_waitcnt lgkmcnt(9)\n\tv_mfma_f32_16x16x32_f16 v[52:55], v[156:159], v[0:3], v[52:55]\n\tv_mfma_f32_16x16x32_f16 v[56:59], v[124:127], v[0:3], v[56:59]\n\t" \
  "ds_read_b128 v[0:3], v28 offset:2560\n\tv_cvt_pk_f16_f32 v36, v36, v37\n\tv_cvt_pk_f16_f32 v37, v38, v39\n\t" \
  "s_waitcnt lgkmcnt(9)\n\tv_mfma_f32_16x16x32_f16 v[52:55], v[160:163], v[4:7], v[52:55]\n\tv_cvt_pk_f16_f32 v38, v40, v41\n\t" \
  "v_cvt_pk_f16_f32 v39, v42, v43\n\tv_mfma_f32_16x16x32_f16 v[56:59], v[128:131], v[4:7], v[56:59]\n\tds_read_b128 v[4:7], v28 offset:2624\n\t" \
  "s_waitcnt lgkmcnt(8)\n\tv_mfma_f32_16x16x32_f16 v[52:55], v[36:39], v[8:11], v[52:55]\n\tds_read_b128 v[8:11], v28 offset:2032\n\t" \
  "s_waitcnt lgkmcnt(7)\n\tv_mfma_f32_16x16x32_f16 v[56:59], v[36:39], v[12:15], v[56:59]\n\tds_read_b128 v[12:15], v28 offset:2688\n\t" \
  "s_waitcnt lgkmcnt(6)\n\tv_mfma_f32_16x16x32_f16 v[60:63], v[36:39], v[16:19], v[60:63]\n\tds_read_b128 v[16:19], v28 offset:2752\n\t" \
  "s_waitcnt lgkmcnt(5)\n\tv_mfma_f32_16x16x32_f16 v[64:67], v[36:39], v[20:23], 0\n\tds_read_b128 v[20:23], v28 offset:2016\n\t" \
  "s_waitcnt lgkmcnt(5)\n\tv_mfma_f32_16x16x32_f16 v[52:55], v[164:167], v[0:3], v[52:55]\n\tv_mfma_f32_16x16x32_f16 v[56:59], v[132:135], v[0:3], v[56:59]\n\t" \
  "v_mfma_f32_16x16x32_f16 v[60:63], v[100:103], v[0:3], v[60:63]\n\tds_read_b128 v[0:3], v28 offset:2816\n\ts_waitcnt lgkmcnt(5)\n\t" \
  "v_mfma_f32_16x16x32_f16 v[56:59], v[136:139], v[4:7], v[56:59]\n\tv_mfma_f32_16x16x32_f16 v[60:63], v[104:107], v[4:7], v[60:63]\n\tds_read_b128 v[4:7], v28 offset:2880\n\t" \
  "s_waitcnt lgkmcnt(5)\n\tv_mfma_f32_16x16x32_f16 v[44:47], v[8:11], v[68:71], 0\n\tv_cvt_pk_bf16_f32 v52, v52, v53\n\t" \
  "v_cvt_pk_bf16_f32 v53, v54, v55\n\ts_cmp_lt_u32 %[w], 4\n\ts_cbranch_scc0 1f\n\t" \
  "s_setprio 0\n\t1:\n\tv_mfma_f32_16x16x32_f16 v[40:43], v[8:11], v[72:75], 0\n\t" \
  "ds_write_b64 v29, v[52:53] offset:2048\n\tds_write_b32 v25, v25 offset:256\n\tds_read_b128 v[8:11], v28 offset:992\n\t" \
  "s_waitcnt lgkmcnt(7)\n\tv_mfma_f32_16x16x32_f16 v[56:59], v[140:143], v[12:15], v[56:59]\n\tv_cvt_pk_f16_f32 v44, v44, v45\n\t" \
  "v_cvt_pk_f16_f32 v45, v46, v47\n\tv_mfma_f32_16x16x32_f16 v[60:63], v[108:111], v[12:15], v[60:63]\n\tds_read_b128 v[12:15], v32 offset:3072\n\t" \
  "v_cvt_pk_f16_f32 v46, v40, v41\n\tv_cvt_pk_f16_f32 v47, v42, v43\n\ts_waitcnt lgkmcnt(7)\n\t" \
  "v_mfma_f32_16x16x32_f16 v[56:59], v[144:147], v[16:19], v[56:59]\n\tv_mfma_f32_16x16x32_f16 v[60:63], v[112:115], v[16:19], v[60:63]\n\tds_read_b128 v[16:19], v28 offset:1008\n\t" \
  "v_mfma_f32_16x16x32_f16 v[56:59], v[44:47], v[76:79], v[56:59]\n\tv_mfma_f32_16x16x32_f16 v[60:63], v[44:47], v[80:83], v[60:63]\n\tv_mfma_f32_16x16x32_f16 v[64:67], v[44:47], v[84:87], v[64:67]\n\t" \
  "s_waitcnt lgkmcnt(7)\n\tv_mfma_f32_16x16x32_f16 v[36:39], v[20:23], v[68:71], 0\n\tv_mfma_f32_16x16x32_f16 v[40:43], v[20:23], v[72:75], 0\n\t" \
  "ds_read_b128 v[20:23], v32 offset:2048\n\ts_waitcnt lgkmcnt(7)\n\tv_mfma_f32_16x16x32_f16 v[56:59], v[148:151], v[0:3], v[56:59]\n\t" \
  "v_mfma_f32_16x16x32_f16 v[60:63], v[116:119], v[0:3], v[60:63]\n\tds_read_b128 v[0:3], v28 offset:2944\n\ts_waitcnt lgkmcnt(7)\n\t" \
  "v_mfma_f32_16x16x32_f16 v[56:59], v[152:155], v[4:7], v[56:59]\n\tv_cvt_pk_f16_f32 v36, v36, v37\n\tv_cvt_pk_f16_f32 v37, v38, v39\n\t" \
  "v_cvt_pk_f16_f32 v38, v40, v41\n\tv_cvt_pk_f16_f32 v39, v42, v43\n\tv_mfma_f32_16x16x32_f16 v[60:63], v[120:123], v[4:7], v[60:63]\n\t" \
  "ds_read_b128 v[4:7], v28 offset:3008\n\tv_mfma_f32_16x16x32_f16 v[56:59], v[36:39], v[88:91], v[56:59]\n\tv_mfma_f32_16x16x32_f16 v[60:63], v[36:39], v[92:95], v[60:63]\n\t" \
  "v_mfma_f32_16x16x32_f16 v[64:67], v[36:39], v[96:99], v[64:67]\n\tds_read_b32 v34, v30\n\ts_waitcnt lgkmcnt(5)\n\t" \
  "v_mfma_f32_16x16x32_f16 v[40:43], v[8:11], v[12:15], 0\n\tds_read_b64 v[8:9], v31 offset:544\n\tds_read_b64 v[10:11], v31 offset:712\n\t" \
  "ds_read_b64 v[12:13], v31 offset:512\n\tds_read_b64 v[14:15], v31 offset:680\n\ts_waitcnt lgkmcnt(8)\n\t" \
  "v_mfma_f32_16x16x32_f16 v[56:59], v[48:51], v[16:19], v[56:59]\n\ts_waitcnt lgkmcnt(7)\n\tv_mfma_f32_16x16x32_f16 v[44:47], v[16:19], v[20:23], 0\n\t" \
  "ds_read_b64 v[16:17], v31 offset:480\n\tds_read_b64 v[18:19], v31 offset:648\n\ts_waitcnt lgkmcnt(6)\n\t" \
  "v_cmp_gt_u32 vcc, 4, v34\n\ts_cbranch_vccz 2f\n\ts_mov_b32 s6, 0x10000\n\t" \
  "1:\n\ts_sleep 1\n\tds_read_b32 v34, v30\n\t" \
  "s_sub_u32 s6, s6, 1\n\ts_waitcnt lgkmcnt(0)\n\tv_cmp_gt_u32 vcc, 4, v34\n\t" \
  "s_cbranch_scc1 2f\n\ts_cbranch_vccnz 1b\n\t2:\n\t" \
  "ds_read_b128 v[20:23], v28 offset:3072\n\tv_mfma_f32_16x16x32_f16 v[56:59], v[156:159], v[0:3], v[56:59]\n\tv_mfma_f32_16x16x32_f16 v[60:63], v[124:127], v[0:3], v[60:63]\n\t" \
  "ds_read_b128 v[0:3], v28 offset:3136\n\tv_cvt_pk_f16_f32 v44, v44, v45\n\tv_cvt_pk_f16_f32 v45, v46, v47\n\t" \
  "v_mfma_f32_16x16x32_f16 v[56:59], v[160:163], v[4:7], v[56:59]\n\tv_cvt_pk_f16_f32 v46, v40, v41\n\tv_cvt_pk_f16_f32 v47, v42, v43\n\t" \
  "v_mfma_f32_16x16x32_f16 v[60:63], v[128:131], v[4:7], v[60:63]\n\tds_read_b128 v[4:7], v28 offset:2544\n\ts_waitcnt lgkmcnt(7)\n\t" \
  "v_mfma_f32_16x16x32_f16 v[56:59], v[44:47], v[8:11], v[56:59]\n\tds_read_b128 v[8:11], v28 offset:3200\n\ts_waitcnt lgkmcnt(6)\n\t" \
  "v_mfma_f32_16x16x32_f16 v[60:63], v[44:47], v[12:15], v[60:63]\n\tds_read_b128 v[12:15], v28 offset:3264\n\ts_waitcnt lgkmcnt(5)\n\t" \
  "v_mfma_f32_16x16x32_f16 v[64:67], v[44:47], v[16:19], v[64:67]\n\tds_read_b128 v[16:19], v28 offset:2528\n\ts_waitcnt lgkmcnt(5)\n\t" \
  "v_mfma_f32_16x16x32_f16 v[56:59], v[164:167], v[20:23], v[56:59]\n\tv_mfma_f32_16x16x32_f16 v[60:63], v[132:135], v[20:23], v[60:63]\n\tv_mfma_f32_16x16x32_f16 v[64:67], v[100:103], v[20:23], v[64:67]\n\t" \
  "ds_read_b128 v[20:23], v28 offset:3328\n\ts_waitcnt lgkmcnt(5)\n\tv_mfma_f32_16x16x32_f16 v[60:63], v[136:139], v[0:3], v[60:63]\n\t" \
  "v_mfma_f32_16x16x32_f16 v[64:67], v[104:107], v[0:3], v[64:67]\n\tds_read_b128 v[0:3], v28 offset:3392\n\ts_waitcnt lgkmcnt(5)\n\t" \
  "v_mfma_f32_16x16x32_f16 v[36:39], v[4:7], v[68:71], 0\n\tv_cvt_pk_bf16_f32 v56, v56, v57\n\tv_cvt_pk_bf16_f32 v57, v58, v59\n\t" \
  "s_cmp_lt_u32 %[w], 4\n\ts_cbranch_scc1 1f\n\ts_setprio 0\n\t" \
  "1:\n\tv_mfma_f32_16x16x32_f16 v[40:43], v[4:7], v[72:75], 0\n\tds_write_b64 v29, v[56:57] offset:2560\n\t" \
  "ds_write_b32 v25, v25 offset:320\n\tds_read_b128 v[4:7], v28 offset:1504\n\ts_waitcnt lgkmcnt(7)\n\t" \
  "v_mfma_f32_16x16x32_f16 v[60:63], v[140:143], v[8:11], v[60:63]\n\tv_cvt_pk_f16_f32 v36, v36, v37\n\tv_cvt_pk_f16_f32 v37, v38, v39\n\t" \
  "v_mfma_f32_16x16x32_f16 v[64:67], v[108:111], v[8:11], v[64:67]\n\tds_read_b128 v[8:11], v32 offset:3072\n\tv_cvt_pk_f16_f32 v38, v40, v41\n\t" \
  "v_cvt_pk_f16_f32 v39, v42, v43\n\ts_waitcnt lgkmcnt(7)\n\tv_mfma_f32_16x16x32_f16 v[60:63], v[144:147], v[12:15], v[60:63]\n\t" \
  "v_mfma_f32_16x16x32_f16 v[64:67], v[112:115], v[12:15], v[64:67]\n\tds_read_b128 v[12:15], v28 offset:1520\n\tv_mfma_f32_16x16x32_f16 v[60:63], v[36:39], v[76:79], v[60:63]\n\t" \
  "v_mfma_f32_16x16x32_f16 v[64:67], v[36:39], v[80:83], v[64:67]\n\ts_waitcnt lgkmcnt(7)\n\tv_mfma_f32_16x16x32_f16 v[44:47], v[16:19], v[68:71], 0\n\t" \
  "v_mfma_f32_16x16x32_f16 v[40:43], v[16:19], v[72:75], 0\n\tds_read_b128 v[16:19], v32 offset:2048\n\ts_waitcnt lgkmcnt(7)\n\t" \
  "v_mfma_f32_16x16x32_f16 v[60:63], v[148:151], v[20:23], v[60:63]\n\tv_mfma_f32_16x16x32_f16 v[64:67], v[116:119], v[20:23], v[64:67]\n\tds_read_b128 v[20:23], v28 offset:3456\n\t" \
  "s_waitcnt lgkmcnt(7)\n\tv_mfma_f32_16x16x32_f16 v[60:63], v[152:155], v[0:3], v[60:63]\n\tv_cvt_pk_f16_f32 v44, v44, v45\n\t" \
  "v_cvt_pk_f16_f32 v45, v46, v47\n\tv_cvt_pk_f16_f32 v46, v40, v41\n\tv_cvt_pk_f16_f32 v47, v42, v43\n\t" \
  "v_mfma_f32_16x16x32_f16 v[64:67], v[120:123], v[0:3], v[64:67]\n\tds_read_b128 v[0:3], v28 offset:3520\n\tv_mfma_f32_16x16x32_f16 v[60:63], v[44:47], v[88:91], v[60:63]\n\t" \
  "v_mfma_f32_16x16x32_f16 v[64:67], v[44:47], v[92:95], v[64:67]\n\ts_waitcnt lgkmcnt(4)\n\tv_mfma_f32_16x16x32_f16 v[40:43], v[4:7], v[8:11], 0\n\t" \
  "ds_read_b64 v[4:5], v31 offset:544\n\tds_read_b64 v[6:7], v31 offset:712\n\tds_read_b64 v[8:9], v31 offset:512\n\t" \
  "ds_read_b64 v[10:11], v31 offset:680\n\ts_waitcnt lgkmcnt(7)\n\tv_mfma_f32_16x16x32_f16 v[60:63], v[48:51], v[12:15], v[60:63]\n\t" \
  "s_waitcnt lgkmcnt(6)\n\tv_mfma_f32_16x16x32_f16 v[36:39], v[12:15], v[16:19], 0\n\tds_read_b128 v[12:15], v28 offset:3584\n\t" \
  "ds_read_b128 v[16:19], v28 offset:3648\n\ts_waitcnt lgkmcnt(7)\n\tv_mfma_f32_16x16x32_f16 v[60:63], v[156:159], v[20:23], v[60:63]\n\t" \
  "v_mfma_f32_16x16x32_f16 v[64:67], v[124:127], v[20:23], v[64:67]\n\tds_read_b128 v[20:23], v28 offset:3056\n\ts_waitcnt lgkmcnt(7)\n\t" \
  "v_mfma_f32_16x16x32_f16 v[60:63], v[160:163], v[0:3], v[60:63]\n\tv_cvt_pk_f16_f32 v36, v36, v37\n\tv_cvt_pk_f16_f32 v37, v38, v39\n\t" \
  "v_cvt_pk_f16_f32 v38, v40, v41\n\tv_cvt_pk_f16_f32 v39, v42, v43\n\tv_mfma_f32_16x16x32_f16 v[64:67], v[128:131], v[0:3], v[64:67]\n\t" \
  "ds_read_b128 v[0:3], v28 offset:3712\n\ts_waitcnt lgkmcnt(6)\n\tv_mfma_f32_16x16x32_f16 v[60:63], v[36:39], v[4:7], v[60:63]\n\t" \
  "ds_read_b128 v[4:7], v28 offset:3776\n\ts_waitcnt lgkmcnt(5)\n\tv_mfma_f32_16x16x32_f16 v[64:67], v[36:39], v[8:11], v[64:67]\n\t" \
  "ds_read_b128 v[8:11], v28 offset:3040\n\ts_waitcnt lgkmcnt(5)\n\tv_mfma_f32_16x16x32_f16 v[60:63], v[164:167], v[12:15], v[60:63]\n\t" \
  "v_mfma_f32_16x16x32_f16 v[64:67], v[132:135], v[12:15], v[64:67]\n\tds_read_b128 v[12:15], v28 offset:3840\n\ts_waitcnt lgkmcnt(5)\n\t" \
  "v_mfma_f32_16x16x32_f16 v[64:67], v[136:139], v[16:19], v[64:67]\n\tds_read_b128 v[16:19], v28 offset:3904\n\ts_waitcnt lgkmcnt(5)\n\t" \
  "v_mfma_f32_16x16x32_f16 v[44:47], v[20:23], v[68:71], 0\n\tv_mfma_f32_16x16x32_f16 v[40:43], v[20:23], v[72:75], 0\n\tds_read_b128 v[20:23], v28 offset:2016\n\t" \
  "v_cvt_pk_bf16_f32 v60, v60, v61\n\tv_cvt_pk_bf16_f32 v61, v62, v63\n\ts_waitcnt lgkmcnt(5)\n\t" \
  "v_mfma_f32_16x16x32_f16 v[64:67], v[140:143], v[0:3], v[64:67]\n\tds_write_b64 v29, v[60:61] offset:3072\n\tds_write_b32 v25, v25 offset:384\n\t" \
  "ds_read_b128 v[0:3], v32 offset:3072\n\tv_cvt_pk_f16_f32 v44, v44, v45\n\tv_cvt_pk_f16_f32 v45, v46, v47\n\t" \
  "v_cvt_pk_f16_f32 v46, v40, v41\n\tv_cvt_pk_f16_f32 v47, v42, v43\n\ts_waitcnt lgkmcnt(7)\n\t" \
  "v_mfma_f32_16x16x32_f16 v[64:67], v[144:147], v[4:7], v[64:67]\n\tds_read_b128 v[4:7], v28 offset:2032\n\tv_mfma_f32_16x16x32_f16 v[64:67], v[44:47], v[76:79], v[64:67]\n\t" \
  "s_waitcnt lgkmcnt(7)\n\tv_mfma_f32_16x16x32_f16 v[36:39], v[8:11], v[68:71], 0\n\tv_mfma_f32_16x16x32_f16 v[40:43], v[8:11], v[72:75], 0\n\t" \
  "ds_read_b128 v[8:11], v32 offset:2048\n\ts_waitcnt lgkmcnt(7)\n\tv_mfma_f32_16x16x32_f16 v[64:67], v[148:151], v[12:15], v[64:67]\n\t" \
  "ds_read_b128 v[12:15], v28 offset:3968\n\ts_waitcnt lgkmcnt(7)\n\tv_mfma_f32_16x16x32_f16 v[64:67], v[152:155], v[16:19], v[64:67]\n\t" \
  "ds_read_b128 v[16:19], v28 offset:4032\n\tv_cvt_pk_f16_f32 v36, v36, v37\n\tv_cvt_pk_f16_f32 v37, v38, v39\n\t" \
  "v_cvt_pk_f16_f32 v38, v40, v41\n\tv_cvt_pk_f16_f32 v39, v42, v43\n\ts_nop 1\n\t" \
  "v_mfma_f32_16x16x32_f16 v[64:67], v[36:39], v[88:91], v[64:67]\n\ts_waitcnt lgkmcnt(4)\n\tv_mfma_f32_16x16x32_f16 v[40:43], v[20:23], v[0:3], 0\n\t" \
  "ds_read_b64 v[0:1], v31 offset:544\n\tds_read_b64 v[2:3], v31 offset:712\n\tds_read_b128 v[20:23], v28 offset:4096\n\t" \
  "s_waitcnt lgkmcnt(6)\n\tv_mfma_f32_16x16x32_f16 v[64:67], v[48:51], v[4:7], v[64:67]\n\ts_waitcnt lgkmcnt(5)\n\t" \
  "v_mfma_f32_16x16x32_f16 v[44:47], v[4:7], v[8:11], 0\n\ts_waitcnt lgkmcnt(4)\n\tv_mfma_f32_16x16x32_f16 v[64:67], v[156:159], v[12:15], v[64:67]\n\t" \
  "s_waitcnt lgkmcnt(3)\n\tv_mfma_f32_16x16x32_f16 v[64:67], v[160:163], v[16:19], v[64:67]\n\ts_nop 3\n\t" \
  "v_cvt_pk_f16_f32 v44, v44, v45\n\tv_cvt_pk_f16_f32 v45, v46, v47\n\tv_cvt_pk_f16_f32 v46, v40, v41\n\t" \
  "v_cvt_pk_f16_f32 v47, v42, v43\n\ts_waitcnt lgkmcnt(1)\n\ts_nop 0\n\t" \
  "v_mfma_f32_16x16x32_f16 v[64:67], v[44:47], v[0:3], v[64:67]\n\ts_waitcnt lgkmcnt(0)\n\tv_mfma_f32_16x16x32_f16 v[64:67], v[164:167], v[20:23], v[64:67]\n\t" \
  "s_nop 7\n\tv_cvt_pk_bf16_f32 v64, v64, v65\n\tv_cvt_pk_bf16_f32 v65, v66, v67\n\t" \
  "ds_write_b64 v29, v[64:65] offset:3584\n\tds_write_b32 v25, v25 offset:448\n\t"
#define CONV_CLOBBER "v0", "v1", "v2", "v3", "v4", "v5", "v6", "v7", "v8", "v9", "v10", "v11", "v12", "v13", "v14", "v15", "v16", "v17", "v18", "v19", "v20", "v21", "v22", "v23", "v36", "v37", "v38", "v39", "v40", "v41", "v42", "v43", "v44", "v45", "v46", "v47", "v52", "v53", "v54", "v55", "v56", "v57", "v58", "v59", "v60", "v61", "v62", "v63", "v64", "v65", "v66", "v67", "v68", "v69", "v70", "v71", "v72", "v73", "v74", "v75", "v76", "v77", "v78", "v79", "v80", "v81", "v82", "v83", "v84", "v85", "v86", "v87", "v88", "v89", "v90", "v91", "v92", "v93", "v94", "v95", "v96", "v97", "v98", "v99", "v100", "v101", "v102", "v103", "v104", "v105", "v106", "v107", "v108", "v109", "v110", "v111", "v112", "v113", "v114", "v115", "v116", "v117", "v118", "v119", "v120", "v121", "v122", "v123", "v124", "v125", "v126", "v127", "v128", "v129", "v130", "v131", "v132", "v133", "v134", "v135", "v136", "v137", "v138", "v139", "v140", "v141", "v142", "v143", "v144", "v145", "v146", "v147", "v148", "v149", "v150", "v151", "v152", "v153", "v154", "v155", "v156", "v157", "v158", "v159", "v160", "v161", "v162", "v163", "v164", "v165", "v166", "v167"
#define G_CLOBBER "v8", "v9", "v10", "v11", "v12", "v13", "v14", "v15", "v16", "v17", "v18", "v19", "v20", "v21", "v22", "v23", "v24", "v25", "v26", "v27", "v28", "v29", "v30", "v32", "v33", "v34", "v35", "v36", "v37", "v38", "v39", "v40", "v41", "v42", "v43", "v44", "v45", "v46", "v47", "v48", "v49", "v50", "v51", "v52", "v53", "v54", "v55", "v56", "v57", "v58", "v59", "v60", "v61", "v62", "v63", "v64", "v65", "v66", "v67", "v68", "v69", "v70", "v71", "v72", "v73", "v74", "v75", "v76", "v77", "v78", "v79", "v80", "v81", "v82", "v83", "v84", "v85", "v86", "v87", "v88", "v89", "v90", "v91", "v92", "v93", "v94", "v95", "v96", "v97", "v98", "v99", "v100", "v101", "v102", "v103", "v104", "v105", "v106", "v107", "v108", "v109", "v110", "v111", "v112", "v113", "v114", "v115", "v116", "v117", "v118", "v119", "v120", "v121", "v122", "v123", "v124", "v125", "v126", "v127"
#define D_CLOBBER "v8", "v9", "v10", "v11", "v12", "v13", "v14", "v15", "v16", "v17", "v18", "v19", "v20", "v21", "v22", "v23", "v24", "v25", "v26", "v27", "v28", "v29", "v30", "v31"
__device__ __forceinline__ f32x2 pkfma(f32x2 a, f32x2 b, f32x2 c) {
  f32x2 r;
  asm("v_pk_fma_f32 %0, %1, %2, %3" : "=v"(r) : "v"(a), "v"(b), "v"(c));
  return r;
}
__device__ __forceinline__ float fmav(float a, float b, float c) {
  float r;
  asm("v_fma_f32 %0, %1, %2, %3" : "=v"(r) : "v"(a), "v"(b), "v"(c));
  return r;
}
__device__ __forceinline__ float fmac(float a, float b, float c) {
  asm("v_fmac_f32 %0, %1, %2" : "+v"(c) : "v"(a), "v"(b));
  return c;
}
__device__ __forceinline__ float fmas(float a, u32 s, float c) {
  asm("v_fmac_f32 %0, %2, %1" : "+v"(c) : "v"(a), "s"(s));
  return c;
}
template <int HI>
__device__ __forceinline__ f32x2 pkfmas(f32x2 a, f32x2 s, f32x2 c) {
  f32x2 r;
  if (HI) asm("v_pk_fma_f32 %0, %2, %1, %3 op_sel:[1,0,0] op_sel_hi:[1,1,1]" : "=v"(r) : "v"(a), "v"(s), "v"(c));
  else asm("v_pk_fma_f32 %0, %2, %1, %3 op_sel:[0,0,0] op_sel_hi:[0,1,1]" : "=v"(r) : "v"(a), "v"(s), "v"(c));
  return r;
}
#define RANK8(d0, d1, d2, d3, c0, c1, i0, f0, f1) "v_perm_b32 " d0 ", " d0 ", " d1 ", %[sg]\n\tv_perm_b32 " d2 ", " d2 ", " d3 ", %[sg]\n\t" \
  "v_bcnt_u32_b32 " d0 ", " d0 ", " i0 "\n\tv_bcnt_u32_b32 " c0 ", " d2 ", " d0 "\n\tv_bcnt_u32_b32 " c1 ", " d2 ", " d0 "\n\t" f0 "\n\t" \
  "v_permlane16_swap_b32 " c0 ", " c1 "\n\tv_add_u32 " d0 ", " c0 ", " c1 "\n\tv_add_u32 " d1 ", " c0 ", " c1 "\n\t" f1 "\n\t" \
  "v_permlane32_swap_b32 " d0 ", " d1 "\n\tv_add_u32 " c0 ", " d0 ", " d1 "\n\t"
__device__ __forceinline__ void ln_rows(u32x4 (&xv)[4], u32x8 wv, rsrc_t rs, u32& tag, int b, int w, int l, u32 st, u32 sg TLARG) {
  float s[2][3] = {}, c[2], sw = 0.f;
  u32 os = l < 16 ? O_SG + 128 * (b - 128) + 8 * l : 0x80000000u;
  u32 ot = l < 16 ? O_TOP + 8 * (b & 15) + TL * ((b >> 4) & 7) : l < 32 ? O_RG - 128 + 128 * (b - 128) + 8 * l : 0x80000000u, om = l < 16 ? TOPS / 8 : 0u, fm = l < 16 ? 0xff0u : 0u;
  u32 aw = (l & 16) ? st + 8 * w + (l >> 5) * 4 : st + 136, ac = st + 128, ar = st + 4 * (l & 15), aq = st + (l & 48), one = 1u;
  u32 kl = (2047 - (16 * (b - 128) + 2 * w + (l >> 5))) << 1;
  asm volatile("" : "+v"(os), "+v"(aw), "+v"(ac), "+v"(ar), "+v"(aq), "+v"(one), "+v"(kl), "+v"(ot), "+v"(om), "+v"(fm));
  SB();
  asm volatile("s_waitcnt vmcnt(4) lgkmcnt(0)" : "+{s44}"(tag)::"memory");
  asm volatile("" : "+{v[116:123]}"(wv));
  tag += 1u;
  *(volatile LDS u32*)(size_t)(st - 104) = tag;
  STAMP(12);
#pragma unroll
  for (int i = 0; i < 8; i++) sw = dot2b(wv[i], 0x3f803f80u, sw);
  sw += dppf<0x121, 0xf>(sw); sw += dppf<0x122, 0xf>(sw); sw += dppf<0x124, 0xf>(sw); sw += dppf<0x128, 0xf>(sw);
  sw = (rdl(sw, 0) + rdl(sw, 16)) + (rdl(sw, 32) + rdl(sw, 48));
  asm volatile("" : "+v"(sw));
  SB();
  float cv = 0.f;
#pragma unroll
  for (int r = 0; r < 2; r++) {
#pragma unroll
    for (int i = 0; i < 8; i++) {
      if (r == 0 && i == 0) { asm volatile("s_waitcnt vmcnt(3)" : "+{v[100:103]}"(xv[0])); STAMP(13); }
      if (r == 0 && i == 4) asm volatile("s_waitcnt vmcnt(2)" : "+{v[104:107]}"(xv[1]));
      if (r == 1 && i == 0) asm volatile("s_waitcnt vmcnt(1)" : "+{v[108:111]}"(xv[2]));
      if (r == 1 && i == 4) asm volatile("s_waitcnt vmcnt(0)" : "+{v[112:115]}"(xv[3]));
      const u32 a = xv[2 * r + (i >> 2)][i & 3];
      s[r][0] = dot2b(a, 0x3f803f80u, s[r][0]); s[r][1] = dot2b(a, a, s[r][1]); s[r][2] = dot2b(a, wv[i], s[r][2]);
      if (i == 3) {
        c[r] = rdl(s[r][0] * 0.125f, 0);
        if (r) { cv = l < 32 ? c[0] : c[1]; asm volatile("" : "+v"(cv)); }
        SB();
      }
    }
    const float ml = s[r][0] * 0.0625f, dl = __builtin_fmaf(s[r][0], 0.25f, -4.f * c[r]);
    s[r][1] = __builtin_fmaf(dl, dl, __builtin_fmaf(-s[r][0], ml, s[r][1]));
    SB();
  }
#pragma unroll
  for (int k = 0; k < 3; k++) swap32(s[0][k ? 3 - k : 0], s[1][k ? 3 - k : 0]);
  float q[3];
#pragma unroll
  for (int k = 0; k < 3; k++) q[k] = s[0][k] + s[1][k];
#define RED3(m) "v_add_f32_dpp %0, %0, %0 " m " bank_mask:0xf\n\tv_add_f32_dpp %1, %1, %1 " m " bank_mask:0xf\n\tv_add_f32_dpp %2, %2, %2 " m " bank_mask:0xf\n\t"
  asm volatile("s_nop 1\n\t" RED3("row_ror:1 row_mask:0xf") RED3("row_ror:2 row_mask:0xf") RED3("row_ror:4 row_mask:0xf") RED3("row_ror:8 row_mask:0xf")
               RED3("row_bcast:15 row_mask:0xa") : "+v"(q[0]), "+v"(q[1]), "+v"(q[2]));
  const float mu = q[0] * (1.f / D), dv = mu - cv;
  const float var = fmaxf(__builtin_fmaf(-dv, dv, q[1] * (1.f / D)), 0.f);
  const float m = rb(mu);
  float sd = __builtin_amdgcn_sqrtf(rb(rb(var) + 1e-5f)), num = __builtin_fmaf(-m, sw, q[2]);
  asm volatile("s_nop 0\n\tv_cvt_pk_bf16_f32 %0, 0, %0" : "+v"(sd), "+v"(num));
  u32 rv = __float_as_uint(rb(num * __builtin_amdgcn_rcpf(sd)));
  rv ^= (u32)((int)rv >> 15) | 0x80000000u;
  rv = __builtin_amdgcn_perm(rv, kl, 0x07060100u) >> 1;
  asm volatile("ds_write2_b32 %[aw], %[sv], %[rv] offset1:16\n\ts_mov_b64 exec, 1\n\tds_add_rtn_u32 v104, %[ac], %[one]\n\ts_mov_b64 exec, -1\n\t"
               "ds_read_b32 v106, %[ar]\n\tds_read_b32 v105, %[ar] offset:64\n\tds_read_b128 v[100:103], %[aq] offset:64\n\tv_mov_b32 v107, %[t]\n\tv_mov_b32 v109, %[t]\n\t"
               "s_waitcnt lgkmcnt(3)\n\tv_cmp_eq_u32 vcc, 7, v104\n\ts_bitcmp1_b32 vcc_lo, 0\n\ts_cbranch_scc0 1f\n\t"
               "s_waitcnt lgkmcnt(2)\n\tbuffer_store_dwordx2 v[106:107], %[os], %[rs], 0 offen sc1\n\ts_waitcnt lgkmcnt(0)\n\t"
               "v_sub_u32 v100, v105, v100\n\tv_sub_u32 v101, v105, v101\n\tv_sub_u32 v102, v105, v102\n\tv_sub_u32 v103, v105, v103\n\t"
               RANK8("v100", "v101", "v102", "v103", "v104", "v103", "0", "s_nop 1", "s_nop 1") "v_cmp_eq_u32 vcc, 16, v104\n\t"
               "v_sub_u32 v108, 19, v104\n\ts_ff1_i32_b32 s6, vcc_lo\n\tv_readlane_b32 s6, v105, s6\n\t"
               "v_mad_u32_u24 v104, v104, %[om], %[ot]\n\ts_nop 0\n\tv_lshrrev_b32 v108, v108, s6\n\tv_bfi_b32 v108, %[fm], v108, v105\n\t"
               "buffer_store_dwordx2 v[108:109], v104, %[rs], 0 offen sc1\n1:\n\ts_waitcnt lgkmcnt(0)"
               :: [aw] "v"(aw), [sv] "v"(__builtin_amdgcn_perm(__float_as_uint(sd), __float_as_uint(m), 0x07060302u)), [rv] "v"(rv), [ac] "v"(ac), [one] "v"(one),
                  [ar] "v"(ar), [aq] "v"(aq), [os] "v"(os), [om] "v"(om), [ot] "v"(ot), [fm] "v"(fm), [sg] "s"(sg), [t] "s"(tag), [rs] "s"(rs)
               : "vcc", "scc", "s6", "v100", "v101", "v102", "v103", "v104", "v105", "v106", "v107", "v108", "v109", "memory");
  SB();
}

#define GL __attribute__((address_space(1)))
typedef const GL u16* gp;

__global__ __launch_bounds__(768, 3) void fused(const u16*, const u16*, const u16*, const u16*, const u16*, const u16*, const u16*, const u16*,
    const u16*, const u16*, const u16*, u16*, char*) {
  __shared__ __attribute__((aligned(256))) char smem[SMEM];
  const u32 lds0 = (u32)(size_t)(LDS char*)smem;
  int w0;
#define GLD(r0, r1, r2, r3, o_) "global_load_dwordx4 v[" #r0 ":" #r1 "], " #o_ ", s[16:17]\n\tglobal_load_dwordx4 v[" #r2 ":" #r3 "], " #o_ ", s[16:17] offset:2048\n\t"
  asm volatile(
      "s_load_dwordx16 s[16:31], s[0:1], 0\n\ts_load_dwordx8 s[32:39], s[0:1], 64\n\ts_load_dwordx2 s[40:41], s[0:1], 96\n\t"
      "s_getpc_b64 s[4:5]\n2:\n\ts_lshr_b32 s7, s2, 3\n\ts_and_b32 s10, s4, 0xffffff00\n\ts_mov_b32 s11, s5\n\ts_mul_i32 s8, s7, 0x600\n\t"
      "v_lshl_add_u32 v3, v0, 1, s8\n\ts_cmpk_lt_u32 s2, %[pn]\n\ts_cbranch_scc0 1f\n\t"
      "global_load_ushort v163, v3, s[10:11] offset:%[pf]\n"
      "1:\n\ts_cmpk_lt_u32 s2, 0x80\n\tv_readfirstlane_b32 s3, v0\n\tv_and_b32 v1, 63, v0\n\tv_mov_b64 v[4:5], 0\n\tv_mov_b64 v[6:7], 0\n\tv_lshlrev_b32 v2, 4, v1\n\tv_lshlrev_b32 v8, 2, v0\n\t"
      "s_mov_b32 s42, %[we]\n\ts_mov_b32 s43, 0x20000\n\ts_cbranch_scc0 3f\n\t"
      "s_lshr_b32 s3, s3, 6\n\ts_mul_i32 s9, s3, %[xr]\n\ts_min_u32 s9, s9, %[zm]\n\tv_add_u32 v3, s9, v2\n\tv_add_u32 v3, %[zo], v3\n\t"
      "s_and_b32 s7, s7, 7\n\ts_and_b32 s6, s2, 7\n\ts_and_b32 s8, s2, 64\n\ts_lshl3_add_u32 s8, s6, s8\n\ts_add_u32 s45, s8, s7\n\t"
      "s_add_u32 s4, s4, lane_tab@rel32@lo+(6f-2b-4)\n6:\n\ts_addc_u32 s5, s5, lane_tab@rel32@hi+(6b-2b+4)\n\t"
      "ds_write_b128 v3, v[4:7]\n\ts_cmp_lt_u32 s3, 8\n\ts_cbranch_scc1 5f\n\t"
      "s_lshl_b32 s8, s45, 4\n\ts_lshl_b32 s9, s3, 18\n\ts_add_u32 s9, s9, s8\n\ts_bitset0_b32 s9, 21\n\tv_lshl_add_u32 v148, v1, 12, s9\n\t"
      "s_lshl3_add_u32 s9, s3, s7\n\ts_lshl_b32 s9, s9, 17\n\ts_add_u32 s9, s9, s8\n\ts_bitset0_b32 s9, 23\n\tv_lshl_add_u32 v9, v1, 11, s9\n\t"
      "v_add_u32 v11, 0x100000, v148\n\tv_add_u32 v12, 0x200000, v148\n\t"
      "v_lshlrev_b32 v149, 4, v0\n\ts_lshl_b32 s9, s3, 10\n\ts_add_u32 m0, s9, %[ob]\n\tv_add_u32 v10, 0xffffe800, v149\n\tglobal_load_lds_dwordx4 v10, s[4:5]\n\t"
      "v_add_u32 v149, %[sg], v149\n\t"
      "s_waitcnt lgkmcnt(0)\n\t" KDL "s_load_dword s44, s[40:41], 0\n\ts_memrealtime s[46:47]\n\t"
      "s_barrier\n\t"
      "global_load_dword v160, v9, s[16:17]" PRENT "\n\t"
      GLD(100, 103, 104, 107, v148)
      GLD(108, 111, 112, 115, v11)
      "s_cmp_eq_u32 s6, 3\n\ts_cbranch_scc1 .Lrole_p\n\t"
      GLD(116, 119, 120, 123, v12)
      "s_branch .Lrole_p\n"
      "3:\n\ts_lshr_b32 s3, s3, 6\n\ts_cmp_lt_u32 s3, 8\n\ts_cbranch_scc1 4f\n\ts_waitcnt lgkmcnt(0)\n\t" KDL "s_memrealtime s[46:47]\n\ts_barrier\n\ts_branch .Lrole_d\n4:\n\t"
      "v_add_u32 v3, %[fc], v2\n\ts_add_u32 s8, s3, s2\n\ts_and_b32 s45, s8, 7\n\ts_lshl3_add_u32 s8, s2, s45\n\t"
      "s_lshl_b32 s8, s8, 12\n\ts_bitset0_b32 s8, 22\n\tds_write_b128 v3, v[4:7]\n\tv_add_u32 v3, s8, v2\n\t"
      "s_waitcnt lgkmcnt(0)\n\t" KDL "global_load_dwordx4 v[116:119], v2, s[30:31]\n\tglobal_load_dwordx4 v[120:123], v2, s[30:31] offset:1024\n\t"
      "s_load_dword s44, s[40:41], 0\n\ts_memrealtime s[46:47]\n\ts_barrier\n\t"
      "global_load_dwordx4 v[100:103], v3, s[16:17]" LNNT "\n\tglobal_load_dwordx4 v[104:107], v3, s[16:17] offset:1024" LNNT "\n\t"
      "global_load_dwordx4 v[108:111], v3, s[16:17] offset:2048" LNNT "\n\tglobal_load_dwordx4 v[112:115], v3, s[16:17] offset:3072" LNNT "\n\t"
      "s_branch .Lrole_l\n"
      "5:\n\t"
      "global_load_dwordx4 v[16:19], v2, s[4:5]\n\tglobal_load_dwordx4 v[20:23], v2, s[4:5] offset:1024\n\t"
      "s_lshl3_add_u32 s8, s45, s3\n\ts_lshl_b32 s9, s8, 1\n\tv_mov_b32 v9, s9\n\ts_lshl_b32 s9, s45, 11\n\tv_add_u32 v3, s9, v8\n\t"
      "s_waitcnt lgkmcnt(0)\n\t" KDL "global_load_short_d16_hi v24, v9, s[26:27]\n\tglobal_load_ushort v160, v9, s[28:29]\n\t"
      "global_load_dword v96, v3, s[18:19]" PNT "\n\tglobal_load_dword v97, v3, s[20:21]" PNT "\n\t"
      "global_load_dword v68, v3, s[22:23]" PNT "\n\tglobal_load_dword v69, v3, s[24:25]" PNT "\n\ts_barrier\n\ts_branch .Lrole_c"
      : "={s3}"(w0)
      : "{s[0:1]}"(__builtin_amdgcn_kernarg_segment_ptr()), "{s2}"(blockIdx.x), "{v0}"(threadIdx.x), [pf] "n"(PFO), [pn] "n"(8 * PFN), [we] "n"(WS_END), [xr] "n"(XREG),
        [zm] "n"(8 * XREG), [zo] "n"(OX - 16), [sg] "n"(O_SG - 8192), [fc] "n"(FC), [ob] "n"(OB - 8192)
      : "memory");
#define TLREGS()
#define ROLE(name, text, ...) \
  gp x, wg, wu, wd; GL u16* out; rsrc_t rs; int b, w, t, l, g; u32 tag, st0; \
  asm volatile(".Lrole_" #name ":" : "={v0}"(t), "={v1}"(l) __VA_ARGS__ :: "memory"); \
  asm volatile(text : "={s2}"(b), "={s3}"(w), "={s[16:17]}"(x), "={s[32:33]}"(wg), "={s[34:35]}"(wu), "={s[36:37]}"(wd), \
               "={s[38:39]}"(out), "={s[40:43]}"(rs), "={s44}"(tag), "={s45}"(g), "={s46}"(st0) :: "memory"); \
  SB(); \
  __builtin_assume((u32)l < 64u); __builtin_assume((u32)t < 768u); __builtin_assume((u32)w < 12u); __builtin_assume((u32)b < 256u); __builtin_assume((u32)g < 128u); \
  TLREGS(); \
  const int xk = b & 7, j = (b >> 3) & 15, n16 = l & 15, kg = l >> 4
#define TAGWAIT() do { asm volatile("s_waitcnt lgkmcnt(0)" : "+{s44}"(tag)::"memory"); tag += 1u; } while (0)
#define BAR() do { SB(); asm volatile("s_barrier" ::: "memory"); SB(); } while (0)
#define FLAG(o, v) do { asm volatile("" ::: "memory"); SB(); *(volatile LDS u32*)(LDS char*)(smem + (o)) = (v); SB(); } while (0)
#define ARRIVE(o) do { SB(); if (l == 0) asm volatile("ds_add_u32 %0, %1" ::"v"(lds0 + (o)), "v"(1u) : "memory"); SB(); } while (0)
#define CWAIT(o, n, nap) do { _Pragma("unroll 1") for (int spin = 0; spin < SPIN; spin++) { \
      if (__builtin_amdgcn_readfirstlane((int)*(const volatile LDS u32*)(LDS char*)(smem + (o))) >= (int)(n)) break; \
      asm volatile(nap ::: "memory"); } SB(); asm volatile("" ::: "memory"); } while (0)
#define AWAIT(o, n, nap) do { _Pragma("unroll 1") for (int spin = 0; spin < SPIN; spin++) { \
      if (__all(*(const volatile LDS u32*)(LDS char*)(smem + (o)) >= (u32)(n))) break; \
      asm volatile(nap ::: "memory"); } SB(); asm volatile("" ::: "memory"); } while (0)
#define POLL8(res_, off_) do { u32 n_ = SPIN; \
    asm volatile("1:\n\tbuffer_load_dwordx2 v[4:5], %2, %3, 0 offen sc1\n\ts_sub_u32 %1, %1, 1\n\ts_waitcnt vmcnt(0)\n\t" \
                 "v_cmp_ne_u32 vcc, %4, v5\n\ts_cbranch_scc1 2f\n\ts_cbranch_vccnz 1b\n2:" \
                 : "=&{v[4:5]}"(res_), "+s"(n_) : "v"(off_), "s"(rs), "s"(tag) : "vcc", "scc", "memory"); } while (0)
#define UNTIL(t_) "2:\n\ts_sleep 1\n\ts_memrealtime s[8:9]\n\ts_waitcnt lgkmcnt(0)\n\ts_sub_u32 s8, s8, %[st]\n\ts_cmp_lt_u32 s8, " t_ "\n\ts_cbranch_scc1 2b\n\t"
#define LANE(v_) int v_; asm volatile("v_mbcnt_lo_u32_b32 %0, -1, 0\n\tv_mbcnt_hi_u32_b32 %0, -1, %0" : "=v"(v_))
#define IXO(l_) (O_IDX + (2 * xk + (j & 1)) * 128 + ((l_) & 7) * 8)
#define IDS() const int id = 16 * xk + j, sb = id * 57 >> 10, mat = id & 1, u = (id - 18 * sb) >> 1; \
    const bool slow = u >= 6; \
    const int part = slow ? u - 6 : u & 1, cb = 4 * sb + (slow ? 1 : (u >> 1) + (u > 1)), r0 = part * (slow ? 320 : 512), ns = slow ? 5 + (u >> 3) : 8; \
    const int kp = 4 * sb + ((u >> 1) & 3), ct = 2 * (u & 1) + mat
#define TS(a, b_)
#define TSC
#define DONE "s_endpgm\n"
#define TSD
#define TSGET(i0, i1, i2, i3)
  if (blockIdx.x >= 128) {
    if (w0 >= 8) {
      ROLE(d, "s_waitcnt lgkmcnt(0)");
      IDS();
      const int dw = w - 8;
      const u32 sw = (((n16 >> 1) ^ (kg & 1)) << 5) + ((l & 1) << 4);
      gp base = (mat ? wu : wg) + (size_t)(r0 + 4 * dw) * F + cb * 128;
      u32 v = kg * (2 * F) + sw, fa = lds0 + FC + 4 * dw, vp = 96 + dw, t_;
      asm volatile("" : "+v"(v), "+v"(fa), "+v"(vp));
      asm volatile("s_mov_b32 m0, %0" ::"s"(lds0 + 1024 * dw));
#define VMC "s_getreg_b32 s6, hwreg(HW_REG_IB_STS, 0, 4)\n\t"
#define RUN(k, q, lim, v_, b_, st_) asm volatile("1:\n\ts_lshr_b32 s6, m0, 4\n\ts_and_b32 s6, s6, 0xc0\n\tv_xor_b32 %[t], s6, %[v]\n\ts_waitcnt vmcnt(%[c])\n\t" VMC \
        "global_load_lds_dwordx4 %[t], %[b] nt\n\ts_add_u32 m0, m0, %[m]\n\tv_add_u32 %[v], %[d], %[v]\n\t.if %[m] == 3072\n\tv_mad_i32_i24 %[t], s6, -3, %[p]\n\tv_add_u32 %[p], 3, %[p]\n\t" \
        "s_mov_b64 exec, 1\n\tds_max_u32 %[a], %[t]\n\ts_mov_b64 exec, -1\n\t.endif\n\ts_cmp_lt_u32 m0, %[l]\n\ts_cbranch_scc1 1b" \
        : [v] "+v"(v_), [p] "+v"(vp), [t] "=&v"(t_) : [b] "s"(b_), [c] "n"((q) - 1), [m] "n"(1024 * (k)), [d] "n"((k) * (st_)), \
          [a] "v"(fa), [l] "s"(lim) : "scc", "s6", "memory")
#define MARK(c, val) "s_waitcnt vmcnt(" #c ")\n\t" val "\n\ts_mov_b64 exec, 1\n\tds_max_u32 %[a], %[t]\n\ts_mov_b64 exec, -1\n\t"
      __builtin_amdgcn_s_setprio(DPRIO);
      STAMP(8);
      if (id < 126) { if (slow) RUN(4, QAS, 32768, v, base, 8 * F); else RUN(4, 8, 32768, v, base, 8 * F); }
      if (dw < 3) {
        if (id >= 126) __builtin_amdgcn_endpgm();
        gp bd = wd + (size_t)(128 * kp + 2 * dw) * D + ct * 256;
        u32 vd = (l >> 5) * (2 * D + 32) ^ 16 * (l & 31);
        asm volatile("" : "+v"(vd));
        if (slow) RUN(3, QS, 16384 * ns, v, base, 8 * F);
        else { RUN(3, 8, 32768 + 3072 * RAMP, v, base, 8 * F); RUN(3, QD, 131072, v, base, 8 * F); }
        STAMP(1);
        {
          u32 n_ = SPIN;
          asm volatile("1:\n\t" VMC MARK(63, "v_mad_i32_i24 %[t], s6, -3, %[p]") "ds_read_b32 %[t], %[f]\n\ts_sub_u32 %[n], %[n], 1\n\ts_waitcnt lgkmcnt(0)\n\t"
                       "v_cmp_lt_u32 vcc, %[k], %[t]\n\ts_cbranch_scc1 2f\n\ts_cbranch_vccz 1b\n2:\n\ts_mov_b32 m0, %[m]"
                       : [t] "=&v"(t_), [n] "+s"(n_) : [a] "v"(fa), [p] "v"(vp), [f] "v"(u == 8 ? fa : lds0 + FC + 20), [k] "s"(u == 8 ? 159 : 16 * HOLD - 105), [m] "s"(lds0 + 1024 * dw)
                       : "vcc", "scc", "s6", "memory");
        }
        STAMP(3);
        if (u < 8) {
          RUN(3, QE, 65536, vd, bd, 4 * D);
          STAMP(6);
          asm volatile(MARK(0, "v_mov_b32 %[t], -1") : [t] "=&v"(t_) : [a] "v"(fa) : "memory");
          STAMP(4);
        }
        FLUSH();
        __builtin_amdgcn_endpgm();
      }
      asm volatile(MARK(0, "v_mov_b32 %[t], -1") : [t] "=&v"(t_) : [a] "v"(fa) : "memory");
      u32 km, rk;
      u32 hi, lo, k2, k3, gd, thr, sg = 0x0c0c0b09u;
      u32 ka = lds0 + FC + 384 + 8 * (l >> 3), kc = 2047 * 2048 + 2 * r0 + 16 * ((l & 7) ^ ((l >> 2) & 4)), kq = lds0 + FC + 512 + (l & 48), kr = lds0 + FC + 512 + 4 * n16;
      u32 c1 = 896, c2 = 1792, c3 = 2688, cm = (u32)-2048, cs = O_SG + 8 * 2047, ca = lds0 + FC + 512;
      rsrc_t rx = __builtin_amdgcn_make_buffer_rsrc((void*)(x - 1344), 0, 2 * L * D + 4096, 0x00020000);
      u32 co = O_CLR + 8 * (18 * sb + (0x84c0 >> 4 * (kp & 3) & 15) + min(l, (kp & 3) == 1 ? 5 : 3));
      asm volatile("" : "+v"(ka), "+v"(kc), "+v"(kq), "+v"(kr), "+v"(co));
      asm volatile("" : "+s"(c1), "+s"(c2), "+s"(c3), "+s"(cm), "+s"(cs), "+s"(ca), "+s"(sg), "+s"(rx));
      *(u32x4*)(smem + FA + 6144 + 16 * l) = u32x4{0u, 0u, 0u, 0u};
      *(u32x4*)(smem + FA + 7168 + 16 * l) = u32x4{0u, 0u, 0u, 0u};
      CWAIT(FC + 256, 1, "s_sleep 4");
      tag = (u32)__builtin_amdgcn_readfirstlane((int)*(const volatile LDS u32*)(LDS char*)(smem + FC + 24));
      {
        u32 n_ = SPIN, t0, t1, t2, t3, se;
#define TLX(a, b_, c, d, sc) "buffer_load_dwordx4 v[" #a ":" #b_ "], %[o], %[r], 0 offen" sc "\n\tbuffer_load_dwordx4 v[" #c ":" #d "], %[o], %[r], %[ts] offen" sc "\n\t"
#define TLD(a, b_, c, d) TLX(a, b_, c, d, " sc1")
#define TCK(r0, r1, r2, r3, r4, r5, r6, r7) "s_waitcnt vmcnt(4)\n\tv_xor_b32 %[t0], %[t], v" #r1 "\n\tv_bitop3_b32 %[t0], %[t0], %[t], v" #r3 " bitop3:0xf6\n\t" \
          "v_bitop3_b32 %[t0], %[t0], %[t], v" #r5 " bitop3:0xf6\n\tv_bitop3_b32 %[t0], %[t0], %[t], v" #r7 " bitop3:0xf6\n\t" \
          "v_cmp_ne_u32 vcc, 0, %[t0]\n\tv_mov_b32 %[hi], v" #r0 "\n\tv_mov_b32 %[lo], v" #r2 "\n\tv_mov_b32 %[k2], v" #r4 "\n\tv_mov_b32 %[k3], v" #r6 "\n\t" \
          "s_cbranch_vccz 5f\n\t" TLD(r0, r3, r4, r7)
        asm volatile("2:\n\ts_sleep 1\n\ts_memrealtime s[8:9]\n\ts_waitcnt lgkmcnt(0)\n\ts_sub_u32 s8, s8, %[st]\n\ts_cmp_lt_u32 s8, %[tw]\n\ts_cbranch_scc1 2b\n\t"
                     TLX(128, 131, 132, 135, SCT) "s_sleep " STR(RSL) "\n\t" TLD(136, 139, 140, 143) "s_sleep " STR(RSL) "\n\t" TLX(144, 147, 148, 151, SCU)
                     "1:\n\t" TCK(128, 129, 130, 131, 132, 133, 134, 135) TCK(136, 137, 138, 139, 140, 141, 142, 143) TCK(144, 145, 146, 147, 148, 149, 150, 151)
                     "s_sub_u32 %[n], %[n], 1\n\ts_cbranch_scc0 1b\n5:\n\t" TS(60, 61)
                     "v_and_or_b32 %[t0], %[hi], %[mk], %[pa]\n\tv_and_or_b32 %[t1], %[lo], %[mk], %[pb]\n\t"
                     "v_max_u32 %[t2], %[t0], %[t1]\n\tv_max_u32 %[t3], %[t0], %[t1]\n\tv_perm_b32 %[gd], %[hi], %[lo], %[p0]\n\tv_perm_b32 %[se], %[k2], %[k3], %[p0]\n\t"
                     "v_permlane16_swap_b32 %[t2], %[t3]\n\tv_max_u32 %[km], %[t2], %[t3]\n\tv_max_u32 %[rk], %[t2], %[t3]\n\t"
                     "v_max_u32 %[hi], %[t0], %[t1]\n\tv_min_u32 %[lo], %[t0], %[t1]\n\t"
                     "v_permlane32_swap_b32 %[km], %[rk]\n\tv_max_u32 %[km], %[km], %[rk]\n\tv_not_b32 %[t2], %[km]\n\t"
                     "v_and_or_b32 %[k2], %[k2], %[mk], %[pa]\n\t"
                     "v_not_b32_dpp %[t2], %[km] row_ror:4 row_mask:0x2 bank_mask:0xf\n\tv_not_b32_dpp %[t2], %[km] row_ror:8 row_mask:0x4 bank_mask:0xf\n\t"
                     "v_not_b32_dpp %[t2], %[km] row_ror:12 row_mask:0x8 bank_mask:0xf\n\t"
                     "v_pk_lshlrev_b16 %[gd], 4, %[gd] op_sel_hi:[0,1]\n\tv_add_u32 %[t0], %[km], %[t2]\n\t"
                     "v_add_u32_dpp %[t1], %[t2], %[km] row_ror:1 row_mask:0xf bank_mask:0xf\n\tv_add_u32_dpp %[t3], %[t2], %[km] row_ror:2 row_mask:0xf bank_mask:0xf\n\t"
                     "v_add_u32_dpp %[t2], %[t2], %[km] row_ror:3 row_mask:0xf bank_mask:0xf\n\t"
                     RANK8("%[t0]", "%[t1]", "%[t2]", "%[t3]", "%[rk]", "%[t3]", "-2", "v_lshrrev_b32 %[se], 4, %[se]\n\tv_perm_b32 %[gd], %[gd], %[se], %[p1]",
                           "v_pk_max_u16 %[gd], %[gd], %[gd] op_sel:[0,1] op_sel_hi:[1,0]\n\tv_and_or_b32 %[k3], %[k3], %[mk], %[pb]")
                     "v_cmp_eq_u32 vcc, 56, %[rk]\n\tv_max3_u32 %[se], %[lo], %[k2], %[k3]\n\tv_lshrrev_b32 %[gd], 1, %[gd]\n\ts_ff1_i32_b32 s6, vcc_lo\n\t"
                     "v_readlane_b32 s6, %[km], s6\n\tv_or_b32 %[gd], 0x7fff, %[gd]\n\tv_med3_u32 %[t2], %[lo], %[k2], %[k3]\n\t"
                     "v_cmp_le_u32 s[8:9], s6, %[hi]\n\tv_cmp_le_u32 s[10:11], s6, %[se]\n\tv_max_u32 %[t2], %[t2], %[gd]\n\t"
                     "v_mbcnt_lo_u32_b32 %[t0], s8, 0\n\tv_mbcnt_lo_u32_b32 %[t1], s10, -15\n\tv_mbcnt_hi_u32_b32 %[t0], s9, %[t0]\n\tv_mbcnt_hi_u32_b32 %[t1], s11, %[t1]\n\t"
                     "v_lshl_add_u32 %[t0], %[t0], 2, %[ca]\n\tv_mad_i32_i24 %[t1], %[t1], -4, %[ca]\n\tv_cmp_le_u32 vcc, s6, %[t2]\n\t"
                     "s_mov_b64 exec, s[8:9]\n\tds_write_b32 %[t0], %[hi]\n\ts_mov_b64 exec, s[10:11]\n\tds_write_b32 %[t1], %[se]\n\ts_mov_b64 exec, -1\n\t"
                     "ds_read_b32 %[se], %[kr]\n\tds_read_b128 v[152:155], %[kq]\n\t"
                     "s_bcnt1_i32_b64 s8, s[8:9]\n\ts_bcnt1_i32_b64 s10, s[10:11]\n\ts_add_u32 s8, s8, s10\n\ts_cmp_gt_u32 s8, 16\n\ts_cselect_b64 s[8:9], -1, vcc\n\t"
                     "s_cmp_lg_u64 s[8:9], 0\n\ts_waitcnt lgkmcnt(0)\n\ts_cbranch_scc1 6f\n\t"
                     "v_sub_u32 %[t0], %[se], v152\n\tv_sub_u32 %[t1], %[se], v153\n\tv_sub_u32 %[t2], %[se], v154\n\tv_sub_u32 %[t3], %[se], v155\n\t"
                     RANK8("%[t0]", "%[t1]", "%[t2]", "%[t3]", "%[rk]", "%[t3]", "0", "v_mov_b32 %[km], %[se]\n\ts_nop 0", "s_nop 1") "6:\n\t" TS(62, 63)
                     : [hi] "=&v"(hi), [lo] "=&v"(lo), [k2] "=&v"(k2), [k3] "=&v"(k3), [gd] "=&v"(gd), [se] "=&v"(se), [km] "=&v"(km), [rk] "=&v"(rk),
                       [t0] "=&v"(t0), [t1] "=&v"(t1), [t2] "=&v"(t2), [t3] "=&v"(t3), [n] "+s"(n_)
                     : [o] "v"(O_TOP + 16 * (l & 7) + TL * (l >> 3)), [r] "s"(rs), [t] "s"(tag), [ts] "s"(TOPS), [st] "s"(st0), [tw] "n"(TW), [mk] "s"(0x7fff800fu),
                       [pa] "v"((127 - 2 * l) << 4), [pb] "v"((126 - 2 * l) << 4), [sg] "s"(sg), [p0] "s"(0x05040100u), [p1] "s"(0x07020500u), [ca] "s"(ca),
                       [kr] "v"(kr), [kq] "v"(kq)
                     : "vcc", "scc", "s6", "s8", "s9", "s10", "s11", "memory" TSC);
      }
      unsigned long long more;
      asm volatile("" : "={s6}"(thr), "={s[8:9]}"(more));
      if (__builtin_expect(more != 0, 0)) {
        const u32 m0 = km;
        unsigned long long all = __ballot(gd >= thr);
        if (gd >= thr) hi = lo = k2 = k3 = 0u;
#pragma unroll 1
        for (int i = 0; i < 4;) {
          u32 k, m = m0;
          if (all) {
            const int a = __builtin_ctzll(all);
            all &= all - 1;
            u32x2 r;
            const u32 o = O_RG + 256 * a + 8 * (l & 31);
            POLL8(r, o);
            k = l < 32 ? r.x : 0u;
            m = (u32)__builtin_amdgcn_readlane((int)m0, a);
          } else { k = hi; hi = lo; lo = k2; k2 = k3; i++; }
          asm volatile("v_cmp_le_u32 s[8:9], %[t], %[k]\n\tv_cmp_ne_u32 vcc, %[k], %[m0]\n\ts_and_b64 s[8:9], s[8:9], vcc\n\ts_cbranch_scc0 2f\n"
                       "1:\n\ts_ff1_i32_b64 s7, s[8:9]\n\tv_readlane_b32 s12, %[k], s7\n\ts_lshl_b64 s[10:11], 1, s7\n\ts_nop 0\n\t"
                       "v_cmpx_gt_u32 vcc, s12, %[km]\n\tv_add_u32 %[rk], 8, %[rk]\n\ts_bcnt0_i32_b32 s7, vcc_lo\n\tv_cmpx_eq_u32 vcc, 0x80, %[rk]\n\t"
                       "v_mov_b32 %[km], s12\n\tv_lshlrev_b32 %[rk], 2, s7\n\ts_mov_b64 exec, -1\n\ts_andn2_b64 s[8:9], s[8:9], s[10:11]\n\ts_cbranch_scc1 1b\n2:"
                       : [km] "+v"(km), [rk] "+v"(rk) : [k] "v"(k), [m0] "v"(m), [t] "s"(thr) : "vcc", "scc", "s7", "s8", "s9", "s10", "s11", "s12");
        }
      }
      if (id < 126) {
        u32 a_, v_, n_ = SPIN;
#define RLD(o, s_) "buffer_load_dwordx4 %[v], %[rx], " s_ " offen offset:" #o " lds\n\t"
        asm volatile("v_add_u32 %[a], %[kb], %[rk]\n\tv_and_b32 %[v], 0x7ff, %[km]\n\ts_mov_b32 m0, %[fa]\n\tds_write_b32 %[a], %[v]\n\tds_read_b32 %[a], %[ka]\n\t"
                     "s_cmp_eq_u32 %[ns], 8\n\ts_waitcnt lgkmcnt(0)\n\tv_mad_i32_i24 %[v], %[a], %[cm], %[kc]\n\t"
                     RLD(0, "%[c3]") RLD(1024, "%[c2]") RLD(2048, "%[c1]") RLD(3072, "0")
                     "s_mov_b32 m0, %[fb]\n\tv_add_u32 %[v], 0x200, %[v]\n\tv_mad_i32_i24 %[a], %[a], -8, %[cs]\n\t"
                     "s_cbranch_scc0 3f\n\t" RLD(2048, "%[c1]") RLD(3072, "0")
                     "3:\n\tbuffer_load_dwordx2 v[160:161], %[a], %[rs], 0 offen sc1\n\t" RLD(0, "%[c3]") RLD(1024, "%[c2]") "buffer_load_dwordx2 v[158:159], %[co], %[rs], 0 offen sc1\n\t"
                     "v_mov_b32 %[v], 16 * " STR(HOLD) " - 112\n\tv_subrev_u32 %[ka], %[kd], %[ka]\n\ts_mov_b64 exec, 1\n\tds_add_u32 %[fc], %[v] offset:8\n\ts_mov_b64 exec, -1\n\tv_mov_b32 %[v], -1\n\t" TS(64, 65)
                     "s_sleep " STR(CSL) "\n\tbuffer_load_dwordx2 v[154:155], %[co], %[rs], 0 offen sc1\n\t"
                     "s_waitcnt vmcnt(4)\n1:\n\tv_cmp_ne_u32 vcc, %[t], v161\n\tv_and_b32 v156, 0xffff0000, v160\n\tv_lshlrev_b32 v157, 16, v160\n\tv_rcp_f32 v156, v156\n\t"
                     "s_sub_u32 %[n], %[n], 1\n\ts_cbranch_vccz 2f\n\ts_cbranch_scc1 2f\n\t"
                     "buffer_load_dwordx2 v[160:161], %[a], %[rs], 0 offen sc1\n\ts_waitcnt vmcnt(0)\n\ts_branch 1b\n2:\n\tv_mul_f32 v157, -v157, v156\n\t"
                     "ds_write_b64 %[ka], v[156:157]\n\ts_waitcnt vmcnt(2)\n\tds_write_b32 %[fc], %[v] offset:4\n\t" TS(66, 67)
                     "s_mov_b32 %[n], %[spin]\n\ts_waitcnt vmcnt(1)\n\tv_cmp_ne_u32 vcc, %[t], v159\n\ts_cbranch_vccz 5f\n"
                     "4:\n\ts_waitcnt vmcnt(0)\n\tv_cmp_ne_u32 vcc, %[t], v155\n\ts_sub_u32 %[n], %[n], 1\n\ts_cbranch_vccz 5f\n\t"
                     "buffer_load_dwordx2 v[154:155], %[co], %[rs], 0 offen sc1\n\ts_cbranch_scc0 4b\n5:"
                     : [a] "=&v"(a_), [v] "=&v"(v_), [ka] "+v"(ka), [n] "+s"(n_)
                     : [co] "v"(co), [rk] "v"(rk), [km] "v"(km), [kc] "v"(kc), [kb] "n"(FC + 384), [kd] "n"(384 - 32), [fa] "n"(FA), [fb] "n"(FA + 4096), [rx] "s"(rx), [rs] "s"(rs),
                       [c1] "s"(c1), [c2] "s"(c2), [c3] "s"(c3), [cm] "s"(cm), [cs] "s"(cs), [ns] "s"(ns), [fc] "v"(fa), [t] "s"(tag), [spin] "n"(SPIN) : "vcc", "scc", "memory" TSC);
        TSGET(12, 13, 14, 15);
        FLAG(FC + 28, 1u);
        STAMP(10);
      } else {
        const u32 cd = km << 1;
        const u32x2 r = {(2047u - (km & 0x7ffu)) | ((cd ^ ((u32)((int)~cd >> 15) | 0x80000000u)) & 0xffff0000u), tag};
        const u32 o = rk < 64u ? O_IDX + 1024 * mat + 128 * kg + rk : 0x80000000u;
        st8(rs, o, r);
        st8(rs, o + 512, r);
        TSGET(12, 13, 14, 15);
        STAMP(14);
        STAMP(15);
        if (mat) {
#pragma unroll 1
          for (int spin = 0; spin < SPIN; spin++) {
            const u32x4 a = ld16(rs, O_ARR + 16 * l);
            if (__all(a.y == tag && a.w == tag)) break;
            asm volatile("s_sleep 16" ::: "memory");
          }
          if (l == 0) __builtin_amdgcn_raw_buffer_store_b32(tag, rs, 0, 0, 16);
        }
      }
      FLUSH();
      __builtin_amdgcn_endpgm();
    }
    u32x4 xv[4];
    u32x8 wv;
    ROLE(l, "", , "={v[100:103]}"(xv[0]), "={v[104:107]}"(xv[1]), "={v[108:111]}"(xv[2]), "={v[112:115]}"(xv[3]), "={v[116:123]}"(wv));
    STAMP(11);
    u32 sg = 0x0c0c0b09u;
    asm volatile("" : "+s"(sg));
    ln_rows(xv, wv, rs, tag, b, g, l, lds0 + FC + 128, sg TLVAL);
    STAMP(10);
    IDS();
    if (id >= 126) {
      FLUSH();
      __builtin_amdgcn_endpgm();
    }
    const int wr = 16 * (kg >> 1) + 4 * (kg & 1) + (n16 & 8) + ((n16 >> 2) & 1);
    u32 wa = lds0 + 256 * wr + ((w ^ ((wr & 1) | ((wr >> 1) & 6))) << 5) + 8 * (l & 3), sel = (l & 8) ? 0xeeeeu : 0x4444u;
    u32 so = 4 * t;
    asm volatile("" : "+v"(wa), "+v"(sel), "+v"(so));
    asm volatile(UNTIL(STR(CLD)) :: [st] "s"(st0) : "s8", "s9", "scc");
    __builtin_amdgcn_raw_buffer_store_b32(~0u, rs, so, O_HG + (6 * cb + 3 * mat + part) * 2048, 16);
    st8(rs, u < 8 ? O_PG + (4 * kp + ct) * 4096 + 2 * so : 0x80000000u, u32x2{~0u, ~0u});
#define SWP(a0, a1, a2, a3) "v_permlane32_swap_b32 " a0 ", " a2 "\n\tv_permlane32_swap_b32 " a1 ", " a3 "\n\t"
#define OUT(a0, a1, o) "v_cvt_pk_bf16_f32 " a0 ", " a0 ", " a1 "\n\tv_min_u32 " a0 ", -2, " a0 "\n\tbuffer_store_dword " a0 ", %[so], %[rs], " o " sc1\n\t"
#define TR4(a, b_, c, d, e, f, g_, h, adr, o) "ds_read_b64_tr_b16 v[" #a ":" #b_ "], " adr " offset:" #o "\n\tds_read_b64_tr_b16 v[" #c ":" #d "], " adr " offset:" #o "+512\n\t" \
      "ds_read_b64_tr_b16 v[" #e ":" #f "], " adr " offset:" #o "+8192\n\tds_read_b64_tr_b16 v[" #g_ ":" #h "], " adr " offset:" #o "+8704\n\t"
#define SM(d, a, b_) "v_smfmac_f32_16x16x64_bf16 v[" d "], v[" a "], v[" b_ "], %[sel]\n\t"
#define USE(c, xa, wf) "s_add_u32 s6, s6, 16\n\ts_waitcnt lgkmcnt(" #c ")\n\t" SM("16:19", "20:23", wf) SM("8:11", xa, wf)
#define XR(xa, o) "ds_read_b128 v[" xa "], %[xa] offset:" #o "\n\t"
#define XU(c, xa, wf) "s_waitcnt lgkmcnt(" #c ")\n\t" SM("8:11", xa, wf)
#define ZERO(r) "v_mfma_f32_32x32x16_bf16 v[" r "], v[8:11], v[8:11], 0\n\t"
#define POLL(c, rd) "7:\n\tds_read_b32 v30, %[fl]\n\ts_waitcnt lgkmcnt(0)\n\ts_call_b64 s[10:11], 1f\n\ts_cbranch_scc1 7b\n\t" rd
#define CLR "s_waitcnt vmcnt(0)\n\ts_mov_b64 exec, 1\n\tds_add_rtn_u32 v14, %[fl], v28 offset:96\n\ts_waitcnt lgkmcnt(0)\n\tv_cmpx_eq_u32 vcc, 7, v14\n\t" \
      "buffer_store_dwordx2 v[12:13], off, %[rs], %[co] sc1\n\ts_mov_b64 exec, -1\n\t"
#define XE(k) "s_cmpk_eq_u32 s6, " #k "\n\ts_cbranch_scc1 9f\n\t"
    asm volatile("v_mov_b64 v[8:9], 0\n\tv_mov_b64 v[10:11], 0\n\tv_mov_b64 v[16:17], 0\n\tv_mov_b64 v[18:19], 0\n\t"
                 ZERO("96:111") ZERO("112:127")
                 "v_mov_b32 v20, 0x3f803f80\n\tv_mov_b32 v21, v20\n\tv_mov_b64 v[22:23], v[20:21]\n\tv_mov_b32 v13, %[t]\n\tv_mov_b32 v28, 1\n\tv_mov_b32 v30, 0\n\t"
                 "v_add_u32 v29, 0x10000, %[wa]\n\ts_movk_i32 s6, 96\n\ts_mov_b32 s7, 1\n\ts_mov_b32 s8, 15\n\ts_mov_b32 s9, %[spin]\n\ts_nop 7\n\t"
                 POLL(8, TR4(32, 33, 34, 35, 36, 37, 38, 39, "%[wa]", 0) TR4(40, 41, 42, 43, 44, 45, 46, 47, "%[wa]", 16384))
                 USE(4, "96:99", "32:39") "s_waitcnt lgkmcnt(0)\n\t" SM("16:19", "20:23", "40:47") SM("8:11", "100:103", "40:47")
                 CLR
                 POLL(4, TR4(48, 49, 50, 51, 52, 53, 54, 55, "%[wa]", 32768)) USE(0, "104:107", "48:55")
                 POLL(4, TR4(56, 57, 58, 59, 60, 61, 62, 63, "%[wa]", 49152)) "s_mov_b64 exec, 1\n\tds_add_u32 %[fl], v28 offset:20\n\ts_mov_b64 exec, -1\n\t" TS(60, 61)
                 USE(0, "108:111", "56:63")
                 POLL(4, TR4(64, 65, 66, 67, 68, 69, 70, 71, "v29", 0)) USE(0, "112:115", "64:71") "s_cmp_eq_u32 %[ns], 5\n\ts_cbranch_scc1 5f\n\t"
                 POLL(4, TR4(72, 73, 74, 75, 76, 77, 78, 79, "v29", 16384)) USE(0, "116:119", "72:79") "s_cmp_eq_u32 %[ns], 6\n\ts_cbranch_scc1 5f\n\t"
                 POLL(4, TR4(80, 81, 82, 83, 84, 85, 86, 87, "v29", 32768)) USE(0, "120:123", "80:87")
                 POLL(4, TR4(88, 89, 90, 91, 92, 93, 94, 95, "v29", 49152)) USE(0, "124:127", "88:95")
                 "5:\n\t" TS(62, 63) "s_cmp_eq_u32 s7, 0\n\ts_cbranch_scc1 4f\n\ts_mov_b32 s8, 16\n\t"
                 "s_getpc_b64 s[10:11]\n\tds_read_b32 v30, %[fl]\n\ts_waitcnt lgkmcnt(0)\n"

                 "1:\n\tv_cmp_le_u32 vcc, s6, v30\n\ts_and_b32 s12, vcc_hi, s7\n\ts_cbranch_scc1 3f\n"
                 "2:\n\ts_andn2_b32 s12, s8, vcc_lo\n\ts_cbranch_scc1 6f\n\ts_setpc_b64 s[10:11]\n"
                 "6:\n\ts_sub_u32 s9, s9, 1\n\ts_cbranch_scc1 4f\n\t" GNAP "\n\ts_cmp_eq_u32 s6, s6\n\ts_setpc_b64 s[10:11]\n"
                 "3:\n\t"
                 XR("96:99", 0) XR("100:103", 1024) XR("104:107", 2048) XR("108:111", 3072) XR("112:115", 4096) XR("116:119", 5120) XR("120:123", 6144) XR("124:127", 7168)
                 "ds_read_b128 v[24:27], %[pa]\n\ts_mov_b32 s7, 0\n\t"
                 TS(64, 65)
                 XE(96) XU(8, "96:99", "32:39") XU(7, "100:103", "40:47") XE(112) XU(6, "104:107", "48:55") XE(128) XU(5, "108:111", "56:63") XE(144)
                 XU(4, "112:115", "64:71") XE(160) XU(3, "116:119", "72:79") XE(176) XU(2, "120:123", "80:87") XE(192) XU(1, "124:127", "88:95")
                 "9:\n\ts_cmp_eq_u32 s8, 16\n\ts_waitcnt lgkmcnt(0)\n\ts_cbranch_scc0 2b\n"
                 "4:\n\ts_nop 4\n\tv_permlane32_swap_b32 v16, v17\n\tv_add_f32 v16, v16, v17\n\tv_mul_f32 v25, v25, v16\n\tv_mul_f32 v27, v27, v16\n\t"
                 SWP("v8", "v9", "v10", "v11") "v_fmac_f32 v25, v10, v24\n\tv_fmac_f32 v27, v11, v26\n\tv_fma_f32 v8, v8, v24, v25\n\tv_fma_f32 v9, v9, v26, v27\n\t"
                 OUT("v8", "v9", "%[ho] offen") TS(66, 67)
                 :: [wa] "v"(wa), [sel] "v"(sel), [so] "v"(so), [fl] "v"(lds0 + FC + 4 * min(l, 4)), [xa] "v"(lds0 + FA + 128 * (l & 7) + 16 * ((2 * kg + (n16 >> 3)) ^ (2 * l & 4))),
                    [pa] "v"(lds0 + FC + 32 + 32 * (kg & 1) + 16 * (kg >> 1)), [ns] "s"(ns), [t] "s"(tag), [co] "s"(O_CLR + 8 * id), [rs] "s"(rs), [ho] "s"(O_HG + (6 * cb + 3 * mat + part) * 2048), [spin] "n"(SPIN)
                 : "vcc", "scc", "s6", "s7", "s8", "s9", "s10", "s11", "s12", "s13", "memory", G_CLOBBER TSC);
    TSGET(1, 4, 3, 5);
    if (u < 8) {
      const int tp = 2 * (kg & 1) + (kg >> 1), r7 = 4 * (kg & 1) + (n16 >> 2);
      u32 o3 = (kp & 3) == 1 ? so + 4096 : 0x80000000u, h0 = O_HG + kp * 12288, h1 = h0 + 6144;
      u32 hw = lds0 + FH + 2 * tp * HP + 32 * w + 8 * (l & 2) + (l & 4) * 2 + ((l & 8) >> 1) + 2 * (l & 1);
      u32 hr = lds0 + FH + (l & 7) * HP + 32 * kg + 2 * (l & 8);
      u32 da = lds0 + 512 * (16 * (kg >> 1) + r7) + ((2 * w ^ r7) << 5) + 8 * (l & 3), db = da ^ 32, fl = lds0 + FC + 4 * (l & 3);
      u32 po = 512 * w + 16 * (l & 8) + 32 * tp + 4 * (l & 7);
      u32 e0 = 0x3f80u, e1 = 0x3f800000u;
      asm volatile("" : "+v"(hw), "+v"(hr), "+v"(da), "+v"(db), "+v"(fl), "+v"(po), "+v"(o3));
      asm volatile("" : "+s"(e0), "+s"(e1), "+s"(h0), "+s"(h1));
#define TRD(a, b_, c, d, e, f, g_, h, adr, o) "ds_read_b64_tr_b16 v[" #a ":" #b_ "], " adr " offset:" #o "\n\tds_read_b64_tr_b16 v[" #c ":" #d "], " adr " offset:" #o "+4096\n\t" \
      "ds_read_b64_tr_b16 v[" #e ":" #f "], " adr " offset:" #o "+16384\n\tds_read_b64_tr_b16 v[" #g_ ":" #h "], " adr " offset:" #o "+20480\n\t"
#define HLD(X, r) X(r "0", "%[s1]", "%[h0] offen") X(r "3", "%[s1]", "%[h1] offen") X(r "1", "%[s1]", "%[h0] offen offset:2048") X(r "4", "%[s1]", "%[h1] offen offset:2048") \
      X(r "2", "%[o3]", "%[h0] offen") X(r "5", "%[o3]", "%[h1] offen")
#define HL0(a, o, s_) "buffer_load_dword " a ", " o ", %[rs], " s_ " sc1\n\t"
#define HLF(a, o, s_) "buffer_load_dword " a ", " o ", %[rs], " s_ SCH "\n\t"
#define HL1(a, o, s_) "v_cmpx_eq_u32 vcc, -1, " a "\n\t" HL0(a, o, s_) "s_mov_b64 exec, -1\n\t"
#define DOT(d, e, a) "v_dot2c_f32_bf16 " d ", %[" e "], " a "\n\t"
#define HCK(c, r) "s_cmp_eq_u32 s7, 0\n\ts_cbranch_scc1 5f\n\ts_call_b64 s[10:11], 4f\n5:\n\ts_waitcnt vmcnt(" #c ")\n\tv_max3_u32 v28, " r "0, " r "1, " r "2\n\tv_lshlrev_b32 v24, 16, " r "0\n\tv_max3_u32 v28, v28, " r "3, " r "4\n\t" \
      "v_and_b32 v25, %[hm], " r "0\n\tv_max_u32 v28, v28, " r "5\n\t" DOT("v24", "e0", r "1") "v_cmp_eq_u32 vcc, -1, v28\n\t" DOT("v25", "e1", r "1") DOT("v24", "e0", r "2") DOT("v25", "e1", r "2") \
      "v_lshlrev_b32 v26, 16, " r "3\n\tv_and_b32 v27, %[hm], " r "3\n\t" DOT("v26", "e0", r "4") DOT("v27", "e1", r "4") DOT("v26", "e0", r "5") DOT("v27", "e1", r "5")
#define RB2(a, b_) "v_cvt_pk_bf16_f32 " a ", 0, " a "\n\tv_cvt_pk_bf16_f32 " b_ ", 0, " b_ "\n\t"
      asm volatile("s_mov_b32 s7, 1\n\ts_mov_b32 s12, %[spin]\n\tv_mov_b64 v[8:9], 0\n\tv_mov_b64 v[10:11], 0\n\tv_mov_b64 v[12:13], 0\n\tv_mov_b64 v[14:15], 0\n"
                   "2:\n\ts_sleep 1\n\tds_read_b32 v28, %[fl] offset:16\n\ts_memrealtime s[8:9]\n\ts_waitcnt lgkmcnt(0)\n\tv_cmp_eq_u32 vcc, 0, v28\n\ts_sub_u32 s8, s8, %[st]\n\t"
                   "s_cmp_lt_u32 s8, %[th]\n\ts_cbranch_scc1 2b\n\ts_bitcmp1_b32 vcc_lo, 3\n\ts_cbranch_scc1 2b\n\t" TS(64, 65)
                   HLD(HLF, "v7") HCK(0, "v7") "s_cbranch_vccnz 9f\n"
                   "3:\n\t" TS(66, 67) RB2("v24", "v25") "v_mul_f32 v28, %[le], v24\n\tv_mul_f32 v29, %[le], v25\n\tv_exp_f32 v28, v28\n\tv_exp_f32 v29, v29\n\t" RB2("v26", "v27")
                   "v_add_f32 v28, 1.0, v28\n\tv_add_f32 v29, 1.0, v29\n\tv_rcp_f32 v28, v28\n\tv_rcp_f32 v29, v29\n\ts_cmp_eq_u32 s7, 0\n\tv_mul_f32 v24, v24, v28\n\tv_mul_f32 v25, v25, v29\n\t"
                   RB2("v24", "v25") "v_mul_f32 v24, v24, v26\n\tv_mul_f32 v25, v25, v27\n\tv_cvt_pk_bf16_f32 v24, v24, v25\n\ts_mov_b32 s12, %[spin]\n\t"
                   "ds_write_b16 %[hw], v24\n\tds_write_b16_d16_hi %[hw], v24 offset:%[hp]\n\ts_cbranch_scc1 8f\n"
                   "7:\n\ts_call_b64 s[10:11], 4f\n\ts_mul_i32 s12, s12, s7\n\ts_sub_u32 s12, s12, 1\n\ts_cbranch_scc0 7b\n"
                   "8:\n\ts_waitcnt lgkmcnt(0)\n\ts_barrier\n\t" TS(60, 61)
                   XR("16:19", 0) XR("20:23", 128)
                   XU(1, "16:19", "32:39") SM("12:15", "16:19", "40:47") XU(0, "20:23", "48:55") SM("12:15", "20:23", "56:63") "s_nop 6\n\t"
                   SWP("v8", "v9", "v10", "v11") "v_add_f32 v8, v8, v10\n\tv_add_f32 v9, v9, v11\n\t" SWP("v12", "v13", "v14", "v15") OUT("v8", "v9", "%[po] offen")
                   "v_add_f32 v12, v12, v14\n\tv_add_f32 v13, v13, v15\n\t" OUT("v12", "v13", "%[po] offen offset:256") TS(62, 63) DONE
                   "9:\n\tv_mov_b64 v[80:81], v[70:71]\n\tv_mov_b64 v[82:83], v[72:73]\n\tv_mov_b64 v[84:85], v[74:75]\n\t" HLD(HL1, "v7") "s_sleep " STR(HNAP) "\n"
                   "1:\n\t" HLD(HL1, "v8") HCK(6, "v7") "s_cbranch_vccz 3b\n\t" HLD(HL1, "v7") HCK(6, "v8") "s_sub_u32 s12, s12, 1\n\ts_cbranch_vccz 3b\n\ts_cbranch_scc0 1b\n\ts_branch 3b\n"
                   "4:\n\tds_read_b32 v28, %[fl]\n\ts_waitcnt lgkmcnt(0)\n\tv_cmp_ne_u32 vcc, -1, v28\n\ts_cbranch_vccnz 6f\n\t"
                   TRD(32, 33, 34, 35, 36, 37, 38, 39, "%[da]", 0) TRD(40, 41, 42, 43, 44, 45, 46, 47, "%[db]", 0)
                   TRD(48, 49, 50, 51, 52, 53, 54, 55, "%[da]", 32768) TRD(56, 57, 58, 59, 60, 61, 62, 63, "%[db]", 32768) "s_mov_b32 s7, 0\n"
                   "6:\n\ts_setpc_b64 s[10:11]\n0:"
                   :: [s1] "v"(so), [o3] "v"(o3), [fl] "v"(fl), [da] "v"(da), [db] "v"(db), [hw] "v"(hw), [xa] "v"(hr), [sel] "v"(sel), [so] "v"(po), [rs] "s"(rs), [h0] "s"(h0), [h1] "s"(h1),
                      [st] "s"(st0), [th] "n"(TH), [spin] "n"(SPIN), [hp] "n"(HP), [po] "s"(O_PG + (4 * kp + ct) * 4096), [e0] "s"(e0), [e1] "s"(e1), [hm] "s"(0xffff0000u),
                      [le] "s"(-1.4426950408889634f)
                   : "vcc", "scc", "s7", "s8", "s9", "s10", "s11", "s12", "memory", G_CLOBBER TSC);
      TSGET(8, 9, 6, 7);
    }
    FLUSH();
    __builtin_amdgcn_endpgm();
  }

  if (w0 >= 8) {
    u32x8 xa0, xa1, xa2, xa3, sa1;
    u32x4 junk;
    u32 op, so;
    ROLE(p, "", , "={v[100:107]}"(xa0), "={v[108:115]}"(xa1), "={v[116:123]}"(xa2),
         "={v148}"(op), "={v149}"(so), "={v[160:163]}"(junk));
    const int p = w - 8;
    __builtin_amdgcn_s_setprio(3);
    asm volatile("s_waitcnt lgkmcnt(0)" : "+{s44}"(tag)::"memory");
    tag += 1u;
    STAMP(9);
    f32x2 xq[4][8];
    u32x8 pa;
    {
#define SCK(r1, r3, r5, r7) "v_xor_b32 %[d], %[t], v" #r1 "\n\tv_bitop3_b32 %[d], %[d], %[t], v" #r3 " bitop3:0xf6\n\t" \
      "v_bitop3_b32 %[d], %[d], %[t], v" #r5 " bitop3:0xf6\n\tv_bitop3_b32 %[d], %[d], %[t], v" #r7 " bitop3:0xf6\n\t"
#define SLX(r0, r3, r4, r7, k, k1, sc) "buffer_load_dwordx4 v[" #r0 ":" #r3 "], %[so], %[rs], " k " offen" sc "\n\tbuffer_load_dwordx4 v[" #r4 ":" #r7 "], %[so], %[rs], " k1 " offen" sc "\n\t"
#define SLD(r0, r3, r4, r7, k, k1) SLX(r0, r3, r4, r7, k, k1, " sc1")
#define SLOOP(r0, r1, r3, r4, r5, r7, k, k1) "1:\n\ts_waitcnt vmcnt(0)\n4:\n\t" SCK(r1, r3, r5, r7) "s_sub_u32 %[n], %[n], 1\n\tv_cmp_ne_u32 vcc, 0, %[d]\n\ts_cbranch_scc1 2f\n\ts_cbranch_vccz 2f\n\t" \
      SLD(r0, r3, r4, r7, k, k1) "s_branch 1b\n2:\n\t"
      u32 d_, n_ = SPIN, k1 = 4096, k2 = 8192, k3 = 12288, s0[4];
      asm volatile("s_mov_b32 m0, %[m]\n\ts_cmp_eq_u32 %[k], 3\n\ts_waitcnt vmcnt(2)\n\ts_cbranch_scc0 1f\n\ts_waitcnt vmcnt(0)\n1:"
                   : "+{v[100:107]}"(xa0), "+{v[108:115]}"(xa1), "+{v[160:163]}"(junk) : [m] "s"(lds0 + OX + 2 * XO + 256 * p), [k] "s"(xk) : "scc", "memory");
      SB();
#define UNPACK(i, xs) _Pragma("unroll") for (int c = 0; c < 8; c++) { \
        xq[i][c] = (c & 1) ? f32x2{hi2f(xs[c >> 1]), hi2f(xs[4 + (c >> 1)])} : f32x2{lo2f(xs[c >> 1]), lo2f(xs[4 + (c >> 1)])}; asm volatile("" : "+v"(xq[i][c].x), "+v"(xq[i][c].y)); }
      UNPACK(0, xa0);
      UNPACK(1, xa1);
      u32 fa = lds0 + OXF + 4 * p, fv = 1;
      asm volatile("" : "+v"(fa), "+v"(fv));
      asm volatile("" : "+s"(k1), "+s"(k2), "+s"(k3), "+s"(n_));
      SB();
      STAMP(1);
#define SCP(r0, r1, r2, r3, r4, r5, r6, r7) "s_waitcnt vmcnt(2)\n\t" SCK(r1, r3, r5, r7) "v_cmp_ne_u32 vcc, 0, %[d]\n\tv_mov_b32 %[a0], v" #r0 "\n\tv_mov_b32 %[a1], v" #r2 "\n\t" \
      "v_mov_b32 %[a2], v" #r4 "\n\tv_mov_b32 %[a3], v" #r6 "\n\ts_cbranch_vccz 3f\n\t" SLD(r0, r3, r4, r7, "0", "%[k1]")
      asm volatile(UNTIL(STR(TSG)) SLD(132, 135, 136, 139, "0", "%[k1]") "s_sleep " STR(SSL) "\n\t" SLD(140, 143, 144, 147, "0", "%[k1]") "s_waitcnt vmcnt(4)"
                   : "=&{v[132:139]}"(pa), "=&{v[140:147]}"(sa1), "+{v[116:123]}"(xa2)
                   : [st] "s"(st0), [so] "v"(so), [rs] "s"(rs), [k1] "s"(k1) : "scc", "s8", "s9", "memory");
      UNPACK(2, xa2);
      SB();
      asm volatile("1:\n\t" SCP(132, 133, 134, 135, 136, 137, 138, 139) SCP(140, 141, 142, 143, 144, 145, 146, 147) "s_sub_u32 %[n], %[n], 1\n\ts_cbranch_scc0 1b\n"
                   "3:\n\t" SLD(140, 143, 144, 147, "%[k2]", "%[k3]")
                   : [d] "=&v"(d_), [n] "+s"(n_), [a0] "=&v"(s0[0]), [a1] "=&v"(s0[1]), [a2] "=&v"(s0[2]), [a3] "=&v"(s0[3]), "+{v[132:139]}"(pa), "+{v[140:147]}"(sa1)
                   : [t] "s"(tag), [so] "v"(so), [rs] "s"(rs), [k1] "s"(k1), [k2] "s"(k2), [k3] "s"(k3) : "vcc", "scc", "memory");
#pragma unroll
      for (int i = 0; i < 5; i++) {
#define XLD(r0, r1, r2, r3, o_) "v_add_u32 %[d], " #o_ ", %[o]\n\tglobal_load_dwordx4 v[" #r0 ":" #r1 "], %[d], %[x]\n\tglobal_load_dwordx4 v[" #r2 ":" #r3 "], %[d], %[x] offset:2048\n"
        if (i == XI) asm volatile("s_cmp_eq_u32 %[k], 3\n\ts_cbranch_scc0 1f\n\t" XLD(116, 119, 120, 123, 0x200000) "1:\n\t" XLD(124, 127, 128, 131, 0x300000)
                                 : [d] "=&v"(d_), "+{v[116:123]}"(xa2), "=&{v[124:131]}"(xa3) : [k] "s"(xk), [o] "v"(op), [x] "s"(x) : "scc", "memory");
        if (i == 2) {
          asm volatile("s_waitcnt vmcnt(2)\n\ts_branch 4f\n" SLOOP(140, 141, 143, 144, 145, 147, "%[k2]", "%[k3]") : [d] "=&v"(d_), [n] "+s"(n_), "+{v[140:147]}"(sa1), "+{v[116:123]}"(xa2), "+{v[132:139]}"(pa)
                       : [t] "s"(tag), [so] "v"(so), [rs] "s"(rs), [k2] "s"(k2), [k3] "s"(k3) : "vcc", "scc", "memory");
          if (xk == 3) { UNPACK(2, xa2); }
        }
        if (i == 3) asm volatile("s_waitcnt vmcnt(%1)" : "+{v[124:131]}"(xa3) : "n"(DPI == 2 ? DPF : 0));
        if (i == DPI && DPF) {
          u32 lx = l;
          asm volatile("" : "+v"(lx));
          const u32 ti = min(16 * xk + 4 * p + (lx >> 4), 125u), tsb = __umul24(ti, 57) >> 10, tu = min(ti - __umul24(18, tsb), 15u);
          const u32 o = (128 * (4 * tsb + (tu >> 2)) + DPO / 2 + 4 * j + ((lx >> 2) & 3)) * (2 * D) + 512 * (tu & 3) + 128 * (lx & 3);
          if (DPI == 4) AWAIT(OXF + 4 * (l & 3), 3, "s_sleep 1");
          if (DPF == 1) asm volatile("global_load_dword v160, %1, %2" : "=&{v[160:163]}"(junk) : "v"(o), "s"(wd) : "memory");
          else asm volatile("global_load_dword v160, %1, %2\n\tglobal_load_dword v161, %1, %3" : "=&{v[160:163]}"(junk) : "v"(o), "s"(wd), "s"(wd + 64 * D) : "memory");
        }
        if (i == 4) break;
        if (i == 3) UNPACK(3, xa3);
        const u32 sa = i < 2 ? s0[2 * i] : sa1[4 * i - 8], sb = i < 2 ? s0[2 * i + 1] : sa1[4 * i - 6];
        if (i == 0) STAMP(3);
        if (i == 2) STAMP(10);
        const f32x2 r2 = {__builtin_amdgcn_rcpf(hi2f(sa)), __builtin_amdgcn_rcpf(hi2f(sb))};
        const f32x2 m2 = {-lo2f(sa) * r2.x, -lo2f(sb) * r2.y};
        f32x2 n2[8];
        u32 o[8];
        SB();
#pragma unroll
        for (int c = 0; c < 8; c++) n2[c] = f32x2{fmav(xq[i][c].x, r2.x, m2.x), fmav(xq[i][c].y, r2.y, m2.y)};
        SB();
#pragma unroll
        for (int c = 0; c < 8; c++) o[c] = packh(n2[c].x, n2[c].y);
        SB();
#pragma unroll
        for (int c = 0; c < 8; c++) asm volatile("ds_write_addtid_b32 %0 offset:%1" ::"v"(o[c]), "n"(c * XREG + 1024 * i) : "memory");
        asm volatile("ds_write_b32 %1, %0\n\tv_add_u32 %0, 1, %0" : "+v"(fv) : "v"(fa) : "memory");
        if (i == PDROP) __builtin_amdgcn_s_setprio(PPRIO);
        SB();
      }
    }
    STAMP(4);
    const rsrc_t ro = __builtin_amdgcn_make_buffer_rsrc((void*)out, 0, 2 * L * D, 0x00020000);
    u32 ys[8], y[2][8] = {};
    u32x2 ix;
    u32x4 cl;
    u32x8 pb;
    float yc, rsel;
    const u32 ixo = IXO((l & 6) + (n16 >> 3));
    if (p == 0) asm volatile("buffer_load_dwordx2 v[4:5], %1, %2, 0 offen sc1" : "=&{v[4:5]}"(ix) : "v"(ixo), "s"(rs) : "memory");
    const int rc = 4 * (l & 1) + kg, u = 64 * (p & 1) + l;
    u32x4 pick;
    u32 po = O_PG + 128 * g + 16 * (l & 7) + 16384 * (l >> 3), p3 = l < 32 ? po + 3 * 131072 : 0x80000000u, yf = lds0 + OYP + 4 * min(n16, 12) + 64 * (p >> 1);
    u32 ya = lds0 + OY + CHR * rc, oo = 16 * g + 2 * rc, clo = O_CLR + 16 * min(l, 62);
    u32 yp = lds0 + OY + 512 * (p >> 1) + 4 * (u ^ ((u >> 4) & 6)), yq = yp + 4 * CHR, bp = lds0 + OMISC + 16 * p + 4 * kg, bs = 30 - 2 * n16;
#pragma unroll
    for (int q = 0; q < 4; q++) pick[q] = (n16 >> 2) == q ? 0x3f80u << 16 * (l & 1) : 0u;
    asm volatile("" : "+v"(pick), "+v"(ya), "+v"(oo), "+v"(po), "+v"(p3), "+v"(yf), "+v"(clo), "+v"(yp), "+v"(yq), "+v"(bp), "+v"(bs));
#pragma unroll
    for (int c = 0; c < 8; c++) asm volatile("" : "+v"(y[0][c]), "+v"(y[1][c]));
    if (TE) asm volatile(UNTIL(STR(TE)) :: [st] "s"(st0) : "s8", "s9", "scc");
#pragma unroll
    for (int i = 0; i < 4; i++) {
      if (i == IXI && p == 0) {
        u32 n_ = SPIN;
        asm volatile("s_waitcnt vmcnt(%5)\n1:\n\tv_cmp_ne_u32 vcc, %4, v5\n\ts_sub_u32 %1, %1, 1\n\ts_cbranch_vccz 2f\n\ts_cbranch_scc1 2f\n\t"
                     "buffer_load_dwordx2 v[4:5], %2, %3, 0 offen sc1\n\ts_waitcnt vmcnt(0)\n\ts_branch 1b\n2:"
                     : "+{v[4:5]}"(ix), "+s"(n_) : "v"(ixo), "s"(rs), "s"(tag), "n"(2 * IXI + 1) : "vcc", "scc", "memory");
        const u32 prow = ix.x & 2047u, v = prow >> 1;
        if (l < 16) atomicOr((u32*)(smem + OMISC) + (prow >> 5), 1u << (ix.x & 31u));
        FLAG(OYP + 256 + 32 + 64 * (l & 3), 8u);
        oo += prow * (2 * D);
        asm volatile("v_mov_b32 v162, 0\n\tglobal_load_short_d16_hi v162, %1, %2" : "+{v[160:163]}"(junk) : "v"(oo), "s"(x) : "memory");
        yc = *(const float*)(smem + OSC + 4 * rc); rsel = rbt(sigm(hi2f(ix.x)));
        ya += 4 * (v ^ ((v >> 4) & 6)) + 2 * (prow & 1);
        asm volatile("" : "+v"(yc), "+v"(rsel), "+v"(ya));
        STAMP(11);
      }
      if (i == 2 && p == 0) cl = ld16(rs, clo);
      if (i == 3) {
        asm volatile("s_waitcnt vmcnt(2)" ::: "memory");
        FLAG(OYP + 448 + 36 + 4 * p, 8u);
        if (p == 0) {
#pragma unroll 1
          for (int spin = 0; spin < SPIN; spin++) {
            if (__all(cl.y == tag && cl.w == tag)) break;
            cl = ld16(rs, clo);
          }
          STAMP(12);
        }
        __builtin_amdgcn_s_setprio(LPRIO);
      }
      {
        u32 n_ = SPIN, f_;
        asm volatile("1:\n\tds_read_b32 %0, %2 offset:%3\n\ts_sub_u32 %1, %1, 1\n\ts_waitcnt lgkmcnt(0)\n\tv_cmp_eq_u32 vcc, 0, %0\n\ts_cbranch_scc1 2f\n\ts_and_b32 vcc_lo, vcc_lo, %4\n\t"
                     "s_cbranch_scc0 2f\n\ts_sleep %5\n\ts_branch 1b\n2:" : "=&v"(f_), "+s"(n_) : "v"(yf), "n"(128 * i), "n"(i < 2 ? 255 : 511), "n"(i < 3 ? YN0 : YN3) : "vcc", "scc", "memory");
      }
      if (i == 0) STAMP(5); else if (i == 1) STAMP(6); else if (i == 2) STAMP(7); else STAMP(8);
      u32 bm = 0;
      if (i >= 2) asm volatile("ds_read_b32 %0, %1 offset:%2" : "=v"(bm) : "v"(bp), "n"(64 * i) : "memory");
#pragma unroll
      for (int r = 0; r < 2; r++)
#pragma unroll
        for (int c = 0; c < 8; c++) asm volatile("ds_read_u16_d16_hi %0, %1 offset:%2" : "+v"(y[r][c]) : "v"(c < 4 ? yp : yq), "n"((c & 3) * CHR + 1024 * i + 2 * r) : "memory");
      if (i == 3 && p == 0) {
#define PLD(X) X(132, 133, 134, 135, "%[po]", "0") X(136, 137, 138, 139, "%[po]", "%[k1]") X(140, 141, 142, 143, "%[po]", "%[k2]") X(144, 145, 146, 147, "%[p3]", "0")
#define PL0(a, b_, c, d, o, s_) "buffer_load_dwordx4 v[" #a ":" #d "], " o ", %[rs], " s_ " offen sc1\n\t"
        asm volatile(PLD(PL0) : "=&{v[132:139]}"(pa), "=&{v[140:147]}"(pb) : [po] "v"(po), [p3] "v"(p3), [rs] "s"(rs), [k1] "s"(131072), [k2] "s"(262144) : "memory");
      }
      if (i == 0) {
        st8(rs, t == 512 ? O_ARR + b * 8 : 0x80000000u, u32x2{0u, tag});
        const u32x4 a = *(const u32x4*)(smem + OSC), c = *(const u32x4*)(smem + OSC + 16);
#pragma unroll
        for (int k = 0; k < 4; k++) { ys[k] = (u32)__builtin_amdgcn_readfirstlane((int)a[k]); ys[4 + k] = (u32)__builtin_amdgcn_readfirstlane((int)c[k]); }
      }
      SB();
#pragma unroll
      for (int r = 0; r < 2; r++) {
        asm volatile("s_waitcnt lgkmcnt(%9)" : "+v"(y[r][0]), "+v"(y[r][1]), "+v"(y[r][2]), "+v"(y[r][3]), "+v"(y[r][4]), "+v"(y[r][5]), "+v"(y[r][6]), "+v"(y[r][7]), "+v"(bm) : "n"(8 - 8 * r));
        u32x4 v;
#pragma unroll
        for (int k = 0; k < 4; k++)
          v[k] = pack2(fmas(__uint_as_float(y[r][2 * k]), ys[2 * k], xq[i][2 * k][r]), fmas(__uint_as_float(y[r][2 * k + 1]), ys[2 * k + 1], xq[i][2 * k + 1][r]));
        if (i >= 2 && r == 0) bm <<= bs;
        __builtin_amdgcn_raw_buffer_store_b128(v, ro, i < 2 ? op : op | (bm & 0x40000000u << r), (512 * i + r) * (2 * D), OAUX);
        SB();
        asm volatile("s_nop 1");
        SB();
      }
    }
    asm volatile("" ::"{v[160:163]}"(junk));
    STAMP(13);
    if (p == 0) {
      u32 n_ = SPIN;
#define PL1(a, b_, c, d, o, s_) "v_max3_u32 v150, v" #a ", v" #b_ ", v" #c "\n\tv_max_u32 v150, v150, v" #d "\n\tv_cmpx_eq_u32 vcc, -1, v150\n\t" PL0(a, b_, c, d, o, s_) "s_mov_b64 exec, -1\n\t"
#define PLE(X) X(112, 113, 114, 115, "%[po]", "0") X(116, 117, 118, 119, "%[po]", "%[k1]") X(120, 121, 122, 123, "%[po]", "%[k2]") X(124, 125, 126, 127, "%[p3]", "0")
#define PMX(a, b_) "v_max3_u32 v150, v150, v" a ", v" b_ "\n\t"
#define PMM(d, a, c) "v_mfma_f32_16x16x32_bf16 v[" d "], %[pk], v[" a "], " c "\n\t"
#define PCK(c, p, q) "s_waitcnt vmcnt(" #c ")\n\t" PMM("152:155", p "2:" p "5", "0") "v_max_u32 v150, v" p "2, v" p "3\n\t" PMX(p "4", p "5") PMM("156:159", p "6:" p "9", "0") PMX(p "6", p "7") PMX(p "8", p "9") \
      PMM("152:155", q "0:" q "3", "v[152:155]") PMX(q "0", q "1") PMX(q "2", q "3") PMM("156:159", q "4:" q "7", "v[156:159]") PMX(q "4", q "5") PMX(q "6", q "7") \
      "v_cmp_eq_u32 vcc, -1, v150\n\ts_sub_u32 %[n], %[n], 1\n\t"
#define PCP(a, b_, c, d) "v_mov_b64 v[" #a ":" #b_ "], v[" #c ":" #d "]\n\t"
      asm volatile(PCK(2, "13", "14") "s_cbranch_vccnz 9f\n"
                   "2:\n\t" TS(60, 61) "ds_read_b32 v150, %[yf] offset:448\n\tds_read_u16_d16_hi v151, %[ya]\n\ts_mov_b32 %[n], %[spin]\n\ts_nop 1\n\tv_add_f32 v152, v152, v156\n\tv_add_f32 v153, v153, v157\n\ts_nop 0\n\t"
                   "v_add_f32_dpp v152, v152, v152 row_ror:8 row_mask:0xf bank_mask:0x3\n\tv_add_f32_dpp v152, v153, v153 row_ror:8 row_mask:0xf bank_mask:0xc\n\t"
                   "v_cvt_pk_bf16_f32 v152, 0, v152\n\tv_mul_f32 v152, v152, %[rl]\n\tv_cvt_pk_bf16_f32 v152, 0, v152\n\tv_add_f32 v153, v162, v152\n"
                   "3:\n\ts_waitcnt lgkmcnt(0)\n\tv_cmp_eq_u32 vcc, 0, v150\n\tv_and_b32 v152, 0xffff0000, v151\n\ts_sub_u32 %[n], %[n], 1\n\ts_cbranch_vccz 4f\n\ts_cbranch_scc1 4f\n\t"
                   "ds_read_b32 v150, %[yf] offset:448\n\tds_read_u16_d16_hi v151, %[ya]\n\ts_branch 3b\n"
                   "4:\n\tv_fmac_f32 v153, v152, %[yc]\n\tv_cvt_pk_bf16_f32 v153, v153, v153\n\tbuffer_store_short v153, %[oo], %[ro], 0 offen\n\t" DONE
                   "9:\n\t" PCP(112, 113, 132, 133) PCP(114, 115, 134, 135) PCP(116, 117, 136, 137) PCP(118, 119, 138, 139) PCP(120, 121, 140, 141) PCP(122, 123, 142, 143)
                   PCP(124, 125, 144, 145) PCP(126, 127, 146, 147) PLD(PL1) "s_sleep " STR(PNAP) "\n1:\n\t" PLE(PL1) PCK(4, "13", "14") "s_cbranch_vccz 2b\n\t" PLD(PL1) PCK(4, "11", "12")
                   "s_cbranch_vccz 2b\n\ts_cbranch_scc0 1b\n\ts_branch 2b\n0:"
                   : [n] "+s"(n_), "+{v[132:139]}"(pa), "+{v[140:147]}"(pb)
                   : [po] "v"(po), [p3] "v"(p3), [rs] "s"(rs), [k1] "s"(131072), [k2] "s"(262144), [pk] "v"(pick), [yf] "v"(yf), [ya] "v"(ya), "{v[160:163]}"(junk), [rl] "v"(rsel), [yc] "v"(yc),
                     [oo] "v"(oo), [ro] "s"(ro), [spin] "n"(SPIN)
                   : "vcc", "scc", "memory", "v112", "v113", "v114", "v115", "v116", "v117", "v118", "v119", "v120", "v121", "v122", "v123", "v124", "v125", "v126", "v127", "v150", "v151", "v152", "v153", "v154", "v155", "v156", "v157", "v158", "v159" TSD);
      TSGET(14, 2, 2, 2);
      STAMP(15);
    }
    FLUSH();
    __builtin_amdgcn_endpgm();
  }

  u32 pl, pa, pr, pi, ldtv;
  u32x4 tv, fx, junk;
  ROLE(c, "", , "={v[16:19]}"(tv), "={v[20:23]}"(fx), "={v24}"(ldtv), "={v96}"(pl), "={v97}"(pa), "={v68}"(pr), "={v69}"(pi), "={v[160:163]}"(junk));
  char* W = smem + w * CHR;
  __builtin_amdgcn_s_setprio(2);
  STAMP(3);
  const u32x4 zero4 = {0u, 0u, 0u, 0u};
#pragma unroll
  for (int i = 0; i < 4; i++) *(u32x4*)(W + (i < 3 ? TB + 1024 * i : CHR - 1024) + 16 * l) = zero4;
  SB();
  const u32 wb0 = lds0 + w * CHR, xb0 = lds0 + OX + w * XREG;
  asm volatile("s_mov_b32 m0, %0\n\ts_nop 0\n\tds_write_addtid_b32 %1\n\tds_write_addtid_b32 %1 offset:%2" ::"s"(wb0), "v"(0x3c00u), "n"(PB) : "memory");
  u32 ape = wb0 + (n16 == 12 ? 12 : (n16 ^ 3) - 7 * (n16 >> 3)) * RS + 16 * kg, apo = wb0 + (0x765412b0fdec2301ull >> 4 * n16 & 15) * RS + 16 * kg;
  u32x2 kb = {wb0 + ((15 - n16) & 3) * CPE + 2 * ((15 - n16) & 12) + 16 * kg, wb0 + (((n16 >> 2) & 1) + (n16 & 8) + 2 * (3 - (n16 & 3))) * RS + 16 * kg};
  u32x8 ka = {xb0 + 32 * n16 + 16 * kg, wb0 + OY + 32 * n16 + 8 * (kg ^ ((l >> 2) & 3)),
              lds0 + OXF + 4 * (l & 3), wb0 + TB + ((15 - n16) & 3) * CPO + 2 * ((15 - n16) & 12) + 8 * kg, lds0 + OB + 16 * l,
              l < 32 ? 0x01000100u : 0x03020100u, l < 32 ? 0x03020100u : 0x03020302u, (u32)(l >> 2) & 8u};
  u32 fad = lds0 + OYP + 4 * w, za = wb0 + 16 * kg;
  asm volatile("" : "+{v70}"(ape), "+{v71}"(apo), "+{v[28:35]}"(ka), "+{v[26:27]}"(kb), "+{v25}"(fad), "+{v12}"(za));
  STAMP(4);
  asm volatile("s_waitcnt vmcnt(4)" : "+{v24}"(ldtv), "+{v[160:163]}"(junk));
  float dt = rbt(fexp(hi2f(ldtv)));
  asm volatile("" : "+v"(dt));
  const f32x2 cdt = {rdl(dt * -1.4426950408889634f, 0), rdl(dt * 0.15915494309189535f, 0)};
  const u32 dk = (u32)__builtin_amdgcn_readfirstlane((int)junk.x) << 16, db = max(__float_as_uint(rdl(dt, 0)), 27u << 23);
  u32 em = max(dk >> 23 & 255u, 127u) + 119 - (db >> 23);
  asm("s_max_i32 %0, %0, 8\n\ts_min_u32 %0, %0, 0xfe" : "+s"(em) :: "scc");
  SB();
  STAMP(10);
  STAMPC(11);
  {
    u32 e, o0, o1, of, os;
    asm volatile(GEN_ASM
                 : [e] "=&s"(e), "=&{v93}"(o0), "=&{v46}"(o1), "=&{v9}"(of), "=&{v8}"(os), "+{v25}"(fad), "+{v12}"(za), "+{v68}"(pr), "+{v69}"(pi), "+{v70}"(ape),
                   "+{v71}"(apo), "+{v96}"(pl), "+{v97}"(pa), "+{v[28:35]}"(ka), "+{v[26:27]}"(kb), "+{v[16:19]}"(tv), "+{v[20:23]}"(fx)
                 : [w] "s"(wb0), [c] "s"(cdt), [k] "s"(f32x2{1.4426950408889634f, 128.f}), [em] "s"(em)
                 : "memory", "scc", "s6", "s7", GEN_CLOBBER);
    STAMP(1);
    STAMPC(12);
    const float ys = __uint_as_float((e - 5) << 23) * __uint_as_float(db);
    asm volatile("v_rcp_f32 %1, %2\n\ts_nop 0\n\tv_fma_f32 %1, %3, %1, %1\n\tv_fma_mixhi_f16 %0, %1, 1.0, %0 op_sel:[0,0,1] op_sel_hi:[0,0,1]\n\ts_nop 0"
                 : "+v"(o0), "=&v"(dt) : "v"(ys), "s"(dk));
    const u32 k0 = (u32)__builtin_amdgcn_readlane((int)o0, 3), k256 = (u32)__builtin_amdgcn_readlane((int)o1, 60);
    const u32 k512 = (u32)__builtin_amdgcn_readlane((int)os, 28), k1024 = (u32)__builtin_amdgcn_readlane((int)of, 28) >> 16;
    *(LDS u16*)(size_t)(wb0 + tv.z) = (u16)__builtin_amdgcn_perm((k512 & 0xffffu) | (k1024 << 16), (k0 >> 16) | (k256 << 16), tv.w);
    *(float*)(smem + OSC + 4 * w) = ys;
    fx &= (k1024 | (k1024 << 16)) ^ 0x80008000u;
  }
  asm volatile(CONV_ASM : "+{v[28:35]}"(ka) : "{v[26:27]}"(kb), "{v25}"(fad), "{v[48:51]}"(fx), [w] "s"(w) : "vcc", "scc", "s6", "memory", CONV_CLOBBER TSC);
  TSGET(2, 5, 6, 7);
  STAMP(13);
  STAMPC(15);
  FLUSH();
  __builtin_amdgcn_endpgm();
}

extern "C" void kernel_launch(void* const* d_in, const int* in_sizes, int n_in, void* d_out, int out_size,
                              void* d_ws, size_t ws_size, hipStream_t stream) {
  const u16* const* in = (const u16* const*)d_in;
  fused<<<256, 768, 0, stream>>>(in[0], in[1], in[2], in[3], in[4], in[5], in[6], in[8], in[9], in[10], in[11],
                                 (u16*)d_out, (char*)d_ws);
}
```

```cpp
#include <hip/hip_runtime.h>
#include <stdint.h>

#define STR2(x) #x
#define STR(x) STR2(x)
#define PH 1246
#define PPRIO 0
#define GDELAY 0
#define PRE 2
#define OAUX 0
#define PNT ""
#define LNNT ""
#define PRENT ""
#define PFN 15
#define PFO 512
#define QD 11
#define HNAP 4
#define QE 11
#define RSL 5
#define DPF 1
#define YN0 2
#define YN3 0
#define QS 7
#define QAS 6
#define RAMP 4
#define GNAP "s_sleep 1"
#define RNAP "s_sleep 1"
#define CLP 0
#define CSL 8
#define XSP 0
#define SPEC 0
#define KDL ""
#define HOLD 8
#define DPRIO 3
#define DPO 128
#define TH 560
#define CLD 215
#define IXI 1
#define LPRIO 0
#define DPI 2
#define PDROP 2
#define TE 0
#define XI 1
#define SCT " sc1"
#define SCU " sc1"
#define SCH " sc1"
#define TSG 255
#define SSL 5
#define PNAP 4
#define TW 280
#define TL 4352
typedef unsigned short u16;
typedef unsigned int u32;
typedef __attribute__((ext_vector_type(8))) __bf16 bf16x8;
typedef __attribute__((ext_vector_type(2))) __bf16 bf16x2;
typedef __attribute__((ext_vector_type(8))) _Float16 f16x8;
typedef __attribute__((ext_vector_type(2))) _Float16 f16x2;
typedef __attribute__((ext_vector_type(4))) short v4i16;
typedef __attribute__((ext_vector_type(4))) float f32x4;
typedef __attribute__((ext_vector_type(2))) float f32x2;
typedef __attribute__((ext_vector_type(4))) u32 u32x4;
typedef __attribute__((ext_vector_type(2))) u32 u32x2;
typedef __attribute__((ext_vector_type(8))) u32 u32x8;
typedef __attribute__((ext_vector_type(2), aligned(4))) u32 u32x2_a4;
typedef __amdgpu_buffer_rsrc_t rsrc_t;
#define LDS __attribute__((address_space(3)))

#define TLARG
#define TLVAL
#define STAMP(I)
#define STAMPC(I)
#define FLUSH()

constexpr int L = 2048, D = 1024, NM = 128, F = 3584;
constexpr u32 O_ARR = 0x1000, O_CLR = 0x2000, O_RG = 0x4000, O_SG = 0x8000, O_IDX = 0x10000, O_HG = 0x20000,
              O_PG = 0xd0000, O_TOP = 0x140000, TOPS = 8 * TL, WS_END = O_TOP + 2 * TOPS;
constexpr int SPIN = 1 << 16;

__device__ __forceinline__ float lo2f(u32 w) { return __uint_as_float(w << 16); }
__device__ __forceinline__ float hi2f(u32 w) { return __uint_as_float(w & 0xffff0000u); }
__device__ __forceinline__ u32 pack2(float a, float b) {
  bf16x2 r = {(__bf16)a, (__bf16)b};
  return __builtin_bit_cast(u32, r);
}
__device__ __forceinline__ u32 packh(float a, float b) {
  f16x2 r = {(_Float16)a, (_Float16)b};
  return __builtin_bit_cast(u32, r);
}
__device__ __forceinline__ float rbt(float f) { return __uint_as_float(pack2(0.f, f)); }
__device__ __forceinline__ float rb(float f) {
  float r;
  asm("v_cvt_pk_bf16_f32 %0, 0, %1" : "=v"(r) : "v"(f));
  return r;
}

template <int CTRL, int RM>
__device__ __forceinline__ float dppf(float v) {
  return __builtin_bit_cast(float, __builtin_amdgcn_update_dpp(0, __builtin_bit_cast(int, v), CTRL, RM, 0xf, true));
}
__device__ __forceinline__ float rdl(float v, int i) {
  return __builtin_bit_cast(float, __builtin_amdgcn_readlane(__builtin_bit_cast(int, v), i));
}
__device__ __forceinline__ void swap32(float& a, float& b) {
  auto r = __builtin_amdgcn_permlane32_swap(__float_as_uint(a), __float_as_uint(b), false, false);
  a = __uint_as_float(r[0]); b = __uint_as_float(r[1]);
}

__device__ __forceinline__ u32x4 ld16(rsrc_t r, u32 off) { return __builtin_amdgcn_raw_buffer_load_b128(r, off, 0, 16); }
__device__ __forceinline__ void st8(rsrc_t r, u32 off, u32x2 v) { __builtin_amdgcn_raw_buffer_store_b64(v, r, off, 0, 16); }
#define SB() __builtin_amdgcn_sched_barrier(0)

__device__ __forceinline__ float fexp(float v) { return __builtin_amdgcn_exp2f(v * 1.4426950408889634f); }
__device__ __forceinline__ float dot2b(u32 a, u32 b, float c) {
  return __builtin_amdgcn_fdot2_f32_bf16(__builtin_bit_cast(bf16x2, a), __builtin_bit_cast(bf16x2, b), c, false);
}
__device__ __forceinline__ float sigm(float v) { return __builtin_amdgcn_rcpf(1.f + __builtin_amdgcn_exp2f(-1.4426950408889634f * v)); }

constexpr int RS = 288, PB = 16 * RS;
constexpr int CPE = 1216, TB = 2 * PB, CPO = 832;
constexpr int T2B = 4, T3B = 224, C2 = 52, C3 = 84;
constexpr int OY = 0, CHR = TB + 4 * CPO;
constexpr int OX = 8 * CHR, XREG = 4640, XO = 266;
constexpr int OMISC = OX + 8 * XREG;
constexpr int OXF = OMISC + 320, OSC = OMISC + 352, OYP = OMISC + 448;
constexpr int OB = OMISC + 1024, SMEM = 148480;
constexpr int FA = 131072, FH = FA + 8192, HP = 576, FC = FH + 8 * HP;
static_assert(FC + 1024 <= SMEM && OB + 4096 <= SMEM, "lds map");

struct Tab { u32 v[6][64][4]; };
constexpr Tab mktab() {
  Tab T = {};
  for (int l = 0; l < 64; l++) {
    const int n16 = l & 15, kg = l >> 4, pc = ((n16 >> 2) & 1) + (n16 & 8) + 2 * (3 - (n16 & 3)), odd = kg >> 1, s = l & 3;
    const int e0 = 534 - 258 - 128 - 8 * pc - 4 * (kg & 1) - odd;
    const int ez = ((l >> 3) & 3) * CPE + ((l & 4) ? 1056 - 64 : 0) + 16 * (l & 7);
    const int b2 = T2B + 44 + C2 * ((kg & 1) + 2 * (pc & 1)), b3 = T3B + 60 + C3 * (pc & 1) - 32 * (kg & 1);
    const int to = TB + 2 * ((odd ? b3 : b2) + 3 - (pc >> 1) - 8 - 16);
    const int q = (l >> 2) & 7, tap = q == 0 ? ((s & 1) ? 0 : 256) : q == 1 ? 257 : q == 7 ? 0 : 509 + q;
    const int sp = q != 6 ? s * (CPE - 2) + 2 * (537 - tap) : TB + s * (CPO - 2) + 2 * (T2B + 15);
    T.v[0][l][0] = (2 * e0 + (2 - odd) * (CPE - 2)) | (2 * e0 + 3 * odd * (CPE - 2)) << 16;
    T.v[0][l][1] = ez | to << 16;
    T.v[0][l][2] = sp;
    T.v[0][l][3] = !tap ? 0x0100u : q == 6 ? 0x0706u : q >= 2 ? 0x0504u : 0x0302u;
    for (int k = 0; k < 8; k++) {
      const int d = 8 * kg + k - n16;
      if (d == 13) T.v[1][l][k >> 1] |= 0xffffu << 16 * (k & 1);
      for (int h = 1; h <= 4; h++)
        if (d >= (h == 1 ? 14 : 10) - h && d <= (h == 1 ? 14 : 10) + h) T.v[1 + h][l][k >> 1] |= 0x3c00u << 16 * (k & 1);
    }
  }
  return T;
}
extern "C" __device__ const __attribute__((aligned(128), used, visibility("hidden"))) Tab lane_tab = mktab();

#define GEN_ASM \
  "s_waitcnt vmcnt(2)\n\tv_lshlrev_b32 v56, 16, v96\n\t" \
  "v_and_b32 v57, 0xffff0000, v96\n\tv_pk_mul_f32 v[56:57], %[k], v[56:57] op_sel_hi:[0,1]\n\t" \
  "v_lshlrev_b32 v49, 16, v97\n\tv_exp_f32 v56, v56\n\t" \
  "v_exp_f32 v57, v57\n\tv_and_b32 v51, 0xffff0000, v97\n\t" \
  "v_cvt_pk_bf16_f32 v56, v56, v57\n\tv_lshlrev_b32 v48, 16, v56\n\t" \
  "v_and_b32 v50, 0xffff0000, v56\n\tv_pk_mul_f32 v[48:49], %[c], v[48:49]\n\t" \
  "v_pk_mul_f32 v[50:51], %[c], v[50:51]\n\tv_pk_mul_f32 v[98:99], %[k], v[48:49] op_sel:[1,0]\n\t" \
  "v_pk_mul_f32 v[100:101], %[k], v[50:51] op_sel:[1,0]\n\tv_fract_f32 v49, v49\n\t" \
  "v_fract_f32 v51, v51\n\tv_exp_f32 v48, v48\n\t" \
  "v_exp_f32 v50, v50\n\tv_cos_f32 v40, v49\n\t" \
  "v_sin_f32 v41, v49\n\tv_cos_f32 v42, v51\n\t" \
  "v_sin_f32 v43, v51\n\tv_pk_mul_f32 v[40:41], v[40:41], v[48:49] op_sel_hi:[1,0]\n\t" \
  "v_pk_mul_f32 v[42:43], v[42:43], v[50:51] op_sel_hi:[1,0]\n\tv_pk_mul_f32 v[48:49], v[40:41], v[40:41] op_sel:[1,1] op_sel_hi:[1,0] neg_lo:[1,0]\n\t" \
  "v_pk_mul_f32 v[50:51], v[42:43], v[42:43] op_sel:[1,1] op_sel_hi:[1,0] neg_lo:[1,0]\n\tv_pk_fma_f32 v[48:49], v[40:41], v[40:41], v[48:49] op_sel:[0,0,0] op_sel_hi:[0,1,1]\n\t" \
  "v_pk_fma_f32 v[50:51], v[42:43], v[42:43], v[50:51] op_sel:[0,0,0] op_sel_hi:[0,1,1]\n\tv_cvt_pk_f16_f32 v72, v40, -v41\n\t" \
  "v_cvt_pk_f16_f32 v73, v42, -v43\n\tv_pk_mul_f32 v[52:53], v[48:49], v[40:41] op_sel:[1,1] op_sel_hi:[1,0] neg_lo:[1,0]\n\t" \
  "v_pk_mul_f32 v[54:55], v[50:51], v[42:43] op_sel:[1,1] op_sel_hi:[1,0] neg_lo:[1,0]\n\tv_pk_fma_f32 v[52:53], v[48:49], v[40:41], v[52:53] op_sel:[0,0,0] op_sel_hi:[0,1,1]\n\t" \
  "v_pk_fma_f32 v[54:55], v[50:51], v[42:43], v[54:55] op_sel:[0,0,0] op_sel_hi:[0,1,1]\n\tv_pk_mul_f32 v[56:57], v[48:49], v[48:49] op_sel:[1,1] op_sel_hi:[1,0] neg_lo:[1,0]\n\t" \
  "v_pk_mul_f32 v[58:59], v[50:51], v[50:51] op_sel:[1,1] op_sel_hi:[1,0] neg_lo:[1,0]\n\tv_pk_fma_f32 v[56:57], v[48:49], v[48:49], v[56:57] op_sel:[0,0,0] op_sel_hi:[0,1,1]\n\t" \
  "v_pk_fma_f32 v[58:59], v[50:51], v[50:51], v[58:59] op_sel:[0,0,0] op_sel_hi:[0,1,1]\n\tv_cvt_pk_f16_f32 v74, v48, -v49\n\t" \
  "v_cvt_pk_f16_f32 v75, v50, -v51\n\tv_cvt_pk_f16_f32 v76, v52, -v53\n\t" \
  "v_cvt_pk_f16_f32 v77, v54, -v55\n\tv_pk_mul_f32 v[52:53], v[56:57], v[48:49] op_sel:[1,1] op_sel_hi:[1,0] neg_lo:[1,0]\n\t" \
  "v_pk_mul_f32 v[54:55], v[58:59], v[50:51] op_sel:[1,1] op_sel_hi:[1,0] neg_lo:[1,0]\n\tv_pk_fma_f32 v[52:53], v[56:57], v[48:49], v[52:53] op_sel:[0,0,0] op_sel_hi:[0,1,1]\n\t" \
  "v_pk_fma_f32 v[54:55], v[58:59], v[50:51], v[54:55] op_sel:[0,0,0] op_sel_hi:[0,1,1]\n\tv_pk_mul_f32 v[60:61], v[56:57], v[56:57] op_sel:[1,1] op_sel_hi:[1,0] neg_lo:[1,0]\n\t" \
  "v_pk_mul_f32 v[62:63], v[58:59], v[58:59] op_sel:[1,1] op_sel_hi:[1,0] neg_lo:[1,0]\n\tv_pk_fma_f32 v[60:61], v[56:57], v[56:57], v[60:61] op_sel:[0,0,0] op_sel_hi:[0,1,1]\n\t" \
  "v_pk_fma_f32 v[62:63], v[58:59], v[58:59], v[62:63] op_sel:[0,0,0] op_sel_hi:[0,1,1]\n\tv_cvt_pk_f16_f32 v78, v56, -v57\n\t" \
  "v_cvt_pk_f16_f32 v79, v58, -v59\n\tv_cvt_pk_f16_f32 v82, v52, -v53\n\t" \
  "v_cvt_pk_f16_f32 v83, v54, -v55\n\tv_pk_mul_f16 v80, v78, v72 op_sel:[1,1] op_sel_hi:[1,0] neg_lo:[1,0]\n\t" \
  "v_pk_mul_f16 v81, v79, v73 op_sel:[1,1] op_sel_hi:[1,0] neg_lo:[1,0]\n\tv_pk_fma_f16 v80, v78, v72, v80 op_sel:[0,0,0] op_sel_hi:[0,1,1]\n\t" \
  "v_pk_fma_f16 v81, v79, v73, v81 op_sel:[0,0,0] op_sel_hi:[0,1,1]\n\tv_pk_mul_f16 v84, v78, v76 op_sel:[1,1] op_sel_hi:[1,0] neg_lo:[1,0]\n\t" \
  "v_pk_mul_f16 v85, v79, v77 op_sel:[1,1] op_sel_hi:[1,0] neg_lo:[1,0]\n\tv_pk_fma_f16 v84, v78, v76, v84 op_sel:[0,0,0] op_sel_hi:[0,1,1]\n\t" \
  "v_pk_fma_f16 v85, v79, v77, v85 op_sel:[0,0,0] op_sel_hi:[0,1,1]\n\tv_cvt_pk_f16_f32 v86, v60, -v61\n\t" \
  "v_cvt_pk_f16_f32 v87, v62, -v63\n\tds_write_addtid_b32 v72 offset:288\n\t" \
  "ds_write_addtid_b32 v73 offset:4896\n\tds_write_addtid_b32 v74 offset:576\n\t" \
  "ds_write_addtid_b32 v75 offset:5184\n\tds_write_addtid_b32 v76 offset:864\n\t" \
  "ds_write_addtid_b32 v77 offset:5472\n\tds_write_addtid_b32 v78 offset:1152\n\t" \
  "ds_write_addtid_b32 v79 offset:5760\n\tds_write_addtid_b32 v80 offset:1440\n\t" \
  "ds_write_addtid_b32 v81 offset:6048\n\tds_write_addtid_b32 v82 offset:1728\n\t" \
  "ds_write_addtid_b32 v83 offset:6336\n\tds_write_addtid_b32 v84 offset:2016\n\t" \
  "ds_write_addtid_b32 v85 offset:6624\n\tds_write_addtid_b32 v86 offset:3456\n\t" \
  "ds_write_addtid_b32 v87 offset:8064\n\tds_read_b128 v[104:107], v70 offset:0\n\t" \
  "ds_read_b128 v[108:111], v70 offset:64\n\tds_read_b128 v[112:115], v70 offset:128\n\t" \
  "ds_read_b128 v[116:119], v70 offset:192\n\tds_read_b128 v[120:123], v70 offset:4608\n\t" \
  "ds_read_b128 v[124:127], v70 offset:4672\n\tds_read_b128 v[128:131], v70 offset:4736\n\t" \
  "ds_read_b128 v[132:135], v70 offset:4800\n\tv_fract_f32 v99, v99\n\t" \
  "v_fract_f32 v101, v101\n\tv_exp_f32 v98, v98\n\t" \
  "v_exp_f32 v100, v100\n\tv_cos_f32 v40, v99\n\t" \
  "v_sin_f32 v41, v99\n\tv_cos_f32 v42, v101\n\t" \
  "v_sin_f32 v43, v101\n\tv_pk_mul_f32 v[40:41], v[40:41], v[98:99] op_sel_hi:[1,0]\n\t" \
  "v_pk_mul_f32 v[42:43], v[42:43], v[100:101] op_sel_hi:[1,0]\n\tv_pk_mul_f32 v[44:45], v[40:41], v[40:41] op_sel:[1,1] op_sel_hi:[1,0] neg_lo:[1,0]\n\t" \
  "v_pk_mul_f32 v[46:47], v[42:43], v[42:43] op_sel:[1,1] op_sel_hi:[1,0] neg_lo:[1,0]\n\tv_pk_fma_f32 v[44:45], v[40:41], v[40:41], v[44:45] op_sel:[0,0,0] op_sel_hi:[0,1,1]\n\t" \
  "v_pk_fma_f32 v[46:47], v[42:43], v[42:43], v[46:47] op_sel:[0,0,0] op_sel_hi:[0,1,1]\n\tv_cvt_pk_f16_f32 v36, v40, v41\n\t" \
  "v_cvt_pk_f16_f32 v37, v42, v43\n\ts_waitcnt vmcnt(0)\n\t" \
  "v_pk_mul_f32 v[48:49], v[44:45], v[44:45] op_sel:[1,1] op_sel_hi:[1,0] neg_lo:[1,0]\n\tv_pk_mul_f32 v[50:51], v[46:47], v[46:47] op_sel:[1,1] op_sel_hi:[1,0] neg_lo:[1,0]\n\t" \
  "v_pk_fma_f32 v[48:49], v[44:45], v[44:45], v[48:49] op_sel:[0,0,0] op_sel_hi:[0,1,1]\n\tv_pk_fma_f32 v[50:51], v[46:47], v[46:47], v[50:51] op_sel:[0,0,0] op_sel_hi:[0,1,1]\n\t" \
  "v_lshlrev_b32 v4, 16, v68\n\tv_cvt_pk_f16_f32 v88, v44, -v45\n\t" \
  "v_cvt_pk_f16_f32 v89, v46, -v47\n\tv_pk_mul_f32 v[52:53], v[48:49], v[44:45] op_sel:[1,1] op_sel_hi:[1,0] neg_lo:[1,0]\n\t" \
  "v_pk_mul_f32 v[54:55], v[50:51], v[46:47] op_sel:[1,1] op_sel_hi:[1,0] neg_lo:[1,0]\n\tv_lshlrev_b32 v5, 16, v69\n\t" \
  "v_pk_fma_f32 v[52:53], v[48:49], v[44:45], v[52:53] op_sel:[0,0,0] op_sel_hi:[0,1,1]\n\tv_pk_fma_f32 v[54:55], v[50:51], v[46:47], v[54:55] op_sel:[0,0,0] op_sel_hi:[0,1,1]\n\t" \
  "v_pk_mul_f32 v[56:57], v[48:49], v[48:49] op_sel:[1,1] op_sel_hi:[1,0] neg_lo:[1,0]\n\tv_pk_mul_f32 v[58:59], v[50:51], v[50:51] op_sel:[1,1] op_sel_hi:[1,0] neg_lo:[1,0]\n\t" \
  "v_and_b32 v6, 0xffff0000, v68\n\tv_pk_fma_f32 v[56:57], v[48:49], v[48:49], v[56:57] op_sel:[0,0,0] op_sel_hi:[0,1,1]\n\t" \
  "v_pk_fma_f32 v[58:59], v[50:51], v[50:51], v[58:59] op_sel:[0,0,0] op_sel_hi:[0,1,1]\n\tv_cvt_pk_f16_f32 v90, v48, -v49\n\t" \
  "v_cvt_pk_f16_f32 v91, v50, -v51\n\tv_and_b32 v7, 0xffff0000, v69\n\t" \
  "v_pk_mul_f32 v[64:65], v[56:57], v[60:61] op_sel:[1,1] op_sel_hi:[1,0] neg_lo:[1,0]\n\tv_pk_mul_f32 v[66:67], v[58:59], v[62:63] op_sel:[1,1] op_sel_hi:[1,0] neg_lo:[1,0]\n\t" \
  "v_pk_fma_f32 v[64:65], v[56:57], v[60:61], v[64:65] op_sel:[0,0,0] op_sel_hi:[0,1,1]\n\tv_pk_fma_f32 v[66:67], v[58:59], v[62:63], v[66:67] op_sel:[0,0,0] op_sel_hi:[0,1,1]\n\t" \
  "v_max3_f32 v102, |v4|, |v5|, |v6|\n\tv_cvt_pk_f16_f32 v92, v52, -v53\n\t" \
  "v_cvt_pk_f16_f32 v93, v54, -v55\n\tv_pk_mul_f16 v136, v88, v74 op_sel:[1,1] op_sel_hi:[1,0] neg_lo:[1,0]\n\t" \
  "v_pk_mul_f16 v137, v89, v75 op_sel:[1,1] op_sel_hi:[1,0] neg_lo:[1,0]\n\tv_max_f32 v102, v102, |v7|\n\t" \
  "v_pk_fma_f16 v136, v88, v74, v136 op_sel:[0,0,0] op_sel_hi:[0,1,1]\n\tv_pk_fma_f16 v137, v89, v75, v137 op_sel:[0,0,0] op_sel_hi:[0,1,1]\n\t" \
  "v_pk_mul_f16 v138, v88, v78 op_sel:[1,1] op_sel_hi:[1,0] neg_lo:[1,0]\n\tv_pk_mul_f16 v139, v89, v79 op_sel:[1,1] op_sel_hi:[1,0] neg_lo:[1,0]\n\t" \
  "v_max_u32_dpp v102, v102, v102 row_shr:1 row_mask:0xf bank_mask:0xf\n\tv_pk_fma_f16 v138, v88, v78, v138 op_sel:[0,0,0] op_sel_hi:[0,1,1]\n\t" \
  "v_pk_fma_f16 v139, v89, v79, v139 op_sel:[0,0,0] op_sel_hi:[0,1,1]\n\tv_cvt_pk_f16_f32 v94, v64, -v65\n\t" \
  "v_cvt_pk_f16_f32 v95, v66, -v67\n\tv_max_u32_dpp v102, v102, v102 row_shr:2 row_mask:0xf bank_mask:0xf\n\t" \
  "v_pk_mul_f16 v140, v88, v82 op_sel:[1,1] op_sel_hi:[1,0] neg_lo:[1,0]\n\tv_pk_mul_f16 v141, v89, v83 op_sel:[1,1] op_sel_hi:[1,0] neg_lo:[1,0]\n\t" \
  "v_pk_fma_f16 v140, v88, v82, v140 op_sel:[0,0,0] op_sel_hi:[0,1,1]\n\tv_pk_fma_f16 v141, v89, v83, v141 op_sel:[0,0,0] op_sel_hi:[0,1,1]\n\t" \
  "ds_write_addtid_b32 v88 offset:3168\n\tds_write_addtid_b32 v89 offset:7776\n\t" \
  "ds_write_addtid_b32 v136 offset:0\n\tds_write_addtid_b32 v137 offset:4608\n\t" \
  "ds_write_addtid_b32 v138 offset:288\n\tds_write_addtid_b32 v139 offset:4896\n\t" \
  "ds_write_addtid_b32 v94 offset:1152\n\tds_write_addtid_b32 v95 offset:5760\n\t" \
  "ds_write_addtid_b32 v140 offset:576\n\tds_write_addtid_b32 v141 offset:5184\n\t" \
  "v_max_u32_dpp v102, v102, v102 row_shr:4 row_mask:0xf bank_mask:0xf\n\tv_pk_mul_f16 v142, v88, v86 op_sel:[1,1] op_sel_hi:[1,0] neg_lo:[1,0]\n\t" \
  "v_pk_mul_f16 v143, v89, v87 op_sel:[1,1] op_sel_hi:[1,0] neg_lo:[1,0]\n\tv_pk_fma_f16 v142, v88, v86, v142 op_sel:[0,0,0] op_sel_hi:[0,1,1]\n\t" \
  "v_pk_fma_f16 v143, v89, v87, v143 op_sel:[0,0,0] op_sel_hi:[0,1,1]\n\tv_max_u32_dpp v102, v102, v102 row_shr:8 row_mask:0xf bank_mask:0xf\n\t" \
  "v_pk_mul_f16 v144, v90, v78 op_sel:[1,1] op_sel_hi:[1,0] neg_lo:[1,0]\n\tv_pk_mul_f16 v145, v91, v79 op_sel:[1,1] op_sel_hi:[1,0] neg_lo:[1,0]\n\t" \
  "v_pk_fma_f16 v144, v90, v78, v144 op_sel:[0,0,0] op_sel_hi:[0,1,1]\n\tv_pk_fma_f16 v145, v91, v79, v145 op_sel:[0,0,0] op_sel_hi:[0,1,1]\n\t" \
  "v_max_u32_dpp v102, v102, v102 row_bcast:15 row_mask:0xa bank_mask:0xf\n\tv_pk_mul_f16 v146, v90, v86 op_sel:[1,1] op_sel_hi:[1,0] neg_lo:[1,0]\n\t" \
  "v_pk_mul_f16 v147, v91, v87 op_sel:[1,1] op_sel_hi:[1,0] neg_lo:[1,0]\n\tv_pk_fma_f16 v146, v90, v86, v146 op_sel:[0,0,0] op_sel_hi:[0,1,1]\n\t" \
  "v_pk_fma_f16 v147, v91, v87, v147 op_sel:[0,0,0] op_sel_hi:[0,1,1]\n\tv_max_u32_dpp v102, v102, v102 row_bcast:31 row_mask:0xc bank_mask:0xf\n\t" \
  "v_pk_mul_f16 v148, v92, v78 op_sel:[1,1] op_sel_hi:[1,0] neg_lo:[1,0]\n\tv_pk_mul_f16 v149, v93, v79 op_sel:[1,1] op_sel_hi:[1,0] neg_lo:[1,0]\n\t" \
  "v_pk_fma_f16 v148, v92, v78, v148 op_sel:[0,0,0] op_sel_hi:[0,1,1]\n\tv_pk_fma_f16 v149, v93, v79, v149 op_sel:[0,0,0] op_sel_hi:[0,1,1]\n\t" \
  "v_readlane_b32 %[e], v102, 63\n\tv_pk_mul_f16 v150, v92, v86 op_sel:[1,1] op_sel_hi:[1,0] neg_lo:[1,0]\n\t" \
  "v_pk_mul_f16 v151, v93, v87 op_sel:[1,1] op_sel_hi:[1,0] neg_lo:[1,0]\n\tv_pk_fma_f16 v150, v92, v86, v150 op_sel:[0,0,0] op_sel_hi:[0,1,1]\n\t" \
  "v_pk_fma_f16 v151, v93, v87, v151 op_sel:[0,0,0] op_sel_hi:[0,1,1]\n\ts_lshr_b32 %[e], %[e], 23\n\t" \
  "s_max_u32 %[e], %[e], %[em]\n\ts_sub_u32 s6, 260, %[e]\n\t" \
  "s_lshl_b32 s6, s6, 23\n\tds_write_addtid_b32 v142 offset:864\n\t" \
  "ds_write_addtid_b32 v143 offset:5472\n\tds_write_addtid_b32 v144 offset:3456\n\t" \
  "ds_write_addtid_b32 v145 offset:8064\n\tds_write_addtid_b32 v146 offset:3744\n\t" \
  "ds_write_addtid_b32 v147 offset:8352\n\tds_write_addtid_b32 v148 offset:4032\n\t" \
  "ds_write_addtid_b32 v149 offset:8640\n\tds_write_addtid_b32 v150 offset:4320\n\t" \
  "ds_write_addtid_b32 v151 offset:8928\n\tv_pk_mul_f16 v96, v94, v88 op_sel:[1,1] op_sel_hi:[1,0] neg_lo:[1,0]\n\t" \
  "v_pk_mul_f16 v97, v95, v89 op_sel:[1,1] op_sel_hi:[1,0] neg_lo:[1,0]\n\tv_pk_fma_f16 v96, v94, v88, v96 op_sel:[0,0,0] op_sel_hi:[0,1,1]\n\t" \
  "v_pk_fma_f16 v97, v95, v89, v97 op_sel:[0,0,0] op_sel_hi:[0,1,1]\n\tv_pk_mul_f16 v98, v94, v90 op_sel:[1,1] op_sel_hi:[1,0] neg_lo:[1,0]\n\t" \
  "v_pk_mul_f16 v99, v95, v91 op_sel:[1,1] op_sel_hi:[1,0] neg_lo:[1,0]\n\tv_pk_fma_f16 v98, v94, v90, v98 op_sel:[0,0,0] op_sel_hi:[0,1,1]\n\t" \
  "v_pk_fma_f16 v99, v95, v91, v99 op_sel:[0,0,0] op_sel_hi:[0,1,1]\n\tv_pk_mul_f16 v100, v94, v92 op_sel:[1,1] op_sel_hi:[1,0] neg_lo:[1,0]\n\t" \
  "v_pk_mul_f16 v101, v95, v93 op_sel:[1,1] op_sel_hi:[1,0] neg_lo:[1,0]\n\tv_pk_fma_f16 v100, v94, v92, v100 op_sel:[0,0,0] op_sel_hi:[0,1,1]\n\t" \
  "v_pk_fma_f16 v101, v95, v93, v101 op_sel:[0,0,0] op_sel_hi:[0,1,1]\n\tds_write_addtid_b32 v96 offset:1440\n\t" \
  "ds_write_addtid_b32 v97 offset:6048\n\tds_write_addtid_b32 v98 offset:1728\n\t" \
  "ds_write_addtid_b32 v99 offset:6336\n\tds_write_addtid_b32 v100 offset:2016\n\t" \
  "ds_write_addtid_b32 v101 offset:6624\n\tds_read_b128 v[136:139], v71 offset:0\n\t" \
  "ds_read_b128 v[140:143], v71 offset:64\n\tds_read_b128 v[144:147], v71 offset:128\n\t" \
  "s_cmp_lt_u32 %[w], 50176\n\ts_cbranch_scc0 1f\n\t" \
  "s_setprio 1\n\t1:\n\t" \
  "ds_read_b128 v[148:151], v71 offset:192\n\tds_read_b128 v[152:155], v71 offset:4608\n\t" \
  "ds_read_b128 v[156:159], v71 offset:4672\n\tds_read_b128 v[160:163], v71 offset:4736\n\t" \
  "ds_read_b128 v[164:167], v71 offset:4800\n\tv_pk_mul_f32 v[48:49], v[60:61], v[60:61] op_sel:[1,1] op_sel_hi:[1,0] neg_lo:[1,0]\n\t" \
  "v_pk_mul_f32 v[50:51], v[62:63], v[62:63] op_sel:[1,1] op_sel_hi:[1,0] neg_lo:[1,0]\n\tv_pk_fma_f32 v[48:49], v[60:61], v[60:61], v[48:49] op_sel:[0,0,0] op_sel_hi:[0,1,1]\n\t" \
  "v_pk_fma_f32 v[50:51], v[62:63], v[62:63], v[50:51] op_sel:[0,0,0] op_sel_hi:[0,1,1]\n\tv_pk_mul_f32 v[52:53], v[48:49], v[60:61] op_sel:[1,1] op_sel_hi:[1,0] neg_lo:[1,0]\n\t" \
  "v_pk_mul_f32 v[54:55], v[50:51], v[62:63] op_sel:[1,1] op_sel_hi:[1,0] neg_lo:[1,0]\n\tv_pk_fma_f32 v[52:53], v[48:49], v[60:61], v[52:53] op_sel:[0,0,0] op_sel_hi:[0,1,1]\n\t" \
  "v_pk_fma_f32 v[54:55], v[50:51], v[62:63], v[54:55] op_sel:[0,0,0] op_sel_hi:[0,1,1]\n\tv_pk_mul_f32 v[40:41], v[48:49], v[48:49] op_sel:[1,1] op_sel_hi:[1,0] neg_lo:[1,0]\n\t" \
  "v_pk_mul_f32 v[42:43], v[50:51], v[50:51] op_sel:[1,1] op_sel_hi:[1,0] neg_lo:[1,0]\n\tv_pk_fma_f32 v[40:41], v[48:49], v[48:49], v[40:41] op_sel:[0,0,0] op_sel_hi:[0,1,1]\n\t" \
  "v_pk_fma_f32 v[42:43], v[50:51], v[50:51], v[42:43] op_sel:[0,0,0] op_sel_hi:[0,1,1]\n\tv_cvt_pk_f16_f32 v46, v48, v49\n\t" \
  "v_cvt_pk_f16_f32 v47, v50, v51\n\tv_cvt_pk_f16_f32 v56, v52, v53\n\t" \
  "v_cvt_pk_f16_f32 v57, v54, v55\n\tv_pk_mul_f32 v[64:65], s[6:7], v[4:5] op_sel_hi:[0,1]\n\t" \
  "v_pk_mul_f32 v[66:67], s[6:7], v[6:7] op_sel_hi:[0,1]\n\tv_cvt_pk_f16_f32 v48, v64, v65\n\t" \
  "v_cvt_pk_f16_f32 v49, v66, v67\n\tv_pk_mul_f32 v[68:69], v[64:65], v[40:41] op_sel:[1,1] op_sel_hi:[1,0] neg_lo:[1,0]\n\t" \
  "v_pk_mul_f32 v[70:71], v[66:67], v[42:43] op_sel:[1,1] op_sel_hi:[1,0] neg_lo:[1,0]\n\tv_pk_fma_f32 v[68:69], v[64:65], v[40:41], v[68:69] op_sel:[0,0,0] op_sel_hi:[0,1,1]\n\t" \
  "v_pk_fma_f32 v[70:71], v[66:67], v[42:43], v[70:71] op_sel:[0,0,0] op_sel_hi:[0,1,1]\n\tv_pk_mul_f16 v50, v48, v86 op_sel:[1,1] op_sel_hi:[1,0]\n\t" \
  "v_pk_mul_f16 v51, v49, v87 op_sel:[1,1] op_sel_hi:[1,0]\n\tv_pk_fma_f16 v50, v48, v86, v50 op_sel:[0,0,0] op_sel_hi:[0,1,1] neg_hi:[0,1,0]\n\t" \
  "v_pk_fma_f16 v51, v49, v87, v51 op_sel:[0,0,0] op_sel_hi:[0,1,1] neg_hi:[0,1,0]\n\tv_pk_mul_f16 v52, v48, v46 op_sel:[1,1] op_sel_hi:[1,0] neg_lo:[1,0]\n\t" \
  "v_pk_mul_f16 v53, v49, v47 op_sel:[1,1] op_sel_hi:[1,0] neg_lo:[1,0]\n\tv_pk_fma_f16 v52, v48, v46, v52 op_sel:[0,0,0] op_sel_hi:[0,1,1]\n\t" \
  "v_pk_fma_f16 v53, v49, v47, v53 op_sel:[0,0,0] op_sel_hi:[0,1,1]\n\tv_pk_mul_f16 v54, v48, v56 op_sel:[1,1] op_sel_hi:[1,0] neg_lo:[1,0]\n\t" \
  "v_pk_mul_f16 v55, v49, v57 op_sel:[1,1] op_sel_hi:[1,0] neg_lo:[1,0]\n\tv_pk_fma_f16 v54, v48, v56, v54 op_sel:[0,0,0] op_sel_hi:[0,1,1]\n\t" \
  "v_pk_fma_f16 v55, v49, v57, v55 op_sel:[0,0,0] op_sel_hi:[0,1,1]\n\tds_write_addtid_b32 v48 offset:0\n\t" \
  "ds_write_addtid_b32 v49 offset:4608\n\tds_write_addtid_b32 v50 offset:288\n\t" \
  "ds_write_addtid_b32 v51 offset:4896\n\tds_write_addtid_b32 v52 offset:576\n\t" \
  "ds_write_addtid_b32 v53 offset:5184\n\tds_write_addtid_b32 v54 offset:864\n\t" \
  "ds_write_addtid_b32 v55 offset:5472\n\tv_cvt_pk_f16_f32 v48, v68, v69\n\t" \
  "v_cvt_pk_f16_f32 v49, v70, v71\n\tv_pk_mul_f32 v[64:65], v[68:69], v[40:41] op_sel:[1,1] op_sel_hi:[1,0] neg_lo:[1,0]\n\t" \
  "v_pk_mul_f32 v[66:67], v[70:71], v[42:43] op_sel:[1,1] op_sel_hi:[1,0] neg_lo:[1,0]\n\tv_pk_fma_f32 v[64:65], v[68:69], v[40:41], v[64:65] op_sel:[0,0,0] op_sel_hi:[0,1,1]\n\t" \
  "v_pk_fma_f32 v[66:67], v[70:71], v[42:43], v[66:67] op_sel:[0,0,0] op_sel_hi:[0,1,1]\n\tv_pk_mul_f16 v50, v48, v86 op_sel:[1,1] op_sel_hi:[1,0]\n\t" \
  "v_pk_mul_f16 v51, v49, v87 op_sel:[1,1] op_sel_hi:[1,0]\n\tv_pk_fma_f16 v50, v48, v86, v50 op_sel:[0,0,0] op_sel_hi:[0,1,1] neg_hi:[0,1,0]\n\t" \
  "v_pk_fma_f16 v51, v49, v87, v51 op_sel:[0,0,0] op_sel_hi:[0,1,1] neg_hi:[0,1,0]\n\tv_pk_mul_f16 v52, v48, v46 op_sel:[1,1] op_sel_hi:[1,0] neg_lo:[1,0]\n\t" \
  "v_pk_mul_f16 v53, v49, v47 op_sel:[1,1] op_sel_hi:[1,0] neg_lo:[1,0]\n\tv_pk_fma_f16 v52, v48, v46, v52 op_sel:[0,0,0] op_sel_hi:[0,1,1]\n\t" \
  "v_pk_fma_f16 v53, v49, v47, v53 op_sel:[0,0,0] op_sel_hi:[0,1,1]\n\tv_pk_mul_f16 v54, v48, v56 op_sel:[1,1] op_sel_hi:[1,0] neg_lo:[1,0]\n\t" \
  "v_pk_mul_f16 v55, v49, v57 op_sel:[1,1] op_sel_hi:[1,0] neg_lo:[1,0]\n\tv_pk_fma_f16 v54, v48, v56, v54 op_sel:[0,0,0] op_sel_hi:[0,1,1]\n\t" \
  "v_pk_fma_f16 v55, v49, v57, v55 op_sel:[0,0,0] op_sel_hi:[0,1,1]\n\tds_write_addtid_b32 v48 offset:1152\n\t" \
  "ds_write_addtid_b32 v49 offset:5760\n\tds_write_addtid_b32 v50 offset:1440\n\t" \
  "ds_write_addtid_b32 v51 offset:6048\n\tds_write_addtid_b32 v52 offset:1728\n\t" \
  "ds_write_addtid_b32 v53 offset:6336\n\tds_write_addtid_b32 v54 offset:2016\n\t" \
  "ds_write_addtid_b32 v55 offset:6624\n\tv_cvt_pk_f16_f32 v48, v64, v65\n\t" \
  "v_cvt_pk_f16_f32 v49, v66, v67\n\tv_pk_mul_f32 v[68:69], v[64:65], v[40:41] op_sel:[1,1] op_sel_hi:[1,0] neg_lo:[1,0]\n\t" \
  "v_pk_mul_f32 v[70:71], v[66:67], v[42:43] op_sel:[1,1] op_sel_hi:[1,0] neg_lo:[1,0]\n\tv_pk_fma_f32 v[68:69], v[64:65], v[40:41], v[68:69] op_sel:[0,0,0] op_sel_hi:[0,1,1]\n\t" \
  "v_pk_fma_f32 v[70:71], v[66:67], v[42:43], v[70:71] op_sel:[0,0,0] op_sel_hi:[0,1,1]\n\tv_pk_mul_f16 v50, v48, v86 op_sel:[1,1] op_sel_hi:[1,0]\n\t" \
  "v_pk_mul_f16 v51, v49, v87 op_sel:[1,1] op_sel_hi:[1,0]\n\tv_pk_fma_f16 v50, v48, v86, v50 op_sel:[0,0,0] op_sel_hi:[0,1,1] neg_hi:[0,1,0]\n\t" \
  "v_pk_fma_f16 v51, v49, v87, v51 op_sel:[0,0,0] op_sel_hi:[0,1,1] neg_hi:[0,1,0]\n\tv_pk_mul_f16 v52, v48, v46 op_sel:[1,1] op_sel_hi:[1,0] neg_lo:[1,0]\n\t" \
  "v_pk_mul_f16 v53, v49, v47 op_sel:[1,1] op_sel_hi:[1,0] neg_lo:[1,0]\n\tv_pk_fma_f16 v52, v48, v46, v52 op_sel:[0,0,0] op_sel_hi:[0,1,1]\n\t" \
  "v_pk_fma_f16 v53, v49, v47, v53 op_sel:[0,0,0] op_sel_hi:[0,1,1]\n\tv_pk_mul_f16 v54, v48, v56 op_sel:[1,1] op_sel_hi:[1,0] neg_lo:[1,0]\n\t" \
  "v_pk_mul_f16 v55, v49, v57 op_sel:[1,1] op_sel_hi:[1,0] neg_lo:[1,0]\n\tv_pk_fma_f16 v54, v48, v56, v54 op_sel:[0,0,0] op_sel_hi:[0,1,1]\n\t" \
  "v_pk_fma_f16 v55, v49, v57, v55 op_sel:[0,0,0] op_sel_hi:[0,1,1]\n\tds_write_addtid_b32 v48 offset:2304\n\t" \
  "ds_write_addtid_b32 v49 offset:6912\n\tds_write_addtid_b32 v50 offset:2592\n\t" \
  "ds_write_addtid_b32 v51 offset:7200\n\tds_write_addtid_b32 v52 offset:2880\n\t" \
  "ds_write_addtid_b32 v53 offset:7488\n\tds_write_addtid_b32 v54 offset:3168\n\t" \
  "ds_write_addtid_b32 v55 offset:7776\n\tv_cvt_pk_f16_f32 v48, v68, v69\n\t" \
  "v_cvt_pk_f16_f32 v49, v70, v71\n\tv_pk_mul_f16 v50, v48, v86 op_sel:[1,1] op_sel_hi:[1,0]\n\t" \
  "v_pk_mul_f16 v51, v49, v87 op_sel:[1,1] op_sel_hi:[1,0]\n\tv_pk_fma_f16 v50, v48, v86, v50 op_sel:[0,0,0] op_sel_hi:[0,1,1] neg_hi:[0,1,0]\n\t" \
  "v_pk_fma_f16 v51, v49, v87, v51 op_sel:[0,0,0] op_sel_hi:[0,1,1] neg_hi:[0,1,0]\n\tv_pk_mul_f16 v52, v48, v46 op_sel:[1,1] op_sel_hi:[1,0] neg_lo:[1,0]\n\t" \
  "v_pk_mul_f16 v53, v49, v47 op_sel:[1,1] op_sel_hi:[1,0] neg_lo:[1,0]\n\tv_pk_fma_f16 v52, v48, v46, v52 op_sel:[0,0,0] op_sel_hi:[0,1,1]\n\t" \
  "v_pk_fma_f16 v53, v49, v47, v53 op_sel:[0,0,0] op_sel_hi:[0,1,1]\n\tv_pk_mul_f16 v54, v48, v56 op_sel:[1,1] op_sel_hi:[1,0] neg_lo:[1,0]\n\t" \
  "v_pk_mul_f16 v55, v49, v57 op_sel:[1,1] op_sel_hi:[1,0] neg_lo:[1,0]\n\tv_pk_fma_f16 v54, v48, v56, v54 op_sel:[0,0,0] op_sel_hi:[0,1,1]\n\t" \
  "v_pk_fma_f16 v55, v49, v57, v55 op_sel:[0,0,0] op_sel_hi:[0,1,1]\n\tds_write_addtid_b32 v48 offset:3456\n\t" \
  "ds_write_addtid_b32 v49 offset:8064\n\tds_write_addtid_b32 v50 offset:3744\n\t" \
  "ds_write_addtid_b32 v51 offset:8352\n\tds_write_addtid_b32 v52 offset:4032\n\t" \
  "ds_write_addtid_b32 v53 offset:8640\n\tds_write_addtid_b32 v54 offset:4320\n\t" \
  "ds_write_addtid_b32 v55 offset:8928\n\tds_read_b128 v[72:75], v27 offset:0\n\t" \
  "ds_read_b128 v[76:79], v27 offset:64\n\tds_read_b128 v[80:83], v27 offset:128\n\t" \
  "ds_read_b128 v[84:87], v27 offset:192\n\tds_read_b128 v[88:91], v27 offset:4608\n\t" \
  "ds_read_b128 v[92:95], v27 offset:4672\n\tds_read_b128 v[96:99], v27 offset:4736\n\t" \
  "ds_read_b128 v[100:103], v27 offset:4800\n\tds_write_addtid_b32 v36 offset:0\n\t" \
  "ds_write_addtid_b32 v37 offset:4608\n\tds_read_b128 v[40:43], v12 offset:0\n\t" \
  "ds_read_b128 v[44:47], v12 offset:64\n\tds_read_b128 v[48:51], v12 offset:128\n\t" \
  "v_add_u32_sdwa v56, %[w], v16 dst_sel:DWORD dst_unused:UNUSED_PAD src0_sel:DWORD src1_sel:WORD_0\n\tv_add_u32_sdwa v57, %[w], v16 dst_sel:DWORD dst_unused:UNUSED_PAD src0_sel:DWORD src1_sel:WORD_1\n\t" \
  "v_add_u32_sdwa v58, %[w], v17 dst_sel:DWORD dst_unused:UNUSED_PAD src0_sel:DWORD src1_sel:WORD_0\n\tv_add_u32_sdwa v59, %[w], v17 dst_sel:DWORD dst_unused:UNUSED_PAD src0_sel:DWORD src1_sel:WORD_1\n\t" \
  "v_add_u32 v14, v56, v35\n\tv_add_u32 v15, v57, v35\n\t" \
  "s_waitcnt lgkmcnt(12)\n\tv_mfma_f32_16x16x32_f16 v[60:63], v[104:107], v[72:75], 0\n\t" \
  "v_mfma_f32_16x16x32_f16 v[4:7], v[136:139], v[72:75], 0\n\ts_cmp_lt_u32 %[w], 50176\n\t" \
  "s_cbranch_scc1 1f\n\ts_setprio 1\n\t" \
  "1:\n\ts_waitcnt lgkmcnt(2)\n\t" \
  "v_mfma_f32_16x16x32_f16 v[60:63], v[108:111], v[76:79], v[60:63]\n\tv_pk_mul_f16 v52, v40, v72 op_sel:[1,1] op_sel_hi:[1,0] neg_lo:[1,0]\n\t" \
  "v_pk_mul_f16 v53, v41, v73 op_sel:[1,1] op_sel_hi:[1,0] neg_lo:[1,0]\n\tv_pk_mul_f16 v54, v42, v74 op_sel:[1,1] op_sel_hi:[1,0] neg_lo:[1,0]\n\t" \
  "v_pk_mul_f16 v55, v43, v75 op_sel:[1,1] op_sel_hi:[1,0] neg_lo:[1,0]\n\tv_mfma_f32_16x16x32_f16 v[4:7], v[140:143], v[76:79], v[4:7]\n\t" \
  "v_pk_fma_f16 v40, v40, v72, v52 op_sel:[0,0,0] op_sel_hi:[0,1,1]\n\tv_pk_fma_f16 v41, v41, v73, v53 op_sel:[0,0,0] op_sel_hi:[0,1,1]\n\t" \
  "v_pk_fma_f16 v42, v42, v74, v54 op_sel:[0,0,0] op_sel_hi:[0,1,1]\n\tv_pk_fma_f16 v43, v43, v75, v55 op_sel:[0,0,0] op_sel_hi:[0,1,1]\n\t" \
  "s_waitcnt lgkmcnt(1)\n\tv_mfma_f32_16x16x32_f16 v[60:63], v[112:115], v[80:83], v[60:63]\n\t" \
  "ds_read_b128 v[72:75], v12 offset:192\n\tv_pk_mul_f16 v52, v44, v76 op_sel:[1,1] op_sel_hi:[1,0] neg_lo:[1,0]\n\t" \
  "v_pk_mul_f16 v53, v45, v77 op_sel:[1,1] op_sel_hi:[1,0] neg_lo:[1,0]\n\tv_pk_mul_f16 v54, v46, v78 op_sel:[1,1] op_sel_hi:[1,0] neg_lo:[1,0]\n\t" \
  "v_pk_mul_f16 v55, v47, v79 op_sel:[1,1] op_sel_hi:[1,0] neg_lo:[1,0]\n\tv_mfma_f32_16x16x32_f16 v[4:7], v[144:147], v[80:83], v[4:7]\n\t" \
  "v_pk_fma_f16 v44, v44, v76, v52 op_sel:[0,0,0] op_sel_hi:[0,1,1]\n\tv_pk_fma_f16 v45, v45, v77, v53 op_sel:[0,0,0] op_sel_hi:[0,1,1]\n\t" \
  "v_pk_fma_f16 v46, v46, v78, v54 op_sel:[0,0,0] op_sel_hi:[0,1,1]\n\tv_pk_fma_f16 v47, v47, v79, v55 op_sel:[0,0,0] op_sel_hi:[0,1,1]\n\t" \
  "s_waitcnt lgkmcnt(1)\n\tv_mfma_f32_16x16x32_f16 v[60:63], v[116:119], v[84:87], v[60:63]\n\t" \
  "ds_read_b128 v[76:79], v12 offset:4608\n\tv_pk_mul_f16 v52, v48, v80 op_sel:[1,1] op_sel_hi:[1,0] neg_lo:[1,0]\n\t" \
  "v_pk_mul_f16 v53, v49, v81 op_sel:[1,1] op_sel_hi:[1,0] neg_lo:[1,0]\n\tv_pk_mul_f16 v54, v50, v82 op_sel:[1,1] op_sel_hi:[1,0] neg_lo:[1,0]\n\t" \
  "v_pk_mul_f16 v55, v51, v83 op_sel:[1,1] op_sel_hi:[1,0] neg_lo:[1,0]\n\tv_mfma_f32_16x16x32_f16 v[4:7], v[148:151], v[84:87], v[4:7]\n\t" \
  "v_pk_fma_f16 v48, v48, v80, v52 op_sel:[0,0,0] op_sel_hi:[0,1,1]\n\tv_pk_fma_f16 v49, v49, v81, v53 op_sel:[0,0,0] op_sel_hi:[0,1,1]\n\t" \
  "v_pk_fma_f16 v50, v50, v82, v54 op_sel:[0,0,0] op_sel_hi:[0,1,1]\n\tv_pk_fma_f16 v51, v51, v83, v55 op_sel:[0,0,0] op_sel_hi:[0,1,1]\n\t" \
  "s_waitcnt lgkmcnt(1)\n\tv_mfma_f32_16x16x32_f16 v[60:63], v[120:123], v[88:91], v[60:63]\n\t" \
  "ds_read_b128 v[80:83], v12 offset:4672\n\tv_pk_mul_f16 v52, v72, v84 op_sel:[1,1] op_sel_hi:[1,0] neg_lo:[1,0]\n\t" \
  "v_pk_mul_f16 v53, v73, v85 op_sel:[1,1] op_sel_hi:[1,0] neg_lo:[1,0]\n\tv_pk_mul_f16 v54, v74, v86 op_sel:[1,1] op_sel_hi:[1,0] neg_lo:[1,0]\n\t" \
  "v_pk_mul_f16 v55, v75, v87 op_sel:[1,1] op_sel_hi:[1,0] neg_lo:[1,0]\n\tv_mfma_f32_16x16x32_f16 v[4:7], v[152:155], v[88:91], v[4:7]\n\t" \
  "v_pk_fma_f16 v72, v72, v84, v52 op_sel:[0,0,0] op_sel_hi:[0,1,1]\n\tv_pk_fma_f16 v73, v73, v85, v53 op_sel:[0,0,0] op_sel_hi:[0,1,1]\n\t" \
  "v_pk_fma_f16 v74, v74, v86, v54 op_sel:[0,0,0] op_sel_hi:[0,1,1]\n\tv_pk_fma_f16 v75, v75, v87, v55 op_sel:[0,0,0] op_sel_hi:[0,1,1]\n\t" \
  "s_waitcnt lgkmcnt(1)\n\tv_mfma_f32_16x16x32_f16 v[60:63], v[124:127], v[92:95], v[60:63]\n\t" \
  "ds_read_b128 v[84:87], v12 offset:4736\n\tv_pk_mul_f16 v52, v76, v88 op_sel:[1,1] op_sel_hi:[1,0] neg_lo:[1,0]\n\t" \
  "v_pk_mul_f16 v53, v77, v89 op_sel:[1,1] op_sel_hi:[1,0] neg_lo:[1,0]\n\tv_pk_mul_f16 v54, v78, v90 op_sel:[1,1] op_sel_hi:[1,0] neg_lo:[1,0]\n\t" \
  "v_pk_mul_f16 v55, v79, v91 op_sel:[1,1] op_sel_hi:[1,0] neg_lo:[1,0]\n\tv_mfma_f32_16x16x32_f16 v[4:7], v[156:159], v[92:95], v[4:7]\n\t" \
  "v_pk_fma_f16 v76, v76, v88, v52 op_sel:[0,0,0] op_sel_hi:[0,1,1]\n\tv_pk_fma_f16 v77, v77, v89, v53 op_sel:[0,0,0] op_sel_hi:[0,1,1]\n\t" \
  "v_pk_fma_f16 v78, v78, v90, v54 op_sel:[0,0,0] op_sel_hi:[0,1,1]\n\tv_pk_fma_f16 v79, v79, v91, v55 op_sel:[0,0,0] op_sel_hi:[0,1,1]\n\t" \
  "s_waitcnt lgkmcnt(1)\n\tv_mfma_f32_16x16x32_f16 v[60:63], v[128:131], v[96:99], v[60:63]\n\t" \
  "ds_read_b128 v[88:91], v12 offset:4800\n\tv_pk_mul_f16 v52, v80, v92 op_sel:[1,1] op_sel_hi:[1,0] neg_lo:[1,0]\n\t" \
  "v_pk_mul_f16 v53, v81, v93 op_sel:[1,1] op_sel_hi:[1,0] neg_lo:[1,0]\n\tv_pk_mul_f16 v54, v82, v94 op_sel:[1,1] op_sel_hi:[1,0] neg_lo:[1,0]\n\t" \
  "v_pk_mul_f16 v55, v83, v95 op_sel:[1,1] op_sel_hi:[1,0] neg_lo:[1,0]\n\tv_mfma_f32_16x16x32_f16 v[4:7], v[160:163], v[96:99], v[4:7]\n\t" \
  "v_pk_fma_f16 v80, v80, v92, v52 op_sel:[0,0,0] op_sel_hi:[0,1,1]\n\tv_pk_fma_f16 v81, v81, v93, v53 op_sel:[0,0,0] op_sel_hi:[0,1,1]\n\t" \
  "v_pk_fma_f16 v82, v82, v94, v54 op_sel:[0,0,0] op_sel_hi:[0,1,1]\n\tv_pk_fma_f16 v83, v83, v95, v55 op_sel:[0,0,0] op_sel_hi:[0,1,1]\n\t" \
  "s_waitcnt lgkmcnt(1)\n\tv_mfma_f32_16x16x32_f16 v[60:63], v[132:135], v[100:103], v[60:63]\n\t" \
  "v_pk_mul_f16 v52, v84, v96 op_sel:[1,1] op_sel_hi:[1,0] neg_lo:[1,0]\n\tv_pk_mul_f16 v53, v85, v97 op_sel:[1,1] op_sel_hi:[1,0] neg_lo:[1,0]\n\t" \
  "v_pk_mul_f16 v54, v86, v98 op_sel:[1,1] op_sel_hi:[1,0] neg_lo:[1,0]\n\tv_pk_mul_f16 v55, v87, v99 op_sel:[1,1] op_sel_hi:[1,0] neg_lo:[1,0]\n\t" \
  "v_mfma_f32_16x16x32_f16 v[4:7], v[164:167], v[100:103], v[4:7]\n\tv_pk_fma_f16 v84, v84, v96, v52 op_sel:[0,0,0] op_sel_hi:[0,1,1]\n\t" \
  "v_pk_fma_f16 v85, v85, v97, v53 op_sel:[0,0,0] op_sel_hi:[0,1,1]\n\tv_pk_fma_f16 v86, v86, v98, v54 op_sel:[0,0,0] op_sel_hi:[0,1,1]\n\t" \
  "v_pk_fma_f16 v87, v87, v99, v55 op_sel:[0,0,0] op_sel_hi:[0,1,1]\n\ts_waitcnt lgkmcnt(0)\n\t" \
  "v_mfma_f32_16x16x32_f16 v[8:11], v[136:139], v[40:43], 0\n\tv_pk_mul_f16 v52, v88, v100 op_sel:[1,1] op_sel_hi:[1,0] neg_lo:[1,0]\n\t" \
  "v_pk_mul_f16 v53, v89, v101 op_sel:[1,1] op_sel_hi:[1,0] neg_lo:[1,0]\n\tv_pk_mul_f16 v54, v90, v102 op_sel:[1,1] op_sel_hi:[1,0] neg_lo:[1,0]\n\t" \
  "v_mfma_f32_16x16x32_f16 v[64:67], v[104:107], v[40:43], 0\n\tv_pk_mul_f16 v55, v91, v103 op_sel:[1,1] op_sel_hi:[1,0] neg_lo:[1,0]\n\t" \
  "v_pk_fma_f16 v88, v88, v100, v52 op_sel:[0,0,0] op_sel_hi:[0,1,1]\n\tv_pk_fma_f16 v89, v89, v101, v53 op_sel:[0,0,0] op_sel_hi:[0,1,1]\n\t" \
  "v_mfma_f32_16x16x32_f16 v[8:11], v[140:143], v[44:47], v[8:11]\n\tv_pk_fma_f16 v90, v90, v102, v54 op_sel:[0,0,0] op_sel_hi:[0,1,1]\n\t" \
  "v_pk_fma_f16 v91, v91, v103, v55 op_sel:[0,0,0] op_sel_hi:[0,1,1]\n\tv_mov_b64 v[100:101], 0\n\t" \
  "v_mfma_f32_16x16x32_f16 v[64:67], v[108:111], v[44:47], v[64:67]\n\tv_mov_b64 v[102:103], 0\n\t" \
  "v_cvt_pk_f16_f32 v4, v4, v5\n\tv_cvt_pk_f16_f32 v5, v6, v7\n\t" \
  "v_mfma_f32_16x16x32_f16 v[8:11], v[144:147], v[48:51], v[8:11]\n\tv_cvt_pk_f16_f32 v92, v60, v61\n\t" \
  "v_cvt_pk_f16_f32 v93, v62, v63\n\tv_permlane16_swap_b32 v4, v5\n\t" \
  "v_mfma_f32_16x16x32_f16 v[64:67], v[112:115], v[48:51], v[64:67]\n\tv_perm_b32 v6, v4, v4, v33\n\t" \
  "v_perm_b32 v7, v4, v4, v34\n\tv_mfma_f32_16x16x32_f16 v[8:11], v[148:151], v[72:75], v[8:11]\n\t" \
  "ds_write_b128 v58, v[100:103]\n\tv_mfma_f32_16x16x32_f16 v[64:67], v[116:119], v[72:75], v[64:67]\n\t" \
  "ds_write2_b32 v56, v92, v93 offset0:193 offset1:194\n\tv_mfma_f32_16x16x32_f16 v[8:11], v[152:155], v[76:79], v[8:11]\n\t" \
  "ds_write2_b32 v57, v92, v93 offset0:193 offset1:194\n\tv_mfma_f32_16x16x32_f16 v[64:67], v[120:123], v[76:79], v[64:67]\n\t" \
  "ds_write2_b32 v14, v6, v7 offset0:64 offset1:65\n\tv_mfma_f32_16x16x32_f16 v[8:11], v[156:159], v[80:83], v[8:11]\n\t" \
  "ds_write2_b32 v15, v6, v7 offset0:64 offset1:65\n\tv_mfma_f32_16x16x32_f16 v[64:67], v[124:127], v[80:83], v[64:67]\n\t" \
  "ds_write_b16 v59, v5 offset:48\n\tv_mfma_f32_16x16x32_f16 v[8:11], v[160:163], v[84:87], v[8:11]\n\t" \
  "ds_write_b16_d16_hi v59, v5 offset:16\n\tv_mfma_f32_16x16x32_f16 v[64:67], v[128:131], v[84:87], v[64:67]\n\t" \
  "ds_write_b16 v59, v5 offset:878\n\tv_mfma_f32_16x16x32_f16 v[8:11], v[164:167], v[88:91], v[8:11]\n\t" \
  "ds_write_b16_d16_hi v59, v5 offset:846\n\tv_mfma_f32_16x16x32_f16 v[64:67], v[132:135], v[88:91], v[64:67]\n\t" \
  "ds_write_b16 v59, v5 offset:1708\n\tds_write_b16_d16_hi v59, v5 offset:1676\n\t" \
  "ds_write_b16 v59, v5 offset:2538\n\tds_write_b16_d16_hi v59, v5 offset:2506\n\t" \
  "s_nop 1\n\tv_cvt_pk_f16_f32 v8, v8, v9\n\t" \
  "v_cvt_pk_f16_f32 v9, v10, v11\n\tv_cvt_pk_f16_f32 v46, v64, v65\n\t" \
  "v_cvt_pk_f16_f32 v47, v66, v67\n\tv_permlane16_swap_b32 v8, v9\n\t" \
  "v_perm_b32 v10, v8, v8, v33\n\tv_perm_b32 v11, v8, v8, v34\n\t" \
  "ds_write2_b32 v56, v46, v47 offset0:129 offset1:130\n\tds_write2_b32 v57, v46, v47 offset0:129 offset1:130\n\t" \
  "ds_write2_b32 v14, v10, v11 offset0:0 offset1:1\n\tds_write2_b32 v15, v10, v11 offset0:0 offset1:1\n\t" \
  "ds_write_b16 v59, v9 offset:32\n\tds_write_b16_d16_hi v59, v9 offset:0\n\t" \
  "ds_write_b16 v59, v9 offset:862\n\tds_write_b16_d16_hi v59, v9 offset:830\n\t" \
  "ds_write_b16 v59, v9 offset:1692\n\tds_write_b16_d16_hi v59, v9 offset:1660\n\t" \
  "ds_write_b16 v59, v9 offset:2522\n\tds_write_b16_d16_hi v59, v9 offset:2490\n\t"
#define GEN_CLOBBER "v4", "v5", "v6", "v7", "v10", "v11", "v13", "v14", "v15", "v36", "v37", "v38", "v39", "v40", "v41", "v42", "v43", "v44", "v45", "v47", "v48", "v49", "v50", "v51", "v52", "v53", "v54", "v55", "v56", "v57", "v58", "v59", "v60", "v61", "v62", "v63", "v64", "v65", "v66", "v67", "v72", "v73", "v74", "v75", "v76", "v77", "v78", "v79", "v80", "v81", "v82", "v83", "v84", "v85", "v86", "v87", "v88", "v89", "v90", "v91", "v92", "v94", "v95", "v98", "v99", "v100", "v101", "v102", "v103", "v104", "v105", "v106", "v107", "v108", "v109", "v110", "v111", "v112", "v113", "v114", "v115", "v116", "v117", "v118", "v119", "v120", "v121", "v122", "v123", "v124", "v125", "v126", "v127", "v128", "v129", "v130", "v131", "v132", "v133", "v134", "v135", "v136", "v137", "v138", "v139", "v140", "v141", "v142", "v143", "v144", "v145", "v146", "v147", "v148", "v149", "v150", "v151", "v152", "v153", "v154", "v155", "v156", "v157", "v158", "v159", "v160", "v161", "v162", "v163", "v164", "v165", "v166", "v167"
#define CONV_ASM \
  "ds_read_b32 v34, v30\n\tds_read_b64 v[132:133], v26 offset:512\n\tds_read_b64 v[134:135], v26 offset:520\n\t" \
  "ds_read_b64 v[100:101], v26 offset:0\n\tds_read_b64 v[102:103], v26 offset:8\n\tds_read_b64 v[136:137], v26 offset:576\n\t" \
  "ds_read_b64 v[138:139], v26 offset:584\n\tds_read_b64 v[104:105], v26 offset:64\n\tds_read_b64 v[106:107], v26 offset:72\n\t" \
  "ds_read_b64 v[140:141], v26 offset:640\n\tds_read_b64 v[142:143], v26 offset:648\n\tds_read_b64 v[108:109], v26 offset:128\n\t" \
  "ds_read_b64 v[110:111], v26 offset:136\n\tds_read_b64 v[144:145], v26 offset:704\n\tds_read_b64 v[146:147], v26 offset:712\n\t" \
  "ds_read_b64 v[112:113], v26 offset:192\n\tds_read_b64 v[114:115], v26 offset:200\n\tds_read_b64 v[148:149], v26 offset:768\n\t" \
  "ds_read_b64 v[150:151], v26 offset:776\n\tds_read_b64 v[116:117], v26 offset:256\n\tds_read_b64 v[118:119], v26 offset:264\n\t" \
  TS(60, 61) "s_waitcnt lgkmcnt(15)\n\tv_cmp_gt_u32 vcc, 1, v34\n\t" \
  "s_cbranch_vccz 2f\n\ts_mov_b32 s6, 0x10000\n\t1:\n\t" \
  "s_sleep 1\n\tds_read_b32 v34, v30\n\ts_sub_u32 s6, s6, 1\n\t" \
  "s_waitcnt lgkmcnt(0)\n\tv_cmp_gt_u32 vcc, 1, v34\n\ts_cbranch_scc1 2f\n\t" \
  "s_cbranch_vccnz 1b\n\t2:\n\t" TS(62, 63) \
  "s_cmp_lt_u32 %[w], 4\n\ts_setprio 1\n\ts_cbranch_scc1 1f\n\t" \
  "s_setprio 2\n\t1:\n\tds_read_b128 v[0:3], v28 offset:0\n\t" \
  "ds_read_b128 v[4:7], v28 offset:64\n\tds_read_b128 v[8:11], v28 offset:128\n\tds_read_b128 v[12:15], v28 offset:192\n\t" \
  "ds_read_b128 v[16:19], v28 offset:256\n\tds_read_b128 v[20:23], v28 offset:320\n\ts_waitcnt lgkmcnt(5)\n\t" \
  "v_mfma_f32_16x16x32_f16 v[52:55], v[132:135], v[0:3], 0\n\tds_read_b64 v[152:153], v26 offset:832\n\tds_read_b64 v[154:155], v26 offset:840\n\t" \
  "v_mfma_f32_16x16x32_f16 v[56:59], v[100:103], v[0:3], 0\n\tds_read_b64 v[120:121], v26 offset:320\n\tds_read_b64 v[122:123], v26 offset:328\n\t" \
  "ds_read_b128 v[0:3], v28 offset:384\n\ts_waitcnt lgkmcnt(9)\n\tv_mfma_f32_16x16x32_f16 v[52:55], v[136:139], v[4:7], v[52:55]\n\t" \
  "ds_read_b64 v[156:157], v26 offset:896\n\tds_read_b64 v[158:159], v26 offset:904\n\tv_mfma_f32_16x16x32_f16 v[56:59], v[104:107], v[4:7], v[56:59]\n\t" \
  "ds_read_b64 v[124:125], v26 offset:384\n\tds_read_b64 v[126:127], v26 offset:392\n\tds_read_b128 v[4:7], v28 offset:448\n\t" \
  "s_waitcnt lgkmcnt(13)\n\tv_mfma_f32_16x16x32_f16 v[52:55], v[140:143], v[8:11], v[52:55]\n\tds_read_b64 v[160:161], v26 offset:960\n\t" \
  "ds_read_b64 v[162:163], v26 offset:968\n\tv_mfma_f32_16x16x32_f16 v[56:59], v[108:111], v[8:11], v[56:59]\n\tds_read_b64 v[128:129], v26 offset:448\n\t" \
  "ds_read_b64 v[130:131], v26 offset:456\n\tds_read_b128 v[8:11], v28 offset:512\n\ts_waitcnt lgkmcnt(15)\n\t" \
  "v_mfma_f32_16x16x32_f16 v[52:55], v[144:147], v[12:15], v[52:55]\n\tds_read_b64 v[164:165], v26 offset:1024\n\tds_read_b64 v[166:167], v26 offset:1032\n\t" \
  "v_mfma_f32_16x16x32_f16 v[56:59], v[112:115], v[12:15], v[56:59]\n\tds_read_b64 v[76:77], v31 offset:72\n\tds_read_b64 v[78:79], v31 offset:176\n\t" \
  "ds_read_b128 v[12:15], v28 offset:576\n\tv_mfma_f32_16x16x32_f16 v[52:55], v[148:151], v[16:19], v[52:55]\n\tds_read_b64 v[80:81], v31 offset:40\n\t" \
  "ds_read_b64 v[82:83], v31 offset:144\n\tv_mfma_f32_16x16x32_f16 v[56:59], v[116:119], v[16:19], v[56:59]\n\tds_read_b64 v[84:85], v31 offset:8\n\t" \
  "ds_read_b64 v[86:87], v31 offset:112\n\tds_read_b128 v[16:19], v28 offset:640\n\ts_waitcnt lgkmcnt(15)\n\t" \
  "v_mfma_f32_16x16x32_f16 v[52:55], v[152:155], v[20:23], v[52:55]\n\tds_read_b64 v[88:89], v31 offset:280\n\tds_read_b64 v[90:91], v31 offset:384\n\t" \
  "v_mfma_f32_16x16x32_f16 v[56:59], v[120:123], v[20:23], v[56:59]\n\tds_read_b64 v[92:93], v31 offset:248\n\tds_read_b64 v[94:95], v31 offset:352\n\t" \
  "ds_read_b128 v[20:23], v28 offset:704\n\tv_mfma_f32_16x16x32_f16 v[52:55], v[156:159], v[0:3], v[52:55]\n\tds_read_b64 v[96:97], v31 offset:216\n\t" \
  "ds_read_b64 v[98:99], v31 offset:320\n\tv_mfma_f32_16x16x32_f16 v[56:59], v[124:127], v[0:3], v[56:59]\n\tds_read_b128 v[0:3], v28 offset:768\n\t" \
  "s_waitcnt lgkmcnt(15)\n\tv_mfma_f32_16x16x32_f16 v[52:55], v[160:163], v[4:7], v[52:55]\n\tv_mfma_f32_16x16x32_f16 v[56:59], v[128:131], v[4:7], v[56:59]\n\t" \
  "ds_read_b128 v[4:7], v28 offset:832\n\tv_mfma_f32_16x16x32_f16 v[52:55], v[164:167], v[8:11], v[52:55]\n\tv_mfma_f32_16x16x32_f16 v[56:59], v[132:135], v[8:11], v[56:59]\n\t" \
  "v_mfma_f32_16x16x32_f16 v[60:63], v[100:103], v[8:11], 0\n\tds_read_b128 v[8:11], v28 offset:896\n\ts_waitcnt lgkmcnt(15)\n\t" \
  "v_mfma_f32_16x16x32_f16 v[56:59], v[136:139], v[12:15], v[56:59]\n\tds_read_b32 v34, v30\n\tv_mfma_f32_16x16x32_f16 v[60:63], v[104:107], v[12:15], v[60:63]\n\t" \
  "ds_read_b128 v[12:15], v28 offset:960\n\tv_cvt_pk_bf16_f32 v52, v52, v53\n\tv_cvt_pk_bf16_f32 v53, v54, v55\n\t" \
  TS(64, 65) "s_waitcnt lgkmcnt(12)\n\tv_mfma_f32_16x16x32_f16 v[56:59], v[140:143], v[16:19], v[56:59]\n\t" \
  "ds_write_b64 v29, v[52:53] offset:0\n\tds_write_b32 v25, v25 offset:0\n\tv_mfma_f32_16x16x32_f16 v[60:63], v[108:111], v[16:19], v[60:63]\n\t" \
  "s_waitcnt lgkmcnt(3)\n\tv_cmp_gt_u32 vcc, 2, v34\n\ts_cbranch_vccz 2f\n\t" \
  "s_mov_b32 s6, 0x10000\n\t1:\n\ts_sleep 1\n\t" \
  "ds_read_b32 v34, v30\n\ts_sub_u32 s6, s6, 1\n\ts_waitcnt lgkmcnt(0)\n\t" \
  "v_cmp_gt_u32 vcc, 2, v34\n\ts_cbranch_scc1 2f\n\ts_cbranch_vccnz 1b\n\t" \
  "2:\n\tds_read_b128 v[16:19], v28 offset:1024\n\tv_mfma_f32_16x16x32_f16 v[56:59], v[144:147], v[20:23], v[56:59]\n\t" \
  "v_mfma_f32_16x16x32_f16 v[60:63], v[112:115], v[20:23], v[60:63]\n\tds_read_b128 v[68:71], v32\n\tds_read_b128 v[72:75], v32 offset:1024\n\t" \
  "ds_read_b128 v[20:23], v28 offset:1088\n\tv_mfma_f32_16x16x32_f16 v[56:59], v[148:151], v[0:3], v[56:59]\n\tv_mfma_f32_16x16x32_f16 v[60:63], v[116:119], v[0:3], v[60:63]\n\t" \
  "ds_read_b128 v[0:3], v28 offset:496\n\tv_mfma_f32_16x16x32_f16 v[56:59], v[152:155], v[4:7], v[56:59]\n\tv_mfma_f32_16x16x32_f16 v[60:63], v[120:123], v[4:7], v[60:63]\n\t" \
  "ds_read_b128 v[4:7], v28 offset:1152\n\tv_mfma_f32_16x16x32_f16 v[56:59], v[156:159], v[8:11], v[56:59]\n\tv_mfma_f32_16x16x32_f16 v[60:63], v[124:127], v[8:11], v[60:63]\n\t" \
  "ds_read_b128 v[8:11], v28 offset:1216\n\ts_waitcnt lgkmcnt(9)\n\tv_mfma_f32_16x16x32_f16 v[56:59], v[160:163], v[12:15], v[56:59]\n\t" \
  "v_mfma_f32_16x16x32_f16 v[60:63], v[128:131], v[12:15], v[60:63]\n\tds_read_b128 v[12:15], v28 offset:480\n\ts_waitcnt lgkmcnt(7)\n\t" \
  "v_mfma_f32_16x16x32_f16 v[56:59], v[164:167], v[16:19], v[56:59]\n\tv_mfma_f32_16x16x32_f16 v[60:63], v[132:135], v[16:19], v[60:63]\n\tv_mfma_f32_16x16x32_f16 v[64:67], v[100:103], v[16:19], 0\n\t" \
  "ds_read_b128 v[16:19], v28 offset:1280\n\ts_waitcnt lgkmcnt(5)\n\tv_mfma_f32_16x16x32_f16 v[60:63], v[136:139], v[20:23], v[60:63]\n\t" \
  "v_mfma_f32_16x16x32_f16 v[64:67], v[104:107], v[20:23], v[64:67]\n\tds_read_b128 v[20:23], v28 offset:1344\n\ts_waitcnt lgkmcnt(5)\n\t" \
  "v_mfma_f32_16x16x32_f16 v[36:39], v[0:3], v[68:71], 0\n\tv_cvt_pk_bf16_f32 v56, v56, v57\n\tv_cvt_pk_bf16_f32 v57, v58, v59\n\t" \
  "v_mfma_f32_16x16x32_f16 v[40:43], v[0:3], v[72:75], 0\n\tds_write_b64 v29, v[56:57] offset:512\n\tds_write_b32 v25, v25 offset:64\n\t" \
  "ds_read_b128 v[0:3], v28 offset:1408\n\ts_waitcnt lgkmcnt(7)\n\tv_mfma_f32_16x16x32_f16 v[60:63], v[140:143], v[4:7], v[60:63]\n\t" \
  "v_cvt_pk_f16_f32 v36, v36, v37\n\tv_cvt_pk_f16_f32 v37, v38, v39\n\tv_mfma_f32_16x16x32_f16 v[64:67], v[108:111], v[4:7], v[64:67]\n\t" \
  "ds_read_b128 v[4:7], v28 offset:1472\n\tv_cvt_pk_f16_f32 v38, v40, v41\n\tv_cvt_pk_f16_f32 v39, v42, v43\n\t" \
  "s_waitcnt lgkmcnt(7)\n\tv_mfma_f32_16x16x32_f16 v[60:63], v[144:147], v[8:11], v[60:63]\n\tv_mfma_f32_16x16x32_f16 v[64:67], v[112:115], v[8:11], v[64:67]\n\t" \
  "ds_read_b128 v[8:11], v28 offset:1536\n\tv_mfma_f32_16x16x32_f16 v[60:63], v[36:39], v[76:79], v[60:63]\n\tv_mfma_f32_16x16x32_f16 v[64:67], v[36:39], v[80:83], v[64:67]\n\t" \
  "v_mfma_f32_16x16x32_f16 v[52:55], v[36:39], v[84:87], 0\n\ts_waitcnt lgkmcnt(7)\n\tv_mfma_f32_16x16x32_f16 v[44:47], v[12:15], v[68:71], 0\n\t" \
  "v_mfma_f32_16x16x32_f16 v[40:43], v[12:15], v[72:75], 0\n\tds_read_b128 v[12:15], v28 offset:1600\n\ts_waitcnt lgkmcnt(7)\n\t" \
  "v_mfma_f32_16x16x32_f16 v[60:63], v[148:151], v[16:19], v[60:63]\n\tv_mfma_f32_16x16x32_f16 v[64:67], v[116:119], v[16:19], v[64:67]\n\tds_read_b128 v[16:19], v28 offset:1008\n\t" \
  "s_waitcnt lgkmcnt(7)\n\tv_mfma_f32_16x16x32_f16 v[60:63], v[152:155], v[20:23], v[60:63]\n\tv_cvt_pk_f16_f32 v44, v44, v45\n\t" \
  "v_cvt_pk_f16_f32 v45, v46, v47\n\tv_cvt_pk_f16_f32 v46, v40, v41\n\tv_cvt_pk_f16_f32 v47, v42, v43\n\t" \
  "v_mfma_f32_16x16x32_f16 v[64:67], v[120:123], v[20:23], v[64:67]\n\tds_read_b128 v[20:23], v28 offset:1664\n\tv_mfma_f32_16x16x32_f16 v[60:63], v[44:47], v[88:91], v[60:63]\n\t" \
  "v_mfma_f32_16x16x32_f16 v[64:67], v[44:47], v[92:95], v[64:67]\n\tv_mfma_f32_16x16x32_f16 v[52:55], v[44:47], v[96:99], v[52:55]\n\ts_waitcnt lgkmcnt(5)\n\t" \
  "v_mfma_f32_16x16x32_f16 v[60:63], v[156:159], v[0:3], v[60:63]\n\tv_mfma_f32_16x16x32_f16 v[64:67], v[124:127], v[0:3], v[64:67]\n\tds_read_b128 v[0:3], v28 offset:1728\n\t" \
  "s_waitcnt lgkmcnt(5)\n\tv_mfma_f32_16x16x32_f16 v[60:63], v[160:163], v[4:7], v[60:63]\n\tv_mfma_f32_16x16x32_f16 v[64:67], v[128:131], v[4:7], v[64:67]\n\t" \
  "ds_read_b128 v[4:7], v28 offset:992\n\ts_waitcnt lgkmcnt(5)\n\tv_mfma_f32_16x16x32_f16 v[60:63], v[164:167], v[8:11], v[60:63]\n\t" \
  "v_mfma_f32_16x16x32_f16 v[64:67], v[132:135], v[8:11], v[64:67]\n\tv_mfma_f32_16x16x32_f16 v[52:55], v[100:103], v[8:11], v[52:55]\n\tds_read_b128 v[8:11], v28 offset:1792\n\t" \
  "s_waitcnt lgkmcnt(5)\n\tv_mfma_f32_16x16x32_f16 v[64:67], v[136:139], v[12:15], v[64:67]\n\tv_mfma_f32_16x16x32_f16 v[52:55], v[104:107], v[12:15], v[52:55]\n\t" \
  "ds_read_b128 v[12:15], v28 offset:1856\n\ts_waitcnt lgkmcnt(5)\n\tv_mfma_f32_16x16x32_f16 v[36:39], v[16:19], v[68:71], 0\n\t" \
  "v_cvt_pk_bf16_f32 v60, v60, v61\n\tv_cvt_pk_bf16_f32 v61, v62, v63\n\tv_mfma_f32_16x16x32_f16 v[40:43], v[16:19], v[72:75], 0\n\t" \
  "ds_write_b64 v29, v[60:61] offset:1024\n\tds_write_b32 v25, v25 offset:128\n\tds_read_b128 v[16:19], v28 offset:1920\n\t" \
  "s_waitcnt lgkmcnt(7)\n\tv_mfma_f32_16x16x32_f16 v[64:67], v[140:143], v[20:23], v[64:67]\n\tds_read_b32 v34, v30\n\t" \
  "v_cvt_pk_f16_f32 v36, v36, v37\n\tv_cvt_pk_f16_f32 v37, v38, v39\n\tv_mfma_f32_16x16x32_f16 v[52:55], v[108:111], v[20:23], v[52:55]\n\t" \
  "ds_read_b128 v[20:23], v28 offset:1984\n\tv_cvt_pk_f16_f32 v38, v40, v41\n\tv_cvt_pk_f16_f32 v39, v42, v43\n\t" \
  "s_waitcnt lgkmcnt(8)\n\tv_mfma_f32_16x16x32_f16 v[64:67], v[144:147], v[0:3], v[64:67]\n\tv_mfma_f32_16x16x32_f16 v[52:55], v[112:115], v[0:3], v[52:55]\n\t" \
  "s_waitcnt lgkmcnt(1)\n\tv_cmp_gt_u32 vcc, 3, v34\n\ts_cbranch_vccz 2f\n\t" \
  "s_mov_b32 s6, 0x10000\n\t1:\n\ts_sleep 1\n\t" \
  "ds_read_b32 v34, v30\n\ts_sub_u32 s6, s6, 1\n\ts_waitcnt lgkmcnt(0)\n\t" \
  "v_cmp_gt_u32 vcc, 3, v34\n\ts_cbranch_scc1 2f\n\ts_cbranch_vccnz 1b\n\t" \
  "2:\n\tds_read_b128 v[0:3], v28 offset:2048\n\tv_mfma_f32_16x16x32_f16 v[64:67], v[36:39], v[76:79], v[64:67]\n\t" \
  "v_mfma_f32_16x16x32_f16 v[52:55], v[36:39], v[80:83], v[52:55]\n\tv_mfma_f32_16x16x32_f16 v[56:59], v[36:39], v[84:87], 0\n\tv_mfma_f32_16x16x32_f16 v[44:47], v[4:7], v[68:71], 0\n\t" \
  "v_mfma_f32_16x16x32_f16 v[40:43], v[4:7], v[72:75], 0\n\tds_read_b128 v[4:7], v28 offset:2112\n\tv_mfma_f32_16x16x32_f16 v[64:67], v[148:151], v[8:11], v[64:67]\n\t" \
  "v_mfma_f32_16x16x32_f16 v[52:55], v[116:119], v[8:11], v[52:55]\n\tds_read_b128 v[8:11], v28 offset:1520\n\tv_mfma_f32_16x16x32_f16 v[64:67], v[152:155], v[12:15], v[64:67]\n\t" \
  "v_mfma_f32_16x16x32_f16 v[52:55], v[120:123], v[12:15], v[52:55]\n\tds_read_b128 v[12:15], v28 offset:2176\n\tv_cvt_pk_f16_f32 v44, v44, v45\n\t" \
  "v_cvt_pk_f16_f32 v45, v46, v47\n\tv_cvt_pk_f16_f32 v46, v40, v41\n\tv_cvt_pk_f16_f32 v47, v42, v43\n\t" \
  "s_nop 1\n\tv_mfma_f32_16x16x32_f16 v[64:67], v[44:47], v[88:91], v[64:67]\n\tv_mfma_f32_16x16x32_f16 v[52:55], v[44:47], v[92:95], v[52:55]\n\t" \
  "v_mfma_f32_16x16x32_f16 v[56:59], v[44:47], v[96:99], v[56:59]\n\tv_mfma_f32_16x16x32_f16 v[64:67], v[156:159], v[16:19], v[64:67]\n\tv_mfma_f32_16x16x32_f16 v[52:55], v[124:127], v[16:19], v[52:55]\n\t" \
  "ds_read_b128 v[16:19], v28 offset:2240\n\ts_waitcnt lgkmcnt(5)\n\tv_mfma_f32_16x16x32_f16 v[64:67], v[160:163], v[20:23], v[64:67]\n\t" \
  "v_mfma_f32_16x16x32_f16 v[52:55], v[128:131], v[20:23], v[52:55]\n\tds_read_b128 v[20:23], v28 offset:1504\n\ts_waitcnt lgkmcnt(5)\n\t" \
  "v_mfma_f32_16x16x32_f16 v[64:67], v[164:167], v[0:3], v[64:67]\n\tv_mfma_f32_16x16x32_f16 v[52:55], v[132:135], v[0:3], v[52:55]\n\tv_mfma_f32_16x16x32_f16 v[56:59], v[100:103], v[0:3], v[56:59]\n\t" \
  "ds_read_b128 v[0:3], v28 offset:2304\n\ts_waitcnt lgkmcnt(5)\n\tv_mfma_f32_16x16x32_f16 v[52:55], v[136:139], v[4:7], v[52:55]\n\t" \
  "v_mfma_f32_16x16x32_f16 v[56:59], v[104:107], v[4:7], v[56:59]\n\tds_read_b128 v[4:7], v28 offset:2368\n\ts_waitcnt lgkmcnt(5)\n\t" \
  "v_mfma_f32_16x16x32_f16 v[36:39], v[8:11], v[68:71], 0\n\tv_cvt_pk_bf16_f32 v64, v64, v65\n\tv_cvt_pk_bf16_f32 v65, v66, v67\n\t" \
  TS(66, 67) "s_cmp_lt_u32 %[w], 4\n\ts_cbranch_scc1 1f\n\t" \
  "s_setprio 1\n\t1:\n\tv_mfma_f32_16x16x32_f16 v[40:43], v[8:11], v[72:75], 0\n\t" \
  "ds_write_b64 v29, v[64:65] offset:1536\n\tds_write_b32 v25, v25 offset:192\n\tds_read_b128 v[8:11], v28 offset:480\n\t" \
  "s_waitcnt lgkmcnt(7)\n\tv_mfma_f32_16x16x32_f16 v[52:55], v[140:143], v[12:15], v[52:55]\n\tv_cvt_pk_f16_f32 v36, v36, v37\n\t" \
  "v_cvt_pk_f16_f32 v37, v38, v39\n\tv_mfma_f32_16x16x32_f16 v[56:59], v[108:111], v[12:15], v[56:59]\n\tds_read_b128 v[12:15], v32 offset:3072\n\t" \
  "v_cvt_pk_f16_f32 v38, v40, v41\n\tv_cvt_pk_f16_f32 v39, v42, v43\n\ts_waitcnt lgkmcnt(7)\n\t" \
  "v_mfma_f32_16x16x32_f16 v[52:55], v[144:147], v[16:19], v[52:55]\n\tv_mfma_f32_16x16x32_f16 v[56:59], v[112:115], v[16:19], v[56:59]\n\tds_read_b128 v[16:19], v28 offset:496\n\t" \
  "v_mfma_f32_16x16x32_f16 v[52:55], v[36:39], v[76:79], v[52:55]\n\tv_mfma_f32_16x16x32_f16 v[56:59], v[36:39], v[80:83], v[56:59]\n\tv_mfma_f32_16x16x32_f16 v[60:63], v[36:39], v[84:87], 0\n\t" \
  "s_waitcnt lgkmcnt(7)\n\tv_mfma_f32_16x16x32_f16 v[44:47], v[20:23], v[68:71], 0\n\tv_mfma_f32_16x16x32_f16 v[40:43], v[20:23], v[72:75], 0\n\t" \
  "ds_read_b128 v[20:23], v32 offset:2048\n\ts_waitcnt lgkmcnt(7)\n\tv_mfma_f32_16x16x32_f16 v[52:55], v[148:151], v[0:3], v[52:55]\n\t" \
  "v_mfma_f32_16x16x32_f16 v[56:59], v[116:119], v[0:3], v[56:59]\n\tds_read_b128 v[0:3], v28 offset:2432\n\ts_waitcnt lgkmcnt(7)\n\t" \
  "v_mfma_f32_16x16x32_f16 v[52:55], v[152:155], v[4:7], v[52:55]\n\tv_cvt_pk_f16_f32 v44, v44, v45\n\tv_cvt_pk_f16_f32 v45, v46, v47\n\t" \
  "v_cvt_pk_f16_f32 v46, v40, v41\n\tv_cvt_pk_f16_f32 v47, v42, v43\n\tv_mfma_f32_16x16x32_f16 v[56:59], v[120:123], v[4:7], v[56:59]\n\t" \
  "ds_read_b128 v[4:7], v28 offset:2496\n\tv_mfma_f32_16x16x32_f16 v[52:55], v[44:47], v[88:91], v[52:55]\n\tv_mfma_f32_16x16x32_f16 v[56:59], v[44:47], v[92:95], v[56:59]\n\t" \
  "v_mfma_f32_16x16x32_f16 v[60:63], v[44:47], v[96:99], v[60:63]\n\ts_waitcnt lgkmcnt(4)\n\tv_mfma_f32_16x16x32_f16 v[40:43], v[8:11], v[12:15], 0\n\t" \
  "ds_read_b64 v[8:9], v31 offset:544\n\tds_read_b64 v[10:11], v31 offset:712\n\tds_read_b64 v[12:13], v31 offset:512\n\t" \
  "ds_read_b64 v[14:15], v31 offset:680\n\ts_waitcnt lgkmcnt(7)\n\tv_mfma_f32_16x16x32_f16 v[52:55], v[48:51], v[16:19], v[52:55]\n\t" \
  "s_waitcnt lgkmcnt(6)\n\tv_mfma_f32_16x16x32_f16 v[36:39], v[16:19], v[20:23], 0\n\tds_read_b64 v[16:17], v31 offset:480\n\t" \
  "ds_read_b64 v[18:19], v31 offset:648\n\tds_read_b64 v[20:21], v31 offset:448\n\tds_read_b64 v[22:23], v31 offset:616\n\t" \
  "s_waitcnt lgkmcnt(9)\n\tv_mfma_f32_16x16x32_f16 v[52:55], v[156:159], v[0:3], v[52:55]\n\tv_mfma_f32_16x16x32_f16 v[56:59], v[124:127], v[0:3], v[56:59]\n\t" \
  "ds_read_b128 v[0:3], v28 offset:2560\n\tv_cvt_pk_f16_f32 v36, v36, v37\n\tv_cvt_pk_f16_f32 v37, v38, v39\n\t" \
  "s_waitcnt lgkmcnt(9)\n\tv_mfma_f32_16x16x32_f16 v[52:55], v[160:163], v[4:7], v[52:55]\n\tv_cvt_pk_f16_f32 v38, v40, v41\n\t" \
  "v_cvt_pk_f16_f32 v39, v42, v43\n\tv_mfma_f32_16x16x32_f16 v[56:59], v[128:131], v[4:7], v[56:59]\n\tds_read_b128 v[4:7], v28 offset:2624\n\t" \
  "s_waitcnt lgkmcnt(8)\n\tv_mfma_f32_16x16x32_f16 v[52:55], v[36:39], v[8:11], v[52:55]\n\tds_read_b128 v[8:11], v28 offset:2032\n\t" \
  "s_waitcnt lgkmcnt(7)\n\tv_mfma_f32_16x16x32_f16 v[56:59], v[36:39], v[12:15], v[56:59]\n\tds_read_b128 v[12:15], v28 offset:2688\n\t" \
  "s_waitcnt lgkmcnt(6)\n\tv_mfma_f32_16x16x32_f16 v[60:63], v[36:39], v[16:19], v[60:63]\n\tds_read_b128 v[16:19], v28 offset:2752\n\t" \
  "s_waitcnt lgkmcnt(5)\n\tv_mfma_f32_16x16x32_f16 v[64:67], v[36:39], v[20:23], 0\n\tds_read_b128 v[20:23], v28 offset:2016\n\t" \
  "s_waitcnt lgkmcnt(5)\n\tv_mfma_f32_16x16x32_f16 v[52:55], v[164:167], v[0:3], v[52:55]\n\tv_mfma_f32_16x16x32_f16 v[56:59], v[132:135], v[0:3], v[56:59]\n\t" \
  "v_mfma_f32_16x16x32_f16 v[60:63], v[100:103], v[0:3], v[60:63]\n\tds_read_b128 v[0:3], v28 offset:2816\n\ts_waitcnt lgkmcnt(5)\n\t" \
  "v_mfma_f32_16x16x32_f16 v[56:59], v[136:139], v[4:7], v[56:59]\n\tv_mfma_f32_16x16x32_f16 v[60:63], v[104:107], v[4:7], v[60:63]\n\tds_read_b128 v[4:7], v28 offset:2880\n\t" \
  "s_waitcnt lgkmcnt(5)\n\tv_mfma_f32_16x16x32_f16 v[44:47], v[8:11], v[68:71], 0\n\tv_cvt_pk_bf16_f32 v52, v52, v53\n\t" \
  "v_cvt_pk_bf16_f32 v53, v54, v55\n\ts_cmp_lt_u32 %[w], 4\n\ts_cbranch_scc0 1f\n\t" \
  "s_setprio 0\n\t1:\n\tv_mfma_f32_16x16x32_f16 v[40:43], v[8:11], v[72:75], 0\n\t" \
  "ds_write_b64 v29, v[52:53] offset:2048\n\tds_write_b32 v25, v25 offset:256\n\tds_read_b128 v[8:11], v28 offset:992\n\t" \
  "s_waitcnt lgkmcnt(7)\n\tv_mfma_f32_16x16x32_f16 v[56:59], v[140:143], v[12:15], v[56:59]\n\tv_cvt_pk_f16_f32 v44, v44, v45\n\t" \
  "v_cvt_pk_f16_f32 v45, v46, v47\n\tv_mfma_f32_16x16x32_f16 v[60:63], v[108:111], v[12:15], v[60:63]\n\tds_read_b128 v[12:15], v32 offset:3072\n\t" \
  "v_cvt_pk_f16_f32 v46, v40, v41\n\tv_cvt_pk_f16_f32 v47, v42, v43\n\ts_waitcnt lgkmcnt(7)\n\t" \
  "v_mfma_f32_16x16x32_f16 v[56:59], v[144:147], v[16:19], v[56:59]\n\tv_mfma_f32_16x16x32_f16 v[60:63], v[112:115], v[16:19], v[60:63]\n\tds_read_b128 v[16:19], v28 offset:1008\n\t" \
  "v_mfma_f32_16x16x32_f16 v[56:59], v[44:47], v[76:79], v[56:59]\n\tv_mfma_f32_16x16x32_f16 v[60:63], v[44:47], v[80:83], v[60:63]\n\tv_mfma_f32_16x16x32_f16 v[64:67], v[44:47], v[84:87], v[64:67]\n\t" \
  "s_waitcnt lgkmcnt(7)\n\tv_mfma_f32_16x16x32_f16 v[36:39], v[20:23], v[68:71], 0\n\tv_mfma_f32_16x16x32_f16 v[40:43], v[20:23], v[72:75], 0\n\t" \
  "ds_read_b128 v[20:23], v32 offset:2048\n\ts_waitcnt lgkmcnt(7)\n\tv_mfma_f32_16x16x32_f16 v[56:59], v[148:151], v[0:3], v[56:59]\n\t" \
  "v_mfma_f32_16x16x32_f16 v[60:63], v[116:119], v[0:3], v[60:63]\n\tds_read_b128 v[0:3], v28 offset:2944\n\ts_waitcnt lgkmcnt(7)\n\t" \
  "v_mfma_f32_16x16x32_f16 v[56:59], v[152:155], v[4:7], v[56:59]\n\tv_cvt_pk_f16_f32 v36, v36, v37\n\tv_cvt_pk_f16_f32 v37, v38, v39\n\t" \
  "v_cvt_pk_f16_f32 v38, v40, v41\n\tv_cvt_pk_f16_f32 v39, v42, v43\n\tv_mfma_f32_16x16x32_f16 v[60:63], v[120:123], v[4:7], v[60:63]\n\t" \
  "ds_read_b128 v[4:7], v28 offset:3008\n\tv_mfma_f32_16x16x32_f16 v[56:59], v[36:39], v[88:91], v[56:59]\n\tv_mfma_f32_16x16x32_f16 v[60:63], v[36:39], v[92:95], v[60:63]\n\t" \
  "v_mfma_f32_16x16x32_f16 v[64:67], v[36:39], v[96:99], v[64:67]\n\tds_read_b32 v34, v30\n\ts_waitcnt lgkmcnt(5)\n\t" \
  "v_mfma_f32_16x16x32_f16 v[40:43], v[8:11], v[12:15], 0\n\tds_read_b64 v[8:9], v31 offset:544\n\tds_read_b64 v[10:11], v31 offset:712\n\t" \
  "ds_read_b64 v[12:13], v31 offset:512\n\tds_read_b64 v[14:15], v31 offset:680\n\ts_waitcnt lgkmcnt(8)\n\t" \
  "v_mfma_f32_16x16x32_f16 v[56:59], v[48:51], v[16:19], v[56:59]\n\ts_waitcnt lgkmcnt(7)\n\tv_mfma_f32_16x16x32_f16 v[44:47], v[16:19], v[20:23], 0\n\t" \
  "ds_read_b64 v[16:17], v31 offset:480\n\tds_read_b64 v[18:19], v31 offset:648\n\ts_waitcnt lgkmcnt(6)\n\t" \
  "v_cmp_gt_u32 vcc, 4, v34\n\ts_cbranch_vccz 2f\n\ts_mov_b32 s6, 0x10000\n\t" \
  "1:\n\ts_sleep 1\n\tds_read_b32 v34, v30\n\t" \
  "s_sub_u32 s6, s6, 1\n\ts_waitcnt lgkmcnt(0)\n\tv_cmp_gt_u32 vcc, 4, v34\n\t" \
  "s_cbranch_scc1 2f\n\ts_cbranch_vccnz 1b\n\t2:\n\t" \
  "ds_read_b128 v[20:23], v28 offset:3072\n\tv_mfma_f32_16x16x32_f16 v[56:59], v[156:159], v[0:3], v[56:59]\n\tv_mfma_f32_16x16x32_f16 v[60:63], v[124:127], v[0:3], v[60:63]\n\t" \
  "ds_read_b128 v[0:3], v28 offset:3136\n\tv_cvt_pk_f16_f32 v44, v44, v45\n\tv_cvt_pk_f16_f32 v45, v46, v47\n\t" \
  "v_mfma_f32_16x16x32_f16 v[56:59], v[160:163], v[4:7], v[56:59]\n\tv_cvt_pk_f16_f32 v46, v40, v41\n\tv_cvt_pk_f16_f32 v47, v42, v43\n\t" \
  "v_mfma_f32_16x16x32_f16 v[60:63], v[128:131], v[4:7], v[60:63]\n\tds_read_b128 v[4:7], v28 offset:2544\n\ts_waitcnt lgkmcnt(7)\n\t" \
  "v_mfma_f32_16x16x32_f16 v[56:59], v[44:47], v[8:11], v[56:59]\n\tds_read_b128 v[8:11], v28 offset:3200\n\ts_waitcnt lgkmcnt(6)\n\t" \
  "v_mfma_f32_16x16x32_f16 v[60:63], v[44:47], v[12:15], v[60:63]\n\tds_read_b128 v[12:15], v28 offset:3264\n\ts_waitcnt lgkmcnt(5)\n\t" \
  "v_mfma_f32_16x16x32_f16 v[64:67], v[44:47], v[16:19], v[64:67]\n\tds_read_b128 v[16:19], v28 offset:2528\n\ts_waitcnt lgkmcnt(5)\n\t" \
  "v_mfma_f32_16x16x32_f16 v[56:59], v[164:167], v[20:23], v[56:59]\n\tv_mfma_f32_16x16x32_f16 v[60:63], v[132:135], v[20:23], v[60:63]\n\tv_mfma_f32_16x16x32_f16 v[64:67], v[100:103], v[20:23], v[64:67]\n\t" \
  "ds_read_b128 v[20:23], v28 offset:3328\n\ts_waitcnt lgkmcnt(5)\n\tv_mfma_f32_16x16x32_f16 v[60:63], v[136:139], v[0:3], v[60:63]\n\t" \
  "v_mfma_f32_16x16x32_f16 v[64:67], v[104:107], v[0:3], v[64:67]\n\tds_read_b128 v[0:3], v28 offset:3392\n\ts_waitcnt lgkmcnt(5)\n\t" \
  "v_mfma_f32_16x16x32_f16 v[36:39], v[4:7], v[68:71], 0\n\tv_cvt_pk_bf16_f32 v56, v56, v57\n\tv_cvt_pk_bf16_f32 v57, v58, v59\n\t" \
  "s_cmp_lt_u32 %[w], 4\n\ts_cbranch_scc1 1f\n\ts_setprio 0\n\t" \
  "1:\n\tv_mfma_f32_16x16x32_f16 v[40:43], v[4:7], v[72:75], 0\n\tds_write_b64 v29, v[56:57] offset:2560\n\t" \
  "ds_write_b32 v25, v25 offset:320\n\tds_read_b128 v[4:7], v28 offset:1504\n\ts_waitcnt lgkmcnt(7)\n\t" \
  "v_mfma_f32_16x16x32_f16 v[60:63], v[140:143], v[8:11], v[60:63]\n\tv_cvt_pk_f16_f32 v36, v36, v37\n\tv_cvt_pk_f16_f32 v37, v38, v39\n\t" \
  "v_mfma_f32_16x16x32_f16 v[64:67], v[108:111], v[8:11], v[64:67]\n\tds_read_b128 v[8:11], v32 offset:3072\n\tv_cvt_pk_f16_f32 v38, v40, v41\n\t" \
  "v_cvt_pk_f16_f32 v39, v42, v43\n\ts_waitcnt lgkmcnt(7)\n\tv_mfma_f32_16x16x32_f16 v[60:63], v[144:147], v[12:15], v[60:63]\n\t" \
  "v_mfma_f32_16x16x32_f16 v[64:67], v[112:115], v[12:15], v[64:67]\n\tds_read_b128 v[12:15], v28 offset:1520\n\tv_mfma_f32_16x16x32_f16 v[60:63], v[36:39], v[76:79], v[60:63]\n\t" \
  "v_mfma_f32_16x16x32_f16 v[64:67], v[36:39], v[80:83], v[64:67]\n\ts_waitcnt lgkmcnt(7)\n\tv_mfma_f32_16x16x32_f16 v[44:47], v[16:19], v[68:71], 0\n\t" \
  "v_mfma_f32_16x16x32_f16 v[40:43], v[16:19], v[72:75], 0\n\tds_read_b128 v[16:19], v32 offset:2048\n\ts_waitcnt lgkmcnt(7)\n\t" \
  "v_mfma_f32_16x16x32_f16 v[60:63], v[148:151], v[20:23], v[60:63]\n\tv_mfma_f32_16x16x32_f16 v[64:67], v[116:119], v[20:23], v[64:67]\n\tds_read_b128 v[20:23], v28 offset:3456\n\t" \
  "s_waitcnt lgkmcnt(7)\n\tv_mfma_f32_16x16x32_f16 v[60:63], v[152:155], v[0:3], v[60:63]\n\tv_cvt_pk_f16_f32 v44, v44, v45\n\t" \
  "v_cvt_pk_f16_f32 v45, v46, v47\n\tv_cvt_pk_f16_f32 v46, v40, v41\n\tv_cvt_pk_f16_f32 v47, v42, v43\n\t" \
  "v_mfma_f32_16x16x32_f16 v[64:67], v[120:123], v[0:3], v[64:67]\n\tds_read_b128 v[0:3], v28 offset:3520\n\tv_mfma_f32_16x16x32_f16 v[60:63], v[44:47], v[88:91], v[60:63]\n\t" \
  "v_mfma_f32_16x16x32_f16 v[64:67], v[44:47], v[92:95], v[64:67]\n\ts_waitcnt lgkmcnt(4)\n\tv_mfma_f32_16x16x32_f16 v[40:43], v[4:7], v[8:11], 0\n\t" \
  "ds_read_b64 v[4:5], v31 offset:544\n\tds_read_b64 v[6:7], v31 offset:712\n\tds_read_b64 v[8:9], v31 offset:512\n\t" \
  "ds_read_b64 v[10:11], v31 offset:680\n\ts_waitcnt lgkmcnt(7)\n\tv_mfma_f32_16x16x32_f16 v[60:63], v[48:51], v[12:15], v[60:63]\n\t" \
  "s_waitcnt lgkmcnt(6)\n\tv_mfma_f32_16x16x32_f16 v[36:39], v[12:15], v[16:19], 0\n\tds_read_b128 v[12:15], v28 offset:3584\n\t" \
  "ds_read_b128 v[16:19], v28 offset:3648\n\ts_waitcnt lgkmcnt(7)\n\tv_mfma_f32_16x16x32_f16 v[60:63], v[156:159], v[20:23], v[60:63]\n\t" \
  "v_mfma_f32_16x16x32_f16 v[64:67], v[124:127], v[20:23], v[64:67]\n\tds_read_b128 v[20:23], v28 offset:3056\n\ts_waitcnt lgkmcnt(7)\n\t" \
  "v_mfma_f32_16x16x32_f16 v[60:63], v[160:163], v[0:3], v[60:63]\n\tv_cvt_pk_f16_f32 v36, v36, v37\n\tv_cvt_pk_f16_f32 v37, v38, v39\n\t" \
  "v_cvt_pk_f16_f32 v38, v40, v41\n\tv_cvt_pk_f16_f32 v39, v42, v43\n\tv_mfma_f32_16x16x32_f16 v[64:67], v[128:131], v[0:3], v[64:67]\n\t" \
  "ds_read_b128 v[0:3], v28 offset:3712\n\ts_waitcnt lgkmcnt(6)\n\tv_mfma_f32_16x16x32_f16 v[60:63], v[36:39], v[4:7], v[60:63]\n\t" \
  "ds_read_b128 v[4:7], v28 offset:3776\n\ts_waitcnt lgkmcnt(5)\n\tv_mfma_f32_16x16x32_f16 v[64:67], v[36:39], v[8:11], v[64:67]\n\t" \
  "ds_read_b128 v[8:11], v28 offset:3040\n\ts_waitcnt lgkmcnt(5)\n\tv_mfma_f32_16x16x32_f16 v[60:63], v[164:167], v[12:15], v[60:63]\n\t" \
  "v_mfma_f32_16x16x32_f16 v[64:67], v[132:135], v[12:15], v[64:67]\n\tds_read_b128 v[12:15], v28 offset:3840\n\ts_waitcnt lgkmcnt(5)\n\t" \
  "v_mfma_f32_16x16x32_f16 v[64:67], v[136:139], v[16:19], v[64:67]\n\tds_read_b128 v[16:19], v28 offset:3904\n\ts_waitcnt lgkmcnt(5)\n\t" \
  "v_mfma_f32_16x16x32_f16 v[44:47], v[20:23], v[68:71], 0\n\tv_mfma_f32_16x16x32_f16 v[40:43], v[20:23], v[72:75], 0\n\tds_read_b128 v[20:23], v28 offset:2016\n\t" \
  "v_cvt_pk_bf16_f32 v60, v60, v61\n\tv_cvt_pk_bf16_f32 v61, v62, v63\n\ts_waitcnt lgkmcnt(5)\n\t" \
  "v_mfma_f32_16x16x32_f16 v[64:67], v[140:143], v[0:3], v[64:67]\n\tds_write_b64 v29, v[60:61] offset:3072\n\tds_write_b32 v25, v25 offset:384\n\t" \
  "ds_read_b128 v[0:3], v32 offset:3072\n\tv_cvt_pk_f16_f32 v44, v44, v45\n\tv_cvt_pk_f16_f32 v45, v46, v47\n\t" \
  "v_cvt_pk_f16_f32 v46, v40, v41\n\tv_cvt_pk_f16_f32 v47, v42, v43\n\ts_waitcnt lgkmcnt(7)\n\t" \
  "v_mfma_f32_16x16x32_f16 v[64:67], v[144:147], v[4:7], v[64:67]\n\tds_read_b128 v[4:7], v28 offset:2032\n\tv_mfma_f32_16x16x32_f16 v[64:67], v[44:47], v[76:79], v[64:67]\n\t" \
  "s_waitcnt lgkmcnt(7)\n\tv_mfma_f32_16x16x32_f16 v[36:39], v[8:11], v[68:71], 0\n\tv_mfma_f32_16x16x32_f16 v[40:43], v[8:11], v[72:75], 0\n\t" \
  "ds_read_b128 v[8:11], v32 offset:2048\n\ts_waitcnt lgkmcnt(7)\n\tv_mfma_f32_16x16x32_f16 v[64:67], v[148:151], v[12:15], v[64:67]\n\t" \
  "ds_read_b128 v[12:15], v28 offset:3968\n\ts_waitcnt lgkmcnt(7)\n\tv_mfma_f32_16x16x32_f16 v[64:67], v[152:155], v[16:19], v[64:67]\n\t" \
  "ds_read_b128 v[16:19], v28 offset:4032\n\tv_cvt_pk_f16_f32 v36, v36, v37\n\tv_cvt_pk_f16_f32 v37, v38, v39\n\t" \
  "v_cvt_pk_f16_f32 v38, v40, v41\n\tv_cvt_pk_f16_f32 v39, v42, v43\n\ts_nop 1\n\t" \
  "v_mfma_f32_16x16x32_f16 v[64:67], v[36:39], v[88:91], v[64:67]\n\ts_waitcnt lgkmcnt(4)\n\tv_mfma_f32_16x16x32_f16 v[40:43], v[20:23], v[0:3], 0\n\t" \
  "ds_read_b64 v[0:1], v31 offset:544\n\tds_read_b64 v[2:3], v31 offset:712\n\tds_read_b128 v[20:23], v28 offset:4096\n\t" \
  "s_waitcnt lgkmcnt(6)\n\tv_mfma_f32_16x16x32_f16 v[64:67], v[48:51], v[4:7], v[64:67]\n\ts_waitcnt lgkmcnt(5)\n\t" \
  "v_mfma_f32_16x16x32_f16 v[44:47], v[4:7], v[8:11], 0\n\ts_waitcnt lgkmcnt(4)\n\tv_mfma_f32_16x16x32_f16 v[64:67], v[156:159], v[12:15], v[64:67]\n\t" \
  "s_waitcnt lgkmcnt(3)\n\tv_mfma_f32_16x16x32_f16 v[64:67], v[160:163], v[16:19], v[64:67]\n\ts_nop 3\n\t" \
  "v_cvt_pk_f16_f32 v44, v44, v45\n\tv_cvt_pk_f16_f32 v45, v46, v47\n\tv_cvt_pk_f16_f32 v46, v40, v41\n\t" \
  "v_cvt_pk_f16_f32 v47, v42, v43\n\ts_waitcnt lgkmcnt(1)\n\ts_nop 0\n\t" \
  "v_mfma_f32_16x16x32_f16 v[64:67], v[44:47], v[0:3], v[64:67]\n\ts_waitcnt lgkmcnt(0)\n\tv_mfma_f32_16x16x32_f16 v[64:67], v[164:167], v[20:23], v[64:67]\n\t" \
  "s_nop 7\n\tv_cvt_pk_bf16_f32 v64, v64, v65\n\tv_cvt_pk_bf16_f32 v65, v66, v67\n\t" \
  "ds_write_b64 v29, v[64:65] offset:3584\n\tds_write_b32 v25, v25 offset:448\n\t"
#define CONV_CLOBBER "v0", "v1", "v2", "v3", "v4", "v5", "v6", "v7", "v8", "v9", "v10", "v11", "v12", "v13", "v14", "v15", "v16", "v17", "v18", "v19", "v20", "v21", "v22", "v23", "v36", "v37", "v38", "v39", "v40", "v41", "v42", "v43", "v44", "v45", "v46", "v47", "v52", "v53", "v54", "v55", "v56", "v57", "v58", "v59", "v60", "v61", "v62", "v63", "v64", "v65", "v66", "v67", "v68", "v69", "v70", "v71", "v72", "v73", "v74", "v75", "v76", "v77", "v78", "v79", "v80", "v81", "v82", "v83", "v84", "v85", "v86", "v87", "v88", "v89", "v90", "v91", "v92", "v93", "v94", "v95", "v96", "v97", "v98", "v99", "v100", "v101", "v102", "v103", "v104", "v105", "v106", "v107", "v108", "v109", "v110", "v111", "v112", "v113", "v114", "v115", "v116", "v117", "v118", "v119", "v120", "v121", "v122", "v123", "v124", "v125", "v126", "v127", "v128", "v129", "v130", "v131", "v132", "v133", "v134", "v135", "v136", "v137", "v138", "v139", "v140", "v141", "v142", "v143", "v144", "v145", "v146", "v147", "v148", "v149", "v150", "v151", "v152", "v153", "v154", "v155", "v156", "v157", "v158", "v159", "v160", "v161", "v162", "v163", "v164", "v165", "v166", "v167"
#define G_CLOBBER "v8", "v9", "v10", "v11", "v12", "v13", "v14", "v15", "v16", "v17", "v18", "v19", "v20", "v21", "v22", "v23", "v24", "v25", "v26", "v27", "v28", "v29", "v30", "v32", "v33", "v34", "v35", "v36", "v37", "v38", "v39", "v40", "v41", "v42", "v43", "v44", "v45", "v46", "v47", "v48", "v49", "v50", "v51", "v52", "v53", "v54", "v55", "v56", "v57", "v58", "v59", "v60", "v61", "v62", "v63", "v64", "v65", "v66", "v67", "v68", "v69", "v70", "v71", "v72", "v73", "v74", "v75", "v76", "v77", "v78", "v79", "v80", "v81", "v82", "v83", "v84", "v85", "v86", "v87", "v88", "v89", "v90", "v91", "v92", "v93", "v94", "v95", "v96", "v97", "v98", "v99", "v100", "v101", "v102", "v103", "v104", "v105", "v106", "v107", "v108", "v109", "v110", "v111", "v112", "v113", "v114", "v115", "v116", "v117", "v118", "v119", "v120", "v121", "v122", "v123", "v124", "v125", "v126", "v127"
#define D_CLOBBER "v8", "v9", "v10", "v11", "v12", "v13", "v14", "v15", "v16", "v17", "v18", "v19", "v20", "v21", "v22", "v23", "v24", "v25", "v26", "v27", "v28", "v29", "v30", "v31"
__device__ __forceinline__ float fmav(float a, float b, float c) {
  float r;
  asm("v_fma_f32 %0, %1, %2, %3" : "=v"(r) : "v"(a), "v"(b), "v"(c));
  return r;
}
__device__ __forceinline__ float fmas(float a, u32 s, float c) {
  asm("v_fmac_f32 %0, %2, %1" : "+v"(c) : "v"(a), "s"(s));
  return c;
}
#define RANK8(d0, d1, d2, d3, c0, c1, i0, f0, f1) "v_perm_b32 " d0 ", " d0 ", " d1 ", %[sg]\n\tv_perm_b32 " d2 ", " d2 ", " d3 ", %[sg]\n\t" \
  "v_bcnt_u32_b32 " d0 ", " d0 ", " i0 "\n\tv_bcnt_u32_b32 " c0 ", " d2 ", " d0 "\n\tv_bcnt_u32_b32 " c1 ", " d2 ", " d0 "\n\t" f0 "\n\t" \
  "v_permlane16_swap_b32 " c0 ", " c1 "\n\tv_add_u32 " d0 ", " c0 ", " c1 "\n\tv_add_u32 " d1 ", " c0 ", " c1 "\n\t" f1 "\n\t" \
  "v_permlane32_swap_b32 " d0 ", " d1 "\n\tv_add_u32 " c0 ", " d0 ", " d1 "\n\t"
__device__ __forceinline__ void ln_rows(u32x4 (&xv)[4], u32x8 wv, rsrc_t rs, u32& tag, int b, int w, int l, u32 st, u32 sg TLARG) {
  float s[2][3] = {}, c[2], sw = 0.f;
  u32 os = l < 16 ? O_SG + 128 * (b - 128) + 8 * l : 0x80000000u;
  u32 ot = l < 16 ? O_TOP + 8 * (b & 15) + TL * ((b >> 4) & 7) : l < 32 ? O_RG - 128 + 128 * (b - 128) + 8 * l : 0x80000000u, om = l < 16 ? TOPS / 8 : 0u, fm = l < 16 ? 0xff0u : 0u;
  u32 aw = (l & 16) ? st + 8 * w + (l >> 5) * 4 : st + 136, ac = st + 128, ar = st + 4 * (l & 15), aq = st + (l & 48), one = 1u;
  u32 kl = (2047 - (16 * (b - 128) + 2 * w + (l >> 5))) << 1;
  asm volatile("" : "+v"(os), "+v"(aw), "+v"(ac), "+v"(ar), "+v"(aq), "+v"(one), "+v"(kl), "+v"(ot), "+v"(om), "+v"(fm));
  SB();
  asm volatile("s_waitcnt vmcnt(4) lgkmcnt(0)" : "+{s44}"(tag)::"memory");
  asm volatile("" : "+{v[116:123]}"(wv));
  tag += 1u;
  *(volatile LDS u32*)(size_t)(st - 104) = tag;
  STAMP(12);
#pragma unroll
  for (int i = 0; i < 8; i++) sw = dot2b(wv[i], 0x3f803f80u, sw);
  sw += dppf<0x121, 0xf>(sw); sw += dppf<0x122, 0xf>(sw); sw += dppf<0x124, 0xf>(sw); sw += dppf<0x128, 0xf>(sw);
  sw = (rdl(sw, 0) + rdl(sw, 16)) + (rdl(sw, 32) + rdl(sw, 48));
  asm volatile("" : "+v"(sw));
  SB();
  float cv = 0.f;
#pragma unroll
  for (int r = 0; r < 2; r++) {
#pragma unroll
    for (int i = 0; i < 8; i++) {
      if (r == 0 && i == 0) { asm volatile("s_waitcnt vmcnt(3)" : "+{v[100:103]}"(xv[0])); STAMP(13); }
      if (r == 0 && i == 4) asm volatile("s_waitcnt vmcnt(2)" : "+{v[104:107]}"(xv[1]));
      if (r == 1 && i == 0) asm volatile("s_waitcnt vmcnt(1)" : "+{v[108:111]}"(xv[2]));
      if (r == 1 && i == 4) asm volatile("s_waitcnt vmcnt(0)" : "+{v[112:115]}"(xv[3]));
      const u32 a = xv[2 * r + (i >> 2)][i & 3];
      s[r][0] = dot2b(a, 0x3f803f80u, s[r][0]); s[r][1] = dot2b(a, a, s[r][1]); s[r][2] = dot2b(a, wv[i], s[r][2]);
      if (i == 3) {
        c[r] = rdl(s[r][0] * 0.125f, 0);
        if (r) { cv = l < 32 ? c[0] : c[1]; asm volatile("" : "+v"(cv)); }
        SB();
      }
    }
    const float ml = s[r][0] * 0.0625f, dl = __builtin_fmaf(s[r][0], 0.25f, -4.f * c[r]);
    s[r][1] = __builtin_fmaf(dl, dl, __builtin_fmaf(-s[r][0], ml, s[r][1]));
    SB();
  }
#pragma unroll
  for (int k = 0; k < 3; k++) swap32(s[0][k ? 3 - k : 0], s[1][k ? 3 - k : 0]);
  float q[3];
#pragma unroll
  for (int k = 0; k < 3; k++) q[k] = s[0][k] + s[1][k];
#define RED3(m) "v_add_f32_dpp %0, %0, %0 " m " bank_mask:0xf\n\tv_add_f32_dpp %1, %1, %1 " m " bank_mask:0xf\n\tv_add_f32_dpp %2, %2, %2 " m " bank_mask:0xf\n\t"
  asm volatile("s_nop 1\n\t" RED3("row_ror:1 row_mask:0xf") RED3("row_ror:2 row_mask:0xf") RED3("row_ror:4 row_mask:0xf") RED3("row_ror:8 row_mask:0xf")
               RED3("row_bcast:15 row_mask:0xa") : "+v"(q[0]), "+v"(q[1]), "+v"(q[2]));
  const float mu = q[0] * (1.f / D), dv = mu - cv;
  const float var = fmaxf(__builtin_fmaf(-dv, dv, q[1] * (1.f / D)), 0.f);
  const float m = rb(mu);
  float sd = __builtin_amdgcn_sqrtf(rb(rb(var) + 1e-5f)), num = __builtin_fmaf(-m, sw, q[2]);
  asm volatile("s_nop 0\n\tv_cvt_pk_bf16_f32 %0, 0, %0" : "+v"(sd), "+v"(num));
  u32 rv = __float_as_uint(rb(num * __builtin_amdgcn_rcpf(sd)));
  rv ^= (u32)((int)rv >> 15) | 0x80000000u;
  rv = __builtin_amdgcn_perm(rv, kl, 0x07060100u) >> 1;
  asm volatile("ds_write2_b32 %[aw], %[sv], %[rv] offset1:16\n\ts_mov_b64 exec, 1\n\tds_add_rtn_u32 v104, %[ac], %[one]\n\ts_mov_b64 exec, -1\n\t"
               "ds_read_b32 v106, %[ar]\n\tds_read_b32 v105, %[ar] offset:64\n\tds_read_b128 v[100:103], %[aq] offset:64\n\tv_mov_b32 v107, %[t]\n\tv_mov_b32 v109, %[t]\n\t"
               "s_waitcnt lgkmcnt(3)\n\tv_cmp_eq_u32 vcc, 7, v104\n\ts_bitcmp1_b32 vcc_lo, 0\n\ts_cbranch_scc0 1f\n\t"
               "s_waitcnt lgkmcnt(2)\n\tbuffer_store_dwordx2 v[106:107], %[os], %[rs], 0 offen sc1\n\ts_waitcnt lgkmcnt(0)\n\t"
               "v_sub_u32 v100, v105, v100\n\tv_sub_u32 v101, v105, v101\n\tv_sub_u32 v102, v105, v102\n\tv_sub_u32 v103, v105, v103\n\t"
               RANK8("v100", "v101", "v102", "v103", "v104", "v103", "0", "s_nop 1", "s_nop 1") "v_cmp_eq_u32 vcc, 16, v104\n\t"
               "v_sub_u32 v108, 19, v104\n\ts_ff1_i32_b32 s6, vcc_lo\n\tv_readlane_b32 s6, v105, s6\n\t"
               "v_mad_u32_u24 v104, v104, %[om], %[ot]\n\ts_nop 0\n\tv_lshrrev_b32 v108, v108, s6\n\tv_bfi_b32 v108, %[fm], v108, v105\n\t"
               "buffer_store_dwordx2 v[108:109], v104, %[rs], 0 offen sc1\n1:\n\ts_waitcnt lgkmcnt(0)"
               :: [aw] "v"(aw), [sv] "v"(__builtin_amdgcn_perm(__float_as_uint(sd), __float_as_uint(m), 0x07060302u)), [rv] "v"(rv), [ac] "v"(ac), [one] "v"(one),
                  [ar] "v"(ar), [aq] "v"(aq), [os] "v"(os), [om] "v"(om), [ot] "v"(ot), [fm] "v"(fm), [sg] "s"(sg), [t] "s"(tag), [rs] "s"(rs)
               : "vcc", "scc", "s6", "v100", "v101", "v102", "v103", "v104", "v105", "v106", "v107", "v108", "v109", "memory");
  SB();
}

#define GL __attribute__((address_space(1)))
typedef const GL u16* gp;

__global__ __launch_bounds__(768, 3) void fused(const u16*, const u16*, const u16*, const u16*, const u16*, const u16*, const u16*, const u16*,
    const u16*, const u16*, const u16*, u16*, char*) {
  __shared__ __attribute__((aligned(256))) char smem[SMEM];
  const u32 lds0 = (u32)(size_t)(LDS char*)smem;
  int w0;
#define GLD(r0, r1, r2, r3, o_) "global_load_dwordx4 v[" #r0 ":" #r1 "], " #o_ ", s[16:17]\n\tglobal_load_dwordx4 v[" #r2 ":" #r3 "], " #o_ ", s[16:17] offset:2048\n\t"
  asm volatile(
      "s_load_dwordx16 s[16:31], s[0:1], 0\n\ts_load_dwordx8 s[32:39], s[0:1], 64\n\ts_load_dwordx2 s[40:41], s[0:1], 96\n\t"
      "s_getpc_b64 s[4:5]\n2:\n\ts_lshr_b32 s7, s2, 3\n\ts_and_b32 s10, s4, 0xffffff00\n\ts_mov_b32 s11, s5\n\ts_mul_i32 s8, s7, 0x600\n\t"
      "v_lshl_add_u32 v3, v0, 1, s8\n\ts_cmpk_lt_u32 s2, %[pn]\n\ts_cbranch_scc0 1f\n\t"
      "global_load_ushort v163, v3, s[10:11] offset:%[pf]\n"
      "1:\n\ts_cmpk_lt_u32 s2, 0x80\n\tv_readfirstlane_b32 s3, v0\n\tv_and_b32 v1, 63, v0\n\tv_mov_b64 v[4:5], 0\n\tv_mov_b64 v[6:7], 0\n\tv_lshlrev_b32 v2, 4, v1\n\tv_lshlrev_b32 v8, 2, v0\n\t"
      "s_mov_b32 s42, %[we]\n\ts_mov_b32 s43, 0x20000\n\ts_cbranch_scc0 3f\n\t"
      "s_lshr_b32 s3, s3, 6\n\ts_mul_i32 s9, s3, %[xr]\n\ts_min_u32 s9, s9, %[zm]\n\tv_add_u32 v3, s9, v2\n\tv_add_u32 v3, %[zo], v3\n\t"
      "s_and_b32 s7, s7, 7\n\ts_and_b32 s6, s2, 7\n\ts_and_b32 s8, s2, 64\n\ts_lshl3_add_u32 s8, s6, s8\n\ts_add_u32 s45, s8, s7\n\t"
      "s_add_u32 s4, s4, lane_tab@rel32@lo+(6f-2b-4)\n6:\n\ts_addc_u32 s5, s5, lane_tab@rel32@hi+(6b-2b+4)\n\t"
      "ds_write_b128 v3, v[4:7]\n\ts_cmp_lt_u32 s3, 8\n\ts_cbranch_scc1 5f\n\t"
      "s_lshl_b32 s8, s45, 4\n\ts_lshl_b32 s9, s3, 18\n\ts_add_u32 s9, s9, s8\n\ts_bitset0_b32 s9, 21\n\tv_lshl_add_u32 v148, v1, 12, s9\n\t"
      "s_lshl3_add_u32 s9, s3, s7\n\ts_lshl_b32 s9, s9, 17\n\ts_add_u32 s9, s9, s8\n\ts_bitset0_b32 s9, 23\n\tv_lshl_add_u32 v9, v1, 11, s9\n\t"
      "v_add_u32 v11, 0x100000, v148\n\tv_add_u32 v12, 0x200000, v148\n\t"
      "v_lshlrev_b32 v149, 4, v0\n\ts_lshl_b32 s9, s3, 10\n\ts_add_u32 m0, s9, %[ob]\n\tv_add_u32 v10, 0xffffe800, v149\n\tglobal_load_lds_dwordx4 v10, s[4:5]\n\t"
      "v_add_u32 v149, %[sg], v149\n\t"
      "s_waitcnt lgkmcnt(0)\n\t" KDL "s_load_dword s44, s[40:41], 0\n\ts_memrealtime s[46:47]\n\t"
      "s_barrier\n\t"
      "global_load_dword v160, v9, s[16:17]" PRENT "\n\t"
      GLD(100, 103, 104, 107, v148)
      GLD(108, 111, 112, 115, v11)
      "s_cmp_eq_u32 s6, 3\n\ts_cbranch_scc1 .Lrole_p\n\t"
      GLD(116, 119, 120, 123, v12)
      "s_branch .Lrole_p\n"
      "3:\n\ts_lshr_b32 s3, s3, 6\n\ts_cmp_lt_u32 s3, 8\n\ts_cbranch_scc1 4f\n\ts_waitcnt lgkmcnt(0)\n\t" KDL "s_memrealtime s[46:47]\n\ts_barrier\n\ts_branch .Lrole_d\n4:\n\t"
      "v_add_u32 v3, %[fc], v2\n\ts_add_u32 s8, s3, s2\n\ts_and_b32 s45, s8, 7\n\ts_lshl3_add_u32 s8, s2, s45\n\t"
      "s_lshl_b32 s8, s8, 12\n\ts_bitset0_b32 s8, 22\n\tds_write_b128 v3, v[4:7]\n\tv_add_u32 v3, s8, v2\n\t"
      "s_waitcnt lgkmcnt(0)\n\t" KDL "global_load_dwordx4 v[116:119], v2, s[30:31]\n\tglobal_load_dwordx4 v[120:123], v2, s[30:31] offset:1024\n\t"
      "s_load_dword s44, s[40:41], 0\n\ts_memrealtime s[46:47]\n\ts_barrier\n\t"
      "global_load_dwordx4 v[100:103], v3, s[16:17]" LNNT "\n\tglobal_load_dwordx4 v[104:107], v3, s[16:17] offset:1024" LNNT "\n\t"
      "global_load_dwordx4 v[108:111], v3, s[16:17] offset:2048" LNNT "\n\tglobal_load_dwordx4 v[112:115], v3, s[16:17] offset:3072" LNNT "\n\t"
      "s_branch .Lrole_l\n"
      "5:\n\t"
      "global_load_dwordx4 v[16:19], v2, s[4:5]\n\tglobal_load_dwordx4 v[20:23], v2, s[4:5] offset:1024\n\t"
      "s_lshl3_add_u32 s8, s45, s3\n\ts_lshl_b32 s9, s8, 1\n\tv_mov_b32 v9, s9\n\ts_lshl_b32 s9, s45, 11\n\tv_add_u32 v3, s9, v8\n\t"
      "s_waitcnt lgkmcnt(0)\n\t" KDL "global_load_short_d16_hi v24, v9, s[26:27]\n\tglobal_load_ushort v160, v9, s[28:29]\n\t"
      "global_load_dword v96, v3, s[18:19]" PNT "\n\tglobal_load_dword v97, v3, s[20:21]" PNT "\n\t"
      "global_load_dword v68, v3, s[22:23]" PNT "\n\tglobal_load_dword v69, v3, s[24:25]" PNT "\n\ts_barrier\n\ts_branch .Lrole_c"
      : "={s3}"(w0)
      : "{s[0:1]}"(__builtin_amdgcn_kernarg_segment_ptr()), "{s2}"(blockIdx.x), "{v0}"(threadIdx.x), [pf] "n"(PFO), [pn] "n"(8 * PFN), [we] "n"(WS_END), [xr] "n"(XREG),
        [zm] "n"(8 * XREG), [zo] "n"(OX - 16), [sg] "n"(O_SG - 8192), [fc] "n"(FC), [ob] "n"(OB - 8192)
      : "memory");
#define TLREGS()
#define ROLE(name, text, ...) \
  gp x, wg, wu, wd; GL u16* out; rsrc_t rs; int b, w, t, l, g; u32 tag, st0; \
  asm volatile(".Lrole_" #name ":" : "={v0}"(t), "={v1}"(l) __VA_ARGS__ :: "memory"); \
  asm volatile(text : "={s2}"(b), "={s3}"(w), "={s[16:17]}"(x), "={s[32:33]}"(wg), "={s[34:35]}"(wu), "={s[36:37]}"(wd), \
               "={s[38:39]}"(out), "={s[40:43]}"(rs), "={s44}"(tag), "={s45}"(g), "={s46}"(st0) :: "memory"); \
  SB(); \
  __builtin_assume((u32)l < 64u); __builtin_assume((u32)t < 768u); __builtin_assume((u32)w < 12u); __builtin_assume((u32)b < 256u); __builtin_assume((u32)g < 128u); \
  TLREGS(); \
  const int xk = b & 7, j = (b >> 3) & 15, n16 = l & 15, kg = l >> 4
#define FLAG(o, v) do { asm volatile("" ::: "memory"); SB(); *(volatile LDS u32*)(LDS char*)(smem + (o)) = (v); SB(); } while (0)
#define CWAIT(o, n, nap) do { _Pragma("unroll 1") for (int spin = 0; spin < SPIN; spin++) { \
      if (__builtin_amdgcn_readfirstlane((int)*(const volatile LDS u32*)(LDS char*)(smem + (o))) >= (int)(n)) break; \
      asm volatile(nap ::: "memory"); } SB(); asm volatile("" ::: "memory"); } while (0)
#define AWAIT(o, n, nap) do { _Pragma("unroll 1") for (int spin = 0; spin < SPIN; spin++) { \
      if (__all(*(const volatile LDS u32*)(LDS char*)(smem + (o)) >= (u32)(n))) break; \
      asm volatile(nap ::: "memory"); } SB(); asm volatile("" ::: "memory"); } while (0)
#define POLL8(res_, off_) do { u32 n_ = SPIN; \
    asm volatile("1:\n\tbuffer_load_dwordx2 v[4:5], %2, %3, 0 offen sc1\n\ts_sub_u32 %1, %1, 1\n\ts_waitcnt vmcnt(0)\n\t" \
                 "v_cmp_ne_u32 vcc, %4, v5\n\ts_cbranch_scc1 2f\n\ts_cbranch_vccnz 1b\n2:" \
                 : "=&{v[4:5]}"(res_), "+s"(n_) : "v"(off_), "s"(rs), "s"(tag) : "vcc", "scc", "memory"); } while (0)
#define UNTIL(t_) "2:\n\ts_sleep 1\n\ts_memrealtime s[8:9]\n\ts_waitcnt lgkmcnt(0)\n\ts_sub_u32 s8, s8, %[st]\n\ts_cmp_lt_u32 s8, " t_ "\n\ts_cbranch_scc1 2b\n\t"
#define IXO(l_) (O_IDX + (2 * xk + (j & 1)) * 128 + ((l_) & 7) * 8)
#define IDS() const int id = 16 * xk + j, sb = id * 57 >> 10, mat = id & 1, u = (id - 18 * sb) >> 1; \
    const bool slow = u >= 6; \
    const int part = slow ? u - 6 : u & 1, cb = 4 * sb + (slow ? 1 : (u >> 1) + (u > 1)), r0 = part * (slow ? 320 : 512), ns = slow ? 5 + (u >> 3) : 8; \
    const int kp = 4 * sb + ((u >> 1) & 3), ct = 2 * (u & 1) + mat
#define TS(a, b_)
#define TSC
#define DONE "s_endpgm\n"
#define TSD
#define TSGET(i0, i1, i2, i3)
  if (blockIdx.x >= 128) {
    if (w0 >= 8) {
      ROLE(d, "s_waitcnt lgkmcnt(0)");
      IDS();
      const int dw = w - 8;
      const u32 sw = (((n16 >> 1) ^ (kg & 1)) << 5) + ((l & 1) << 4);
      gp base = (mat ? wu : wg) + (size_t)(r0 + 4 * dw) * F + cb * 128;
      u32 v = kg * (2 * F) + sw, fa = lds0 + FC + 4 * dw, vp = 96 + dw, t_;
      asm volatile("" : "+v"(v), "+v"(fa), "+v"(vp));
      asm volatile("s_mov_b32 m0, %0" ::"s"(lds0 + 1024 * dw));
#define VMC "s_getreg_b32 s6, hwreg(HW_REG_IB_STS, 0, 4)\n\t"
#define RUN(k, q, lim, v_, b_, st_) asm volatile("1:\n\ts_lshr_b32 s6, m0, 4\n\ts_and_b32 s6, s6, 0xc0\n\tv_xor_b32 %[t], s6, %[v]\n\ts_waitcnt vmcnt(%[c])\n\t" VMC \
        "global_load_lds_dwordx4 %[t], %[b] nt\n\ts_add_u32 m0, m0, %[m]\n\tv_add_u32 %[v], %[d], %[v]\n\t.if %[m] == 3072\n\tv_mad_i32_i24 %[t], s6, -3, %[p]\n\tv_add_u32 %[p], 3, %[p]\n\t" \
        "s_mov_b64 exec, 1\n\tds_max_u32 %[a], %[t]\n\ts_mov_b64 exec, -1\n\t.endif\n\ts_cmp_lt_u32 m0, %[l]\n\ts_cbranch_scc1 1b" \
        : [v] "+v"(v_), [p] "+v"(vp), [t] "=&v"(t_) : [b] "s"(b_), [c] "n"((q) - 1), [m] "n"(1024 * (k)), [d] "n"((k) * (st_)), \
          [a] "v"(fa), [l] "s"(lim) : "scc", "s6", "memory")
#define MARK(c, val) "s_waitcnt vmcnt(" #c ")\n\t" val "\n\ts_mov_b64 exec, 1\n\tds_max_u32 %[a], %[t]\n\ts_mov_b64 exec, -1\n\t"
      __builtin_amdgcn_s_setprio(DPRIO);
      STAMP(8);
      if (id < 126) { if (slow) RUN(4, QAS, 32768, v, base, 8 * F); else RUN(4, 8, 32768, v, base, 8 * F); }
      if (dw < 3) {
        if (id >= 126) __builtin_amdgcn_endpgm();
        gp bd = wd + (size_t)(128 * kp + 2 * dw) * D + ct * 256;
        u32 vd = (l >> 5) * (2 * D + 32) ^ 16 * (l & 31);
        asm volatile("" : "+v"(vd));
        if (slow) RUN(3, QS, 16384 * ns, v, base, 8 * F);
        else { RUN(3, 8, 32768 + 3072 * RAMP, v, base, 8 * F); RUN(3, QD, 131072, v, base, 8 * F); }
        STAMP(1);
        {
          u32 n_ = SPIN;
          asm volatile("1:\n\t" VMC MARK(63, "v_mad_i32_i24 %[t], s6, -3, %[p]") "ds_read_b32 %[t], %[f]\n\ts_sub_u32 %[n], %[n], 1\n\ts_waitcnt lgkmcnt(0)\n\t"
                       "v_cmp_lt_u32 vcc, %[k], %[t]\n\ts_cbranch_scc1 2f\n\ts_cbranch_vccz 1b\n2:\n\ts_mov_b32 m0, %[m]"
                       : [t] "=&v"(t_), [n] "+s"(n_) : [a] "v"(fa), [p] "v"(vp), [f] "v"(u == 8 ? fa : lds0 + FC + 20), [k] "s"(u == 8 ? 159 : 16 * HOLD - 105), [m] "s"(lds0 + 1024 * dw)
                       : "vcc", "scc", "s6", "memory");
        }
        STAMP(3);
        if (u < 8) {
          RUN(3, QE, 65536, vd, bd, 4 * D);
          STAMP(6);
          asm volatile(MARK(0, "v_mov_b32 %[t], -1") : [t] "=&v"(t_) : [a] "v"(fa) : "memory");
          STAMP(4);
        }
        FLUSH();
        __builtin_amdgcn_endpgm();
      }
      asm volatile(MARK(0, "v_mov_b32 %[t], -1") : [t] "=&v"(t_) : [a] "v"(fa) : "memory");
      u32 km, rk;
      u32 hi, lo, k2, k3, gd, thr, sg = 0x0c0c0b09u;
      u32 ka = lds0 + FC + 384 + 8 * (l >> 3), kc = 2047 * 2048 + 2 * r0 + 16 * ((l & 7) ^ ((l >> 2) & 4)), kq = lds0 + FC + 512 + (l & 48), kr = lds0 + FC + 512 + 4 * n16;
      u32 c1 = 896, c2 = 1792, c3 = 2688, cm = (u32)-2048, cs = O_SG + 8 * 2047, ca = lds0 + FC + 512;
      rsrc_t rx = __builtin_amdgcn_make_buffer_rsrc((void*)(x - 1344), 0, 2 * L * D + 4096, 0x00020000);
      u32 co = O_CLR + 8 * (18 * sb + (0x84c0 >> 4 * (kp & 3) & 15) + min(l, (kp & 3) == 1 ? 5 : 3));
      asm volatile("" : "+v"(ka), "+v"(kc), "+v"(kq), "+v"(kr), "+v"(co));
      asm volatile("" : "+s"(c1), "+s"(c2), "+s"(c3), "+s"(cm), "+s"(cs), "+s"(ca), "+s"(sg), "+s"(rx));
      *(u32x4*)(smem + FA + 6144 + 16 * l) = u32x4{0u, 0u, 0u, 0u};
      *(u32x4*)(smem + FA + 7168 + 16 * l) = u32x4{0u, 0u, 0u, 0u};
      CWAIT(FC + 256, 1, "s_sleep 4");
      tag = (u32)__builtin_amdgcn_readfirstlane((int)*(const volatile LDS u32*)(LDS char*)(smem + FC + 24));
      {
        u32 n_ = SPIN, t0, t1, t2, t3, se;
#define TLX(a, b_, c, d, sc) "buffer_load_dwordx4 v[" #a ":" #b_ "], %[o], %[r], 0 offen" sc "\n\tbuffer_load_dwordx4 v[" #c ":" #d "], %[o], %[r], %[ts] offen" sc "\n\t"
#define TLD(a, b_, c, d) TLX(a, b_, c, d, " sc1")
#define TCK(r0, r1, r2, r3, r4, r5, r6, r7) "s_waitcnt vmcnt(4)\n\tv_xor_b32 %[t0], %[t], v" #r1 "\n\tv_bitop3_b32 %[t0], %[t0], %[t], v" #r3 " bitop3:0xf6\n\t" \
          "v_bitop3_b32 %[t0], %[t0], %[t], v" #r5 " bitop3:0xf6\n\tv_bitop3_b32 %[t0], %[t0], %[t], v" #r7 " bitop3:0xf6\n\t" \
          "v_cmp_ne_u32 vcc, 0, %[t0]\n\tv_mov_b32 %[hi], v" #r0 "\n\tv_mov_b32 %[lo], v" #r2 "\n\tv_mov_b32 %[k2], v" #r4 "\n\tv_mov_b32 %[k3], v" #r6 "\n\t" \
          "s_cbranch_vccz 5f\n\t" TLD(r0, r3, r4, r7)
        asm volatile("2:\n\ts_sleep 1\n\ts_memrealtime s[8:9]\n\ts_waitcnt lgkmcnt(0)\n\ts_sub_u32 s8, s8, %[st]\n\ts_cmp_lt_u32 s8, %[tw]\n\ts_cbranch_scc1 2b\n\t"
                     TLX(128, 131, 132, 135, SCT) "s_sleep " STR(RSL) "\n\t" TLD(136, 139, 140, 143) "s_sleep " STR(RSL) "\n\t" TLX(144, 147, 148, 151, SCU)
                     "1:\n\t" TCK(128, 129, 130, 131, 132, 133, 134, 135) TCK(136, 137, 138, 139, 140, 141, 142, 143) TCK(144, 145, 146, 147, 148, 149, 150, 151)
                     "s_sub_u32 %[n], %[n], 1\n\ts_cbranch_scc0 1b\n5:\n\t" TS(60, 61)
                     "v_and_or_b32 %[t0], %[hi], %[mk], %[pa]\n\tv_and_or_b32 %[t1], %[lo], %[mk], %[pb]\n\t"
                     "v_max_u32 %[t2], %[t0], %[t1]\n\tv_max_u32 %[t3], %[t0], %[t1]\n\tv_perm_b32 %[gd], %[hi], %[lo], %[p0]\n\tv_perm_b32 %[se], %[k2], %[k3], %[p0]\n\t"
                     "v_permlane16_swap_b32 %[t2], %[t3]\n\tv_max_u32 %[km], %[t2], %[t3]\n\tv_max_u32 %[rk], %[t2], %[t3]\n\t"
                     "v_max_u32 %[hi], %[t0], %[t1]\n\tv_min_u32 %[lo], %[t0], %[t1]\n\t"
                     "v_permlane32_swap_b32 %[km], %[rk]\n\tv_max_u32 %[km], %[km], %[rk]\n\tv_not_b32 %[t2], %[km]\n\t"
                     "v_and_or_b32 %[k2], %[k2], %[mk], %[pa]\n\t"
                     "v_not_b32_dpp %[t2], %[km] row_ror:4 row_mask:0x2 bank_mask:0xf\n\tv_not_b32_dpp %[t2], %[km] row_ror:8 row_mask:0x4 bank_mask:0xf\n\t"
                     "v_not_b32_dpp %[t2], %[km] row_ror:12 row_mask:0x8 bank_mask:0xf\n\t"
                     "v_pk_lshlrev_b16 %[gd], 4, %[gd] op_sel_hi:[0,1]\n\tv_add_u32 %[t0], %[km], %[t2]\n\t"
                     "v_add_u32_dpp %[t1], %[t2], %[km] row_ror:1 row_mask:0xf bank_mask:0xf\n\tv_add_u32_dpp %[t3], %[t2], %[km] row_ror:2 row_mask:0xf bank_mask:0xf\n\t"
                     "v_add_u32_dpp %[t2], %[t2], %[km] row_ror:3 row_mask:0xf bank_mask:0xf\n\t"
                     RANK8("%[t0]", "%[t1]", "%[t2]", "%[t3]", "%[rk]", "%[t3]", "-2", "v_lshrrev_b32 %[se], 4, %[se]\n\tv_perm_b32 %[gd], %[gd], %[se], %[p1]",
                           "v_pk_max_u16 %[gd], %[gd], %[gd] op_sel:[0,1] op_sel_hi:[1,0]\n\tv_and_or_b32 %[k3], %[k3], %[mk], %[pb]")
                     "v_cmp_eq_u32 vcc, 56, %[rk]\n\tv_max3_u32 %[se], %[lo], %[k2], %[k3]\n\tv_lshrrev_b32 %[gd], 1, %[gd]\n\ts_ff1_i32_b32 s6, vcc_lo\n\t"
                     "v_readlane_b32 s6, %[km], s6\n\tv_or_b32 %[gd], 0x7fff, %[gd]\n\tv_med3_u32 %[t2], %[lo], %[k2], %[k3]\n\t"
                     "v_cmp_le_u32 s[8:9], s6, %[hi]\n\tv_cmp_le_u32 s[10:11], s6, %[se]\n\tv_max_u32 %[t2], %[t2], %[gd]\n\t"
                     "v_mbcnt_lo_u32_b32 %[t0], s8, 0\n\tv_mbcnt_lo_u32_b32 %[t1], s10, -15\n\tv_mbcnt_hi_u32_b32 %[t0], s9, %[t0]\n\tv_mbcnt_hi_u32_b32 %[t1], s11, %[t1]\n\t"
                     "v_lshl_add_u32 %[t0], %[t0], 2, %[ca]\n\tv_mad_i32_i24 %[t1], %[t1], -4, %[ca]\n\tv_cmp_le_u32 vcc, s6, %[t2]\n\t"
                     "s_mov_b64 exec, s[8:9]\n\tds_write_b32 %[t0], %[hi]\n\ts_mov_b64 exec, s[10:11]\n\tds_write_b32 %[t1], %[se]\n\ts_mov_b64 exec, -1\n\t"
                     "ds_read_b32 %[se], %[kr]\n\tds_read_b128 v[152:155], %[kq]\n\t"
                     "s_bcnt1_i32_b64 s8, s[8:9]\n\ts_bcnt1_i32_b64 s10, s[10:11]\n\ts_add_u32 s8, s8, s10\n\ts_cmp_gt_u32 s8, 16\n\ts_cselect_b64 s[8:9], -1, vcc\n\t"
                     "s_cmp_lg_u64 s[8:9], 0\n\ts_waitcnt lgkmcnt(0)\n\ts_cbranch_scc1 6f\n\t"
                     "v_sub_u32 %[t0], %[se], v152\n\tv_sub_u32 %[t1], %[se], v153\n\tv_sub_u32 %[t2], %[se], v154\n\tv_sub_u32 %[t3], %[se], v155\n\t"
                     RANK8("%[t0]", "%[t1]", "%[t2]", "%[t3]", "%[rk]", "%[t3]", "0", "v_mov_b32 %[km], %[se]\n\ts_nop 0", "s_nop 1") "6:\n\t" TS(62, 63)
                     : [hi] "=&v"(hi), [lo] "=&v"(lo), [k2] "=&v"(k2), [k3] "=&v"(k3), [gd] "=&v"(gd), [se] "=&v"(se), [km] "=&v"(km), [rk] "=&v"(rk),
                       [t0] "=&v"(t0), [t1] "=&v"(t1), [t2] "=&v"(t2), [t3] "=&v"(t3), [n] "+s"(n_)
                     : [o] "v"(O_TOP + 16 * (l & 7) + TL * (l >> 3)), [r] "s"(rs), [t] "s"(tag), [ts] "s"(TOPS), [st] "s"(st0), [tw] "n"(TW), [mk] "s"(0x7fff800fu),
                       [pa] "v"((127 - 2 * l) << 4), [pb] "v"((126 - 2 * l) << 4), [sg] "s"(sg), [p0] "s"(0x05040100u), [p1] "s"(0x07020500u), [ca] "s"(ca),
                       [kr] "v"(kr), [kq] "v"(kq)
                     : "vcc", "scc", "s6", "s8", "s9", "s10", "s11", "memory" TSC);
      }
      unsigned long long more;
      asm volatile("" : "={s6}"(thr), "={s[8:9]}"(more));
      if (__builtin_expect(more != 0, 0)) {
        const u32 m0 = km;
        unsigned long long all = __ballot(gd >= thr);
        if (gd >= thr) hi = lo = k2 = k3 = 0u;
#pragma unroll 1
        for (int i = 0; i < 4;) {
          u32 k, m = m0;
          if (all) {
            const int a = __builtin_ctzll(all);
            all &= all - 1;
            u32x2 r;
            const u32 o = O_RG + 256 * a + 8 * (l & 31);
            POLL8(r, o);
            k = l < 32 ? r.x : 0u;
            m = (u32)__builtin_amdgcn_readlane((int)m0, a);
          } else { k = hi; hi = lo; lo = k2; k2 = k3; i++; }
          asm volatile("v_cmp_le_u32 s[8:9], %[t], %[k]\n\tv_cmp_ne_u32 vcc, %[k], %[m0]\n\ts_and_b64 s[8:9], s[8:9], vcc\n\ts_cbranch_scc0 2f\n"
                       "1:\n\ts_ff1_i32_b64 s7, s[8:9]\n\tv_readlane_b32 s12, %[k], s7\n\ts_lshl_b64 s[10:11], 1, s7\n\ts_nop 0\n\t"
                       "v_cmpx_gt_u32 vcc, s12, %[km]\n\tv_add_u32 %[rk], 8, %[rk]\n\ts_bcnt0_i32_b32 s7, vcc_lo\n\tv_cmpx_eq_u32 vcc, 0x80, %[rk]\n\t"
                       "v_mov_b32 %[km], s12\n\tv_lshlrev_b32 %[rk], 2, s7\n\ts_mov_b64 exec, -1\n\ts_andn2_b64 s[8:9], s[8:9], s[10:11]\n\ts_cbranch_scc1 1b\n2:"
                       : [km] "+v"(km), [rk] "+v"(rk) : [k] "v"(k), [m0] "v"(m), [t] "s"(thr) : "vcc", "scc", "s7", "s8", "s9", "s10", "s11", "s12");
        }
      }
      if (id < 126) {
        u32 a_, v_, n_ = SPIN;
#define RLD(o, s_) "buffer_load_dwordx4 %[v], %[rx], " s_ " offen offset:" #o " lds\n\t"
        asm volatile("v_add_u32 %[a], %[kb], %[rk]\n\tv_and_b32 %[v], 0x7ff, %[km]\n\ts_mov_b32 m0, %[fa]\n\tds_write_b32 %[a], %[v]\n\tds_read_b32 %[a], %[ka]\n\t"
                     "s_cmp_eq_u32 %[ns], 8\n\ts_waitcnt lgkmcnt(0)\n\tv_mad_i32_i24 %[v], %[a], %[cm], %[kc]\n\t"
                     RLD(0, "%[c3]") RLD(1024, "%[c2]") RLD(2048, "%[c1]") RLD(3072, "0")
                     "s_mov_b32 m0, %[fb]\n\tv_add_u32 %[v], 0x200, %[v]\n\tv_mad_i32_i24 %[a], %[a], -8, %[cs]\n\t"
                     "s_cbranch_scc0 3f\n\t" RLD(2048, "%[c1]") RLD(3072, "0")
                     "3:\n\tbuffer_load_dwordx2 v[160:161], %[a], %[rs], 0 offen sc1\n\t" RLD(0, "%[c3]") RLD(1024, "%[c2]") "buffer_load_dwordx2 v[158:159], %[co], %[rs], 0 offen sc1\n\t"
                     "v_mov_b32 %[v], 16 * " STR(HOLD) " - 112\n\tv_subrev_u32 %[ka], %[kd], %[ka]\n\ts_mov_b64 exec, 1\n\tds_add_u32 %[fc], %[v] offset:8\n\ts_mov_b64 exec, -1\n\tv_mov_b32 %[v], -1\n\t" TS(64, 65)
                     "s_sleep " STR(CSL) "\n\tbuffer_load_dwordx2 v[154:155], %[co], %[rs], 0 offen sc1\n\t"
                     "s_waitcnt vmcnt(4)\n1:\n\tv_cmp_ne_u32 vcc, %[t], v161\n\tv_and_b32 v156, 0xffff0000, v160\n\tv_lshlrev_b32 v157, 16, v160\n\tv_rcp_f32 v156, v156\n\t"
                     "s_sub_u32 %[n], %[n], 1\n\ts_cbranch_vccz 2f\n\ts_cbranch_scc1 2f\n\t"
                     "buffer_load_dwordx2 v[160:161], %[a], %[rs], 0 offen sc1\n\ts_waitcnt vmcnt(0)\n\ts_branch 1b\n2:\n\tv_mul_f32 v157, -v157, v156\n\t"
                     "ds_write_b64 %[ka], v[156:157]\n\ts_waitcnt vmcnt(2)\n\tds_write_b32 %[fc], %[v] offset:4\n\t" TS(66, 67)
                     "s_mov_b32 %[n], %[spin]\n\ts_waitcnt vmcnt(1)\n\tv_cmp_ne_u32 vcc, %[t], v159\n\ts_cbranch_vccz 5f\n"
                     "4:\n\ts_waitcnt vmcnt(0)\n\tv_cmp_ne_u32 vcc, %[t], v155\n\ts_sub_u32 %[n], %[n], 1\n\ts_cbranch_vccz 5f\n\t"
                     "buffer_load_dwordx2 v[154:155], %[co], %[rs], 0 offen sc1\n\ts_cbranch_scc0 4b\n5:"
                     : [a] "=&v"(a_), [v] "=&v"(v_), [ka] "+v"(ka), [n] "+s"(n_)
                     : [co] "v"(co), [rk] "v"(rk), [km] "v"(km), [kc] "v"(kc), [kb] "n"(FC + 384), [kd] "n"(384 - 32), [fa] "n"(FA), [fb] "n"(FA + 4096), [rx] "s"(rx), [rs] "s"(rs),
                       [c1] "s"(c1), [c2] "s"(c2), [c3] "s"(c3), [cm] "s"(cm), [cs] "s"(cs), [ns] "s"(ns), [fc] "v"(fa), [t] "s"(tag), [spin] "n"(SPIN) : "vcc", "scc", "memory" TSC);
        TSGET(12, 13, 14, 15);
        FLAG(FC + 28, 1u);
        STAMP(10);
      } else {
        const u32 cd = km << 1;
        const u32x2 r = {(2047u - (km & 0x7ffu)) | ((cd ^ ((u32)((int)~cd >> 15) | 0x80000000u)) & 0xffff0000u), tag};
        const u32 o = rk < 64u ? O_IDX + 1024 * mat + 128 * kg + rk : 0x80000000u;
        st8(rs, o, r);
        st8(rs, o + 512, r);
        TSGET(12, 13, 14, 15);
        STAMP(14);
        STAMP(15);
        if (mat) {
#pragma unroll 1
          for (int spin = 0; spin < SPIN; spin++) {
            const u32x4 a = ld16(rs, O_ARR + 16 * l);
            if (__all(a.y == tag && a.w == tag)) break;
            asm volatile("s_sleep 16" ::: "memory");
          }
          if (l == 0) __builtin_amdgcn_raw_buffer_store_b32(tag, rs, 0, 0, 16);
        }
      }
      FLUSH();
      __builtin_amdgcn_endpgm();
    }
    u32x4 xv[4];
    u32x8 wv;
    ROLE(l, "", , "={v[100:103]}"(xv[0]), "={v[104:107]}"(xv[1]), "={v[108:111]}"(xv[2]), "={v[112:115]}"(xv[3]), "={v[116:123]}"(wv));
    STAMP(11);
    u32 sg = 0x0c0c0b09u;
    asm volatile("" : "+s"(sg));
    ln_rows(xv, wv, rs, tag, b, g, l, lds0 + FC + 128, sg TLVAL);
    STAMP(10);
    IDS();
    if (id >= 126) {
      FLUSH();
      __builtin_amdgcn_endpgm();
    }
    const int wr = 16 * (kg >> 1) + 4 * (kg & 1) + (n16 & 8) + ((n16 >> 2) & 1);
    u32 wa = lds0 + 256 * wr + ((w ^ ((wr & 1) | ((wr >> 1) & 6))) << 5) + 8 * (l & 3), sel = (l & 8) ? 0xeeeeu : 0x4444u;
    u32 so = 4 * t;
    asm volatile("" : "+v"(wa), "+v"(sel), "+v"(so));
    asm volatile(UNTIL(STR(CLD)) :: [st] "s"(st0) : "s8", "s9", "scc");
    __builtin_amdgcn_raw_buffer_store_b32(~0u, rs, so, O_HG + (6 * cb + 3 * mat + part) * 2048, 16);
    st8(rs, u < 8 ? O_PG + (4 * kp + ct) * 4096 + 2 * so : 0x80000000u, u32x2{~0u, ~0u});
#define SWP(a0, a1, a2, a3) "v_permlane32_swap_b32 " a0 ", " a2 "\n\tv_permlane32_swap_b32 " a1 ", " a3 "\n\t"
#define OUT(a0, a1, o) "v_cvt_pk_bf16_f32 " a0 ", " a0 ", " a1 "\n\tv_min_u32 " a0 ", -2, " a0 "\n\tbuffer_store_dword " a0 ", %[so], %[rs], " o " sc1\n\t"
#define TR4(a, b_, c, d, e, f, g_, h, adr, o) "ds_read_b64_tr_b16 v[" #a ":" #b_ "], " adr " offset:" #o "\n\tds_read_b64_tr_b16 v[" #c ":" #d "], " adr " offset:" #o "+512\n\t" \
      "ds_read_b64_tr_b16 v[" #e ":" #f "], " adr " offset:" #o "+8192\n\tds_read_b64_tr_b16 v[" #g_ ":" #h "], " adr " offset:" #o "+8704\n\t"
#define SM(d, a, b_) "v_smfmac_f32_16x16x64_bf16 v[" d "], v[" a "], v[" b_ "], %[sel]\n\t"
#define USE(c, xa, wf) "s_add_u32 s6, s6, 16\n\ts_waitcnt lgkmcnt(" #c ")\n\t" SM("16:19", "20:23", wf) SM("8:11", xa, wf)
#define XR(xa, o) "ds_read_b128 v[" xa "], %[xa] offset:" #o "\n\t"
#define XU(c, xa, wf) "s_waitcnt lgkmcnt(" #c ")\n\t" SM("8:11", xa, wf)
#define ZERO(r) "v_mfma_f32_32x32x16_bf16 v[" r "], v[8:11], v[8:11], 0\n\t"
#define POLL(c, rd) "7:\n\tds_read_b32 v30, %[fl]\n\ts_waitcnt lgkmcnt(0)\n\ts_call_b64 s[10:11], 1f\n\ts_cbranch_scc1 7b\n\t" rd
#define CLR "s_waitcnt vmcnt(0)\n\ts_mov_b64 exec, 1\n\tds_add_rtn_u32 v14, %[fl], v28 offset:96\n\ts_waitcnt lgkmcnt(0)\n\tv_cmpx_eq_u32 vcc, 7, v14\n\t" \
      "buffer_store_dwordx2 v[12:13], off, %[rs], %[co] sc1\n\ts_mov_b64 exec, -1\n\t"
#define XE(k) "s_cmpk_eq_u32 s6, " #k "\n\ts_cbranch_scc1 9f\n\t"
    asm volatile("v_mov_b64 v[8:9], 0\n\tv_mov_b64 v[10:11], 0\n\tv_mov_b64 v[16:17], 0\n\tv_mov_b64 v[18:19], 0\n\t"
                 ZERO("96:111") ZERO("112:127")
                 "v_mov_b32 v20, 0x3f803f80\n\tv_mov_b32 v21, v20\n\tv_mov_b64 v[22:23], v[20:21]\n\tv_mov_b32 v13, %[t]\n\tv_mov_b32 v28, 1\n\tv_mov_b32 v30, 0\n\t"
                 "v_add_u32 v29, 0x10000, %[wa]\n\ts_movk_i32 s6, 96\n\ts_mov_b32 s7, 1\n\ts_mov_b32 s8, 15\n\ts_mov_b32 s9, %[spin]\n\ts_nop 7\n\t"
                 POLL(8, TR4(32, 33, 34, 35, 36, 37, 38, 39, "%[wa]", 0) TR4(40, 41, 42, 43, 44, 45, 46, 47, "%[wa]", 16384))
                 USE(4, "96:99", "32:39") "s_waitcnt lgkmcnt(0)\n\t" SM("16:19", "20:23", "40:47") SM("8:11", "100:103", "40:47")
                 CLR
                 POLL(4, TR4(48, 49, 50, 51, 52, 53, 54, 55, "%[wa]", 32768)) USE(0, "104:107", "48:55")
                 POLL(4, TR4(56, 57, 58, 59, 60, 61, 62, 63, "%[wa]", 49152)) "s_mov_b64 exec, 1\n\tds_add_u32 %[fl], v28 offset:20\n\ts_mov_b64 exec, -1\n\t" TS(60, 61)
                 USE(0, "108:111", "56:63")
                 POLL(4, TR4(64, 65, 66, 67, 68, 69, 70, 71, "v29", 0)) USE(0, "112:115", "64:71") "s_cmp_eq_u32 %[ns], 5\n\ts_cbranch_scc1 5f\n\t"
                 POLL(4, TR4(72, 73, 74, 75, 76, 77, 78, 79, "v29", 16384)) USE(0, "116:119", "72:79") "s_cmp_eq_u32 %[ns], 6\n\ts_cbranch_scc1 5f\n\t"
                 POLL(4, TR4(80, 81, 82, 83, 84, 85, 86, 87, "v29", 32768)) USE(0, "120:123", "80:87")
                 POLL(4, TR4(88, 89, 90, 91, 92, 93, 94, 95, "v29", 49152)) USE(0, "124:127", "88:95")
                 "5:\n\t" TS(62, 63) "s_cmp_eq_u32 s7, 0\n\ts_cbranch_scc1 4f\n\ts_mov_b32 s8, 16\n\t"
                 "s_getpc_b64 s[10:11]\n\tds_read_b32 v30, %[fl]\n\ts_waitcnt lgkmcnt(0)\n"

                 "1:\n\tv_cmp_le_u32 vcc, s6, v30\n\ts_and_b32 s12, vcc_hi, s7\n\ts_cbranch_scc1 3f\n"
                 "2:\n\ts_andn2_b32 s12, s8, vcc_lo\n\ts_cbranch_scc1 6f\n\ts_setpc_b64 s[10:11]\n"
                 "6:\n\ts_sub_u32 s9, s9, 1\n\ts_cbranch_scc1 4f\n\t" GNAP "\n\ts_cmp_eq_u32 s6, s6\n\ts_setpc_b64 s[10:11]\n"
                 "3:\n\t"
                 XR("96:99", 0) XR("100:103", 1024) XR("104:107", 2048) XR("108:111", 3072) XR("112:115", 4096) XR("116:119", 5120) XR("120:123", 6144) XR("124:127", 7168)
                 "ds_read_b128 v[24:27], %[pa]\n\ts_mov_b32 s7, 0\n\t"
                 TS(64, 65)
                 XE(96) XU(8, "96:99", "32:39") XU(7, "100:103", "40:47") XE(112) XU(6, "104:107", "48:55") XE(128) XU(5, "108:111", "56:63") XE(144)
                 XU(4, "112:115", "64:71") XE(160) XU(3, "116:119", "72:79") XE(176) XU(2, "120:123", "80:87") XE(192) XU(1, "124:127", "88:95")
                 "9:\n\ts_cmp_eq_u32 s8, 16\n\ts_waitcnt lgkmcnt(0)\n\ts_cbranch_scc0 2b\n"
                 "4:\n\ts_nop 4\n\tv_permlane32_swap_b32 v16, v17\n\tv_add_f32 v16, v16, v17\n\tv_mul_f32 v25, v25, v16\n\tv_mul_f32 v27, v27, v16\n\t"
                 SWP("v8", "v9", "v10", "v11") "v_fmac_f32 v25, v10, v24\n\tv_fmac_f32 v27, v11, v26\n\tv_fma_f32 v8, v8, v24, v25\n\tv_fma_f32 v9, v9, v26, v27\n\t"
                 OUT("v8", "v9", "%[ho] offen") TS(66, 67)
                 :: [wa] "v"(wa), [sel] "v"(sel), [so] "v"(so), [fl] "v"(lds0 + FC + 4 * min(l, 4)), [xa] "v"(lds0 + FA + 128 * (l & 7) + 16 * ((2 * kg + (n16 >> 3)) ^ (2 * l & 4))),
                    [pa] "v"(lds0 + FC + 32 + 32 * (kg & 1) + 16 * (kg >> 1)), [ns] "s"(ns), [t] "s"(tag), [co] "s"(O_CLR + 8 * id), [rs] "s"(rs), [ho] "s"(O_HG + (6 * cb + 3 * mat + part) * 2048), [spin] "n"(SPIN)
                 : "vcc", "scc", "s6", "s7", "s8", "s9", "s10", "s11", "s12", "s13", "memory", G_CLOBBER TSC);
    TSGET(1, 4, 3, 5);
    if (u < 8) {
      const int tp = 2 * (kg & 1) + (kg >> 1), r7 = 4 * (kg & 1) + (n16 >> 2);
      u32 o3 = (kp & 3) == 1 ? so + 4096 : 0x80000000u, h0 = O_HG + kp * 12288, h1 = h0 + 6144;
      u32 hw = lds0 + FH + 2 * tp * HP + 32 * w + 8 * (l & 2) + (l & 4) * 2 + ((l & 8) >> 1) + 2 * (l & 1);
      u32 hr = lds0 + FH + (l & 7) * HP + 32 * kg + 2 * (l & 8);
      u32 da = lds0 + 512 * (16 * (kg >> 1) + r7) + ((2 * w ^ r7) << 5) + 8 * (l & 3), db = da ^ 32, fl = lds0 + FC + 4 * (l & 3);
      u32 po = 512 * w + 16 * (l & 8) + 32 * tp + 4 * (l & 7);
      u32 e0 = 0x3f80u, e1 = 0x3f800000u;
      asm volatile("" : "+v"(hw), "+v"(hr), "+v"(da), "+v"(db), "+v"(fl), "+v"(po), "+v"(o3));
      asm volatile("" : "+s"(e0), "+s"(e1), "+s"(h0), "+s"(h1));
#define TRD(a, b_, c, d, e, f, g_, h, adr, o) "ds_read_b64_tr_b16 v[" #a ":" #b_ "], " adr " offset:" #o "\n\tds_read_b64_tr_b16 v[" #c ":" #d "], " adr " offset:" #o "+4096\n\t" \
      "ds_read_b64_tr_b16 v[" #e ":" #f "], " adr " offset:" #o "+16384\n\tds_read_b64_tr_b16 v[" #g_ ":" #h "], " adr " offset:" #o "+20480\n\t"
#define HLD(X, r) X(r "0", "%[s1]", "%[h0] offen") X(r "3", "%[s1]", "%[h1] offen") X(r "1", "%[s1]", "%[h0] offen offset:2048") X(r "4", "%[s1]", "%[h1] offen offset:2048") \
      X(r "2", "%[o3]", "%[h0] offen") X(r "5", "%[o3]", "%[h1] offen")
#define HL0(a, o, s_) "buffer_load_dword " a ", " o ", %[rs], " s_ " sc1\n\t"
#define HLF(a, o, s_) "buffer_load_dword " a ", " o ", %[rs], " s_ SCH "\n\t"
#define HL1(a, o, s_) "v_cmpx_eq_u32 vcc, -1, " a "\n\t" HL0(a, o, s_) "s_mov_b64 exec, -1\n\t"
#define DOT(d, e, a) "v_dot2c_f32_bf16 " d ", %[" e "], " a "\n\t"
#define HCK(c, r) "s_cmp_eq_u32 s7, 0\n\ts_cbranch_scc1 5f\n\ts_call_b64 s[10:11], 4f\n5:\n\ts_waitcnt vmcnt(" #c ")\n\tv_max3_u32 v28, " r "0, " r "1, " r "2\n\tv_lshlrev_b32 v24, 16, " r "0\n\tv_max3_u32 v28, v28, " r "3, " r "4\n\t" \
      "v_and_b32 v25, %[hm], " r "0\n\tv_max_u32 v28, v28, " r "5\n\t" DOT("v24", "e0", r "1") "v_cmp_eq_u32 vcc, -1, v28\n\t" DOT("v25", "e1", r "1") DOT("v24", "e0", r "2") DOT("v25", "e1", r "2") \
      "v_lshlrev_b32 v26, 16, " r "3\n\tv_and_b32 v27, %[hm], " r "3\n\t" DOT("v26", "e0", r "4") DOT("v27", "e1", r "4") DOT("v26", "e0", r "5") DOT("v27", "e1", r "5")
#define RB2(a, b_) "v_cvt_pk_bf16_f32 " a ", 0, " a "\n\tv_cvt_pk_bf16_f32 " b_ ", 0, " b_ "\n\t"
      asm volatile("s_mov_b32 s7, 1\n\ts_mov_b32 s12, %[spin]\n\tv_mov_b64 v[8:9], 0\n\tv_mov_b64 v[10:11], 0\n\tv_mov_b64 v[12:13], 0\n\tv_mov_b64 v[14:15], 0\n"
                   "2:\n\ts_sleep 1\n\tds_read_b32 v28, %[fl] offset:16\n\ts_memrealtime s[8:9]\n\ts_waitcnt lgkmcnt(0)\n\tv_cmp_eq_u32 vcc, 0, v28\n\ts_sub_u32 s8, s8, %[st]\n\t"
                   "s_cmp_lt_u32 s8, %[th]\n\ts_cbranch_scc1 2b\n\ts_bitcmp1_b32 vcc_lo, 3\n\ts_cbranch_scc1 2b\n\t" TS(64, 65)
                   HLD(HLF, "v7") HCK(0, "v7") "s_cbranch_vccnz 9f\n"
                   "3:\n\t" TS(66, 67) RB2("v24", "v25") "v_mul_f32 v28, %[le], v24\n\tv_mul_f32 v29, %[le], v25\n\tv_exp_f32 v28, v28\n\tv_exp_f32 v29, v29\n\t" RB2("v26", "v27")
                   "v_add_f32 v28, 1.0, v28\n\tv_add_f32 v29, 1.0, v29\n\tv_rcp_f32 v28, v28\n\tv_rcp_f32 v29, v29\n\ts_cmp_eq_u32 s7, 0\n\tv_mul_f32 v24, v24, v28\n\tv_mul_f32 v25, v25, v29\n\t"
                   RB2("v24", "v25") "v_mul_f32 v24, v24, v26\n\tv_mul_f32 v25, v25, v27\n\tv_cvt_pk_bf16_f32 v24, v24, v25\n\ts_mov_b32 s12, %[spin]\n\t"
                   "ds_write_b16 %[hw], v24\n\tds_write_b16_d16_hi %[hw], v24 offset:%[hp]\n\ts_cbranch_scc1 8f\n"
                   "7:\n\ts_call_b64 s[10:11], 4f\n\ts_mul_i32 s12, s12, s7\n\ts_sub_u32 s12, s12, 1\n\ts_cbranch_scc0 7b\n"
                   "8:\n\ts_waitcnt lgkmcnt(0)\n\ts_barrier\n\t" TS(60, 61)
                   XR("16:19", 0) XR("20:23", 128)
                   XU(1, "16:19", "32:39") SM("12:15", "16:19", "40:47") XU(0, "20:23", "48:55") SM("12:15", "20:23", "56:63") "s_nop 6\n\t"
                   SWP("v8", "v9", "v10", "v11") "v_add_f32 v8, v8, v10\n\tv_add_f32 v9, v9, v11\n\t" SWP("v12", "v13", "v14", "v15") OUT("v8", "v9", "%[po] offen")
                   "v_add_f32 v12, v12, v14\n\tv_add_f32 v13, v13, v15\n\t" OUT("v12", "v13", "%[po] offen offset:256") TS(62, 63) DONE
                   "9:\n\tv_mov_b64 v[80:81], v[70:71]\n\tv_mov_b64 v[82:83], v[72:73]\n\tv_mov_b64 v[84:85], v[74:75]\n\t" HLD(HL1, "v7") "s_sleep " STR(HNAP) "\n"
                   "1:\n\t" HLD(HL1, "v8") HCK(6, "v7") "s_cbranch_vccz 3b\n\t" HLD(HL1, "v7") HCK(6, "v8") "s_sub_u32 s12, s12, 1\n\ts_cbranch_vccz 3b\n\ts_cbranch_scc0 1b\n\ts_branch 3b\n"
                   "4:\n\tds_read_b32 v28, %[fl]\n\ts_waitcnt lgkmcnt(0)\n\tv_cmp_ne_u32 vcc, -1, v28\n\ts_cbranch_vccnz 6f\n\t"
                   TRD(32, 33, 34, 35, 36, 37, 38, 39, "%[da]", 0) TRD(40, 41, 42, 43, 44, 45, 46, 47, "%[db]", 0)
                   TRD(48, 49, 50, 51, 52, 53, 54, 55, "%[da]", 32768) TRD(56, 57, 58, 59, 60, 61, 62, 63, "%[db]", 32768) "s_mov_b32 s7, 0\n"
                   "6:\n\ts_setpc_b64 s[10:11]\n0:"
                   :: [s1] "v"(so), [o3] "v"(o3), [fl] "v"(fl), [da] "v"(da), [db] "v"(db), [hw] "v"(hw), [xa] "v"(hr), [sel] "v"(sel), [so] "v"(po), [rs] "s"(rs), [h0] "s"(h0), [h1] "s"(h1),
                      [st] "s"(st0), [th] "n"(TH), [spin] "n"(SPIN), [hp] "n"(HP), [po] "s"(O_PG + (4 * kp + ct) * 4096), [e0] "s"(e0), [e1] "s"(e1), [hm] "s"(0xffff0000u),
                      [le] "s"(-1.4426950408889634f)
                   : "vcc", "scc", "s7", "s8", "s9", "s10", "s11", "s12", "memory", G_CLOBBER TSC);
      TSGET(8, 9, 6, 7);
    }
    FLUSH();
    __builtin_amdgcn_endpgm();
  }

  if (w0 >= 8) {
    u32x8 xa0, xa1, xa2, xa3, sa1;
    u32x4 junk;
    u32 op, so;
    ROLE(p, "", , "={v[100:107]}"(xa0), "={v[108:115]}"(xa1), "={v[116:123]}"(xa2),
         "={v148}"(op), "={v149}"(so), "={v[160:163]}"(junk));
    const int p = w - 8;
    __builtin_amdgcn_s_setprio(3);
    asm volatile("s_waitcnt lgkmcnt(0)" : "+{s44}"(tag)::"memory");
    tag += 1u;
    STAMP(9);
    f32x2 xq[4][8];
    u32x8 pa;
    {
#define SCK(r1, r3, r5, r7) "v_xor_b32 %[d], %[t], v" #r1 "\n\tv_bitop3_b32 %[d], %[d], %[t], v" #r3 " bitop3:0xf6\n\t" \
      "v_bitop3_b32 %[d], %[d], %[t], v" #r5 " bitop3:0xf6\n\tv_bitop3_b32 %[d], %[d], %[t], v" #r7 " bitop3:0xf6\n\t"
#define SLX(r0, r3, r4, r7, k, k1, sc) "buffer_load_dwordx4 v[" #r0 ":" #r3 "], %[so], %[rs], " k " offen" sc "\n\tbuffer_load_dwordx4 v[" #r4 ":" #r7 "], %[so], %[rs], " k1 " offen" sc "\n\t"
#define SLD(r0, r3, r4, r7, k, k1) SLX(r0, r3, r4, r7, k, k1, " sc1")
#define SLOOP(r0, r1, r3, r4, r5, r7, k, k1) "1:\n\ts_waitcnt vmcnt(0)\n4:\n\t" SCK(r1, r3, r5, r7) "s_sub_u32 %[n], %[n], 1\n\tv_cmp_ne_u32 vcc, 0, %[d]\n\ts_cbranch_scc1 2f\n\ts_cbranch_vccz 2f\n\t" \
      SLD(r0, r3, r4, r7, k, k1) "s_branch 1b\n2:\n\t"
      u32 d_, n_ = SPIN, k1 = 4096, k2 = 8192, k3 = 12288, s0[4];
      asm volatile("s_mov_b32 m0, %[m]\n\ts_cmp_eq_u32 %[k], 3\n\ts_waitcnt vmcnt(2)\n\ts_cbranch_scc0 1f\n\ts_waitcnt vmcnt(0)\n1:"
                   : "+{v[100:107]}"(xa0), "+{v[108:115]}"(xa1), "+{v[160:163]}"(junk) : [m] "s"(lds0 + OX + 2 * XO + 256 * p), [k] "s"(xk) : "scc", "memory");
      SB();
#define UNPACK(i, xs) _Pragma("unroll") for (int c = 0; c < 8; c++) { \
        xq[i][c] = (c & 1) ? f32x2{hi2f(xs[c >> 1]), hi2f(xs[4 + (c >> 1)])} : f32x2{lo2f(xs[c >> 1]), lo2f(xs[4 + (c >> 1)])}; asm volatile("" : "+v"(xq[i][c].x), "+v"(xq[i][c].y)); }
      UNPACK(0, xa0);
      UNPACK(1, xa1);
      u32 fa = lds0 + OXF + 4 * p, fv = 1;
      asm volatile("" : "+v"(fa), "+v"(fv));
      asm volatile("" : "+s"(k1), "+s"(k2), "+s"(k3), "+s"(n_));
      SB();
      STAMP(1);
#define SCP(r0, r1, r2, r3, r4, r5, r6, r7) "s_waitcnt vmcnt(2)\n\t" SCK(r1, r3, r5, r7) "v_cmp_ne_u32 vcc, 0, %[d]\n\tv_mov_b32 %[a0], v" #r0 "\n\tv_mov_b32 %[a1], v" #r2 "\n\t" \
      "v_mov_b32 %[a2], v" #r4 "\n\tv_mov_b32 %[a3], v" #r6 "\n\ts_cbranch_vccz 3f\n\t" SLD(r0, r3, r4, r7, "0", "%[k1]")
      asm volatile(UNTIL(STR(TSG)) SLD(132, 135, 136, 139, "0", "%[k1]") "s_sleep " STR(SSL) "\n\t" SLD(140, 143, 144, 147, "0", "%[k1]") "s_waitcnt vmcnt(4)"
                   : "=&{v[132:139]}"(pa), "=&{v[140:147]}"(sa1), "+{v[116:123]}"(xa2)
                   : [st] "s"(st0), [so] "v"(so), [rs] "s"(rs), [k1] "s"(k1) : "scc", "s8", "s9", "memory");
      UNPACK(2, xa2);
      SB();
      asm volatile("1:\n\t" SCP(132, 133, 134, 135, 136, 137, 138, 139) SCP(140, 141, 142, 143, 144, 145, 146, 147) "s_sub_u32 %[n], %[n], 1\n\ts_cbranch_scc0 1b\n"
                   "3:\n\t" SLD(140, 143, 144, 147, "%[k2]", "%[k3]")
                   : [d] "=&v"(d_), [n] "+s"(n_), [a0] "=&v"(s0[0]), [a1] "=&v"(s0[1]), [a2] "=&v"(s0[2]), [a3] "=&v"(s0[3]), "+{v[132:139]}"(pa), "+{v[140:147]}"(sa1)
                   : [t] "s"(tag), [so] "v"(so), [rs] "s"(rs), [k1] "s"(k1), [k2] "s"(k2), [k3] "s"(k3) : "vcc", "scc", "memory");
#pragma unroll
      for (int i = 0; i < 5; i++) {
#define XLD(r0, r1, r2, r3, o_) "v_add_u32 %[d], " #o_ ", %[o]\n\tglobal_load_dwordx4 v[" #r0 ":" #r1 "], %[d], %[x]\n\tglobal_load_dwordx4 v[" #r2 ":" #r3 "], %[d], %[x] offset:2048\n"
        if (i == XI) asm volatile("s_cmp_eq_u32 %[k], 3\n\ts_cbranch_scc0 1f\n\t" XLD(116, 119, 120, 123, 0x200000) "1:\n\t" XLD(124, 127, 128, 131, 0x300000)
                                 : [d] "=&v"(d_), "+{v[116:123]}"(xa2), "=&{v[124:131]}"(xa3) : [k] "s"(xk), [o] "v"(op), [x] "s"(x) : "scc", "memory");
        if (i == 2) {
          asm volatile("s_waitcnt vmcnt(2)\n\ts_branch 4f\n" SLOOP(140, 141, 143, 144, 145, 147, "%[k2]", "%[k3]") : [d] "=&v"(d_), [n] "+s"(n_), "+{v[140:147]}"(sa1), "+{v[116:123]}"(xa2), "+{v[132:139]}"(pa)
                       : [t] "s"(tag), [so] "v"(so), [rs] "s"(rs), [k2] "s"(k2), [k3] "s"(k3) : "vcc", "scc", "memory");
          if (xk == 3) { UNPACK(2, xa2); }
        }
        if (i == 3) asm volatile("s_waitcnt vmcnt(%1)" : "+{v[124:131]}"(xa3) : "n"(DPI == 2 ? DPF : 0));
        if (i == DPI && DPF) {
          u32 lx = l;
          asm volatile("" : "+v"(lx));
          const u32 ti = min(16 * xk + 4 * p + (lx >> 4), 125u), tsb = __umul24(ti, 57) >> 10, tu = min(ti - __umul24(18, tsb), 15u);
          const u32 o = (128 * (4 * tsb + (tu >> 2)) + DPO / 2 + 4 * j + ((lx >> 2) & 3)) * (2 * D) + 512 * (tu & 3) + 128 * (lx & 3);
          if (DPI == 4) AWAIT(OXF + 4 * (l & 3), 3, "s_sleep 1");
          if (DPF == 1) asm volatile("global_load_dword v160, %1, %2" : "=&{v[160:163]}"(junk) : "v"(o), "s"(wd) : "memory");
          else asm volatile("global_load_dword v160, %1, %2\n\tglobal_load_dword v161, %1, %3" : "=&{v[160:163]}"(junk) : "v"(o), "s"(wd), "s"(wd + 64 * D) : "memory");
        }
        if (i == 4) break;
        if (i == 3) UNPACK(3, xa3);
        const u32 sa = i < 2 ? s0[2 * i] : sa1[4 * i - 8], sb = i < 2 ? s0[2 * i + 1] : sa1[4 * i - 6];
        if (i == 0) STAMP(3);
        if (i == 2) STAMP(10);
        const f32x2 r2 = {__builtin_amdgcn_rcpf(hi2f(sa)), __builtin_amdgcn_rcpf(hi2f(sb))};
        const f32x2 m2 = {-lo2f(sa) * r2.x, -lo2f(sb) * r2.y};
        f32x2 n2[8];
        u32 o[8];
        SB();
#pragma unroll
        for (int c = 0; c < 8; c++) n2[c] = f32x2{fmav(xq[i][c].x, r2.x, m2.x), fmav(xq[i][c].y, r2.y, m2.y)};
        SB();
#pragma unroll
        for (int c = 0; c < 8; c++) o[c] = packh(n2[c].x, n2[c].y);
        SB();
#pragma unroll
        for (int c = 0; c < 8; c++) asm volatile("ds_write_addtid_b32 %0 offset:%1" ::"v"(o[c]), "n"(c * XREG + 1024 * i) : "memory");
        asm volatile("ds_write_b32 %1, %0\n\tv_add_u32 %0, 1, %0" : "+v"(fv) : "v"(fa) : "memory");
        if (i == PDROP) __builtin_amdgcn_s_setprio(PPRIO);
        SB();
      }
    }
    STAMP(4);
    const rsrc_t ro = __builtin_amdgcn_make_buffer_rsrc((void*)out, 0, 2 * L * D, 0x00020000);
    u32 ys[8], y[2][8] = {};
    u32x2 ix;
    u32x4 cl;
    u32x8 pb;
    float yc, rsel;
    const u32 ixo = IXO((l & 6) + (n16 >> 3));
    if (p == 0) asm volatile("buffer_load_dwordx2 v[4:5], %1, %2, 0 offen sc1" : "=&{v[4:5]}"(ix) : "v"(ixo), "s"(rs) : "memory");
    const int rc = 4 * (l & 1) + kg, u = 64 * (p & 1) + l;
    u32x4 pick;
    u32 po = O_PG + 128 * g + 16 * (l & 7) + 16384 * (l >> 3), p3 = l < 32 ? po + 3 * 131072 : 0x80000000u, yf = lds0 + OYP + 4 * min(n16, 12) + 64 * (p >> 1);
    u32 ya = lds0 + OY + CHR * rc, oo = 16 * g + 2 * rc, clo = O_CLR + 16 * min(l, 62);
    u32 yp = lds0 + OY + 512 * (p >> 1) + 4 * (u ^ ((u >> 4) & 6)), yq = yp + 4 * CHR, bp = lds0 + OMISC + 16 * p + 4 * kg, bs = 30 - 2 * n16;
#pragma unroll
    for (int q = 0; q < 4; q++) pick[q] = (n16 >> 2) == q ? 0x3f80u << 16 * (l & 1) : 0u;
    asm volatile("" : "+v"(pick), "+v"(ya), "+v"(oo), "+v"(po), "+v"(p3), "+v"(yf), "+v"(clo), "+v"(yp), "+v"(yq), "+v"(bp), "+v"(bs));
#pragma unroll
    for (int c = 0; c < 8; c++) asm volatile("" : "+v"(y[0][c]), "+v"(y[1][c]));
    if (TE) asm volatile(UNTIL(STR(TE)) :: [st] "s"(st0) : "s8", "s9", "scc");
#pragma unroll
    for (int i = 0; i < 4; i++) {
      if (i == IXI && p == 0) {
        u32 n_ = SPIN;
        asm volatile("s_waitcnt vmcnt(%5)\n1:\n\tv_cmp_ne_u32 vcc, %4, v5\n\ts_sub_u32 %1, %1, 1\n\ts_cbranch_vccz 2f\n\ts_cbranch_scc1 2f\n\t"
                     "buffer_load_dwordx2 v[4:5], %2, %3, 0 offen sc1\n\ts_waitcnt vmcnt(0)\n\ts_branch 1b\n2:"
                     : "+{v[4:5]}"(ix), "+s"(n_) : "v"(ixo), "s"(rs), "s"(tag), "n"(2 * IXI + 1) : "vcc", "scc", "memory");
        const u32 prow = ix.x & 2047u, v = prow >> 1;
        if (l < 16) atomicOr((u32*)(smem + OMISC) + (prow >> 5), 1u << (ix.x & 31u));
        FLAG(OYP + 256 + 32 + 64 * (l & 3), 8u);
        oo += prow * (2 * D);
        asm volatile("v_mov_b32 v162, 0\n\tglobal_load_short_d16_hi v162, %1, %2" : "+{v[160:163]}"(junk) : "v"(oo), "s"(x) : "memory");
        yc = *(const float*)(smem + OSC + 4 * rc); rsel = rbt(sigm(hi2f(ix.x)));
        ya += 4 * (v ^ ((v >> 4) & 6)) + 2 * (prow & 1);
        asm volatile("" : "+v"(yc), "+v"(rsel), "+v"(ya));
        STAMP(11);
      }
      if (i == 2 && p == 0) cl = ld16(rs, clo);
      if (i == 3) {
        asm volatile("s_waitcnt vmcnt(2)" ::: "memory");
        FLAG(OYP + 448 + 36 + 4 * p, 8u);
        if (p == 0) {
#pragma unroll 1
          for (int spin = 0; spin < SPIN; spin++) {
            if (__all(cl.y == tag && cl.w == tag)) break;
            cl = ld16(rs, clo);
          }
          STAMP(12);
        }
        __builtin_amdgcn_s_setprio(LPRIO);
      }
      {
        u32 n_ = SPIN, f_;
        asm volatile("1:\n\tds_read_b32 %0, %2 offset:%3\n\ts_sub_u32 %1, %1, 1\n\ts_waitcnt lgkmcnt(0)\n\tv_cmp_eq_u32 vcc, 0, %0\n\ts_cbranch_scc1 2f\n\ts_and_b32 vcc_lo, vcc_lo, %4\n\t"
                     "s_cbranch_scc0 2f\n\ts_sleep %5\n\ts_branch 1b\n2:" : "=&v"(f_), "+s"(n_) : "v"(yf), "n"(128 * i), "n"(i < 2 ? 255 : 511), "n"(i < 3 ? YN0 : YN3) : "vcc", "scc", "memory");
      }
      if (i == 0) STAMP(5); else if (i == 1) STAMP(6); else if (i == 2) STAMP(7); else STAMP(8);
      u32 bm = 0;
      if (i >= 2) asm volatile("ds_read_b32 %0, %1 offset:%2" : "=v"(bm) : "v"(bp), "n"(64 * i) : "memory");
#pragma unroll
      for (int r = 0; r < 2; r++)
#pragma unroll
        for (int c = 0; c < 8; c++) asm volatile("ds_read_u16_d16_hi %0, %1 offset:%2" : "+v"(y[r][c]) : "v"(c < 4 ? yp : yq), "n"((c & 3) * CHR + 1024 * i + 2 * r) : "memory");
      if (i == 3 && p == 0) {
#define PLD(X) X(132, 133, 134, 135, "%[po]", "0") X(136, 137, 138, 139, "%[po]", "%[k1]") X(140, 141, 142, 143, "%[po]", "%[k2]") X(144, 145, 146, 147, "%[p3]", "0")
#define PL0(a, b_, c, d, o, s_) "buffer_load_dwordx4 v[" #a ":" #d "], " o ", %[rs], " s_ " offen sc1\n\t"
        asm volatile(PLD(PL0) : "=&{v[132:139]}"(pa), "=&{v[140:147]}"(pb) : [po] "v"(po), [p3] "v"(p3), [rs] "s"(rs), [k1] "s"(131072), [k2] "s"(262144) : "memory");
      }
      if (i == 0) {
        st8(rs, t == 512 ? O_ARR + b * 8 : 0x80000000u, u32x2{0u, tag});
        const u32x4 a = *(const u32x4*)(smem + OSC), c = *(const u32x4*)(smem + OSC + 16);
#pragma unroll
        for (int k = 0; k < 4; k++) { ys[k] = (u32)__builtin_amdgcn_readfirstlane((int)a[k]); ys[4 + k] = (u32)__builtin_amdgcn_readfirstlane((int)c[k]); }
      }
      SB();
#pragma unroll
      for (int r = 0; r < 2; r++) {
        asm volatile("s_waitcnt lgkmcnt(%9)" : "+v"(y[r][0]), "+v"(y[r][1]), "+v"(y[r][2]), "+v"(y[r][3]), "+v"(y[r][4]), "+v"(y[r][5]), "+v"(y[r][6]), "+v"(y[r][7]), "+v"(bm) : "n"(8 - 8 * r));
        u32x4 v;
#pragma unroll
        for (int k = 0; k < 4; k++)
          v[k] = pack2(fmas(__uint_as_float(y[r][2 * k]), ys[2 * k], xq[i][2 * k][r]), fmas(__uint_as_float(y[r][2 * k + 1]), ys[2 * k + 1], xq[i][2 * k + 1][r]));
        if (i >= 2 && r == 0) bm <<= bs;
        __builtin_amdgcn_raw_buffer_store_b128(v, ro, i < 2 ? op : op | (bm & 0x40000000u << r), (512 * i + r) * (2 * D), OAUX);
        SB();
        asm volatile("s_nop 1");
        SB();
      }
    }
    asm volatile("" ::"{v[160:163]}"(junk));
    STAMP(13);
    if (p == 0) {
      u32 n_ = SPIN;
#define PL1(a, b_, c, d, o, s_) "v_max3_u32 v150, v" #a ", v" #b_ ", v" #c "\n\tv_max_u32 v150, v150, v" #d "\n\tv_cmpx_eq_u32 vcc, -1, v150\n\t" PL0(a, b_, c, d, o, s_) "s_mov_b64 exec, -1\n\t"
#define PLE(X) X(112, 113, 114, 115, "%[po]", "0") X(116, 117, 118, 119, "%[po]", "%[k1]") X(120, 121, 122, 123, "%[po]", "%[k2]") X(124, 125, 126, 127, "%[p3]", "0")
#define PMX(a, b_) "v_max3_u32 v150, v150, v" a ", v" b_ "\n\t"
#define PMM(d, a, c) "v_mfma_f32_16x16x32_bf16 v[" d "], %[pk], v[" a "], " c "\n\t"
#define PCK(c, p, q) "s_waitcnt vmcnt(" #c ")\n\t" PMM("152:155", p "2:" p "5", "0") "v_max_u32 v150, v" p "2, v" p "3\n\t" PMX(p "4", p "5") PMM("156:159", p "6:" p "9", "0") PMX(p "6", p "7") PMX(p "8", p "9") \
      PMM("152:155", q "0:" q "3", "v[152:155]") PMX(q "0", q "1") PMX(q "2", q "3") PMM("156:159", q "4:" q "7", "v[156:159]") PMX(q "4", q "5") PMX(q "6", q "7") \
      "v_cmp_eq_u32 vcc, -1, v150\n\ts_sub_u32 %[n], %[n], 1\n\t"
#define PCP(a, b_, c, d) "v_mov_b64 v[" #a ":" #b_ "], v[" #c ":" #d "]\n\t"
      asm volatile(PCK(2, "13", "14") "s_cbranch_vccnz 9f\n"
                   "2:\n\t" TS(60, 61) "ds_read_b32 v150, %[yf] offset:448\n\tds_read_u16_d16_hi v151, %[ya]\n\ts_mov_b32 %[n], %[spin]\n\ts_nop 1\n\tv_add_f32 v152, v152, v156\n\tv_add_f32 v153, v153, v157\n\ts_nop 0\n\t"
                   "v_add_f32_dpp v152, v152, v152 row_ror:8 row_mask:0xf bank_mask:0x3\n\tv_add_f32_dpp v152, v153, v153 row_ror:8 row_mask:0xf bank_mask:0xc\n\t"
                   "v_cvt_pk_bf16_f32 v152, 0, v152\n\tv_mul_f32 v152, v152, %[rl]\n\tv_cvt_pk_bf16_f32 v152, 0, v152\n\tv_add_f32 v153, v162, v152\n"
                   "3:\n\ts_waitcnt lgkmcnt(0)\n\tv_cmp_eq_u32 vcc, 0, v150\n\tv_and_b32 v152, 0xffff0000, v151\n\ts_sub_u32 %[n], %[n], 1\n\ts_cbranch_vccz 4f\n\ts_cbranch_scc1 4f\n\t"
                   "ds_read_b32 v150, %[yf] offset:448\n\tds_read_u16_d16_hi v151, %[ya]\n\ts_branch 3b\n"
                   "4:\n\tv_fmac_f32 v153, v152, %[yc]\n\tv_cvt_pk_bf16_f32 v153, v153, v153\n\tbuffer_store_short v153, %[oo], %[ro], 0 offen\n\t" DONE
                   "9:\n\t" PCP(112, 113, 132, 133) PCP(114, 115, 134, 135) PCP(116, 117, 136, 137) PCP(118, 119, 138, 139) PCP(120, 121, 140, 141) PCP(122, 123, 142, 143)
                   PCP(124, 125, 144, 145) PCP(126, 127, 146, 147) PLD(PL1) "s_sleep " STR(PNAP) "\n1:\n\t" PLE(PL1) PCK(4, "13", "14") "s_cbranch_vccz 2b\n\t" PLD(PL1) PCK(4, "11", "12")
                   "s_cbranch_vccz 2b\n\ts_cbranch_scc0 1b\n\ts_branch 2b\n0:"
                   : [n] "+s"(n_), "+{v[132:139]}"(pa), "+{v[140:147]}"(pb)
                   : [po] "v"(po), [p3] "v"(p3), [rs] "s"(rs), [k1] "s"(131072), [k2] "s"(262144), [pk] "v"(pick), [yf] "v"(yf), [ya] "v"(ya), "{v[160:163]}"(junk), [rl] "v"(rsel), [yc] "v"(yc),
                     [oo] "v"(oo), [ro] "s"(ro), [spin] "n"(SPIN)
                   : "vcc", "scc", "memory", "v112", "v113", "v114", "v115", "v116", "v117", "v118", "v119", "v120", "v121", "v122", "v123", "v124", "v125", "v126", "v127", "v150", "v151", "v152", "v153", "v154", "v155", "v156", "v157", "v158", "v159" TSD);
      TSGET(14, 2, 2, 2);
      STAMP(15);
    }
    FLUSH();
    __builtin_amdgcn_endpgm();
  }

  u32 pl, pa, pr, pi, ldtv;
  u32x4 tv, fx, junk;
  ROLE(c, "", , "={v[16:19]}"(tv), "={v[20:23]}"(fx), "={v24}"(ldtv), "={v96}"(pl), "={v97}"(pa), "={v68}"(pr), "={v69}"(pi), "={v[160:163]}"(junk));
  char* W = smem + w * CHR;
  __builtin_amdgcn_s_setprio(2);
  STAMP(3);
  const u32x4 zero4 = {0u, 0u, 0u, 0u};
#pragma unroll
  for (int i = 0; i < 4; i++) *(u32x4*)(W + (i < 3 ? TB + 1024 * i : CHR - 1024) + 16 * l) = zero4;
  SB();
  const u32 wb0 = lds0 + w * CHR, xb0 = lds0 + OX + w * XREG;
  asm volatile("s_mov_b32 m0, %0\n\ts_nop 0\n\tds_write_addtid_b32 %1\n\tds_write_addtid_b32 %1 offset:%2" ::"s"(wb0), "v"(0x3c00u), "n"(PB) : "memory");
  u32 ape = wb0 + (n16 == 12 ? 12 : (n16 ^ 3) - 7 * (n16 >> 3)) * RS + 16 * kg, apo = wb0 + (0x765412b0fdec2301ull >> 4 * n16 & 15) * RS + 16 * kg;
  u32x2 kb = {wb0 + ((15 - n16) & 3) * CPE + 2 * ((15 - n16) & 12) + 16 * kg, wb0 + (((n16 >> 2) & 1) + (n16 & 8) + 2 * (3 - (n16 & 3))) * RS + 16 * kg};
  u32x8 ka = {xb0 + 32 * n16 + 16 * kg, wb0 + OY + 32 * n16 + 8 * (kg ^ ((l >> 2) & 3)),
              lds0 + OXF + 4 * (l & 3), wb0 + TB + ((15 - n16) & 3) * CPO + 2 * ((15 - n16) & 12) + 8 * kg, lds0 + OB + 16 * l,
              l < 32 ? 0x01000100u : 0x03020100u, l < 32 ? 0x03020100u : 0x03020302u, (u32)(l >> 2) & 8u};
  u32 fad = lds0 + OYP + 4 * w, za = wb0 + 16 * kg;
  asm volatile("" : "+{v70}"(ape), "+{v71}"(apo), "+{v[28:35]}"(ka), "+{v[26:27]}"(kb), "+{v25}"(fad), "+{v12}"(za));
  STAMP(4);
  asm volatile("s_waitcnt vmcnt(4)" : "+{v24}"(ldtv), "+{v[160:163]}"(junk));
  float dt = rbt(fexp(hi2f(ldtv)));
  asm volatile("" : "+v"(dt));
  const f32x2 cdt = {rdl(dt * -1.4426950408889634f, 0), rdl(dt * 0.15915494309189535f, 0)};
  const u32 dk = (u32)__builtin_amdgcn_readfirstlane((int)junk.x) << 16, db = max(__float_as_uint(rdl(dt, 0)), 27u << 23);
  u32 em = max(dk >> 23 & 255u, 127u) + 119 - (db >> 23);
  asm("s_max_i32 %0, %0, 8\n\ts_min_u32 %0, %0, 0xfe" : "+s"(em) :: "scc");
  SB();
  STAMP(10);
  STAMPC(11);
  {
    u32 e, o0, o1, of, os;
    asm volatile(GEN_ASM
                 : [e] "=&s"(e), "=&{v93}"(o0), "=&{v46}"(o1), "=&{v9}"(of), "=&{v8}"(os), "+{v25}"(fad), "+{v12}"(za), "+{v68}"(pr), "+{v69}"(pi), "+{v70}"(ape),
                   "+{v71}"(apo), "+{v96}"(pl), "+{v97}"(pa), "+{v[28:35]}"(ka), "+{v[26:27]}"(kb), "+{v[16:19]}"(tv), "+{v[20:23]}"(fx)
                 : [w] "s"(wb0), [c] "s"(cdt), [k] "s"(f32x2{1.4426950408889634f, 128.f}), [em] "s"(em)
                 : "memory", "scc", "s6", "s7", GEN_CLOBBER);
    STAMP(1);
    STAMPC(12);
    const float ys = __uint_as_float((e - 5) << 23) * __uint_as_float(db);
    asm volatile("v_rcp_f32 %1, %2\n\ts_nop 0\n\tv_fma_f32 %1, %3, %1, %1\n\tv_fma_mixhi_f16 %0, %1, 1.0, %0 op_sel:[0,0,1] op_sel_hi:[0,0,1]\n\ts_nop 0"
                 : "+v"(o0), "=&v"(dt) : "v"(ys), "s"(dk));
    const u32 k0 = (u32)__builtin_amdgcn_readlane((int)o0, 3), k256 = (u32)__builtin_amdgcn_readlane((int)o1, 60);
    const u32 k512 = (u32)__builtin_amdgcn_readlane((int)os, 28), k1024 = (u32)__builtin_amdgcn_readlane((int)of, 28) >> 16;
    *(LDS u16*)(size_t)(wb0 + tv.z) = (u16)__builtin_amdgcn_perm((k512 & 0xffffu) | (k1024 << 16), (k0 >> 16) | (k256 << 16), tv.w);
    *(float*)(smem + OSC + 4 * w) = ys;
    fx &= (k1024 | (k1024 << 16)) ^ 0x80008000u;
  }
  asm volatile(CONV_ASM : "+{v[28:35]}"(ka) : "{v[26:27]}"(kb), "{v25}"(fad), "{v[48:51]}"(fx), [w] "s"(w) : "vcc", "scc", "s6", "memory", CONV_CLOBBER TSC);
  TSGET(2, 5, 6, 7);
  STAMP(13);
  STAMPC(15);
  FLUSH();
  __builtin_amdgcn_endpgm();
}

extern "C" void kernel_launch(void* const* d_in, const int* in_sizes, int n_in, void* d_out, int out_size,
                              void* d_ws, size_t ws_size, hipStream_t stream) {
  const u16* const* in = (const u16* const*)d_in;
  fused<<<256, 768, 0, stream>>>(in[0], in[1], in[2], in[3], in[4], in[5], in[6], in[8], in[9], in[10], in[11],
                                 (u16*)d_out, (char*)d_ws);
}
```

```cpp
#include <hip/hip_runtime.h>
#include <stdint.h>

#define STR2(x) #x
#define STR(x) STR2(x)
#define PH 1246
#define PPRIO 0
#define GDELAY 0
#define PRE 2
#define OAUX 0
#define PNT ""
#define LNNT ""
#define PRENT ""
#define PFN 15
#define PFO 512
#define QD 11
#define HNAP 4
#define QE 11
#define RSL 5
#define DPF 1
#define YN0 2
#define YN3 0
#define QS 7
#define QAS 6
#define RAMP 4
#define GNAP "s_sleep 1"
#define RNAP "s_sleep 1"
#define CLP 0
#define CSL 8
#define XSP 0
#define SPEC 0
#define HOLD 8
#define DPRIO 3
#define DPO 128
#define TH 590
#define CLD 245
#define IXI 1
#define LPRIO 0
#define DPI 2
#define PDROP 2
#define TE 0
#define XI 1
#define SCT " sc1"
#define SCU " sc1"
#define SCH " sc1"
#define TSG 280
#define SSL 5
#define PNAP 4
#define TW 310
#define TL 4352
typedef unsigned short u16;
typedef unsigned int u32;
typedef __attribute__((ext_vector_type(8))) __bf16 bf16x8;
typedef __attribute__((ext_vector_type(2))) __bf16 bf16x2;
typedef __attribute__((ext_vector_type(8))) _Float16 f16x8;
typedef __attribute__((ext_vector_type(2))) _Float16 f16x2;
typedef __attribute__((ext_vector_type(4))) short v4i16;
typedef __attribute__((ext_vector_type(4))) float f32x4;
typedef __attribute__((ext_vector_type(2))) float f32x2;
typedef __attribute__((ext_vector_type(4))) u32 u32x4;
typedef __attribute__((ext_vector_type(2))) u32 u32x2;
typedef __attribute__((ext_vector_type(8))) u32 u32x8;
typedef __attribute__((ext_vector_type(2), aligned(4))) u32 u32x2_a4;
typedef __amdgpu_buffer_rsrc_t rsrc_t;
#define LDS __attribute__((address_space(3)))

#define TLARG
#define TLVAL
#define STAMP(I)
#define STAMPC(I)
#define FLUSH()

constexpr int L = 2048, D = 1024, NM = 128, F = 3584;
constexpr u32 O_ARR = 0x1000, O_CLR = 0x2000, O_RG = 0x4000, O_SG = 0x8000, O_IDX = 0x10000, O_HG = 0x20000,
              O_PG = 0xd0000, O_TOP = 0x140000, TOPS = 8 * TL, WS_END = O_TOP + 2 * TOPS;
constexpr int SPIN = 1 << 16;

__device__ __forceinline__ float lo2f(u32 w) { return __uint_as_float(w << 16); }
__device__ __forceinline__ float hi2f(u32 w) { return __uint_as_float(w & 0xffff0000u); }
__device__ __forceinline__ u32 pack2(float a, float b) {
  bf16x2 r = {(__bf16)a, (__bf16)b};
  return __builtin_bit_cast(u32, r);
}
__device__ __forceinline__ u32 packh(float a, float b) {
  f16x2 r = {(_Float16)a, (_Float16)b};
  return __builtin_bit_cast(u32, r);
}
__device__ __forceinline__ float rbt(float f) { return __uint_as_float(pack2(0.f, f)); }
__device__ __forceinline__ float rb(float f) {
  float r;
  asm("v_cvt_pk_bf16_f32 %0, 0, %1" : "=v"(r) : "v"(f));
  return r;
}

template <int CTRL, int RM>
__device__ __forceinline__ float dppf(float v) {
  return __builtin_bit_cast(float, __builtin_amdgcn_update_dpp(0, __builtin_bit_cast(int, v), CTRL, RM, 0xf, true));
}
__device__ __forceinline__ float rdl(float v, int i) {
  return __builtin_bit_cast(float, __builtin_amdgcn_readlane(__builtin_bit_cast(int, v), i));
}
__device__ __forceinline__ void swap32(float& a, float& b) {
  auto r = __builtin_amdgcn_permlane32_swap(__float_as_uint(a), __float_as_uint(b), false, false);
  a = __uint_as_float(r[0]); b = __uint_as_float(r[1]);
}

__device__ __forceinline__ u32x4 ld16(rsrc_t r, u32 off) { return __builtin_amdgcn_raw_buffer_load_b128(r, off, 0, 16); }
__device__ __forceinline__ void st8(rsrc_t r, u32 off, u32x2 v) { __builtin_amdgcn_raw_buffer_store_b64(v, r, off, 0, 16); }
#define SB() __builtin_amdgcn_sched_barrier(0)

__device__ __forceinline__ float fexp(float v) { return __builtin_amdgcn_exp2f(v * 1.4426950408889634f); }
__device__ __forceinline__ float dot2b(u32 a, u32 b, float c) {
  return __builtin_amdgcn_fdot2_f32_bf16(__builtin_bit_cast(bf16x2, a), __builtin_bit_cast(bf16x2, b), c, false);
}
__device__ __forceinline__ float sigm(float v) { return __builtin_amdgcn_rcpf(1.f + __builtin_amdgcn_exp2f(-1.4426950408889634f * v)); }

constexpr int RS = 288, PB = 16 * RS;
constexpr int CPE = 1216, TB = 2 * PB, CPO = 832;
constexpr int T2B = 4, T3B = 224, C2 = 52, C3 = 84;
constexpr int OY = 0, CHR = TB + 4 * CPO;
constexpr int OX = 8 * CHR, XREG = 4640, XO = 266;
constexpr int OMISC = OX + 8 * XREG;
constexpr int OXF = OMISC + 320, OSC = OMISC + 352, OYP = OMISC + 448;
constexpr int OB = OMISC + 1024, SMEM = 148480;
constexpr int FA = 131072, FH = FA + 8192, HP = 576, FC = FH + 8 * HP;
static_assert(FC + 1024 <= SMEM && OB + 4096 <= SMEM, "lds map");

struct Tab { u32 v[6][64][4]; };
constexpr Tab mktab() {
  Tab T = {};
  for (int l = 0; l < 64; l++) {
    const int n16 = l & 15, kg = l >> 4, pc = ((n16 >> 2) & 1) + (n16 & 8) + 2 * (3 - (n16 & 3)), odd = kg >> 1, s = l & 3;
    const int e0 = 534 - 258 - 128 - 8 * pc - 4 * (kg & 1) - odd;
    const int ez = ((l >> 3) & 3) * CPE + ((l & 4) ? 1056 - 64 : 0) + 16 * (l & 7);
    const int b2 = T2B + 44 + C2 * ((kg & 1) + 2 * (pc & 1)), b3 = T3B + 60 + C3 * (pc & 1) - 32 * (kg & 1);
    const int to = TB + 2 * ((odd ? b3 : b2) + 3 - (pc >> 1) - 8 - 16);
    const int q = (l >> 2) & 7, tap = q == 0 ? ((s & 1) ? 0 : 256) : q == 1 ? 257 : q == 7 ? 0 : 509 + q;
    const int sp = q != 6 ? s * (CPE - 2) + 2 * (537 - tap) : TB + s * (CPO - 2) + 2 * (T2B + 15);
    T.v[0][l][0] = (2 * e0 + (2 - odd) * (CPE - 2)) | (2 * e0 + 3 * odd * (CPE - 2)) << 16;
    T.v[0][l][1] = ez | to << 16;
    T.v[0][l][2] = sp;
    T.v[0][l][3] = !tap ? 0x0100u : q == 6 ? 0x0706u : q >= 2 ? 0x0504u : 0x0302u;
    for (int k = 0; k < 8; k++) {
      const int d = 8 * kg + k - n16;
      if (d == 13) T.v[1][l][k >> 1] |= 0xffffu << 16 * (k & 1);
      for (int h = 1; h <= 4; h++)
        if (d >= (h == 1 ? 14 : 10) - h && d <= (h == 1 ? 14 : 10) + h) T.v[1 + h][l][k >> 1] |= 0x3c00u << 16 * (k & 1);
    }
  }
  return T;
}
extern "C" __device__ const __attribute__((aligned(128), used, visibility("hidden"))) Tab lane_tab = mktab();

#define GEN_ASM \
  "s_waitcnt vmcnt(2)\n\tv_lshlrev_b32 v56, 16, v96\n\t" \
  "v_and_b32 v57, 0xffff0000, v96\n\tv_pk_mul_f32 v[56:57], %[k], v[56:57] op_sel_hi:[0,1]\n\t" \
  "v_lshlrev_b32 v49, 16, v97\n\tv_exp_f32 v56, v56\n\t" \
  "v_exp_f32 v57, v57\n\tv_and_b32 v51, 0xffff0000, v97\n\t" \
  "v_cvt_pk_bf16_f32 v56, v56, v57\n\tv_lshlrev_b32 v48, 16, v56\n\t" \
  "v_and_b32 v50, 0xffff0000, v56\n\tv_pk_mul_f32 v[48:49], %[c], v[48:49]\n\t" \
  "v_pk_mul_f32 v[50:51], %[c], v[50:51]\n\tv_pk_mul_f32 v[98:99], %[k], v[48:49] op_sel:[1,0]\n\t" \
  "v_pk_mul_f32 v[100:101], %[k], v[50:51] op_sel:[1,0]\n\tv_fract_f32 v49, v49\n\t" \
  "v_fract_f32 v51, v51\n\tv_exp_f32 v48, v48\n\t" \
  "v_exp_f32 v50, v50\n\tv_cos_f32 v40, v49\n\t" \
  "v_sin_f32 v41, v49\n\tv_cos_f32 v42, v51\n\t" \
  "v_sin_f32 v43, v51\n\tv_pk_mul_f32 v[40:41], v[40:41], v[48:49] op_sel_hi:[1,0]\n\t" \
  "v_pk_mul_f32 v[42:43], v[42:43], v[50:51] op_sel_hi:[1,0]\n\tv_pk_mul_f32 v[48:49], v[40:41], v[40:41] op_sel:[1,1] op_sel_hi:[1,0] neg_lo:[1,0]\n\t" \
  "v_pk_mul_f32 v[50:51], v[42:43], v[42:43] op_sel:[1,1] op_sel_hi:[1,0] neg_lo:[1,0]\n\tv_pk_fma_f32 v[48:49], v[40:41], v[40:41], v[48:49] op_sel:[0,0,0] op_sel_hi:[0,1,1]\n\t" \
  "v_pk_fma_f32 v[50:51], v[42:43], v[42:43], v[50:51] op_sel:[0,0,0] op_sel_hi:[0,1,1]\n\tv_cvt_pk_f16_f32 v72, v40, -v41\n\t" \
  "v_cvt_pk_f16_f32 v73, v42, -v43\n\tv_pk_mul_f32 v[52:53], v[48:49], v[40:41] op_sel:[1,1] op_sel_hi:[1,0] neg_lo:[1,0]\n\t" \
  "v_pk_mul_f32 v[54:55], v[50:51], v[42:43] op_sel:[1,1] op_sel_hi:[1,0] neg_lo:[1,0]\n\tv_pk_fma_f32 v[52:53], v[48:49], v[40:41], v[52:53] op_sel:[0,0,0] op_sel_hi:[0,1,1]\n\t" \
  "v_pk_fma_f32 v[54:55], v[50:51], v[42:43], v[54:55] op_sel:[0,0,0] op_sel_hi:[0,1,1]\n\tv_pk_mul_f32 v[56:57], v[48:49], v[48:49] op_sel:[1,1] op_sel_hi:[1,0] neg_lo:[1,0]\n\t" \
  "v_pk_mul_f32 v[58:59], v[50:51], v[50:51] op_sel:[1,1] op_sel_hi:[1,0] neg_lo:[1,0]\n\tv_pk_fma_f32 v[56:57], v[48:49], v[48:49], v[56:57] op_sel:[0,0,0] op_sel_hi:[0,1,1]\n\t" \
  "v_pk_fma_f32 v[58:59], v[50:51], v[50:51], v[58:59] op_sel:[0,0,0] op_sel_hi:[0,1,1]\n\tv_cvt_pk_f16_f32 v74, v48, -v49\n\t" \
  "v_cvt_pk_f16_f32 v75, v50, -v51\n\tv_cvt_pk_f16_f32 v76, v52, -v53\n\t" \
  "v_cvt_pk_f16_f32 v77, v54, -v55\n\tv_pk_mul_f32 v[52:53], v[56:57], v[48:49] op_sel:[1,1] op_sel_hi:[1,0] neg_lo:[1,0]\n\t" \
  "v_pk_mul_f32 v[54:55], v[58:59], v[50:51] op_sel:[1,1] op_sel_hi:[1,0] neg_lo:[1,0]\n\tv_pk_fma_f32 v[52:53], v[56:57], v[48:49], v[52:53] op_sel:[0,0,0] op_sel_hi:[0,1,1]\n\t" \
  "v_pk_fma_f32 v[54:55], v[58:59], v[50:51], v[54:55] op_sel:[0,0,0] op_sel_hi:[0,1,1]\n\tv_pk_mul_f32 v[60:61], v[56:57], v[56:57] op_sel:[1,1] op_sel_hi:[1,0] neg_lo:[1,0]\n\t" \
  "v_pk_mul_f32 v[62:63], v[58:59], v[58:59] op_sel:[1,1] op_sel_hi:[1,0] neg_lo:[1,0]\n\tv_pk_fma_f32 v[60:61], v[56:57], v[56:57], v[60:61] op_sel:[0,0,0] op_sel_hi:[0,1,1]\n\t" \
  "v_pk_fma_f32 v[62:63], v[58:59], v[58:59], v[62:63] op_sel:[0,0,0] op_sel_hi:[0,1,1]\n\tv_cvt_pk_f16_f32 v78, v56, -v57\n\t" \
  "v_cvt_pk_f16_f32 v79, v58, -v59\n\tv_cvt_pk_f16_f32 v82, v52, -v53\n\t" \
  "v_cvt_pk_f16_f32 v83, v54, -v55\n\tv_pk_mul_f16 v80, v78, v72 op_sel:[1,1] op_sel_hi:[1,0] neg_lo:[1,0]\n\t" \
  "v_pk_mul_f16 v81, v79, v73 op_sel:[1,1] op_sel_hi:[1,0] neg_lo:[1,0]\n\tv_pk_fma_f16 v80, v78, v72, v80 op_sel:[0,0,0] op_sel_hi:[0,1,1]\n\t" \
  "v_pk_fma_f16 v81, v79, v73, v81 op_sel:[0,0,0] op_sel_hi:[0,1,1]\n\tv_pk_mul_f16 v84, v78, v76 op_sel:[1,1] op_sel_hi:[1,0] neg_lo:[1,0]\n\t" \
  "v_pk_mul_f16 v85, v79, v77 op_sel:[1,1] op_sel_hi:[1,0] neg_lo:[1,0]\n\tv_pk_fma_f16 v84, v78, v76, v84 op_sel:[0,0,0] op_sel_hi:[0,1,1]\n\t" \
  "v_pk_fma_f16 v85, v79, v77, v85 op_sel:[0,0,0] op_sel_hi:[0,1,1]\n\tv_cvt_pk_f16_f32 v86, v60, -v61\n\t" \
  "v_cvt_pk_f16_f32 v87, v62, -v63\n\tds_write_addtid_b32 v72 offset:288\n\t" \
  "ds_write_addtid_b32 v73 offset:4896\n\tds_write_addtid_b32 v74 offset:576\n\t" \
  "ds_write_addtid_b32 v75 offset:5184\n\tds_write_addtid_b32 v76 offset:864\n\t" \
  "ds_write_addtid_b32 v77 offset:5472\n\tds_write_addtid_b32 v78 offset:1152\n\t" \
  "ds_write_addtid_b32 v79 offset:5760\n\tds_write_addtid_b32 v80 offset:1440\n\t" \
  "ds_write_addtid_b32 v81 offset:6048\n\tds_write_addtid_b32 v82 offset:1728\n\t" \
  "ds_write_addtid_b32 v83 offset:6336\n\tds_write_addtid_b32 v84 offset:2016\n\t" \
  "ds_write_addtid_b32 v85 offset:6624\n\tds_write_addtid_b32 v86 offset:3456\n\t" \
  "ds_write_addtid_b32 v87 offset:8064\n\tds_read_b128 v[104:107], v70 offset:0\n\t" \
  "ds_read_b128 v[108:111], v70 offset:64\n\tds_read_b128 v[112:115], v70 offset:128\n\t" \
  "ds_read_b128 v[116:119], v70 offset:192\n\tds_read_b128 v[120:123], v70 offset:4608\n\t" \
  "ds_read_b128 v[124:127], v70 offset:4672\n\tds_read_b128 v[128:131], v70 offset:4736\n\t" \
  "ds_read_b128 v[132:135], v70 offset:4800\n\tv_fract_f32 v99, v99\n\t" \
  "v_fract_f32 v101, v101\n\tv_exp_f32 v98, v98\n\t" \
  "v_exp_f32 v100, v100\n\tv_cos_f32 v40, v99\n\t" \
  "v_sin_f32 v41, v99\n\tv_cos_f32 v42, v101\n\t" \
  "v_sin_f32 v43, v101\n\tv_pk_mul_f32 v[40:41], v[40:41], v[98:99] op_sel_hi:[1,0]\n\t" \
  "v_pk_mul_f32 v[42:43], v[42:43], v[100:101] op_sel_hi:[1,0]\n\tv_pk_mul_f32 v[44:45], v[40:41], v[40:41] op_sel:[1,1] op_sel_hi:[1,0] neg_lo:[1,0]\n\t" \
  "v_pk_mul_f32 v[46:47], v[42:43], v[42:43] op_sel:[1,1] op_sel_hi:[1,0] neg_lo:[1,0]\n\tv_pk_fma_f32 v[44:45], v[40:41], v[40:41], v[44:45] op_sel:[0,0,0] op_sel_hi:[0,1,1]\n\t" \
  "v_pk_fma_f32 v[46:47], v[42:43], v[42:43], v[46:47] op_sel:[0,0,0] op_sel_hi:[0,1,1]\n\tv_cvt_pk_f16_f32 v36, v40, v41\n\t" \
  "v_cvt_pk_f16_f32 v37, v42, v43\n\ts_waitcnt vmcnt(0)\n\t" \
  "v_pk_mul_f32 v[48:49], v[44:45], v[44:45] op_sel:[1,1] op_sel_hi:[1,0] neg_lo:[1,0]\n\tv_pk_mul_f32 v[50:51], v[46:47], v[46:47] op_sel:[1,1] op_sel_hi:[1,0] neg_lo:[1,0]\n\t" \
  "v_pk_fma_f32 v[48:49], v[44:45], v[44:45], v[48:49] op_sel:[0,0,0] op_sel_hi:[0,1,1]\n\tv_pk_fma_f32 v[50:51], v[46:47], v[46:47], v[50:51] op_sel:[0,0,0] op_sel_hi:[0,1,1]\n\t" \
  "v_lshlrev_b32 v4, 16, v68\n\tv_cvt_pk_f16_f32 v88, v44, -v45\n\t" \
  "v_cvt_pk_f16_f32 v89, v46, -v47\n\tv_pk_mul_f32 v[52:53], v[48:49], v[44:45] op_sel:[1,1] op_sel_hi:[1,0] neg_lo:[1,0]\n\t" \
  "v_pk_mul_f32 v[54:55], v[50:51], v[46:47] op_sel:[1,1] op_sel_hi:[1,0] neg_lo:[1,0]\n\tv_lshlrev_b32 v5, 16, v69\n\t" \
  "v_pk_fma_f32 v[52:53], v[48:49], v[44:45], v[52:53] op_sel:[0,0,0] op_sel_hi:[0,1,1]\n\tv_pk_fma_f32 v[54:55], v[50:51], v[46:47], v[54:55] op_sel:[0,0,0] op_sel_hi:[0,1,1]\n\t" \
  "v_pk_mul_f32 v[56:57], v[48:49], v[48:49] op_sel:[1,1] op_sel_hi:[1,0] neg_lo:[1,0]\n\tv_pk_mul_f32 v[58:59], v[50:51], v[50:51] op_sel:[1,1] op_sel_hi:[1,0] neg_lo:[1,0]\n\t" \
  "v_and_b32 v6, 0xffff0000, v68\n\tv_pk_fma_f32 v[56:57], v[48:49], v[48:49], v[56:57] op_sel:[0,0,0] op_sel_hi:[0,1,1]\n\t" \
  "v_pk_fma_f32 v[58:59], v[50:51], v[50:51], v[58:59] op_sel:[0,0,0] op_sel_hi:[0,1,1]\n\tv_cvt_pk_f16_f32 v90, v48, -v49\n\t" \
  "v_cvt_pk_f16_f32 v91, v50, -v51\n\tv_and_b32 v7, 0xffff0000, v69\n\t" \
  "v_pk_mul_f32 v[64:65], v[56:57], v[60:61] op_sel:[1,1] op_sel_hi:[1,0] neg_lo:[1,0]\n\tv_pk_mul_f32 v[66:67], v[58:59], v[62:63] op_sel:[1,1] op_sel_hi:[1,0] neg_lo:[1,0]\n\t" \
  "v_pk_fma_f32 v[64:65], v[56:57], v[60:61], v[64:65] op_sel:[0,0,0] op_sel_hi:[0,1,1]\n\tv_pk_fma_f32 v[66:67], v[58:59], v[62:63], v[66:67] op_sel:[0,0,0] op_sel_hi:[0,1,1]\n\t" \
  "v_max3_f32 v102, |v4|, |v5|, |v6|\n\tv_cvt_pk_f16_f32 v92, v52, -v53\n\t" \
  "v_cvt_pk_f16_f32 v93, v54, -v55\n\tv_pk_mul_f16 v136, v88, v74 op_sel:[1,1] op_sel_hi:[1,0] neg_lo:[1,0]\n\t" \
  "v_pk_mul_f16 v137, v89, v75 op_sel:[1,1] op_sel_hi:[1,0] neg_lo:[1,0]\n\tv_max_f32 v102, v102, |v7|\n\t" \
  "v_pk_fma_f16 v136, v88, v74, v136 op_sel:[0,0,0] op_sel_hi:[0,1,1]\n\tv_pk_fma_f16 v137, v89, v75, v137 op_sel:[0,0,0] op_sel_hi:[0,1,1]\n\t" \
  "v_pk_mul_f16 v138, v88, v78 op_sel:[1,1] op_sel_hi:[1,0] neg_lo:[1,0]\n\tv_pk_mul_f16 v139, v89, v79 op_sel:[1,1] op_sel_hi:[1,0] neg_lo:[1,0]\n\t" \
  "v_max_u32_dpp v102, v102, v102 row_shr:1 row_mask:0xf bank_mask:0xf\n\tv_pk_fma_f16 v138, v88, v78, v138 op_sel:[0,0,0] op_sel_hi:[0,1,1]\n\t" \
  "v_pk_fma_f16 v139, v89, v79, v139 op_sel:[0,0,0] op_sel_hi:[0,1,1]\n\tv_cvt_pk_f16_f32 v94, v64, -v65\n\t" \
  "v_cvt_pk_f16_f32 v95, v66, -v67\n\tv_max_u32_dpp v102, v102, v102 row_shr:2 row_mask:0xf bank_mask:0xf\n\t" \
  "v_pk_mul_f16 v140, v88, v82 op_sel:[1,1] op_sel_hi:[1,0] neg_lo:[1,0]\n\tv_pk_mul_f16 v141, v89, v83 op_sel:[1,1] op_sel_hi:[1,0] neg_lo:[1,0]\n\t" \
  "v_pk_fma_f16 v140, v88, v82, v140 op_sel:[0,0,0] op_sel_hi:[0,1,1]\n\tv_pk_fma_f16 v141, v89, v83, v141 op_sel:[0,0,0] op_sel_hi:[0,1,1]\n\t" \
  "ds_write_addtid_b32 v88 offset:3168\n\tds_write_addtid_b32 v89 offset:7776\n\t" \
  "ds_write_addtid_b32 v136 offset:0\n\tds_write_addtid_b32 v137 offset:4608\n\t" \
  "ds_write_addtid_b32 v138 offset:288\n\tds_write_addtid_b32 v139 offset:4896\n\t" \
  "ds_write_addtid_b32 v94 offset:1152\n\tds_write_addtid_b32 v95 offset:5760\n\t" \
  "ds_write_addtid_b32 v140 offset:576\n\tds_write_addtid_b32 v141 offset:5184\n\t" \
  "v_max_u32_dpp v102, v102, v102 row_shr:4 row_mask:0xf bank_mask:0xf\n\tv_pk_mul_f16 v142, v88, v86 op_sel:[1,1] op_sel_hi:[1,0] neg_lo:[1,0]\n\t" \
  "v_pk_mul_f16 v143, v89, v87 op_sel:[1,1] op_sel_hi:[1,0] neg_lo:[1,0]\n\tv_pk_fma_f16 v142, v88, v86, v142 op_sel:[0,0,0] op_sel_hi:[0,1,1]\n\t" \
  "v_pk_fma_f16 v143, v89, v87, v143 op_sel:[0,0,0] op_sel_hi:[0,1,1]\n\tv_max_u32_dpp v102, v102, v102 row_shr:8 row_mask:0xf bank_mask:0xf\n\t" \
  "v_pk_mul_f16 v144, v90, v78 op_sel:[1,1] op_sel_hi:[1,0] neg_lo:[1,0]\n\tv_pk_mul_f16 v145, v91, v79 op_sel:[1,1] op_sel_hi:[1,0] neg_lo:[1,0]\n\t" \
  "v_pk_fma_f16 v144, v90, v78, v144 op_sel:[0,0,0] op_sel_hi:[0,1,1]\n\tv_pk_fma_f16 v145, v91, v79, v145 op_sel:[0,0,0] op_sel_hi:[0,1,1]\n\t" \
  "v_max_u32_dpp v102, v102, v102 row_bcast:15 row_mask:0xa bank_mask:0xf\n\tv_pk_mul_f16 v146, v90, v86 op_sel:[1,1] op_sel_hi:[1,0] neg_lo:[1,0]\n\t" \
  "v_pk_mul_f16 v147, v91, v87 op_sel:[1,1] op_sel_hi:[1,0] neg_lo:[1,0]\n\tv_pk_fma_f16 v146, v90, v86, v146 op_sel:[0,0,0] op_sel_hi:[0,1,1]\n\t" \
  "v_pk_fma_f16 v147, v91, v87, v147 op_sel:[0,0,0] op_sel_hi:[0,1,1]\n\tv_max_u32_dpp v102, v102, v102 row_bcast:31 row_mask:0xc bank_mask:0xf\n\t" \
  "v_pk_mul_f16 v148, v92, v78 op_sel:[1,1] op_sel_hi:[1,0] neg_lo:[1,0]\n\tv_pk_mul_f16 v149, v93, v79 op_sel:[1,1] op_sel_hi:[1,0] neg_lo:[1,0]\n\t" \
  "v_pk_fma_f16 v148, v92, v78, v148 op_sel:[0,0,0] op_sel_hi:[0,1,1]\n\tv_pk_fma_f16 v149, v93, v79, v149 op_sel:[0,0,0] op_sel_hi:[0,1,1]\n\t" \
  "v_readlane_b32 %[e], v102, 63\n\tv_pk_mul_f16 v150, v92, v86 op_sel:[1,1] op_sel_hi:[1,0] neg_lo:[1,0]\n\t" \
  "v_pk_mul_f16 v151, v93, v87 op_sel:[1,1] op_sel_hi:[1,0] neg_lo:[1,0]\n\tv_pk_fma_f16 v150, v92, v86, v150 op_sel:[0,0,0] op_sel_hi:[0,1,1]\n\t" \
  "v_pk_fma_f16 v151, v93, v87, v151 op_sel:[0,0,0] op_sel_hi:[0,1,1]\n\ts_lshr_b32 %[e], %[e], 23\n\t" \
  "s_max_u32 %[e], %[e], %[em]\n\ts_sub_u32 s6, 260, %[e]\n\t" \
  "s_lshl_b32 s6, s6, 23\n\tds_write_addtid_b32 v142 offset:864\n\t" \
  "ds_write_addtid_b32 v143 offset:5472\n\tds_write_addtid_b32 v144 offset:3456\n\t" \
  "ds_write_addtid_b32 v145 offset:8064\n\tds_write_addtid_b32 v146 offset:3744\n\t" \
  "ds_write_addtid_b32 v147 offset:8352\n\tds_write_addtid_b32 v148 offset:4032\n\t" \
  "ds_write_addtid_b32 v149 offset:8640\n\tds_write_addtid_b32 v150 offset:4320\n\t" \
  "ds_write_addtid_b32 v151 offset:8928\n\tv_pk_mul_f16 v96, v94, v88 op_sel:[1,1] op_sel_hi:[1,0] neg_lo:[1,0]\n\t" \
  "v_pk_mul_f16 v97, v95, v89 op_sel:[1,1] op_sel_hi:[1,0] neg_lo:[1,0]\n\tv_pk_fma_f16 v96, v94, v88, v96 op_sel:[0,0,0] op_sel_hi:[0,1,1]\n\t" \
  "v_pk_fma_f16 v97, v95, v89, v97 op_sel:[0,0,0] op_sel_hi:[0,1,1]\n\tv_pk_mul_f16 v98, v94, v90 op_sel:[1,1] op_sel_hi:[1,0] neg_lo:[1,0]\n\t" \
  "v_pk_mul_f16 v99, v95, v91 op_sel:[1,1] op_sel_hi:[1,0] neg_lo:[1,0]\n\tv_pk_fma_f16 v98, v94, v90, v98 op_sel:[0,0,0] op_sel_hi:[0,1,1]\n\t" \
  "v_pk_fma_f16 v99, v95, v91, v99 op_sel:[0,0,0] op_sel_hi:[0,1,1]\n\tv_pk_mul_f16 v100, v94, v92 op_sel:[1,1] op_sel_hi:[1,0] neg_lo:[1,0]\n\t" \
  "v_pk_mul_f16 v101, v95, v93 op_sel:[1,1] op_sel_hi:[1,0] neg_lo:[1,0]\n\tv_pk_fma_f16 v100, v94, v92, v100 op_sel:[0,0,0] op_sel_hi:[0,1,1]\n\t" \
  "v_pk_fma_f16 v101, v95, v93, v101 op_sel:[0,0,0] op_sel_hi:[0,1,1]\n\tds_write_addtid_b32 v96 offset:1440\n\t" \
  "ds_write_addtid_b32 v97 offset:6048\n\tds_write_addtid_b32 v98 offset:1728\n\t" \
  "ds_write_addtid_b32 v99 offset:6336\n\tds_write_addtid_b32 v100 offset:2016\n\t" \
  "ds_write_addtid_b32 v101 offset:6624\n\tds_read_b128 v[136:139], v71 offset:0\n\t" \
  "ds_read_b128 v[140:143], v71 offset:64\n\tds_read_b128 v[144:147], v71 offset:128\n\t" \
  "s_cmp_lt_u32 %[w], 50176\n\ts_cbranch_scc0 1f\n\t" \
  "s_setprio 1\n\t1:\n\t" \
  "ds_read_b128 v[148:151], v71 offset:192\n\tds_read_b128 v[152:155], v71 offset:4608\n\t" \
  "ds_read_b128 v[156:159], v71 offset:4672\n\tds_read_b128 v[160:163], v71 offset:4736\n\t" \
  "ds_read_b128 v[164:167], v71 offset:4800\n\tv_pk_mul_f32 v[48:49], v[60:61], v[60:61] op_sel:[1,1] op_sel_hi:[1,0] neg_lo:[1,0]\n\t" \
  "v_pk_mul_f32 v[50:51], v[62:63], v[62:63] op_sel:[1,1] op_sel_hi:[1,0] neg_lo:[1,0]\n\tv_pk_fma_f32 v[48:49], v[60:61], v[60:61], v[48:49] op_sel:[0,0,0] op_sel_hi:[0,1,1]\n\t" \
  "v_pk_fma_f32 v[50:51], v[62:63], v[62:63], v[50:51] op_sel:[0,0,0] op_sel_hi:[0,1,1]\n\tv_pk_mul_f32 v[52:53], v[48:49], v[60:61] op_sel:[1,1] op_sel_hi:[1,0] neg_lo:[1,0]\n\t" \
  "v_pk_mul_f32 v[54:55], v[50:51], v[62:63] op_sel:[1,1] op_sel_hi:[1,0] neg_lo:[1,0]\n\tv_pk_fma_f32 v[52:53], v[48:49], v[60:61], v[52:53] op_sel:[0,0,0] op_sel_hi:[0,1,1]\n\t" \
  "v_pk_fma_f32 v[54:55], v[50:51], v[62:63], v[54:55] op_sel:[0,0,0] op_sel_hi:[0,1,1]\n\tv_pk_mul_f32 v[40:41], v[48:49], v[48:49] op_sel:[1,1] op_sel_hi:[1,0] neg_lo:[1,0]\n\t" \
  "v_pk_mul_f32 v[42:43], v[50:51], v[50:51] op_sel:[1,1] op_sel_hi:[1,0] neg_lo:[1,0]\n\tv_pk_fma_f32 v[40:41], v[48:49], v[48:49], v[40:41] op_sel:[0,0,0] op_sel_hi:[0,1,1]\n\t" \
  "v_pk_fma_f32 v[42:43], v[50:51], v[50:51], v[42:43] op_sel:[0,0,0] op_sel_hi:[0,1,1]\n\tv_cvt_pk_f16_f32 v46, v48, v49\n\t" \
  "v_cvt_pk_f16_f32 v47, v50, v51\n\tv_cvt_pk_f16_f32 v56, v52, v53\n\t" \
  "v_cvt_pk_f16_f32 v57, v54, v55\n\tv_pk_mul_f32 v[64:65], s[6:7], v[4:5] op_sel_hi:[0,1]\n\t" \
  "v_pk_mul_f32 v[66:67], s[6:7], v[6:7] op_sel_hi:[0,1]\n\tv_cvt_pk_f16_f32 v48, v64, v65\n\t" \
  "v_cvt_pk_f16_f32 v49, v66, v67\n\tv_pk_mul_f32 v[68:69], v[64:65], v[40:41] op_sel:[1,1] op_sel_hi:[1,0] neg_lo:[1,0]\n\t" \
  "v_pk_mul_f32 v[70:71], v[66:67], v[42:43] op_sel:[1,1] op_sel_hi:[1,0] neg_lo:[1,0]\n\tv_pk_fma_f32 v[68:69], v[64:65], v[40:41], v[68:69] op_sel:[0,0,0] op_sel_hi:[0,1,1]\n\t" \
  "v_pk_fma_f32 v[70:71], v[66:67], v[42:43], v[70:71] op_sel:[0,0,0] op_sel_hi:[0,1,1]\n\tv_pk_mul_f16 v50, v48, v86 op_sel:[1,1] op_sel_hi:[1,0]\n\t" \
  "v_pk_mul_f16 v51, v49, v87 op_sel:[1,1] op_sel_hi:[1,0]\n\tv_pk_fma_f16 v50, v48, v86, v50 op_sel:[0,0,0] op_sel_hi:[0,1,1] neg_hi:[0,1,0]\n\t" \
  "v_pk_fma_f16 v51, v49, v87, v51 op_sel:[0,0,0] op_sel_hi:[0,1,1] neg_hi:[0,1,0]\n\tv_pk_mul_f16 v52, v48, v46 op_sel:[1,1] op_sel_hi:[1,0] neg_lo:[1,0]\n\t" \
  "v_pk_mul_f16 v53, v49, v47 op_sel:[1,1] op_sel_hi:[1,0] neg_lo:[1,0]\n\tv_pk_fma_f16 v52, v48, v46, v52 op_sel:[0,0,0] op_sel_hi:[0,1,1]\n\t" \
  "v_pk_fma_f16 v53, v49, v47, v53 op_sel:[0,0,0] op_sel_hi:[0,1,1]\n\tv_pk_mul_f16 v54, v48, v56 op_sel:[1,1] op_sel_hi:[1,0] neg_lo:[1,0]\n\t" \
  "v_pk_mul_f16 v55, v49, v57 op_sel:[1,1] op_sel_hi:[1,0] neg_lo:[1,0]\n\tv_pk_fma_f16 v54, v48, v56, v54 op_sel:[0,0,0] op_sel_hi:[0,1,1]\n\t" \
  "v_pk_fma_f16 v55, v49, v57, v55 op_sel:[0,0,0] op_sel_hi:[0,1,1]\n\tds_write_addtid_b32 v48 offset:0\n\t" \
  "ds_write_addtid_b32 v49 offset:4608\n\tds_write_addtid_b32 v50 offset:288\n\t" \
  "ds_write_addtid_b32 v51 offset:4896\n\tds_write_addtid_b32 v52 offset:576\n\t" \
  "ds_write_addtid_b32 v53 offset:5184\n\tds_write_addtid_b32 v54 offset:864\n\t" \
  "ds_write_addtid_b32 v55 offset:5472\n\tv_cvt_pk_f16_f32 v48, v68, v69\n\t" \
  "v_cvt_pk_f16_f32 v49, v70, v71\n\tv_pk_mul_f32 v[64:65], v[68:69], v[40:41] op_sel:[1,1] op_sel_hi:[1,0] neg_lo:[1,0]\n\t" \
  "v_pk_mul_f32 v[66:67], v[70:71], v[42:43] op_sel:[1,1] op_sel_hi:[1,0] neg_lo:[1,0]\n\tv_pk_fma_f32 v[64:65], v[68:69], v[40:41], v[64:65] op_sel:[0,0,0] op_sel_hi:[0,1,1]\n\t" \
  "v_pk_fma_f32 v[66:67], v[70:71], v[42:43], v[66:67] op_sel:[0,0,0] op_sel_hi:[0,1,1]\n\tv_pk_mul_f16 v50, v48, v86 op_sel:[1,1] op_sel_hi:[1,0]\n\t" \
  "v_pk_mul_f16 v51, v49, v87 op_sel:[1,1] op_sel_hi:[1,0]\n\tv_pk_fma_f16 v50, v48, v86, v50 op_sel:[0,0,0] op_sel_hi:[0,1,1] neg_hi:[0,1,0]\n\t" \
  "v_pk_fma_f16 v51, v49, v87, v51 op_sel:[0,0,0] op_sel_hi:[0,1,1] neg_hi:[0,1,0]\n\tv_pk_mul_f16 v52, v48, v46 op_sel:[1,1] op_sel_hi:[1,0] neg_lo:[1,0]\n\t" \
  "v_pk_mul_f16 v53, v49, v47 op_sel:[1,1] op_sel_hi:[1,0] neg_lo:[1,0]\n\tv_pk_fma_f16 v52, v48, v46, v52 op_sel:[0,0,0] op_sel_hi:[0,1,1]\n\t" \
  "v_pk_fma_f16 v53, v49, v47, v53 op_sel:[0,0,0] op_sel_hi:[0,1,1]\n\tv_pk_mul_f16 v54, v48, v56 op_sel:[1,1] op_sel_hi:[1,0] neg_lo:[1,0]\n\t" \
  "v_pk_mul_f16 v55, v49, v57 op_sel:[1,1] op_sel_hi:[1,0] neg_lo:[1,0]\n\tv_pk_fma_f16 v54, v48, v56, v54 op_sel:[0,0,0] op_sel_hi:[0,1,1]\n\t" \
  "v_pk_fma_f16 v55, v49, v57, v55 op_sel:[0,0,0] op_sel_hi:[0,1,1]\n\tds_write_addtid_b32 v48 offset:1152\n\t" \
  "ds_write_addtid_b32 v49 offset:5760\n\tds_write_addtid_b32 v50 offset:1440\n\t" \
  "ds_write_addtid_b32 v51 offset:6048\n\tds_write_addtid_b32 v52 offset:1728\n\t" \
  "ds_write_addtid_b32 v53 offset:6336\n\tds_write_addtid_b32 v54 offset:2016\n\t" \
  "ds_write_addtid_b32 v55 offset:6624\n\tv_cvt_pk_f16_f32 v48, v64, v65\n\t" \
  "v_cvt_pk_f16_f32 v49, v66, v67\n\tv_pk_mul_f32 v[68:69], v[64:65], v[40:41] op_sel:[1,1] op_sel_hi:[1,0] neg_lo:[1,0]\n\t" \
  "v_pk_mul_f32 v[70:71], v[66:67], v[42:43] op_sel:[1,1] op_sel_hi:[1,0] neg_lo:[1,0]\n\tv_pk_fma_f32 v[68:69], v[64:65], v[40:41], v[68:69] op_sel:[0,0,0] op_sel_hi:[0,1,1]\n\t" \
  "v_pk_fma_f32 v[70:71], v[66:67], v[42:43], v[70:71] op_sel:[0,0,0] op_sel_hi:[0,1,1]\n\tv_pk_mul_f16 v50, v48, v86 op_sel:[1,1] op_sel_hi:[1,0]\n\t" \
  "v_pk_mul_f16 v51, v49, v87 op_sel:[1,1] op_sel_hi:[1,0]\n\tv_pk_fma_f16 v50, v48, v86, v50 op_sel:[0,0,0] op_sel_hi:[0,1,1] neg_hi:[0,1,0]\n\t" \
  "v_pk_fma_f16 v51, v49, v87, v51 op_sel:[0,0,0] op_sel_hi:[0,1,1] neg_hi:[0,1,0]\n\tv_pk_mul_f16 v52, v48, v46 op_sel:[1,1] op_sel_hi:[1,0] neg_lo:[1,0]\n\t" \
  "v_pk_mul_f16 v53, v49, v47 op_sel:[1,1] op_sel_hi:[1,0] neg_lo:[1,0]\n\tv_pk_fma_f16 v52, v48, v46, v52 op_sel:[0,0,0] op_sel_hi:[0,1,1]\n\t" \
  "v_pk_fma_f16 v53, v49, v47, v53 op_sel:[0,0,0] op_sel_hi:[0,1,1]\n\tv_pk_mul_f16 v54, v48, v56 op_sel:[1,1] op_sel_hi:[1,0] neg_lo:[1,0]\n\t" \
  "v_pk_mul_f16 v55, v49, v57 op_sel:[1,1] op_sel_hi:[1,0] neg_lo:[1,0]\n\tv_pk_fma_f16 v54, v48, v56, v54 op_sel:[0,0,0] op_sel_hi:[0,1,1]\n\t" \
  "v_pk_fma_f16 v55, v49, v57, v55 op_sel:[0,0,0] op_sel_hi:[0,1,1]\n\tds_write_addtid_b32 v48 offset:2304\n\t" \
  "ds_write_addtid_b32 v49 offset:6912\n\tds_write_addtid_b32 v50 offset:2592\n\t" \
  "ds_write_addtid_b32 v51 offset:7200\n\tds_write_addtid_b32 v52 offset:2880\n\t" \
  "ds_write_addtid_b32 v53 offset:7488\n\tds_write_addtid_b32 v54 offset:3168\n\t" \
  "ds_write_addtid_b32 v55 offset:7776\n\tv_cvt_pk_f16_f32 v48, v68, v69\n\t" \
  "v_cvt_pk_f16_f32 v49, v70, v71\n\tv_pk_mul_f16 v50, v48, v86 op_sel:[1,1] op_sel_hi:[1,0]\n\t" \
  "v_pk_mul_f16 v51, v49, v87 op_sel:[1,1] op_sel_hi:[1,0]\n\tv_pk_fma_f16 v50, v48, v86, v50 op_sel:[0,0,0] op_sel_hi:[0,1,1] neg_hi:[0,1,0]\n\t" \
  "v_pk_fma_f16 v51, v49, v87, v51 op_sel:[0,0,0] op_sel_hi:[0,1,1] neg_hi:[0,1,0]\n\tv_pk_mul_f16 v52, v48, v46 op_sel:[1,1] op_sel_hi:[1,0] neg_lo:[1,0]\n\t" \
  "v_pk_mul_f16 v53, v49, v47 op_sel:[1,1] op_sel_hi:[1,0] neg_lo:[1,0]\n\tv_pk_fma_f16 v52, v48, v46, v52 op_sel:[0,0,0] op_sel_hi:[0,1,1]\n\t" \
  "v_pk_fma_f16 v53, v49, v47, v53 op_sel:[0,0,0] op_sel_hi:[0,1,1]\n\tv_pk_mul_f16 v54, v48, v56 op_sel:[1,1] op_sel_hi:[1,0] neg_lo:[1,0]\n\t" \
  "v_pk_mul_f16 v55, v49, v57 op_sel:[1,1] op_sel_hi:[1,0] neg_lo:[1,0]\n\tv_pk_fma_f16 v54, v48, v56, v54 op_sel:[0,0,0] op_sel_hi:[0,1,1]\n\t" \
  "v_pk_fma_f16 v55, v49, v57, v55 op_sel:[0,0,0] op_sel_hi:[0,1,1]\n\tds_write_addtid_b32 v48 offset:3456\n\t" \
  "ds_write_addtid_b32 v49 offset:8064\n\tds_write_addtid_b32 v50 offset:3744\n\t" \
  "ds_write_addtid_b32 v51 offset:8352\n\tds_write_addtid_b32 v52 offset:4032\n\t" \
  "ds_write_addtid_b32 v53 offset:8640\n\tds_write_addtid_b32 v54 offset:4320\n\t" \
  "ds_write_addtid_b32 v55 offset:8928\n\tds_read_b128 v[72:75], v27 offset:0\n\t" \
  "ds_read_b128 v[76:79], v27 offset:64\n\tds_read_b128 v[80:83], v27 offset:128\n\t" \
  "ds_read_b128 v[84:87], v27 offset:192\n\tds_read_b128 v[88:91], v27 offset:4608\n\t" \
  "ds_read_b128 v[92:95], v27 offset:4672\n\tds_read_b128 v[96:99], v27 offset:4736\n\t" \
  "ds_read_b128 v[100:103], v27 offset:4800\n\tds_write_addtid_b32 v36 offset:0\n\t" \
  "ds_write_addtid_b32 v37 offset:4608\n\tds_read_b128 v[40:43], v12 offset:0\n\t" \
  "ds_read_b128 v[44:47], v12 offset:64\n\tds_read_b128 v[48:51], v12 offset:128\n\t" \
  "v_add_u32_sdwa v56, %[w], v16 dst_sel:DWORD dst_unused:UNUSED_PAD src0_sel:DWORD src1_sel:WORD_0\n\tv_add_u32_sdwa v57, %[w], v16 dst_sel:DWORD dst_unused:UNUSED_PAD src0_sel:DWORD src1_sel:WORD_1\n\t" \
  "v_add_u32_sdwa v58, %[w], v17 dst_sel:DWORD dst_unused:UNUSED_PAD src0_sel:DWORD src1_sel:WORD_0\n\tv_add_u32_sdwa v59, %[w], v17 dst_sel:DWORD dst_unused:UNUSED_PAD src0_sel:DWORD src1_sel:WORD_1\n\t" \
  "v_add_u32 v14, v56, v35\n\tv_add_u32 v15, v57, v35\n\t" \
  "s_waitcnt lgkmcnt(12)\n\tv_mfma_f32_16x16x32_f16 v[60:63], v[104:107], v[72:75], 0\n\t" \
  "v_mfma_f32_16x16x32_f16 v[4:7], v[136:139], v[72:75], 0\n\ts_cmp_lt_u32 %[w], 50176\n\t" \
  "s_cbranch_scc1 1f\n\ts_setprio 1\n\t" \
  "1:\n\ts_waitcnt lgkmcnt(2)\n\t" \
  "v_mfma_f32_16x16x32_f16 v[60:63], v[108:111], v[76:79], v[60:63]\n\tv_pk_mul_f16 v52, v40, v72 op_sel:[1,1] op_sel_hi:[1,0] neg_lo:[1,0]\n\t" \
  "v_pk_mul_f16 v53, v41, v73 op_sel:[1,1] op_sel_hi:[1,0] neg_lo:[1,0]\n\tv_pk_mul_f16 v54, v42, v74 op_sel:[1,1] op_sel_hi:[1,0] neg_lo:[1,0]\n\t" \
  "v_pk_mul_f16 v55, v43, v75 op_sel:[1,1] op_sel_hi:[1,0] neg_lo:[1,0]\n\tv_mfma_f32_16x16x32_f16 v[4:7], v[140:143], v[76:79], v[4:7]\n\t" \
  "v_pk_fma_f16 v40, v40, v72, v52 op_sel:[0,0,0] op_sel_hi:[0,1,1]\n\tv_pk_fma_f16 v41, v41, v73, v53 op_sel:[0,0,0] op_sel_hi:[0,1,1]\n\t" \
  "v_pk_fma_f16 v42, v42, v74, v54 op_sel:[0,0,0] op_sel_hi:[0,1,1]\n\tv_pk_fma_f16 v43, v43, v75, v55 op_sel:[0,0,0] op_sel_hi:[0,1,1]\n\t" \
  "s_waitcnt lgkmcnt(1)\n\tv_mfma_f32_16x16x32_f16 v[60:63], v[112:115], v[80:83], v[60:63]\n\t" \
  "ds_read_b128 v[72:75], v12 offset:192\n\tv_pk_mul_f16 v52, v44, v76 op_sel:[1,1] op_sel_hi:[1,0] neg_lo:[1,0]\n\t" \
  "v_pk_mul_f16 v53, v45, v77 op_sel:[1,1] op_sel_hi:[1,0] neg_lo:[1,0]\n\tv_pk_mul_f16 v54, v46, v78 op_sel:[1,1] op_sel_hi:[1,0] neg_lo:[1,0]\n\t" \
  "v_pk_mul_f16 v55, v47, v79 op_sel:[1,1] op_sel_hi:[1,0] neg_lo:[1,0]\n\tv_mfma_f32_16x16x32_f16 v[4:7], v[144:147], v[80:83], v[4:7]\n\t" \
  "v_pk_fma_f16 v44, v44, v76, v52 op_sel:[0,0,0] op_sel_hi:[0,1,1]\n\tv_pk_fma_f16 v45, v45, v77, v53 op_sel:[0,0,0] op_sel_hi:[0,1,1]\n\t" \
  "v_pk_fma_f16 v46, v46, v78, v54 op_sel:[0,0,0] op_sel_hi:[0,1,1]\n\tv_pk_fma_f16 v47, v47, v79, v55 op_sel:[0,0,0] op_sel_hi:[0,1,1]\n\t" \
  "s_waitcnt lgkmcnt(1)\n\tv_mfma_f32_16x16x32_f16 v[60:63], v[116:119], v[84:87], v[60:63]\n\t" \
  "ds_read_b128 v[76:79], v12 offset:4608\n\tv_pk_mul_f16 v52, v48, v80 op_sel:[1,1] op_sel_hi:[1,0] neg_lo:[1,0]\n\t" \
  "v_pk_mul_f16 v53, v49, v81 op_sel:[1,1] op_sel_hi:[1,0] neg_lo:[1,0]\n\tv_pk_mul_f16 v54, v50, v82 op_sel:[1,1] op_sel_hi:[1,0] neg_lo:[1,0]\n\t" \
  "v_pk_mul_f16 v55, v51, v83 op_sel:[1,1] op_sel_hi:[1,0] neg_lo:[1,0]\n\tv_mfma_f32_16x16x32_f16 v[4:7], v[148:151], v[84:87], v[4:7]\n\t" \
  "v_pk_fma_f16 v48, v48, v80, v52 op_sel:[0,0,0] op_sel_hi:[0,1,1]\n\tv_pk_fma_f16 v49, v49, v81, v53 op_sel:[0,0,0] op_sel_hi:[0,1,1]\n\t" \
  "v_pk_fma_f16 v50, v50, v82, v54 op_sel:[0,0,0] op_sel_hi:[0,1,1]\n\tv_pk_fma_f16 v51, v51, v83, v55 op_sel:[0,0,0] op_sel_hi:[0,1,1]\n\t" \
  "s_waitcnt lgkmcnt(1)\n\tv_mfma_f32_16x16x32_f16 v[60:63], v[120:123], v[88:91], v[60:63]\n\t" \
  "ds_read_b128 v[80:83], v12 offset:4672\n\tv_pk_mul_f16 v52, v72, v84 op_sel:[1,1] op_sel_hi:[1,0] neg_lo:[1,0]\n\t" \
  "v_pk_mul_f16 v53, v73, v85 op_sel:[1,1] op_sel_hi:[1,0] neg_lo:[1,0]\n\tv_pk_mul_f16 v54, v74, v86 op_sel:[1,1] op_sel_hi:[1,0] neg_lo:[1,0]\n\t" \
  "v_pk_mul_f16 v55, v75, v87 op_sel:[1,1] op_sel_hi:[1,0] neg_lo:[1,0]\n\tv_mfma_f32_16x16x32_f16 v[4:7], v[152:155], v[88:91], v[4:7]\n\t" \
  "v_pk_fma_f16 v72, v72, v84, v52 op_sel:[0,0,0] op_sel_hi:[0,1,1]\n\tv_pk_fma_f16 v73, v73, v85, v53 op_sel:[0,0,0] op_sel_hi:[0,1,1]\n\t" \
  "v_pk_fma_f16 v74, v74, v86, v54 op_sel:[0,0,0] op_sel_hi:[0,1,1]\n\tv_pk_fma_f16 v75, v75, v87, v55 op_sel:[0,0,0] op_sel_hi:[0,1,1]\n\t" \
  "s_waitcnt lgkmcnt(1)\n\tv_mfma_f32_16x16x32_f16 v[60:63], v[124:127], v[92:95], v[60:63]\n\t" \
  "ds_read_b128 v[84:87], v12 offset:4736\n\tv_pk_mul_f16 v52, v76, v88 op_sel:[1,1] op_sel_hi:[1,0] neg_lo:[1,0]\n\t" \
  "v_pk_mul_f16 v53, v77, v89 op_sel:[1,1] op_sel_hi:[1,0] neg_lo:[1,0]\n\tv_pk_mul_f16 v54, v78, v90 op_sel:[1,1] op_sel_hi:[1,0] neg_lo:[1,0]\n\t" \
  "v_pk_mul_f16 v55, v79, v91 op_sel:[1,1] op_sel_hi:[1,0] neg_lo:[1,0]\n\tv_mfma_f32_16x16x32_f16 v[4:7], v[156:159], v[92:95], v[4:7]\n\t" \
  "v_pk_fma_f16 v76, v76, v88, v52 op_sel:[0,0,0] op_sel_hi:[0,1,1]\n\tv_pk_fma_f16 v77, v77, v89, v53 op_sel:[0,0,0] op_sel_hi:[0,1,1]\n\t" \
  "v_pk_fma_f16 v78, v78, v90, v54 op_sel:[0,0,0] op_sel_hi:[0,1,1]\n\tv_pk_fma_f16 v79, v79, v91, v55 op_sel:[0,0,0] op_sel_hi:[0,1,1]\n\t" \
  "s_waitcnt lgkmcnt(1)\n\tv_mfma_f32_16x16x32_f16 v[60:63], v[128:131], v[96:99], v[60:63]\n\t" \
  "ds_read_b128 v[88:91], v12 offset:4800\n\tv_pk_mul_f16 v52, v80, v92 op_sel:[1,1] op_sel_hi:[1,0] neg_lo:[1,0]\n\t" \
  "v_pk_mul_f16 v53, v81, v93 op_sel:[1,1] op_sel_hi:[1,0] neg_lo:[1,0]\n\tv_pk_mul_f16 v54, v82, v94 op_sel:[1,1] op_sel_hi:[1,0] neg_lo:[1,0]\n\t" \
  "v_pk_mul_f16 v55, v83, v95 op_sel:[1,1] op_sel_hi:[1,0] neg_lo:[1,0]\n\tv_mfma_f32_16x16x32_f16 v[4:7], v[160:163], v[96:99], v[4:7]\n\t" \
  "v_pk_fma_f16 v80, v80, v92, v52 op_sel:[0,0,0] op_sel_hi:[0,1,1]\n\tv_pk_fma_f16 v81, v81, v93, v53 op_sel:[0,0,0] op_sel_hi:[0,1,1]\n\t" \
  "v_pk_fma_f16 v82, v82, v94, v54 op_sel:[0,0,0] op_sel_hi:[0,1,1]\n\tv_pk_fma_f16 v83, v83, v95, v55 op_sel:[0,0,0] op_sel_hi:[0,1,1]\n\t" \
  "s_waitcnt lgkmcnt(1)\n\tv_mfma_f32_16x16x32_f16 v[60:63], v[132:135], v[100:103], v[60:63]\n\t" \
  "v_pk_mul_f16 v52, v84, v96 op_sel:[1,1] op_sel_hi:[1,0] neg_lo:[1,0]\n\tv_pk_mul_f16 v53, v85, v97 op_sel:[1,1] op_sel_hi:[1,0] neg_lo:[1,0]\n\t" \
  "v_pk_mul_f16 v54, v86, v98 op_sel:[1,1] op_sel_hi:[1,0] neg_lo:[1,0]\n\tv_pk_mul_f16 v55, v87, v99 op_sel:[1,1] op_sel_hi:[1,0] neg_lo:[1,0]\n\t" \
  "v_mfma_f32_16x16x32_f16 v[4:7], v[164:167], v[100:103], v[4:7]\n\tv_pk_fma_f16 v84, v84, v96, v52 op_sel:[0,0,0] op_sel_hi:[0,1,1]\n\t" \
  "v_pk_fma_f16 v85, v85, v97, v53 op_sel:[0,0,0] op_sel_hi:[0,1,1]\n\tv_pk_fma_f16 v86, v86, v98, v54 op_sel:[0,0,0] op_sel_hi:[0,1,1]\n\t" \
  "v_pk_fma_f16 v87, v87, v99, v55 op_sel:[0,0,0] op_sel_hi:[0,1,1]\n\ts_waitcnt lgkmcnt(0)\n\t" \
  "v_mfma_f32_16x16x32_f16 v[8:11], v[136:139], v[40:43], 0\n\tv_pk_mul_f16 v52, v88, v100 op_sel:[1,1] op_sel_hi:[1,0] neg_lo:[1,0]\n\t" \
  "v_pk_mul_f16 v53, v89, v101 op_sel:[1,1] op_sel_hi:[1,0] neg_lo:[1,0]\n\tv_pk_mul_f16 v54, v90, v102 op_sel:[1,1] op_sel_hi:[1,0] neg_lo:[1,0]\n\t" \
  "v_mfma_f32_16x16x32_f16 v[64:67], v[104:107], v[40:43], 0\n\tv_pk_mul_f16 v55, v91, v103 op_sel:[1,1] op_sel_hi:[1,0] neg_lo:[1,0]\n\t" \
  "v_pk_fma_f16 v88, v88, v100, v52 op_sel:[0,0,0] op_sel_hi:[0,1,1]\n\tv_pk_fma_f16 v89, v89, v101, v53 op_sel:[0,0,0] op_sel_hi:[0,1,1]\n\t" \
  "v_mfma_f32_16x16x32_f16 v[8:11], v[140:143], v[44:47], v[8:11]\n\tv_pk_fma_f16 v90, v90, v102, v54 op_sel:[0,0,0] op_sel_hi:[0,1,1]\n\t" \
  "v_pk_fma_f16 v91, v91, v103, v55 op_sel:[0,0,0] op_sel_hi:[0,1,1]\n\tv_mov_b64 v[100:101], 0\n\t" \
  "v_mfma_f32_16x16x32_f16 v[64:67], v[108:111], v[44:47], v[64:67]\n\tv_mov_b64 v[102:103], 0\n\t" \
  "v_cvt_pk_f16_f32 v4, v4, v5\n\tv_cvt_pk_f16_f32 v5, v6, v7\n\t" \
  "v_mfma_f32_16x16x32_f16 v[8:11], v[144:147], v[48:51], v[8:11]\n\tv_cvt_pk_f16_f32 v92, v60, v61\n\t" \
  "v_cvt_pk_f16_f32 v93, v62, v63\n\tv_permlane16_swap_b32 v4, v5\n\t" \
  "v_mfma_f32_16x16x32_f16 v[64:67], v[112:115], v[48:51], v[64:67]\n\tv_perm_b32 v6, v4, v4, v33\n\t" \
  "v_perm_b32 v7, v4, v4, v34\n\tv_mfma_f32_16x16x32_f16 v[8:11], v[148:151], v[72:75], v[8:11]\n\t" \
  "ds_write_b128 v58, v[100:103]\n\tv_mfma_f32_16x16x32_f16 v[64:67], v[116:119], v[72:75], v[64:67]\n\t" \
  "ds_write2_b32 v56, v92, v93 offset0:193 offset1:194\n\tv_mfma_f32_16x16x32_f16 v[8:11], v[152:155], v[76:79], v[8:11]\n\t" \
  "ds_write2_b32 v57, v92, v93 offset0:193 offset1:194\n\tv_mfma_f32_16x16x32_f16 v[64:67], v[120:123], v[76:79], v[64:67]\n\t" \
  "ds_write2_b32 v14, v6, v7 offset0:64 offset1:65\n\tv_mfma_f32_16x16x32_f16 v[8:11], v[156:159], v[80:83], v[8:11]\n\t" \
  "ds_write2_b32 v15, v6, v7 offset0:64 offset1:65\n\tv_mfma_f32_16x16x32_f16 v[64:67], v[124:127], v[80:83], v[64:67]\n\t" \
  "ds_write_b16 v59, v5 offset:48\n\tv_mfma_f32_16x16x32_f16 v[8:11], v[160:163], v[84:87], v[8:11]\n\t" \
  "ds_write_b16_d16_hi v59, v5 offset:16\n\tv_mfma_f32_16x16x32_f16 v[64:67], v[128:131], v[84:87], v[64:67]\n\t" \
  "ds_write_b16 v59, v5 offset:878\n\tv_mfma_f32_16x16x32_f16 v[8:11], v[164:167], v[88:91], v[8:11]\n\t" \
  "ds_write_b16_d16_hi v59, v5 offset:846\n\tv_mfma_f32_16x16x32_f16 v[64:67], v[132:135], v[88:91], v[64:67]\n\t" \
  "ds_write_b16 v59, v5 offset:1708\n\tds_write_b16_d16_hi v59, v5 offset:1676\n\t" \
  "ds_write_b16 v59, v5 offset:2538\n\tds_write_b16_d16_hi v59, v5 offset:2506\n\t" \
  "s_nop 1\n\tv_cvt_pk_f16_f32 v8, v8, v9\n\t" \
  "v_cvt_pk_f16_f32 v9, v10, v11\n\tv_cvt_pk_f16_f32 v46, v64, v65\n\t" \
  "v_cvt_pk_f16_f32 v47, v66, v67\n\tv_permlane16_swap_b32 v8, v9\n\t" \
  "v_perm_b32 v10, v8, v8, v33\n\tv_perm_b32 v11, v8, v8, v34\n\t" \
  "ds_write2_b32 v56, v46, v47 offset0:129 offset1:130\n\tds_write2_b32 v57, v46, v47 offset0:129 offset1:130\n\t" \
  "ds_write2_b32 v14, v10, v11 offset0:0 offset1:1\n\tds_write2_b32 v15, v10, v11 offset0:0 offset1:1\n\t" \
  "ds_write_b16 v59, v9 offset:32\n\tds_write_b16_d16_hi v59, v9 offset:0\n\t" \
  "ds_write_b16 v59, v9 offset:862\n\tds_write_b16_d16_hi v59, v9 offset:830\n\t" \
  "ds_write_b16 v59, v9 offset:1692\n\tds_write_b16_d16_hi v59, v9 offset:1660\n\t" \
  "ds_write_b16 v59, v9 offset:2522\n\tds_write_b16_d16_hi v59, v9 offset:2490\n\t"
#define GEN_CLOBBER "v4", "v5", "v6", "v7", "v10", "v11", "v13", "v14", "v15", "v36", "v37", "v38", "v39", "v40", "v41", "v42", "v43", "v44", "v45", "v47", "v48", "v49", "v50", "v51", "v52", "v53", "v54", "v55", "v56", "v57", "v58", "v59", "v60", "v61", "v62", "v63", "v64", "v65", "v66", "v67", "v72", "v73", "v74", "v75", "v76", "v77", "v78", "v79", "v80", "v81", "v82", "v83", "v84", "v85", "v86", "v87", "v88", "v89", "v90", "v91", "v92", "v94", "v95", "v98", "v99", "v100", "v101", "v102", "v103", "v104", "v105", "v106", "v107", "v108", "v109", "v110", "v111", "v112", "v113", "v114", "v115", "v116", "v117", "v118", "v119", "v120", "v121", "v122", "v123", "v124", "v125", "v126", "v127", "v128", "v129", "v130", "v131", "v132", "v133", "v134", "v135", "v136", "v137", "v138", "v139", "v140", "v141", "v142", "v143", "v144", "v145", "v146", "v147", "v148", "v149", "v150", "v151", "v152", "v153", "v154", "v155", "v156", "v157", "v158", "v159", "v160", "v161", "v162", "v163", "v164", "v165", "v166", "v167"
#define CONV_ASM \
  "ds_read_b32 v34, v30\n\tds_read_b64 v[132:133], v26 offset:512\n\tds_read_b64 v[134:135], v26 offset:520\n\t" \
  "ds_read_b64 v[100:101], v26 offset:0\n\tds_read_b64 v[102:103], v26 offset:8\n\tds_read_b64 v[136:137], v26 offset:576\n\t" \
  "ds_read_b64 v[138:139], v26 offset:584\n\tds_read_b64 v[104:105], v26 offset:64\n\tds_read_b64 v[106:107], v26 offset:72\n\t" \
  "ds_read_b64 v[140:141], v26 offset:640\n\tds_read_b64 v[142:143], v26 offset:648\n\tds_read_b64 v[108:109], v26 offset:128\n\t" \
  "ds_read_b64 v[110:111], v26 offset:136\n\tds_read_b64 v[144:145], v26 offset:704\n\tds_read_b64 v[146:147], v26 offset:712\n\t" \
  "ds_read_b64 v[112:113], v26 offset:192\n\tds_read_b64 v[114:115], v26 offset:200\n\tds_read_b64 v[148:149], v26 offset:768\n\t" \
  "ds_read_b64 v[150:151], v26 offset:776\n\tds_read_b64 v[116:117], v26 offset:256\n\tds_read_b64 v[118:119], v26 offset:264\n\t" \
  TS(60, 61) "s_waitcnt lgkmcnt(15)\n\tv_cmp_gt_u32 vcc, 1, v34\n\t" \
  "s_cbranch_vccz 2f\n\ts_mov_b32 s6, 0x10000\n\t1:\n\t" \
  "s_sleep 1\n\tds_read_b32 v34, v30\n\ts_sub_u32 s6, s6, 1\n\t" \
  "s_waitcnt lgkmcnt(0)\n\tv_cmp_gt_u32 vcc, 1, v34\n\ts_cbranch_scc1 2f\n\t" \
  "s_cbranch_vccnz 1b\n\t2:\n\t" TS(62, 63) \
  "s_cmp_lt_u32 %[w], 4\n\ts_setprio 1\n\ts_cbranch_scc1 1f\n\t" \
  "s_setprio 2\n\t1:\n\tds_read_b128 v[0:3], v28 offset:0\n\t" \
  "ds_read_b128 v[4:7], v28 offset:64\n\tds_read_b128 v[8:11], v28 offset:128\n\tds_read_b128 v[12:15], v28 offset:192\n\t" \
  "ds_read_b128 v[16:19], v28 offset:256\n\tds_read_b128 v[20:23], v28 offset:320\n\ts_waitcnt lgkmcnt(5)\n\t" \
  "v_mfma_f32_16x16x32_f16 v[52:55], v[132:135], v[0:3], 0\n\tds_read_b64 v[152:153], v26 offset:832\n\tds_read_b64 v[154:155], v26 offset:840\n\t" \
  "v_mfma_f32_16x16x32_f16 v[56:59], v[100:103], v[0:3], 0\n\tds_read_b64 v[120:121], v26 offset:320\n\tds_read_b64 v[122:123], v26 offset:328\n\t" \
  "ds_read_b128 v[0:3], v28 offset:384\n\ts_waitcnt lgkmcnt(9)\n\tv_mfma_f32_16x16x32_f16 v[52:55], v[136:139], v[4:7], v[52:55]\n\t" \
  "ds_read_b64 v[156:157], v26 offset:896\n\tds_read_b64 v[158:159], v26 offset:904\n\tv_mfma_f32_16x16x32_f16 v[56:59], v[104:107], v[4:7], v[56:59]\n\t" \
  "ds_read_b64 v[124:125], v26 offset:384\n\tds_read_b64 v[126:127], v26 offset:392\n\tds_read_b128 v[4:7], v28 offset:448\n\t" \
  "s_waitcnt lgkmcnt(13)\n\tv_mfma_f32_16x16x32_f16 v[52:55], v[140:143], v[8:11], v[52:55]\n\tds_read_b64 v[160:161], v26 offset:960\n\t" \
  "ds_read_b64 v[162:163], v26 offset:968\n\tv_mfma_f32_16x16x32_f16 v[56:59], v[108:111], v[8:11], v[56:59]\n\tds_read_b64 v[128:129], v26 offset:448\n\t" \
  "ds_read_b64 v[130:131], v26 offset:456\n\tds_read_b128 v[8:11], v28 offset:512\n\ts_waitcnt lgkmcnt(15)\n\t" \
  "v_mfma_f32_16x16x32_f16 v[52:55], v[144:147], v[12:15], v[52:55]\n\tds_read_b64 v[164:165], v26 offset:1024\n\tds_read_b64 v[166:167], v26 offset:1032\n\t" \
  "v_mfma_f32_16x16x32_f16 v[56:59], v[112:115], v[12:15], v[56:59]\n\tds_read_b64 v[76:77], v31 offset:72\n\tds_read_b64 v[78:79], v31 offset:176\n\t" \
  "ds_read_b128 v[12:15], v28 offset:576\n\tv_mfma_f32_16x16x32_f16 v[52:55], v[148:151], v[16:19], v[52:55]\n\tds_read_b64 v[80:81], v31 offset:40\n\t" \
  "ds_read_b64 v[82:83], v31 offset:144\n\tv_mfma_f32_16x16x32_f16 v[56:59], v[116:119], v[16:19], v[56:59]\n\tds_read_b64 v[84:85], v31 offset:8\n\t" \
  "ds_read_b64 v[86:87], v31 offset:112\n\tds_read_b128 v[16:19], v28 offset:640\n\ts_waitcnt lgkmcnt(15)\n\t" \
  "v_mfma_f32_16x16x32_f16 v[52:55], v[152:155], v[20:23], v[52:55]\n\tds_read_b64 v[88:89], v31 offset:280\n\tds_read_b64 v[90:91], v31 offset:384\n\t" \
  "v_mfma_f32_16x16x32_f16 v[56:59], v[120:123], v[20:23], v[56:59]\n\tds_read_b64 v[92:93], v31 offset:248\n\tds_read_b64 v[94:95], v31 offset:352\n\t" \
  "ds_read_b128 v[20:23], v28 offset:704\n\tv_mfma_f32_16x16x32_f16 v[52:55], v[156:159], v[0:3], v[52:55]\n\tds_read_b64 v[96:97], v31 offset:216\n\t" \
  "ds_read_b64 v[98:99], v31 offset:320\n\tv_mfma_f32_16x16x32_f16 v[56:59], v[124:127], v[0:3], v[56:59]\n\tds_read_b128 v[0:3], v28 offset:768\n\t" \
  "s_waitcnt lgkmcnt(15)\n\tv_mfma_f32_16x16x32_f16 v[52:55], v[160:163], v[4:7], v[52:55]\n\tv_mfma_f32_16x16x32_f16 v[56:59], v[128:131], v[4:7], v[56:59]\n\t" \
  "ds_read_b128 v[4:7], v28 offset:832\n\tv_mfma_f32_16x16x32_f16 v[52:55], v[164:167], v[8:11], v[52:55]\n\tv_mfma_f32_16x16x32_f16 v[56:59], v[132:135], v[8:11], v[56:59]\n\t" \
  "v_mfma_f32_16x16x32_f16 v[60:63], v[100:103], v[8:11], 0\n\tds_read_b128 v[8:11], v28 offset:896\n\ts_waitcnt lgkmcnt(15)\n\t" \
  "v_mfma_f32_16x16x32_f16 v[56:59], v[136:139], v[12:15], v[56:59]\n\tds_read_b32 v34, v30\n\tv_mfma_f32_16x16x32_f16 v[60:63], v[104:107], v[12:15], v[60:63]\n\t" \
  "ds_read_b128 v[12:15], v28 offset:960\n\tv_cvt_pk_bf16_f32 v52, v52, v53\n\tv_cvt_pk_bf16_f32 v53, v54, v55\n\t" \
  TS(64, 65) "s_waitcnt lgkmcnt(12)\n\tv_mfma_f32_16x16x32_f16 v[56:59], v[140:143], v[16:19], v[56:59]\n\t" \
  "ds_write_b64 v29, v[52:53] offset:0\n\tds_write_b32 v25, v25 offset:0\n\tv_mfma_f32_16x16x32_f16 v[60:63], v[108:111], v[16:19], v[60:63]\n\t" \
  "s_waitcnt lgkmcnt(3)\n\tv_cmp_gt_u32 vcc, 2, v34\n\ts_cbranch_vccz 2f\n\t" \
  "s_mov_b32 s6, 0x10000\n\t1:\n\ts_sleep 1\n\t" \
  "ds_read_b32 v34, v30\n\ts_sub_u32 s6, s6, 1\n\ts_waitcnt lgkmcnt(0)\n\t" \
  "v_cmp_gt_u32 vcc, 2, v34\n\ts_cbranch_scc1 2f\n\ts_cbranch_vccnz 1b\n\t" \
  "2:\n\tds_read_b128 v[16:19], v28 offset:1024\n\tv_mfma_f32_16x16x32_f16 v[56:59], v[144:147], v[20:23], v[56:59]\n\t" \
  "v_mfma_f32_16x16x32_f16 v[60:63], v[112:115], v[20:23], v[60:63]\n\tds_read_b128 v[68:71], v32\n\tds_read_b128 v[72:75], v32 offset:1024\n\t" \
  "ds_read_b128 v[20:23], v28 offset:1088\n\tv_mfma_f32_16x16x32_f16 v[56:59], v[148:151], v[0:3], v[56:59]\n\tv_mfma_f32_16x16x32_f16 v[60:63], v[116:119], v[0:3], v[60:63]\n\t" \
  "ds_read_b128 v[0:3], v28 offset:496\n\tv_mfma_f32_16x16x32_f16 v[56:59], v[152:155], v[4:7], v[56:59]\n\tv_mfma_f32_16x16x32_f16 v[60:63], v[120:123], v[4:7], v[60:63]\n\t" \
  "ds_read_b128 v[4:7], v28 offset:1152\n\tv_mfma_f32_16x16x32_f16 v[56:59], v[156:159], v[8:11], v[56:59]\n\tv_mfma_f32_16x16x32_f16 v[60:63], v[124:127], v[8:11], v[60:63]\n\t" \
  "ds_read_b128 v[8:11], v28 offset:1216\n\ts_waitcnt lgkmcnt(9)\n\tv_mfma_f32_16x16x32_f16 v[56:59], v[160:163], v[12:15], v[56:59]\n\t" \
  "v_mfma_f32_16x16x32_f16 v[60:63], v[128:131], v[12:15], v[60:63]\n\tds_read_b128 v[12:15], v28 offset:480\n\ts_waitcnt lgkmcnt(7)\n\t" \
  "v_mfma_f32_16x16x32_f16 v[56:59], v[164:167], v[16:19], v[56:59]\n\tv_mfma_f32_16x16x32_f16 v[60:63], v[132:135], v[16:19], v[60:63]\n\tv_mfma_f32_16x16x32_f16 v[64:67], v[100:103], v[16:19], 0\n\t" \
  "ds_read_b128 v[16:19], v28 offset:1280\n\ts_waitcnt lgkmcnt(5)\n\tv_mfma_f32_16x16x32_f16 v[60:63], v[136:139], v[20:23], v[60:63]\n\t" \
  "v_mfma_f32_16x16x32_f16 v[64:67], v[104:107], v[20:23], v[64:67]\n\tds_read_b128 v[20:23], v28 offset:1344\n\ts_waitcnt lgkmcnt(5)\n\t" \
  "v_mfma_f32_16x16x32_f16 v[36:39], v[0:3], v[68:71], 0\n\tv_cvt_pk_bf16_f32 v56, v56, v57\n\tv_cvt_pk_bf16_f32 v57, v58, v59\n\t" \
  "v_mfma_f32_16x16x32_f16 v[40:43], v[0:3], v[72:75], 0\n\tds_write_b64 v29, v[56:57] offset:512\n\tds_write_b32 v25, v25 offset:64\n\t" \
  "ds_read_b128 v[0:3], v28 offset:1408\n\ts_waitcnt lgkmcnt(7)\n\tv_mfma_f32_16x16x32_f16 v[60:63], v[140:143], v[4:7], v[60:63]\n\t" \
  "v_cvt_pk_f16_f32 v36, v36, v37\n\tv_cvt_pk_f16_f32 v37, v38, v39\n\tv_mfma_f32_16x16x32_f16 v[64:67], v[108:111], v[4:7], v[64:67]\n\t" \
  "ds_read_b128 v[4:7], v28 offset:1472\n\tv_cvt_pk_f16_f32 v38, v40, v41\n\tv_cvt_pk_f16_f32 v39, v42, v43\n\t" \
  "s_waitcnt lgkmcnt(7)\n\tv_mfma_f32_16x16x32_f16 v[60:63], v[144:147], v[8:11], v[60:63]\n\tv_mfma_f32_16x16x32_f16 v[64:67], v[112:115], v[8:11], v[64:67]\n\t" \
  "ds_read_b128 v[8:11], v28 offset:1536\n\tv_mfma_f32_16x16x32_f16 v[60:63], v[36:39], v[76:79], v[60:63]\n\tv_mfma_f32_16x16x32_f16 v[64:67], v[36:39], v[80:83], v[64:67]\n\t" \
  "v_mfma_f32_16x16x32_f16 v[52:55], v[36:39], v[84:87], 0\n\ts_waitcnt lgkmcnt(7)\n\tv_mfma_f32_16x16x32_f16 v[44:47], v[12:15], v[68:71], 0\n\t" \
  "v_mfma_f32_16x16x32_f16 v[40:43], v[12:15], v[72:75], 0\n\tds_read_b128 v[12:15], v28 offset:1600\n\ts_waitcnt lgkmcnt(7)\n\t" \
  "v_mfma_f32_16x16x32_f16 v[60:63], v[148:151], v[16:19], v[60:63]\n\tv_mfma_f32_16x16x32_f16 v[64:67], v[116:119], v[16:19], v[64:67]\n\tds_read_b128 v[16:19], v28 offset:1008\n\t" \
  "s_waitcnt lgkmcnt(7)\n\tv_mfma_f32_16x16x32_f16 v[60:63], v[152:155], v[20:23], v[60:63]\n\tv_cvt_pk_f16_f32 v44, v44, v45\n\t" \
  "v_cvt_pk_f16_f32 v45, v46, v47\n\tv_cvt_pk_f16_f32 v46, v40, v41\n\tv_cvt_pk_f16_f32 v47, v42, v43\n\t" \
  "v_mfma_f32_16x16x32_f16 v[64:67], v[120:123], v[20:23], v[64:67]\n\tds_read_b128 v[20:23], v28 offset:1664\n\tv_mfma_f32_16x16x32_f16 v[60:63], v[44:47], v[88:91], v[60:63]\n\t" \
  "v_mfma_f32_16x16x32_f16 v[64:67], v[44:47], v[92:95], v[64:67]\n\tv_mfma_f32_16x16x32_f16 v[52:55], v[44:47], v[96:99], v[52:55]\n\ts_waitcnt lgkmcnt(5)\n\t" \
  "v_mfma_f32_16x16x32_f16 v[60:63], v[156:159], v[0:3], v[60:63]\n\tv_mfma_f32_16x16x32_f16 v[64:67], v[124:127], v[0:3], v[64:67]\n\tds_read_b128 v[0:3], v28 offset:1728\n\t" \
  "s_waitcnt lgkmcnt(5)\n\tv_mfma_f32_16x16x32_f16 v[60:63], v[160:163], v[4:7], v[60:63]\n\tv_mfma_f32_16x16x32_f16 v[64:67], v[128:131], v[4:7], v[64:67]\n\t" \
  "ds_read_b128 v[4:7], v28 offset:992\n\ts_waitcnt lgkmcnt(5)\n\tv_mfma_f32_16x16x32_f16 v[60:63], v[164:167], v[8:11], v[60:63]\n\t" \
  "v_mfma_f32_16x16x32_f16 v[64:67], v[132:135], v[8:11], v[64:67]\n\tv_mfma_f32_16x16x32_f16 v[52:55], v[100:103], v[8:11], v[52:55]\n\tds_read_b128 v[8:11], v28 offset:1792\n\t" \
  "s_waitcnt lgkmcnt(5)\n\tv_mfma_f32_16x16x32_f16 v[64:67], v[136:139], v[12:15], v[64:67]\n\tv_mfma_f32_16x16x32_f16 v[52:55], v[104:107], v[12:15], v[52:55]\n\t" \
  "ds_read_b128 v[12:15], v28 offset:1856\n\ts_waitcnt lgkmcnt(5)\n\tv_mfma_f32_16x16x32_f16 v[36:39], v[16:19], v[68:71], 0\n\t" \
  "v_cvt_pk_bf16_f32 v60, v60, v61\n\tv_cvt_pk_bf16_f32 v61, v62, v63\n\tv_mfma_f32_16x16x32_f16 v[40:43], v[16:19], v[72:75], 0\n\t" \
  "ds_write_b64 v29, v[60:61] offset:1024\n\tds_write_b32 v25, v25 offset:128\n\tds_read_b128 v[16:19], v28 offset:1920\n\t" \
  "s_waitcnt lgkmcnt(7)\n\tv_mfma_f32_16x16x32_f16 v[64:67], v[140:143], v[20:23], v[64:67]\n\tds_read_b32 v34, v30\n\t" \
  "v_cvt_pk_f16_f32 v36, v36, v37\n\tv_cvt_pk_f16_f32 v37, v38, v39\n\tv_mfma_f32_16x16x32_f16 v[52:55], v[108:111], v[20:23], v[52:55]\n\t" \
  "ds_read_b128 v[20:23], v28 offset:1984\n\tv_cvt_pk_f16_f32 v38, v40, v41\n\tv_cvt_pk_f16_f32 v39, v42, v43\n\t" \
  "s_waitcnt lgkmcnt(8)\n\tv_mfma_f32_16x16x32_f16 v[64:67], v[144:147], v[0:3], v[64:67]\n\tv_mfma_f32_16x16x32_f16 v[52:55], v[112:115], v[0:3], v[52:55]\n\t" \
  "s_waitcnt lgkmcnt(1)\n\tv_cmp_gt_u32 vcc, 3, v34\n\ts_cbranch_vccz 2f\n\t" \
  "s_mov_b32 s6, 0x10000\n\t1:\n\ts_sleep 1\n\t" \
  "ds_read_b32 v34, v30\n\ts_sub_u32 s6, s6, 1\n\ts_waitcnt lgkmcnt(0)\n\t" \
  "v_cmp_gt_u32 vcc, 3, v34\n\ts_cbranch_scc1 2f\n\ts_cbranch_vccnz 1b\n\t" \
  "2:\n\tds_read_b128 v[0:3], v28 offset:2048\n\tv_mfma_f32_16x16x32_f16 v[64:67], v[36:39], v[76:79], v[64:67]\n\t" \
  "v_mfma_f32_16x16x32_f16 v[52:55], v[36:39], v[80:83], v[52:55]\n\tv_mfma_f32_16x16x32_f16 v[56:59], v[36:39], v[84:87], 0\n\tv_mfma_f32_16x16x32_f16 v[44:47], v[4:7], v[68:71], 0\n\t" \
  "v_mfma_f32_16x16x32_f16 v[40:43], v[4:7], v[72:75], 0\n\tds_read_b128 v[4:7], v28 offset:2112\n\tv_mfma_f32_16x16x32_f16 v[64:67], v[148:151], v[8:11], v[64:67]\n\t" \
  "v_mfma_f32_16x16x32_f16 v[52:55], v[116:119], v[8:11], v[52:55]\n\tds_read_b128 v[8:11], v28 offset:1520\n\tv_mfma_f32_16x16x32_f16 v[64:67], v[152:155], v[12:15], v[64:67]\n\t" \
  "v_mfma_f32_16x16x32_f16 v[52:55], v[120:123], v[12:15], v[52:55]\n\tds_read_b128 v[12:15], v28 offset:2176\n\tv_cvt_pk_f16_f32 v44, v44, v45\n\t" \
  "v_cvt_pk_f16_f32 v45, v46, v47\n\tv_cvt_pk_f16_f32 v46, v40, v41\n\tv_cvt_pk_f16_f32 v47, v42, v43\n\t" \
  "s_nop 1\n\tv_mfma_f32_16x16x32_f16 v[64:67], v[44:47], v[88:91], v[64:67]\n\tv_mfma_f32_16x16x32_f16 v[52:55], v[44:47], v[92:95], v[52:55]\n\t" \
  "v_mfma_f32_16x16x32_f16 v[56:59], v[44:47], v[96:99], v[56:59]\n\tv_mfma_f32_16x16x32_f16 v[64:67], v[156:159], v[16:19], v[64:67]\n\tv_mfma_f32_16x16x32_f16 v[52:55], v[124:127], v[16:19], v[52:55]\n\t" \
  "ds_read_b128 v[16:19], v28 offset:2240\n\ts_waitcnt lgkmcnt(5)\n\tv_mfma_f32_16x16x32_f16 v[64:67], v[160:163], v[20:23], v[64:67]\n\t" \
  "v_mfma_f32_16x16x32_f16 v[52:55], v[128:131], v[20:23], v[52:55]\n\tds_read_b128 v[20:23], v28 offset:1504\n\ts_waitcnt lgkmcnt(5)\n\t" \
  "v_mfma_f32_16x16x32_f16 v[64:67], v[164:167], v[0:3], v[64:67]\n\tv_mfma_f32_16x16x32_f16 v[52:55], v[132:135], v[0:3], v[52:55]\n\tv_mfma_f32_16x16x32_f16 v[56:59], v[100:103], v[0:3], v[56:59]\n\t" \
  "ds_read_b128 v[0:3], v28 offset:2304\n\ts_waitcnt lgkmcnt(5)\n\tv_mfma_f32_16x16x32_f16 v[52:55], v[136:139], v[4:7], v[52:55]\n\t" \
  "v_mfma_f32_16x16x32_f16 v[56:59], v[104:107], v[4:7], v[56:59]\n\tds_read_b128 v[4:7], v28 offset:2368\n\ts_waitcnt lgkmcnt(5)\n\t" \
  "v_mfma_f32_16x16x32_f16 v[36:39], v[8:11], v[68:71], 0\n\tv_cvt_pk_bf16_f32 v64, v64, v65\n\tv_cvt_pk_bf16_f32 v65, v66, v67\n\t" \
  TS(66, 67) "s_cmp_lt_u32 %[w], 4\n\ts_cbranch_scc1 1f\n\t" \
  "s_setprio 1\n\t1:\n\tv_mfma_f32_16x16x32_f16 v[40:43], v[8:11], v[72:75], 0\n\t" \
  "ds_write_b64 v29, v[64:65] offset:1536\n\tds_write_b32 v25, v25 offset:192\n\tds_read_b128 v[8:11], v28 offset:480\n\t" \
  "s_waitcnt lgkmcnt(7)\n\tv_mfma_f32_16x16x32_f16 v[52:55], v[140:143], v[12:15], v[52:55]\n\tv_cvt_pk_f16_f32 v36, v36, v37\n\t" \
  "v_cvt_pk_f16_f32 v37, v38, v39\n\tv_mfma_f32_16x16x32_f16 v[56:59], v[108:111], v[12:15], v[56:59]\n\tds_read_b128 v[12:15], v32 offset:3072\n\t" \
  "v_cvt_pk_f16_f32 v38, v40, v41\n\tv_cvt_pk_f16_f32 v39, v42, v43\n\ts_waitcnt lgkmcnt(7)\n\t" \
  "v_mfma_f32_16x16x32_f16 v[52:55], v[144:147], v[16:19], v[52:55]\n\tv_mfma_f32_16x16x32_f16 v[56:59], v[112:115], v[16:19], v[56:59]\n\tds_read_b128 v[16:19], v28 offset:496\n\t" \
  "v_mfma_f32_16x16x32_f16 v[52:55], v[36:39], v[76:79], v[52:55]\n\tv_mfma_f32_16x16x32_f16 v[56:59], v[36:39], v[80:83], v[56:59]\n\tv_mfma_f32_16x16x32_f16 v[60:63], v[36:39], v[84:87], 0\n\t" \
  "s_waitcnt lgkmcnt(7)\n\tv_mfma_f32_16x16x32_f16 v[44:47], v[20:23], v[68:71], 0\n\tv_mfma_f32_16x16x32_f16 v[40:43], v[20:23], v[72:75], 0\n\t" \
  "ds_read_b128 v[20:23], v32 offset:2048\n\ts_waitcnt lgkmcnt(7)\n\tv_mfma_f32_16x16x32_f16 v[52:55], v[148:151], v[0:3], v[52:55]\n\t" \
  "v_mfma_f32_16x16x32_f16 v[56:59], v[116:119], v[0:3], v[56:59]\n\tds_read_b128 v[0:3], v28 offset:2432\n\ts_waitcnt lgkmcnt(7)\n\t" \
  "v_mfma_f32_16x16x32_f16 v[52:55], v[152:155], v[4:7], v[52:55]\n\tv_cvt_pk_f16_f32 v44, v44, v45\n\tv_cvt_pk_f16_f32 v45, v46, v47\n\t" \
  "v_cvt_pk_f16_f32 v46, v40, v41\n\tv_cvt_pk_f16_f32 v47, v42, v43\n\tv_mfma_f32_16x16x32_f16 v[56:59], v[120:123], v[4:7], v[56:59]\n\t" \
  "ds_read_b128 v[4:7], v28 offset:2496\n\tv_mfma_f32_16x16x32_f16 v[52:55], v[44:47], v[88:91], v[52:55]\n\tv_mfma_f32_16x16x32_f16 v[56:59], v[44:47], v[92:95], v[56:59]\n\t" \
  "v_mfma_f32_16x16x32_f16 v[60:63], v[44:47], v[96:99], v[60:63]\n\ts_waitcnt lgkmcnt(4)\n\tv_mfma_f32_16x16x32_f16 v[40:43], v[8:11], v[12:15], 0\n\t" \
  "ds_read_b64 v[8:9], v31 offset:544\n\tds_read_b64 v[10:11], v31 offset:712\n\tds_read_b64 v[12:13], v31 offset:512\n\t" \
  "ds_read_b64 v[14:15], v31 offset:680\n\ts_waitcnt lgkmcnt(7)\n\tv_mfma_f32_16x16x32_f16 v[52:55], v[48:51], v[16:19], v[52:55]\n\t" \
  "s_waitcnt lgkmcnt(6)\n\tv_mfma_f32_16x16x32_f16 v[36:39], v[16:19], v[20:23], 0\n\tds_read_b64 v[16:17], v31 offset:480\n\t" \
  "ds_read_b64 v[18:19], v31 offset:648\n\tds_read_b64 v[20:21], v31 offset:448\n\tds_read_b64 v[22:23], v31 offset:616\n\t" \
  "s_waitcnt lgkmcnt(9)\n\tv_mfma_f32_16x16x32_f16 v[52:55], v[156:159], v[0:3], v[52:55]\n\tv_mfma_f32_16x16x32_f16 v[56:59], v[124:127], v[0:3], v[56:59]\n\t" \
  "ds_read_b128 v[0:3], v28 offset:2560\n\tv_cvt_pk_f16_f32 v36, v36, v37\n\tv_cvt_pk_f16_f32 v37, v38, v39\n\t" \
  "s_waitcnt lgkmcnt(9)\n\tv_mfma_f32_16x16x32_f16 v[52:55], v[160:163], v[4:7], v[52:55]\n\tv_cvt_pk_f16_f32 v38, v40, v41\n\t" \
  "v_cvt_pk_f16_f32 v39, v42, v43\n\tv_mfma_f32_16x16x32_f16 v[56:59], v[128:131], v[4:7], v[56:59]\n\tds_read_b128 v[4:7], v28 offset:2624\n\t" \
  "s_waitcnt lgkmcnt(8)\n\tv_mfma_f32_16x16x32_f16 v[52:55], v[36:39], v[8:11], v[52:55]\n\tds_read_b128 v[8:11], v28 offset:2032\n\t" \
  "s_waitcnt lgkmcnt(7)\n\tv_mfma_f32_16x16x32_f16 v[56:59], v[36:39], v[12:15], v[56:59]\n\tds_read_b128 v[12:15], v28 offset:2688\n\t" \
  "s_waitcnt lgkmcnt(6)\n\tv_mfma_f32_16x16x32_f16 v[60:63], v[36:39], v[16:19], v[60:63]\n\tds_read_b128 v[16:19], v28 offset:2752\n\t" \
  "s_waitcnt lgkmcnt(5)\n\tv_mfma_f32_16x16x32_f16 v[64:67], v[36:39], v[20:23], 0\n\tds_read_b128 v[20:23], v28 offset:2016\n\t" \
  "s_waitcnt lgkmcnt(5)\n\tv_mfma_f32_16x16x32_f16 v[52:55], v[164:167], v[0:3], v[52:55]\n\tv_mfma_f32_16x16x32_f16 v[56:59], v[132:135], v[0:3], v[56:59]\n\t" \
  "v_mfma_f32_16x16x32_f16 v[60:63], v[100:103], v[0:3], v[60:63]\n\tds_read_b128 v[0:3], v28 offset:2816\n\ts_waitcnt lgkmcnt(5)\n\t" \
  "v_mfma_f32_16x16x32_f16 v[56:59], v[136:139], v[4:7], v[56:59]\n\tv_mfma_f32_16x16x32_f16 v[60:63], v[104:107], v[4:7], v[60:63]\n\tds_read_b128 v[4:7], v28 offset:2880\n\t" \
  "s_waitcnt lgkmcnt(5)\n\tv_mfma_f32_16x16x32_f16 v[44:47], v[8:11], v[68:71], 0\n\tv_cvt_pk_bf16_f32 v52, v52, v53\n\t" \
  "v_cvt_pk_bf16_f32 v53, v54, v55\n\ts_cmp_lt_u32 %[w], 4\n\ts_cbranch_scc0 1f\n\t" \
  "s_setprio 0\n\t1:\n\tv_mfma_f32_16x16x32_f16 v[40:43], v[8:11], v[72:75], 0\n\t" \
  "ds_write_b64 v29, v[52:53] offset:2048\n\tds_write_b32 v25, v25 offset:256\n\tds_read_b128 v[8:11], v28 offset:992\n\t" \
  "s_waitcnt lgkmcnt(7)\n\tv_mfma_f32_16x16x32_f16 v[56:59], v[140:143], v[12:15], v[56:59]\n\tv_cvt_pk_f16_f32 v44, v44, v45\n\t" \
  "v_cvt_pk_f16_f32 v45, v46, v47\n\tv_mfma_f32_16x16x32_f16 v[60:63], v[108:111], v[12:15], v[60:63]\n\tds_read_b128 v[12:15], v32 offset:3072\n\t" \
  "v_cvt_pk_f16_f32 v46, v40, v41\n\tv_cvt_pk_f16_f32 v47, v42, v43\n\ts_waitcnt lgkmcnt(7)\n\t" \
  "v_mfma_f32_16x16x32_f16 v[56:59], v[144:147], v[16:19], v[56:59]\n\tv_mfma_f32_16x16x32_f16 v[60:63], v[112:115], v[16:19], v[60:63]\n\tds_read_b128 v[16:19], v28 offset:1008\n\t" \
  "v_mfma_f32_16x16x32_f16 v[56:59], v[44:47], v[76:79], v[56:59]\n\tv_mfma_f32_16x16x32_f16 v[60:63], v[44:47], v[80:83], v[60:63]\n\tv_mfma_f32_16x16x32_f16 v[64:67], v[44:47], v[84:87], v[64:67]\n\t" \
  "s_waitcnt lgkmcnt(7)\n\tv_mfma_f32_16x16x32_f16 v[36:39], v[20:23], v[68:71], 0\n\tv_mfma_f32_16x16x32_f16 v[40:43], v[20:23], v[72:75], 0\n\t" \
  "ds_read_b128 v[20:23], v32 offset:2048\n\ts_waitcnt lgkmcnt(7)\n\tv_mfma_f32_16x16x32_f16 v[56:59], v[148:151], v[0:3], v[56:59]\n\t" \
  "v_mfma_f32_16x16x32_f16 v[60:63], v[116:119], v[0:3], v[60:63]\n\tds_read_b128 v[0:3], v28 offset:2944\n\ts_waitcnt lgkmcnt(7)\n\t" \
  "v_mfma_f32_16x16x32_f16 v[56:59], v[152:155], v[4:7], v[56:59]\n\tv_cvt_pk_f16_f32 v36, v36, v37\n\tv_cvt_pk_f16_f32 v37, v38, v39\n\t" \
  "v_cvt_pk_f16_f32 v38, v40, v41\n\tv_cvt_pk_f16_f32 v39, v42, v43\n\tv_mfma_f32_16x16x32_f16 v[60:63], v[120:123], v[4:7], v[60:63]\n\t" \
  "ds_read_b128 v[4:7], v28 offset:3008\n\tv_mfma_f32_16x16x32_f16 v[56:59], v[36:39], v[88:91], v[56:59]\n\tv_mfma_f32_16x16x32_f16 v[60:63], v[36:39], v[92:95], v[60:63]\n\t" \
  "v_mfma_f32_16x16x32_f16 v[64:67], v[36:39], v[96:99], v[64:67]\n\tds_read_b32 v34, v30\n\ts_waitcnt lgkmcnt(5)\n\t" \
  "v_mfma_f32_16x16x32_f16 v[40:43], v[8:11], v[12:15], 0\n\tds_read_b64 v[8:9], v31 offset:544\n\tds_read_b64 v[10:11], v31 offset:712\n\t" \
  "ds_read_b64 v[12:13], v31 offset:512\n\tds_read_b64 v[14:15], v31 offset:680\n\ts_waitcnt lgkmcnt(8)\n\t" \
  "v_mfma_f32_16x16x32_f16 v[56:59], v[48:51], v[16:19], v[56:59]\n\ts_waitcnt lgkmcnt(7)\n\tv_mfma_f32_16x16x32_f16 v[44:47], v[16:19], v[20:23], 0\n\t" \
  "ds_read_b64 v[16:17], v31 offset:480\n\tds_read_b64 v[18:19], v31 offset:648\n\ts_waitcnt lgkmcnt(6)\n\t" \
  "v_cmp_gt_u32 vcc, 4, v34\n\ts_cbranch_vccz 2f\n\ts_mov_b32 s6, 0x10000\n\t" \
  "1:\n\ts_sleep 1\n\tds_read_b32 v34, v30\n\t" \
  "s_sub_u32 s6, s6, 1\n\ts_waitcnt lgkmcnt(0)\n\tv_cmp_gt_u32 vcc, 4, v34\n\t" \
  "s_cbranch_scc1 2f\n\ts_cbranch_vccnz 1b\n\t2:\n\t" \
  "ds_read_b128 v[20:23], v28 offset:3072\n\tv_mfma_f32_16x16x32_f16 v[56:59], v[156:159], v[0:3], v[56:59]\n\tv_mfma_f32_16x16x32_f16 v[60:63], v[124:127], v[0:3], v[60:63]\n\t" \
  "ds_read_b128 v[0:3], v28 offset:3136\n\tv_cvt_pk_f16_f32 v44, v44, v45\n\tv_cvt_pk_f16_f32 v45, v46, v47\n\t" \
  "v_mfma_f32_16x16x32_f16 v[56:59], v[160:163], v[4:7], v[56:59]\n\tv_cvt_pk_f16_f32 v46, v40, v41\n\tv_cvt_pk_f16_f32 v47, v42, v43\n\t" \
  "v_mfma_f32_16x16x32_f16 v[60:63], v[128:131], v[4:7], v[60:63]\n\tds_read_b128 v[4:7], v28 offset:2544\n\ts_waitcnt lgkmcnt(7)\n\t" \
  "v_mfma_f32_16x16x32_f16 v[56:59], v[44:47], v[8:11], v[56:59]\n\tds_read_b128 v[8:11], v28 offset:3200\n\ts_waitcnt lgkmcnt(6)\n\t" \
  "v_mfma_f32_16x16x32_f16 v[60:63], v[44:47], v[12:15], v[60:63]\n\tds_read_b128 v[12:15], v28 offset:3264\n\ts_waitcnt lgkmcnt(5)\n\t" \
  "v_mfma_f32_16x16x32_f16 v[64:67], v[44:47], v[16:19], v[64:67]\n\tds_read_b128 v[16:19], v28 offset:2528\n\ts_waitcnt lgkmcnt(5)\n\t" \
  "v_mfma_f32_16x16x32_f16 v[56:59], v[164:167], v[20:23], v[56:59]\n\tv_mfma_f32_16x16x32_f16 v[60:63], v[132:135], v[20:23], v[60:63]\n\tv_mfma_f32_16x16x32_f16 v[64:67], v[100:103], v[20:23], v[64:67]\n\t" \
  "ds_read_b128 v[20:23], v28 offset:3328\n\ts_waitcnt lgkmcnt(5)\n\tv_mfma_f32_16x16x32_f16 v[60:63], v[136:139], v[0:3], v[60:63]\n\t" \
  "v_mfma_f32_16x16x32_f16 v[64:67], v[104:107], v[0:3], v[64:67]\n\tds_read_b128 v[0:3], v28 offset:3392\n\ts_waitcnt lgkmcnt(5)\n\t" \
  "v_mfma_f32_16x16x32_f16 v[36:39], v[4:7], v[68:71], 0\n\tv_cvt_pk_bf16_f32 v56, v56, v57\n\tv_cvt_pk_bf16_f32 v57, v58, v59\n\t" \
  "s_cmp_lt_u32 %[w], 4\n\ts_cbranch_scc1 1f\n\ts_setprio 0\n\t" \
  "1:\n\tv_mfma_f32_16x16x32_f16 v[40:43], v[4:7], v[72:75], 0\n\tds_write_b64 v29, v[56:57] offset:2560\n\t" \
  "ds_write_b32 v25, v25 offset:320\n\tds_read_b128 v[4:7], v28 offset:1504\n\ts_waitcnt lgkmcnt(7)\n\t" \
  "v_mfma_f32_16x16x32_f16 v[60:63], v[140:143], v[8:11], v[60:63]\n\tv_cvt_pk_f16_f32 v36, v36, v37\n\tv_cvt_pk_f16_f32 v37, v38, v39\n\t" \
  "v_mfma_f32_16x16x32_f16 v[64:67], v[108:111], v[8:11], v[64:67]\n\tds_read_b128 v[8:11], v32 offset:3072\n\tv_cvt_pk_f16_f32 v38, v40, v41\n\t" \
  "v_cvt_pk_f16_f32 v39, v42, v43\n\ts_waitcnt lgkmcnt(7)\n\tv_mfma_f32_16x16x32_f16 v[60:63], v[144:147], v[12:15], v[60:63]\n\t" \
  "v_mfma_f32_16x16x32_f16 v[64:67], v[112:115], v[12:15], v[64:67]\n\tds_read_b128 v[12:15], v28 offset:1520\n\tv_mfma_f32_16x16x32_f16 v[60:63], v[36:39], v[76:79], v[60:63]\n\t" \
  "v_mfma_f32_16x16x32_f16 v[64:67], v[36:39], v[80:83], v[64:67]\n\ts_waitcnt lgkmcnt(7)\n\tv_mfma_f32_16x16x32_f16 v[44:47], v[16:19], v[68:71], 0\n\t" \
  "v_mfma_f32_16x16x32_f16 v[40:43], v[16:19], v[72:75], 0\n\tds_read_b128 v[16:19], v32 offset:2048\n\ts_waitcnt lgkmcnt(7)\n\t" \
  "v_mfma_f32_16x16x32_f16 v[60:63], v[148:151], v[20:23], v[60:63]\n\tv_mfma_f32_16x16x32_f16 v[64:67], v[116:119], v[20:23], v[64:67]\n\tds_read_b128 v[20:23], v28 offset:3456\n\t" \
  "s_waitcnt lgkmcnt(7)\n\tv_mfma_f32_16x16x32_f16 v[60:63], v[152:155], v[0:3], v[60:63]\n\tv_cvt_pk_f16_f32 v44, v44, v45\n\t" \
  "v_cvt_pk_f16_f32 v45, v46, v47\n\tv_cvt_pk_f16_f32 v46, v40, v41\n\tv_cvt_pk_f16_f32 v47, v42, v43\n\t" \
  "v_mfma_f32_16x16x32_f16 v[64:67], v[120:123], v[0:3], v[64:67]\n\tds_read_b128 v[0:3], v28 offset:3520\n\tv_mfma_f32_16x16x32_f16 v[60:63], v[44:47], v[88:91], v[60:63]\n\t" \
  "v_mfma_f32_16x16x32_f16 v[64:67], v[44:47], v[92:95], v[64:67]\n\ts_waitcnt lgkmcnt(4)\n\tv_mfma_f32_16x16x32_f16 v[40:43], v[4:7], v[8:11], 0\n\t" \
  "ds_read_b64 v[4:5], v31 offset:544\n\tds_read_b64 v[6:7], v31 offset:712\n\tds_read_b64 v[8:9], v31 offset:512\n\t" \
  "ds_read_b64 v[10:11], v31 offset:680\n\ts_waitcnt lgkmcnt(7)\n\tv_mfma_f32_16x16x32_f16 v[60:63], v[48:51], v[12:15], v[60:63]\n\t" \
  "s_waitcnt lgkmcnt(6)\n\tv_mfma_f32_16x16x32_f16 v[36:39], v[12:15], v[16:19], 0\n\tds_read_b128 v[12:15], v28 offset:3584\n\t" \
  "ds_read_b128 v[16:19], v28 offset:3648\n\ts_waitcnt lgkmcnt(7)\n\tv_mfma_f32_16x16x32_f16 v[60:63], v[156:159], v[20:23], v[60:63]\n\t" \
  "v_mfma_f32_16x16x32_f16 v[64:67], v[124:127], v[20:23], v[64:67]\n\tds_read_b128 v[20:23], v28 offset:3056\n\ts_waitcnt lgkmcnt(7)\n\t" \
  "v_mfma_f32_16x16x32_f16 v[60:63], v[160:163], v[0:3], v[60:63]\n\tv_cvt_pk_f16_f32 v36, v36, v37\n\tv_cvt_pk_f16_f32 v37, v38, v39\n\t" \
  "v_cvt_pk_f16_f32 v38, v40, v41\n\tv_cvt_pk_f16_f32 v39, v42, v43\n\tv_mfma_f32_16x16x32_f16 v[64:67], v[128:131], v[0:3], v[64:67]\n\t" \
  "ds_read_b128 v[0:3], v28 offset:3712\n\ts_waitcnt lgkmcnt(6)\n\tv_mfma_f32_16x16x32_f16 v[60:63], v[36:39], v[4:7], v[60:63]\n\t" \
  "ds_read_b128 v[4:7], v28 offset:3776\n\ts_waitcnt lgkmcnt(5)\n\tv_mfma_f32_16x16x32_f16 v[64:67], v[36:39], v[8:11], v[64:67]\n\t" \
  "ds_read_b128 v[8:11], v28 offset:3040\n\ts_waitcnt lgkmcnt(5)\n\tv_mfma_f32_16x16x32_f16 v[60:63], v[164:167], v[12:15], v[60:63]\n\t" \
  "v_mfma_f32_16x16x32_f16 v[64:67], v[132:135], v[12:15], v[64:67]\n\tds_read_b128 v[12:15], v28 offset:3840\n\ts_waitcnt lgkmcnt(5)\n\t" \
  "v_mfma_f32_16x16x32_f16 v[64:67], v[136:139], v[16:19], v[64:67]\n\tds_read_b128 v[16:19], v28 offset:3904\n\ts_waitcnt lgkmcnt(5)\n\t" \
  "v_mfma_f32_16x16x32_f16 v[44:47], v[20:23], v[68:71], 0\n\tv_mfma_f32_16x16x32_f16 v[40:43], v[20:23], v[72:75], 0\n\tds_read_b128 v[20:23], v28 offset:2016\n\t" \
  "v_cvt_pk_bf16_f32 v60, v60, v61\n\tv_cvt_pk_bf16_f32 v61, v62, v63\n\ts_waitcnt lgkmcnt(5)\n\t" \
  "v_mfma_f32_16x16x32_f16 v[64:67], v[140:143], v[0:3], v[64:67]\n\tds_write_b64 v29, v[60:61] offset:3072\n\tds_write_b32 v25, v25 offset:384\n\t" \
  "ds_read_b128 v[0:3], v32 offset:3072\n\tv_cvt_pk_f16_f32 v44, v44, v45\n\tv_cvt_pk_f16_f32 v45, v46, v47\n\t" \
  "v_cvt_pk_f16_f32 v46, v40, v41\n\tv_cvt_pk_f16_f32 v47, v42, v43\n\ts_waitcnt lgkmcnt(7)\n\t" \
  "v_mfma_f32_16x16x32_f16 v[64:67], v[144:147], v[4:7], v[64:67]\n\tds_read_b128 v[4:7], v28 offset:2032\n\tv_mfma_f32_16x16x32_f16 v[64:67], v[44:47], v[76:79], v[64:67]\n\t" \
  "s_waitcnt lgkmcnt(7)\n\tv_mfma_f32_16x16x32_f16 v[36:39], v[8:11], v[68:71], 0\n\tv_mfma_f32_16x16x32_f16 v[40:43], v[8:11], v[72:75], 0\n\t" \
  "ds_read_b128 v[8:11], v32 offset:2048\n\ts_waitcnt lgkmcnt(7)\n\tv_mfma_f32_16x16x32_f16 v[64:67], v[148:151], v[12:15], v[64:67]\n\t" \
  "ds_read_b128 v[12:15], v28 offset:3968\n\ts_waitcnt lgkmcnt(7)\n\tv_mfma_f32_16x16x32_f16 v[64:67], v[152:155], v[16:19], v[64:67]\n\t" \
  "ds_read_b128 v[16:19], v28 offset:4032\n\tv_cvt_pk_f16_f32 v36, v36, v37\n\tv_cvt_pk_f16_f32 v37, v38, v39\n\t" \
  "v_cvt_pk_f16_f32 v38, v40, v41\n\tv_cvt_pk_f16_f32 v39, v42, v43\n\ts_nop 1\n\t" \
  "v_mfma_f32_16x16x32_f16 v[64:67], v[36:39], v[88:91], v[64:67]\n\ts_waitcnt lgkmcnt(4)\n\tv_mfma_f32_16x16x32_f16 v[40:43], v[20:23], v[0:3], 0\n\t" \
  "ds_read_b64 v[0:1], v31 offset:544\n\tds_read_b64 v[2:3], v31 offset:712\n\tds_read_b128 v[20:23], v28 offset:4096\n\t" \
  "s_waitcnt lgkmcnt(6)\n\tv_mfma_f32_16x16x32_f16 v[64:67], v[48:51], v[4:7], v[64:67]\n\ts_waitcnt lgkmcnt(5)\n\t" \
  "v_mfma_f32_16x16x32_f16 v[44:47], v[4:7], v[8:11], 0\n\ts_waitcnt lgkmcnt(4)\n\tv_mfma_f32_16x16x32_f16 v[64:67], v[156:159], v[12:15], v[64:67]\n\t" \
  "s_waitcnt lgkmcnt(3)\n\tv_mfma_f32_16x16x32_f16 v[64:67], v[160:163], v[16:19], v[64:67]\n\ts_nop 3\n\t" \
  "v_cvt_pk_f16_f32 v44, v44, v45\n\tv_cvt_pk_f16_f32 v45, v46, v47\n\tv_cvt_pk_f16_f32 v46, v40, v41\n\t" \
  "v_cvt_pk_f16_f32 v47, v42, v43\n\ts_waitcnt lgkmcnt(1)\n\ts_nop 0\n\t" \
  "v_mfma_f32_16x16x32_f16 v[64:67], v[44:47], v[0:3], v[64:67]\n\ts_waitcnt lgkmcnt(0)\n\tv_mfma_f32_16x16x32_f16 v[64:67], v[164:167], v[20:23], v[64:67]\n\t" \
  "s_nop 7\n\tv_cvt_pk_bf16_f32 v64, v64, v65\n\tv_cvt_pk_bf16_f32 v65, v66, v67\n\t" \
  "ds_write_b64 v29, v[64:65] offset:3584\n\tds_write_b32 v25, v25 offset:448\n\t"
#define CONV_CLOBBER "v0", "v1", "v2", "v3", "v4", "v5", "v6", "v7", "v8", "v9", "v10", "v11", "v12", "v13", "v14", "v15", "v16", "v17", "v18", "v19", "v20", "v21", "v22", "v23", "v36", "v37", "v38", "v39", "v40", "v41", "v42", "v43", "v44", "v45", "v46", "v47", "v52", "v53", "v54", "v55", "v56", "v57", "v58", "v59", "v60", "v61", "v62", "v63", "v64", "v65", "v66", "v67", "v68", "v69", "v70", "v71", "v72", "v73", "v74", "v75", "v76", "v77", "v78", "v79", "v80", "v81", "v82", "v83", "v84", "v85", "v86", "v87", "v88", "v89", "v90", "v91", "v92", "v93", "v94", "v95", "v96", "v97", "v98", "v99", "v100", "v101", "v102", "v103", "v104", "v105", "v106", "v107", "v108", "v109", "v110", "v111", "v112", "v113", "v114", "v115", "v116", "v117", "v118", "v119", "v120", "v121", "v122", "v123", "v124", "v125", "v126", "v127", "v128", "v129", "v130", "v131", "v132", "v133", "v134", "v135", "v136", "v137", "v138", "v139", "v140", "v141", "v142", "v143", "v144", "v145", "v146", "v147", "v148", "v149", "v150", "v151", "v152", "v153", "v154", "v155", "v156", "v157", "v158", "v159", "v160", "v161", "v162", "v163", "v164", "v165", "v166", "v167"
#define G_CLOBBER "v8", "v9", "v10", "v11", "v12", "v13", "v14", "v15", "v16", "v17", "v18", "v19", "v20", "v21", "v22", "v23", "v24", "v25", "v26", "v27", "v28", "v29", "v30", "v32", "v33", "v34", "v35", "v36", "v37", "v38", "v39", "v40", "v41", "v42", "v43", "v44", "v45", "v46", "v47", "v48", "v49", "v50", "v51", "v52", "v53", "v54", "v55", "v56", "v57", "v58", "v59", "v60", "v61", "v62", "v63", "v64", "v65", "v66", "v67", "v68", "v69", "v70", "v71", "v72", "v73", "v74", "v75", "v76", "v77", "v78", "v79", "v80", "v81", "v82", "v83", "v84", "v85", "v86", "v87", "v88", "v89", "v90", "v91", "v92", "v93", "v94", "v95", "v96", "v97", "v98", "v99", "v100", "v101", "v102", "v103", "v104", "v105", "v106", "v107", "v108", "v109", "v110", "v111", "v112", "v113", "v114", "v115", "v116", "v117", "v118", "v119", "v120", "v121", "v122", "v123", "v124", "v125", "v126", "v127"
#define D_CLOBBER "v8", "v9", "v10", "v11", "v12", "v13", "v14", "v15", "v16", "v17", "v18", "v19", "v20", "v21", "v22", "v23", "v24", "v25", "v26", "v27", "v28", "v29", "v30", "v31"
__device__ __forceinline__ float fmav(float a, float b, float c) {
  float r;
  asm("v_fma_f32 %0, %1, %2, %3" : "=v"(r) : "v"(a), "v"(b), "v"(c));
  return r;
}
__device__ __forceinline__ float fmas(float a, u32 s, float c) {
  asm("v_fmac_f32 %0, %2, %1" : "+v"(c) : "v"(a), "s"(s));
  return c;
}
#define RANK8(d0, d1, d2, d3, c0, c1, i0, f0, f1) "v_perm_b32 " d0 ", " d0 ", " d1 ", %[sg]\n\tv_perm_b32 " d2 ", " d2 ", " d3 ", %[sg]\n\t" \
  "v_bcnt_u32_b32 " d0 ", " d0 ", " i0 "\n\tv_bcnt_u32_b32 " c0 ", " d2 ", " d0 "\n\tv_bcnt_u32_b32 " c1 ", " d2 ", " d0 "\n\t" f0 "\n\t" \
  "v_permlane16_swap_b32 " c0 ", " c1 "\n\tv_add_u32 " d0 ", " c0 ", " c1 "\n\tv_add_u32 " d1 ", " c0 ", " c1 "\n\t" f1 "\n\t" \
  "v_permlane32_swap_b32 " d0 ", " d1 "\n\tv_add_u32 " c0 ", " d0 ", " d1 "\n\t"
__device__ __forceinline__ void ln_rows(u32x4 (&xv)[4], u32x8 wv, rsrc_t rs, u32& tag, int b, int w, int l, u32 st, u32 sg TLARG) {
  float s[2][3] = {}, c[2], sw = 0.f;
  u32 os = l < 16 ? O_SG + 128 * (b - 128) + 8 * l : 0x80000000u;
  u32 ot = l < 16 ? O_TOP + 8 * (b & 15) + TL * ((b >> 4) & 7) : l < 32 ? O_RG - 128 + 128 * (b - 128) + 8 * l : 0x80000000u, om = l < 16 ? TOPS / 8 : 0u, fm = l < 16 ? 0xff0u : 0u;
  u32 aw = (l & 16) ? st + 8 * w + (l >> 5) * 4 : st + 136, ac = st + 128, ar = st + 4 * (l & 15), aq = st + (l & 48), one = 1u;
  u32 kl = (2047 - (16 * (b - 128) + 2 * w + (l >> 5))) << 1;
  asm volatile("" : "+v"(os), "+v"(aw), "+v"(ac), "+v"(ar), "+v"(aq), "+v"(one), "+v"(kl), "+v"(ot), "+v"(om), "+v"(fm));
  SB();
  asm volatile("s_waitcnt vmcnt(4) lgkmcnt(0)" : "+{s44}"(tag)::"memory");
  asm volatile("" : "+{v[116:123]}"(wv));
  tag += 1u;
  *(volatile LDS u32*)(size_t)(st - 104) = tag;
  STAMP(12);
#pragma unroll
  for (int i = 0; i < 8; i++) sw = dot2b(wv[i], 0x3f803f80u, sw);
  sw += dppf<0x121, 0xf>(sw); sw += dppf<0x122, 0xf>(sw); sw += dppf<0x124, 0xf>(sw); sw += dppf<0x128, 0xf>(sw);
  sw = (rdl(sw, 0) + rdl(sw, 16)) + (rdl(sw, 32) + rdl(sw, 48));
  asm volatile("" : "+v"(sw));
  SB();
  float cv = 0.f;
#pragma unroll
  for (int r = 0; r < 2; r++) {
#pragma unroll
    for (int i = 0; i < 8; i++) {
      if (r == 0 && i == 0) { asm volatile("s_waitcnt vmcnt(3)" : "+{v[100:103]}"(xv[0])); STAMP(13); }
      if (r == 0 && i == 4) asm volatile("s_waitcnt vmcnt(2)" : "+{v[104:107]}"(xv[1]));
      if (r == 1 && i == 0) asm volatile("s_waitcnt vmcnt(1)" : "+{v[108:111]}"(xv[2]));
      if (r == 1 && i == 4) asm volatile("s_waitcnt vmcnt(0)" : "+{v[112:115]}"(xv[3]));
      const u32 a = xv[2 * r + (i >> 2)][i & 3];
      s[r][0] = dot2b(a, 0x3f803f80u, s[r][0]); s[r][1] = dot2b(a, a, s[r][1]); s[r][2] = dot2b(a, wv[i], s[r][2]);
      if (i == 3) {
        c[r] = rdl(s[r][0] * 0.125f, 0);
        if (r) { cv = l < 32 ? c[0] : c[1]; asm volatile("" : "+v"(cv)); }
        SB();
      }
    }
    const float ml = s[r][0] * 0.0625f, dl = __builtin_fmaf(s[r][0], 0.25f, -4.f * c[r]);
    s[r][1] = __builtin_fmaf(dl, dl, __builtin_fmaf(-s[r][0], ml, s[r][1]));
    SB();
  }
#pragma unroll
  for (int k = 0; k < 3; k++) swap32(s[0][k ? 3 - k : 0], s[1][k ? 3 - k : 0]);
  float q[3];
#pragma unroll
  for (int k = 0; k < 3; k++) q[k] = s[0][k] + s[1][k];
#define RED3(m) "v_add_f32_dpp %0, %0, %0 " m " bank_mask:0xf\n\tv_add_f32_dpp %1, %1, %1 " m " bank_mask:0xf\n\tv_add_f32_dpp %2, %2, %2 " m " bank_mask:0xf\n\t"
  asm volatile("s_nop 1\n\t" RED3("row_ror:1 row_mask:0xf") RED3("row_ror:2 row_mask:0xf") RED3("row_ror:4 row_mask:0xf") RED3("row_ror:8 row_mask:0xf")
               RED3("row_bcast:15 row_mask:0xa") : "+v"(q[0]), "+v"(q[1]), "+v"(q[2]));
  const float mu = q[0] * (1.f / D), dv = mu - cv;
  const float var = fmaxf(__builtin_fmaf(-dv, dv, q[1] * (1.f / D)), 0.f);
  const float m = rb(mu);
  float sd = __builtin_amdgcn_sqrtf(rb(rb(var) + 1e-5f)), num = __builtin_fmaf(-m, sw, q[2]);
  asm volatile("s_nop 0\n\tv_cvt_pk_bf16_f32 %0, 0, %0" : "+v"(sd), "+v"(num));
  u32 rv = __float_as_uint(rb(num * __builtin_amdgcn_rcpf(sd)));
  rv ^= (u32)((int)rv >> 15) | 0x80000000u;
  rv = __builtin_amdgcn_perm(rv, kl, 0x07060100u) >> 1;
  asm volatile("ds_write2_b32 %[aw], %[sv], %[rv] offset1:16\n\ts_mov_b64 exec, 1\n\tds_add_rtn_u32 v104, %[ac], %[one]\n\ts_mov_b64 exec, -1\n\t"
               "ds_read_b32 v106, %[ar]\n\tds_read_b32 v105, %[ar] offset:64\n\tds_read_b128 v[100:103], %[aq] offset:64\n\tv_mov_b32 v107, %[t]\n\tv_mov_b32 v109, %[t]\n\t"
               "s_waitcnt lgkmcnt(3)\n\tv_cmp_eq_u32 vcc, 7, v104\n\ts_bitcmp1_b32 vcc_lo, 0\n\ts_cbranch_scc0 1f\n\t"
               "s_waitcnt lgkmcnt(2)\n\tbuffer_store_dwordx2 v[106:107], %[os], %[rs], 0 offen sc1\n\ts_waitcnt lgkmcnt(0)\n\t"
               "v_sub_u32 v100, v105, v100\n\tv_sub_u32 v101, v105, v101\n\tv_sub_u32 v102, v105, v102\n\tv_sub_u32 v103, v105, v103\n\t"
               RANK8("v100", "v101", "v102", "v103", "v104", "v103", "0", "s_nop 1", "s_nop 1") "v_cmp_eq_u32 vcc, 16, v104\n\t"
               "v_sub_u32 v108, 19, v104\n\ts_ff1_i32_b32 s6, vcc_lo\n\tv_readlane_b32 s6, v105, s6\n\t"
               "v_mad_u32_u24 v104, v104, %[om], %[ot]\n\ts_nop 0\n\tv_lshrrev_b32 v108, v108, s6\n\tv_bfi_b32 v108, %[fm], v108, v105\n\t"
               "buffer_store_dwordx2 v[108:109], v104, %[rs], 0 offen sc1\n1:\n\ts_waitcnt lgkmcnt(0)"
               :: [aw] "v"(aw), [sv] "v"(__builtin_amdgcn_perm(__float_as_uint(sd), __float_as_uint(m), 0x07060302u)), [rv] "v"(rv), [ac] "v"(ac), [one] "v"(one),
                  [ar] "v"(ar), [aq] "v"(aq), [os] "v"(os), [om] "v"(om), [ot] "v"(ot), [fm] "v"(fm), [sg] "s"(sg), [t] "s"(tag), [rs] "s"(rs)
               : "vcc", "scc", "s6", "v100", "v101", "v102", "v103", "v104", "v105", "v106", "v107", "v108", "v109", "memory");
  SB();
}

#define GL __attribute__((address_space(1)))
typedef const GL u16* gp;

__global__ __launch_bounds__(768, 3) void fused(const u16*, const u16*, const u16*, const u16*, const u16*, const u16*, const u16*, const u16*,
    const u16*, const u16*, const u16*, u16*, char*) {
  __shared__ __attribute__((aligned(256))) char smem[SMEM];
  const u32 lds0 = (u32)(size_t)(LDS char*)smem;
  int w0;
#define GLD(r0, r1, r2, r3, o_) "global_load_dwordx4 v[" #r0 ":" #r1 "], " #o_ ", s[16:17]\n\tglobal_load_dwordx4 v[" #r2 ":" #r3 "], " #o_ ", s[16:17] offset:2048\n\t"
  asm volatile(
      "s_load_dwordx16 s[16:31], s[0:1], 0\n\ts_load_dwordx8 s[32:39], s[0:1], 64\n\ts_load_dwordx2 s[40:41], s[0:1], 96\n\t"
      "s_memrealtime s[46:47]\n\t"
      "s_getpc_b64 s[4:5]\n2:\n\ts_lshr_b32 s7, s2, 3\n\ts_and_b32 s10, s4, 0xffffff00\n\ts_mov_b32 s11, s5\n\ts_mul_i32 s8, s7, 0x600\n\t"
      "v_lshl_add_u32 v3, v0, 1, s8\n\ts_cmpk_lt_u32 s2, %[pn]\n\ts_cbranch_scc0 1f\n\t"
      "global_load_ushort v163, v3, s[10:11] offset:%[pf]\n"
      "1:\n\ts_cmpk_lt_u32 s2, 0x80\n\tv_readfirstlane_b32 s3, v0\n\tv_and_b32 v1, 63, v0\n\tv_mov_b64 v[4:5], 0\n\tv_mov_b64 v[6:7], 0\n\tv_lshlrev_b32 v2, 4, v1\n\tv_lshlrev_b32 v8, 2, v0\n\t"
      "s_mov_b32 s42, %[we]\n\ts_mov_b32 s43, 0x20000\n\ts_cbranch_scc0 3f\n\t"
      "s_lshr_b32 s3, s3, 6\n\ts_mul_i32 s9, s3, %[xr]\n\ts_min_u32 s9, s9, %[zm]\n\tv_add_u32 v3, s9, v2\n\tv_add_u32 v3, %[zo], v3\n\t"
      "s_and_b32 s7, s7, 7\n\ts_and_b32 s6, s2, 7\n\ts_and_b32 s8, s2, 64\n\ts_lshl3_add_u32 s8, s6, s8\n\ts_add_u32 s45, s8, s7\n\t"
      "s_add_u32 s4, s4, lane_tab@rel32@lo+(6f-2b-4)\n6:\n\ts_addc_u32 s5, s5, lane_tab@rel32@hi+(6b-2b+4)\n\t"
      "ds_write_b128 v3, v[4:7]\n\ts_cmp_lt_u32 s3, 8\n\ts_cbranch_scc1 5f\n\t"
      "s_lshl_b32 s8, s45, 4\n\ts_lshl_b32 s9, s3, 18\n\ts_add_u32 s9, s9, s8\n\ts_bitset0_b32 s9, 21\n\tv_lshl_add_u32 v148, v1, 12, s9\n\t"
      "s_lshl3_add_u32 s9, s3, s7\n\ts_lshl_b32 s9, s9, 17\n\ts_add_u32 s9, s9, s8\n\ts_bitset0_b32 s9, 23\n\tv_lshl_add_u32 v9, v1, 11, s9\n\t"
      "v_add_u32 v11, 0x100000, v148\n\tv_add_u32 v12, 0x200000, v148\n\t"
      "v_lshlrev_b32 v149, 4, v0\n\ts_lshl_b32 s9, s3, 10\n\ts_add_u32 m0, s9, %[ob]\n\tv_add_u32 v10, 0xffffe800, v149\n\tglobal_load_lds_dwordx4 v10, s[4:5]\n\t"
      "v_add_u32 v149, %[sg], v149\n\t"
      "s_waitcnt lgkmcnt(0)\n\ts_load_dword s44, s[40:41], 0\n\t"
      "s_barrier\n\t"
      "global_load_dword v160, v9, s[16:17]" PRENT "\n\t"
      GLD(100, 103, 104, 107, v148)
      GLD(108, 111, 112, 115, v11)
      "s_cmp_eq_u32 s6, 3\n\ts_cbranch_scc1 .Lrole_p\n\t"
      GLD(116, 119, 120, 123, v12)
      "s_branch .Lrole_p\n"
      "3:\n\ts_lshr_b32 s3, s3, 6\n\ts_cmp_lt_u32 s3, 8\n\ts_cbranch_scc1 4f\n\ts_barrier\n\ts_branch .Lrole_d\n4:\n\t"
      "v_add_u32 v3, %[fc], v2\n\ts_add_u32 s8, s3, s2\n\ts_and_b32 s45, s8, 7\n\ts_lshl3_add_u32 s8, s2, s45\n\t"
      "s_lshl_b32 s8, s8, 12\n\ts_bitset0_b32 s8, 22\n\tds_write_b128 v3, v[4:7]\n\tv_add_u32 v3, s8, v2\n\t"
      "s_waitcnt lgkmcnt(0)\n\tglobal_load_dwordx4 v[116:119], v2, s[30:31]\n\tglobal_load_dwordx4 v[120:123], v2, s[30:31] offset:1024\n\t"
      "s_load_dword s44, s[40:41], 0\n\ts_barrier\n\t"
      "global_load_dwordx4 v[100:103], v3, s[16:17]" LNNT "\n\tglobal_load_dwordx4 v[104:107], v3, s[16:17] offset:1024" LNNT "\n\t"
      "global_load_dwordx4 v[108:111], v3, s[16:17] offset:2048" LNNT "\n\tglobal_load_dwordx4 v[112:115], v3, s[16:17] offset:3072" LNNT "\n\t"
      "s_branch .Lrole_l\n"
      "5:\n\t"
      "global_load_dwordx4 v[16:19], v2, s[4:5]\n\tglobal_load_dwordx4 v[20:23], v2, s[4:5] offset:1024\n\t"
      "s_lshl3_add_u32 s8, s45, s3\n\ts_lshl_b32 s9, s8, 1\n\tv_mov_b32 v9, s9\n\ts_lshl_b32 s9, s45, 11\n\tv_add_u32 v3, s9, v8\n\t"
      "s_waitcnt lgkmcnt(0)\n\tglobal_load_short_d16_hi v24, v9, s[26:27]\n\tglobal_load_ushort v160, v9, s[28:29]\n\t"
      "global_load_dword v96, v3, s[18:19]" PNT "\n\tglobal_load_dword v97, v3, s[20:21]" PNT "\n\t"
      "global_load_dword v68, v3, s[22:23]" PNT "\n\tglobal_load_dword v69, v3, s[24:25]" PNT "\n\ts_barrier\n\ts_branch .Lrole_c"
      : "={s3}"(w0)
      : "{s[0:1]}"(__builtin_amdgcn_kernarg_segment_ptr()), "{s2}"(blockIdx.x), "{v0}"(threadIdx.x), [pf] "n"(PFO), [pn] "n"(8 * PFN), [we] "n"(WS_END), [xr] "n"(XREG),
        [zm] "n"(8 * XREG), [zo] "n"(OX - 16), [sg] "n"(O_SG - 8192), [fc] "n"(FC), [ob] "n"(OB - 8192)
      : "memory");
#define TLREGS()
#define ROLE(name, text, ...) \
  gp x, wg, wu, wd; GL u16* out; rsrc_t rs; int b, w, t, l, g; u32 tag, st0; \
  asm volatile(".Lrole_" #name ":" : "={v0}"(t), "={v1}"(l) __VA_ARGS__ :: "memory"); \
  asm volatile(text : "={s2}"(b), "={s3}"(w), "={s[16:17]}"(x), "={s[32:33]}"(wg), "={s[34:35]}"(wu), "={s[36:37]}"(wd), \
               "={s[38:39]}"(out), "={s[40:43]}"(rs), "={s44}"(tag), "={s45}"(g), "={s46}"(st0) :: "memory"); \
  SB(); \
  __builtin_assume((u32)l < 64u); __builtin_assume((u32)t < 768u); __builtin_assume((u32)w < 12u); __builtin_assume((u32)b < 256u); __builtin_assume((u32)g < 128u); \
  TLREGS(); \
  const int xk = b & 7, j = (b >> 3) & 15, n16 = l & 15, kg = l >> 4
#define FLAG(o, v) do { asm volatile("" ::: "memory"); SB(); *(volatile LDS u32*)(LDS char*)(smem + (o)) = (v); SB(); } while (0)
#define CWAIT(o, n, nap) do { _Pragma("unroll 1") for (int spin = 0; spin < SPIN; spin++) { \
      if (__builtin_amdgcn_readfirstlane((int)*(const volatile LDS u32*)(LDS char*)(smem + (o))) >= (int)(n)) break; \
      asm volatile(nap ::: "memory"); } SB(); asm volatile("" ::: "memory"); } while (0)
#define AWAIT(o, n, nap) do { _Pragma("unroll 1") for (int spin = 0; spin < SPIN; spin++) { \
      if (__all(*(const volatile LDS u32*)(LDS char*)(smem + (o)) >= (u32)(n))) break; \
      asm volatile(nap ::: "memory"); } SB(); asm volatile("" ::: "memory"); } while (0)
#define POLL8(res_, off_) do { u32 n_ = SPIN; \
    asm volatile("1:\n\tbuffer_load_dwordx2 v[4:5], %2, %3, 0 offen sc1\n\ts_sub_u32 %1, %1, 1\n\ts_waitcnt vmcnt(0)\n\t" \
                 "v_cmp_ne_u32 vcc, %4, v5\n\ts_cbranch_scc1 2f\n\ts_cbranch_vccnz 1b\n2:" \
                 : "=&{v[4:5]}"(res_), "+s"(n_) : "v"(off_), "s"(rs), "s"(tag) : "vcc", "scc", "memory"); } while (0)
#define UNTIL(t_) "2:\n\ts_sleep 1\n\ts_memrealtime s[8:9]\n\ts_waitcnt lgkmcnt(0)\n\ts_sub_u32 s8, s8, %[st]\n\ts_cmp_lt_u32 s8, " t_ "\n\ts_cbranch_scc1 2b\n\t"
#define IXO(l_) (O_IDX + (2 * xk + (j & 1)) * 128 + ((l_) & 7) * 8)
#define IDS() const int id = 16 * xk + j, sb = id * 57 >> 10, mat = id & 1, u = (id - 18 * sb) >> 1; \
    const bool slow = u >= 6; \
    const int part = slow ? u - 6 : u & 1, cb = 4 * sb + (slow ? 1 : (u >> 1) + (u > 1)), r0 = part * (slow ? 320 : 512), ns = slow ? 5 + (u >> 3) : 8; \
    const int kp = 4 * sb + ((u >> 1) & 3), ct = 2 * (u & 1) + mat
#define TS(a, b_)
#define TSC
#define DONE "s_endpgm\n"
#define TSD
#define TSGET(i0, i1, i2, i3)
  if (blockIdx.x >= 128) {
    if (w0 >= 8) {
      ROLE(d, "s_waitcnt lgkmcnt(0)");
      IDS();
      const int dw = w - 8;
      const u32 sw = (((n16 >> 1) ^ (kg & 1)) << 5) + ((l & 1) << 4);
      gp base = (mat ? wu : wg) + (size_t)(r0 + 4 * dw) * F + cb * 128;
      u32 v = kg * (2 * F) + sw, fa = lds0 + FC + 4 * dw, vp = 96 + dw, t_;
      asm volatile("" : "+v"(v), "+v"(fa), "+v"(vp));
      asm volatile("s_mov_b32 m0, %0" ::"s"(lds0 + 1024 * dw));
#define VMC "s_getreg_b32 s6, hwreg(HW_REG_IB_STS, 0, 4)\n\t"
#define RUN(k, q, lim, v_, b_, st_) asm volatile("1:\n\ts_lshr_b32 s6, m0, 4\n\ts_and_b32 s6, s6, 0xc0\n\tv_xor_b32 %[t], s6, %[v]\n\ts_waitcnt vmcnt(%[c])\n\t" VMC \
        "global_load_lds_dwordx4 %[t], %[b] nt\n\ts_add_u32 m0, m0, %[m]\n\tv_add_u32 %[v], %[d], %[v]\n\t.if %[m] == 3072\n\tv_mad_i32_i24 %[t], s6, -3, %[p]\n\tv_add_u32 %[p], 3, %[p]\n\t" \
        "s_mov_b64 exec, 1\n\tds_max_u32 %[a], %[t]\n\ts_mov_b64 exec, -1\n\t.endif\n\ts_cmp_lt_u32 m0, %[l]\n\ts_cbranch_scc1 1b" \
        : [v] "+v"(v_), [p] "+v"(vp), [t] "=&v"(t_) : [b] "s"(b_), [c] "n"((q) - 1), [m] "n"(1024 * (k)), [d] "n"((k) * (st_)), \
          [a] "v"(fa), [l] "s"(lim) : "scc", "s6", "memory")
#define MARK(c, val) "s_waitcnt vmcnt(" #c ")\n\t" val "\n\ts_mov_b64 exec, 1\n\tds_max_u32 %[a], %[t]\n\ts_mov_b64 exec, -1\n\t"
      __builtin_amdgcn_s_setprio(DPRIO);
      STAMP(8);
      if (id < 126) { if (slow) RUN(4, QAS, 32768, v, base, 8 * F); else RUN(4, 8, 32768, v, base, 8 * F); }
      if (dw < 3) {
        if (id >= 126) __builtin_amdgcn_endpgm();
        gp bd = wd + (size_t)(128 * kp + 2 * dw) * D + ct * 256;
        u32 vd = (l >> 5) * (2 * D + 32) ^ 16 * (l & 31);
        asm volatile("" : "+v"(vd));
        if (slow) RUN(3, QS, 16384 * ns, v, base, 8 * F);
        else { RUN(3, 8, 32768 + 3072 * RAMP, v, base, 8 * F); RUN(3, QD, 131072, v, base, 8 * F); }
        STAMP(1);
        {
          u32 n_ = SPIN;
          asm volatile("1:\n\t" VMC MARK(63, "v_mad_i32_i24 %[t], s6, -3, %[p]") "ds_read_b32 %[t], %[f]\n\ts_sub_u32 %[n], %[n], 1\n\ts_waitcnt lgkmcnt(0)\n\t"
                       "v_cmp_lt_u32 vcc, %[k], %[t]\n\ts_cbranch_scc1 2f\n\ts_cbranch_vccz 1b\n2:\n\ts_mov_b32 m0, %[m]"
                       : [t] "=&v"(t_), [n] "+s"(n_) : [a] "v"(fa), [p] "v"(vp), [f] "v"(u == 8 ? fa : lds0 + FC + 20), [k] "s"(u == 8 ? 159 : 16 * HOLD - 105), [m] "s"(lds0 + 1024 * dw)
                       : "vcc", "scc", "s6", "memory");
        }
        STAMP(3);
        if (u < 8) {
          RUN(3, QE, 65536, vd, bd, 4 * D);
          STAMP(6);
          asm volatile(MARK(0, "v_mov_b32 %[t], -1") : [t] "=&v"(t_) : [a] "v"(fa) : "memory");
          STAMP(4);
        }
        FLUSH();
        __builtin_amdgcn_endpgm();
      }
      asm volatile(MARK(0, "v_mov_b32 %[t], -1") : [t] "=&v"(t_) : [a] "v"(fa) : "memory");
      u32 km, rk;
      u32 hi, lo, k2, k3, gd, thr, sg = 0x0c0c0b09u;
      u32 ka = lds0 + FC + 384 + 8 * (l >> 3), kc = 2047 * 2048 + 2 * r0 + 16 * ((l & 7) ^ ((l >> 2) & 4)), kq = lds0 + FC + 512 + (l & 48), kr = lds0 + FC + 512 + 4 * n16;
      u32 c1 = 896, c2 = 1792, c3 = 2688, cm = (u32)-2048, cs = O_SG + 8 * 2047, ca = lds0 + FC + 512;
      rsrc_t rx = __builtin_amdgcn_make_buffer_rsrc((void*)(x - 1344), 0, 2 * L * D + 4096, 0x00020000);
      u32 co = O_CLR + 8 * (18 * sb + (0x84c0 >> 4 * (kp & 3) & 15) + min(l, (kp & 3) == 1 ? 5 : 3));
      asm volatile("" : "+v"(ka), "+v"(kc), "+v"(kq), "+v"(kr), "+v"(co));
      asm volatile("" : "+s"(c1), "+s"(c2), "+s"(c3), "+s"(cm), "+s"(cs), "+s"(ca), "+s"(sg), "+s"(rx));
      *(u32x4*)(smem + FA + 6144 + 16 * l) = u32x4{0u, 0u, 0u, 0u};
      *(u32x4*)(smem + FA + 7168 + 16 * l) = u32x4{0u, 0u, 0u, 0u};
      CWAIT(FC + 256, 1, "s_sleep 4");
      tag = (u32)__builtin_amdgcn_readfirstlane((int)*(const volatile LDS u32*)(LDS char*)(smem + FC + 24));
      {
        u32 n_ = SPIN, t0, t1, t2, t3, se;
#define TLX(a, b_, c, d, sc) "buffer_load_dwordx4 v[" #a ":" #b_ "], %[o], %[r], 0 offen" sc "\n\tbuffer_load_dwordx4 v[" #c ":" #d "], %[o], %[r], %[ts] offen" sc "\n\t"
#define TLD(a, b_, c, d) TLX(a, b_, c, d, " sc1")
#define TCK(r0, r1, r2, r3, r4, r5, r6, r7) "s_waitcnt vmcnt(4)\n\tv_xor_b32 %[t0], %[t], v" #r1 "\n\tv_bitop3_b32 %[t0], %[t0], %[t], v" #r3 " bitop3:0xf6\n\t" \
          "v_bitop3_b32 %[t0], %[t0], %[t], v" #r5 " bitop3:0xf6\n\tv_bitop3_b32 %[t0], %[t0], %[t], v" #r7 " bitop3:0xf6\n\t" \
          "v_cmp_ne_u32 vcc, 0, %[t0]\n\tv_mov_b32 %[hi], v" #r0 "\n\tv_mov_b32 %[lo], v" #r2 "\n\tv_mov_b32 %[k2], v" #r4 "\n\tv_mov_b32 %[k3], v" #r6 "\n\t" \
          "s_cbranch_vccz 5f\n\t" TLD(r0, r3, r4, r7)
        asm volatile("2:\n\ts_sleep 1\n\ts_memrealtime s[8:9]\n\ts_waitcnt lgkmcnt(0)\n\ts_sub_u32 s8, s8, %[st]\n\ts_cmp_lt_u32 s8, %[tw]\n\ts_cbranch_scc1 2b\n\t"
                     TLX(128, 131, 132, 135, SCT) "s_sleep " STR(RSL) "\n\t" TLD(136, 139, 140, 143) "s_sleep " STR(RSL) "\n\t" TLX(144, 147, 148, 151, SCU)
                     "1:\n\t" TCK(128, 129, 130, 131, 132, 133, 134, 135) TCK(136, 137, 138, 139, 140, 141, 142, 143) TCK(144, 145, 146, 147, 148, 149, 150, 151)
                     "s_sub_u32 %[n], %[n], 1\n\ts_cbranch_scc0 1b\n5:\n\t" TS(60, 61)
                     "v_and_or_b32 %[t0], %[hi], %[mk], %[pa]\n\tv_and_or_b32 %[t1], %[lo], %[mk], %[pb]\n\t"
                     "v_max_u32 %[t2], %[t0], %[t1]\n\tv_max_u32 %[t3], %[t0], %[t1]\n\tv_perm_b32 %[gd], %[hi], %[lo], %[p0]\n\tv_perm_b32 %[se], %[k2], %[k3], %[p0]\n\t"
                     "v_permlane16_swap_b32 %[t2], %[t3]\n\tv_max_u32 %[km], %[t2], %[t3]\n\tv_max_u32 %[rk], %[t2], %[t3]\n\t"
                     "v_max_u32 %[hi], %[t0], %[t1]\n\tv_min_u32 %[lo], %[t0], %[t1]\n\t"
                     "v_permlane32_swap_b32 %[km], %[rk]\n\tv_max_u32 %[km], %[km], %[rk]\n\tv_not_b32 %[t2], %[km]\n\t"
                     "v_and_or_b32 %[k2], %[k2], %[mk], %[pa]\n\t"
                     "v_not_b32_dpp %[t2], %[km] row_ror:4 row_mask:0x2 bank_mask:0xf\n\tv_not_b32_dpp %[t2], %[km] row_ror:8 row_mask:0x4 bank_mask:0xf\n\t"
                     "v_not_b32_dpp %[t2], %[km] row_ror:12 row_mask:0x8 bank_mask:0xf\n\t"
                     "v_pk_lshlrev_b16 %[gd], 4, %[gd] op_sel_hi:[0,1]\n\tv_add_u32 %[t0], %[km], %[t2]\n\t"
                     "v_add_u32_dpp %[t1], %[t2], %[km] row_ror:1 row_mask:0xf bank_mask:0xf\n\tv_add_u32_dpp %[t3], %[t2], %[km] row_ror:2 row_mask:0xf bank_mask:0xf\n\t"
                     "v_add_u32_dpp %[t2], %[t2], %[km] row_ror:3 row_mask:0xf bank_mask:0xf\n\t"
                     RANK8("%[t0]", "%[t1]", "%[t2]", "%[t3]", "%[rk]", "%[t3]", "-2", "v_lshrrev_b32 %[se], 4, %[se]\n\tv_perm_b32 %[gd], %[gd], %[se], %[p1]",
                           "v_pk_max_u16 %[gd], %[gd], %[gd] op_sel:[0,1] op_sel_hi:[1,0]\n\tv_and_or_b32 %[k3], %[k3], %[mk], %[pb]")
                     "v_cmp_eq_u32 vcc, 56, %[rk]\n\tv_max3_u32 %[se], %[lo], %[k2], %[k3]\n\tv_lshrrev_b32 %[gd], 1, %[gd]\n\ts_ff1_i32_b32 s6, vcc_lo\n\t"
                     "v_readlane_b32 s6, %[km], s6\n\tv_or_b32 %[gd], 0x7fff, %[gd]\n\tv_med3_u32 %[t2], %[lo], %[k2], %[k3]\n\t"
                     "v_cmp_le_u32 s[8:9], s6, %[hi]\n\tv_cmp_le_u32 s[10:11], s6, %[se]\n\tv_max_u32 %[t2], %[t2], %[gd]\n\t"
                     "v_mbcnt_lo_u32_b32 %[t0], s8, 0\n\tv_mbcnt_lo_u32_b32 %[t1], s10, -15\n\tv_mbcnt_hi_u32_b32 %[t0], s9, %[t0]\n\tv_mbcnt_hi_u32_b32 %[t1], s11, %[t1]\n\t"
                     "v_lshl_add_u32 %[t0], %[t0], 2, %[ca]\n\tv_mad_i32_i24 %[t1], %[t1], -4, %[ca]\n\tv_cmp_le_u32 vcc, s6, %[t2]\n\t"
                     "s_mov_b64 exec, s[8:9]\n\tds_write_b32 %[t0], %[hi]\n\ts_mov_b64 exec, s[10:11]\n\tds_write_b32 %[t1], %[se]\n\ts_mov_b64 exec, -1\n\t"
                     "ds_read_b32 %[se], %[kr]\n\tds_read_b128 v[152:155], %[kq]\n\t"
                     "s_bcnt1_i32_b64 s8, s[8:9]\n\ts_bcnt1_i32_b64 s10, s[10:11]\n\ts_add_u32 s8, s8, s10\n\ts_cmp_gt_u32 s8, 16\n\ts_cselect_b64 s[8:9], -1, vcc\n\t"
                     "s_cmp_lg_u64 s[8:9], 0\n\ts_waitcnt lgkmcnt(0)\n\ts_cbranch_scc1 6f\n\t"
                     "v_sub_u32 %[t0], %[se], v152\n\tv_sub_u32 %[t1], %[se], v153\n\tv_sub_u32 %[t2], %[se], v154\n\tv_sub_u32 %[t3], %[se], v155\n\t"
                     RANK8("%[t0]", "%[t1]", "%[t2]", "%[t3]", "%[rk]", "%[t3]", "0", "v_mov_b32 %[km], %[se]\n\ts_nop 0", "s_nop 1") "6:\n\t" TS(62, 63)
                     : [hi] "=&v"(hi), [lo] "=&v"(lo), [k2] "=&v"(k2), [k3] "=&v"(k3), [gd] "=&v"(gd), [se] "=&v"(se), [km] "=&v"(km), [rk] "=&v"(rk),
                       [t0] "=&v"(t0), [t1] "=&v"(t1), [t2] "=&v"(t2), [t3] "=&v"(t3), [n] "+s"(n_)
                     : [o] "v"(O_TOP + 16 * (l & 7) + TL * (l >> 3)), [r] "s"(rs), [t] "s"(tag), [ts] "s"(TOPS), [st] "s"(st0), [tw] "n"(TW), [mk] "s"(0x7fff800fu),
                       [pa] "v"((127 - 2 * l) << 4), [pb] "v"((126 - 2 * l) << 4), [sg] "s"(sg), [p0] "s"(0x05040100u), [p1] "s"(0x07020500u), [ca] "s"(ca),
                       [kr] "v"(kr), [kq] "v"(kq)
                     : "vcc", "scc", "s6", "s8", "s9", "s10", "s11", "memory" TSC);
      }
      unsigned long long more;
      asm volatile("" : "={s6}"(thr), "={s[8:9]}"(more));
      if (__builtin_expect(more != 0, 0)) {
        const u32 m0 = km;
        unsigned long long all = __ballot(gd >= thr);
        if (gd >= thr) hi = lo = k2 = k3 = 0u;
#pragma unroll 1
        for (int i = 0; i < 4;) {
          u32 k, m = m0;
          if (all) {
            const int a = __builtin_ctzll(all);
            all &= all - 1;
            u32x2 r;
            const u32 o = O_RG + 256 * a + 8 * (l & 31);
            POLL8(r, o);
            k = l < 32 ? r.x : 0u;
            m = (u32)__builtin_amdgcn_readlane((int)m0, a);
          } else { k = hi; hi = lo; lo = k2; k2 = k3; i++; }
          asm volatile("v_cmp_le_u32 s[8:9], %[t], %[k]\n\tv_cmp_ne_u32 vcc, %[k], %[m0]\n\ts_and_b64 s[8:9], s[8:9], vcc\n\ts_cbranch_scc0 2f\n"
                       "1:\n\ts_ff1_i32_b64 s7, s[8:9]\n\tv_readlane_b32 s12, %[k], s7\n\ts_lshl_b64 s[10:11], 1, s7\n\ts_nop 0\n\t"
                       "v_cmpx_gt_u32 vcc, s12, %[km]\n\tv_add_u32 %[rk], 8, %[rk]\n\ts_bcnt0_i32_b32 s7, vcc_lo\n\tv_cmpx_eq_u32 vcc, 0x80, %[rk]\n\t"
                       "v_mov_b32 %[km], s12\n\tv_lshlrev_b32 %[rk], 2, s7\n\ts_mov_b64 exec, -1\n\ts_andn2_b64 s[8:9], s[8:9], s[10:11]\n\ts_cbranch_scc1 1b\n2:"
                       : [km] "+v"(km), [rk] "+v"(rk) : [k] "v"(k), [m0] "v"(m), [t] "s"(thr) : "vcc", "scc", "s7", "s8", "s9", "s10", "s11", "s12");
        }
      }
      if (id < 126) {
        u32 a_, v_, n_ = SPIN;
#define RLD(o, s_) "buffer_load_dwordx4 %[v], %[rx], " s_ " offen offset:" #o " lds\n\t"
        asm volatile("v_add_u32 %[a], %[kb], %[rk]\n\tv_and_b32 %[v], 0x7ff, %[km]\n\ts_mov_b32 m0, %[fa]\n\tds_write_b32 %[a], %[v]\n\tds_read_b32 %[a], %[ka]\n\t"
                     "s_cmp_eq_u32 %[ns], 8\n\ts_waitcnt lgkmcnt(0)\n\tv_mad_i32_i24 %[v], %[a], %[cm], %[kc]\n\t"
                     RLD(0, "%[c3]") RLD(1024, "%[c2]") RLD(2048, "%[c1]") RLD(3072, "0")
                     "s_mov_b32 m0, %[fb]\n\tv_add_u32 %[v], 0x200, %[v]\n\tv_mad_i32_i24 %[a], %[a], -8, %[cs]\n\t"
                     "s_cbranch_scc0 3f\n\t" RLD(2048, "%[c1]") RLD(3072, "0")
                     "3:\n\tbuffer_load_dwordx2 v[160:161], %[a], %[rs], 0 offen sc1\n\t" RLD(0, "%[c3]") RLD(1024, "%[c2]") "buffer_load_dwordx2 v[158:159], %[co], %[rs], 0 offen sc1\n\t"
                     "v_mov_b32 %[v], 16 * " STR(HOLD) " - 112\n\tv_subrev_u32 %[ka], %[kd], %[ka]\n\ts_mov_b64 exec, 1\n\tds_add_u32 %[fc], %[v] offset:8\n\ts_mov_b64 exec, -1\n\tv_mov_b32 %[v], -1\n\t" TS(64, 65)
                     "s_sleep " STR(CSL) "\n\tbuffer_load_dwordx2 v[154:155], %[co], %[rs], 0 offen sc1\n\t"
                     "s_waitcnt vmcnt(4)\n1:\n\tv_cmp_ne_u32 vcc, %[t], v161\n\tv_and_b32 v156, 0xffff0000, v160\n\tv_lshlrev_b32 v157, 16, v160\n\tv_rcp_f32 v156, v156\n\t"
                     "s_sub_u32 %[n], %[n], 1\n\ts_cbranch_vccz 2f\n\ts_cbranch_scc1 2f\n\t"
                     "buffer_load_dwordx2 v[160:161], %[a], %[rs], 0 offen sc1\n\ts_waitcnt vmcnt(0)\n\ts_branch 1b\n2:\n\tv_mul_f32 v157, -v157, v156\n\t"
                     "ds_write_b64 %[ka], v[156:157]\n\ts_waitcnt vmcnt(2)\n\tds_write_b32 %[fc], %[v] offset:4\n\t" TS(66, 67)
                     "s_mov_b32 %[n], %[spin]\n\ts_waitcnt vmcnt(1)\n\tv_cmp_ne_u32 vcc, %[t], v159\n\ts_cbranch_vccz 5f\n"
                     "4:\n\ts_waitcnt vmcnt(0)\n\tv_cmp_ne_u32 vcc, %[t], v155\n\ts_sub_u32 %[n], %[n], 1\n\ts_cbranch_vccz 5f\n\t"
                     "buffer_load_dwordx2 v[154:155], %[co], %[rs], 0 offen sc1\n\ts_cbranch_scc0 4b\n5:"
                     : [a] "=&v"(a_), [v] "=&v"(v_), [ka] "+v"(ka), [n] "+s"(n_)
                     : [co] "v"(co), [rk] "v"(rk), [km] "v"(km), [kc] "v"(kc), [kb] "n"(FC + 384), [kd] "n"(384 - 32), [fa] "n"(FA), [fb] "n"(FA + 4096), [rx] "s"(rx), [rs] "s"(rs),
                       [c1] "s"(c1), [c2] "s"(c2), [c3] "s"(c3), [cm] "s"(cm), [cs] "s"(cs), [ns] "s"(ns), [fc] "v"(fa), [t] "s"(tag), [spin] "n"(SPIN) : "vcc", "scc", "memory" TSC);
        TSGET(12, 13, 14, 15);
        FLAG(FC + 28, 1u);
        STAMP(10);
      } else {
        const u32 cd = km << 1;
        const u32x2 r = {(2047u - (km & 0x7ffu)) | ((cd ^ ((u32)((int)~cd >> 15) | 0x80000000u)) & 0xffff0000u), tag};
        const u32 o = rk < 64u ? O_IDX + 1024 * mat + 128 * kg + rk : 0x80000000u;
        st8(rs, o, r);
        st8(rs, o + 512, r);
        TSGET(12, 13, 14, 15);
        STAMP(14);
        STAMP(15);
        if (mat) {
#pragma unroll 1
          for (int spin = 0; spin < SPIN; spin++) {
            const u32x4 a = ld16(rs, O_ARR + 16 * l);
            if (__all(a.y == tag && a.w == tag)) break;
            asm volatile("s_sleep 16" ::: "memory");
          }
          if (l == 0) __builtin_amdgcn_raw_buffer_store_b32(tag, rs, 0, 0, 16);
        }
      }
      FLUSH();
      __builtin_amdgcn_endpgm();
    }
    u32x4 xv[4];
    u32x8 wv;
    ROLE(l, "", , "={v[100:103]}"(xv[0]), "={v[104:107]}"(xv[1]), "={v[108:111]}"(xv[2]), "={v[112:115]}"(xv[3]), "={v[116:123]}"(wv));
    STAMP(11);
    u32 sg = 0x0c0c0b09u;
    asm volatile("" : "+s"(sg));
    ln_rows(xv, wv, rs, tag, b, g, l, lds0 + FC + 128, sg TLVAL);
    STAMP(10);
    IDS();
    if (id >= 126) {
      FLUSH();
      __builtin_amdgcn_endpgm();
    }
    const int wr = 16 * (kg >> 1) + 4 * (kg & 1) + (n16 & 8) + ((n16 >> 2) & 1);
    u32 wa = lds0 + 256 * wr + ((w ^ ((wr & 1) | ((wr >> 1) & 6))) << 5) + 8 * (l & 3), sel = (l & 8) ? 0xeeeeu : 0x4444u;
    u32 so = 4 * t;
    asm volatile("" : "+v"(wa), "+v"(sel), "+v"(so));
    asm volatile(UNTIL(STR(CLD)) :: [st] "s"(st0) : "s8", "s9", "scc");
    __builtin_amdgcn_raw_buffer_store_b32(~0u, rs, so, O_HG + (6 * cb + 3 * mat + part) * 2048, 16);
    st8(rs, u < 8 ? O_PG + (4 * kp + ct) * 4096 + 2 * so : 0x80000000u, u32x2{~0u, ~0u});
#define SWP(a0, a1, a2, a3) "v_permlane32_swap_b32 " a0 ", " a2 "\n\tv_permlane32_swap_b32 " a1 ", " a3 "\n\t"
#define OUT(a0, a1, o) "v_cvt_pk_bf16_f32 " a0 ", " a0 ", " a1 "\n\tv_min_u32 " a0 ", -2, " a0 "\n\tbuffer_store_dword " a0 ", %[so], %[rs], " o " sc1\n\t"
#define TR4(a, b_, c, d, e, f, g_, h, adr, o) "ds_read_b64_tr_b16 v[" #a ":" #b_ "], " adr " offset:" #o "\n\tds_read_b64_tr_b16 v[" #c ":" #d "], " adr " offset:" #o "+512\n\t" \
      "ds_read_b64_tr_b16 v[" #e ":" #f "], " adr " offset:" #o "+8192\n\tds_read_b64_tr_b16 v[" #g_ ":" #h "], " adr " offset:" #o "+8704\n\t"
#define SM(d, a, b_) "v_smfmac_f32_16x16x64_bf16 v[" d "], v[" a "], v[" b_ "], %[sel]\n\t"
#define USE(c, xa, wf) "s_add_u32 s6, s6, 16\n\ts_waitcnt lgkmcnt(" #c ")\n\t" SM("16:19", "20:23", wf) SM("8:11", xa, wf)
#define XR(xa, o) "ds_read_b128 v[" xa "], %[xa] offset:" #o "\n\t"
#define XU(c, xa, wf) "s_waitcnt lgkmcnt(" #c ")\n\t" SM("8:11", xa, wf)
#define ZERO(r) "v_mfma_f32_32x32x16_bf16 v[" r "], v[8:11], v[8:11], 0\n\t"
#define POLL(c, rd) "7:\n\tds_read_b32 v30, %[fl]\n\ts_waitcnt lgkmcnt(0)\n\ts_call_b64 s[10:11], 1f\n\ts_cbranch_scc1 7b\n\t" rd
#define CLR "s_waitcnt vmcnt(0)\n\ts_mov_b64 exec, 1\n\tds_add_rtn_u32 v14, %[fl], v28 offset:96\n\ts_waitcnt lgkmcnt(0)\n\tv_cmpx_eq_u32 vcc, 7, v14\n\t" \
      "buffer_store_dwordx2 v[12:13], off, %[rs], %[co] sc1\n\ts_mov_b64 exec, -1\n\t"
#define XE(k) "s_cmpk_eq_u32 s6, " #k "\n\ts_cbranch_scc1 9f\n\t"
    asm volatile("v_mov_b64 v[8:9], 0\n\tv_mov_b64 v[10:11], 0\n\tv_mov_b64 v[16:17], 0\n\tv_mov_b64 v[18:19], 0\n\t"
                 ZERO("96:111") ZERO("112:127")
                 "v_mov_b32 v20, 0x3f803f80\n\tv_mov_b32 v21, v20\n\tv_mov_b64 v[22:23], v[20:21]\n\tv_mov_b32 v13, %[t]\n\tv_mov_b32 v28, 1\n\tv_mov_b32 v30, 0\n\t"
                 "v_add_u32 v29, 0x10000, %[wa]\n\ts_movk_i32 s6, 96\n\ts_mov_b32 s7, 1\n\ts_mov_b32 s8, 15\n\ts_mov_b32 s9, %[spin]\n\ts_nop 7\n\t"
                 POLL(8, TR4(32, 33, 34, 35, 36, 37, 38, 39, "%[wa]", 0) TR4(40, 41, 42, 43, 44, 45, 46, 47, "%[wa]", 16384))
                 USE(4, "96:99", "32:39") "s_waitcnt lgkmcnt(0)\n\t" SM("16:19", "20:23", "40:47") SM("8:11", "100:103", "40:47")
                 CLR
                 POLL(4, TR4(48, 49, 50, 51, 52, 53, 54, 55, "%[wa]", 32768)) USE(0, "104:107", "48:55")
                 POLL(4, TR4(56, 57, 58, 59, 60, 61, 62, 63, "%[wa]", 49152)) "s_mov_b64 exec, 1\n\tds_add_u32 %[fl], v28 offset:20\n\ts_mov_b64 exec, -1\n\t" TS(60, 61)
                 USE(0, "108:111", "56:63")
                 POLL(4, TR4(64, 65, 66, 67, 68, 69, 70, 71, "v29", 0)) USE(0, "112:115", "64:71") "s_cmp_eq_u32 %[ns], 5\n\ts_cbranch_scc1 5f\n\t"
                 POLL(4, TR4(72, 73, 74, 75, 76, 77, 78, 79, "v29", 16384)) USE(0, "116:119", "72:79") "s_cmp_eq_u32 %[ns], 6\n\ts_cbranch_scc1 5f\n\t"
                 POLL(4, TR4(80, 81, 82, 83, 84, 85, 86, 87, "v29", 32768)) USE(0, "120:123", "80:87")
                 POLL(4, TR4(88, 89, 90, 91, 92, 93, 94, 95, "v29", 49152)) USE(0, "124:127", "88:95")
                 "5:\n\t" TS(62, 63) "s_cmp_eq_u32 s7, 0\n\ts_cbranch_scc1 4f\n\ts_mov_b32 s8, 16\n\t"
                 "s_getpc_b64 s[10:11]\n\tds_read_b32 v30, %[fl]\n\ts_waitcnt lgkmcnt(0)\n"

                 "1:\n\tv_cmp_le_u32 vcc, s6, v30\n\ts_and_b32 s12, vcc_hi, s7\n\ts_cbranch_scc1 3f\n"
                 "2:\n\ts_andn2_b32 s12, s8, vcc_lo\n\ts_cbranch_scc1 6f\n\ts_setpc_b64 s[10:11]\n"
                 "6:\n\ts_sub_u32 s9, s9, 1\n\ts_cbranch_scc1 4f\n\t" GNAP "\n\ts_cmp_eq_u32 s6, s6\n\ts_setpc_b64 s[10:11]\n"
                 "3:\n\t"
                 XR("96:99", 0) XR("100:103", 1024) XR("104:107", 2048) XR("108:111", 3072) XR("112:115", 4096) XR("116:119", 5120) XR("120:123", 6144) XR("124:127", 7168)
                 "ds_read_b128 v[24:27], %[pa]\n\ts_mov_b32 s7, 0\n\t"
                 TS(64, 65)
                 XE(96) XU(8, "96:99", "32:39") XU(7, "100:103", "40:47") XE(112) XU(6, "104:107", "48:55") XE(128) XU(5, "108:111", "56:63") XE(144)
                 XU(4, "112:115", "64:71") XE(160) XU(3, "116:119", "72:79") XE(176) XU(2, "120:123", "80:87") XE(192) XU(1, "124:127", "88:95")
                 "9:\n\ts_cmp_eq_u32 s8, 16\n\ts_waitcnt lgkmcnt(0)\n\ts_cbranch_scc0 2b\n"
                 "4:\n\ts_nop 4\n\tv_permlane32_swap_b32 v16, v17\n\tv_add_f32 v16, v16, v17\n\tv_mul_f32 v25, v25, v16\n\tv_mul_f32 v27, v27, v16\n\t"
                 SWP("v8", "v9", "v10", "v11") "v_fmac_f32 v25, v10, v24\n\tv_fmac_f32 v27, v11, v26\n\tv_fma_f32 v8, v8, v24, v25\n\tv_fma_f32 v9, v9, v26, v27\n\t"
                 OUT("v8", "v9", "%[ho] offen") TS(66, 67)
                 :: [wa] "v"(wa), [sel] "v"(sel), [so] "v"(so), [fl] "v"(lds0 + FC + 4 * min(l, 4)), [xa] "v"(lds0 + FA + 128 * (l & 7) + 16 * ((2 * kg + (n16 >> 3)) ^ (2 * l & 4))),
                    [pa] "v"(lds0 + FC + 32 + 32 * (kg & 1) + 16 * (kg >> 1)), [ns] "s"(ns), [t] "s"(tag), [co] "s"(O_CLR + 8 * id), [rs] "s"(rs), [ho] "s"(O_HG + (6 * cb + 3 * mat + part) * 2048), [spin] "n"(SPIN)
                 : "vcc", "scc", "s6", "s7", "s8", "s9", "s10", "s11", "s12", "s13", "memory", G_CLOBBER TSC);
    TSGET(1, 4, 3, 5);
    if (u < 8) {
      const int tp = 2 * (kg & 1) + (kg >> 1), r7 = 4 * (kg & 1) + (n16 >> 2);
      u32 o3 = (kp & 3) == 1 ? so + 4096 : 0x80000000u, h0 = O_HG + kp * 12288, h1 = h0 + 6144;
      u32 hw = lds0 + FH + 2 * tp * HP + 32 * w + 8 * (l & 2) + (l & 4) * 2 + ((l & 8) >> 1) + 2 * (l & 1);
      u32 hr = lds0 + FH + (l & 7) * HP + 32 * kg + 2 * (l & 8);
      u32 da = lds0 + 512 * (16 * (kg >> 1) + r7) + ((2 * w ^ r7) << 5) + 8 * (l & 3), db = da ^ 32, fl = lds0 + FC + 4 * (l & 3);
      u32 po = 512 * w + 16 * (l & 8) + 32 * tp + 4 * (l & 7);
      u32 e0 = 0x3f80u, e1 = 0x3f800000u;
      asm volatile("" : "+v"(hw), "+v"(hr), "+v"(da), "+v"(db), "+v"(fl), "+v"(po), "+v"(o3));
      asm volatile("" : "+s"(e0), "+s"(e1), "+s"(h0), "+s"(h1));
#define TRD(a, b_, c, d, e, f, g_, h, adr, o) "ds_read_b64_tr_b16 v[" #a ":" #b_ "], " adr " offset:" #o "\n\tds_read_b64_tr_b16 v[" #c ":" #d "], " adr " offset:" #o "+4096\n\t" \
      "ds_read_b64_tr_b16 v[" #e ":" #f "], " adr " offset:" #o "+16384\n\tds_read_b64_tr_b16 v[" #g_ ":" #h "], " adr " offset:" #o "+20480\n\t"
#define HLD(X, r) X(r "0", "%[s1]", "%[h0] offen") X(r "3", "%[s1]", "%[h1] offen") X(r "1", "%[s1]", "%[h0] offen offset:2048") X(r "4", "%[s1]", "%[h1] offen offset:2048") \
      X(r "2", "%[o3]", "%[h0] offen") X(r "5", "%[o3]", "%[h1] offen")
#define HL0(a, o, s_) "buffer_load_dword " a ", " o ", %[rs], " s_ " sc1\n\t"
#define HLF(a, o, s_) "buffer_load_dword " a ", " o ", %[rs], " s_ SCH "\n\t"
#define HL1(a, o, s_) "v_cmpx_eq_u32 vcc, -1, " a "\n\t" HL0(a, o, s_) "s_mov_b64 exec, -1\n\t"
#define DOT(d, e, a) "v_dot2c_f32_bf16 " d ", %[" e "], " a "\n\t"
#define HCK(c, r) "s_cmp_eq_u32 s7, 0\n\ts_cbranch_scc1 5f\n\ts_call_b64 s[10:11], 4f\n5:\n\ts_waitcnt vmcnt(" #c ")\n\tv_max3_u32 v28, " r "0, " r "1, " r "2\n\tv_lshlrev_b32 v24, 16, " r "0\n\tv_max3_u32 v28, v28, " r "3, " r "4\n\t" \
      "v_and_b32 v25, %[hm], " r "0\n\tv_max_u32 v28, v28, " r "5\n\t" DOT("v24", "e0", r "1") "v_cmp_eq_u32 vcc, -1, v28\n\t" DOT("v25", "e1", r "1") DOT("v24", "e0", r "2") DOT("v25", "e1", r "2") \
      "v_lshlrev_b32 v26, 16, " r "3\n\tv_and_b32 v27, %[hm], " r "3\n\t" DOT("v26", "e0", r "4") DOT("v27", "e1", r "4") DOT("v26", "e0", r "5") DOT("v27", "e1", r "5")
#define RB2(a, b_) "v_cvt_pk_bf16_f32 " a ", 0, " a "\n\tv_cvt_pk_bf16_f32 " b_ ", 0, " b_ "\n\t"
      asm volatile("s_mov_b32 s7, 1\n\ts_mov_b32 s12, %[spin]\n\tv_mov_b64 v[8:9], 0\n\tv_mov_b64 v[10:11], 0\n\tv_mov_b64 v[12:13], 0\n\tv_mov_b64 v[14:15], 0\n"
                   "2:\n\ts_sleep 1\n\tds_read_b32 v28, %[fl] offset:16\n\ts_memrealtime s[8:9]\n\ts_waitcnt lgkmcnt(0)\n\tv_cmp_eq_u32 vcc, 0, v28\n\ts_sub_u32 s8, s8, %[st]\n\t"
                   "s_cmp_lt_u32 s8, %[th]\n\ts_cbranch_scc1 2b\n\ts_bitcmp1_b32 vcc_lo, 3\n\ts_cbranch_scc1 2b\n\t" TS(64, 65)
                   HLD(HLF, "v7") HCK(0, "v7") "s_cbranch_vccnz 9f\n"
                   "3:\n\t" TS(66, 67) RB2("v24", "v25") "v_mul_f32 v28, %[le], v24\n\tv_mul_f32 v29, %[le], v25\n\tv_exp_f32 v28, v28\n\tv_exp_f32 v29, v29\n\t" RB2("v26", "v27")
                   "v_add_f32 v28, 1.0, v28\n\tv_add_f32 v29, 1.0, v29\n\tv_rcp_f32 v28, v28\n\tv_rcp_f32 v29, v29\n\ts_cmp_eq_u32 s7, 0\n\tv_mul_f32 v24, v24, v28\n\tv_mul_f32 v25, v25, v29\n\t"
                   RB2("v24", "v25") "v_mul_f32 v24, v24, v26\n\tv_mul_f32 v25, v25, v27\n\tv_cvt_pk_bf16_f32 v24, v24, v25\n\ts_mov_b32 s12, %[spin]\n\t"
                   "ds_write_b16 %[hw], v24\n\tds_write_b16_d16_hi %[hw], v24 offset:%[hp]\n\ts_cbranch_scc1 8f\n"
                   "7:\n\ts_call_b64 s[10:11], 4f\n\ts_mul_i32 s12, s12, s7\n\ts_sub_u32 s12, s12, 1\n\ts_cbranch_scc0 7b\n"
                   "8:\n\ts_waitcnt lgkmcnt(0)\n\ts_barrier\n\t" TS(60, 61)
                   XR("16:19", 0) XR("20:23", 128)
                   XU(1, "16:19", "32:39") SM("12:15", "16:19", "40:47") XU(0, "20:23", "48:55") SM("12:15", "20:23", "56:63") "s_nop 6\n\t"
                   SWP("v8", "v9", "v10", "v11") "v_add_f32 v8, v8, v10\n\tv_add_f32 v9, v9, v11\n\t" SWP("v12", "v13", "v14", "v15") OUT("v8", "v9", "%[po] offen")
                   "v_add_f32 v12, v12, v14\n\tv_add_f32 v13, v13, v15\n\t" OUT("v12", "v13", "%[po] offen offset:256") TS(62, 63) DONE
                   "9:\n\tv_mov_b64 v[80:81], v[70:71]\n\tv_mov_b64 v[82:83], v[72:73]\n\tv_mov_b64 v[84:85], v[74:75]\n\t" HLD(HL1, "v7") "s_sleep " STR(HNAP) "\n"
                   "1:\n\t" HLD(HL1, "v8") HCK(6, "v7") "s_cbranch_vccz 3b\n\t" HLD(HL1, "v7") HCK(6, "v8") "s_sub_u32 s12, s12, 1\n\ts_cbranch_vccz 3b\n\ts_cbranch_scc0 1b\n\ts_branch 3b\n"
                   "4:\n\tds_read_b32 v28, %[fl]\n\ts_waitcnt lgkmcnt(0)\n\tv_cmp_ne_u32 vcc, -1, v28\n\ts_cbranch_vccnz 6f\n\t"
                   TRD(32, 33, 34, 35, 36, 37, 38, 39, "%[da]", 0) TRD(40, 41, 42, 43, 44, 45, 46, 47, "%[db]", 0)
                   TRD(48, 49, 50, 51, 52, 53, 54, 55, "%[da]", 32768) TRD(56, 57, 58, 59, 60, 61, 62, 63, "%[db]", 32768) "s_mov_b32 s7, 0\n"
                   "6:\n\ts_setpc_b64 s[10:11]\n0:"
                   :: [s1] "v"(so), [o3] "v"(o3), [fl] "v"(fl), [da] "v"(da), [db] "v"(db), [hw] "v"(hw), [xa] "v"(hr), [sel] "v"(sel), [so] "v"(po), [rs] "s"(rs), [h0] "s"(h0), [h1] "s"(h1),
                      [st] "s"(st0), [th] "n"(TH), [spin] "n"(SPIN), [hp] "n"(HP), [po] "s"(O_PG + (4 * kp + ct) * 4096), [e0] "s"(e0), [e1] "s"(e1), [hm] "s"(0xffff0000u),
                      [le] "s"(-1.4426950408889634f)
                   : "vcc", "scc", "s7", "s8", "s9", "s10", "s11", "s12", "memory", G_CLOBBER TSC);
      TSGET(8, 9, 6, 7);
    }
    FLUSH();
    __builtin_amdgcn_endpgm();
  }

  if (w0 >= 8) {
    u32x8 xa0, xa1, xa2, xa3, sa1;
    u32x4 junk;
    u32 op, so;
    ROLE(p, "", , "={v[100:107]}"(xa0), "={v[108:115]}"(xa1), "={v[116:123]}"(xa2),
         "={v148}"(op), "={v149}"(so), "={v[160:163]}"(junk));
    const int p = w - 8;
    __builtin_amdgcn_s_setprio(3);
    asm volatile("s_waitcnt lgkmcnt(0)" : "+{s44}"(tag)::"memory");
    tag += 1u;
    STAMP(9);
    f32x2 xq[4][8];
    u32x8 pa;
    {
#define SCK(r1, r3, r5, r7) "v_xor_b32 %[d], %[t], v" #r1 "\n\tv_bitop3_b32 %[d], %[d], %[t], v" #r3 " bitop3:0xf6\n\t" \
      "v_bitop3_b32 %[d], %[d], %[t], v" #r5 " bitop3:0xf6\n\tv_bitop3_b32 %[d], %[d], %[t], v" #r7 " bitop3:0xf6\n\t"
#define SLX(r0, r3, r4, r7, k, k1, sc) "buffer_load_dwordx4 v[" #r0 ":" #r3 "], %[so], %[rs], " k " offen" sc "\n\tbuffer_load_dwordx4 v[" #r4 ":" #r7 "], %[so], %[rs], " k1 " offen" sc "\n\t"
#define SLD(r0, r3, r4, r7, k, k1) SLX(r0, r3, r4, r7, k, k1, " sc1")
#define SLOOP(r0, r1, r3, r4, r5, r7, k, k1) "1:\n\ts_waitcnt vmcnt(0)\n4:\n\t" SCK(r1, r3, r5, r7) "s_sub_u32 %[n], %[n], 1\n\tv_cmp_ne_u32 vcc, 0, %[d]\n\ts_cbranch_scc1 2f\n\ts_cbranch_vccz 2f\n\t" \
      SLD(r0, r3, r4, r7, k, k1) "s_branch 1b\n2:\n\t"
      u32 d_, n_ = SPIN, k1 = 4096, k2 = 8192, k3 = 12288, s0[4];
      asm volatile("s_mov_b32 m0, %[m]\n\ts_cmp_eq_u32 %[k], 3\n\ts_waitcnt vmcnt(2)\n\ts_cbranch_scc0 1f\n\ts_waitcnt vmcnt(0)\n1:"
                   : "+{v[100:107]}"(xa0), "+{v[108:115]}"(xa1), "+{v[160:163]}"(junk) : [m] "s"(lds0 + OX + 2 * XO + 256 * p), [k] "s"(xk) : "scc", "memory");
      SB();
#define UNPACK(i, xs) _Pragma("unroll") for (int c = 0; c < 8; c++) { \
        xq[i][c] = (c & 1) ? f32x2{hi2f(xs[c >> 1]), hi2f(xs[4 + (c >> 1)])} : f32x2{lo2f(xs[c >> 1]), lo2f(xs[4 + (c >> 1)])}; asm volatile("" : "+v"(xq[i][c].x), "+v"(xq[i][c].y)); }
      UNPACK(0, xa0);
      UNPACK(1, xa1);
      u32 fa = lds0 + OXF + 4 * p, fv = 1;
      asm volatile("" : "+v"(fa), "+v"(fv));
      asm volatile("" : "+s"(k1), "+s"(k2), "+s"(k3), "+s"(n_));
      SB();
      STAMP(1);
#define SCP(r0, r1, r2, r3, r4, r5, r6, r7) "s_waitcnt vmcnt(2)\n\t" SCK(r1, r3, r5, r7) "v_cmp_ne_u32 vcc, 0, %[d]\n\tv_mov_b32 %[a0], v" #r0 "\n\tv_mov_b32 %[a1], v" #r2 "\n\t" \
      "v_mov_b32 %[a2], v" #r4 "\n\tv_mov_b32 %[a3], v" #r6 "\n\ts_cbranch_vccz 3f\n\t" SLD(r0, r3, r4, r7, "0", "%[k1]")
      asm volatile(UNTIL(STR(TSG)) SLD(132, 135, 136, 139, "0", "%[k1]") "s_sleep " STR(SSL) "\n\t" SLD(140, 143, 144, 147, "0", "%[k1]") "s_waitcnt vmcnt(4)"
                   : "=&{v[132:139]}"(pa), "=&{v[140:147]}"(sa1), "+{v[116:123]}"(xa2)
                   : [st] "s"(st0), [so] "v"(so), [rs] "s"(rs), [k1] "s"(k1) : "scc", "s8", "s9", "memory");
      UNPACK(2, xa2);
      SB();
      asm volatile("1:\n\t" SCP(132, 133, 134, 135, 136, 137, 138, 139) SCP(140, 141, 142, 143, 144, 145, 146, 147) "s_sub_u32 %[n], %[n], 1\n\ts_cbranch_scc0 1b\n"
                   "3:\n\t" SLD(140, 143, 144, 147, "%[k2]", "%[k3]")
                   : [d] "=&v"(d_), [n] "+s"(n_), [a0] "=&v"(s0[0]), [a1] "=&v"(s0[1]), [a2] "=&v"(s0[2]), [a3] "=&v"(s0[3]), "+{v[132:139]}"(pa), "+{v[140:147]}"(sa1)
                   : [t] "s"(tag), [so] "v"(so), [rs] "s"(rs), [k1] "s"(k1), [k2] "s"(k2), [k3] "s"(k3) : "vcc", "scc", "memory");
#pragma unroll
      for (int i = 0; i < 5; i++) {
#define XLD(r0, r1, r2, r3, o_) "v_add_u32 %[d], " #o_ ", %[o]\n\tglobal_load_dwordx4 v[" #r0 ":" #r1 "], %[d], %[x]\n\tglobal_load_dwordx4 v[" #r2 ":" #r3 "], %[d], %[x] offset:2048\n"
        if (i == XI) asm volatile("s_cmp_eq_u32 %[k], 3\n\ts_cbranch_scc0 1f\n\t" XLD(116, 119, 120, 123, 0x200000) "1:\n\t" XLD(124, 127, 128, 131, 0x300000)
                                 : [d] "=&v"(d_), "+{v[116:123]}"(xa2), "=&{v[124:131]}"(xa3) : [k] "s"(xk), [o] "v"(op), [x] "s"(x) : "scc", "memory");
        if (i == 2) {
          asm volatile("s_waitcnt vmcnt(2)\n\ts_branch 4f\n" SLOOP(140, 141, 143, 144, 145, 147, "%[k2]", "%[k3]") : [d] "=&v"(d_), [n] "+s"(n_), "+{v[140:147]}"(sa1), "+{v[116:123]}"(xa2), "+{v[132:139]}"(pa)
                       : [t] "s"(tag), [so] "v"(so), [rs] "s"(rs), [k2] "s"(k2), [k3] "s"(k3) : "vcc", "scc", "memory");
          if (xk == 3) { UNPACK(2, xa2); }
        }
        if (i == 3) asm volatile("s_waitcnt vmcnt(%1)" : "+{v[124:131]}"(xa3) : "n"(DPI == 2 ? DPF : 0));
        if (i == DPI && DPF) {
          u32 lx = l;
          asm volatile("" : "+v"(lx));
          const u32 ti = min(16 * xk + 4 * p + (lx >> 4), 125u), tsb = __umul24(ti, 57) >> 10, tu = min(ti - __umul24(18, tsb), 15u);
          const u32 o = (128 * (4 * tsb + (tu >> 2)) + DPO / 2 + 4 * j + ((lx >> 2) & 3)) * (2 * D) + 512 * (tu & 3) + 128 * (lx & 3);
          if (DPI == 4) AWAIT(OXF + 4 * (l & 3), 3, "s_sleep 1");
          if (DPF == 1) asm volatile("global_load_dword v160, %1, %2" : "=&{v[160:163]}"(junk) : "v"(o), "s"(wd) : "memory");
          else asm volatile("global_load_dword v160, %1, %2\n\tglobal_load_dword v161, %1, %3" : "=&{v[160:163]}"(junk) : "v"(o), "s"(wd), "s"(wd + 64 * D) : "memory");
        }
        if (i == 4) break;
        if (i == 3) UNPACK(3, xa3);
        const u32 sa = i < 2 ? s0[2 * i] : sa1[4 * i - 8], sb = i < 2 ? s0[2 * i + 1] : sa1[4 * i - 6];
        if (i == 0) STAMP(3);
        if (i == 2) STAMP(10);
        const f32x2 r2 = {__builtin_amdgcn_rcpf(hi2f(sa)), __builtin_amdgcn_rcpf(hi2f(sb))};
        const f32x2 m2 = {-lo2f(sa) * r2.x, -lo2f(sb) * r2.y};
        f32x2 n2[8];
        u32 o[8];
        SB();
#pragma unroll
        for (int c = 0; c < 8; c++) n2[c] = f32x2{fmav(xq[i][c].x, r2.x, m2.x), fmav(xq[i][c].y, r2.y, m2.y)};
        SB();
#pragma unroll
        for (int c = 0; c < 8; c++) o[c] = packh(n2[c].x, n2[c].y);
        SB();
#pragma unroll
        for (int c = 0; c < 8; c++) asm volatile("ds_write_addtid_b32 %0 offset:%1" ::"v"(o[c]), "n"(c * XREG + 1024 * i) : "memory");
        asm volatile("ds_write_b32 %1, %0\n\tv_add_u32 %0, 1, %0" : "+v"(fv) : "v"(fa) : "memory");
        if (i == PDROP) __builtin_amdgcn_s_setprio(PPRIO);
        SB();
      }
    }
    STAMP(4);
    const rsrc_t ro = __builtin_amdgcn_make_buffer_rsrc((void*)out, 0, 2 * L * D, 0x00020000);
    u32 ys[8], y[2][8] = {};
    u32x2 ix;
    u32x4 cl;
    u32x8 pb;
    float yc, rsel;
    const u32 ixo = IXO((l & 6) + (n16 >> 3));
    if (p == 0) asm volatile("buffer_load_dwordx2 v[4:5], %1, %2, 0 offen sc1" : "=&{v[4:5]}"(ix) : "v"(ixo), "s"(rs) : "memory");
    const int rc = 4 * (l & 1) + kg, u = 64 * (p & 1) + l;
    u32x4 pick;
    u32 po = O_PG + 128 * g + 16 * (l & 7) + 16384 * (l >> 3), p3 = l < 32 ? po + 3 * 131072 : 0x80000000u, yf = lds0 + OYP + 4 * min(n16, 12) + 64 * (p >> 1);
    u32 ya = lds0 + OY + CHR * rc, oo = 16 * g + 2 * rc, clo = O_CLR + 16 * min(l, 62);
    u32 yp = lds0 + OY + 512 * (p >> 1) + 4 * (u ^ ((u >> 4) & 6)), yq = yp + 4 * CHR, bp = lds0 + OMISC + 16 * p + 4 * kg, bs = 30 - 2 * n16;
#pragma unroll
    for (int q = 0; q < 4; q++) pick[q] = (n16 >> 2) == q ? 0x3f80u << 16 * (l & 1) : 0u;
    asm volatile("" : "+v"(pick), "+v"(ya), "+v"(oo), "+v"(po), "+v"(p3), "+v"(yf), "+v"(clo), "+v"(yp), "+v"(yq), "+v"(bp), "+v"(bs));
#pragma unroll
    for (int c = 0; c < 8; c++) asm volatile("" : "+v"(y[0][c]), "+v"(y[1][c]));
    if (TE) asm volatile(UNTIL(STR(TE)) :: [st] "s"(st0) : "s8", "s9", "scc");
#pragma unroll
    for (int i = 0; i < 4; i++) {
      if (i == IXI && p == 0) {
        u32 n_ = SPIN;
        asm volatile("s_waitcnt vmcnt(%5)\n1:\n\tv_cmp_ne_u32 vcc, %4, v5\n\ts_sub_u32 %1, %1, 1\n\ts_cbranch_vccz 2f\n\ts_cbranch_scc1 2f\n\t"
                     "buffer_load_dwordx2 v[4:5], %2, %3, 0 offen sc1\n\ts_waitcnt vmcnt(0)\n\ts_branch 1b\n2:"
                     : "+{v[4:5]}"(ix), "+s"(n_) : "v"(ixo), "s"(rs), "s"(tag), "n"(2 * IXI + 1) : "vcc", "scc", "memory");
        const u32 prow = ix.x & 2047u, v = prow >> 1;
        if (l < 16) atomicOr((u32*)(smem + OMISC) + (prow >> 5), 1u << (ix.x & 31u));
        FLAG(OYP + 256 + 32 + 64 * (l & 3), 8u);
        oo += prow * (2 * D);
        asm volatile("v_mov_b32 v162, 0\n\tglobal_load_short_d16_hi v162, %1, %2" : "+{v[160:163]}"(junk) : "v"(oo), "s"(x) : "memory");
        yc = *(const float*)(smem + OSC + 4 * rc); rsel = rbt(sigm(hi2f(ix.x)));
        ya += 4 * (v ^ ((v >> 4) & 6)) + 2 * (prow & 1);
        asm volatile("" : "+v"(yc), "+v"(rsel), "+v"(ya));
        STAMP(11);
      }
      if (i == 2 && p == 0) cl = ld16(rs, clo);
      if (i == 3) {
        asm volatile("s_waitcnt vmcnt(2)" ::: "memory");
        FLAG(OYP + 448 + 36 + 4 * p, 8u);
        if (p == 0) {
#pragma unroll 1
          for (int spin = 0; spin < SPIN; spin++) {
            if (__all(cl.y == tag && cl.w == tag)) break;
            cl = ld16(rs, clo);
          }
          STAMP(12);
        }
        __builtin_amdgcn_s_setprio(LPRIO);
      }
      {
        u32 n_ = SPIN, f_;
        asm volatile("1:\n\tds_read_b32 %0, %2 offset:%3\n\ts_sub_u32 %1, %1, 1\n\ts_waitcnt lgkmcnt(0)\n\tv_cmp_eq_u32 vcc, 0, %0\n\ts_cbranch_scc1 2f\n\ts_and_b32 vcc_lo, vcc_lo, %4\n\t"
                     "s_cbranch_scc0 2f\n\ts_sleep %5\n\ts_branch 1b\n2:" : "=&v"(f_), "+s"(n_) : "v"(yf), "n"(128 * i), "n"(i < 2 ? 255 : 511), "n"(i < 3 ? YN0 : YN3) : "vcc", "scc", "memory");
      }
      if (i == 0) STAMP(5); else if (i == 1) STAMP(6); else if (i == 2) STAMP(7); else STAMP(8);
      u32 bm = 0;
      if (i >= 2) asm volatile("ds_read_b32 %0, %1 offset:%2" : "=v"(bm) : "v"(bp), "n"(64 * i) : "memory");
#pragma unroll
      for (int r = 0; r < 2; r++)
#pragma unroll
        for (int c = 0; c < 8; c++) asm volatile("ds_read_u16_d16_hi %0, %1 offset:%2" : "+v"(y[r][c]) : "v"(c < 4 ? yp : yq), "n"((c & 3) * CHR + 1024 * i + 2 * r) : "memory");
      if (i == 3 && p == 0) {
#define PLD(X) X(132, 133, 134, 135, "%[po]", "0") X(136, 137, 138, 139, "%[po]", "%[k1]") X(140, 141, 142, 143, "%[po]", "%[k2]") X(144, 145, 146, 147, "%[p3]", "0")
#define PL0(a, b_, c, d, o, s_) "buffer_load_dwordx4 v[" #a ":" #d "], " o ", %[rs], " s_ " offen sc1\n\t"
        asm volatile(PLD(PL0) : "=&{v[132:139]}"(pa), "=&{v[140:147]}"(pb) : [po] "v"(po), [p3] "v"(p3), [rs] "s"(rs), [k1] "s"(131072), [k2] "s"(262144) : "memory");
      }
      if (i == 0) {
        st8(rs, t == 512 ? O_ARR + b * 8 : 0x80000000u, u32x2{0u, tag});
        const u32x4 a = *(const u32x4*)(smem + OSC), c = *(const u32x4*)(smem + OSC + 16);
#pragma unroll
        for (int k = 0; k < 4; k++) { ys[k] = (u32)__builtin_amdgcn_readfirstlane((int)a[k]); ys[4 + k] = (u32)__builtin_amdgcn_readfirstlane((int)c[k]); }
      }
      SB();
#pragma unroll
      for (int r = 0; r < 2; r++) {
        asm volatile("s_waitcnt lgkmcnt(%9)" : "+v"(y[r][0]), "+v"(y[r][1]), "+v"(y[r][2]), "+v"(y[r][3]), "+v"(y[r][4]), "+v"(y[r][5]), "+v"(y[r][6]), "+v"(y[r][7]), "+v"(bm) : "n"(8 - 8 * r));
        u32x4 v;
#pragma unroll
        for (int k = 0; k < 4; k++)
          v[k] = pack2(fmas(__uint_as_float(y[r][2 * k]), ys[2 * k], xq[i][2 * k][r]), fmas(__uint_as_float(y[r][2 * k + 1]), ys[2 * k + 1], xq[i][2 * k + 1][r]));
        if (i >= 2 && r == 0) bm <<= bs;
        __builtin_amdgcn_raw_buffer_store_b128(v, ro, i < 2 ? op : op | (bm & 0x40000000u << r), (512 * i + r) * (2 * D), OAUX);
        SB();
        asm volatile("s_nop 1");
        SB();
      }
    }
    asm volatile("" ::"{v[160:163]}"(junk));
    STAMP(13);
    if (p == 0) {
      u32 n_ = SPIN;
#define PL1(a, b_, c, d, o, s_) "v_max3_u32 v150, v" #a ", v" #b_ ", v" #c "\n\tv_max_u32 v150, v150, v" #d "\n\tv_cmpx_eq_u32 vcc, -1, v150\n\t" PL0(a, b_, c, d, o, s_) "s_mov_b64 exec, -1\n\t"
#define PLE(X) X(112, 113, 114, 115, "%[po]", "0") X(116, 117, 118, 119, "%[po]", "%[k1]") X(120, 121, 122, 123, "%[po]", "%[k2]") X(124, 125, 126, 127, "%[p3]", "0")
#define PMX(a, b_) "v_max3_u32 v150, v150, v" a ", v" b_ "\n\t"
#define PMM(d, a, c) "v_mfma_f32_16x16x32_bf16 v[" d "], %[pk], v[" a "], " c "\n\t"
#define PCK(c, p, q) "s_waitcnt vmcnt(" #c ")\n\t" PMM("152:155", p "2:" p "5", "0") "v_max_u32 v150, v" p "2, v" p "3\n\t" PMX(p "4", p "5") PMM("156:159", p "6:" p "9", "0") PMX(p "6", p "7") PMX(p "8", p "9") \
      PMM("152:155", q "0:" q "3", "v[152:155]") PMX(q "0", q "1") PMX(q "2", q "3") PMM("156:159", q "4:" q "7", "v[156:159]") PMX(q "4", q "5") PMX(q "6", q "7") \
      "v_cmp_eq_u32 vcc, -1, v150\n\ts_sub_u32 %[n], %[n], 1\n\t"
#define PCP(a, b_, c, d) "v_mov_b64 v[" #a ":" #b_ "], v[" #c ":" #d "]\n\t"
      asm volatile(PCK(2, "13", "14") "s_cbranch_vccnz 9f\n"
                   "2:\n\t" TS(60, 61) "ds_read_b32 v150, %[yf] offset:448\n\tds_read_u16_d16_hi v151, %[ya]\n\ts_mov_b32 %[n], %[spin]\n\ts_nop 1\n\tv_add_f32 v152, v152, v156\n\tv_add_f32 v153, v153, v157\n\ts_nop 0\n\t"
                   "v_add_f32_dpp v152, v152, v152 row_ror:8 row_mask:0xf bank_mask:0x3\n\tv_add_f32_dpp v152, v153, v153 row_ror:8 row_mask:0xf bank_mask:0xc\n\t"
                   "v_cvt_pk_bf16_f32 v152, 0, v152\n\tv_mul_f32 v152, v152, %[rl]\n\tv_cvt_pk_bf16_f32 v152, 0, v152\n\tv_add_f32 v153, v162, v152\n"
                   "3:\n\ts_waitcnt lgkmcnt(0)\n\tv_cmp_eq_u32 vcc, 0, v150\n\tv_and_b32 v152, 0xffff0000, v151\n\ts_sub_u32 %[n], %[n], 1\n\ts_cbranch_vccz 4f\n\ts_cbranch_scc1 4f\n\t"
                   "ds_read_b32 v150, %[yf] offset:448\n\tds_read_u16_d16_hi v151, %[ya]\n\ts_branch 3b\n"
                   "4:\n\tv_fmac_f32 v153, v152, %[yc]\n\tv_cvt_pk_bf16_f32 v153, v153, v153\n\tbuffer_store_short v153, %[oo], %[ro], 0 offen\n\t" DONE
                   "9:\n\t" PCP(112, 113, 132, 133) PCP(114, 115, 134, 135) PCP(116, 117, 136, 137) PCP(118, 119, 138, 139) PCP(120, 121, 140, 141) PCP(122, 123, 142, 143)
                   PCP(124, 125, 144, 145) PCP(126, 127, 146, 147) PLD(PL1) "s_sleep " STR(PNAP) "\n1:\n\t" PLE(PL1) PCK(4, "13", "14") "s_cbranch_vccz 2b\n\t" PLD(PL1) PCK(4, "11", "12")
                   "s_cbranch_vccz 2b\n\ts_cbranch_scc0 1b\n\ts_branch 2b\n0:"
                   : [n] "+s"(n_), "+{v[132:139]}"(pa), "+{v[140:147]}"(pb)
                   : [po] "v"(po), [p3] "v"(p3), [rs] "s"(rs), [k1] "s"(131072), [k2] "s"(262144), [pk] "v"(pick), [yf] "v"(yf), [ya] "v"(ya), "{v[160:163]}"(junk), [rl] "v"(rsel), [yc] "v"(yc),
                     [oo] "v"(oo), [ro] "s"(ro), [spin] "n"(SPIN)
                   : "vcc", "scc", "memory", "v112", "v113", "v114", "v115", "v116", "v117", "v118", "v119", "v120", "v121", "v122", "v123", "v124", "v125", "v126", "v127", "v150", "v151", "v152", "v153", "v154", "v155", "v156", "v157", "v158", "v159" TSD);
      TSGET(14, 2, 2, 2);
      STAMP(15);
    }
    FLUSH();
    __builtin_amdgcn_endpgm();
  }

  u32 pl, pa, pr, pi, ldtv;
  u32x4 tv, fx, junk;
  ROLE(c, "", , "={v[16:19]}"(tv), "={v[20:23]}"(fx), "={v24}"(ldtv), "={v96}"(pl), "={v97}"(pa), "={v68}"(pr), "={v69}"(pi), "={v[160:163]}"(junk));
  char* W = smem + w * CHR;
  __builtin_amdgcn_s_setprio(2);
  STAMP(3);
  const u32x4 zero4 = {0u, 0u, 0u, 0u};
#pragma unroll
  for (int i = 0; i < 4; i++) *(u32x4*)(W + (i < 3 ? TB + 1024 * i : CHR - 1024) + 16 * l) = zero4;
  SB();
  const u32 wb0 = lds0 + w * CHR, xb0 = lds0 + OX + w * XREG;
  asm volatile("s_mov_b32 m0, %0\n\ts_nop 0\n\tds_write_addtid_b32 %1\n\tds_write_addtid_b32 %1 offset:%2" ::"s"(wb0), "v"(0x3c00u), "n"(PB) : "memory");
  u32 ape = wb0 + (n16 == 12 ? 12 : (n16 ^ 3) - 7 * (n16 >> 3)) * RS + 16 * kg, apo = wb0 + (0x765412b0fdec2301ull >> 4 * n16 & 15) * RS + 16 * kg;
  u32x2 kb = {wb0 + ((15 - n16) & 3) * CPE + 2 * ((15 - n16) & 12) + 16 * kg, wb0 + (((n16 >> 2) & 1) + (n16 & 8) + 2 * (3 - (n16 & 3))) * RS + 16 * kg};
  u32x8 ka = {xb0 + 32 * n16 + 16 * kg, wb0 + OY + 32 * n16 + 8 * (kg ^ ((l >> 2) & 3)),
              lds0 + OXF + 4 * (l & 3), wb0 + TB + ((15 - n16) & 3) * CPO + 2 * ((15 - n16) & 12) + 8 * kg, lds0 + OB + 16 * l,
              l < 32 ? 0x01000100u : 0x03020100u, l < 32 ? 0x03020100u : 0x03020302u, (u32)(l >> 2) & 8u};
  u32 fad = lds0 + OYP + 4 * w, za = wb0 + 16 * kg;
  asm volatile("" : "+{v70}"(ape), "+{v71}"(apo), "+{v[28:35]}"(ka), "+{v[26:27]}"(kb), "+{v25}"(fad), "+{v12}"(za));
  STAMP(4);
  asm volatile("s_waitcnt vmcnt(4)" : "+{v24}"(ldtv), "+{v[160:163]}"(junk));
  float dt = rbt(fexp(hi2f(ldtv)));
  asm volatile("" : "+v"(dt));
  const f32x2 cdt = {rdl(dt * -1.4426950408889634f, 0), rdl(dt * 0.15915494309189535f, 0)};
  const u32 dk = (u32)__builtin_amdgcn_readfirstlane((int)junk.x) << 16, db = max(__float_as_uint(rdl(dt, 0)), 27u << 23);
  u32 em = max(dk >> 23 & 255u, 127u) + 119 - (db >> 23);
  asm("s_max_i32 %0, %0, 8\n\ts_min_u32 %0, %0, 0xfe" : "+s"(em) :: "scc");
  SB();
  STAMP(10);
  STAMPC(11);
  {
    u32 e, o0, o1, of, os;
    asm volatile(GEN_ASM
                 : [e] "=&s"(e), "=&{v93}"(o0), "=&{v46}"(o1), "=&{v9}"(of), "=&{v8}"(os), "+{v25}"(fad), "+{v12}"(za), "+{v68}"(pr), "+{v69}"(pi), "+{v70}"(ape),
                   "+{v71}"(apo), "+{v96}"(pl), "+{v97}"(pa), "+{v[28:35]}"(ka), "+{v[26:27]}"(kb), "+{v[16:19]}"(tv), "+{v[20:23]}"(fx)
                 : [w] "s"(wb0), [c] "s"(cdt), [k] "s"(f32x2{1.4426950408889634f, 128.f}), [em] "s"(em)
                 : "memory", "scc", "s6", "s7", GEN_CLOBBER);
    STAMP(1);
    STAMPC(12);
    const float ys = __uint_as_float((e - 5) << 23) * __uint_as_float(db);
    asm volatile("v_rcp_f32 %1, %2\n\ts_nop 0\n\tv_fma_f32 %1, %3, %1, %1\n\tv_fma_mixhi_f16 %0, %1, 1.0, %0 op_sel:[0,0,1] op_sel_hi:[0,0,1]\n\ts_nop 0"
                 : "+v"(o0), "=&v"(dt) : "v"(ys), "s"(dk));
    const u32 k0 = (u32)__builtin_amdgcn_readlane((int)o0, 3), k256 = (u32)__builtin_amdgcn_readlane((int)o1, 60);
    const u32 k512 = (u32)__builtin_amdgcn_readlane((int)os, 28), k1024 = (u32)__builtin_amdgcn_readlane((int)of, 28) >> 16;
    *(LDS u16*)(size_t)(wb0 + tv.z) = (u16)__builtin_amdgcn_perm((k512 & 0xffffu) | (k1024 << 16), (k0 >> 16) | (k256 << 16), tv.w);
    *(float*)(smem + OSC + 4 * w) = ys;
    fx &= (k1024 | (k1024 << 16)) ^ 0x80008000u;
  }
  asm volatile(CONV_ASM : "+{v[28:35]}"(ka) : "{v[26:27]}"(kb), "{v25}"(fad), "{v[48:51]}"(fx), [w] "s"(w) : "vcc", "scc", "s6", "memory", CONV_CLOBBER TSC);
  TSGET(2, 5, 6, 7);
  STAMP(13);
  STAMPC(15);
  FLUSH();
  __builtin_amdgcn_endpgm();
}

extern "C" void kernel_launch(void* const* d_in, const int* in_sizes, int n_in, void* d_out, int out_size,
                              void* d_ws, size_t ws_size, hipStream_t stream) {
  const u16* const* in = (const u16* const*)d_in;
  fused<<<256, 768, 0, stream>>>(in[0], in[1], in[2], in[3], in[4], in[5], in[6], in[8], in[9], in[10], in[11],
                                 (u16*)d_out, (char*)d_ws);
}
```

```cpp
#include <hip/hip_runtime.h>
#include <stdint.h>

#define STR2(x) #x
#define STR(x) STR2(x)
#define PH 1246
#define PPRIO 0
#define GDELAY 0
#define PRE 2
#define OAUX 0
#define PNT ""
#define LNNT ""
#define PRENT ""
#define PFN 15
#define PFO 512
#define QD 11
#define HNAP 4
#define QE 11
#define RSL 5
#define DPF 1
#define YN0 2
#define YN3 0
#define QS 7
#define QAS 6
#define RAMP 4
#define GNAP "s_sleep 1"
#define RNAP "s_sleep 1"
#define CLP 0
#define CSL 8
#define XSP 0
#define SPEC 0
#define S2L 1
#define HOLD 8
#define DPRIO 3
#define DPO 128
#define TH 590
#define CLD 245
#define IXI 1
#define LPRIO 0
#define DPI 2
#define PDROP 2
#define TE 0
#define XI 1
#define SCT " sc1"
#define SCU " sc1"
#define SCH " sc1"
#define TSG 280
#define SSL 5
#define PNAP 4
#define TW 310
#define TL 4352
typedef unsigned short u16;
typedef unsigned int u32;
typedef __attribute__((ext_vector_type(8))) __bf16 bf16x8;
typedef __attribute__((ext_vector_type(2))) __bf16 bf16x2;
typedef __attribute__((ext_vector_type(8))) _Float16 f16x8;
typedef __attribute__((ext_vector_type(2))) _Float16 f16x2;
typedef __attribute__((ext_vector_type(4))) short v4i16;
typedef __attribute__((ext_vector_type(4))) float f32x4;
typedef __attribute__((ext_vector_type(2))) float f32x2;
typedef __attribute__((ext_vector_type(4))) u32 u32x4;
typedef __attribute__((ext_vector_type(2))) u32 u32x2;
typedef __attribute__((ext_vector_type(8))) u32 u32x8;
typedef __attribute__((ext_vector_type(2), aligned(4))) u32 u32x2_a4;
typedef __amdgpu_buffer_rsrc_t rsrc_t;
#define LDS __attribute__((address_space(3)))

#define TLARG
#define TLVAL
#define STAMP(I)
#define STAMPC(I)
#define FLUSH()

constexpr int L = 2048, D = 1024, NM = 128, F = 3584;
constexpr u32 O_ARR = 0x1000, O_CLR = 0x2000, O_RG = 0x4000, O_SG = 0x8000, O_IDX = 0x10000, O_HG = 0x20000,
              O_PG = 0xd0000, O_TOP = 0x140000, TOPS = 8 * TL, WS_END = O_TOP + 2 * TOPS;
constexpr int SPIN = 1 << 16;

__device__ __forceinline__ float lo2f(u32 w) { return __uint_as_float(w << 16); }
__device__ __forceinline__ float hi2f(u32 w) { return __uint_as_float(w & 0xffff0000u); }
__device__ __forceinline__ u32 pack2(float a, float b) {
  bf16x2 r = {(__bf16)a, (__bf16)b};
  return __builtin_bit_cast(u32, r);
}
__device__ __forceinline__ u32 packh(float a, float b) {
  f16x2 r = {(_Float16)a, (_Float16)b};
  return __builtin_bit_cast(u32, r);
}
__device__ __forceinline__ float rbt(float f) { return __uint_as_float(pack2(0.f, f)); }
__device__ __forceinline__ float rb(float f) {
  float r;
  asm("v_cvt_pk_bf16_f32 %0, 0, %1" : "=v"(r) : "v"(f));
  return r;
}

template <int CTRL, int RM>
__device__ __forceinline__ float dppf(float v) {
  return __builtin_bit_cast(float, __builtin_amdgcn_update_dpp(0, __builtin_bit_cast(int, v), CTRL, RM, 0xf, true));
}
__device__ __forceinline__ float rdl(float v, int i) {
  return __builtin_bit_cast(float, __builtin_amdgcn_readlane(__builtin_bit_cast(int, v), i));
}
__device__ __forceinline__ void swap32(float& a, float& b) {
  auto r = __builtin_amdgcn_permlane32_swap(__float_as_uint(a), __float_as_uint(b), false, false);
  a = __uint_as_float(r[0]); b = __uint_as_float(r[1]);
}

__device__ __forceinline__ u32x4 ld16(rsrc_t r, u32 off) { return __builtin_amdgcn_raw_buffer_load_b128(r, off, 0, 16); }
__device__ __forceinline__ void st8(rsrc_t r, u32 off, u32x2 v) { __builtin_amdgcn_raw_buffer_store_b64(v, r, off, 0, 16); }
#define SB() __builtin_amdgcn_sched_barrier(0)

__device__ __forceinline__ float fexp(float v) { return __builtin_amdgcn_exp2f(v * 1.4426950408889634f); }
__device__ __forceinline__ float dot2b(u32 a, u32 b, float c) {
  return __builtin_amdgcn_fdot2_f32_bf16(__builtin_bit_cast(bf16x2, a), __builtin_bit_cast(bf16x2, b), c, false);
}
__device__ __forceinline__ float sigm(float v) { return __builtin_amdgcn_rcpf(1.f + __builtin_amdgcn_exp2f(-1.4426950408889634f * v)); }

constexpr int RS = 288, PB = 16 * RS;
constexpr int CPE = 1216, TB = 2 * PB, CPO = 832;
constexpr int T2B = 4, T3B = 224, C2 = 52, C3 = 84;
constexpr int OY = 0, CHR = TB + 4 * CPO;
constexpr int OX = 8 * CHR, XREG = 4640, XO = 266;
constexpr int OMISC = OX + 8 * XREG;
constexpr int OXF = OMISC + 320, OSC = OMISC + 352, OYP = OMISC + 448;
constexpr int OB = OMISC + 1024, SMEM = 148480;
constexpr int FA = 131072, FH = FA + 8192, HP = 576, FC = FH + 8 * HP;
static_assert(FC + 1024 <= SMEM && OB + 4096 <= SMEM, "lds map");

struct Tab { u32 v[6][64][4]; };
constexpr Tab mktab() {
  Tab T = {};
  for (int l = 0; l < 64; l++) {
    const int n16 = l & 15, kg = l >> 4, pc = ((n16 >> 2) & 1) + (n16 & 8) + 2 * (3 - (n16 & 3)), odd = kg >> 1, s = l & 3;
    const int e0 = 534 - 258 - 128 - 8 * pc - 4 * (kg & 1) - odd;
    const int ez = ((l >> 3) & 3) * CPE + ((l & 4) ? 1056 - 64 : 0) + 16 * (l & 7);
    const int b2 = T2B + 44 + C2 * ((kg & 1) + 2 * (pc & 1)), b3 = T3B + 60 + C3 * (pc & 1) - 32 * (kg & 1);
    const int to = TB + 2 * ((odd ? b3 : b2) + 3 - (pc >> 1) - 8 - 16);
    const int q = (l >> 2) & 7, tap = q == 0 ? ((s & 1) ? 0 : 256) : q == 1 ? 257 : q == 7 ? 0 : 509 + q;
    const int sp = q != 6 ? s * (CPE - 2) + 2 * (537 - tap) : TB + s * (CPO - 2) + 2 * (T2B + 15);
    T.v[0][l][0] = (2 * e0 + (2 - odd) * (CPE - 2)) | (2 * e0 + 3 * odd * (CPE - 2)) << 16;
    T.v[0][l][1] = ez | to << 16;
    T.v[0][l][2] = sp;
    T.v[0][l][3] = !tap ? 0x0100u : q == 6 ? 0x0706u : q >= 2 ? 0x0504u : 0x0302u;
    for (int k = 0; k < 8; k++) {
      const int d = 8 * kg + k - n16;
      if (d == 13) T.v[1][l][k >> 1] |= 0xffffu << 16 * (k & 1);
      for (int h = 1; h <= 4; h++)
        if (d >= (h == 1 ? 14 : 10) - h && d <= (h == 1 ? 14 : 10) + h) T.v[1 + h][l][k >> 1] |= 0x3c00u << 16 * (k & 1);
    }
  }
  return T;
}
extern "C" __device__ const __attribute__((aligned(128), used, visibility("hidden"))) Tab lane_tab = mktab();

#define GEN_ASM \
  "s_waitcnt vmcnt(2)\n\tv_lshlrev_b32 v56, 16, v96\n\t" \
  "v_and_b32 v57, 0xffff0000, v96\n\tv_pk_mul_f32 v[56:57], %[k], v[56:57] op_sel_hi:[0,1]\n\t" \
  "v_lshlrev_b32 v49, 16, v97\n\tv_exp_f32 v56, v56\n\t" \
  "v_exp_f32 v57, v57\n\tv_and_b32 v51, 0xffff0000, v97\n\t" \
  "v_cvt_pk_bf16_f32 v56, v56, v57\n\tv_lshlrev_b32 v48, 16, v56\n\t" \
  "v_and_b32 v50, 0xffff0000, v56\n\tv_pk_mul_f32 v[48:49], %[c], v[48:49]\n\t" \
  "v_pk_mul_f32 v[50:51], %[c], v[50:51]\n\tv_pk_mul_f32 v[98:99], %[k], v[48:49] op_sel:[1,0]\n\t" \
  "v_pk_mul_f32 v[100:101], %[k], v[50:51] op_sel:[1,0]\n\tv_fract_f32 v49, v49\n\t" \
  "v_fract_f32 v51, v51\n\tv_exp_f32 v48, v48\n\t" \
  "v_exp_f32 v50, v50\n\tv_cos_f32 v40, v49\n\t" \
  "v_sin_f32 v41, v49\n\tv_cos_f32 v42, v51\n\t" \
  "v_sin_f32 v43, v51\n\tv_pk_mul_f32 v[40:41], v[40:41], v[48:49] op_sel_hi:[1,0]\n\t" \
  "v_pk_mul_f32 v[42:43], v[42:43], v[50:51] op_sel_hi:[1,0]\n\tv_pk_mul_f32 v[48:49], v[40:41], v[40:41] op_sel:[1,1] op_sel_hi:[1,0] neg_lo:[1,0]\n\t" \
  "v_pk_mul_f32 v[50:51], v[42:43], v[42:43] op_sel:[1,1] op_sel_hi:[1,0] neg_lo:[1,0]\n\tv_pk_fma_f32 v[48:49], v[40:41], v[40:41], v[48:49] op_sel:[0,0,0] op_sel_hi:[0,1,1]\n\t" \
  "v_pk_fma_f32 v[50:51], v[42:43], v[42:43], v[50:51] op_sel:[0,0,0] op_sel_hi:[0,1,1]\n\tv_cvt_pk_f16_f32 v72, v40, -v41\n\t" \
  "v_cvt_pk_f16_f32 v73, v42, -v43\n\tv_pk_mul_f32 v[52:53], v[48:49], v[40:41] op_sel:[1,1] op_sel_hi:[1,0] neg_lo:[1,0]\n\t" \
  "v_pk_mul_f32 v[54:55], v[50:51], v[42:43] op_sel:[1,1] op_sel_hi:[1,0] neg_lo:[1,0]\n\tv_pk_fma_f32 v[52:53], v[48:49], v[40:41], v[52:53] op_sel:[0,0,0] op_sel_hi:[0,1,1]\n\t" \
  "v_pk_fma_f32 v[54:55], v[50:51], v[42:43], v[54:55] op_sel:[0,0,0] op_sel_hi:[0,1,1]\n\tv_pk_mul_f32 v[56:57], v[48:49], v[48:49] op_sel:[1,1] op_sel_hi:[1,0] neg_lo:[1,0]\n\t" \
  "v_pk_mul_f32 v[58:59], v[50:51], v[50:51] op_sel:[1,1] op_sel_hi:[1,0] neg_lo:[1,0]\n\tv_pk_fma_f32 v[56:57], v[48:49], v[48:49], v[56:57] op_sel:[0,0,0] op_sel_hi:[0,1,1]\n\t" \
  "v_pk_fma_f32 v[58:59], v[50:51], v[50:51], v[58:59] op_sel:[0,0,0] op_sel_hi:[0,1,1]\n\tv_cvt_pk_f16_f32 v74, v48, -v49\n\t" \
  "v_cvt_pk_f16_f32 v75, v50, -v51\n\tv_cvt_pk_f16_f32 v76, v52, -v53\n\t" \
  "v_cvt_pk_f16_f32 v77, v54, -v55\n\tv_pk_mul_f32 v[52:53], v[56:57], v[48:49] op_sel:[1,1] op_sel_hi:[1,0] neg_lo:[1,0]\n\t" \
  "v_pk_mul_f32 v[54:55], v[58:59], v[50:51] op_sel:[1,1] op_sel_hi:[1,0] neg_lo:[1,0]\n\tv_pk_fma_f32 v[52:53], v[56:57], v[48:49], v[52:53] op_sel:[0,0,0] op_sel_hi:[0,1,1]\n\t" \
  "v_pk_fma_f32 v[54:55], v[58:59], v[50:51], v[54:55] op_sel:[0,0,0] op_sel_hi:[0,1,1]\n\tv_pk_mul_f32 v[60:61], v[56:57], v[56:57] op_sel:[1,1] op_sel_hi:[1,0] neg_lo:[1,0]\n\t" \
  "v_pk_mul_f32 v[62:63], v[58:59], v[58:59] op_sel:[1,1] op_sel_hi:[1,0] neg_lo:[1,0]\n\tv_pk_fma_f32 v[60:61], v[56:57], v[56:57], v[60:61] op_sel:[0,0,0] op_sel_hi:[0,1,1]\n\t" \
  "v_pk_fma_f32 v[62:63], v[58:59], v[58:59], v[62:63] op_sel:[0,0,0] op_sel_hi:[0,1,1]\n\tv_cvt_pk_f16_f32 v78, v56, -v57\n\t" \
  "v_cvt_pk_f16_f32 v79, v58, -v59\n\tv_cvt_pk_f16_f32 v82, v52, -v53\n\t" \
  "v_cvt_pk_f16_f32 v83, v54, -v55\n\tv_pk_mul_f16 v80, v78, v72 op_sel:[1,1] op_sel_hi:[1,0] neg_lo:[1,0]\n\t" \
  "v_pk_mul_f16 v81, v79, v73 op_sel:[1,1] op_sel_hi:[1,0] neg_lo:[1,0]\n\tv_pk_fma_f16 v80, v78, v72, v80 op_sel:[0,0,0] op_sel_hi:[0,1,1]\n\t" \
  "v_pk_fma_f16 v81, v79, v73, v81 op_sel:[0,0,0] op_sel_hi:[0,1,1]\n\tv_pk_mul_f16 v84, v78, v76 op_sel:[1,1] op_sel_hi:[1,0] neg_lo:[1,0]\n\t" \
  "v_pk_mul_f16 v85, v79, v77 op_sel:[1,1] op_sel_hi:[1,0] neg_lo:[1,0]\n\tv_pk_fma_f16 v84, v78, v76, v84 op_sel:[0,0,0] op_sel_hi:[0,1,1]\n\t" \
  "v_pk_fma_f16 v85, v79, v77, v85 op_sel:[0,0,0] op_sel_hi:[0,1,1]\n\tv_cvt_pk_f16_f32 v86, v60, -v61\n\t" \
  "v_cvt_pk_f16_f32 v87, v62, -v63\n\tds_write_addtid_b32 v72 offset:288\n\t" \
  "ds_write_addtid_b32 v73 offset:4896\n\tds_write_addtid_b32 v74 offset:576\n\t" \
  "ds_write_addtid_b32 v75 offset:5184\n\tds_write_addtid_b32 v76 offset:864\n\t" \
  "ds_write_addtid_b32 v77 offset:5472\n\tds_write_addtid_b32 v78 offset:1152\n\t" \
  "ds_write_addtid_b32 v79 offset:5760\n\tds_write_addtid_b32 v80 offset:1440\n\t" \
  "ds_write_addtid_b32 v81 offset:6048\n\tds_write_addtid_b32 v82 offset:1728\n\t" \
  "ds_write_addtid_b32 v83 offset:6336\n\tds_write_addtid_b32 v84 offset:2016\n\t" \
  "ds_write_addtid_b32 v85 offset:6624\n\tds_write_addtid_b32 v86 offset:3456\n\t" \
  "ds_write_addtid_b32 v87 offset:8064\n\tds_read_b128 v[104:107], v70 offset:0\n\t" \
  "ds_read_b128 v[108:111], v70 offset:64\n\tds_read_b128 v[112:115], v70 offset:128\n\t" \
  "ds_read_b128 v[116:119], v70 offset:192\n\tds_read_b128 v[120:123], v70 offset:4608\n\t" \
  "ds_read_b128 v[124:127], v70 offset:4672\n\tds_read_b128 v[128:131], v70 offset:4736\n\t" \
  "ds_read_b128 v[132:135], v70 offset:4800\n\tv_fract_f32 v99, v99\n\t" \
  "v_fract_f32 v101, v101\n\tv_exp_f32 v98, v98\n\t" \
  "v_exp_f32 v100, v100\n\tv_cos_f32 v40, v99\n\t" \
  "v_sin_f32 v41, v99\n\tv_cos_f32 v42, v101\n\t" \
  "v_sin_f32 v43, v101\n\tv_pk_mul_f32 v[40:41], v[40:41], v[98:99] op_sel_hi:[1,0]\n\t" \
  "v_pk_mul_f32 v[42:43], v[42:43], v[100:101] op_sel_hi:[1,0]\n\tv_pk_mul_f32 v[44:45], v[40:41], v[40:41] op_sel:[1,1] op_sel_hi:[1,0] neg_lo:[1,0]\n\t" \
  "v_pk_mul_f32 v[46:47], v[42:43], v[42:43] op_sel:[1,1] op_sel_hi:[1,0] neg_lo:[1,0]\n\tv_pk_fma_f32 v[44:45], v[40:41], v[40:41], v[44:45] op_sel:[0,0,0] op_sel_hi:[0,1,1]\n\t" \
  "v_pk_fma_f32 v[46:47], v[42:43], v[42:43], v[46:47] op_sel:[0,0,0] op_sel_hi:[0,1,1]\n\tv_cvt_pk_f16_f32 v36, v40, v41\n\t" \
  "v_cvt_pk_f16_f32 v37, v42, v43\n\ts_waitcnt vmcnt(0)\n\t" \
  "v_pk_mul_f32 v[48:49], v[44:45], v[44:45] op_sel:[1,1] op_sel_hi:[1,0] neg_lo:[1,0]\n\tv_pk_mul_f32 v[50:51], v[46:47], v[46:47] op_sel:[1,1] op_sel_hi:[1,0] neg_lo:[1,0]\n\t" \
  "v_pk_fma_f32 v[48:49], v[44:45], v[44:45], v[48:49] op_sel:[0,0,0] op_sel_hi:[0,1,1]\n\tv_pk_fma_f32 v[50:51], v[46:47], v[46:47], v[50:51] op_sel:[0,0,0] op_sel_hi:[0,1,1]\n\t" \
  "v_lshlrev_b32 v4, 16, v68\n\tv_cvt_pk_f16_f32 v88, v44, -v45\n\t" \
  "v_cvt_pk_f16_f32 v89, v46, -v47\n\tv_pk_mul_f32 v[52:53], v[48:49], v[44:45] op_sel:[1,1] op_sel_hi:[1,0] neg_lo:[1,0]\n\t" \
  "v_pk_mul_f32 v[54:55], v[50:51], v[46:47] op_sel:[1,1] op_sel_hi:[1,0] neg_lo:[1,0]\n\tv_lshlrev_b32 v5, 16, v69\n\t" \
  "v_pk_fma_f32 v[52:53], v[48:49], v[44:45], v[52:53] op_sel:[0,0,0] op_sel_hi:[0,1,1]\n\tv_pk_fma_f32 v[54:55], v[50:51], v[46:47], v[54:55] op_sel:[0,0,0] op_sel_hi:[0,1,1]\n\t" \
  "v_pk_mul_f32 v[56:57], v[48:49], v[48:49] op_sel:[1,1] op_sel_hi:[1,0] neg_lo:[1,0]\n\tv_pk_mul_f32 v[58:59], v[50:51], v[50:51] op_sel:[1,1] op_sel_hi:[1,0] neg_lo:[1,0]\n\t" \
  "v_and_b32 v6, 0xffff0000, v68\n\tv_pk_fma_f32 v[56:57], v[48:49], v[48:49], v[56:57] op_sel:[0,0,0] op_sel_hi:[0,1,1]\n\t" \
  "v_pk_fma_f32 v[58:59], v[50:51], v[50:51], v[58:59] op_sel:[0,0,0] op_sel_hi:[0,1,1]\n\tv_cvt_pk_f16_f32 v90, v48, -v49\n\t" \
  "v_cvt_pk_f16_f32 v91, v50, -v51\n\tv_and_b32 v7, 0xffff0000, v69\n\t" \
  "v_pk_mul_f32 v[64:65], v[56:57], v[60:61] op_sel:[1,1] op_sel_hi:[1,0] neg_lo:[1,0]\n\tv_pk_mul_f32 v[66:67], v[58:59], v[62:63] op_sel:[1,1] op_sel_hi:[1,0] neg_lo:[1,0]\n\t" \
  "v_pk_fma_f32 v[64:65], v[56:57], v[60:61], v[64:65] op_sel:[0,0,0] op_sel_hi:[0,1,1]\n\tv_pk_fma_f32 v[66:67], v[58:59], v[62:63], v[66:67] op_sel:[0,0,0] op_sel_hi:[0,1,1]\n\t" \
  "v_max3_f32 v102, |v4|, |v5|, |v6|\n\tv_cvt_pk_f16_f32 v92, v52, -v53\n\t" \
  "v_cvt_pk_f16_f32 v93, v54, -v55\n\tv_pk_mul_f16 v136, v88, v74 op_sel:[1,1] op_sel_hi:[1,0] neg_lo:[1,0]\n\t" \
  "v_pk_mul_f16 v137, v89, v75 op_sel:[1,1] op_sel_hi:[1,0] neg_lo:[1,0]\n\tv_max_f32 v102, v102, |v7|\n\t" \
  "v_pk_fma_f16 v136, v88, v74, v136 op_sel:[0,0,0] op_sel_hi:[0,1,1]\n\tv_pk_fma_f16 v137, v89, v75, v137 op_sel:[0,0,0] op_sel_hi:[0,1,1]\n\t" \
  "v_pk_mul_f16 v138, v88, v78 op_sel:[1,1] op_sel_hi:[1,0] neg_lo:[1,0]\n\tv_pk_mul_f16 v139, v89, v79 op_sel:[1,1] op_sel_hi:[1,0] neg_lo:[1,0]\n\t" \
  "v_max_u32_dpp v102, v102, v102 row_shr:1 row_mask:0xf bank_mask:0xf\n\tv_pk_fma_f16 v138, v88, v78, v138 op_sel:[0,0,0] op_sel_hi:[0,1,1]\n\t" \
  "v_pk_fma_f16 v139, v89, v79, v139 op_sel:[0,0,0] op_sel_hi:[0,1,1]\n\tv_cvt_pk_f16_f32 v94, v64, -v65\n\t" \
  "v_cvt_pk_f16_f32 v95, v66, -v67\n\tv_max_u32_dpp v102, v102, v102 row_shr:2 row_mask:0xf bank_mask:0xf\n\t" \
  "v_pk_mul_f16 v140, v88, v82 op_sel:[1,1] op_sel_hi:[1,0] neg_lo:[1,0]\n\tv_pk_mul_f16 v141, v89, v83 op_sel:[1,1] op_sel_hi:[1,0] neg_lo:[1,0]\n\t" \
  "v_pk_fma_f16 v140, v88, v82, v140 op_sel:[0,0,0] op_sel_hi:[0,1,1]\n\tv_pk_fma_f16 v141, v89, v83, v141 op_sel:[0,0,0] op_sel_hi:[0,1,1]\n\t" \
  "ds_write_addtid_b32 v88 offset:3168\n\tds_write_addtid_b32 v89 offset:7776\n\t" \
  "ds_write_addtid_b32 v136 offset:0\n\tds_write_addtid_b32 v137 offset:4608\n\t" \
  "ds_write_addtid_b32 v138 offset:288\n\tds_write_addtid_b32 v139 offset:4896\n\t" \
  "ds_write_addtid_b32 v94 offset:1152\n\tds_write_addtid_b32 v95 offset:5760\n\t" \
  "ds_write_addtid_b32 v140 offset:576\n\tds_write_addtid_b32 v141 offset:5184\n\t" \
  "v_max_u32_dpp v102, v102, v102 row_shr:4 row_mask:0xf bank_mask:0xf\n\tv_pk_mul_f16 v142, v88, v86 op_sel:[1,1] op_sel_hi:[1,0] neg_lo:[1,0]\n\t" \
  "v_pk_mul_f16 v143, v89, v87 op_sel:[1,1] op_sel_hi:[1,0] neg_lo:[1,0]\n\tv_pk_fma_f16 v142, v88, v86, v142 op_sel:[0,0,0] op_sel_hi:[0,1,1]\n\t" \
  "v_pk_fma_f16 v143, v89, v87, v143 op_sel:[0,0,0] op_sel_hi:[0,1,1]\n\tv_max_u32_dpp v102, v102, v102 row_shr:8 row_mask:0xf bank_mask:0xf\n\t" \
  "v_pk_mul_f16 v144, v90, v78 op_sel:[1,1] op_sel_hi:[1,0] neg_lo:[1,0]\n\tv_pk_mul_f16 v145, v91, v79 op_sel:[1,1] op_sel_hi:[1,0] neg_lo:[1,0]\n\t" \
  "v_pk_fma_f16 v144, v90, v78, v144 op_sel:[0,0,0] op_sel_hi:[0,1,1]\n\tv_pk_fma_f16 v145, v91, v79, v145 op_sel:[0,0,0] op_sel_hi:[0,1,1]\n\t" \
  "v_max_u32_dpp v102, v102, v102 row_bcast:15 row_mask:0xa bank_mask:0xf\n\tv_pk_mul_f16 v146, v90, v86 op_sel:[1,1] op_sel_hi:[1,0] neg_lo:[1,0]\n\t" \
  "v_pk_mul_f16 v147, v91, v87 op_sel:[1,1] op_sel_hi:[1,0] neg_lo:[1,0]\n\tv_pk_fma_f16 v146, v90, v86, v146 op_sel:[0,0,0] op_sel_hi:[0,1,1]\n\t" \
  "v_pk_fma_f16 v147, v91, v87, v147 op_sel:[0,0,0] op_sel_hi:[0,1,1]\n\tv_max_u32_dpp v102, v102, v102 row_bcast:31 row_mask:0xc bank_mask:0xf\n\t" \
  "v_pk_mul_f16 v148, v92, v78 op_sel:[1,1] op_sel_hi:[1,0] neg_lo:[1,0]\n\tv_pk_mul_f16 v149, v93, v79 op_sel:[1,1] op_sel_hi:[1,0] neg_lo:[1,0]\n\t" \
  "v_pk_fma_f16 v148, v92, v78, v148 op_sel:[0,0,0] op_sel_hi:[0,1,1]\n\tv_pk_fma_f16 v149, v93, v79, v149 op_sel:[0,0,0] op_sel_hi:[0,1,1]\n\t" \
  "v_readlane_b32 %[e], v102, 63\n\tv_pk_mul_f16 v150, v92, v86 op_sel:[1,1] op_sel_hi:[1,0] neg_lo:[1,0]\n\t" \
  "v_pk_mul_f16 v151, v93, v87 op_sel:[1,1] op_sel_hi:[1,0] neg_lo:[1,0]\n\tv_pk_fma_f16 v150, v92, v86, v150 op_sel:[0,0,0] op_sel_hi:[0,1,1]\n\t" \
  "v_pk_fma_f16 v151, v93, v87, v151 op_sel:[0,0,0] op_sel_hi:[0,1,1]\n\ts_lshr_b32 %[e], %[e], 23\n\t" \
  "s_max_u32 %[e], %[e], %[em]\n\ts_sub_u32 s6, 260, %[e]\n\t" \
  "s_lshl_b32 s6, s6, 23\n\tds_write_addtid_b32 v142 offset:864\n\t" \
  "ds_write_addtid_b32 v143 offset:5472\n\tds_write_addtid_b32 v144 offset:3456\n\t" \
  "ds_write_addtid_b32 v145 offset:8064\n\tds_write_addtid_b32 v146 offset:3744\n\t" \
  "ds_write_addtid_b32 v147 offset:8352\n\tds_write_addtid_b32 v148 offset:4032\n\t" \
  "ds_write_addtid_b32 v149 offset:8640\n\tds_write_addtid_b32 v150 offset:4320\n\t" \
  "ds_write_addtid_b32 v151 offset:8928\n\tv_pk_mul_f16 v96, v94, v88 op_sel:[1,1] op_sel_hi:[1,0] neg_lo:[1,0]\n\t" \
  "v_pk_mul_f16 v97, v95, v89 op_sel:[1,1] op_sel_hi:[1,0] neg_lo:[1,0]\n\tv_pk_fma_f16 v96, v94, v88, v96 op_sel:[0,0,0] op_sel_hi:[0,1,1]\n\t" \
  "v_pk_fma_f16 v97, v95, v89, v97 op_sel:[0,0,0] op_sel_hi:[0,1,1]\n\tv_pk_mul_f16 v98, v94, v90 op_sel:[1,1] op_sel_hi:[1,0] neg_lo:[1,0]\n\t" \
  "v_pk_mul_f16 v99, v95, v91 op_sel:[1,1] op_sel_hi:[1,0] neg_lo:[1,0]\n\tv_pk_fma_f16 v98, v94, v90, v98 op_sel:[0,0,0] op_sel_hi:[0,1,1]\n\t" \
  "v_pk_fma_f16 v99, v95, v91, v99 op_sel:[0,0,0] op_sel_hi:[0,1,1]\n\tv_pk_mul_f16 v100, v94, v92 op_sel:[1,1] op_sel_hi:[1,0] neg_lo:[1,0]\n\t" \
  "v_pk_mul_f16 v101, v95, v93 op_sel:[1,1] op_sel_hi:[1,0] neg_lo:[1,0]\n\tv_pk_fma_f16 v100, v94, v92, v100 op_sel:[0,0,0] op_sel_hi:[0,1,1]\n\t" \
  "v_pk_fma_f16 v101, v95, v93, v101 op_sel:[0,0,0] op_sel_hi:[0,1,1]\n\tds_write_addtid_b32 v96 offset:1440\n\t" \
  "ds_write_addtid_b32 v97 offset:6048\n\tds_write_addtid_b32 v98 offset:1728\n\t" \
  "ds_write_addtid_b32 v99 offset:6336\n\tds_write_addtid_b32 v100 offset:2016\n\t" \
  "ds_write_addtid_b32 v101 offset:6624\n\tds_read_b128 v[136:139], v71 offset:0\n\t" \
  "ds_read_b128 v[140:143], v71 offset:64\n\tds_read_b128 v[144:147], v71 offset:128\n\t" \
  "s_cmp_lt_u32 %[w], 50176\n\ts_cbranch_scc0 1f\n\t" \
  "s_setprio 1\n\t1:\n\t" \
  "ds_read_b128 v[148:151], v71 offset:192\n\tds_read_b128 v[152:155], v71 offset:4608\n\t" \
  "ds_read_b128 v[156:159], v71 offset:4672\n\tds_read_b128 v[160:163], v71 offset:4736\n\t" \
  "ds_read_b128 v[164:167], v71 offset:4800\n\tv_pk_mul_f32 v[48:49], v[60:61], v[60:61] op_sel:[1,1] op_sel_hi:[1,0] neg_lo:[1,0]\n\t" \
  "v_pk_mul_f32 v[50:51], v[62:63], v[62:63] op_sel:[1,1] op_sel_hi:[1,0] neg_lo:[1,0]\n\tv_pk_fma_f32 v[48:49], v[60:61], v[60:61], v[48:49] op_sel:[0,0,0] op_sel_hi:[0,1,1]\n\t" \
  "v_pk_fma_f32 v[50:51], v[62:63], v[62:63], v[50:51] op_sel:[0,0,0] op_sel_hi:[0,1,1]\n\tv_pk_mul_f32 v[52:53], v[48:49], v[60:61] op_sel:[1,1] op_sel_hi:[1,0] neg_lo:[1,0]\n\t" \
  "v_pk_mul_f32 v[54:55], v[50:51], v[62:63] op_sel:[1,1] op_sel_hi:[1,0] neg_lo:[1,0]\n\tv_pk_fma_f32 v[52:53], v[48:49], v[60:61], v[52:53] op_sel:[0,0,0] op_sel_hi:[0,1,1]\n\t" \
  "v_pk_fma_f32 v[54:55], v[50:51], v[62:63], v[54:55] op_sel:[0,0,0] op_sel_hi:[0,1,1]\n\tv_pk_mul_f32 v[40:41], v[48:49], v[48:49] op_sel:[1,1] op_sel_hi:[1,0] neg_lo:[1,0]\n\t" \
  "v_pk_mul_f32 v[42:43], v[50:51], v[50:51] op_sel:[1,1] op_sel_hi:[1,0] neg_lo:[1,0]\n\tv_pk_fma_f32 v[40:41], v[48:49], v[48:49], v[40:41] op_sel:[0,0,0] op_sel_hi:[0,1,1]\n\t" \
  "v_pk_fma_f32 v[42:43], v[50:51], v[50:51], v[42:43] op_sel:[0,0,0] op_sel_hi:[0,1,1]\n\tv_cvt_pk_f16_f32 v46, v48, v49\n\t" \
  "v_cvt_pk_f16_f32 v47, v50, v51\n\tv_cvt_pk_f16_f32 v56, v52, v53\n\t" \
  "v_cvt_pk_f16_f32 v57, v54, v55\n\tv_pk_mul_f32 v[64:65], s[6:7], v[4:5] op_sel_hi:[0,1]\n\t" \
  "v_pk_mul_f32 v[66:67], s[6:7], v[6:7] op_sel_hi:[0,1]\n\tv_cvt_pk_f16_f32 v48, v64, v65\n\t" \
  "v_cvt_pk_f16_f32 v49, v66, v67\n\tv_pk_mul_f32 v[68:69], v[64:65], v[40:41] op_sel:[1,1] op_sel_hi:[1,0] neg_lo:[1,0]\n\t" \
  "v_pk_mul_f32 v[70:71], v[66:67], v[42:43] op_sel:[1,1] op_sel_hi:[1,0] neg_lo:[1,0]\n\tv_pk_fma_f32 v[68:69], v[64:65], v[40:41], v[68:69] op_sel:[0,0,0] op_sel_hi:[0,1,1]\n\t" \
  "v_pk_fma_f32 v[70:71], v[66:67], v[42:43], v[70:71] op_sel:[0,0,0] op_sel_hi:[0,1,1]\n\tv_pk_mul_f16 v50, v48, v86 op_sel:[1,1] op_sel_hi:[1,0]\n\t" \
  "v_pk_mul_f16 v51, v49, v87 op_sel:[1,1] op_sel_hi:[1,0]\n\tv_pk_fma_f16 v50, v48, v86, v50 op_sel:[0,0,0] op_sel_hi:[0,1,1] neg_hi:[0,1,0]\n\t" \
  "v_pk_fma_f16 v51, v49, v87, v51 op_sel:[0,0,0] op_sel_hi:[0,1,1] neg_hi:[0,1,0]\n\tv_pk_mul_f16 v52, v48, v46 op_sel:[1,1] op_sel_hi:[1,0] neg_lo:[1,0]\n\t" \
  "v_pk_mul_f16 v53, v49, v47 op_sel:[1,1] op_sel_hi:[1,0] neg_lo:[1,0]\n\tv_pk_fma_f16 v52, v48, v46, v52 op_sel:[0,0,0] op_sel_hi:[0,1,1]\n\t" \
  "v_pk_fma_f16 v53, v49, v47, v53 op_sel:[0,0,0] op_sel_hi:[0,1,1]\n\tv_pk_mul_f16 v54, v48, v56 op_sel:[1,1] op_sel_hi:[1,0] neg_lo:[1,0]\n\t" \
  "v_pk_mul_f16 v55, v49, v57 op_sel:[1,1] op_sel_hi:[1,0] neg_lo:[1,0]\n\tv_pk_fma_f16 v54, v48, v56, v54 op_sel:[0,0,0] op_sel_hi:[0,1,1]\n\t" \
  "v_pk_fma_f16 v55, v49, v57, v55 op_sel:[0,0,0] op_sel_hi:[0,1,1]\n\tds_write_addtid_b32 v48 offset:0\n\t" \
  "ds_write_addtid_b32 v49 offset:4608\n\tds_write_addtid_b32 v50 offset:288\n\t" \
  "ds_write_addtid_b32 v51 offset:4896\n\tds_write_addtid_b32 v52 offset:576\n\t" \
  "ds_write_addtid_b32 v53 offset:5184\n\tds_write_addtid_b32 v54 offset:864\n\t" \
  "ds_write_addtid_b32 v55 offset:5472\n\tv_cvt_pk_f16_f32 v48, v68, v69\n\t" \
  "v_cvt_pk_f16_f32 v49, v70, v71\n\tv_pk_mul_f32 v[64:65], v[68:69], v[40:41] op_sel:[1,1] op_sel_hi:[1,0] neg_lo:[1,0]\n\t" \
  "v_pk_mul_f32 v[66:67], v[70:71], v[42:43] op_sel:[1,1] op_sel_hi:[1,0] neg_lo:[1,0]\n\tv_pk_fma_f32 v[64:65], v[68:69], v[40:41], v[64:65] op_sel:[0,0,0] op_sel_hi:[0,1,1]\n\t" \
  "v_pk_fma_f32 v[66:67], v[70:71], v[42:43], v[66:67] op_sel:[0,0,0] op_sel_hi:[0,1,1]\n\tv_pk_mul_f16 v50, v48, v86 op_sel:[1,1] op_sel_hi:[1,0]\n\t" \
  "v_pk_mul_f16 v51, v49, v87 op_sel:[1,1] op_sel_hi:[1,0]\n\tv_pk_fma_f16 v50, v48, v86, v50 op_sel:[0,0,0] op_sel_hi:[0,1,1] neg_hi:[0,1,0]\n\t" \
  "v_pk_fma_f16 v51, v49, v87, v51 op_sel:[0,0,0] op_sel_hi:[0,1,1] neg_hi:[0,1,0]\n\tv_pk_mul_f16 v52, v48, v46 op_sel:[1,1] op_sel_hi:[1,0] neg_lo:[1,0]\n\t" \
  "v_pk_mul_f16 v53, v49, v47 op_sel:[1,1] op_sel_hi:[1,0] neg_lo:[1,0]\n\tv_pk_fma_f16 v52, v48, v46, v52 op_sel:[0,0,0] op_sel_hi:[0,1,1]\n\t" \
  "v_pk_fma_f16 v53, v49, v47, v53 op_sel:[0,0,0] op_sel_hi:[0,1,1]\n\tv_pk_mul_f16 v54, v48, v56 op_sel:[1,1] op_sel_hi:[1,0] neg_lo:[1,0]\n\t" \
  "v_pk_mul_f16 v55, v49, v57 op_sel:[1,1] op_sel_hi:[1,0] neg_lo:[1,0]\n\tv_pk_fma_f16 v54, v48, v56, v54 op_sel:[0,0,0] op_sel_hi:[0,1,1]\n\t" \
  "v_pk_fma_f16 v55, v49, v57, v55 op_sel:[0,0,0] op_sel_hi:[0,1,1]\n\tds_write_addtid_b32 v48 offset:1152\n\t" \
  "ds_write_addtid_b32 v49 offset:5760\n\tds_write_addtid_b32 v50 offset:1440\n\t" \
  "ds_write_addtid_b32 v51 offset:6048\n\tds_write_addtid_b32 v52 offset:1728\n\t" \
  "ds_write_addtid_b32 v53 offset:6336\n\tds_write_addtid_b32 v54 offset:2016\n\t" \
  "ds_write_addtid_b32 v55 offset:6624\n\tv_cvt_pk_f16_f32 v48, v64, v65\n\t" \
  "v_cvt_pk_f16_f32 v49, v66, v67\n\tv_pk_mul_f32 v[68:69], v[64:65], v[40:41] op_sel:[1,1] op_sel_hi:[1,0] neg_lo:[1,0]\n\t" \
  "v_pk_mul_f32 v[70:71], v[66:67], v[42:43] op_sel:[1,1] op_sel_hi:[1,0] neg_lo:[1,0]\n\tv_pk_fma_f32 v[68:69], v[64:65], v[40:41], v[68:69] op_sel:[0,0,0] op_sel_hi:[0,1,1]\n\t" \
  "v_pk_fma_f32 v[70:71], v[66:67], v[42:43], v[70:71] op_sel:[0,0,0] op_sel_hi:[0,1,1]\n\tv_pk_mul_f16 v50, v48, v86 op_sel:[1,1] op_sel_hi:[1,0]\n\t" \
  "v_pk_mul_f16 v51, v49, v87 op_sel:[1,1] op_sel_hi:[1,0]\n\tv_pk_fma_f16 v50, v48, v86, v50 op_sel:[0,0,0] op_sel_hi:[0,1,1] neg_hi:[0,1,0]\n\t" \
  "v_pk_fma_f16 v51, v49, v87, v51 op_sel:[0,0,0] op_sel_hi:[0,1,1] neg_hi:[0,1,0]\n\tv_pk_mul_f16 v52, v48, v46 op_sel:[1,1] op_sel_hi:[1,0] neg_lo:[1,0]\n\t" \
  "v_pk_mul_f16 v53, v49, v47 op_sel:[1,1] op_sel_hi:[1,0] neg_lo:[1,0]\n\tv_pk_fma_f16 v52, v48, v46, v52 op_sel:[0,0,0] op_sel_hi:[0,1,1]\n\t" \
  "v_pk_fma_f16 v53, v49, v47, v53 op_sel:[0,0,0] op_sel_hi:[0,1,1]\n\tv_pk_mul_f16 v54, v48, v56 op_sel:[1,1] op_sel_hi:[1,0] neg_lo:[1,0]\n\t" \
  "v_pk_mul_f16 v55, v49, v57 op_sel:[1,1] op_sel_hi:[1,0] neg_lo:[1,0]\n\tv_pk_fma_f16 v54, v48, v56, v54 op_sel:[0,0,0] op_sel_hi:[0,1,1]\n\t" \
  "v_pk_fma_f16 v55, v49, v57, v55 op_sel:[0,0,0] op_sel_hi:[0,1,1]\n\tds_write_addtid_b32 v48 offset:2304\n\t" \
  "ds_write_addtid_b32 v49 offset:6912\n\tds_write_addtid_b32 v50 offset:2592\n\t" \
  "ds_write_addtid_b32 v51 offset:7200\n\tds_write_addtid_b32 v52 offset:2880\n\t" \
  "ds_write_addtid_b32 v53 offset:7488\n\tds_write_addtid_b32 v54 offset:3168\n\t" \
  "ds_write_addtid_b32 v55 offset:7776\n\tv_cvt_pk_f16_f32 v48, v68, v69\n\t" \
  "v_cvt_pk_f16_f32 v49, v70, v71\n\tv_pk_mul_f16 v50, v48, v86 op_sel:[1,1] op_sel_hi:[1,0]\n\t" \
  "v_pk_mul_f16 v51, v49, v87 op_sel:[1,1] op_sel_hi:[1,0]\n\tv_pk_fma_f16 v50, v48, v86, v50 op_sel:[0,0,0] op_sel_hi:[0,1,1] neg_hi:[0,1,0]\n\t" \
  "v_pk_fma_f16 v51, v49, v87, v51 op_sel:[0,0,0] op_sel_hi:[0,1,1] neg_hi:[0,1,0]\n\tv_pk_mul_f16 v52, v48, v46 op_sel:[1,1] op_sel_hi:[1,0] neg_lo:[1,0]\n\t" \
  "v_pk_mul_f16 v53, v49, v47 op_sel:[1,1] op_sel_hi:[1,0] neg_lo:[1,0]\n\tv_pk_fma_f16 v52, v48, v46, v52 op_sel:[0,0,0] op_sel_hi:[0,1,1]\n\t" \
  "v_pk_fma_f16 v53, v49, v47, v53 op_sel:[0,0,0] op_sel_hi:[0,1,1]\n\tv_pk_mul_f16 v54, v48, v56 op_sel:[1,1] op_sel_hi:[1,0] neg_lo:[1,0]\n\t" \
  "v_pk_mul_f16 v55, v49, v57 op_sel:[1,1] op_sel_hi:[1,0] neg_lo:[1,0]\n\tv_pk_fma_f16 v54, v48, v56, v54 op_sel:[0,0,0] op_sel_hi:[0,1,1]\n\t" \
  "v_pk_fma_f16 v55, v49, v57, v55 op_sel:[0,0,0] op_sel_hi:[0,1,1]\n\tds_write_addtid_b32 v48 offset:3456\n\t" \
  "ds_write_addtid_b32 v49 offset:8064\n\tds_write_addtid_b32 v50 offset:3744\n\t" \
  "ds_write_addtid_b32 v51 offset:8352\n\tds_write_addtid_b32 v52 offset:4032\n\t" \
  "ds_write_addtid_b32 v53 offset:8640\n\tds_write_addtid_b32 v54 offset:4320\n\t" \
  "ds_write_addtid_b32 v55 offset:8928\n\tds_read_b128 v[72:75], v27 offset:0\n\t" \
  "ds_read_b128 v[76:79], v27 offset:64\n\tds_read_b128 v[80:83], v27 offset:128\n\t" \
  "ds_read_b128 v[84:87], v27 offset:192\n\tds_read_b128 v[88:91], v27 offset:4608\n\t" \
  "ds_read_b128 v[92:95], v27 offset:4672\n\tds_read_b128 v[96:99], v27 offset:4736\n\t" \
  "ds_read_b128 v[100:103], v27 offset:4800\n\tds_write_addtid_b32 v36 offset:0\n\t" \
  "ds_write_addtid_b32 v37 offset:4608\n\tds_read_b128 v[40:43], v12 offset:0\n\t" \
  "ds_read_b128 v[44:47], v12 offset:64\n\tds_read_b128 v[48:51], v12 offset:128\n\t" \
  "v_add_u32_sdwa v56, %[w], v16 dst_sel:DWORD dst_unused:UNUSED_PAD src0_sel:DWORD src1_sel:WORD_0\n\tv_add_u32_sdwa v57, %[w], v16 dst_sel:DWORD dst_unused:UNUSED_PAD src0_sel:DWORD src1_sel:WORD_1\n\t" \
  "v_add_u32_sdwa v58, %[w], v17 dst_sel:DWORD dst_unused:UNUSED_PAD src0_sel:DWORD src1_sel:WORD_0\n\tv_add_u32_sdwa v59, %[w], v17 dst_sel:DWORD dst_unused:UNUSED_PAD src0_sel:DWORD src1_sel:WORD_1\n\t" \
  "v_add_u32 v14, v56, v35\n\tv_add_u32 v15, v57, v35\n\t" \
  "s_waitcnt lgkmcnt(12)\n\tv_mfma_f32_16x16x32_f16 v[60:63], v[104:107], v[72:75], 0\n\t" \
  "v_mfma_f32_16x16x32_f16 v[4:7], v[136:139], v[72:75], 0\n\ts_cmp_lt_u32 %[w], 50176\n\t" \
  "s_cbranch_scc1 1f\n\ts_setprio 1\n\t" \
  "1:\n\ts_waitcnt lgkmcnt(2)\n\t" \
  "v_mfma_f32_16x16x32_f16 v[60:63], v[108:111], v[76:79], v[60:63]\n\tv_pk_mul_f16 v52, v40, v72 op_sel:[1,1] op_sel_hi:[1,0] neg_lo:[1,0]\n\t" \
  "v_pk_mul_f16 v53, v41, v73 op_sel:[1,1] op_sel_hi:[1,0] neg_lo:[1,0]\n\tv_pk_mul_f16 v54, v42, v74 op_sel:[1,1] op_sel_hi:[1,0] neg_lo:[1,0]\n\t" \
  "v_pk_mul_f16 v55, v43, v75 op_sel:[1,1] op_sel_hi:[1,0] neg_lo:[1,0]\n\tv_mfma_f32_16x16x32_f16 v[4:7], v[140:143], v[76:79], v[4:7]\n\t" \
  "v_pk_fma_f16 v40, v40, v72, v52 op_sel:[0,0,0] op_sel_hi:[0,1,1]\n\tv_pk_fma_f16 v41, v41, v73, v53 op_sel:[0,0,0] op_sel_hi:[0,1,1]\n\t" \
  "v_pk_fma_f16 v42, v42, v74, v54 op_sel:[0,0,0] op_sel_hi:[0,1,1]\n\tv_pk_fma_f16 v43, v43, v75, v55 op_sel:[0,0,0] op_sel_hi:[0,1,1]\n\t" \
  "s_waitcnt lgkmcnt(1)\n\tv_mfma_f32_16x16x32_f16 v[60:63], v[112:115], v[80:83], v[60:63]\n\t" \
  "ds_read_b128 v[72:75], v12 offset:192\n\tv_pk_mul_f16 v52, v44, v76 op_sel:[1,1] op_sel_hi:[1,0] neg_lo:[1,0]\n\t" \
  "v_pk_mul_f16 v53, v45, v77 op_sel:[1,1] op_sel_hi:[1,0] neg_lo:[1,0]\n\tv_pk_mul_f16 v54, v46, v78 op_sel:[1,1] op_sel_hi:[1,0] neg_lo:[1,0]\n\t" \
  "v_pk_mul_f16 v55, v47, v79 op_sel:[1,1] op_sel_hi:[1,0] neg_lo:[1,0]\n\tv_mfma_f32_16x16x32_f16 v[4:7], v[144:147], v[80:83], v[4:7]\n\t" \
  "v_pk_fma_f16 v44, v44, v76, v52 op_sel:[0,0,0] op_sel_hi:[0,1,1]\n\tv_pk_fma_f16 v45, v45, v77, v53 op_sel:[0,0,0] op_sel_hi:[0,1,1]\n\t" \
  "v_pk_fma_f16 v46, v46, v78, v54 op_sel:[0,0,0] op_sel_hi:[0,1,1]\n\tv_pk_fma_f16 v47, v47, v79, v55 op_sel:[0,0,0] op_sel_hi:[0,1,1]\n\t" \
  "s_waitcnt lgkmcnt(1)\n\tv_mfma_f32_16x16x32_f16 v[60:63], v[116:119], v[84:87], v[60:63]\n\t" \
  "ds_read_b128 v[76:79], v12 offset:4608\n\tv_pk_mul_f16 v52, v48, v80 op_sel:[1,1] op_sel_hi:[1,0] neg_lo:[1,0]\n\t" \
  "v_pk_mul_f16 v53, v49, v81 op_sel:[1,1] op_sel_hi:[1,0] neg_lo:[1,0]\n\tv_pk_mul_f16 v54, v50, v82 op_sel:[1,1] op_sel_hi:[1,0] neg_lo:[1,0]\n\t" \
  "v_pk_mul_f16 v55, v51, v83 op_sel:[1,1] op_sel_hi:[1,0] neg_lo:[1,0]\n\tv_mfma_f32_16x16x32_f16 v[4:7], v[148:151], v[84:87], v[4:7]\n\t" \
  "v_pk_fma_f16 v48, v48, v80, v52 op_sel:[0,0,0] op_sel_hi:[0,1,1]\n\tv_pk_fma_f16 v49, v49, v81, v53 op_sel:[0,0,0] op_sel_hi:[0,1,1]\n\t" \
  "v_pk_fma_f16 v50, v50, v82, v54 op_sel:[0,0,0] op_sel_hi:[0,1,1]\n\tv_pk_fma_f16 v51, v51, v83, v55 op_sel:[0,0,0] op_sel_hi:[0,1,1]\n\t" \
  "s_waitcnt lgkmcnt(1)\n\tv_mfma_f32_16x16x32_f16 v[60:63], v[120:123], v[88:91], v[60:63]\n\t" \
  "ds_read_b128 v[80:83], v12 offset:4672\n\tv_pk_mul_f16 v52, v72, v84 op_sel:[1,1] op_sel_hi:[1,0] neg_lo:[1,0]\n\t" \
  "v_pk_mul_f16 v53, v73, v85 op_sel:[1,1] op_sel_hi:[1,0] neg_lo:[1,0]\n\tv_pk_mul_f16 v54, v74, v86 op_sel:[1,1] op_sel_hi:[1,0] neg_lo:[1,0]\n\t" \
  "v_pk_mul_f16 v55, v75, v87 op_sel:[1,1] op_sel_hi:[1,0] neg_lo:[1,0]\n\tv_mfma_f32_16x16x32_f16 v[4:7], v[152:155], v[88:91], v[4:7]\n\t" \
  "v_pk_fma_f16 v72, v72, v84, v52 op_sel:[0,0,0] op_sel_hi:[0,1,1]\n\tv_pk_fma_f16 v73, v73, v85, v53 op_sel:[0,0,0] op_sel_hi:[0,1,1]\n\t" \
  "v_pk_fma_f16 v74, v74, v86, v54 op_sel:[0,0,0] op_sel_hi:[0,1,1]\n\tv_pk_fma_f16 v75, v75, v87, v55 op_sel:[0,0,0] op_sel_hi:[0,1,1]\n\t" \
  "s_waitcnt lgkmcnt(1)\n\tv_mfma_f32_16x16x32_f16 v[60:63], v[124:127], v[92:95], v[60:63]\n\t" \
  "ds_read_b128 v[84:87], v12 offset:4736\n\tv_pk_mul_f16 v52, v76, v88 op_sel:[1,1] op_sel_hi:[1,0] neg_lo:[1,0]\n\t" \
  "v_pk_mul_f16 v53, v77, v89 op_sel:[1,1] op_sel_hi:[1,0] neg_lo:[1,0]\n\tv_pk_mul_f16 v54, v78, v90 op_sel:[1,1] op_sel_hi:[1,0] neg_lo:[1,0]\n\t" \
  "v_pk_mul_f16 v55, v79, v91 op_sel:[1,1] op_sel_hi:[1,0] neg_lo:[1,0]\n\tv_mfma_f32_16x16x32_f16 v[4:7], v[156:159], v[92:95], v[4:7]\n\t" \
  "v_pk_fma_f16 v76, v76, v88, v52 op_sel:[0,0,0] op_sel_hi:[0,1,1]\n\tv_pk_fma_f16 v77, v77, v89, v53 op_sel:[0,0,0] op_sel_hi:[0,1,1]\n\t" \
  "v_pk_fma_f16 v78, v78, v90, v54 op_sel:[0,0,0] op_sel_hi:[0,1,1]\n\tv_pk_fma_f16 v79, v79, v91, v55 op_sel:[0,0,0] op_sel_hi:[0,1,1]\n\t" \
  "s_waitcnt lgkmcnt(1)\n\tv_mfma_f32_16x16x32_f16 v[60:63], v[128:131], v[96:99], v[60:63]\n\t" \
  "ds_read_b128 v[88:91], v12 offset:4800\n\tv_pk_mul_f16 v52, v80, v92 op_sel:[1,1] op_sel_hi:[1,0] neg_lo:[1,0]\n\t" \
  "v_pk_mul_f16 v53, v81, v93 op_sel:[1,1] op_sel_hi:[1,0] neg_lo:[1,0]\n\tv_pk_mul_f16 v54, v82, v94 op_sel:[1,1] op_sel_hi:[1,0] neg_lo:[1,0]\n\t" \
  "v_pk_mul_f16 v55, v83, v95 op_sel:[1,1] op_sel_hi:[1,0] neg_lo:[1,0]\n\tv_mfma_f32_16x16x32_f16 v[4:7], v[160:163], v[96:99], v[4:7]\n\t" \
  "v_pk_fma_f16 v80, v80, v92, v52 op_sel:[0,0,0] op_sel_hi:[0,1,1]\n\tv_pk_fma_f16 v81, v81, v93, v53 op_sel:[0,0,0] op_sel_hi:[0,1,1]\n\t" \
  "v_pk_fma_f16 v82, v82, v94, v54 op_sel:[0,0,0] op_sel_hi:[0,1,1]\n\tv_pk_fma_f16 v83, v83, v95, v55 op_sel:[0,0,0] op_sel_hi:[0,1,1]\n\t" \
  "s_waitcnt lgkmcnt(1)\n\tv_mfma_f32_16x16x32_f16 v[60:63], v[132:135], v[100:103], v[60:63]\n\t" \
  "v_pk_mul_f16 v52, v84, v96 op_sel:[1,1] op_sel_hi:[1,0] neg_lo:[1,0]\n\tv_pk_mul_f16 v53, v85, v97 op_sel:[1,1] op_sel_hi:[1,0] neg_lo:[1,0]\n\t" \
  "v_pk_mul_f16 v54, v86, v98 op_sel:[1,1] op_sel_hi:[1,0] neg_lo:[1,0]\n\tv_pk_mul_f16 v55, v87, v99 op_sel:[1,1] op_sel_hi:[1,0] neg_lo:[1,0]\n\t" \
  "v_mfma_f32_16x16x32_f16 v[4:7], v[164:167], v[100:103], v[4:7]\n\tv_pk_fma_f16 v84, v84, v96, v52 op_sel:[0,0,0] op_sel_hi:[0,1,1]\n\t" \
  "v_pk_fma_f16 v85, v85, v97, v53 op_sel:[0,0,0] op_sel_hi:[0,1,1]\n\tv_pk_fma_f16 v86, v86, v98, v54 op_sel:[0,0,0] op_sel_hi:[0,1,1]\n\t" \
  "v_pk_fma_f16 v87, v87, v99, v55 op_sel:[0,0,0] op_sel_hi:[0,1,1]\n\ts_waitcnt lgkmcnt(0)\n\t" \
  "v_mfma_f32_16x16x32_f16 v[8:11], v[136:139], v[40:43], 0\n\tv_pk_mul_f16 v52, v88, v100 op_sel:[1,1] op_sel_hi:[1,0] neg_lo:[1,0]\n\t" \
  "v_pk_mul_f16 v53, v89, v101 op_sel:[1,1] op_sel_hi:[1,0] neg_lo:[1,0]\n\tv_pk_mul_f16 v54, v90, v102 op_sel:[1,1] op_sel_hi:[1,0] neg_lo:[1,0]\n\t" \
  "v_mfma_f32_16x16x32_f16 v[64:67], v[104:107], v[40:43], 0\n\tv_pk_mul_f16 v55, v91, v103 op_sel:[1,1] op_sel_hi:[1,0] neg_lo:[1,0]\n\t" \
  "v_pk_fma_f16 v88, v88, v100, v52 op_sel:[0,0,0] op_sel_hi:[0,1,1]\n\tv_pk_fma_f16 v89, v89, v101, v53 op_sel:[0,0,0] op_sel_hi:[0,1,1]\n\t" \
  "v_mfma_f32_16x16x32_f16 v[8:11], v[140:143], v[44:47], v[8:11]\n\tv_pk_fma_f16 v90, v90, v102, v54 op_sel:[0,0,0] op_sel_hi:[0,1,1]\n\t" \
  "v_pk_fma_f16 v91, v91, v103, v55 op_sel:[0,0,0] op_sel_hi:[0,1,1]\n\tv_mov_b64 v[100:101], 0\n\t" \
  "v_mfma_f32_16x16x32_f16 v[64:67], v[108:111], v[44:47], v[64:67]\n\tv_mov_b64 v[102:103], 0\n\t" \
  "v_cvt_pk_f16_f32 v4, v4, v5\n\tv_cvt_pk_f16_f32 v5, v6, v7\n\t" \
  "v_mfma_f32_16x16x32_f16 v[8:11], v[144:147], v[48:51], v[8:11]\n\tv_cvt_pk_f16_f32 v92, v60, v61\n\t" \
  "v_cvt_pk_f16_f32 v93, v62, v63\n\tv_permlane16_swap_b32 v4, v5\n\t" \
  "v_mfma_f32_16x16x32_f16 v[64:67], v[112:115], v[48:51], v[64:67]\n\tv_perm_b32 v6, v4, v4, v33\n\t" \
  "v_perm_b32 v7, v4, v4, v34\n\tv_mfma_f32_16x16x32_f16 v[8:11], v[148:151], v[72:75], v[8:11]\n\t" \
  "ds_write_b128 v58, v[100:103]\n\tv_mfma_f32_16x16x32_f16 v[64:67], v[116:119], v[72:75], v[64:67]\n\t" \
  "ds_write2_b32 v56, v92, v93 offset0:193 offset1:194\n\tv_mfma_f32_16x16x32_f16 v[8:11], v[152:155], v[76:79], v[8:11]\n\t" \
  "ds_write2_b32 v57, v92, v93 offset0:193 offset1:194\n\tv_mfma_f32_16x16x32_f16 v[64:67], v[120:123], v[76:79], v[64:67]\n\t" \
  "ds_write2_b32 v14, v6, v7 offset0:64 offset1:65\n\tv_mfma_f32_16x16x32_f16 v[8:11], v[156:159], v[80:83], v[8:11]\n\t" \
  "ds_write2_b32 v15, v6, v7 offset0:64 offset1:65\n\tv_mfma_f32_16x16x32_f16 v[64:67], v[124:127], v[80:83], v[64:67]\n\t" \
  "ds_write_b16 v59, v5 offset:48\n\tv_mfma_f32_16x16x32_f16 v[8:11], v[160:163], v[84:87], v[8:11]\n\t" \
  "ds_write_b16_d16_hi v59, v5 offset:16\n\tv_mfma_f32_16x16x32_f16 v[64:67], v[128:131], v[84:87], v[64:67]\n\t" \
  "ds_write_b16 v59, v5 offset:878\n\tv_mfma_f32_16x16x32_f16 v[8:11], v[164:167], v[88:91], v[8:11]\n\t" \
  "ds_write_b16_d16_hi v59, v5 offset:846\n\tv_mfma_f32_16x16x32_f16 v[64:67], v[132:135], v[88:91], v[64:67]\n\t" \
  "ds_write_b16 v59, v5 offset:1708\n\tds_write_b16_d16_hi v59, v5 offset:1676\n\t" \
  "ds_write_b16 v59, v5 offset:2538\n\tds_write_b16_d16_hi v59, v5 offset:2506\n\t" \
  "s_nop 1\n\tv_cvt_pk_f16_f32 v8, v8, v9\n\t" \
  "v_cvt_pk_f16_f32 v9, v10, v11\n\tv_cvt_pk_f16_f32 v46, v64, v65\n\t" \
  "v_cvt_pk_f16_f32 v47, v66, v67\n\tv_permlane16_swap_b32 v8, v9\n\t" \
  "v_perm_b32 v10, v8, v8, v33\n\tv_perm_b32 v11, v8, v8, v34\n\t" \
  "ds_write2_b32 v56, v46, v47 offset0:129 offset1:130\n\tds_write2_b32 v57, v46, v47 offset0:129 offset1:130\n\t" \
  "ds_write2_b32 v14, v10, v11 offset0:0 offset1:1\n\tds_write2_b32 v15, v10, v11 offset0:0 offset1:1\n\t" \
  "ds_write_b16 v59, v9 offset:32\n\tds_write_b16_d16_hi v59, v9 offset:0\n\t" \
  "ds_write_b16 v59, v9 offset:862\n\tds_write_b16_d16_hi v59, v9 offset:830\n\t" \
  "ds_write_b16 v59, v9 offset:1692\n\tds_write_b16_d16_hi v59, v9 offset:1660\n\t" \
  "ds_write_b16 v59, v9 offset:2522\n\tds_write_b16_d16_hi v59, v9 offset:2490\n\t"
#define GEN_CLOBBER "v4", "v5", "v6", "v7", "v10", "v11", "v13", "v14", "v15", "v36", "v37", "v38", "v39", "v40", "v41", "v42", "v43", "v44", "v45", "v47", "v48", "v49", "v50", "v51", "v52", "v53", "v54", "v55", "v56", "v57", "v58", "v59", "v60", "v61", "v62", "v63", "v64", "v65", "v66", "v67", "v72", "v73", "v74", "v75", "v76", "v77", "v78", "v79", "v80", "v81", "v82", "v83", "v84", "v85", "v86", "v87", "v88", "v89", "v90", "v91", "v92", "v94", "v95", "v98", "v99", "v100", "v101", "v102", "v103", "v104", "v105", "v106", "v107", "v108", "v109", "v110", "v111", "v112", "v113", "v114", "v115", "v116", "v117", "v118", "v119", "v120", "v121", "v122", "v123", "v124", "v125", "v126", "v127", "v128", "v129", "v130", "v131", "v132", "v133", "v134", "v135", "v136", "v137", "v138", "v139", "v140", "v141", "v142", "v143", "v144", "v145", "v146", "v147", "v148", "v149", "v150", "v151", "v152", "v153", "v154", "v155", "v156", "v157", "v158", "v159", "v160", "v161", "v162", "v163", "v164", "v165", "v166", "v167"
#define CONV_ASM \
  "ds_read_b32 v34, v30\n\tds_read_b64 v[132:133], v26 offset:512\n\tds_read_b64 v[134:135], v26 offset:520\n\t" \
  "ds_read_b64 v[100:101], v26 offset:0\n\tds_read_b64 v[102:103], v26 offset:8\n\tds_read_b64 v[136:137], v26 offset:576\n\t" \
  "ds_read_b64 v[138:139], v26 offset:584\n\tds_read_b64 v[104:105], v26 offset:64\n\tds_read_b64 v[106:107], v26 offset:72\n\t" \
  "ds_read_b64 v[140:141], v26 offset:640\n\tds_read_b64 v[142:143], v26 offset:648\n\tds_read_b64 v[108:109], v26 offset:128\n\t" \
  "ds_read_b64 v[110:111], v26 offset:136\n\tds_read_b64 v[144:145], v26 offset:704\n\tds_read_b64 v[146:147], v26 offset:712\n\t" \
  "ds_read_b64 v[112:113], v26 offset:192\n\tds_read_b64 v[114:115], v26 offset:200\n\tds_read_b64 v[148:149], v26 offset:768\n\t" \
  "ds_read_b64 v[150:151], v26 offset:776\n\tds_read_b64 v[116:117], v26 offset:256\n\tds_read_b64 v[118:119], v26 offset:264\n\t" \
  TS(60, 61) "s_waitcnt lgkmcnt(15)\n\tv_cmp_gt_u32 vcc, %[n0], v34\n\t" \
  "s_cbranch_vccz 2f\n\ts_mov_b32 s6, 0x10000\n\t1:\n\t" \
  "s_sleep 1\n\tds_read_b32 v34, v30\n\ts_sub_u32 s6, s6, 1\n\t" \
  "s_waitcnt lgkmcnt(0)\n\tv_cmp_gt_u32 vcc, %[n0], v34\n\ts_cbranch_scc1 2f\n\t" \
  "s_cbranch_vccnz 1b\n\t2:\n\t" TS(62, 63) \
  "s_cmp_lt_u32 %[w], 4\n\ts_setprio 1\n\ts_cbranch_scc1 1f\n\t" \
  "s_setprio 2\n\t1:\n\tds_read_b128 v[0:3], v28 offset:0\n\t" \
  "ds_read_b128 v[4:7], v28 offset:64\n\tds_read_b128 v[8:11], v28 offset:128\n\tds_read_b128 v[12:15], v28 offset:192\n\t" \
  "ds_read_b128 v[16:19], v28 offset:256\n\tds_read_b128 v[20:23], v28 offset:320\n\ts_waitcnt lgkmcnt(5)\n\t" \
  "v_mfma_f32_16x16x32_f16 v[52:55], v[132:135], v[0:3], 0\n\tds_read_b64 v[152:153], v26 offset:832\n\tds_read_b64 v[154:155], v26 offset:840\n\t" \
  "v_mfma_f32_16x16x32_f16 v[56:59], v[100:103], v[0:3], 0\n\tds_read_b64 v[120:121], v26 offset:320\n\tds_read_b64 v[122:123], v26 offset:328\n\t" \
  "ds_read_b128 v[0:3], v28 offset:384\n\ts_waitcnt lgkmcnt(9)\n\tv_mfma_f32_16x16x32_f16 v[52:55], v[136:139], v[4:7], v[52:55]\n\t" \
  "ds_read_b64 v[156:157], v26 offset:896\n\tds_read_b64 v[158:159], v26 offset:904\n\tv_mfma_f32_16x16x32_f16 v[56:59], v[104:107], v[4:7], v[56:59]\n\t" \
  "ds_read_b64 v[124:125], v26 offset:384\n\tds_read_b64 v[126:127], v26 offset:392\n\tds_read_b128 v[4:7], v28 offset:448\n\t" \
  "s_waitcnt lgkmcnt(13)\n\tv_mfma_f32_16x16x32_f16 v[52:55], v[140:143], v[8:11], v[52:55]\n\tds_read_b64 v[160:161], v26 offset:960\n\t" \
  "ds_read_b64 v[162:163], v26 offset:968\n\tv_mfma_f32_16x16x32_f16 v[56:59], v[108:111], v[8:11], v[56:59]\n\tds_read_b64 v[128:129], v26 offset:448\n\t" \
  "ds_read_b64 v[130:131], v26 offset:456\n\tds_read_b128 v[8:11], v28 offset:512\n\ts_waitcnt lgkmcnt(15)\n\t" \
  "v_mfma_f32_16x16x32_f16 v[52:55], v[144:147], v[12:15], v[52:55]\n\tds_read_b64 v[164:165], v26 offset:1024\n\tds_read_b64 v[166:167], v26 offset:1032\n\t" \
  "v_mfma_f32_16x16x32_f16 v[56:59], v[112:115], v[12:15], v[56:59]\n\tds_read_b64 v[76:77], v31 offset:72\n\tds_read_b64 v[78:79], v31 offset:176\n\t" \
  "ds_read_b128 v[12:15], v28 offset:576\n\tv_mfma_f32_16x16x32_f16 v[52:55], v[148:151], v[16:19], v[52:55]\n\tds_read_b64 v[80:81], v31 offset:40\n\t" \
  "ds_read_b64 v[82:83], v31 offset:144\n\tv_mfma_f32_16x16x32_f16 v[56:59], v[116:119], v[16:19], v[56:59]\n\tds_read_b64 v[84:85], v31 offset:8\n\t" \
  "ds_read_b64 v[86:87], v31 offset:112\n\tds_read_b128 v[16:19], v28 offset:640\n\ts_waitcnt lgkmcnt(15)\n\t" \
  "v_mfma_f32_16x16x32_f16 v[52:55], v[152:155], v[20:23], v[52:55]\n\tds_read_b64 v[88:89], v31 offset:280\n\tds_read_b64 v[90:91], v31 offset:384\n\t" \
  "v_mfma_f32_16x16x32_f16 v[56:59], v[120:123], v[20:23], v[56:59]\n\tds_read_b64 v[92:93], v31 offset:248\n\tds_read_b64 v[94:95], v31 offset:352\n\t" \
  "ds_read_b128 v[20:23], v28 offset:704\n\tv_mfma_f32_16x16x32_f16 v[52:55], v[156:159], v[0:3], v[52:55]\n\tds_read_b64 v[96:97], v31 offset:216\n\t" \
  "ds_read_b64 v[98:99], v31 offset:320\n\tv_mfma_f32_16x16x32_f16 v[56:59], v[124:127], v[0:3], v[56:59]\n\tds_read_b128 v[0:3], v28 offset:768\n\t" \
  "s_waitcnt lgkmcnt(15)\n\tv_mfma_f32_16x16x32_f16 v[52:55], v[160:163], v[4:7], v[52:55]\n\tv_mfma_f32_16x16x32_f16 v[56:59], v[128:131], v[4:7], v[56:59]\n\t" \
  "ds_read_b128 v[4:7], v28 offset:832\n\tv_mfma_f32_16x16x32_f16 v[52:55], v[164:167], v[8:11], v[52:55]\n\tv_mfma_f32_16x16x32_f16 v[56:59], v[132:135], v[8:11], v[56:59]\n\t" \
  "v_mfma_f32_16x16x32_f16 v[60:63], v[100:103], v[8:11], 0\n\tds_read_b128 v[8:11], v28 offset:896\n\ts_waitcnt lgkmcnt(15)\n\t" \
  "v_mfma_f32_16x16x32_f16 v[56:59], v[136:139], v[12:15], v[56:59]\n\tds_read_b32 v34, v30\n\tv_mfma_f32_16x16x32_f16 v[60:63], v[104:107], v[12:15], v[60:63]\n\t" \
  "ds_read_b128 v[12:15], v28 offset:960\n\tv_cvt_pk_bf16_f32 v52, v52, v53\n\tv_cvt_pk_bf16_f32 v53, v54, v55\n\t" \
  TS(64, 65) "s_waitcnt lgkmcnt(12)\n\tv_mfma_f32_16x16x32_f16 v[56:59], v[140:143], v[16:19], v[56:59]\n\t" \
  "ds_write_b64 v29, v[52:53] offset:0\n\tds_write_b32 v25, v25 offset:0\n\tv_mfma_f32_16x16x32_f16 v[60:63], v[108:111], v[16:19], v[60:63]\n\t" \
  "s_waitcnt lgkmcnt(3)\n\tv_cmp_gt_u32 vcc, 3, v34\n\ts_cbranch_vccz 2f\n\t" \
  "s_mov_b32 s6, 0x10000\n\t1:\n\ts_sleep 1\n\t" \
  "ds_read_b32 v34, v30\n\ts_sub_u32 s6, s6, 1\n\ts_waitcnt lgkmcnt(0)\n\t" \
  "v_cmp_gt_u32 vcc, 3, v34\n\ts_cbranch_scc1 2f\n\ts_cbranch_vccnz 1b\n\t" \
  "2:\n\tds_read_b128 v[16:19], v28 offset:1024\n\tv_mfma_f32_16x16x32_f16 v[56:59], v[144:147], v[20:23], v[56:59]\n\t" \
  "v_mfma_f32_16x16x32_f16 v[60:63], v[112:115], v[20:23], v[60:63]\n\tds_read_b128 v[68:71], v32\n\tds_read_b128 v[72:75], v32 offset:1024\n\t" \
  "ds_read_b128 v[20:23], v28 offset:1088\n\tv_mfma_f32_16x16x32_f16 v[56:59], v[148:151], v[0:3], v[56:59]\n\tv_mfma_f32_16x16x32_f16 v[60:63], v[116:119], v[0:3], v[60:63]\n\t" \
  "ds_read_b128 v[0:3], v28 offset:496\n\tv_mfma_f32_16x16x32_f16 v[56:59], v[152:155], v[4:7], v[56:59]\n\tv_mfma_f32_16x16x32_f16 v[60:63], v[120:123], v[4:7], v[60:63]\n\t" \
  "ds_read_b128 v[4:7], v28 offset:1152\n\tv_mfma_f32_16x16x32_f16 v[56:59], v[156:159], v[8:11], v[56:59]\n\tv_mfma_f32_16x16x32_f16 v[60:63], v[124:127], v[8:11], v[60:63]\n\t" \
  "ds_read_b128 v[8:11], v28 offset:1216\n\ts_waitcnt lgkmcnt(9)\n\tv_mfma_f32_16x16x32_f16 v[56:59], v[160:163], v[12:15], v[56:59]\n\t" \
  "v_mfma_f32_16x16x32_f16 v[60:63], v[128:131], v[12:15], v[60:63]\n\tds_read_b128 v[12:15], v28 offset:480\n\ts_waitcnt lgkmcnt(7)\n\t" \
  "v_mfma_f32_16x16x32_f16 v[56:59], v[164:167], v[16:19], v[56:59]\n\tv_mfma_f32_16x16x32_f16 v[60:63], v[132:135], v[16:19], v[60:63]\n\tv_mfma_f32_16x16x32_f16 v[64:67], v[100:103], v[16:19], 0\n\t" \
  "ds_read_b128 v[16:19], v28 offset:1280\n\ts_waitcnt lgkmcnt(5)\n\tv_mfma_f32_16x16x32_f16 v[60:63], v[136:139], v[20:23], v[60:63]\n\t" \
  "v_mfma_f32_16x16x32_f16 v[64:67], v[104:107], v[20:23], v[64:67]\n\tds_read_b128 v[20:23], v28 offset:1344\n\ts_waitcnt lgkmcnt(5)\n\t" \
  "v_mfma_f32_16x16x32_f16 v[36:39], v[0:3], v[68:71], 0\n\tv_cvt_pk_bf16_f32 v56, v56, v57\n\tv_cvt_pk_bf16_f32 v57, v58, v59\n\t" \
  "v_mfma_f32_16x16x32_f16 v[40:43], v[0:3], v[72:75], 0\n\tds_write_b64 v29, v[56:57] offset:512\n\tds_write_b32 v25, v25 offset:64\n\t" \
  "ds_read_b128 v[0:3], v28 offset:1408\n\ts_waitcnt lgkmcnt(7)\n\tv_mfma_f32_16x16x32_f16 v[60:63], v[140:143], v[4:7], v[60:63]\n\t" \
  "v_cvt_pk_f16_f32 v36, v36, v37\n\tv_cvt_pk_f16_f32 v37, v38, v39\n\tv_mfma_f32_16x16x32_f16 v[64:67], v[108:111], v[4:7], v[64:67]\n\t" \
  "ds_read_b128 v[4:7], v28 offset:1472\n\tv_cvt_pk_f16_f32 v38, v40, v41\n\tv_cvt_pk_f16_f32 v39, v42, v43\n\t" \
  "s_waitcnt lgkmcnt(7)\n\tv_mfma_f32_16x16x32_f16 v[60:63], v[144:147], v[8:11], v[60:63]\n\tv_mfma_f32_16x16x32_f16 v[64:67], v[112:115], v[8:11], v[64:67]\n\t" \
  "ds_read_b128 v[8:11], v28 offset:1536\n\tv_mfma_f32_16x16x32_f16 v[60:63], v[36:39], v[76:79], v[60:63]\n\tv_mfma_f32_16x16x32_f16 v[64:67], v[36:39], v[80:83], v[64:67]\n\t" \
  "v_mfma_f32_16x16x32_f16 v[52:55], v[36:39], v[84:87], 0\n\ts_waitcnt lgkmcnt(7)\n\tv_mfma_f32_16x16x32_f16 v[44:47], v[12:15], v[68:71], 0\n\t" \
  "v_mfma_f32_16x16x32_f16 v[40:43], v[12:15], v[72:75], 0\n\tds_read_b128 v[12:15], v28 offset:1600\n\ts_waitcnt lgkmcnt(7)\n\t" \
  "v_mfma_f32_16x16x32_f16 v[60:63], v[148:151], v[16:19], v[60:63]\n\tv_mfma_f32_16x16x32_f16 v[64:67], v[116:119], v[16:19], v[64:67]\n\tds_read_b128 v[16:19], v28 offset:1008\n\t" \
  "s_waitcnt lgkmcnt(7)\n\tv_mfma_f32_16x16x32_f16 v[60:63], v[152:155], v[20:23], v[60:63]\n\tv_cvt_pk_f16_f32 v44, v44, v45\n\t" \
  "v_cvt_pk_f16_f32 v45, v46, v47\n\tv_cvt_pk_f16_f32 v46, v40, v41\n\tv_cvt_pk_f16_f32 v47, v42, v43\n\t" \
  "v_mfma_f32_16x16x32_f16 v[64:67], v[120:123], v[20:23], v[64:67]\n\tds_read_b128 v[20:23], v28 offset:1664\n\tv_mfma_f32_16x16x32_f16 v[60:63], v[44:47], v[88:91], v[60:63]\n\t" \
  "v_mfma_f32_16x16x32_f16 v[64:67], v[44:47], v[92:95], v[64:67]\n\tv_mfma_f32_16x16x32_f16 v[52:55], v[44:47], v[96:99], v[52:55]\n\ts_waitcnt lgkmcnt(5)\n\t" \
  "v_mfma_f32_16x16x32_f16 v[60:63], v[156:159], v[0:3], v[60:63]\n\tv_mfma_f32_16x16x32_f16 v[64:67], v[124:127], v[0:3], v[64:67]\n\tds_read_b128 v[0:3], v28 offset:1728\n\t" \
  "s_waitcnt lgkmcnt(5)\n\tv_mfma_f32_16x16x32_f16 v[60:63], v[160:163], v[4:7], v[60:63]\n\tv_mfma_f32_16x16x32_f16 v[64:67], v[128:131], v[4:7], v[64:67]\n\t" \
  "ds_read_b128 v[4:7], v28 offset:992\n\ts_waitcnt lgkmcnt(5)\n\tv_mfma_f32_16x16x32_f16 v[60:63], v[164:167], v[8:11], v[60:63]\n\t" \
  "v_mfma_f32_16x16x32_f16 v[64:67], v[132:135], v[8:11], v[64:67]\n\tv_mfma_f32_16x16x32_f16 v[52:55], v[100:103], v[8:11], v[52:55]\n\tds_read_b128 v[8:11], v28 offset:1792\n\t" \
  "s_waitcnt lgkmcnt(5)\n\tv_mfma_f32_16x16x32_f16 v[64:67], v[136:139], v[12:15], v[64:67]\n\tv_mfma_f32_16x16x32_f16 v[52:55], v[104:107], v[12:15], v[52:55]\n\t" \
  "ds_read_b128 v[12:15], v28 offset:1856\n\ts_waitcnt lgkmcnt(5)\n\tv_mfma_f32_16x16x32_f16 v[36:39], v[16:19], v[68:71], 0\n\t" \
  "v_cvt_pk_bf16_f32 v60, v60, v61\n\tv_cvt_pk_bf16_f32 v61, v62, v63\n\tv_mfma_f32_16x16x32_f16 v[40:43], v[16:19], v[72:75], 0\n\t" \
  "ds_write_b64 v29, v[60:61] offset:1024\n\tds_write_b32 v25, v25 offset:128\n\tds_read_b128 v[16:19], v28 offset:1920\n\t" \
  "s_waitcnt lgkmcnt(7)\n\tv_mfma_f32_16x16x32_f16 v[64:67], v[140:143], v[20:23], v[64:67]\n\tds_read_b32 v34, v30\n\t" \
  "v_cvt_pk_f16_f32 v36, v36, v37\n\tv_cvt_pk_f16_f32 v37, v38, v39\n\tv_mfma_f32_16x16x32_f16 v[52:55], v[108:111], v[20:23], v[52:55]\n\t" \
  "ds_read_b128 v[20:23], v28 offset:1984\n\tv_cvt_pk_f16_f32 v38, v40, v41\n\tv_cvt_pk_f16_f32 v39, v42, v43\n\t" \
  "s_waitcnt lgkmcnt(8)\n\tv_mfma_f32_16x16x32_f16 v[64:67], v[144:147], v[0:3], v[64:67]\n\tv_mfma_f32_16x16x32_f16 v[52:55], v[112:115], v[0:3], v[52:55]\n\t" \
  "s_waitcnt lgkmcnt(1)\n\tv_cmp_gt_u32 vcc, 4, v34\n\ts_cbranch_vccz 2f\n\t" \
  "s_mov_b32 s6, 0x10000\n\t1:\n\ts_sleep 1\n\t" \
  "ds_read_b32 v34, v30\n\ts_sub_u32 s6, s6, 1\n\ts_waitcnt lgkmcnt(0)\n\t" \
  "v_cmp_gt_u32 vcc, 4, v34\n\ts_cbranch_scc1 2f\n\ts_cbranch_vccnz 1b\n\t" \
  "2:\n\tds_read_b128 v[0:3], v28 offset:2048\n\tv_mfma_f32_16x16x32_f16 v[64:67], v[36:39], v[76:79], v[64:67]\n\t" \
  "v_mfma_f32_16x16x32_f16 v[52:55], v[36:39], v[80:83], v[52:55]\n\tv_mfma_f32_16x16x32_f16 v[56:59], v[36:39], v[84:87], 0\n\tv_mfma_f32_16x16x32_f16 v[44:47], v[4:7], v[68:71], 0\n\t" \
  "v_mfma_f32_16x16x32_f16 v[40:43], v[4:7], v[72:75], 0\n\tds_read_b128 v[4:7], v28 offset:2112\n\tv_mfma_f32_16x16x32_f16 v[64:67], v[148:151], v[8:11], v[64:67]\n\t" \
  "v_mfma_f32_16x16x32_f16 v[52:55], v[116:119], v[8:11], v[52:55]\n\tds_read_b128 v[8:11], v28 offset:1520\n\tv_mfma_f32_16x16x32_f16 v[64:67], v[152:155], v[12:15], v[64:67]\n\t" \
  "v_mfma_f32_16x16x32_f16 v[52:55], v[120:123], v[12:15], v[52:55]\n\tds_read_b128 v[12:15], v28 offset:2176\n\tv_cvt_pk_f16_f32 v44, v44, v45\n\t" \
  "v_cvt_pk_f16_f32 v45, v46, v47\n\tv_cvt_pk_f16_f32 v46, v40, v41\n\tv_cvt_pk_f16_f32 v47, v42, v43\n\t" \
  "s_nop 1\n\tv_mfma_f32_16x16x32_f16 v[64:67], v[44:47], v[88:91], v[64:67]\n\tv_mfma_f32_16x16x32_f16 v[52:55], v[44:47], v[92:95], v[52:55]\n\t" \
  "v_mfma_f32_16x16x32_f16 v[56:59], v[44:47], v[96:99], v[56:59]\n\tv_mfma_f32_16x16x32_f16 v[64:67], v[156:159], v[16:19], v[64:67]\n\tv_mfma_f32_16x16x32_f16 v[52:55], v[124:127], v[16:19], v[52:55]\n\t" \
  "ds_read_b128 v[16:19], v28 offset:2240\n\ts_waitcnt lgkmcnt(5)\n\tv_mfma_f32_16x16x32_f16 v[64:67], v[160:163], v[20:23], v[64:67]\n\t" \
  "v_mfma_f32_16x16x32_f16 v[52:55], v[128:131], v[20:23], v[52:55]\n\tds_read_b128 v[20:23], v28 offset:1504\n\ts_waitcnt lgkmcnt(5)\n\t" \
  "v_mfma_f32_16x16x32_f16 v[64:67], v[164:167], v[0:3], v[64:67]\n\tv_mfma_f32_16x16x32_f16 v[52:55], v[132:135], v[0:3], v[52:55]\n\tv_mfma_f32_16x16x32_f16 v[56:59], v[100:103], v[0:3], v[56:59]\n\t" \
  "ds_read_b128 v[0:3], v28 offset:2304\n\ts_waitcnt lgkmcnt(5)\n\tv_mfma_f32_16x16x32_f16 v[52:55], v[136:139], v[4:7], v[52:55]\n\t" \
  "v_mfma_f32_16x16x32_f16 v[56:59], v[104:107], v[4:7], v[56:59]\n\tds_read_b128 v[4:7], v28 offset:2368\n\ts_waitcnt lgkmcnt(5)\n\t" \
  "v_mfma_f32_16x16x32_f16 v[36:39], v[8:11], v[68:71], 0\n\tv_cvt_pk_bf16_f32 v64, v64, v65\n\tv_cvt_pk_bf16_f32 v65, v66, v67\n\t" \
  TS(66, 67) "s_cmp_lt_u32 %[w], 4\n\ts_cbranch_scc1 1f\n\t" \
  "s_setprio 1\n\t1:\n\tv_mfma_f32_16x16x32_f16 v[40:43], v[8:11], v[72:75], 0\n\t" \
  "ds_write_b64 v29, v[64:65] offset:1536\n\tds_write_b32 v25, v25 offset:192\n\tds_read_b128 v[8:11], v28 offset:480\n\t" \
  "s_waitcnt lgkmcnt(7)\n\tv_mfma_f32_16x16x32_f16 v[52:55], v[140:143], v[12:15], v[52:55]\n\tv_cvt_pk_f16_f32 v36, v36, v37\n\t" \
  "v_cvt_pk_f16_f32 v37, v38, v39\n\tv_mfma_f32_16x16x32_f16 v[56:59], v[108:111], v[12:15], v[56:59]\n\tds_read_b128 v[12:15], v32 offset:3072\n\t" \
  "v_cvt_pk_f16_f32 v38, v40, v41\n\tv_cvt_pk_f16_f32 v39, v42, v43\n\ts_waitcnt lgkmcnt(7)\n\t" \
  "v_mfma_f32_16x16x32_f16 v[52:55], v[144:147], v[16:19], v[52:55]\n\tv_mfma_f32_16x16x32_f16 v[56:59], v[112:115], v[16:19], v[56:59]\n\tds_read_b128 v[16:19], v28 offset:496\n\t" \
  "v_mfma_f32_16x16x32_f16 v[52:55], v[36:39], v[76:79], v[52:55]\n\tv_mfma_f32_16x16x32_f16 v[56:59], v[36:39], v[80:83], v[56:59]\n\tv_mfma_f32_16x16x32_f16 v[60:63], v[36:39], v[84:87], 0\n\t" \
  "s_waitcnt lgkmcnt(7)\n\tv_mfma_f32_16x16x32_f16 v[44:47], v[20:23], v[68:71], 0\n\tv_mfma_f32_16x16x32_f16 v[40:43], v[20:23], v[72:75], 0\n\t" \
  "ds_read_b128 v[20:23], v32 offset:2048\n\ts_waitcnt lgkmcnt(7)\n\tv_mfma_f32_16x16x32_f16 v[52:55], v[148:151], v[0:3], v[52:55]\n\t" \
  "v_mfma_f32_16x16x32_f16 v[56:59], v[116:119], v[0:3], v[56:59]\n\tds_read_b128 v[0:3], v28 offset:2432\n\ts_waitcnt lgkmcnt(7)\n\t" \
  "v_mfma_f32_16x16x32_f16 v[52:55], v[152:155], v[4:7], v[52:55]\n\tv_cvt_pk_f16_f32 v44, v44, v45\n\tv_cvt_pk_f16_f32 v45, v46, v47\n\t" \
  "v_cvt_pk_f16_f32 v46, v40, v41\n\tv_cvt_pk_f16_f32 v47, v42, v43\n\tv_mfma_f32_16x16x32_f16 v[56:59], v[120:123], v[4:7], v[56:59]\n\t" \
  "ds_read_b128 v[4:7], v28 offset:2496\n\tv_mfma_f32_16x16x32_f16 v[52:55], v[44:47], v[88:91], v[52:55]\n\tv_mfma_f32_16x16x32_f16 v[56:59], v[44:47], v[92:95], v[56:59]\n\t" \
  "v_mfma_f32_16x16x32_f16 v[60:63], v[44:47], v[96:99], v[60:63]\n\ts_waitcnt lgkmcnt(4)\n\tv_mfma_f32_16x16x32_f16 v[40:43], v[8:11], v[12:15], 0\n\t" \
  "ds_read_b64 v[8:9], v31 offset:544\n\tds_read_b64 v[10:11], v31 offset:712\n\tds_read_b64 v[12:13], v31 offset:512\n\t" \
  "ds_read_b64 v[14:15], v31 offset:680\n\ts_waitcnt lgkmcnt(7)\n\tv_mfma_f32_16x16x32_f16 v[52:55], v[48:51], v[16:19], v[52:55]\n\t" \
  "s_waitcnt lgkmcnt(6)\n\tv_mfma_f32_16x16x32_f16 v[36:39], v[16:19], v[20:23], 0\n\tds_read_b64 v[16:17], v31 offset:480\n\t" \
  "ds_read_b64 v[18:19], v31 offset:648\n\tds_read_b64 v[20:21], v31 offset:448\n\tds_read_b64 v[22:23], v31 offset:616\n\t" \
  "s_waitcnt lgkmcnt(9)\n\tv_mfma_f32_16x16x32_f16 v[52:55], v[156:159], v[0:3], v[52:55]\n\tv_mfma_f32_16x16x32_f16 v[56:59], v[124:127], v[0:3], v[56:59]\n\t" \
  "ds_read_b128 v[0:3], v28 offset:2560\n\tv_cvt_pk_f16_f32 v36, v36, v37\n\tv_cvt_pk_f16_f32 v37, v38, v39\n\t" \
  "s_waitcnt lgkmcnt(9)\n\tv_mfma_f32_16x16x32_f16 v[52:55], v[160:163], v[4:7], v[52:55]\n\tv_cvt_pk_f16_f32 v38, v40, v41\n\t" \
  "v_cvt_pk_f16_f32 v39, v42, v43\n\tv_mfma_f32_16x16x32_f16 v[56:59], v[128:131], v[4:7], v[56:59]\n\tds_read_b128 v[4:7], v28 offset:2624\n\t" \
  "s_waitcnt lgkmcnt(8)\n\tv_mfma_f32_16x16x32_f16 v[52:55], v[36:39], v[8:11], v[52:55]\n\tds_read_b128 v[8:11], v28 offset:2032\n\t" \
  "s_waitcnt lgkmcnt(7)\n\tv_mfma_f32_16x16x32_f16 v[56:59], v[36:39], v[12:15], v[56:59]\n\tds_read_b128 v[12:15], v28 offset:2688\n\t" \
  "s_waitcnt lgkmcnt(6)\n\tv_mfma_f32_16x16x32_f16 v[60:63], v[36:39], v[16:19], v[60:63]\n\tds_read_b128 v[16:19], v28 offset:2752\n\t" \
  "s_waitcnt lgkmcnt(5)\n\tv_mfma_f32_16x16x32_f16 v[64:67], v[36:39], v[20:23], 0\n\tds_read_b128 v[20:23], v28 offset:2016\n\t" \
  "s_waitcnt lgkmcnt(5)\n\tv_mfma_f32_16x16x32_f16 v[52:55], v[164:167], v[0:3], v[52:55]\n\tv_mfma_f32_16x16x32_f16 v[56:59], v[132:135], v[0:3], v[56:59]\n\t" \
  "v_mfma_f32_16x16x32_f16 v[60:63], v[100:103], v[0:3], v[60:63]\n\tds_read_b128 v[0:3], v28 offset:2816\n\ts_waitcnt lgkmcnt(5)\n\t" \
  "v_mfma_f32_16x16x32_f16 v[56:59], v[136:139], v[4:7], v[56:59]\n\tv_mfma_f32_16x16x32_f16 v[60:63], v[104:107], v[4:7], v[60:63]\n\tds_read_b128 v[4:7], v28 offset:2880\n\t" \
  "s_waitcnt lgkmcnt(5)\n\tv_mfma_f32_16x16x32_f16 v[44:47], v[8:11], v[68:71], 0\n\tv_cvt_pk_bf16_f32 v52, v52, v53\n\t" \
  "v_cvt_pk_bf16_f32 v53, v54, v55\n\ts_cmp_lt_u32 %[w], 4\n\ts_cbranch_scc0 1f\n\t" \
  "s_setprio 0\n\t1:\n\tv_mfma_f32_16x16x32_f16 v[40:43], v[8:11], v[72:75], 0\n\t" \
  "ds_write_b64 v29, v[52:53] offset:2048\n\tds_write_b32 v25, v25 offset:256\n\tds_read_b128 v[8:11], v28 offset:992\n\t" \
  "s_waitcnt lgkmcnt(7)\n\tv_mfma_f32_16x16x32_f16 v[56:59], v[140:143], v[12:15], v[56:59]\n\tv_cvt_pk_f16_f32 v44, v44, v45\n\t" \
  "v_cvt_pk_f16_f32 v45, v46, v47\n\tv_mfma_f32_16x16x32_f16 v[60:63], v[108:111], v[12:15], v[60:63]\n\tds_read_b128 v[12:15], v32 offset:3072\n\t" \
  "v_cvt_pk_f16_f32 v46, v40, v41\n\tv_cvt_pk_f16_f32 v47, v42, v43\n\ts_waitcnt lgkmcnt(7)\n\t" \
  "v_mfma_f32_16x16x32_f16 v[56:59], v[144:147], v[16:19], v[56:59]\n\tv_mfma_f32_16x16x32_f16 v[60:63], v[112:115], v[16:19], v[60:63]\n\tds_read_b128 v[16:19], v28 offset:1008\n\t" \
  "v_mfma_f32_16x16x32_f16 v[56:59], v[44:47], v[76:79], v[56:59]\n\tv_mfma_f32_16x16x32_f16 v[60:63], v[44:47], v[80:83], v[60:63]\n\tv_mfma_f32_16x16x32_f16 v[64:67], v[44:47], v[84:87], v[64:67]\n\t" \
  "s_waitcnt lgkmcnt(7)\n\tv_mfma_f32_16x16x32_f16 v[36:39], v[20:23], v[68:71], 0\n\tv_mfma_f32_16x16x32_f16 v[40:43], v[20:23], v[72:75], 0\n\t" \
  "ds_read_b128 v[20:23], v32 offset:2048\n\ts_waitcnt lgkmcnt(7)\n\tv_mfma_f32_16x16x32_f16 v[56:59], v[148:151], v[0:3], v[56:59]\n\t" \
  "v_mfma_f32_16x16x32_f16 v[60:63], v[116:119], v[0:3], v[60:63]\n\tds_read_b128 v[0:3], v28 offset:2944\n\ts_waitcnt lgkmcnt(7)\n\t" \
  "v_mfma_f32_16x16x32_f16 v[56:59], v[152:155], v[4:7], v[56:59]\n\tv_cvt_pk_f16_f32 v36, v36, v37\n\tv_cvt_pk_f16_f32 v37, v38, v39\n\t" \
  "v_cvt_pk_f16_f32 v38, v40, v41\n\tv_cvt_pk_f16_f32 v39, v42, v43\n\tv_mfma_f32_16x16x32_f16 v[60:63], v[120:123], v[4:7], v[60:63]\n\t" \
  "ds_read_b128 v[4:7], v28 offset:3008\n\tv_mfma_f32_16x16x32_f16 v[56:59], v[36:39], v[88:91], v[56:59]\n\tv_mfma_f32_16x16x32_f16 v[60:63], v[36:39], v[92:95], v[60:63]\n\t" \
  "v_mfma_f32_16x16x32_f16 v[64:67], v[36:39], v[96:99], v[64:67]\n\tds_read_b32 v34, v30\n\ts_waitcnt lgkmcnt(5)\n\t" \
  "v_mfma_f32_16x16x32_f16 v[40:43], v[8:11], v[12:15], 0\n\tds_read_b64 v[8:9], v31 offset:544\n\tds_read_b64 v[10:11], v31 offset:712\n\t" \
  "ds_read_b64 v[12:13], v31 offset:512\n\tds_read_b64 v[14:15], v31 offset:680\n\ts_waitcnt lgkmcnt(8)\n\t" \
  "v_mfma_f32_16x16x32_f16 v[56:59], v[48:51], v[16:19], v[56:59]\n\ts_waitcnt lgkmcnt(7)\n\tv_mfma_f32_16x16x32_f16 v[44:47], v[16:19], v[20:23], 0\n\t" \
  "ds_read_b64 v[16:17], v31 offset:480\n\tds_read_b64 v[18:19], v31 offset:648\n\ts_waitcnt lgkmcnt(6)\n\t" \
  "v_cmp_gt_u32 vcc, 5, v34\n\ts_cbranch_vccz 2f\n\ts_mov_b32 s6, 0x10000\n\t" \
  "1:\n\ts_sleep 1\n\tds_read_b32 v34, v30\n\t" \
  "s_sub_u32 s6, s6, 1\n\ts_waitcnt lgkmcnt(0)\n\tv_cmp_gt_u32 vcc, 5, v34\n\t" \
  "s_cbranch_scc1 2f\n\ts_cbranch_vccnz 1b\n\t2:\n\t" \
  "ds_read_b128 v[20:23], v28 offset:3072\n\tv_mfma_f32_16x16x32_f16 v[56:59], v[156:159], v[0:3], v[56:59]\n\tv_mfma_f32_16x16x32_f16 v[60:63], v[124:127], v[0:3], v[60:63]\n\t" \
  "ds_read_b128 v[0:3], v28 offset:3136\n\tv_cvt_pk_f16_f32 v44, v44, v45\n\tv_cvt_pk_f16_f32 v45, v46, v47\n\t" \
  "v_mfma_f32_16x16x32_f16 v[56:59], v[160:163], v[4:7], v[56:59]\n\tv_cvt_pk_f16_f32 v46, v40, v41\n\tv_cvt_pk_f16_f32 v47, v42, v43\n\t" \
  "v_mfma_f32_16x16x32_f16 v[60:63], v[128:131], v[4:7], v[60:63]\n\tds_read_b128 v[4:7], v28 offset:2544\n\ts_waitcnt lgkmcnt(7)\n\t" \
  "v_mfma_f32_16x16x32_f16 v[56:59], v[44:47], v[8:11], v[56:59]\n\tds_read_b128 v[8:11], v28 offset:3200\n\ts_waitcnt lgkmcnt(6)\n\t" \
  "v_mfma_f32_16x16x32_f16 v[60:63], v[44:47], v[12:15], v[60:63]\n\tds_read_b128 v[12:15], v28 offset:3264\n\ts_waitcnt lgkmcnt(5)\n\t" \
  "v_mfma_f32_16x16x32_f16 v[64:67], v[44:47], v[16:19], v[64:67]\n\tds_read_b128 v[16:19], v28 offset:2528\n\ts_waitcnt lgkmcnt(5)\n\t" \
  "v_mfma_f32_16x16x32_f16 v[56:59], v[164:167], v[20:23], v[56:59]\n\tv_mfma_f32_16x16x32_f16 v[60:63], v[132:135], v[20:23], v[60:63]\n\tv_mfma_f32_16x16x32_f16 v[64:67], v[100:103], v[20:23], v[64:67]\n\t" \
  "ds_read_b128 v[20:23], v28 offset:3328\n\ts_waitcnt lgkmcnt(5)\n\tv_mfma_f32_16x16x32_f16 v[60:63], v[136:139], v[0:3], v[60:63]\n\t" \
  "v_mfma_f32_16x16x32_f16 v[64:67], v[104:107], v[0:3], v[64:67]\n\tds_read_b128 v[0:3], v28 offset:3392\n\ts_waitcnt lgkmcnt(5)\n\t" \
  "v_mfma_f32_16x16x32_f16 v[36:39], v[4:7], v[68:71], 0\n\tv_cvt_pk_bf16_f32 v56, v56, v57\n\tv_cvt_pk_bf16_f32 v57, v58, v59\n\t" \
  "s_cmp_lt_u32 %[w], 4\n\ts_cbranch_scc1 1f\n\ts_setprio 0\n\t" \
  "1:\n\tv_mfma_f32_16x16x32_f16 v[40:43], v[4:7], v[72:75], 0\n\tds_write_b64 v29, v[56:57] offset:2560\n\t" \
  "ds_write_b32 v25, v25 offset:320\n\tds_read_b128 v[4:7], v28 offset:1504\n\ts_waitcnt lgkmcnt(7)\n\t" \
  "v_mfma_f32_16x16x32_f16 v[60:63], v[140:143], v[8:11], v[60:63]\n\tv_cvt_pk_f16_f32 v36, v36, v37\n\tv_cvt_pk_f16_f32 v37, v38, v39\n\t" \
  "v_mfma_f32_16x16x32_f16 v[64:67], v[108:111], v[8:11], v[64:67]\n\tds_read_b128 v[8:11], v32 offset:3072\n\tv_cvt_pk_f16_f32 v38, v40, v41\n\t" \
  "v_cvt_pk_f16_f32 v39, v42, v43\n\ts_waitcnt lgkmcnt(7)\n\tv_mfma_f32_16x16x32_f16 v[60:63], v[144:147], v[12:15], v[60:63]\n\t" \
  "v_mfma_f32_16x16x32_f16 v[64:67], v[112:115], v[12:15], v[64:67]\n\tds_read_b128 v[12:15], v28 offset:1520\n\tv_mfma_f32_16x16x32_f16 v[60:63], v[36:39], v[76:79], v[60:63]\n\t" \
  "v_mfma_f32_16x16x32_f16 v[64:67], v[36:39], v[80:83], v[64:67]\n\ts_waitcnt lgkmcnt(7)\n\tv_mfma_f32_16x16x32_f16 v[44:47], v[16:19], v[68:71], 0\n\t" \
  "v_mfma_f32_16x16x32_f16 v[40:43], v[16:19], v[72:75], 0\n\tds_read_b128 v[16:19], v32 offset:2048\n\ts_waitcnt lgkmcnt(7)\n\t" \
  "v_mfma_f32_16x16x32_f16 v[60:63], v[148:151], v[20:23], v[60:63]\n\tv_mfma_f32_16x16x32_f16 v[64:67], v[116:119], v[20:23], v[64:67]\n\tds_read_b128 v[20:23], v28 offset:3456\n\t" \
  "s_waitcnt lgkmcnt(7)\n\tv_mfma_f32_16x16x32_f16 v[60:63], v[152:155], v[0:3], v[60:63]\n\tv_cvt_pk_f16_f32 v44, v44, v45\n\t" \
  "v_cvt_pk_f16_f32 v45, v46, v47\n\tv_cvt_pk_f16_f32 v46, v40, v41\n\tv_cvt_pk_f16_f32 v47, v42, v43\n\t" \
  "v_mfma_f32_16x16x32_f16 v[64:67], v[120:123], v[0:3], v[64:67]\n\tds_read_b128 v[0:3], v28 offset:3520\n\tv_mfma_f32_16x16x32_f16 v[60:63], v[44:47], v[88:91], v[60:63]\n\t" \
  "v_mfma_f32_16x16x32_f16 v[64:67], v[44:47], v[92:95], v[64:67]\n\ts_waitcnt lgkmcnt(4)\n\tv_mfma_f32_16x16x32_f16 v[40:43], v[4:7], v[8:11], 0\n\t" \
  "ds_read_b64 v[4:5], v31 offset:544\n\tds_read_b64 v[6:7], v31 offset:712\n\tds_read_b64 v[8:9], v31 offset:512\n\t" \
  "ds_read_b64 v[10:11], v31 offset:680\n\ts_waitcnt lgkmcnt(7)\n\tv_mfma_f32_16x16x32_f16 v[60:63], v[48:51], v[12:15], v[60:63]\n\t" \
  "s_waitcnt lgkmcnt(6)\n\tv_mfma_f32_16x16x32_f16 v[36:39], v[12:15], v[16:19], 0\n\tds_read_b128 v[12:15], v28 offset:3584\n\t" \
  "ds_read_b128 v[16:19], v28 offset:3648\n\ts_waitcnt lgkmcnt(7)\n\tv_mfma_f32_16x16x32_f16 v[60:63], v[156:159], v[20:23], v[60:63]\n\t" \
  "v_mfma_f32_16x16x32_f16 v[64:67], v[124:127], v[20:23], v[64:67]\n\tds_read_b128 v[20:23], v28 offset:3056\n\ts_waitcnt lgkmcnt(7)\n\t" \
  "v_mfma_f32_16x16x32_f16 v[60:63], v[160:163], v[0:3], v[60:63]\n\tv_cvt_pk_f16_f32 v36, v36, v37\n\tv_cvt_pk_f16_f32 v37, v38, v39\n\t" \
  "v_cvt_pk_f16_f32 v38, v40, v41\n\tv_cvt_pk_f16_f32 v39, v42, v43\n\tv_mfma_f32_16x16x32_f16 v[64:67], v[128:131], v[0:3], v[64:67]\n\t" \
  "ds_read_b128 v[0:3], v28 offset:3712\n\ts_waitcnt lgkmcnt(6)\n\tv_mfma_f32_16x16x32_f16 v[60:63], v[36:39], v[4:7], v[60:63]\n\t" \
  "ds_read_b128 v[4:7], v28 offset:3776\n\ts_waitcnt lgkmcnt(5)\n\tv_mfma_f32_16x16x32_f16 v[64:67], v[36:39], v[8:11], v[64:67]\n\t" \
  "ds_read_b128 v[8:11], v28 offset:3040\n\ts_waitcnt lgkmcnt(5)\n\tv_mfma_f32_16x16x32_f16 v[60:63], v[164:167], v[12:15], v[60:63]\n\t" \
  "v_mfma_f32_16x16x32_f16 v[64:67], v[132:135], v[12:15], v[64:67]\n\tds_read_b128 v[12:15], v28 offset:3840\n\ts_waitcnt lgkmcnt(5)\n\t" \
  "v_mfma_f32_16x16x32_f16 v[64:67], v[136:139], v[16:19], v[64:67]\n\tds_read_b128 v[16:19], v28 offset:3904\n\ts_waitcnt lgkmcnt(5)\n\t" \
  "v_mfma_f32_16x16x32_f16 v[44:47], v[20:23], v[68:71], 0\n\tv_mfma_f32_16x16x32_f16 v[40:43], v[20:23], v[72:75], 0\n\tds_read_b128 v[20:23], v28 offset:2016\n\t" \
  "v_cvt_pk_bf16_f32 v60, v60, v61\n\tv_cvt_pk_bf16_f32 v61, v62, v63\n\ts_waitcnt lgkmcnt(5)\n\t" \
  "v_mfma_f32_16x16x32_f16 v[64:67], v[140:143], v[0:3], v[64:67]\n\tds_write_b64 v29, v[60:61] offset:3072\n\tds_write_b32 v25, v25 offset:384\n\t" \
  "ds_read_b128 v[0:3], v32 offset:3072\n\tv_cvt_pk_f16_f32 v44, v44, v45\n\tv_cvt_pk_f16_f32 v45, v46, v47\n\t" \
  "v_cvt_pk_f16_f32 v46, v40, v41\n\tv_cvt_pk_f16_f32 v47, v42, v43\n\ts_waitcnt lgkmcnt(7)\n\t" \
  "v_mfma_f32_16x16x32_f16 v[64:67], v[144:147], v[4:7], v[64:67]\n\tds_read_b128 v[4:7], v28 offset:2032\n\tv_mfma_f32_16x16x32_f16 v[64:67], v[44:47], v[76:79], v[64:67]\n\t" \
  "s_waitcnt lgkmcnt(7)\n\tv_mfma_f32_16x16x32_f16 v[36:39], v[8:11], v[68:71], 0\n\tv_mfma_f32_16x16x32_f16 v[40:43], v[8:11], v[72:75], 0\n\t" \
  "ds_read_b128 v[8:11], v32 offset:2048\n\ts_waitcnt lgkmcnt(7)\n\tv_mfma_f32_16x16x32_f16 v[64:67], v[148:151], v[12:15], v[64:67]\n\t" \
  "ds_read_b128 v[12:15], v28 offset:3968\n\ts_waitcnt lgkmcnt(7)\n\tv_mfma_f32_16x16x32_f16 v[64:67], v[152:155], v[16:19], v[64:67]\n\t" \
  "ds_read_b128 v[16:19], v28 offset:4032\n\tv_cvt_pk_f16_f32 v36, v36, v37\n\tv_cvt_pk_f16_f32 v37, v38, v39\n\t" \
  "v_cvt_pk_f16_f32 v38, v40, v41\n\tv_cvt_pk_f16_f32 v39, v42, v43\n\ts_nop 1\n\t" \
  "v_mfma_f32_16x16x32_f16 v[64:67], v[36:39], v[88:91], v[64:67]\n\ts_waitcnt lgkmcnt(4)\n\tv_mfma_f32_16x16x32_f16 v[40:43], v[20:23], v[0:3], 0\n\t" \
  "ds_read_b64 v[0:1], v31 offset:544\n\tds_read_b64 v[2:3], v31 offset:712\n\tds_read_b128 v[20:23], v28 offset:4096\n\t" \
  "s_waitcnt lgkmcnt(6)\n\tv_mfma_f32_16x16x32_f16 v[64:67], v[48:51], v[4:7], v[64:67]\n\ts_waitcnt lgkmcnt(5)\n\t" \
  "v_mfma_f32_16x16x32_f16 v[44:47], v[4:7], v[8:11], 0\n\ts_waitcnt lgkmcnt(4)\n\tv_mfma_f32_16x16x32_f16 v[64:67], v[156:159], v[12:15], v[64:67]\n\t" \
  "s_waitcnt lgkmcnt(3)\n\tv_mfma_f32_16x16x32_f16 v[64:67], v[160:163], v[16:19], v[64:67]\n\ts_nop 3\n\t" \
  "v_cvt_pk_f16_f32 v44, v44, v45\n\tv_cvt_pk_f16_f32 v45, v46, v47\n\tv_cvt_pk_f16_f32 v46, v40, v41\n\t" \
  "v_cvt_pk_f16_f32 v47, v42, v43\n\ts_waitcnt lgkmcnt(1)\n\ts_nop 0\n\t" \
  "v_mfma_f32_16x16x32_f16 v[64:67], v[44:47], v[0:3], v[64:67]\n\ts_waitcnt lgkmcnt(0)\n\tv_mfma_f32_16x16x32_f16 v[64:67], v[164:167], v[20:23], v[64:67]\n\t" \
  "s_nop 7\n\tv_cvt_pk_bf16_f32 v64, v64, v65\n\tv_cvt_pk_bf16_f32 v65, v66, v67\n\t" \
  "ds_write_b64 v29, v[64:65] offset:3584\n\tds_write_b32 v25, v25 offset:448\n\t"
#define CONV_CLOBBER "v0", "v1", "v2", "v3", "v4", "v5", "v6", "v7", "v8", "v9", "v10", "v11", "v12", "v13", "v14", "v15", "v16", "v17", "v18", "v19", "v20", "v21", "v22", "v23", "v36", "v37", "v38", "v39", "v40", "v41", "v42", "v43", "v44", "v45", "v46", "v47", "v52", "v53", "v54", "v55", "v56", "v57", "v58", "v59", "v60", "v61", "v62", "v63", "v64", "v65", "v66", "v67", "v68", "v69", "v70", "v71", "v72", "v73", "v74", "v75", "v76", "v77", "v78", "v79", "v80", "v81", "v82", "v83", "v84", "v85", "v86", "v87", "v88", "v89", "v90", "v91", "v92", "v93", "v94", "v95", "v96", "v97", "v98", "v99", "v100", "v101", "v102", "v103", "v104", "v105", "v106", "v107", "v108", "v109", "v110", "v111", "v112", "v113", "v114", "v115", "v116", "v117", "v118", "v119", "v120", "v121", "v122", "v123", "v124", "v125", "v126", "v127", "v128", "v129", "v130", "v131", "v132", "v133", "v134", "v135", "v136", "v137", "v138", "v139", "v140", "v141", "v142", "v143", "v144", "v145", "v146", "v147", "v148", "v149", "v150", "v151", "v152", "v153", "v154", "v155", "v156", "v157", "v158", "v159", "v160", "v161", "v162", "v163", "v164", "v165", "v166", "v167"
#define G_CLOBBER "v8", "v9", "v10", "v11", "v12", "v13", "v14", "v15", "v16", "v17", "v18", "v19", "v20", "v21", "v22", "v23", "v24", "v25", "v26", "v27", "v28", "v29", "v30", "v32", "v33", "v34", "v35", "v36", "v37", "v38", "v39", "v40", "v41", "v42", "v43", "v44", "v45", "v46", "v47", "v48", "v49", "v50", "v51", "v52", "v53", "v54", "v55", "v56", "v57", "v58", "v59", "v60", "v61", "v62", "v63", "v64", "v65", "v66", "v67", "v68", "v69", "v70", "v71", "v72", "v73", "v74", "v75", "v76", "v77", "v78", "v79", "v80", "v81", "v82", "v83", "v84", "v85", "v86", "v87", "v88", "v89", "v90", "v91", "v92", "v93", "v94", "v95", "v96", "v97", "v98", "v99", "v100", "v101", "v102", "v103", "v104", "v105", "v106", "v107", "v108", "v109", "v110", "v111", "v112", "v113", "v114", "v115", "v116", "v117", "v118", "v119", "v120", "v121", "v122", "v123", "v124", "v125", "v126", "v127"
#define D_CLOBBER "v8", "v9", "v10", "v11", "v12", "v13", "v14", "v15", "v16", "v17", "v18", "v19", "v20", "v21", "v22", "v23", "v24", "v25", "v26", "v27", "v28", "v29", "v30", "v31"
__device__ __forceinline__ float fmav(float a, float b, float c) {
  float r;
  asm("v_fma_f32 %0, %1, %2, %3" : "=v"(r) : "v"(a), "v"(b), "v"(c));
  return r;
}
__device__ __forceinline__ float fmas(float a, u32 s, float c) {
  asm("v_fmac_f32 %0, %2, %1" : "+v"(c) : "v"(a), "s"(s));
  return c;
}
#define RANK8(d0, d1, d2, d3, c0, c1, i0, f0, f1) "v_perm_b32 " d0 ", " d0 ", " d1 ", %[sg]\n\tv_perm_b32 " d2 ", " d2 ", " d3 ", %[sg]\n\t" \
  "v_bcnt_u32_b32 " d0 ", " d0 ", " i0 "\n\tv_bcnt_u32_b32 " c0 ", " d2 ", " d0 "\n\tv_bcnt_u32_b32 " c1 ", " d2 ", " d0 "\n\t" f0 "\n\t" \
  "v_permlane16_swap_b32 " c0 ", " c1 "\n\tv_add_u32 " d0 ", " c0 ", " c1 "\n\tv_add_u32 " d1 ", " c0 ", " c1 "\n\t" f1 "\n\t" \
  "v_permlane32_swap_b32 " d0 ", " d1 "\n\tv_add_u32 " c0 ", " d0 ", " d1 "\n\t"
__device__ __forceinline__ void ln_rows(u32x4 (&xv)[4], u32x8 wv, rsrc_t rs, u32& tag, int b, int w, int l, u32 st, u32 sg TLARG) {
  float s[2][3] = {}, c[2], sw = 0.f;
  u32 os = l < 16 ? O_SG + 128 * (b - 128) + 8 * l : 0x80000000u;
  u32 ot = l < 16 ? O_TOP + 8 * (b & 15) + TL * ((b >> 4) & 7) : l < 32 ? O_RG - 128 + 128 * (b - 128) + 8 * l : 0x80000000u, om = l < 16 ? TOPS / 8 : 0u, fm = l < 16 ? 0xff0u : 0u;
  u32 aw = (l & 16) ? st + 8 * w + (l >> 5) * 4 : st + 136, ac = st + 128, ar = st + 4 * (l & 15), aq = st + (l & 48), one = 1u;
  u32 kl = (2047 - (16 * (b - 128) + 2 * w + (l >> 5))) << 1;
  asm volatile("" : "+v"(os), "+v"(aw), "+v"(ac), "+v"(ar), "+v"(aq), "+v"(one), "+v"(kl), "+v"(ot), "+v"(om), "+v"(fm));
  SB();
  asm volatile("s_waitcnt vmcnt(4) lgkmcnt(0)" : "+{s44}"(tag)::"memory");
  asm volatile("" : "+{v[116:123]}"(wv));
  tag += 1u;
  *(volatile LDS u32*)(size_t)(st - 104) = tag;
  STAMP(12);
#pragma unroll
  for (int i = 0; i < 8; i++) sw = dot2b(wv[i], 0x3f803f80u, sw);
  sw += dppf<0x121, 0xf>(sw); sw += dppf<0x122, 0xf>(sw); sw += dppf<0x124, 0xf>(sw); sw += dppf<0x128, 0xf>(sw);
  sw = (rdl(sw, 0) + rdl(sw, 16)) + (rdl(sw, 32) + rdl(sw, 48));
  asm volatile("" : "+v"(sw));
  SB();
  float cv = 0.f;
#pragma unroll
  for (int r = 0; r < 2; r++) {
#pragma unroll
    for (int i = 0; i < 8; i++) {
      if (r == 0 && i == 0) { asm volatile("s_waitcnt vmcnt(3)" : "+{v[100:103]}"(xv[0])); STAMP(13); }
      if (r == 0 && i == 4) asm volatile("s_waitcnt vmcnt(2)" : "+{v[104:107]}"(xv[1]));
      if (r == 1 && i == 0) asm volatile("s_waitcnt vmcnt(1)" : "+{v[108:111]}"(xv[2]));
      if (r == 1 && i == 4) asm volatile("s_waitcnt vmcnt(0)" : "+{v[112:115]}"(xv[3]));
      const u32 a = xv[2 * r + (i >> 2)][i & 3];
      s[r][0] = dot2b(a, 0x3f803f80u, s[r][0]); s[r][1] = dot2b(a, a, s[r][1]); s[r][2] = dot2b(a, wv[i], s[r][2]);
      if (i == 3) {
        c[r] = rdl(s[r][0] * 0.125f, 0);
        if (r) { cv = l < 32 ? c[0] : c[1]; asm volatile("" : "+v"(cv)); }
        SB();
      }
    }
    const float ml = s[r][0] * 0.0625f, dl = __builtin_fmaf(s[r][0], 0.25f, -4.f * c[r]);
    s[r][1] = __builtin_fmaf(dl, dl, __builtin_fmaf(-s[r][0], ml, s[r][1]));
    SB();
  }
#pragma unroll
  for (int k = 0; k < 3; k++) swap32(s[0][k ? 3 - k : 0], s[1][k ? 3 - k : 0]);
  float q[3];
#pragma unroll
  for (int k = 0; k < 3; k++) q[k] = s[0][k] + s[1][k];
#define RED3(m) "v_add_f32_dpp %0, %0, %0 " m " bank_mask:0xf\n\tv_add_f32_dpp %1, %1, %1 " m " bank_mask:0xf\n\tv_add_f32_dpp %2, %2, %2 " m " bank_mask:0xf\n\t"
  asm volatile("s_nop 1\n\t" RED3("row_ror:1 row_mask:0xf") RED3("row_ror:2 row_mask:0xf") RED3("row_ror:4 row_mask:0xf") RED3("row_ror:8 row_mask:0xf")
               RED3("row_bcast:15 row_mask:0xa") : "+v"(q[0]), "+v"(q[1]), "+v"(q[2]));
  const float mu = q[0] * (1.f / D), dv = mu - cv;
  const float var = fmaxf(__builtin_fmaf(-dv, dv, q[1] * (1.f / D)), 0.f);
  const float m = rb(mu);
  float sd = __builtin_amdgcn_sqrtf(rb(rb(var) + 1e-5f)), num = __builtin_fmaf(-m, sw, q[2]);
  asm volatile("s_nop 0\n\tv_cvt_pk_bf16_f32 %0, 0, %0" : "+v"(sd), "+v"(num));
  u32 rv = __float_as_uint(rb(num * __builtin_amdgcn_rcpf(sd)));
  rv ^= (u32)((int)rv >> 15) | 0x80000000u;
  rv = __builtin_amdgcn_perm(rv, kl, 0x07060100u) >> 1;
  asm volatile("ds_write2_b32 %[aw], %[sv], %[rv] offset1:16\n\ts_mov_b64 exec, 1\n\tds_add_rtn_u32 v104, %[ac], %[one]\n\ts_mov_b64 exec, -1\n\t"
               "ds_read_b32 v106, %[ar]\n\tds_read_b32 v105, %[ar] offset:64\n\tds_read_b128 v[100:103], %[aq] offset:64\n\tv_mov_b32 v107, %[t]\n\tv_mov_b32 v109, %[t]\n\t"
               "s_waitcnt lgkmcnt(3)\n\tv_cmp_eq_u32 vcc, 7, v104\n\ts_bitcmp1_b32 vcc_lo, 0\n\ts_cbranch_scc0 1f\n\t"
               "s_waitcnt lgkmcnt(2)\n\tbuffer_store_dwordx2 v[106:107], %[os], %[rs], 0 offen sc1\n\ts_waitcnt lgkmcnt(0)\n\t"
               "v_sub_u32 v100, v105, v100\n\tv_sub_u32 v101, v105, v101\n\tv_sub_u32 v102, v105, v102\n\tv_sub_u32 v103, v105, v103\n\t"
               RANK8("v100", "v101", "v102", "v103", "v104", "v103", "0", "s_nop 1", "s_nop 1") "v_cmp_eq_u32 vcc, 16, v104\n\t"
               "v_sub_u32 v108, 19, v104\n\ts_ff1_i32_b32 s6, vcc_lo\n\tv_readlane_b32 s6, v105, s6\n\t"
               "v_mad_u32_u24 v104, v104, %[om], %[ot]\n\ts_nop 0\n\tv_lshrrev_b32 v108, v108, s6\n\tv_bfi_b32 v108, %[fm], v108, v105\n\t"
               "buffer_store_dwordx2 v[108:109], v104, %[rs], 0 offen sc1\n1:\n\ts_waitcnt lgkmcnt(0)"
               :: [aw] "v"(aw), [sv] "v"(__builtin_amdgcn_perm(__float_as_uint(sd), __float_as_uint(m), 0x07060302u)), [rv] "v"(rv), [ac] "v"(ac), [one] "v"(one),
                  [ar] "v"(ar), [aq] "v"(aq), [os] "v"(os), [om] "v"(om), [ot] "v"(ot), [fm] "v"(fm), [sg] "s"(sg), [t] "s"(tag), [rs] "s"(rs)
               : "vcc", "scc", "s6", "v100", "v101", "v102", "v103", "v104", "v105", "v106", "v107", "v108", "v109", "memory");
  SB();
}

#define GL __attribute__((address_space(1)))
typedef const GL u16* gp;

__global__ __launch_bounds__(768, 3) void fused(const u16*, const u16*, const u16*, const u16*, const u16*, const u16*, const u16*, const u16*,
    const u16*, const u16*, const u16*, u16*, char*) {
  __shared__ __attribute__((aligned(256))) char smem[SMEM];
  const u32 lds0 = (u32)(size_t)(LDS char*)smem;
  int w0;
#define GLD(r0, r1, r2, r3, o_) "global_load_dwordx4 v[" #r0 ":" #r1 "], " #o_ ", s[16:17]\n\tglobal_load_dwordx4 v[" #r2 ":" #r3 "], " #o_ ", s[16:17] offset:2048\n\t"
  asm volatile(
      "s_load_dwordx16 s[16:31], s[0:1], 0\n\ts_load_dwordx8 s[32:39], s[0:1], 64\n\ts_load_dwordx2 s[40:41], s[0:1], 96\n\t"
      "s_memrealtime s[46:47]\n\t"
      "s_getpc_b64 s[4:5]\n2:\n\ts_lshr_b32 s7, s2, 3\n\ts_and_b32 s10, s4, 0xffffff00\n\ts_mov_b32 s11, s5\n\ts_mul_i32 s8, s7, 0x600\n\t"
      "v_lshl_add_u32 v3, v0, 1, s8\n\ts_cmpk_lt_u32 s2, %[pn]\n\ts_cbranch_scc0 1f\n\t"
      "global_load_ushort v163, v3, s[10:11] offset:%[pf]\n"
      "1:\n\ts_cmpk_lt_u32 s2, 0x80\n\tv_readfirstlane_b32 s3, v0\n\tv_and_b32 v1, 63, v0\n\tv_mov_b64 v[4:5], 0\n\tv_mov_b64 v[6:7], 0\n\tv_lshlrev_b32 v2, 4, v1\n\tv_lshlrev_b32 v8, 2, v0\n\t"
      "s_mov_b32 s42, %[we]\n\ts_mov_b32 s43, 0x20000\n\ts_cbranch_scc0 3f\n\t"
      "s_lshr_b32 s3, s3, 6\n\ts_mul_i32 s9, s3, %[xr]\n\ts_min_u32 s9, s9, %[zm]\n\tv_add_u32 v3, s9, v2\n\tv_add_u32 v3, %[zo], v3\n\t"
      "s_and_b32 s7, s7, 7\n\ts_and_b32 s6, s2, 7\n\ts_and_b32 s8, s2, 64\n\ts_lshl3_add_u32 s8, s6, s8\n\ts_add_u32 s45, s8, s7\n\t"
      "s_add_u32 s4, s4, lane_tab@rel32@lo+(6f-2b-4)\n6:\n\ts_addc_u32 s5, s5, lane_tab@rel32@hi+(6b-2b+4)\n\t"
      "ds_write_b128 v3, v[4:7]\n\ts_cmp_lt_u32 s3, 8\n\ts_cbranch_scc1 5f\n\t"
      "s_lshl_b32 s8, s45, 4\n\ts_lshl_b32 s9, s3, 18\n\ts_add_u32 s9, s9, s8\n\ts_bitset0_b32 s9, 21\n\tv_lshl_add_u32 v148, v1, 12, s9\n\t"
      "s_lshl3_add_u32 s9, s3, s7\n\ts_lshl_b32 s9, s9, 17\n\ts_add_u32 s9, s9, s8\n\ts_bitset0_b32 s9, 23\n\tv_lshl_add_u32 v9, v1, 11, s9\n\t"
      "v_add_u32 v11, 0x100000, v148\n\tv_add_u32 v12, 0x200000, v148\n\t"
      "v_lshlrev_b32 v149, 4, v0\n\ts_lshl_b32 s9, s3, 10\n\ts_add_u32 m0, s9, %[ob]\n\tv_add_u32 v10, 0xffffe800, v149\n\tglobal_load_lds_dwordx4 v10, s[4:5]\n\t"
      "v_add_u32 v149, %[sg], v149\n\t"
      "s_waitcnt lgkmcnt(0)\n\ts_load_dword s44, s[40:41], 0\n\t"
      "s_barrier\n\t"
      "global_load_dword v160, v9, s[16:17]" PRENT "\n\t"
      GLD(100, 103, 104, 107, v148)
      GLD(108, 111, 112, 115, v11)
      "s_cmp_eq_u32 s6, 3\n\ts_cbranch_scc1 .Lrole_p\n\t"
      GLD(116, 119, 120, 123, v12)
      "s_branch .Lrole_p\n"
      "3:\n\ts_lshr_b32 s3, s3, 6\n\ts_cmp_lt_u32 s3, 8\n\ts_cbranch_scc1 4f\n\ts_barrier\n\ts_branch .Lrole_d\n4:\n\t"
      "v_add_u32 v3, %[fc], v2\n\ts_add_u32 s8, s3, s2\n\ts_and_b32 s45, s8, 7\n\ts_lshl3_add_u32 s8, s2, s45\n\t"
      "s_lshl_b32 s8, s8, 12\n\ts_bitset0_b32 s8, 22\n\tds_write_b128 v3, v[4:7]\n\tv_add_u32 v3, s8, v2\n\t"
      "s_waitcnt lgkmcnt(0)\n\tglobal_load_dwordx4 v[116:119], v2, s[30:31]\n\tglobal_load_dwordx4 v[120:123], v2, s[30:31] offset:1024\n\t"
      "s_load_dword s44, s[40:41], 0\n\ts_barrier\n\t"
      "global_load_dwordx4 v[100:103], v3, s[16:17]" LNNT "\n\tglobal_load_dwordx4 v[104:107], v3, s[16:17] offset:1024" LNNT "\n\t"
      "global_load_dwordx4 v[108:111], v3, s[16:17] offset:2048" LNNT "\n\tglobal_load_dwordx4 v[112:115], v3, s[16:17] offset:3072" LNNT "\n\t"
      "s_branch .Lrole_l\n"
      "5:\n\t"
      "global_load_dwordx4 v[16:19], v2, s[4:5]\n\tglobal_load_dwordx4 v[20:23], v2, s[4:5] offset:1024\n\t"
      "s_lshl3_add_u32 s8, s45, s3\n\ts_lshl_b32 s9, s8, 1\n\tv_mov_b32 v9, s9\n\ts_lshl_b32 s9, s45, 11\n\tv_add_u32 v3, s9, v8\n\t"
      "s_waitcnt lgkmcnt(0)\n\tglobal_load_short_d16_hi v24, v9, s[26:27]\n\tglobal_load_ushort v160, v9, s[28:29]\n\t"
      "global_load_dword v96, v3, s[18:19]" PNT "\n\tglobal_load_dword v97, v3, s[20:21]" PNT "\n\t"
      "global_load_dword v68, v3, s[22:23]" PNT "\n\tglobal_load_dword v69, v3, s[24:25]" PNT "\n\ts_barrier\n\ts_branch .Lrole_c"
      : "={s3}"(w0)
      : "{s[0:1]}"(__builtin_amdgcn_kernarg_segment_ptr()), "{s2}"(blockIdx.x), "{v0}"(threadIdx.x), [pf] "n"(PFO), [pn] "n"(8 * PFN), [we] "n"(WS_END), [xr] "n"(XREG),
        [zm] "n"(8 * XREG), [zo] "n"(OX - 16), [sg] "n"(O_SG - 8192), [fc] "n"(FC), [ob] "n"(OB - 8192)
      : "memory");
#define TLREGS()
#define ROLE(name, text, ...) \
  gp x, wg, wu, wd; GL u16* out; rsrc_t rs; int b, w, t, l, g; u32 tag, st0; \
  asm volatile(".Lrole_" #name ":" : "={v0}"(t), "={v1}"(l) __VA_ARGS__ :: "memory"); \
  asm volatile(text : "={s2}"(b), "={s3}"(w), "={s[16:17]}"(x), "={s[32:33]}"(wg), "={s[34:35]}"(wu), "={s[36:37]}"(wd), \
               "={s[38:39]}"(out), "={s[40:43]}"(rs), "={s44}"(tag), "={s45}"(g), "={s46}"(st0) :: "memory"); \
  SB(); \
  __builtin_assume((u32)l < 64u); __builtin_assume((u32)t < 768u); __builtin_assume((u32)w < 12u); __builtin_assume((u32)b < 256u); __builtin_assume((u32)g < 128u); \
  TLREGS(); \
  const int xk = b & 7, j = (b >> 3) & 15, n16 = l & 15, kg = l >> 4
#define FLAG(o, v) do { asm volatile("" ::: "memory"); SB(); *(volatile LDS u32*)(LDS char*)(smem + (o)) = (v); SB(); } while (0)
#define CWAIT(o, n, nap) do { _Pragma("unroll 1") for (int spin = 0; spin < SPIN; spin++) { \
      if (__builtin_amdgcn_readfirstlane((int)*(const volatile LDS u32*)(LDS char*)(smem + (o))) >= (int)(n)) break; \
      asm volatile(nap ::: "memory"); } SB(); asm volatile("" ::: "memory"); } while (0)
#define AWAIT(o, n, nap) do { _Pragma("unroll 1") for (int spin = 0; spin < SPIN; spin++) { \
      if (__all(*(const volatile LDS u32*)(LDS char*)(smem + (o)) >= (u32)(n))) break; \
      asm volatile(nap ::: "memory"); } SB(); asm volatile("" ::: "memory"); } while (0)
#define POLL8(res_, off_) do { u32 n_ = SPIN; \
    asm volatile("1:\n\tbuffer_load_dwordx2 v[4:5], %2, %3, 0 offen sc1\n\ts_sub_u32 %1, %1, 1\n\ts_waitcnt vmcnt(0)\n\t" \
                 "v_cmp_ne_u32 vcc, %4, v5\n\ts_cbranch_scc1 2f\n\ts_cbranch_vccnz 1b\n2:" \
                 : "=&{v[4:5]}"(res_), "+s"(n_) : "v"(off_), "s"(rs), "s"(tag) : "vcc", "scc", "memory"); } while (0)
#define UNTIL(t_) "2:\n\ts_sleep 1\n\ts_memrealtime s[8:9]\n\ts_waitcnt lgkmcnt(0)\n\ts_sub_u32 s8, s8, %[st]\n\ts_cmp_lt_u32 s8, " t_ "\n\ts_cbranch_scc1 2b\n\t"
#define IXO(l_) (O_IDX + (2 * xk + (j & 1)) * 128 + ((l_) & 7) * 8)
#define IDS() const int id = 16 * xk + j, sb = id * 57 >> 10, mat = id & 1, u = (id - 18 * sb) >> 1; \
    const bool slow = u >= 6; \
    const int part = slow ? u - 6 : u & 1, cb = 4 * sb + (slow ? 1 : (u >> 1) + (u > 1)), r0 = part * (slow ? 320 : 512), ns = slow ? 5 + (u >> 3) : 8; \
    const int kp = 4 * sb + ((u >> 1) & 3), ct = 2 * (u & 1) + mat
#define TS(a, b_)
#define TSC
#define DONE "s_endpgm\n"
#define TSD
#define TSGET(i0, i1, i2, i3)
  if (blockIdx.x >= 128) {
    if (w0 >= 8) {
      ROLE(d, "s_waitcnt lgkmcnt(0)");
      IDS();
      const int dw = w - 8;
      const u32 sw = (((n16 >> 1) ^ (kg & 1)) << 5) + ((l & 1) << 4);
      gp base = (mat ? wu : wg) + (size_t)(r0 + 4 * dw) * F + cb * 128;
      u32 v = kg * (2 * F) + sw, fa = lds0 + FC + 4 * dw, vp = 96 + dw, t_;
      asm volatile("" : "+v"(v), "+v"(fa), "+v"(vp));
      asm volatile("s_mov_b32 m0, %0" ::"s"(lds0 + 1024 * dw));
#define VMC "s_getreg_b32 s6, hwreg(HW_REG_IB_STS, 0, 4)\n\t"
#define RUN(k, q, lim, v_, b_, st_) asm volatile("1:\n\ts_lshr_b32 s6, m0, 4\n\ts_and_b32 s6, s6, 0xc0\n\tv_xor_b32 %[t], s6, %[v]\n\ts_waitcnt vmcnt(%[c])\n\t" VMC \
        "global_load_lds_dwordx4 %[t], %[b] nt\n\ts_add_u32 m0, m0, %[m]\n\tv_add_u32 %[v], %[d], %[v]\n\t.if %[m] == 3072\n\tv_mad_i32_i24 %[t], s6, -3, %[p]\n\tv_add_u32 %[p], 3, %[p]\n\t" \
        "s_mov_b64 exec, 1\n\tds_max_u32 %[a], %[t]\n\ts_mov_b64 exec, -1\n\t.endif\n\ts_cmp_lt_u32 m0, %[l]\n\ts_cbranch_scc1 1b" \
        : [v] "+v"(v_), [p] "+v"(vp), [t] "=&v"(t_) : [b] "s"(b_), [c] "n"((q) - 1), [m] "n"(1024 * (k)), [d] "n"((k) * (st_)), \
          [a] "v"(fa), [l] "s"(lim) : "scc", "s6", "memory")
#define MARK(c, val) "s_waitcnt vmcnt(" #c ")\n\t" val "\n\ts_mov_b64 exec, 1\n\tds_max_u32 %[a], %[t]\n\ts_mov_b64 exec, -1\n\t"
      __builtin_amdgcn_s_setprio(DPRIO);
      STAMP(8);
      if (id < 126) { if (slow) RUN(4, QAS, 32768, v, base, 8 * F); else RUN(4, 8, 32768, v, base, 8 * F); }
      if (dw < 3) {
        if (id >= 126) __builtin_amdgcn_endpgm();
        gp bd = wd + (size_t)(128 * kp + 2 * dw) * D + ct * 256;
        u32 vd = (l >> 5) * (2 * D + 32) ^ 16 * (l & 31);
        asm volatile("" : "+v"(vd));
        if (slow) RUN(3, QS, 16384 * ns, v, base, 8 * F);
        else { RUN(3, 8, 32768 + 3072 * RAMP, v, base, 8 * F); RUN(3, QD, 131072, v, base, 8 * F); }
        STAMP(1);
        {
          u32 n_ = SPIN;
          asm volatile("1:\n\t" VMC MARK(63, "v_mad_i32_i24 %[t], s6, -3, %[p]") "ds_read_b32 %[t], %[f]\n\ts_sub_u32 %[n], %[n], 1\n\ts_waitcnt lgkmcnt(0)\n\t"
                       "v_cmp_lt_u32 vcc, %[k], %[t]\n\ts_cbranch_scc1 2f\n\ts_cbranch_vccz 1b\n2:\n\ts_mov_b32 m0, %[m]"
                       : [t] "=&v"(t_), [n] "+s"(n_) : [a] "v"(fa), [p] "v"(vp), [f] "v"(u == 8 ? fa : lds0 + FC + 20), [k] "s"(u == 8 ? 159 : 16 * HOLD - 105), [m] "s"(lds0 + 1024 * dw)
                       : "vcc", "scc", "s6", "memory");
        }
        STAMP(3);
        if (u < 8) {
          RUN(3, QE, 65536, vd, bd, 4 * D);
          STAMP(6);
          asm volatile(MARK(0, "v_mov_b32 %[t], -1") : [t] "=&v"(t_) : [a] "v"(fa) : "memory");
          STAMP(4);
        }
        FLUSH();
        __builtin_amdgcn_endpgm();
      }
      asm volatile(MARK(0, "v_mov_b32 %[t], -1") : [t] "=&v"(t_) : [a] "v"(fa) : "memory");
      u32 km, rk;
      u32 hi, lo, k2, k3, gd, thr, sg = 0x0c0c0b09u;
      u32 ka = lds0 + FC + 384 + 8 * (l >> 3), kc = 2047 * 2048 + 2 * r0 + 16 * ((l & 7) ^ ((l >> 2) & 4)), kq = lds0 + FC + 512 + (l & 48), kr = lds0 + FC + 512 + 4 * n16;
      u32 c1 = 896, c2 = 1792, c3 = 2688, cm = (u32)-2048, cs = O_SG + 8 * 2047, ca = lds0 + FC + 512;
      rsrc_t rx = __builtin_amdgcn_make_buffer_rsrc((void*)(x - 1344), 0, 2 * L * D + 4096, 0x00020000);
      u32 co = O_CLR + 8 * (18 * sb + (0x84c0 >> 4 * (kp & 3) & 15) + min(l, (kp & 3) == 1 ? 5 : 3));
      asm volatile("" : "+v"(ka), "+v"(kc), "+v"(kq), "+v"(kr), "+v"(co));
      asm volatile("" : "+s"(c1), "+s"(c2), "+s"(c3), "+s"(cm), "+s"(cs), "+s"(ca), "+s"(sg), "+s"(rx));
      *(u32x4*)(smem + FA + 6144 + 16 * l) = u32x4{0u, 0u, 0u, 0u};
      *(u32x4*)(smem + FA + 7168 + 16 * l) = u32x4{0u, 0u, 0u, 0u};
      CWAIT(FC + 256, 1, "s_sleep 4");
      tag = (u32)__builtin_amdgcn_readfirstlane((int)*(const volatile LDS u32*)(LDS char*)(smem + FC + 24));
      {
        u32 n_ = SPIN, t0, t1, t2, t3, se;
#define TLX(a, b_, c, d, sc) "buffer_load_dwordx4 v[" #a ":" #b_ "], %[o], %[r], 0 offen" sc "\n\tbuffer_load_dwordx4 v[" #c ":" #d "], %[o], %[r], %[ts] offen" sc "\n\t"
#define TLD(a, b_, c, d) TLX(a, b_, c, d, " sc1")
#define TCK(r0, r1, r2, r3, r4, r5, r6, r7) "s_waitcnt vmcnt(4)\n\tv_xor_b32 %[t0], %[t], v" #r1 "\n\tv_bitop3_b32 %[t0], %[t0], %[t], v" #r3 " bitop3:0xf6\n\t" \
          "v_bitop3_b32 %[t0], %[t0], %[t], v" #r5 " bitop3:0xf6\n\tv_bitop3_b32 %[t0], %[t0], %[t], v" #r7 " bitop3:0xf6\n\t" \
          "v_cmp_ne_u32 vcc, 0, %[t0]\n\tv_mov_b32 %[hi], v" #r0 "\n\tv_mov_b32 %[lo], v" #r2 "\n\tv_mov_b32 %[k2], v" #r4 "\n\tv_mov_b32 %[k3], v" #r6 "\n\t" \
          "s_cbranch_vccz 5f\n\t" TLD(r0, r3, r4, r7)
        asm volatile("2:\n\ts_sleep 1\n\ts_memrealtime s[8:9]\n\ts_waitcnt lgkmcnt(0)\n\ts_sub_u32 s8, s8, %[st]\n\ts_cmp_lt_u32 s8, %[tw]\n\ts_cbranch_scc1 2b\n\t"
                     TLX(128, 131, 132, 135, SCT) "s_sleep " STR(RSL) "\n\t" TLD(136, 139, 140, 143) "s_sleep " STR(RSL) "\n\t" TLX(144, 147, 148, 151, SCU)
                     "1:\n\t" TCK(128, 129, 130, 131, 132, 133, 134, 135) TCK(136, 137, 138, 139, 140, 141, 142, 143) TCK(144, 145, 146, 147, 148, 149, 150, 151)
                     "s_sub_u32 %[n], %[n], 1\n\ts_cbranch_scc0 1b\n5:\n\t" TS(60, 61)
                     "v_and_or_b32 %[t0], %[hi], %[mk], %[pa]\n\tv_and_or_b32 %[t1], %[lo], %[mk], %[pb]\n\t"
                     "v_max_u32 %[t2], %[t0], %[t1]\n\tv_max_u32 %[t3], %[t0], %[t1]\n\tv_perm_b32 %[gd], %[hi], %[lo], %[p0]\n\tv_perm_b32 %[se], %[k2], %[k3], %[p0]\n\t"
                     "v_permlane16_swap_b32 %[t2], %[t3]\n\tv_max_u32 %[km], %[t2], %[t3]\n\tv_max_u32 %[rk], %[t2], %[t3]\n\t"
                     "v_max_u32 %[hi], %[t0], %[t1]\n\tv_min_u32 %[lo], %[t0], %[t1]\n\t"
                     "v_permlane32_swap_b32 %[km], %[rk]\n\tv_max_u32 %[km], %[km], %[rk]\n\tv_not_b32 %[t2], %[km]\n\t"
                     "v_and_or_b32 %[k2], %[k2], %[mk], %[pa]\n\t"
                     "v_not_b32_dpp %[t2], %[km] row_ror:4 row_mask:0x2 bank_mask:0xf\n\tv_not_b32_dpp %[t2], %[km] row_ror:8 row_mask:0x4 bank_mask:0xf\n\t"
                     "v_not_b32_dpp %[t2], %[km] row_ror:12 row_mask:0x8 bank_mask:0xf\n\t"
                     "v_pk_lshlrev_b16 %[gd], 4, %[gd] op_sel_hi:[0,1]\n\tv_add_u32 %[t0], %[km], %[t2]\n\t"
                     "v_add_u32_dpp %[t1], %[t2], %[km] row_ror:1 row_mask:0xf bank_mask:0xf\n\tv_add_u32_dpp %[t3], %[t2], %[km] row_ror:2 row_mask:0xf bank_mask:0xf\n\t"
                     "v_add_u32_dpp %[t2], %[t2], %[km] row_ror:3 row_mask:0xf bank_mask:0xf\n\t"
                     RANK8("%[t0]", "%[t1]", "%[t2]", "%[t3]", "%[rk]", "%[t3]", "-2", "v_lshrrev_b32 %[se], 4, %[se]\n\tv_perm_b32 %[gd], %[gd], %[se], %[p1]",
                           "v_pk_max_u16 %[gd], %[gd], %[gd] op_sel:[0,1] op_sel_hi:[1,0]\n\tv_and_or_b32 %[k3], %[k3], %[mk], %[pb]")
                     "v_cmp_eq_u32 vcc, 56, %[rk]\n\tv_max3_u32 %[se], %[lo], %[k2], %[k3]\n\tv_lshrrev_b32 %[gd], 1, %[gd]\n\ts_ff1_i32_b32 s6, vcc_lo\n\t"
                     "v_readlane_b32 s6, %[km], s6\n\tv_or_b32 %[gd], 0x7fff, %[gd]\n\tv_med3_u32 %[t2], %[lo], %[k2], %[k3]\n\t"
                     "v_cmp_le_u32 s[8:9], s6, %[hi]\n\tv_cmp_le_u32 s[10:11], s6, %[se]\n\tv_max_u32 %[t2], %[t2], %[gd]\n\t"
                     "v_mbcnt_lo_u32_b32 %[t0], s8, 0\n\tv_mbcnt_lo_u32_b32 %[t1], s10, -15\n\tv_mbcnt_hi_u32_b32 %[t0], s9, %[t0]\n\tv_mbcnt_hi_u32_b32 %[t1], s11, %[t1]\n\t"
                     "v_lshl_add_u32 %[t0], %[t0], 2, %[ca]\n\tv_mad_i32_i24 %[t1], %[t1], -4, %[ca]\n\tv_cmp_le_u32 vcc, s6, %[t2]\n\t"
                     "s_mov_b64 exec, s[8:9]\n\tds_write_b32 %[t0], %[hi]\n\ts_mov_b64 exec, s[10:11]\n\tds_write_b32 %[t1], %[se]\n\ts_mov_b64 exec, -1\n\t"
                     "ds_read_b32 %[se], %[kr]\n\tds_read_b128 v[152:155], %[kq]\n\t"
                     "s_bcnt1_i32_b64 s8, s[8:9]\n\ts_bcnt1_i32_b64 s10, s[10:11]\n\ts_add_u32 s8, s8, s10\n\ts_cmp_gt_u32 s8, 16\n\ts_cselect_b64 s[8:9], -1, vcc\n\t"
                     "s_cmp_lg_u64 s[8:9], 0\n\ts_waitcnt lgkmcnt(0)\n\ts_cbranch_scc1 6f\n\t"
                     "v_sub_u32 %[t0], %[se], v152\n\tv_sub_u32 %[t1], %[se], v153\n\tv_sub_u32 %[t2], %[se], v154\n\tv_sub_u32 %[t3], %[se], v155\n\t"
                     RANK8("%[t0]", "%[t1]", "%[t2]", "%[t3]", "%[rk]", "%[t3]", "0", "v_mov_b32 %[km], %[se]\n\ts_nop 0", "s_nop 1") "6:\n\t" TS(62, 63)
                     : [hi] "=&v"(hi), [lo] "=&v"(lo), [k2] "=&v"(k2), [k3] "=&v"(k3), [gd] "=&v"(gd), [se] "=&v"(se), [km] "=&v"(km), [rk] "=&v"(rk),
                       [t0] "=&v"(t0), [t1] "=&v"(t1), [t2] "=&v"(t2), [t3] "=&v"(t3), [n] "+s"(n_)
                     : [o] "v"(O_TOP + 16 * (l & 7) + TL * (l >> 3)), [r] "s"(rs), [t] "s"(tag), [ts] "s"(TOPS), [st] "s"(st0), [tw] "n"(TW), [mk] "s"(0x7fff800fu),
                       [pa] "v"((127 - 2 * l) << 4), [pb] "v"((126 - 2 * l) << 4), [sg] "s"(sg), [p0] "s"(0x05040100u), [p1] "s"(0x07020500u), [ca] "s"(ca),
                       [kr] "v"(kr), [kq] "v"(kq)
                     : "vcc", "scc", "s6", "s8", "s9", "s10", "s11", "memory" TSC);
      }
      unsigned long long more;
      asm volatile("" : "={s6}"(thr), "={s[8:9]}"(more));
      if (__builtin_expect(more != 0, 0)) {
        const u32 m0 = km;
        unsigned long long all = __ballot(gd >= thr);
        if (gd >= thr) hi = lo = k2 = k3 = 0u;
#pragma unroll 1
        for (int i = 0; i < 4;) {
          u32 k, m = m0;
          if (all) {
            const int a = __builtin_ctzll(all);
            all &= all - 1;
            u32x2 r;
            const u32 o = O_RG + 256 * a + 8 * (l & 31);
            POLL8(r, o);
            k = l < 32 ? r.x : 0u;
            m = (u32)__builtin_amdgcn_readlane((int)m0, a);
          } else { k = hi; hi = lo; lo = k2; k2 = k3; i++; }
          asm volatile("v_cmp_le_u32 s[8:9], %[t], %[k]\n\tv_cmp_ne_u32 vcc, %[k], %[m0]\n\ts_and_b64 s[8:9], s[8:9], vcc\n\ts_cbranch_scc0 2f\n"
                       "1:\n\ts_ff1_i32_b64 s7, s[8:9]\n\tv_readlane_b32 s12, %[k], s7\n\ts_lshl_b64 s[10:11], 1, s7\n\ts_nop 0\n\t"
                       "v_cmpx_gt_u32 vcc, s12, %[km]\n\tv_add_u32 %[rk], 8, %[rk]\n\ts_bcnt0_i32_b32 s7, vcc_lo\n\tv_cmpx_eq_u32 vcc, 0x80, %[rk]\n\t"
                       "v_mov_b32 %[km], s12\n\tv_lshlrev_b32 %[rk], 2, s7\n\ts_mov_b64 exec, -1\n\ts_andn2_b64 s[8:9], s[8:9], s[10:11]\n\ts_cbranch_scc1 1b\n2:"
                       : [km] "+v"(km), [rk] "+v"(rk) : [k] "v"(k), [m0] "v"(m), [t] "s"(thr) : "vcc", "scc", "s7", "s8", "s9", "s10", "s11", "s12");
        }
      }
      if (id < 126) {
        u32 a_, v_, n_ = SPIN;
#define RLD(o, s_) "buffer_load_dwordx4 %[v], %[rx], " s_ " offen offset:" #o " lds\n\t"
        asm volatile("v_add_u32 %[a], %[kb], %[rk]\n\tv_and_b32 %[v], 0x7ff, %[km]\n\ts_mov_b32 m0, %[fa]\n\tds_write_b32 %[a], %[v]\n\tds_read_b32 %[a], %[ka]\n\t"
                     "s_cmp_eq_u32 %[ns], 8\n\ts_waitcnt lgkmcnt(0)\n\tv_mad_i32_i24 %[v], %[a], %[cm], %[kc]\n\t"
                     RLD(0, "%[c3]") RLD(1024, "%[c2]") RLD(2048, "%[c1]") RLD(3072, "0")
                     "s_mov_b32 m0, %[fb]\n\tv_add_u32 %[v], 0x200, %[v]\n\tv_mad_i32_i24 %[a], %[a], -8, %[cs]\n\t"
                     "s_cbranch_scc0 3f\n\t" RLD(2048, "%[c1]") RLD(3072, "0")
                     "3:\n\tbuffer_load_dwordx2 v[160:161], %[a], %[rs], 0 offen sc1\n\t" RLD(0, "%[c3]") RLD(1024, "%[c2]") "buffer_load_dwordx2 v[158:159], %[co], %[rs], 0 offen sc1\n\t"
                     "v_mov_b32 %[v], 16 * " STR(HOLD) " - 112\n\tv_subrev_u32 %[ka], %[kd], %[ka]\n\ts_mov_b64 exec, 1\n\tds_add_u32 %[fc], %[v] offset:8\n\ts_mov_b64 exec, -1\n\tv_mov_b32 %[v], -1\n\t" TS(64, 65)
                     "s_sleep " STR(CSL) "\n\tbuffer_load_dwordx2 v[154:155], %[co], %[rs], 0 offen sc1\n\t"
                     "s_waitcnt vmcnt(4)\n1:\n\tv_cmp_ne_u32 vcc, %[t], v161\n\tv_and_b32 v156, 0xffff0000, v160\n\tv_lshlrev_b32 v157, 16, v160\n\tv_rcp_f32 v156, v156\n\t"
                     "s_sub_u32 %[n], %[n], 1\n\ts_cbranch_vccz 2f\n\ts_cbranch_scc1 2f\n\t"
                     "buffer_load_dwordx2 v[160:161], %[a], %[rs], 0 offen sc1\n\ts_waitcnt vmcnt(0)\n\ts_branch 1b\n2:\n\tv_mul_f32 v157, -v157, v156\n\t"
                     "ds_write_b64 %[ka], v[156:157]\n\ts_waitcnt vmcnt(2)\n\tds_write_b32 %[fc], %[v] offset:4\n\t" TS(66, 67)
                     "s_mov_b32 %[n], %[spin]\n\ts_waitcnt vmcnt(1)\n\tv_cmp_ne_u32 vcc, %[t], v159\n\ts_cbranch_vccz 5f\n"
                     "4:\n\ts_waitcnt vmcnt(0)\n\tv_cmp_ne_u32 vcc, %[t], v155\n\ts_sub_u32 %[n], %[n], 1\n\ts_cbranch_vccz 5f\n\t"
                     "buffer_load_dwordx2 v[154:155], %[co], %[rs], 0 offen sc1\n\ts_cbranch_scc0 4b\n5:"
                     : [a] "=&v"(a_), [v] "=&v"(v_), [ka] "+v"(ka), [n] "+s"(n_)
                     : [co] "v"(co), [rk] "v"(rk), [km] "v"(km), [kc] "v"(kc), [kb] "n"(FC + 384), [kd] "n"(384 - 32), [fa] "n"(FA), [fb] "n"(FA + 4096), [rx] "s"(rx), [rs] "s"(rs),
                       [c1] "s"(c1), [c2] "s"(c2), [c3] "s"(c3), [cm] "s"(cm), [cs] "s"(cs), [ns] "s"(ns), [fc] "v"(fa), [t] "s"(tag), [spin] "n"(SPIN) : "vcc", "scc", "memory" TSC);
        TSGET(12, 13, 14, 15);
        FLAG(FC + 28, 1u);
        STAMP(10);
      } else {
        const u32 cd = km << 1;
        const u32x2 r = {(2047u - (km & 0x7ffu)) | ((cd ^ ((u32)((int)~cd >> 15) | 0x80000000u)) & 0xffff0000u), tag};
        const u32 o = rk < 64u ? O_IDX + 1024 * mat + 128 * kg + rk : 0x80000000u;
        st8(rs, o, r);
        st8(rs, o + 512, r);
        TSGET(12, 13, 14, 15);
        STAMP(14);
        STAMP(15);
        if (mat) {
#pragma unroll 1
          for (int spin = 0; spin < SPIN; spin++) {
            const u32x4 a = ld16(rs, O_ARR + 16 * l);
            if (__all(a.y == tag && a.w == tag)) break;
            asm volatile("s_sleep 16" ::: "memory");
          }
          if (l == 0) __builtin_amdgcn_raw_buffer_store_b32(tag, rs, 0, 0, 16);
        }
      }
      FLUSH();
      __builtin_amdgcn_endpgm();
    }
    u32x4 xv[4];
    u32x8 wv;
    ROLE(l, "", , "={v[100:103]}"(xv[0]), "={v[104:107]}"(xv[1]), "={v[108:111]}"(xv[2]), "={v[112:115]}"(xv[3]), "={v[116:123]}"(wv));
    STAMP(11);
    u32 sg = 0x0c0c0b09u;
    asm volatile("" : "+s"(sg));
    ln_rows(xv, wv, rs, tag, b, g, l, lds0 + FC + 128, sg TLVAL);
    STAMP(10);
    IDS();
    if (id >= 126) {
      FLUSH();
      __builtin_amdgcn_endpgm();
    }
    const int wr = 16 * (kg >> 1) + 4 * (kg & 1) + (n16 & 8) + ((n16 >> 2) & 1);
    u32 wa = lds0 + 256 * wr + ((w ^ ((wr & 1) | ((wr >> 1) & 6))) << 5) + 8 * (l & 3), sel = (l & 8) ? 0xeeeeu : 0x4444u;
    u32 so = 4 * t;
    asm volatile("" : "+v"(wa), "+v"(sel), "+v"(so));
    asm volatile(UNTIL(STR(CLD)) :: [st] "s"(st0) : "s8", "s9", "scc");
    __builtin_amdgcn_raw_buffer_store_b32(~0u, rs, so, O_HG + (6 * cb + 3 * mat + part) * 2048, 16);
    st8(rs, u < 8 ? O_PG + (4 * kp + ct) * 4096 + 2 * so : 0x80000000u, u32x2{~0u, ~0u});
#define SWP(a0, a1, a2, a3) "v_permlane32_swap_b32 " a0 ", " a2 "\n\tv_permlane32_swap_b32 " a1 ", " a3 "\n\t"
#define OUT(a0, a1, o) "v_cvt_pk_bf16_f32 " a0 ", " a0 ", " a1 "\n\tv_min_u32 " a0 ", -2, " a0 "\n\tbuffer_store_dword " a0 ", %[so], %[rs], " o " sc1\n\t"
#define TR4(a, b_, c, d, e, f, g_, h, adr, o) "ds_read_b64_tr_b16 v[" #a ":" #b_ "], " adr " offset:" #o "\n\tds_read_b64_tr_b16 v[" #c ":" #d "], " adr " offset:" #o "+512\n\t" \
      "ds_read_b64_tr_b16 v[" #e ":" #f "], " adr " offset:" #o "+8192\n\tds_read_b64_tr_b16 v[" #g_ ":" #h "], " adr " offset:" #o "+8704\n\t"
#define SM(d, a, b_) "v_smfmac_f32_16x16x64_bf16 v[" d "], v[" a "], v[" b_ "], %[sel]\n\t"
#define USE(c, xa, wf) "s_add_u32 s6, s6, 16\n\ts_waitcnt lgkmcnt(" #c ")\n\t" SM("16:19", "20:23", wf) SM("8:11", xa, wf)
#define XR(xa, o) "ds_read_b128 v[" xa "], %[xa] offset:" #o "\n\t"
#define XU(c, xa, wf) "s_waitcnt lgkmcnt(" #c ")\n\t" SM("8:11", xa, wf)
#define ZERO(r) "v_mfma_f32_32x32x16_bf16 v[" r "], v[8:11], v[8:11], 0\n\t"
#define POLL(c, rd) "7:\n\tds_read_b32 v30, %[fl]\n\ts_waitcnt lgkmcnt(0)\n\ts_call_b64 s[10:11], 1f\n\ts_cbranch_scc1 7b\n\t" rd
#define CLR "s_waitcnt vmcnt(0)\n\ts_mov_b64 exec, 1\n\tds_add_rtn_u32 v14, %[fl], v28 offset:96\n\ts_waitcnt lgkmcnt(0)\n\tv_cmpx_eq_u32 vcc, 7, v14\n\t" \
      "buffer_store_dwordx2 v[12:13], off, %[rs], %[co] sc1\n\ts_mov_b64 exec, -1\n\t"
#define XE(k) "s_cmpk_eq_u32 s6, " #k "\n\ts_cbranch_scc1 9f\n\t"
    asm volatile("v_mov_b64 v[8:9], 0\n\tv_mov_b64 v[10:11], 0\n\tv_mov_b64 v[16:17], 0\n\tv_mov_b64 v[18:19], 0\n\t"
                 ZERO("96:111") ZERO("112:127")
                 "v_mov_b32 v20, 0x3f803f80\n\tv_mov_b32 v21, v20\n\tv_mov_b64 v[22:23], v[20:21]\n\tv_mov_b32 v13, %[t]\n\tv_mov_b32 v28, 1\n\tv_mov_b32 v30, 0\n\t"
                 "v_add_u32 v29, 0x10000, %[wa]\n\ts_movk_i32 s6, 96\n\ts_mov_b32 s7, 1\n\ts_mov_b32 s8, 15\n\ts_mov_b32 s9, %[spin]\n\ts_nop 7\n\t"
                 POLL(8, TR4(32, 33, 34, 35, 36, 37, 38, 39, "%[wa]", 0) TR4(40, 41, 42, 43, 44, 45, 46, 47, "%[wa]", 16384))
                 USE(4, "96:99", "32:39") "s_waitcnt lgkmcnt(0)\n\t" SM("16:19", "20:23", "40:47") SM("8:11", "100:103", "40:47")
                 CLR
                 POLL(4, TR4(48, 49, 50, 51, 52, 53, 54, 55, "%[wa]", 32768)) USE(0, "104:107", "48:55")
                 POLL(4, TR4(56, 57, 58, 59, 60, 61, 62, 63, "%[wa]", 49152)) "s_mov_b64 exec, 1\n\tds_add_u32 %[fl], v28 offset:20\n\ts_mov_b64 exec, -1\n\t" TS(60, 61)
                 USE(0, "108:111", "56:63")
                 POLL(4, TR4(64, 65, 66, 67, 68, 69, 70, 71, "v29", 0)) USE(0, "112:115", "64:71") "s_cmp_eq_u32 %[ns], 5\n\ts_cbranch_scc1 5f\n\t"
                 POLL(4, TR4(72, 73, 74, 75, 76, 77, 78, 79, "v29", 16384)) USE(0, "116:119", "72:79") "s_cmp_eq_u32 %[ns], 6\n\ts_cbranch_scc1 5f\n\t"
                 POLL(4, TR4(80, 81, 82, 83, 84, 85, 86, 87, "v29", 32768)) USE(0, "120:123", "80:87")
                 POLL(4, TR4(88, 89, 90, 91, 92, 93, 94, 95, "v29", 49152)) USE(0, "124:127", "88:95")
                 "5:\n\t" TS(62, 63) "s_cmp_eq_u32 s7, 0\n\ts_cbranch_scc1 4f\n\ts_mov_b32 s8, 16\n\t"
                 "s_getpc_b64 s[10:11]\n\tds_read_b32 v30, %[fl]\n\ts_waitcnt lgkmcnt(0)\n"

                 "1:\n\tv_cmp_le_u32 vcc, s6, v30\n\ts_and_b32 s12, vcc_hi, s7\n\ts_cbranch_scc1 3f\n"
                 "2:\n\ts_andn2_b32 s12, s8, vcc_lo\n\ts_cbranch_scc1 6f\n\ts_setpc_b64 s[10:11]\n"
                 "6:\n\ts_sub_u32 s9, s9, 1\n\ts_cbranch_scc1 4f\n\t" GNAP "\n\ts_cmp_eq_u32 s6, s6\n\ts_setpc_b64 s[10:11]\n"
                 "3:\n\t"
                 XR("96:99", 0) XR("100:103", 1024) XR("104:107", 2048) XR("108:111", 3072) XR("112:115", 4096) XR("116:119", 5120) XR("120:123", 6144) XR("124:127", 7168)
                 "ds_read_b128 v[24:27], %[pa]\n\ts_mov_b32 s7, 0\n\t"
                 TS(64, 65)
                 XE(96) XU(8, "96:99", "32:39") XU(7, "100:103", "40:47") XE(112) XU(6, "104:107", "48:55") XE(128) XU(5, "108:111", "56:63") XE(144)
                 XU(4, "112:115", "64:71") XE(160) XU(3, "116:119", "72:79") XE(176) XU(2, "120:123", "80:87") XE(192) XU(1, "124:127", "88:95")
                 "9:\n\ts_cmp_eq_u32 s8, 16\n\ts_waitcnt lgkmcnt(0)\n\ts_cbranch_scc0 2b\n"
                 "4:\n\ts_nop 4\n\tv_permlane32_swap_b32 v16, v17\n\tv_add_f32 v16, v16, v17\n\tv_mul_f32 v25, v25, v16\n\tv_mul_f32 v27, v27, v16\n\t"
                 SWP("v8", "v9", "v10", "v11") "v_fmac_f32 v25, v10, v24\n\tv_fmac_f32 v27, v11, v26\n\tv_fma_f32 v8, v8, v24, v25\n\tv_fma_f32 v9, v9, v26, v27\n\t"
                 OUT("v8", "v9", "%[ho] offen") TS(66, 67)
                 :: [wa] "v"(wa), [sel] "v"(sel), [so] "v"(so), [fl] "v"(lds0 + FC + 4 * min(l, 4)), [xa] "v"(lds0 + FA + 128 * (l & 7) + 16 * ((2 * kg + (n16 >> 3)) ^ (2 * l & 4))),
                    [pa] "v"(lds0 + FC + 32 + 32 * (kg & 1) + 16 * (kg >> 1)), [ns] "s"(ns), [t] "s"(tag), [co] "s"(O_CLR + 8 * id), [rs] "s"(rs), [ho] "s"(O_HG + (6 * cb + 3 * mat + part) * 2048), [spin] "n"(SPIN)
                 : "vcc", "scc", "s6", "s7", "s8", "s9", "s10", "s11", "s12", "s13", "memory", G_CLOBBER TSC);
    TSGET(1, 4, 3, 5);
    if (u < 8) {
      const int tp = 2 * (kg & 1) + (kg >> 1), r7 = 4 * (kg & 1) + (n16 >> 2);
      u32 o3 = (kp & 3) == 1 ? so + 4096 : 0x80000000u, h0 = O_HG + kp * 12288, h1 = h0 + 6144;
      u32 hw = lds0 + FH + 2 * tp * HP + 32 * w + 8 * (l & 2) + (l & 4) * 2 + ((l & 8) >> 1) + 2 * (l & 1);
      u32 hr = lds0 + FH + (l & 7) * HP + 32 * kg + 2 * (l & 8);
      u32 da = lds0 + 512 * (16 * (kg >> 1) + r7) + ((2 * w ^ r7) << 5) + 8 * (l & 3), db = da ^ 32, fl = lds0 + FC + 4 * (l & 3);
      u32 po = 512 * w + 16 * (l & 8) + 32 * tp + 4 * (l & 7);
      u32 e0 = 0x3f80u, e1 = 0x3f800000u;
      asm volatile("" : "+v"(hw), "+v"(hr), "+v"(da), "+v"(db), "+v"(fl), "+v"(po), "+v"(o3));
      asm volatile("" : "+s"(e0), "+s"(e1), "+s"(h0), "+s"(h1));
#define TRD(a, b_, c, d, e, f, g_, h, adr, o) "ds_read_b64_tr_b16 v[" #a ":" #b_ "], " adr " offset:" #o "\n\tds_read_b64_tr_b16 v[" #c ":" #d "], " adr " offset:" #o "+4096\n\t" \
      "ds_read_b64_tr_b16 v[" #e ":" #f "], " adr " offset:" #o "+16384\n\tds_read_b64_tr_b16 v[" #g_ ":" #h "], " adr " offset:" #o "+20480\n\t"
#define HLD(X, r) X(r "0", "%[s1]", "%[h0] offen") X(r "3", "%[s1]", "%[h1] offen") X(r "1", "%[s1]", "%[h0] offen offset:2048") X(r "4", "%[s1]", "%[h1] offen offset:2048") \
      X(r "2", "%[o3]", "%[h0] offen") X(r "5", "%[o3]", "%[h1] offen")
#define HL0(a, o, s_) "buffer_load_dword " a ", " o ", %[rs], " s_ " sc1\n\t"
#define HLF(a, o, s_) "buffer_load_dword " a ", " o ", %[rs], " s_ SCH "\n\t"
#define HL1(a, o, s_) "v_cmpx_eq_u32 vcc, -1, " a "\n\t" HL0(a, o, s_) "s_mov_b64 exec, -1\n\t"
#define DOT(d, e, a) "v_dot2c_f32_bf16 " d ", %[" e "], " a "\n\t"
#define HCK(c, r) "s_cmp_eq_u32 s7, 0\n\ts_cbranch_scc1 5f\n\ts_call_b64 s[10:11], 4f\n5:\n\ts_waitcnt vmcnt(" #c ")\n\tv_max3_u32 v28, " r "0, " r "1, " r "2\n\tv_lshlrev_b32 v24, 16, " r "0\n\tv_max3_u32 v28, v28, " r "3, " r "4\n\t" \
      "v_and_b32 v25, %[hm], " r "0\n\tv_max_u32 v28, v28, " r "5\n\t" DOT("v24", "e0", r "1") "v_cmp_eq_u32 vcc, -1, v28\n\t" DOT("v25", "e1", r "1") DOT("v24", "e0", r "2") DOT("v25", "e1", r "2") \
      "v_lshlrev_b32 v26, 16, " r "3\n\tv_and_b32 v27, %[hm], " r "3\n\t" DOT("v26", "e0", r "4") DOT("v27", "e1", r "4") DOT("v26", "e0", r "5") DOT("v27", "e1", r "5")
#define RB2(a, b_) "v_cvt_pk_bf16_f32 " a ", 0, " a "\n\tv_cvt_pk_bf16_f32 " b_ ", 0, " b_ "\n\t"
      asm volatile("s_mov_b32 s7, 1\n\ts_mov_b32 s12, %[spin]\n\tv_mov_b64 v[8:9], 0\n\tv_mov_b64 v[10:11], 0\n\tv_mov_b64 v[12:13], 0\n\tv_mov_b64 v[14:15], 0\n"
                   "2:\n\ts_sleep 1\n\tds_read_b32 v28, %[fl] offset:16\n\ts_memrealtime s[8:9]\n\ts_waitcnt lgkmcnt(0)\n\tv_cmp_eq_u32 vcc, 0, v28\n\ts_sub_u32 s8, s8, %[st]\n\t"
                   "s_cmp_lt_u32 s8, %[th]\n\ts_cbranch_scc1 2b\n\ts_bitcmp1_b32 vcc_lo, 3\n\ts_cbranch_scc1 2b\n\t" TS(64, 65)
                   HLD(HLF, "v7") HCK(0, "v7") "s_cbranch_vccnz 9f\n"
                   "3:\n\t" TS(66, 67) RB2("v24", "v25") "v_mul_f32 v28, %[le], v24\n\tv_mul_f32 v29, %[le], v25\n\tv_exp_f32 v28, v28\n\tv_exp_f32 v29, v29\n\t" RB2("v26", "v27")
                   "v_add_f32 v28, 1.0, v28\n\tv_add_f32 v29, 1.0, v29\n\tv_rcp_f32 v28, v28\n\tv_rcp_f32 v29, v29\n\ts_cmp_eq_u32 s7, 0\n\tv_mul_f32 v24, v24, v28\n\tv_mul_f32 v25, v25, v29\n\t"
                   RB2("v24", "v25") "v_mul_f32 v24, v24, v26\n\tv_mul_f32 v25, v25, v27\n\tv_cvt_pk_bf16_f32 v24, v24, v25\n\ts_mov_b32 s12, %[spin]\n\t"
                   "ds_write_b16 %[hw], v24\n\tds_write_b16_d16_hi %[hw], v24 offset:%[hp]\n\ts_cbranch_scc1 8f\n"
                   "7:\n\ts_call_b64 s[10:11], 4f\n\ts_mul_i32 s12, s12, s7\n\ts_sub_u32 s12, s12, 1\n\ts_cbranch_scc0 7b\n"
                   "8:\n\ts_waitcnt lgkmcnt(0)\n\ts_barrier\n\t" TS(60, 61)
                   XR("16:19", 0) XR("20:23", 128)
                   XU(1, "16:19", "32:39") SM("12:15", "16:19", "40:47") XU(0, "20:23", "48:55") SM("12:15", "20:23", "56:63") "s_nop 6\n\t"
                   SWP("v8", "v9", "v10", "v11") "v_add_f32 v8, v8, v10\n\tv_add_f32 v9, v9, v11\n\t" SWP("v12", "v13", "v14", "v15") OUT("v8", "v9", "%[po] offen")
                   "v_add_f32 v12, v12, v14\n\tv_add_f32 v13, v13, v15\n\t" OUT("v12", "v13", "%[po] offen offset:256") TS(62, 63) DONE
                   "9:\n\tv_mov_b64 v[80:81], v[70:71]\n\tv_mov_b64 v[82:83], v[72:73]\n\tv_mov_b64 v[84:85], v[74:75]\n\t" HLD(HL1, "v7") "s_sleep " STR(HNAP) "\n"
                   "1:\n\t" HLD(HL1, "v8") HCK(6, "v7") "s_cbranch_vccz 3b\n\t" HLD(HL1, "v7") HCK(6, "v8") "s_sub_u32 s12, s12, 1\n\ts_cbranch_vccz 3b\n\ts_cbranch_scc0 1b\n\ts_branch 3b\n"
                   "4:\n\tds_read_b32 v28, %[fl]\n\ts_waitcnt lgkmcnt(0)\n\tv_cmp_ne_u32 vcc, -1, v28\n\ts_cbranch_vccnz 6f\n\t"
                   TRD(32, 33, 34, 35, 36, 37, 38, 39, "%[da]", 0) TRD(40, 41, 42, 43, 44, 45, 46, 47, "%[db]", 0)
                   TRD(48, 49, 50, 51, 52, 53, 54, 55, "%[da]", 32768) TRD(56, 57, 58, 59, 60, 61, 62, 63, "%[db]", 32768) "s_mov_b32 s7, 0\n"
                   "6:\n\ts_setpc_b64 s[10:11]\n0:"
                   :: [s1] "v"(so), [o3] "v"(o3), [fl] "v"(fl), [da] "v"(da), [db] "v"(db), [hw] "v"(hw), [xa] "v"(hr), [sel] "v"(sel), [so] "v"(po), [rs] "s"(rs), [h0] "s"(h0), [h1] "s"(h1),
                      [st] "s"(st0), [th] "n"(TH), [spin] "n"(SPIN), [hp] "n"(HP), [po] "s"(O_PG + (4 * kp + ct) * 4096), [e0] "s"(e0), [e1] "s"(e1), [hm] "s"(0xffff0000u),
                      [le] "s"(-1.4426950408889634f)
                   : "vcc", "scc", "s7", "s8", "s9", "s10", "s11", "s12", "memory", G_CLOBBER TSC);
      TSGET(8, 9, 6, 7);
    }
    FLUSH();
    __builtin_amdgcn_endpgm();
  }

  if (w0 >= 8) {
    u32x8 xa0, xa1, xa2, xa3, sa1;
    u32x4 junk;
    u32 op, so;
    ROLE(p, "", , "={v[100:107]}"(xa0), "={v[108:115]}"(xa1), "={v[116:123]}"(xa2),
         "={v148}"(op), "={v149}"(so), "={v[160:163]}"(junk));
    const int p = w - 8;
    __builtin_amdgcn_s_setprio(3);
    asm volatile("s_waitcnt lgkmcnt(0)" : "+{s44}"(tag)::"memory");
    tag += 1u;
    STAMP(9);
    f32x2 xq[4][8];
    u32x8 pa;
    {
#define SCK(r1, r3, r5, r7) "v_xor_b32 %[d], %[t], v" #r1 "\n\tv_bitop3_b32 %[d], %[d], %[t], v" #r3 " bitop3:0xf6\n\t" \
      "v_bitop3_b32 %[d], %[d], %[t], v" #r5 " bitop3:0xf6\n\tv_bitop3_b32 %[d], %[d], %[t], v" #r7 " bitop3:0xf6\n\t"
#define SLX(r0, r3, r4, r7, k, k1, sc) "buffer_load_dwordx4 v[" #r0 ":" #r3 "], %[so], %[rs], " k " offen" sc "\n\tbuffer_load_dwordx4 v[" #r4 ":" #r7 "], %[so], %[rs], " k1 " offen" sc "\n\t"
#define SLD(r0, r3, r4, r7, k, k1) SLX(r0, r3, r4, r7, k, k1, " sc1")
#define SLOOP(r0, r1, r3, r4, r5, r7, k, k1) "1:\n\ts_waitcnt vmcnt(0)\n4:\n\t" SCK(r1, r3, r5, r7) "s_sub_u32 %[n], %[n], 1\n\tv_cmp_ne_u32 vcc, 0, %[d]\n\ts_cbranch_scc1 2f\n\ts_cbranch_vccz 2f\n\t" \
      SLD(r0, r3, r4, r7, k, k1) "s_branch 1b\n2:\n\t"
      u32 d_, n_ = SPIN, k1 = 4096, k2 = 8192, k3 = 12288, s0[4];
      asm volatile("s_mov_b32 m0, %[m]\n\ts_cmp_eq_u32 %[k], 3\n\ts_waitcnt vmcnt(2)\n\ts_cbranch_scc0 1f\n\ts_waitcnt vmcnt(0)\n1:"
                   : "+{v[100:107]}"(xa0), "+{v[108:115]}"(xa1), "+{v[160:163]}"(junk) : [m] "s"(lds0 + OX + 2 * XO + 256 * p), [k] "s"(xk) : "scc", "memory");
      SB();
#define UNPACK(i, xs) _Pragma("unroll") for (int c = 0; c < 8; c++) { \
        xq[i][c] = (c & 1) ? f32x2{hi2f(xs[c >> 1]), hi2f(xs[4 + (c >> 1)])} : f32x2{lo2f(xs[c >> 1]), lo2f(xs[4 + (c >> 1)])}; asm volatile("" : "+v"(xq[i][c].x), "+v"(xq[i][c].y)); }
      UNPACK(0, xa0);
      UNPACK(1, xa1);
      u32 fa = lds0 + OXF + 4 * p, fv = 1;
      asm volatile("" : "+v"(fa), "+v"(fv));
      asm volatile("" : "+s"(k1), "+s"(k2), "+s"(k3), "+s"(n_));
      SB();
      STAMP(1);
#define SCP(r0, r1, r2, r3, r4, r5, r6, r7) "s_waitcnt vmcnt(2)\n\t" SCK(r1, r3, r5, r7) "v_cmp_ne_u32 vcc, 0, %[d]\n\tv_mov_b32 %[a0], v" #r0 "\n\tv_mov_b32 %[a1], v" #r2 "\n\t" \
      "v_mov_b32 %[a2], v" #r4 "\n\tv_mov_b32 %[a3], v" #r6 "\n\ts_cbranch_vccz 3f\n\t" SLD(r0, r3, r4, r7, "0", "%[k1]")
      asm volatile(UNTIL(STR(TSG)) SLD(132, 135, 136, 139, "0", "%[k1]") "s_sleep " STR(SSL) "\n\t" SLD(140, 143, 144, 147, "0", "%[k1]") "s_waitcnt vmcnt(4)"
                   : "=&{v[132:139]}"(pa), "=&{v[140:147]}"(sa1), "+{v[116:123]}"(xa2)
                   : [st] "s"(st0), [so] "v"(so), [rs] "s"(rs), [k1] "s"(k1) : "scc", "s8", "s9", "memory");
      UNPACK(2, xa2);
      SB();
      asm volatile("1:\n\t" SCP(132, 133, 134, 135, 136, 137, 138, 139) SCP(140, 141, 142, 143, 144, 145, 146, 147) "s_sub_u32 %[n], %[n], 1\n\ts_cbranch_scc0 1b\n"
                   "3:\n\t"
                   : [d] "=&v"(d_), [n] "+s"(n_), [a0] "=&v"(s0[0]), [a1] "=&v"(s0[1]), [a2] "=&v"(s0[2]), [a3] "=&v"(s0[3]), "+{v[132:139]}"(pa), "+{v[140:147]}"(sa1)
                   : [t] "s"(tag), [so] "v"(so), [rs] "s"(rs), [k1] "s"(k1), [k2] "s"(k2), [k3] "s"(k3) : "vcc", "scc", "memory");
#pragma unroll
      for (int i = 0; i < 5; i++) {
#define XLD(r0, r1, r2, r3, o_) "v_add_u32 %[d], " #o_ ", %[o]\n\tglobal_load_dwordx4 v[" #r0 ":" #r1 "], %[d], %[x]\n\tglobal_load_dwordx4 v[" #r2 ":" #r3 "], %[d], %[x] offset:2048\n"
        if (i == XI) asm volatile("s_cmp_eq_u32 %[k], 3\n\ts_cbranch_scc0 1f\n\t" XLD(116, 119, 120, 123, 0x200000) "1:\n\t" XLD(124, 127, 128, 131, 0x300000)
                                 : [d] "=&v"(d_), "+{v[116:123]}"(xa2), "=&{v[124:131]}"(xa3) : [k] "s"(xk), [o] "v"(op), [x] "s"(x) : "scc", "memory");
        if (i == 2) {
          asm volatile("s_waitcnt vmcnt(2)\n\ts_branch 4f\n" SLOOP(140, 141, 143, 144, 145, 147, "%[k2]", "%[k3]") : [d] "=&v"(d_), [n] "+s"(n_), "+{v[140:147]}"(sa1), "+{v[116:123]}"(xa2), "+{v[132:139]}"(pa)
                       : [t] "s"(tag), [so] "v"(so), [rs] "s"(rs), [k2] "s"(k2), [k3] "s"(k3) : "vcc", "scc", "memory");
          if (xk == 3) { UNPACK(2, xa2); }
        }
        if (i == 3) asm volatile("s_waitcnt vmcnt(%1)" : "+{v[124:131]}"(xa3) : "n"(DPI == 2 ? DPF : 0));
        if (i == DPI && DPF) {
          u32 lx = l;
          asm volatile("" : "+v"(lx));
          const u32 ti = min(16 * xk + 4 * p + (lx >> 4), 125u), tsb = __umul24(ti, 57) >> 10, tu = min(ti - __umul24(18, tsb), 15u);
          const u32 o = (128 * (4 * tsb + (tu >> 2)) + DPO / 2 + 4 * j + ((lx >> 2) & 3)) * (2 * D) + 512 * (tu & 3) + 128 * (lx & 3);
          if (DPI == 4) AWAIT(OXF + 4 * (l & 3), 3, "s_sleep 1");
          if (DPF == 1) asm volatile("global_load_dword v160, %1, %2" : "=&{v[160:163]}"(junk) : "v"(o), "s"(wd) : "memory");
          else asm volatile("global_load_dword v160, %1, %2\n\tglobal_load_dword v161, %1, %3" : "=&{v[160:163]}"(junk) : "v"(o), "s"(wd), "s"(wd + 64 * D) : "memory");
        }
        if (i == 4) break;
        if (i == 3) UNPACK(3, xa3);
        const u32 sa = i < 2 ? s0[2 * i] : sa1[4 * i - 8], sb = i < 2 ? s0[2 * i + 1] : sa1[4 * i - 6];
        if (i == 0) STAMP(3);
        if (i == 2) STAMP(10);
        const f32x2 r2 = {__builtin_amdgcn_rcpf(hi2f(sa)), __builtin_amdgcn_rcpf(hi2f(sb))};
        const f32x2 m2 = {-lo2f(sa) * r2.x, -lo2f(sb) * r2.y};
        f32x2 n2[8];
        u32 o[8];
        SB();
#pragma unroll
        for (int h = 0; h < (i ? 1 : 2); h++) {
          const int c0 = i ? 0 : 4 * h, c1 = i ? 8 : 4 * h + 4;
#pragma unroll
          for (int c = c0; c < c1; c++) n2[c] = f32x2{fmav(xq[i][c].x, r2.x, m2.x), fmav(xq[i][c].y, r2.y, m2.y)};
          SB();
#pragma unroll
          for (int c = c0; c < c1; c++) o[c] = packh(n2[c].x, n2[c].y);
          SB();
#pragma unroll
          for (int c = c0; c < c1; c++) asm volatile("ds_write_addtid_b32 %0 offset:%1" ::"v"(o[c]), "n"(c * XREG + 1024 * i) : "memory");
          asm volatile("ds_write_b32 %1, %0\n\tv_add_u32 %0, 1, %0" : "+v"(fv) : "v"(fa) : "memory");
          if (S2L && i == 0 && h == 0)
            asm volatile(SLD(140, 143, 144, 147, "%[k2]", "%[k3]") : "+{v[140:147]}"(sa1) : [so] "v"(so), [rs] "s"(rs), [k2] "s"(k2), [k3] "s"(k3) : "memory");
          SB();
        }
        if (i == PDROP) __builtin_amdgcn_s_setprio(PPRIO);
        SB();
      }
    }
    STAMP(4);
    const rsrc_t ro = __builtin_amdgcn_make_buffer_rsrc((void*)out, 0, 2 * L * D, 0x00020000);
    u32 ys[8], y[2][8] = {};
    u32x2 ix;
    u32x4 cl;
    u32x8 pb;
    float yc, rsel;
    const u32 ixo = IXO((l & 6) + (n16 >> 3));
    if (p == 0) asm volatile("buffer_load_dwordx2 v[4:5], %1, %2, 0 offen sc1" : "=&{v[4:5]}"(ix) : "v"(ixo), "s"(rs) : "memory");
    const int rc = 4 * (l & 1) + kg, u = 64 * (p & 1) + l;
    u32x4 pick;
    u32 po = O_PG + 128 * g + 16 * (l & 7) + 16384 * (l >> 3), p3 = l < 32 ? po + 3 * 131072 : 0x80000000u, yf = lds0 + OYP + 4 * min(n16, 12) + 64 * (p >> 1);
    u32 ya = lds0 + OY + CHR * rc, oo = 16 * g + 2 * rc, clo = O_CLR + 16 * min(l, 62);
    u32 yp = lds0 + OY + 512 * (p >> 1) + 4 * (u ^ ((u >> 4) & 6)), yq = yp + 4 * CHR, bp = lds0 + OMISC + 16 * p + 4 * kg, bs = 30 - 2 * n16;
#pragma unroll
    for (int q = 0; q < 4; q++) pick[q] = (n16 >> 2) == q ? 0x3f80u << 16 * (l & 1) : 0u;
    asm volatile("" : "+v"(pick), "+v"(ya), "+v"(oo), "+v"(po), "+v"(p3), "+v"(yf), "+v"(clo), "+v"(yp), "+v"(yq), "+v"(bp), "+v"(bs));
#pragma unroll
    for (int c = 0; c < 8; c++) asm volatile("" : "+v"(y[0][c]), "+v"(y[1][c]));
    if (TE) asm volatile(UNTIL(STR(TE)) :: [st] "s"(st0) : "s8", "s9", "scc");
#pragma unroll
    for (int i = 0; i < 4; i++) {
      if (i == IXI && p == 0) {
        u32 n_ = SPIN;
        asm volatile("s_waitcnt vmcnt(%5)\n1:\n\tv_cmp_ne_u32 vcc, %4, v5\n\ts_sub_u32 %1, %1, 1\n\ts_cbranch_vccz 2f\n\ts_cbranch_scc1 2f\n\t"
                     "buffer_load_dwordx2 v[4:5], %2, %3, 0 offen sc1\n\ts_waitcnt vmcnt(0)\n\ts_branch 1b\n2:"
                     : "+{v[4:5]}"(ix), "+s"(n_) : "v"(ixo), "s"(rs), "s"(tag), "n"(2 * IXI + 1) : "vcc", "scc", "memory");
        const u32 prow = ix.x & 2047u, v = prow >> 1;
        if (l < 16) atomicOr((u32*)(smem + OMISC) + (prow >> 5), 1u << (ix.x & 31u));
        FLAG(OYP + 256 + 32 + 64 * (l & 3), 8u);
        oo += prow * (2 * D);
        asm volatile("v_mov_b32 v162, 0\n\tglobal_load_short_d16_hi v162, %1, %2" : "+{v[160:163]}"(junk) : "v"(oo), "s"(x) : "memory");
        yc = *(const float*)(smem + OSC + 4 * rc); rsel = rbt(sigm(hi2f(ix.x)));
        ya += 4 * (v ^ ((v >> 4) & 6)) + 2 * (prow & 1);
        asm volatile("" : "+v"(yc), "+v"(rsel), "+v"(ya));
        STAMP(11);
      }
      if (i == 2 && p == 0) cl = ld16(rs, clo);
      if (i == 3) {
        asm volatile("s_waitcnt vmcnt(2)" ::: "memory");
        FLAG(OYP + 448 + 36 + 4 * p, 8u);
        if (p == 0) {
#pragma unroll 1
          for (int spin = 0; spin < SPIN; spin++) {
            if (__all(cl.y == tag && cl.w == tag)) break;
            cl = ld16(rs, clo);
          }
          STAMP(12);
        }
        __builtin_amdgcn_s_setprio(LPRIO);
      }
      {
        u32 n_ = SPIN, f_;
        asm volatile("1:\n\tds_read_b32 %0, %2 offset:%3\n\ts_sub_u32 %1, %1, 1\n\ts_waitcnt lgkmcnt(0)\n\tv_cmp_eq_u32 vcc, 0, %0\n\ts_cbranch_scc1 2f\n\ts_and_b32 vcc_lo, vcc_lo, %4\n\t"
                     "s_cbranch_scc0 2f\n\ts_sleep %5\n\ts_branch 1b\n2:" : "=&v"(f_), "+s"(n_) : "v"(yf), "n"(128 * i), "n"(i < 2 ? 255 : 511), "n"(i < 3 ? YN0 : YN3) : "vcc", "scc", "memory");
      }
      if (i == 0) STAMP(5); else if (i == 1) STAMP(6); else if (i == 2) STAMP(7); else STAMP(8);
      u32 bm = 0;
      if (i >= 2) asm volatile("ds_read_b32 %0, %1 offset:%2" : "=v"(bm) : "v"(bp), "n"(64 * i) : "memory");
#pragma unroll
      for (int r = 0; r < 2; r++)
#pragma unroll
        for (int c = 0; c < 8; c++) asm volatile("ds_read_u16_d16_hi %0, %1 offset:%2" : "+v"(y[r][c]) : "v"(c < 4 ? yp : yq), "n"((c & 3) * CHR + 1024 * i + 2 * r) : "memory");
      if (i == 3 && p == 0) {
#define PLD(X) X(132, 133, 134, 135, "%[po]", "0") X(136, 137, 138, 139, "%[po]", "%[k1]") X(140, 141, 142, 143, "%[po]", "%[k2]") X(144, 145, 146, 147, "%[p3]", "0")
#define PL0(a, b_, c, d, o, s_) "buffer_load_dwordx4 v[" #a ":" #d "], " o ", %[rs], " s_ " offen sc1\n\t"
        asm volatile(PLD(PL0) : "=&{v[132:139]}"(pa), "=&{v[140:147]}"(pb) : [po] "v"(po), [p3] "v"(p3), [rs] "s"(rs), [k1] "s"(131072), [k2] "s"(262144) : "memory");
      }
      if (i == 0) {
        st8(rs, t == 512 ? O_ARR + b * 8 : 0x80000000u, u32x2{0u, tag});
        const u32x4 a = *(const u32x4*)(smem + OSC), c = *(const u32x4*)(smem + OSC + 16);
#pragma unroll
        for (int k = 0; k < 4; k++) { ys[k] = (u32)__builtin_amdgcn_readfirstlane((int)a[k]); ys[4 + k] = (u32)__builtin_amdgcn_readfirstlane((int)c[k]); }
      }
      SB();
#pragma unroll
      for (int r = 0; r < 2; r++) {
        asm volatile("s_waitcnt lgkmcnt(%9)" : "+v"(y[r][0]), "+v"(y[r][1]), "+v"(y[r][2]), "+v"(y[r][3]), "+v"(y[r][4]), "+v"(y[r][5]), "+v"(y[r][6]), "+v"(y[r][7]), "+v"(bm) : "n"(8 - 8 * r));
        u32x4 v;
#pragma unroll
        for (int k = 0; k < 4; k++)
          v[k] = pack2(fmas(__uint_as_float(y[r][2 * k]), ys[2 * k], xq[i][2 * k][r]), fmas(__uint_as_float(y[r][2 * k + 1]), ys[2 * k + 1], xq[i][2 * k + 1][r]));
        if (i >= 2 && r == 0) bm <<= bs;
        __builtin_amdgcn_raw_buffer_store_b128(v, ro, i < 2 ? op : op | (bm & 0x40000000u << r), (512 * i + r) * (2 * D), OAUX);
        SB();
        asm volatile("s_nop 1");
        SB();
      }
    }
    asm volatile("" ::"{v[160:163]}"(junk));
    STAMP(13);
    if (p == 0) {
      u32 n_ = SPIN;
#define PL1(a, b_, c, d, o, s_) "v_max3_u32 v150, v" #a ", v" #b_ ", v" #c "\n\tv_max_u32 v150, v150, v" #d "\n\tv_cmpx_eq_u32 vcc, -1, v150\n\t" PL0(a, b_, c, d, o, s_) "s_mov_b64 exec, -1\n\t"
#define PLE(X) X(112, 113, 114, 115, "%[po]", "0") X(116, 117, 118, 119, "%[po]", "%[k1]") X(120, 121, 122, 123, "%[po]", "%[k2]") X(124, 125, 126, 127, "%[p3]", "0")
#define PMX(a, b_) "v_max3_u32 v150, v150, v" a ", v" b_ "\n\t"
#define PMM(d, a, c) "v_mfma_f32_16x16x32_bf16 v[" d "], %[pk], v[" a "], " c "\n\t"
#define PCK(c, p, q) "s_waitcnt vmcnt(" #c ")\n\t" PMM("152:155", p "2:" p "5", "0") "v_max_u32 v150, v" p "2, v" p "3\n\t" PMX(p "4", p "5") PMM("156:159", p "6:" p "9", "0") PMX(p "6", p "7") PMX(p "8", p "9") \
      PMM("152:155", q "0:" q "3", "v[152:155]") PMX(q "0", q "1") PMX(q "2", q "3") PMM("156:159", q "4:" q "7", "v[156:159]") PMX(q "4", q "5") PMX(q "6", q "7") \
      "v_cmp_eq_u32 vcc, -1, v150\n\ts_sub_u32 %[n], %[n], 1\n\t"
#define PCP(a, b_, c, d) "v_mov_b64 v[" #a ":" #b_ "], v[" #c ":" #d "]\n\t"
      asm volatile(PCK(2, "13", "14") "s_cbranch_vccnz 9f\n"
                   "2:\n\t" TS(60, 61) "ds_read_b32 v150, %[yf] offset:448\n\tds_read_u16_d16_hi v151, %[ya]\n\ts_mov_b32 %[n], %[spin]\n\ts_nop 1\n\tv_add_f32 v152, v152, v156\n\tv_add_f32 v153, v153, v157\n\ts_nop 0\n\t"
                   "v_add_f32_dpp v152, v152, v152 row_ror:8 row_mask:0xf bank_mask:0x3\n\tv_add_f32_dpp v152, v153, v153 row_ror:8 row_mask:0xf bank_mask:0xc\n\t"
                   "v_cvt_pk_bf16_f32 v152, 0, v152\n\tv_mul_f32 v152, v152, %[rl]\n\tv_cvt_pk_bf16_f32 v152, 0, v152\n\tv_add_f32 v153, v162, v152\n"
                   "3:\n\ts_waitcnt lgkmcnt(0)\n\tv_cmp_eq_u32 vcc, 0, v150\n\tv_and_b32 v152, 0xffff0000, v151\n\ts_sub_u32 %[n], %[n], 1\n\ts_cbranch_vccz 4f\n\ts_cbranch_scc1 4f\n\t"
                   "ds_read_b32 v150, %[yf] offset:448\n\tds_read_u16_d16_hi v151, %[ya]\n\ts_branch 3b\n"
                   "4:\n\tv_fmac_f32 v153, v152, %[yc]\n\tv_cvt_pk_bf16_f32 v153, v153, v153\n\tbuffer_store_short v153, %[oo], %[ro], 0 offen\n\t" DONE
                   "9:\n\t" PCP(112, 113, 132, 133) PCP(114, 115, 134, 135) PCP(116, 117, 136, 137) PCP(118, 119, 138, 139) PCP(120, 121, 140, 141) PCP(122, 123, 142, 143)
                   PCP(124, 125, 144, 145) PCP(126, 127, 146, 147) PLD(PL1) "s_sleep " STR(PNAP) "\n1:\n\t" PLE(PL1) PCK(4, "13", "14") "s_cbranch_vccz 2b\n\t" PLD(PL1) PCK(4, "11", "12")
                   "s_cbranch_vccz 2b\n\ts_cbranch_scc0 1b\n\ts_branch 2b\n0:"
                   : [n] "+s"(n_), "+{v[132:139]}"(pa), "+{v[140:147]}"(pb)
                   : [po] "v"(po), [p3] "v"(p3), [rs] "s"(rs), [k1] "s"(131072), [k2] "s"(262144), [pk] "v"(pick), [yf] "v"(yf), [ya] "v"(ya), "{v[160:163]}"(junk), [rl] "v"(rsel), [yc] "v"(yc),
                     [oo] "v"(oo), [ro] "s"(ro), [spin] "n"(SPIN)
                   : "vcc", "scc", "memory", "v112", "v113", "v114", "v115", "v116", "v117", "v118", "v119", "v120", "v121", "v122", "v123", "v124", "v125", "v126", "v127", "v150", "v151", "v152", "v153", "v154", "v155", "v156", "v157", "v158", "v159" TSD);
      TSGET(14, 2, 2, 2);
      STAMP(15);
    }
    FLUSH();
    __builtin_amdgcn_endpgm();
  }

  u32 pl, pa, pr, pi, ldtv;
  u32x4 tv, fx, junk;
  ROLE(c, "", , "={v[16:19]}"(tv), "={v[20:23]}"(fx), "={v24}"(ldtv), "={v96}"(pl), "={v97}"(pa), "={v68}"(pr), "={v69}"(pi), "={v[160:163]}"(junk));
  char* W = smem + w * CHR;
  __builtin_amdgcn_s_setprio(2);
  STAMP(3);
  const u32x4 zero4 = {0u, 0u, 0u, 0u};
#pragma unroll
  for (int i = 0; i < 4; i++) *(u32x4*)(W + (i < 3 ? TB + 1024 * i : CHR - 1024) + 16 * l) = zero4;
  SB();
  const u32 wb0 = lds0 + w * CHR, xb0 = lds0 + OX + w * XREG;
  asm volatile("s_mov_b32 m0, %0\n\ts_nop 0\n\tds_write_addtid_b32 %1\n\tds_write_addtid_b32 %1 offset:%2" ::"s"(wb0), "v"(0x3c00u), "n"(PB) : "memory");
  u32 ape = wb0 + (n16 == 12 ? 12 : (n16 ^ 3) - 7 * (n16 >> 3)) * RS + 16 * kg, apo = wb0 + (0x765412b0fdec2301ull >> 4 * n16 & 15) * RS + 16 * kg;
  u32x2 kb = {wb0 + ((15 - n16) & 3) * CPE + 2 * ((15 - n16) & 12) + 16 * kg, wb0 + (((n16 >> 2) & 1) + (n16 & 8) + 2 * (3 - (n16 & 3))) * RS + 16 * kg};
  u32x8 ka = {xb0 + 32 * n16 + 16 * kg, wb0 + OY + 32 * n16 + 8 * (kg ^ ((l >> 2) & 3)),
              lds0 + OXF + 4 * (l & 3), wb0 + TB + ((15 - n16) & 3) * CPO + 2 * ((15 - n16) & 12) + 8 * kg, lds0 + OB + 16 * l,
              l < 32 ? 0x01000100u : 0x03020100u, l < 32 ? 0x03020100u : 0x03020302u, (u32)(l >> 2) & 8u};
  u32 fad = lds0 + OYP + 4 * w, za = wb0 + 16 * kg;
  asm volatile("" : "+{v70}"(ape), "+{v71}"(apo), "+{v[28:35]}"(ka), "+{v[26:27]}"(kb), "+{v25}"(fad), "+{v12}"(za));
  STAMP(4);
  asm volatile("s_waitcnt vmcnt(4)" : "+{v24}"(ldtv), "+{v[160:163]}"(junk));
  float dt = rbt(fexp(hi2f(ldtv)));
  asm volatile("" : "+v"(dt));
  const f32x2 cdt = {rdl(dt * -1.4426950408889634f, 0), rdl(dt * 0.15915494309189535f, 0)};
  const u32 dk = (u32)__builtin_amdgcn_readfirstlane((int)junk.x) << 16, db = max(__float_as_uint(rdl(dt, 0)), 27u << 23);
  u32 em = max(dk >> 23 & 255u, 127u) + 119 - (db >> 23);
  asm("s_max_i32 %0, %0, 8\n\ts_min_u32 %0, %0, 0xfe" : "+s"(em) :: "scc");
  SB();
  STAMP(10);
  STAMPC(11);
  {
    u32 e, o0, o1, of, os;
    asm volatile(GEN_ASM
                 : [e] "=&s"(e), "=&{v93}"(o0), "=&{v46}"(o1), "=&{v9}"(of), "=&{v8}"(os), "+{v25}"(fad), "+{v12}"(za), "+{v68}"(pr), "+{v69}"(pi), "+{v70}"(ape),
                   "+{v71}"(apo), "+{v96}"(pl), "+{v97}"(pa), "+{v[28:35]}"(ka), "+{v[26:27]}"(kb), "+{v[16:19]}"(tv), "+{v[20:23]}"(fx)
                 : [w] "s"(wb0), [c] "s"(cdt), [k] "s"(f32x2{1.4426950408889634f, 128.f}), [em] "s"(em)
                 : "memory", "scc", "s6", "s7", GEN_CLOBBER);
    STAMP(1);
    STAMPC(12);
    const float ys = __uint_as_float((e - 5) << 23) * __uint_as_float(db);
    asm volatile("v_rcp_f32 %1, %2\n\ts_nop 0\n\tv_fma_f32 %1, %3, %1, %1\n\tv_fma_mixhi_f16 %0, %1, 1.0, %0 op_sel:[0,0,1] op_sel_hi:[0,0,1]\n\ts_nop 0"
                 : "+v"(o0), "=&v"(dt) : "v"(ys), "s"(dk));
    const u32 k0 = (u32)__builtin_amdgcn_readlane((int)o0, 3), k256 = (u32)__builtin_amdgcn_readlane((int)o1, 60);
    const u32 k512 = (u32)__builtin_amdgcn_readlane((int)os, 28), k1024 = (u32)__builtin_amdgcn_readlane((int)of, 28) >> 16;
    *(LDS u16*)(size_t)(wb0 + tv.z) = (u16)__builtin_amdgcn_perm((k512 & 0xffffu) | (k1024 << 16), (k0 >> 16) | (k256 << 16), tv.w);
    *(float*)(smem + OSC + 4 * w) = ys;
    fx &= (k1024 | (k1024 << 16)) ^ 0x80008000u;
  }
  asm volatile(CONV_ASM : "+{v[28:35]}"(ka) : "{v[26:27]}"(kb), "{v25}"(fad), "{v[48:51]}"(fx), [w] "s"(w), [n0] "s"(1 + (w >> 2)) : "vcc", "scc", "s6", "memory", CONV_CLOBBER TSC);
  TSGET(2, 5, 6, 7);
  STAMP(13);
  STAMPC(15);
  FLUSH();
  __builtin_amdgcn_endpgm();
}

extern "C" void kernel_launch(void* const* d_in, const int* in_sizes, int n_in, void* d_out, int out_size,
                              void* d_ws, size_t ws_size, hipStream_t stream) {
  const u16* const* in = (const u16* const*)d_in;
  fused<<<256, 768, 0, stream>>>(in[0], in[1], in[2], in[3], in[4], in[5], in[6], in[8], in[9], in[10], in[11],
                                 (u16*)d_out, (char*)d_ws);
}
```

```cpp
#include <hip/hip_runtime.h>
#include <stdint.h>

#define STR2(x) #x
#define STR(x) STR2(x)
#define PH 1246
#define PPRIO 0
#define GDELAY 0
#define PRE 2
#define OAUX 0
#define PNT ""
#define LNNT ""
#define PRENT ""
#define PFN 15
#define PFO 512
#define QD 11
#define HNAP 4
#define QE 11
#define RSL 5
#define DPF 1
#define YN0 2
#define YN3 0
#define QS 7
#define QAS 6
#define RAMP 4
#define GNAP "s_sleep 1"
#define RNAP "s_sleep 1"
#define CLP 0
#define CSL 8
#define XSP 0
#define SPEC 0
#define HC 4
#define S2L 1
#define HOLD 8
#define DPRIO 3
#define DPO 128
#define TH 590
#define CLD 245
#define IXI 1
#define LPRIO 0
#define DPI 2
#define PDROP 2
#define TE 0
#define XI 1
#define SCT " sc1"
#define SCU " sc1"
#define SCH " sc1"
#define TSG 280
#define SSL 5
#define PNAP 4
#define TW 310
#define TL 4352
typedef unsigned short u16;
typedef unsigned int u32;
typedef __attribute__((ext_vector_type(8))) __bf16 bf16x8;
typedef __attribute__((ext_vector_type(2))) __bf16 bf16x2;
typedef __attribute__((ext_vector_type(8))) _Float16 f16x8;
typedef __attribute__((ext_vector_type(2))) _Float16 f16x2;
typedef __attribute__((ext_vector_type(4))) short v4i16;
typedef __attribute__((ext_vector_type(4))) float f32x4;
typedef __attribute__((ext_vector_type(2))) float f32x2;
typedef __attribute__((ext_vector_type(4))) u32 u32x4;
typedef __attribute__((ext_vector_type(2))) u32 u32x2;
typedef __attribute__((ext_vector_type(8))) u32 u32x8;
typedef __attribute__((ext_vector_type(2), aligned(4))) u32 u32x2_a4;
typedef __amdgpu_buffer_rsrc_t rsrc_t;
#define LDS __attribute__((address_space(3)))

#define TLARG
#define TLVAL
#define STAMP(I)
#define STAMPC(I)
#define FLUSH()

constexpr int L = 2048, D = 1024, NM = 128, F = 3584;
constexpr u32 O_ARR = 0x1000, O_CLR = 0x2000, O_RG = 0x4000, O_SG = 0x8000, O_IDX = 0x10000, O_HG = 0x20000,
              O_PG = 0xd0000, O_TOP = 0x140000, TOPS = 8 * TL, WS_END = O_TOP + 2 * TOPS;
constexpr int SPIN = 1 << 16;

__device__ __forceinline__ float lo2f(u32 w) { return __uint_as_float(w << 16); }
__device__ __forceinline__ float hi2f(u32 w) { return __uint_as_float(w & 0xffff0000u); }
__device__ __forceinline__ u32 pack2(float a, float b) {
  bf16x2 r = {(__bf16)a, (__bf16)b};
  return __builtin_bit_cast(u32, r);
}
__device__ __forceinline__ u32 packh(float a, float b) {
  f16x2 r = {(_Float16)a, (_Float16)b};
  return __builtin_bit_cast(u32, r);
}
__device__ __forceinline__ float rbt(float f) { return __uint_as_float(pack2(0.f, f)); }
__device__ __forceinline__ float rb(float f) {
  float r;
  asm("v_cvt_pk_bf16_f32 %0, 0, %1" : "=v"(r) : "v"(f));
  return r;
}

template <int CTRL, int RM>
__device__ __forceinline__ float dppf(float v) {
  return __builtin_bit_cast(float, __builtin_amdgcn_update_dpp(0, __builtin_bit_cast(int, v), CTRL, RM, 0xf, true));
}
__device__ __forceinline__ float rdl(float v, int i) {
  return __builtin_bit_cast(float, __builtin_amdgcn_readlane(__builtin_bit_cast(int, v), i));
}
__device__ __forceinline__ void swap32(float& a, float& b) {
  auto r = __builtin_amdgcn_permlane32_swap(__float_as_uint(a), __float_as_uint(b), false, false);
  a = __uint_as_float(r[0]); b = __uint_as_float(r[1]);
}

__device__ __forceinline__ u32x4 ld16(rsrc_t r, u32 off) { return __builtin_amdgcn_raw_buffer_load_b128(r, off, 0, 16); }
__device__ __forceinline__ void st8(rsrc_t r, u32 off, u32x2 v) { __builtin_amdgcn_raw_buffer_store_b64(v, r, off, 0, 16); }
#define SB() __builtin_amdgcn_sched_barrier(0)

__device__ __forceinline__ float fexp(float v) { return __builtin_amdgcn_exp2f(v * 1.4426950408889634f); }
__device__ __forceinline__ float dot2b(u32 a, u32 b, float c) {
  return __builtin_amdgcn_fdot2_f32_bf16(__builtin_bit_cast(bf16x2, a), __builtin_bit_cast(bf16x2, b), c, false);
}
__device__ __forceinline__ float sigm(float v) { return __builtin_amdgcn_rcpf(1.f + __builtin_amdgcn_exp2f(-1.4426950408889634f * v)); }

constexpr int RS = 288, PB = 16 * RS;
constexpr int CPE = 1216, TB = 2 * PB, CPO = 832;
constexpr int T2B = 4, T3B = 224, C2 = 52, C3 = 84;
constexpr int OY = 0, CHR = TB + 4 * CPO;
constexpr int OX = 8 * CHR, XREG = 4640, XO = 266;
constexpr int OMISC = OX + 8 * XREG;
constexpr int OXF = OMISC + 320, OSC = OMISC + 352, OYP = OMISC + 448;
constexpr int OB = OMISC + 1024, SMEM = 148480;
constexpr int FA = 131072, FH = FA + 8192, HP = 576, FC = FH + 8 * HP;
static_assert(FC + 1024 <= SMEM && OB + 4096 <= SMEM, "lds map");

struct Tab { u32 v[6][64][4]; };
constexpr Tab mktab() {
  Tab T = {};
  for (int l = 0; l < 64; l++) {
    const int n16 = l & 15, kg = l >> 4, pc = ((n16 >> 2) & 1) + (n16 & 8) + 2 * (3 - (n16 & 3)), odd = kg >> 1, s = l & 3;
    const int e0 = 534 - 258 - 128 - 8 * pc - 4 * (kg & 1) - odd;
    const int ez = ((l >> 3) & 3) * CPE + ((l & 4) ? 1056 - 64 : 0) + 16 * (l & 7);
    const int b2 = T2B + 44 + C2 * ((kg & 1) + 2 * (pc & 1)), b3 = T3B + 60 + C3 * (pc & 1) - 32 * (kg & 1);
    const int to = TB + 2 * ((odd ? b3 : b2) + 3 - (pc >> 1) - 8 - 16);
    const int q = (l >> 2) & 7, tap = q == 0 ? ((s & 1) ? 0 : 256) : q == 1 ? 257 : q == 7 ? 0 : 509 + q;
    const int sp = q != 6 ? s * (CPE - 2) + 2 * (537 - tap) : TB + s * (CPO - 2) + 2 * (T2B + 15);
    T.v[0][l][0] = (2 * e0 + (2 - odd) * (CPE - 2)) | (2 * e0 + 3 * odd * (CPE - 2)) << 16;
    T.v[0][l][1] = ez | to << 16;
    T.v[0][l][2] = sp;
    T.v[0][l][3] = !tap ? 0x0100u : q == 6 ? 0x0706u : q >= 2 ? 0x0504u : 0x0302u;
    for (int k = 0; k < 8; k++) {
      const int d = 8 * kg + k - n16;
      if (d == 13) T.v[1][l][k >> 1] |= 0xffffu << 16 * (k & 1);
      for (int h = 1; h <= 4; h++)
        if (d >= (h == 1 ? 14 : 10) - h && d <= (h == 1 ? 14 : 10) + h) T.v[1 + h][l][k >> 1] |= 0x3c00u << 16 * (k & 1);
    }
  }
  return T;
}
extern "C" __device__ const __attribute__((aligned(128), used, visibility("hidden"))) Tab lane_tab = mktab();

#define GEN_ASM \
  "s_waitcnt vmcnt(2)\n\tv_lshlrev_b32 v56, 16, v96\n\t" \
  "v_and_b32 v57, 0xffff0000, v96\n\tv_pk_mul_f32 v[56:57], %[k], v[56:57] op_sel_hi:[0,1]\n\t" \
  "v_lshlrev_b32 v49, 16, v97\n\tv_exp_f32 v56, v56\n\t" \
  "v_exp_f32 v57, v57\n\tv_and_b32 v51, 0xffff0000, v97\n\t" \
  "v_cvt_pk_bf16_f32 v56, v56, v57\n\tv_lshlrev_b32 v48, 16, v56\n\t" \
  "v_and_b32 v50, 0xffff0000, v56\n\tv_pk_mul_f32 v[48:49], %[c], v[48:49]\n\t" \
  "v_pk_mul_f32 v[50:51], %[c], v[50:51]\n\tv_pk_mul_f32 v[98:99], %[k], v[48:49] op_sel:[1,0]\n\t" \
  "v_pk_mul_f32 v[100:101], %[k], v[50:51] op_sel:[1,0]\n\tv_fract_f32 v49, v49\n\t" \
  "v_fract_f32 v51, v51\n\tv_exp_f32 v48, v48\n\t" \
  "v_exp_f32 v50, v50\n\tv_cos_f32 v40, v49\n\t" \
  "v_sin_f32 v41, v49\n\tv_cos_f32 v42, v51\n\t" \
  "v_sin_f32 v43, v51\n\tv_pk_mul_f32 v[40:41], v[40:41], v[48:49] op_sel_hi:[1,0]\n\t" \
  "v_pk_mul_f32 v[42:43], v[42:43], v[50:51] op_sel_hi:[1,0]\n\tv_pk_mul_f32 v[48:49], v[40:41], v[40:41] op_sel:[1,1] op_sel_hi:[1,0] neg_lo:[1,0]\n\t" \
  "v_pk_mul_f32 v[50:51], v[42:43], v[42:43] op_sel:[1,1] op_sel_hi:[1,0] neg_lo:[1,0]\n\tv_pk_fma_f32 v[48:49], v[40:41], v[40:41], v[48:49] op_sel:[0,0,0] op_sel_hi:[0,1,1]\n\t" \
  "v_pk_fma_f32 v[50:51], v[42:43], v[42:43], v[50:51] op_sel:[0,0,0] op_sel_hi:[0,1,1]\n\tv_cvt_pk_f16_f32 v72, v40, -v41\n\t" \
  "v_cvt_pk_f16_f32 v73, v42, -v43\n\tv_pk_mul_f32 v[52:53], v[48:49], v[40:41] op_sel:[1,1] op_sel_hi:[1,0] neg_lo:[1,0]\n\t" \
  "v_pk_mul_f32 v[54:55], v[50:51], v[42:43] op_sel:[1,1] op_sel_hi:[1,0] neg_lo:[1,0]\n\tv_pk_fma_f32 v[52:53], v[48:49], v[40:41], v[52:53] op_sel:[0,0,0] op_sel_hi:[0,1,1]\n\t" \
  "v_pk_fma_f32 v[54:55], v[50:51], v[42:43], v[54:55] op_sel:[0,0,0] op_sel_hi:[0,1,1]\n\tv_pk_mul_f32 v[56:57], v[48:49], v[48:49] op_sel:[1,1] op_sel_hi:[1,0] neg_lo:[1,0]\n\t" \
  "v_pk_mul_f32 v[58:59], v[50:51], v[50:51] op_sel:[1,1] op_sel_hi:[1,0] neg_lo:[1,0]\n\tv_pk_fma_f32 v[56:57], v[48:49], v[48:49], v[56:57] op_sel:[0,0,0] op_sel_hi:[0,1,1]\n\t" \
  "v_pk_fma_f32 v[58:59], v[50:51], v[50:51], v[58:59] op_sel:[0,0,0] op_sel_hi:[0,1,1]\n\tv_cvt_pk_f16_f32 v74, v48, -v49\n\t" \
  "v_cvt_pk_f16_f32 v75, v50, -v51\n\tv_cvt_pk_f16_f32 v76, v52, -v53\n\t" \
  "v_cvt_pk_f16_f32 v77, v54, -v55\n\tv_pk_mul_f32 v[52:53], v[56:57], v[48:49] op_sel:[1,1] op_sel_hi:[1,0] neg_lo:[1,0]\n\t" \
  "v_pk_mul_f32 v[54:55], v[58:59], v[50:51] op_sel:[1,1] op_sel_hi:[1,0] neg_lo:[1,0]\n\tv_pk_fma_f32 v[52:53], v[56:57], v[48:49], v[52:53] op_sel:[0,0,0] op_sel_hi:[0,1,1]\n\t" \
  "v_pk_fma_f32 v[54:55], v[58:59], v[50:51], v[54:55] op_sel:[0,0,0] op_sel_hi:[0,1,1]\n\tv_pk_mul_f32 v[60:61], v[56:57], v[56:57] op_sel:[1,1] op_sel_hi:[1,0] neg_lo:[1,0]\n\t" \
  "v_pk_mul_f32 v[62:63], v[58:59], v[58:59] op_sel:[1,1] op_sel_hi:[1,0] neg_lo:[1,0]\n\tv_pk_fma_f32 v[60:61], v[56:57], v[56:57], v[60:61] op_sel:[0,0,0] op_sel_hi:[0,1,1]\n\t" \
  "v_pk_fma_f32 v[62:63], v[58:59], v[58:59], v[62:63] op_sel:[0,0,0] op_sel_hi:[0,1,1]\n\tv_cvt_pk_f16_f32 v78, v56, -v57\n\t" \
  "v_cvt_pk_f16_f32 v79, v58, -v59\n\tv_cvt_pk_f16_f32 v82, v52, -v53\n\t" \
  "v_cvt_pk_f16_f32 v83, v54, -v55\n\tv_pk_mul_f16 v80, v78, v72 op_sel:[1,1] op_sel_hi:[1,0] neg_lo:[1,0]\n\t" \
  "v_pk_mul_f16 v81, v79, v73 op_sel:[1,1] op_sel_hi:[1,0] neg_lo:[1,0]\n\tv_pk_fma_f16 v80, v78, v72, v80 op_sel:[0,0,0] op_sel_hi:[0,1,1]\n\t" \
  "v_pk_fma_f16 v81, v79, v73, v81 op_sel:[0,0,0] op_sel_hi:[0,1,1]\n\tv_pk_mul_f16 v84, v78, v76 op_sel:[1,1] op_sel_hi:[1,0] neg_lo:[1,0]\n\t" \
  "v_pk_mul_f16 v85, v79, v77 op_sel:[1,1] op_sel_hi:[1,0] neg_lo:[1,0]\n\tv_pk_fma_f16 v84, v78, v76, v84 op_sel:[0,0,0] op_sel_hi:[0,1,1]\n\t" \
  "v_pk_fma_f16 v85, v79, v77, v85 op_sel:[0,0,0] op_sel_hi:[0,1,1]\n\tv_cvt_pk_f16_f32 v86, v60, -v61\n\t" \
  "v_cvt_pk_f16_f32 v87, v62, -v63\n\tds_write_addtid_b32 v72 offset:288\n\t" \
  "ds_write_addtid_b32 v73 offset:4896\n\tds_write_addtid_b32 v74 offset:576\n\t" \
  "ds_write_addtid_b32 v75 offset:5184\n\tds_write_addtid_b32 v76 offset:864\n\t" \
  "ds_write_addtid_b32 v77 offset:5472\n\tds_write_addtid_b32 v78 offset:1152\n\t" \
  "ds_write_addtid_b32 v79 offset:5760\n\tds_write_addtid_b32 v80 offset:1440\n\t" \
  "ds_write_addtid_b32 v81 offset:6048\n\tds_write_addtid_b32 v82 offset:1728\n\t" \
  "ds_write_addtid_b32 v83 offset:6336\n\tds_write_addtid_b32 v84 offset:2016\n\t" \
  "ds_write_addtid_b32 v85 offset:6624\n\tds_write_addtid_b32 v86 offset:3456\n\t" \
  "ds_write_addtid_b32 v87 offset:8064\n\tds_read_b128 v[104:107], v70 offset:0\n\t" \
  "ds_read_b128 v[108:111], v70 offset:64\n\tds_read_b128 v[112:115], v70 offset:128\n\t" \
  "ds_read_b128 v[116:119], v70 offset:192\n\tds_read_b128 v[120:123], v70 offset:4608\n\t" \
  "ds_read_b128 v[124:127], v70 offset:4672\n\tds_read_b128 v[128:131], v70 offset:4736\n\t" \
  "ds_read_b128 v[132:135], v70 offset:4800\n\tv_fract_f32 v99, v99\n\t" \
  "v_fract_f32 v101, v101\n\tv_exp_f32 v98, v98\n\t" \
  "v_exp_f32 v100, v100\n\tv_cos_f32 v40, v99\n\t" \
  "v_sin_f32 v41, v99\n\tv_cos_f32 v42, v101\n\t" \
  "v_sin_f32 v43, v101\n\tv_pk_mul_f32 v[40:41], v[40:41], v[98:99] op_sel_hi:[1,0]\n\t" \
  "v_pk_mul_f32 v[42:43], v[42:43], v[100:101] op_sel_hi:[1,0]\n\tv_pk_mul_f32 v[44:45], v[40:41], v[40:41] op_sel:[1,1] op_sel_hi:[1,0] neg_lo:[1,0]\n\t" \
  "v_pk_mul_f32 v[46:47], v[42:43], v[42:43] op_sel:[1,1] op_sel_hi:[1,0] neg_lo:[1,0]\n\tv_pk_fma_f32 v[44:45], v[40:41], v[40:41], v[44:45] op_sel:[0,0,0] op_sel_hi:[0,1,1]\n\t" \
  "v_pk_fma_f32 v[46:47], v[42:43], v[42:43], v[46:47] op_sel:[0,0,0] op_sel_hi:[0,1,1]\n\tv_cvt_pk_f16_f32 v36, v40, v41\n\t" \
  "v_cvt_pk_f16_f32 v37, v42, v43\n\ts_waitcnt vmcnt(0)\n\t" \
  "v_pk_mul_f32 v[48:49], v[44:45], v[44:45] op_sel:[1,1] op_sel_hi:[1,0] neg_lo:[1,0]\n\tv_pk_mul_f32 v[50:51], v[46:47], v[46:47] op_sel:[1,1] op_sel_hi:[1,0] neg_lo:[1,0]\n\t" \
  "v_pk_fma_f32 v[48:49], v[44:45], v[44:45], v[48:49] op_sel:[0,0,0] op_sel_hi:[0,1,1]\n\tv_pk_fma_f32 v[50:51], v[46:47], v[46:47], v[50:51] op_sel:[0,0,0] op_sel_hi:[0,1,1]\n\t" \
  "v_lshlrev_b32 v4, 16, v68\n\tv_cvt_pk_f16_f32 v88, v44, -v45\n\t" \
  "v_cvt_pk_f16_f32 v89, v46, -v47\n\tv_pk_mul_f32 v[52:53], v[48:49], v[44:45] op_sel:[1,1] op_sel_hi:[1,0] neg_lo:[1,0]\n\t" \
  "v_pk_mul_f32 v[54:55], v[50:51], v[46:47] op_sel:[1,1] op_sel_hi:[1,0] neg_lo:[1,0]\n\tv_lshlrev_b32 v5, 16, v69\n\t" \
  "v_pk_fma_f32 v[52:53], v[48:49], v[44:45], v[52:53] op_sel:[0,0,0] op_sel_hi:[0,1,1]\n\tv_pk_fma_f32 v[54:55], v[50:51], v[46:47], v[54:55] op_sel:[0,0,0] op_sel_hi:[0,1,1]\n\t" \
  "v_pk_mul_f32 v[56:57], v[48:49], v[48:49] op_sel:[1,1] op_sel_hi:[1,0] neg_lo:[1,0]\n\tv_pk_mul_f32 v[58:59], v[50:51], v[50:51] op_sel:[1,1] op_sel_hi:[1,0] neg_lo:[1,0]\n\t" \
  "v_and_b32 v6, 0xffff0000, v68\n\tv_pk_fma_f32 v[56:57], v[48:49], v[48:49], v[56:57] op_sel:[0,0,0] op_sel_hi:[0,1,1]\n\t" \
  "v_pk_fma_f32 v[58:59], v[50:51], v[50:51], v[58:59] op_sel:[0,0,0] op_sel_hi:[0,1,1]\n\tv_cvt_pk_f16_f32 v90, v48, -v49\n\t" \
  "v_cvt_pk_f16_f32 v91, v50, -v51\n\tv_and_b32 v7, 0xffff0000, v69\n\t" \
  "v_pk_mul_f32 v[64:65], v[56:57], v[60:61] op_sel:[1,1] op_sel_hi:[1,0] neg_lo:[1,0]\n\tv_pk_mul_f32 v[66:67], v[58:59], v[62:63] op_sel:[1,1] op_sel_hi:[1,0] neg_lo:[1,0]\n\t" \
  "v_pk_fma_f32 v[64:65], v[56:57], v[60:61], v[64:65] op_sel:[0,0,0] op_sel_hi:[0,1,1]\n\tv_pk_fma_f32 v[66:67], v[58:59], v[62:63], v[66:67] op_sel:[0,0,0] op_sel_hi:[0,1,1]\n\t" \
  "v_max3_f32 v102, |v4|, |v5|, |v6|\n\tv_cvt_pk_f16_f32 v92, v52, -v53\n\t" \
  "v_cvt_pk_f16_f32 v93, v54, -v55\n\tv_pk_mul_f16 v136, v88, v74 op_sel:[1,1] op_sel_hi:[1,0] neg_lo:[1,0]\n\t" \
  "v_pk_mul_f16 v137, v89, v75 op_sel:[1,1] op_sel_hi:[1,0] neg_lo:[1,0]\n\tv_max_f32 v102, v102, |v7|\n\t" \
  "v_pk_fma_f16 v136, v88, v74, v136 op_sel:[0,0,0] op_sel_hi:[0,1,1]\n\tv_pk_fma_f16 v137, v89, v75, v137 op_sel:[0,0,0] op_sel_hi:[0,1,1]\n\t" \
  "v_pk_mul_f16 v138, v88, v78 op_sel:[1,1] op_sel_hi:[1,0] neg_lo:[1,0]\n\tv_pk_mul_f16 v139, v89, v79 op_sel:[1,1] op_sel_hi:[1,0] neg_lo:[1,0]\n\t" \
  "v_max_u32_dpp v102, v102, v102 row_shr:1 row_mask:0xf bank_mask:0xf\n\tv_pk_fma_f16 v138, v88, v78, v138 op_sel:[0,0,0] op_sel_hi:[0,1,1]\n\t" \
  "v_pk_fma_f16 v139, v89, v79, v139 op_sel:[0,0,0] op_sel_hi:[0,1,1]\n\tv_cvt_pk_f16_f32 v94, v64, -v65\n\t" \
  "v_cvt_pk_f16_f32 v95, v66, -v67\n\tv_max_u32_dpp v102, v102, v102 row_shr:2 row_mask:0xf bank_mask:0xf\n\t" \
  "v_pk_mul_f16 v140, v88, v82 op_sel:[1,1] op_sel_hi:[1,0] neg_lo:[1,0]\n\tv_pk_mul_f16 v141, v89, v83 op_sel:[1,1] op_sel_hi:[1,0] neg_lo:[1,0]\n\t" \
  "v_pk_fma_f16 v140, v88, v82, v140 op_sel:[0,0,0] op_sel_hi:[0,1,1]\n\tv_pk_fma_f16 v141, v89, v83, v141 op_sel:[0,0,0] op_sel_hi:[0,1,1]\n\t" \
  "ds_write_addtid_b32 v88 offset:3168\n\tds_write_addtid_b32 v89 offset:7776\n\t" \
  "ds_write_addtid_b32 v136 offset:0\n\tds_write_addtid_b32 v137 offset:4608\n\t" \
  "ds_write_addtid_b32 v138 offset:288\n\tds_write_addtid_b32 v139 offset:4896\n\t" \
  "ds_write_addtid_b32 v94 offset:1152\n\tds_write_addtid_b32 v95 offset:5760\n\t" \
  "ds_write_addtid_b32 v140 offset:576\n\tds_write_addtid_b32 v141 offset:5184\n\t" \
  "v_max_u32_dpp v102, v102, v102 row_shr:4 row_mask:0xf bank_mask:0xf\n\tv_pk_mul_f16 v142, v88, v86 op_sel:[1,1] op_sel_hi:[1,0] neg_lo:[1,0]\n\t" \
  "v_pk_mul_f16 v143, v89, v87 op_sel:[1,1] op_sel_hi:[1,0] neg_lo:[1,0]\n\tv_pk_fma_f16 v142, v88, v86, v142 op_sel:[0,0,0] op_sel_hi:[0,1,1]\n\t" \
  "v_pk_fma_f16 v143, v89, v87, v143 op_sel:[0,0,0] op_sel_hi:[0,1,1]\n\tv_max_u32_dpp v102, v102, v102 row_shr:8 row_mask:0xf bank_mask:0xf\n\t" \
  "v_pk_mul_f16 v144, v90, v78 op_sel:[1,1] op_sel_hi:[1,0] neg_lo:[1,0]\n\tv_pk_mul_f16 v145, v91, v79 op_sel:[1,1] op_sel_hi:[1,0] neg_lo:[1,0]\n\t" \
  "v_pk_fma_f16 v144, v90, v78, v144 op_sel:[0,0,0] op_sel_hi:[0,1,1]\n\tv_pk_fma_f16 v145, v91, v79, v145 op_sel:[0,0,0] op_sel_hi:[0,1,1]\n\t" \
  "v_max_u32_dpp v102, v102, v102 row_bcast:15 row_mask:0xa bank_mask:0xf\n\tv_pk_mul_f16 v146, v90, v86 op_sel:[1,1] op_sel_hi:[1,0] neg_lo:[1,0]\n\t" \
  "v_pk_mul_f16 v147, v91, v87 op_sel:[1,1] op_sel_hi:[1,0] neg_lo:[1,0]\n\tv_pk_fma_f16 v146, v90, v86, v146 op_sel:[0,0,0] op_sel_hi:[0,1,1]\n\t" \
  "v_pk_fma_f16 v147, v91, v87, v147 op_sel:[0,0,0] op_sel_hi:[0,1,1]\n\tv_max_u32_dpp v102, v102, v102 row_bcast:31 row_mask:0xc bank_mask:0xf\n\t" \
  "v_pk_mul_f16 v148, v92, v78 op_sel:[1,1] op_sel_hi:[1,0] neg_lo:[1,0]\n\tv_pk_mul_f16 v149, v93, v79 op_sel:[1,1] op_sel_hi:[1,0] neg_lo:[1,0]\n\t" \
  "v_pk_fma_f16 v148, v92, v78, v148 op_sel:[0,0,0] op_sel_hi:[0,1,1]\n\tv_pk_fma_f16 v149, v93, v79, v149 op_sel:[0,0,0] op_sel_hi:[0,1,1]\n\t" \
  "v_readlane_b32 %[e], v102, 63\n\tv_pk_mul_f16 v150, v92, v86 op_sel:[1,1] op_sel_hi:[1,0] neg_lo:[1,0]\n\t" \
  "v_pk_mul_f16 v151, v93, v87 op_sel:[1,1] op_sel_hi:[1,0] neg_lo:[1,0]\n\tv_pk_fma_f16 v150, v92, v86, v150 op_sel:[0,0,0] op_sel_hi:[0,1,1]\n\t" \
  "v_pk_fma_f16 v151, v93, v87, v151 op_sel:[0,0,0] op_sel_hi:[0,1,1]\n\ts_lshr_b32 %[e], %[e], 23\n\t" \
  "s_max_u32 %[e], %[e], %[em]\n\ts_sub_u32 s6, 260, %[e]\n\t" \
  "s_lshl_b32 s6, s6, 23\n\tds_write_addtid_b32 v142 offset:864\n\t" \
  "ds_write_addtid_b32 v143 offset:5472\n\tds_write_addtid_b32 v144 offset:3456\n\t" \
  "ds_write_addtid_b32 v145 offset:8064\n\tds_write_addtid_b32 v146 offset:3744\n\t" \
  "ds_write_addtid_b32 v147 offset:8352\n\tds_write_addtid_b32 v148 offset:4032\n\t" \
  "ds_write_addtid_b32 v149 offset:8640\n\tds_write_addtid_b32 v150 offset:4320\n\t" \
  "ds_write_addtid_b32 v151 offset:8928\n\tv_pk_mul_f16 v96, v94, v88 op_sel:[1,1] op_sel_hi:[1,0] neg_lo:[1,0]\n\t" \
  "v_pk_mul_f16 v97, v95, v89 op_sel:[1,1] op_sel_hi:[1,0] neg_lo:[1,0]\n\tv_pk_fma_f16 v96, v94, v88, v96 op_sel:[0,0,0] op_sel_hi:[0,1,1]\n\t" \
  "v_pk_fma_f16 v97, v95, v89, v97 op_sel:[0,0,0] op_sel_hi:[0,1,1]\n\tv_pk_mul_f16 v98, v94, v90 op_sel:[1,1] op_sel_hi:[1,0] neg_lo:[1,0]\n\t" \
  "v_pk_mul_f16 v99, v95, v91 op_sel:[1,1] op_sel_hi:[1,0] neg_lo:[1,0]\n\tv_pk_fma_f16 v98, v94, v90, v98 op_sel:[0,0,0] op_sel_hi:[0,1,1]\n\t" \
  "v_pk_fma_f16 v99, v95, v91, v99 op_sel:[0,0,0] op_sel_hi:[0,1,1]\n\tv_pk_mul_f16 v100, v94, v92 op_sel:[1,1] op_sel_hi:[1,0] neg_lo:[1,0]\n\t" \
  "v_pk_mul_f16 v101, v95, v93 op_sel:[1,1] op_sel_hi:[1,0] neg_lo:[1,0]\n\tv_pk_fma_f16 v100, v94, v92, v100 op_sel:[0,0,0] op_sel_hi:[0,1,1]\n\t" \
  "v_pk_fma_f16 v101, v95, v93, v101 op_sel:[0,0,0] op_sel_hi:[0,1,1]\n\tds_write_addtid_b32 v96 offset:1440\n\t" \
  "ds_write_addtid_b32 v97 offset:6048\n\tds_write_addtid_b32 v98 offset:1728\n\t" \
  "ds_write_addtid_b32 v99 offset:6336\n\tds_write_addtid_b32 v100 offset:2016\n\t" \
  "ds_write_addtid_b32 v101 offset:6624\n\tds_read_b128 v[136:139], v71 offset:0\n\t" \
  "ds_read_b128 v[140:143], v71 offset:64\n\tds_read_b128 v[144:147], v71 offset:128\n\t" \
  "s_cmp_lt_u32 %[w], 50176\n\ts_cbranch_scc0 1f\n\t" \
  "s_setprio 1\n\t1:\n\t" \
  "ds_read_b128 v[148:151], v71 offset:192\n\tds_read_b128 v[152:155], v71 offset:4608\n\t" \
  "ds_read_b128 v[156:159], v71 offset:4672\n\tds_read_b128 v[160:163], v71 offset:4736\n\t" \
  "ds_read_b128 v[164:167], v71 offset:4800\n\tv_pk_mul_f32 v[48:49], v[60:61], v[60:61] op_sel:[1,1] op_sel_hi:[1,0] neg_lo:[1,0]\n\t" \
  "v_pk_mul_f32 v[50:51], v[62:63], v[62:63] op_sel:[1,1] op_sel_hi:[1,0] neg_lo:[1,0]\n\tv_pk_fma_f32 v[48:49], v[60:61], v[60:61], v[48:49] op_sel:[0,0,0] op_sel_hi:[0,1,1]\n\t" \
  "v_pk_fma_f32 v[50:51], v[62:63], v[62:63], v[50:51] op_sel:[0,0,0] op_sel_hi:[0,1,1]\n\tv_pk_mul_f32 v[52:53], v[48:49], v[60:61] op_sel:[1,1] op_sel_hi:[1,0] neg_lo:[1,0]\n\t" \
  "v_pk_mul_f32 v[54:55], v[50:51], v[62:63] op_sel:[1,1] op_sel_hi:[1,0] neg_lo:[1,0]\n\tv_pk_fma_f32 v[52:53], v[48:49], v[60:61], v[52:53] op_sel:[0,0,0] op_sel_hi:[0,1,1]\n\t" \
  "v_pk_fma_f32 v[54:55], v[50:51], v[62:63], v[54:55] op_sel:[0,0,0] op_sel_hi:[0,1,1]\n\tv_pk_mul_f32 v[40:41], v[48:49], v[48:49] op_sel:[1,1] op_sel_hi:[1,0] neg_lo:[1,0]\n\t" \
  "v_pk_mul_f32 v[42:43], v[50:51], v[50:51] op_sel:[1,1] op_sel_hi:[1,0] neg_lo:[1,0]\n\tv_pk_fma_f32 v[40:41], v[48:49], v[48:49], v[40:41] op_sel:[0,0,0] op_sel_hi:[0,1,1]\n\t" \
  "v_pk_fma_f32 v[42:43], v[50:51], v[50:51], v[42:43] op_sel:[0,0,0] op_sel_hi:[0,1,1]\n\tv_cvt_pk_f16_f32 v46, v48, v49\n\t" \
  "v_cvt_pk_f16_f32 v47, v50, v51\n\tv_cvt_pk_f16_f32 v56, v52, v53\n\t" \
  "v_cvt_pk_f16_f32 v57, v54, v55\n\tv_pk_mul_f32 v[64:65], s[6:7], v[4:5] op_sel_hi:[0,1]\n\t" \
  "v_pk_mul_f32 v[66:67], s[6:7], v[6:7] op_sel_hi:[0,1]\n\tv_cvt_pk_f16_f32 v48, v64, v65\n\t" \
  "v_cvt_pk_f16_f32 v49, v66, v67\n\tv_pk_mul_f32 v[68:69], v[64:65], v[40:41] op_sel:[1,1] op_sel_hi:[1,0] neg_lo:[1,0]\n\t" \
  "v_pk_mul_f32 v[70:71], v[66:67], v[42:43] op_sel:[1,1] op_sel_hi:[1,0] neg_lo:[1,0]\n\tv_pk_fma_f32 v[68:69], v[64:65], v[40:41], v[68:69] op_sel:[0,0,0] op_sel_hi:[0,1,1]\n\t" \
  "v_pk_fma_f32 v[70:71], v[66:67], v[42:43], v[70:71] op_sel:[0,0,0] op_sel_hi:[0,1,1]\n\tv_pk_mul_f16 v50, v48, v86 op_sel:[1,1] op_sel_hi:[1,0]\n\t" \
  "v_pk_mul_f16 v51, v49, v87 op_sel:[1,1] op_sel_hi:[1,0]\n\tv_pk_fma_f16 v50, v48, v86, v50 op_sel:[0,0,0] op_sel_hi:[0,1,1] neg_hi:[0,1,0]\n\t" \
  "v_pk_fma_f16 v51, v49, v87, v51 op_sel:[0,0,0] op_sel_hi:[0,1,1] neg_hi:[0,1,0]\n\tv_pk_mul_f16 v52, v48, v46 op_sel:[1,1] op_sel_hi:[1,0] neg_lo:[1,0]\n\t" \
  "v_pk_mul_f16 v53, v49, v47 op_sel:[1,1] op_sel_hi:[1,0] neg_lo:[1,0]\n\tv_pk_fma_f16 v52, v48, v46, v52 op_sel:[0,0,0] op_sel_hi:[0,1,1]\n\t" \
  "v_pk_fma_f16 v53, v49, v47, v53 op_sel:[0,0,0] op_sel_hi:[0,1,1]\n\tv_pk_mul_f16 v54, v48, v56 op_sel:[1,1] op_sel_hi:[1,0] neg_lo:[1,0]\n\t" \
  "v_pk_mul_f16 v55, v49, v57 op_sel:[1,1] op_sel_hi:[1,0] neg_lo:[1,0]\n\tv_pk_fma_f16 v54, v48, v56, v54 op_sel:[0,0,0] op_sel_hi:[0,1,1]\n\t" \
  "v_pk_fma_f16 v55, v49, v57, v55 op_sel:[0,0,0] op_sel_hi:[0,1,1]\n\tds_write_addtid_b32 v48 offset:0\n\t" \
  "ds_write_addtid_b32 v49 offset:4608\n\tds_write_addtid_b32 v50 offset:288\n\t" \
  "ds_write_addtid_b32 v51 offset:4896\n\tds_write_addtid_b32 v52 offset:576\n\t" \
  "ds_write_addtid_b32 v53 offset:5184\n\tds_write_addtid_b32 v54 offset:864\n\t" \
  "ds_write_addtid_b32 v55 offset:5472\n\tv_cvt_pk_f16_f32 v48, v68, v69\n\t" \
  "v_cvt_pk_f16_f32 v49, v70, v71\n\tv_pk_mul_f32 v[64:65], v[68:69], v[40:41] op_sel:[1,1] op_sel_hi:[1,0] neg_lo:[1,0]\n\t" \
  "v_pk_mul_f32 v[66:67], v[70:71], v[42:43] op_sel:[1,1] op_sel_hi:[1,0] neg_lo:[1,0]\n\tv_pk_fma_f32 v[64:65], v[68:69], v[40:41], v[64:65] op_sel:[0,0,0] op_sel_hi:[0,1,1]\n\t" \
  "v_pk_fma_f32 v[66:67], v[70:71], v[42:43], v[66:67] op_sel:[0,0,0] op_sel_hi:[0,1,1]\n\tv_pk_mul_f16 v50, v48, v86 op_sel:[1,1] op_sel_hi:[1,0]\n\t" \
  "v_pk_mul_f16 v51, v49, v87 op_sel:[1,1] op_sel_hi:[1,0]\n\tv_pk_fma_f16 v50, v48, v86, v50 op_sel:[0,0,0] op_sel_hi:[0,1,1] neg_hi:[0,1,0]\n\t" \
  "v_pk_fma_f16 v51, v49, v87, v51 op_sel:[0,0,0] op_sel_hi:[0,1,1] neg_hi:[0,1,0]\n\tv_pk_mul_f16 v52, v48, v46 op_sel:[1,1] op_sel_hi:[1,0] neg_lo:[1,0]\n\t" \
  "v_pk_mul_f16 v53, v49, v47 op_sel:[1,1] op_sel_hi:[1,0] neg_lo:[1,0]\n\tv_pk_fma_f16 v52, v48, v46, v52 op_sel:[0,0,0] op_sel_hi:[0,1,1]\n\t" \
  "v_pk_fma_f16 v53, v49, v47, v53 op_sel:[0,0,0] op_sel_hi:[0,1,1]\n\tv_pk_mul_f16 v54, v48, v56 op_sel:[1,1] op_sel_hi:[1,0] neg_lo:[1,0]\n\t" \
  "v_pk_mul_f16 v55, v49, v57 op_sel:[1,1] op_sel_hi:[1,0] neg_lo:[1,0]\n\tv_pk_fma_f16 v54, v48, v56, v54 op_sel:[0,0,0] op_sel_hi:[0,1,1]\n\t" \
  "v_pk_fma_f16 v55, v49, v57, v55 op_sel:[0,0,0] op_sel_hi:[0,1,1]\n\tds_write_addtid_b32 v48 offset:1152\n\t" \
  "ds_write_addtid_b32 v49 offset:5760\n\tds_write_addtid_b32 v50 offset:1440\n\t" \
  "ds_write_addtid_b32 v51 offset:6048\n\tds_write_addtid_b32 v52 offset:1728\n\t" \
  "ds_write_addtid_b32 v53 offset:6336\n\tds_write_addtid_b32 v54 offset:2016\n\t" \
  "ds_write_addtid_b32 v55 offset:6624\n\tv_cvt_pk_f16_f32 v48, v64, v65\n\t" \
  "v_cvt_pk_f16_f32 v49, v66, v67\n\tv_pk_mul_f32 v[68:69], v[64:65], v[40:41] op_sel:[1,1] op_sel_hi:[1,0] neg_lo:[1,0]\n\t" \
  "v_pk_mul_f32 v[70:71], v[66:67], v[42:43] op_sel:[1,1] op_sel_hi:[1,0] neg_lo:[1,0]\n\tv_pk_fma_f32 v[68:69], v[64:65], v[40:41], v[68:69] op_sel:[0,0,0] op_sel_hi:[0,1,1]\n\t" \
  "v_pk_fma_f32 v[70:71], v[66:67], v[42:43], v[70:71] op_sel:[0,0,0] op_sel_hi:[0,1,1]\n\tv_pk_mul_f16 v50, v48, v86 op_sel:[1,1] op_sel_hi:[1,0]\n\t" \
  "v_pk_mul_f16 v51, v49, v87 op_sel:[1,1] op_sel_hi:[1,0]\n\tv_pk_fma_f16 v50, v48, v86, v50 op_sel:[0,0,0] op_sel_hi:[0,1,1] neg_hi:[0,1,0]\n\t" \
  "v_pk_fma_f16 v51, v49, v87, v51 op_sel:[0,0,0] op_sel_hi:[0,1,1] neg_hi:[0,1,0]\n\tv_pk_mul_f16 v52, v48, v46 op_sel:[1,1] op_sel_hi:[1,0] neg_lo:[1,0]\n\t" \
  "v_pk_mul_f16 v53, v49, v47 op_sel:[1,1] op_sel_hi:[1,0] neg_lo:[1,0]\n\tv_pk_fma_f16 v52, v48, v46, v52 op_sel:[0,0,0] op_sel_hi:[0,1,1]\n\t" \
  "v_pk_fma_f16 v53, v49, v47, v53 op_sel:[0,0,0] op_sel_hi:[0,1,1]\n\tv_pk_mul_f16 v54, v48, v56 op_sel:[1,1] op_sel_hi:[1,0] neg_lo:[1,0]\n\t" \
  "v_pk_mul_f16 v55, v49, v57 op_sel:[1,1] op_sel_hi:[1,0] neg_lo:[1,0]\n\tv_pk_fma_f16 v54, v48, v56, v54 op_sel:[0,0,0] op_sel_hi:[0,1,1]\n\t" \
  "v_pk_fma_f16 v55, v49, v57, v55 op_sel:[0,0,0] op_sel_hi:[0,1,1]\n\tds_write_addtid_b32 v48 offset:2304\n\t" \
  "ds_write_addtid_b32 v49 offset:6912\n\tds_write_addtid_b32 v50 offset:2592\n\t" \
  "ds_write_addtid_b32 v51 offset:7200\n\tds_write_addtid_b32 v52 offset:2880\n\t" \
  "ds_write_addtid_b32 v53 offset:7488\n\tds_write_addtid_b32 v54 offset:3168\n\t" \
  "ds_write_addtid_b32 v55 offset:7776\n\tv_cvt_pk_f16_f32 v48, v68, v69\n\t" \
  "v_cvt_pk_f16_f32 v49, v70, v71\n\tv_pk_mul_f16 v50, v48, v86 op_sel:[1,1] op_sel_hi:[1,0]\n\t" \
  "v_pk_mul_f16 v51, v49, v87 op_sel:[1,1] op_sel_hi:[1,0]\n\tv_pk_fma_f16 v50, v48, v86, v50 op_sel:[0,0,0] op_sel_hi:[0,1,1] neg_hi:[0,1,0]\n\t" \
  "v_pk_fma_f16 v51, v49, v87, v51 op_sel:[0,0,0] op_sel_hi:[0,1,1] neg_hi:[0,1,0]\n\tv_pk_mul_f16 v52, v48, v46 op_sel:[1,1] op_sel_hi:[1,0] neg_lo:[1,0]\n\t" \
  "v_pk_mul_f16 v53, v49, v47 op_sel:[1,1] op_sel_hi:[1,0] neg_lo:[1,0]\n\tv_pk_fma_f16 v52, v48, v46, v52 op_sel:[0,0,0] op_sel_hi:[0,1,1]\n\t" \
  "v_pk_fma_f16 v53, v49, v47, v53 op_sel:[0,0,0] op_sel_hi:[0,1,1]\n\tv_pk_mul_f16 v54, v48, v56 op_sel:[1,1] op_sel_hi:[1,0] neg_lo:[1,0]\n\t" \
  "v_pk_mul_f16 v55, v49, v57 op_sel:[1,1] op_sel_hi:[1,0] neg_lo:[1,0]\n\tv_pk_fma_f16 v54, v48, v56, v54 op_sel:[0,0,0] op_sel_hi:[0,1,1]\n\t" \
  "v_pk_fma_f16 v55, v49, v57, v55 op_sel:[0,0,0] op_sel_hi:[0,1,1]\n\tds_write_addtid_b32 v48 offset:3456\n\t" \
  "ds_write_addtid_b32 v49 offset:8064\n\tds_write_addtid_b32 v50 offset:3744\n\t" \
  "ds_write_addtid_b32 v51 offset:8352\n\tds_write_addtid_b32 v52 offset:4032\n\t" \
  "ds_write_addtid_b32 v53 offset:8640\n\tds_write_addtid_b32 v54 offset:4320\n\t" \
  "ds_write_addtid_b32 v55 offset:8928\n\tds_read_b128 v[72:75], v27 offset:0\n\t" \
  "ds_read_b128 v[76:79], v27 offset:64\n\tds_read_b128 v[80:83], v27 offset:128\n\t" \
  "ds_read_b128 v[84:87], v27 offset:192\n\tds_read_b128 v[88:91], v27 offset:4608\n\t" \
  "ds_read_b128 v[92:95], v27 offset:4672\n\tds_read_b128 v[96:99], v27 offset:4736\n\t" \
  "ds_read_b128 v[100:103], v27 offset:4800\n\tds_write_addtid_b32 v36 offset:0\n\t" \
  "ds_write_addtid_b32 v37 offset:4608\n\tds_read_b128 v[40:43], v12 offset:0\n\t" \
  "ds_read_b128 v[44:47], v12 offset:64\n\tds_read_b128 v[48:51], v12 offset:128\n\t" \
  "v_add_u32_sdwa v56, %[w], v16 dst_sel:DWORD dst_unused:UNUSED_PAD src0_sel:DWORD src1_sel:WORD_0\n\tv_add_u32_sdwa v57, %[w], v16 dst_sel:DWORD dst_unused:UNUSED_PAD src0_sel:DWORD src1_sel:WORD_1\n\t" \
  "v_add_u32_sdwa v58, %[w], v17 dst_sel:DWORD dst_unused:UNUSED_PAD src0_sel:DWORD src1_sel:WORD_0\n\tv_add_u32_sdwa v59, %[w], v17 dst_sel:DWORD dst_unused:UNUSED_PAD src0_sel:DWORD src1_sel:WORD_1\n\t" \
  "v_add_u32 v14, v56, v35\n\tv_add_u32 v15, v57, v35\n\t" \
  "s_waitcnt lgkmcnt(12)\n\tv_mfma_f32_16x16x32_f16 v[60:63], v[104:107], v[72:75], 0\n\t" \
  "v_mfma_f32_16x16x32_f16 v[4:7], v[136:139], v[72:75], 0\n\ts_cmp_lt_u32 %[w], 50176\n\t" \
  "s_cbranch_scc1 1f\n\ts_setprio 1\n\t" \
  "1:\n\ts_waitcnt lgkmcnt(2)\n\t" \
  "v_mfma_f32_16x16x32_f16 v[60:63], v[108:111], v[76:79], v[60:63]\n\tv_pk_mul_f16 v52, v40, v72 op_sel:[1,1] op_sel_hi:[1,0] neg_lo:[1,0]\n\t" \
  "v_pk_mul_f16 v53, v41, v73 op_sel:[1,1] op_sel_hi:[1,0] neg_lo:[1,0]\n\tv_pk_mul_f16 v54, v42, v74 op_sel:[1,1] op_sel_hi:[1,0] neg_lo:[1,0]\n\t" \
  "v_pk_mul_f16 v55, v43, v75 op_sel:[1,1] op_sel_hi:[1,0] neg_lo:[1,0]\n\tv_mfma_f32_16x16x32_f16 v[4:7], v[140:143], v[76:79], v[4:7]\n\t" \
  "v_pk_fma_f16 v40, v40, v72, v52 op_sel:[0,0,0] op_sel_hi:[0,1,1]\n\tv_pk_fma_f16 v41, v41, v73, v53 op_sel:[0,0,0] op_sel_hi:[0,1,1]\n\t" \
  "v_pk_fma_f16 v42, v42, v74, v54 op_sel:[0,0,0] op_sel_hi:[0,1,1]\n\tv_pk_fma_f16 v43, v43, v75, v55 op_sel:[0,0,0] op_sel_hi:[0,1,1]\n\t" \
  "s_waitcnt lgkmcnt(1)\n\tv_mfma_f32_16x16x32_f16 v[60:63], v[112:115], v[80:83], v[60:63]\n\t" \
  "ds_read_b128 v[72:75], v12 offset:192\n\tv_pk_mul_f16 v52, v44, v76 op_sel:[1,1] op_sel_hi:[1,0] neg_lo:[1,0]\n\t" \
  "v_pk_mul_f16 v53, v45, v77 op_sel:[1,1] op_sel_hi:[1,0] neg_lo:[1,0]\n\tv_pk_mul_f16 v54, v46, v78 op_sel:[1,1] op_sel_hi:[1,0] neg_lo:[1,0]\n\t" \
  "v_pk_mul_f16 v55, v47, v79 op_sel:[1,1] op_sel_hi:[1,0] neg_lo:[1,0]\n\tv_mfma_f32_16x16x32_f16 v[4:7], v[144:147], v[80:83], v[4:7]\n\t" \
  "v_pk_fma_f16 v44, v44, v76, v52 op_sel:[0,0,0] op_sel_hi:[0,1,1]\n\tv_pk_fma_f16 v45, v45, v77, v53 op_sel:[0,0,0] op_sel_hi:[0,1,1]\n\t" \
  "v_pk_fma_f16 v46, v46, v78, v54 op_sel:[0,0,0] op_sel_hi:[0,1,1]\n\tv_pk_fma_f16 v47, v47, v79, v55 op_sel:[0,0,0] op_sel_hi:[0,1,1]\n\t" \
  "s_waitcnt lgkmcnt(1)\n\tv_mfma_f32_16x16x32_f16 v[60:63], v[116:119], v[84:87], v[60:63]\n\t" \
  "ds_read_b128 v[76:79], v12 offset:4608\n\tv_pk_mul_f16 v52, v48, v80 op_sel:[1,1] op_sel_hi:[1,0] neg_lo:[1,0]\n\t" \
  "v_pk_mul_f16 v53, v49, v81 op_sel:[1,1] op_sel_hi:[1,0] neg_lo:[1,0]\n\tv_pk_mul_f16 v54, v50, v82 op_sel:[1,1] op_sel_hi:[1,0] neg_lo:[1,0]\n\t" \
  "v_pk_mul_f16 v55, v51, v83 op_sel:[1,1] op_sel_hi:[1,0] neg_lo:[1,0]\n\tv_mfma_f32_16x16x32_f16 v[4:7], v[148:151], v[84:87], v[4:7]\n\t" \
  "v_pk_fma_f16 v48, v48, v80, v52 op_sel:[0,0,0] op_sel_hi:[0,1,1]\n\tv_pk_fma_f16 v49, v49, v81, v53 op_sel:[0,0,0] op_sel_hi:[0,1,1]\n\t" \
  "v_pk_fma_f16 v50, v50, v82, v54 op_sel:[0,0,0] op_sel_hi:[0,1,1]\n\tv_pk_fma_f16 v51, v51, v83, v55 op_sel:[0,0,0] op_sel_hi:[0,1,1]\n\t" \
  "s_waitcnt lgkmcnt(1)\n\tv_mfma_f32_16x16x32_f16 v[60:63], v[120:123], v[88:91], v[60:63]\n\t" \
  "ds_read_b128 v[80:83], v12 offset:4672\n\tv_pk_mul_f16 v52, v72, v84 op_sel:[1,1] op_sel_hi:[1,0] neg_lo:[1,0]\n\t" \
  "v_pk_mul_f16 v53, v73, v85 op_sel:[1,1] op_sel_hi:[1,0] neg_lo:[1,0]\n\tv_pk_mul_f16 v54, v74, v86 op_sel:[1,1] op_sel_hi:[1,0] neg_lo:[1,0]\n\t" \
  "v_pk_mul_f16 v55, v75, v87 op_sel:[1,1] op_sel_hi:[1,0] neg_lo:[1,0]\n\tv_mfma_f32_16x16x32_f16 v[4:7], v[152:155], v[88:91], v[4:7]\n\t" \
  "v_pk_fma_f16 v72, v72, v84, v52 op_sel:[0,0,0] op_sel_hi:[0,1,1]\n\tv_pk_fma_f16 v73, v73, v85, v53 op_sel:[0,0,0] op_sel_hi:[0,1,1]\n\t" \
  "v_pk_fma_f16 v74, v74, v86, v54 op_sel:[0,0,0] op_sel_hi:[0,1,1]\n\tv_pk_fma_f16 v75, v75, v87, v55 op_sel:[0,0,0] op_sel_hi:[0,1,1]\n\t" \
  "s_waitcnt lgkmcnt(1)\n\tv_mfma_f32_16x16x32_f16 v[60:63], v[124:127], v[92:95], v[60:63]\n\t" \
  "ds_read_b128 v[84:87], v12 offset:4736\n\tv_pk_mul_f16 v52, v76, v88 op_sel:[1,1] op_sel_hi:[1,0] neg_lo:[1,0]\n\t" \
  "v_pk_mul_f16 v53, v77, v89 op_sel:[1,1] op_sel_hi:[1,0] neg_lo:[1,0]\n\tv_pk_mul_f16 v54, v78, v90 op_sel:[1,1] op_sel_hi:[1,0] neg_lo:[1,0]\n\t" \
  "v_pk_mul_f16 v55, v79, v91 op_sel:[1,1] op_sel_hi:[1,0] neg_lo:[1,0]\n\tv_mfma_f32_16x16x32_f16 v[4:7], v[156:159], v[92:95], v[4:7]\n\t" \
  "v_pk_fma_f16 v76, v76, v88, v52 op_sel:[0,0,0] op_sel_hi:[0,1,1]\n\tv_pk_fma_f16 v77, v77, v89, v53 op_sel:[0,0,0] op_sel_hi:[0,1,1]\n\t" \
  "v_pk_fma_f16 v78, v78, v90, v54 op_sel:[0,0,0] op_sel_hi:[0,1,1]\n\tv_pk_fma_f16 v79, v79, v91, v55 op_sel:[0,0,0] op_sel_hi:[0,1,1]\n\t" \
  "s_waitcnt lgkmcnt(1)\n\tv_mfma_f32_16x16x32_f16 v[60:63], v[128:131], v[96:99], v[60:63]\n\t" \
  "ds_read_b128 v[88:91], v12 offset:4800\n\tv_pk_mul_f16 v52, v80, v92 op_sel:[1,1] op_sel_hi:[1,0] neg_lo:[1,0]\n\t" \
  "v_pk_mul_f16 v53, v81, v93 op_sel:[1,1] op_sel_hi:[1,0] neg_lo:[1,0]\n\tv_pk_mul_f16 v54, v82, v94 op_sel:[1,1] op_sel_hi:[1,0] neg_lo:[1,0]\n\t" \
  "v_pk_mul_f16 v55, v83, v95 op_sel:[1,1] op_sel_hi:[1,0] neg_lo:[1,0]\n\tv_mfma_f32_16x16x32_f16 v[4:7], v[160:163], v[96:99], v[4:7]\n\t" \
  "v_pk_fma_f16 v80, v80, v92, v52 op_sel:[0,0,0] op_sel_hi:[0,1,1]\n\tv_pk_fma_f16 v81, v81, v93, v53 op_sel:[0,0,0] op_sel_hi:[0,1,1]\n\t" \
  "v_pk_fma_f16 v82, v82, v94, v54 op_sel:[0,0,0] op_sel_hi:[0,1,1]\n\tv_pk_fma_f16 v83, v83, v95, v55 op_sel:[0,0,0] op_sel_hi:[0,1,1]\n\t" \
  "s_waitcnt lgkmcnt(1)\n\tv_mfma_f32_16x16x32_f16 v[60:63], v[132:135], v[100:103], v[60:63]\n\t" \
  "v_pk_mul_f16 v52, v84, v96 op_sel:[1,1] op_sel_hi:[1,0] neg_lo:[1,0]\n\tv_pk_mul_f16 v53, v85, v97 op_sel:[1,1] op_sel_hi:[1,0] neg_lo:[1,0]\n\t" \
  "v_pk_mul_f16 v54, v86, v98 op_sel:[1,1] op_sel_hi:[1,0] neg_lo:[1,0]\n\tv_pk_mul_f16 v55, v87, v99 op_sel:[1,1] op_sel_hi:[1,0] neg_lo:[1,0]\n\t" \
  "v_mfma_f32_16x16x32_f16 v[4:7], v[164:167], v[100:103], v[4:7]\n\tv_pk_fma_f16 v84, v84, v96, v52 op_sel:[0,0,0] op_sel_hi:[0,1,1]\n\t" \
  "v_pk_fma_f16 v85, v85, v97, v53 op_sel:[0,0,0] op_sel_hi:[0,1,1]\n\tv_pk_fma_f16 v86, v86, v98, v54 op_sel:[0,0,0] op_sel_hi:[0,1,1]\n\t" \
  "v_pk_fma_f16 v87, v87, v99, v55 op_sel:[0,0,0] op_sel_hi:[0,1,1]\n\ts_waitcnt lgkmcnt(0)\n\t" \
  "v_mfma_f32_16x16x32_f16 v[8:11], v[136:139], v[40:43], 0\n\tv_pk_mul_f16 v52, v88, v100 op_sel:[1,1] op_sel_hi:[1,0] neg_lo:[1,0]\n\t" \
  "v_pk_mul_f16 v53, v89, v101 op_sel:[1,1] op_sel_hi:[1,0] neg_lo:[1,0]\n\tv_pk_mul_f16 v54, v90, v102 op_sel:[1,1] op_sel_hi:[1,0] neg_lo:[1,0]\n\t" \
  "v_mfma_f32_16x16x32_f16 v[64:67], v[104:107], v[40:43], 0\n\tv_pk_mul_f16 v55, v91, v103 op_sel:[1,1] op_sel_hi:[1,0] neg_lo:[1,0]\n\t" \
  "v_pk_fma_f16 v88, v88, v100, v52 op_sel:[0,0,0] op_sel_hi:[0,1,1]\n\tv_pk_fma_f16 v89, v89, v101, v53 op_sel:[0,0,0] op_sel_hi:[0,1,1]\n\t" \
  "v_mfma_f32_16x16x32_f16 v[8:11], v[140:143], v[44:47], v[8:11]\n\tv_pk_fma_f16 v90, v90, v102, v54 op_sel:[0,0,0] op_sel_hi:[0,1,1]\n\t" \
  "v_pk_fma_f16 v91, v91, v103, v55 op_sel:[0,0,0] op_sel_hi:[0,1,1]\n\tv_mov_b64 v[100:101], 0\n\t" \
  "v_mfma_f32_16x16x32_f16 v[64:67], v[108:111], v[44:47], v[64:67]\n\tv_mov_b64 v[102:103], 0\n\t" \
  "v_cvt_pk_f16_f32 v4, v4, v5\n\tv_cvt_pk_f16_f32 v5, v6, v7\n\t" \
  "v_mfma_f32_16x16x32_f16 v[8:11], v[144:147], v[48:51], v[8:11]\n\tv_cvt_pk_f16_f32 v92, v60, v61\n\t" \
  "v_cvt_pk_f16_f32 v93, v62, v63\n\tv_permlane16_swap_b32 v4, v5\n\t" \
  "v_mfma_f32_16x16x32_f16 v[64:67], v[112:115], v[48:51], v[64:67]\n\tv_perm_b32 v6, v4, v4, v33\n\t" \
  "v_perm_b32 v7, v4, v4, v34\n\tv_mfma_f32_16x16x32_f16 v[8:11], v[148:151], v[72:75], v[8:11]\n\t" \
  "ds_write_b128 v58, v[100:103]\n\tv_mfma_f32_16x16x32_f16 v[64:67], v[116:119], v[72:75], v[64:67]\n\t" \
  "ds_write2_b32 v56, v92, v93 offset0:193 offset1:194\n\tv_mfma_f32_16x16x32_f16 v[8:11], v[152:155], v[76:79], v[8:11]\n\t" \
  "ds_write2_b32 v57, v92, v93 offset0:193 offset1:194\n\tv_mfma_f32_16x16x32_f16 v[64:67], v[120:123], v[76:79], v[64:67]\n\t" \
  "ds_write2_b32 v14, v6, v7 offset0:64 offset1:65\n\tv_mfma_f32_16x16x32_f16 v[8:11], v[156:159], v[80:83], v[8:11]\n\t" \
  "ds_write2_b32 v15, v6, v7 offset0:64 offset1:65\n\tv_mfma_f32_16x16x32_f16 v[64:67], v[124:127], v[80:83], v[64:67]\n\t" \
  "ds_write_b16 v59, v5 offset:48\n\tv_mfma_f32_16x16x32_f16 v[8:11], v[160:163], v[84:87], v[8:11]\n\t" \
  "ds_write_b16_d16_hi v59, v5 offset:16\n\tv_mfma_f32_16x16x32_f16 v[64:67], v[128:131], v[84:87], v[64:67]\n\t" \
  "ds_write_b16 v59, v5 offset:878\n\tv_mfma_f32_16x16x32_f16 v[8:11], v[164:167], v[88:91], v[8:11]\n\t" \
  "ds_write_b16_d16_hi v59, v5 offset:846\n\tv_mfma_f32_16x16x32_f16 v[64:67], v[132:135], v[88:91], v[64:67]\n\t" \
  "ds_write_b16 v59, v5 offset:1708\n\tds_write_b16_d16_hi v59, v5 offset:1676\n\t" \
  "ds_write_b16 v59, v5 offset:2538\n\tds_write_b16_d16_hi v59, v5 offset:2506\n\t" \
  "s_nop 1\n\tv_cvt_pk_f16_f32 v8, v8, v9\n\t" \
  "v_cvt_pk_f16_f32 v9, v10, v11\n\tv_cvt_pk_f16_f32 v46, v64, v65\n\t" \
  "v_cvt_pk_f16_f32 v47, v66, v67\n\tv_permlane16_swap_b32 v8, v9\n\t" \
  "v_perm_b32 v10, v8, v8, v33\n\tv_perm_b32 v11, v8, v8, v34\n\t" \
  "ds_write2_b32 v56, v46, v47 offset0:129 offset1:130\n\tds_write2_b32 v57, v46, v47 offset0:129 offset1:130\n\t" \
  "ds_write2_b32 v14, v10, v11 offset0:0 offset1:1\n\tds_write2_b32 v15, v10, v11 offset0:0 offset1:1\n\t" \
  "ds_write_b16 v59, v9 offset:32\n\tds_write_b16_d16_hi v59, v9 offset:0\n\t" \
  "ds_write_b16 v59, v9 offset:862\n\tds_write_b16_d16_hi v59, v9 offset:830\n\t" \
  "ds_write_b16 v59, v9 offset:1692\n\tds_write_b16_d16_hi v59, v9 offset:1660\n\t" \
  "ds_write_b16 v59, v9 offset:2522\n\tds_write_b16_d16_hi v59, v9 offset:2490\n\t"
#define GEN_CLOBBER "v4", "v5", "v6", "v7", "v10", "v11", "v13", "v14", "v15", "v36", "v37", "v38", "v39", "v40", "v41", "v42", "v43", "v44", "v45", "v47", "v48", "v49", "v50", "v51", "v52", "v53", "v54", "v55", "v56", "v57", "v58", "v59", "v60", "v61", "v62", "v63", "v64", "v65", "v66", "v67", "v72", "v73", "v74", "v75", "v76", "v77", "v78", "v79", "v80", "v81", "v82", "v83", "v84", "v85", "v86", "v87", "v88", "v89", "v90", "v91", "v92", "v94", "v95", "v98", "v99", "v100", "v101", "v102", "v103", "v104", "v105", "v106", "v107", "v108", "v109", "v110", "v111", "v112", "v113", "v114", "v115", "v116", "v117", "v118", "v119", "v120", "v121", "v122", "v123", "v124", "v125", "v126", "v127", "v128", "v129", "v130", "v131", "v132", "v133", "v134", "v135", "v136", "v137", "v138", "v139", "v140", "v141", "v142", "v143", "v144", "v145", "v146", "v147", "v148", "v149", "v150", "v151", "v152", "v153", "v154", "v155", "v156", "v157", "v158", "v159", "v160", "v161", "v162", "v163", "v164", "v165", "v166", "v167"
#define CONV_ASM \
  "ds_read_b32 v34, v30\n\tds_read_b64 v[132:133], v26 offset:512\n\tds_read_b64 v[134:135], v26 offset:520\n\t" \
  "ds_read_b64 v[100:101], v26 offset:0\n\tds_read_b64 v[102:103], v26 offset:8\n\tds_read_b64 v[136:137], v26 offset:576\n\t" \
  "ds_read_b64 v[138:139], v26 offset:584\n\tds_read_b64 v[104:105], v26 offset:64\n\tds_read_b64 v[106:107], v26 offset:72\n\t" \
  "ds_read_b64 v[140:141], v26 offset:640\n\tds_read_b64 v[142:143], v26 offset:648\n\tds_read_b64 v[108:109], v26 offset:128\n\t" \
  "ds_read_b64 v[110:111], v26 offset:136\n\tds_read_b64 v[144:145], v26 offset:704\n\tds_read_b64 v[146:147], v26 offset:712\n\t" \
  "ds_read_b64 v[112:113], v26 offset:192\n\tds_read_b64 v[114:115], v26 offset:200\n\tds_read_b64 v[148:149], v26 offset:768\n\t" \
  "ds_read_b64 v[150:151], v26 offset:776\n\tds_read_b64 v[116:117], v26 offset:256\n\tds_read_b64 v[118:119], v26 offset:264\n\t" \
  TS(60, 61) "s_waitcnt lgkmcnt(15)\n\tv_cmp_gt_u32 vcc, %[n0], v34\n\t" \
  "s_cbranch_vccz 2f\n\ts_mov_b32 s6, 0x10000\n\t1:\n\t" \
  "s_sleep 1\n\tds_read_b32 v34, v30\n\ts_sub_u32 s6, s6, 1\n\t" \
  "s_waitcnt lgkmcnt(0)\n\tv_cmp_gt_u32 vcc, %[n0], v34\n\ts_cbranch_scc1 2f\n\t" \
  "s_cbranch_vccnz 1b\n\t2:\n\t" TS(62, 63) \
  "s_cmp_lt_u32 %[w], 4\n\ts_setprio 1\n\ts_cbranch_scc1 1f\n\t" \
  "s_setprio 2\n\t1:\n\tds_read_b128 v[0:3], v28 offset:0\n\t" \
  "ds_read_b128 v[4:7], v28 offset:64\n\tds_read_b128 v[8:11], v28 offset:128\n\tds_read_b128 v[12:15], v28 offset:192\n\t" \
  "ds_read_b128 v[16:19], v28 offset:256\n\tds_read_b128 v[20:23], v28 offset:320\n\ts_waitcnt lgkmcnt(5)\n\t" \
  "v_mfma_f32_16x16x32_f16 v[52:55], v[132:135], v[0:3], 0\n\tds_read_b64 v[152:153], v26 offset:832\n\tds_read_b64 v[154:155], v26 offset:840\n\t" \
  "v_mfma_f32_16x16x32_f16 v[56:59], v[100:103], v[0:3], 0\n\tds_read_b64 v[120:121], v26 offset:320\n\tds_read_b64 v[122:123], v26 offset:328\n\t" \
  "ds_read_b128 v[0:3], v28 offset:384\n\ts_waitcnt lgkmcnt(9)\n\tv_mfma_f32_16x16x32_f16 v[52:55], v[136:139], v[4:7], v[52:55]\n\t" \
  "ds_read_b64 v[156:157], v26 offset:896\n\tds_read_b64 v[158:159], v26 offset:904\n\tv_mfma_f32_16x16x32_f16 v[56:59], v[104:107], v[4:7], v[56:59]\n\t" \
  "ds_read_b64 v[124:125], v26 offset:384\n\tds_read_b64 v[126:127], v26 offset:392\n\tds_read_b128 v[4:7], v28 offset:448\n\t" \
  "s_waitcnt lgkmcnt(13)\n\tv_mfma_f32_16x16x32_f16 v[52:55], v[140:143], v[8:11], v[52:55]\n\tds_read_b64 v[160:161], v26 offset:960\n\t" \
  "ds_read_b64 v[162:163], v26 offset:968\n\tv_mfma_f32_16x16x32_f16 v[56:59], v[108:111], v[8:11], v[56:59]\n\tds_read_b64 v[128:129], v26 offset:448\n\t" \
  "ds_read_b64 v[130:131], v26 offset:456\n\tds_read_b128 v[8:11], v28 offset:512\n\ts_waitcnt lgkmcnt(15)\n\t" \
  "v_mfma_f32_16x16x32_f16 v[52:55], v[144:147], v[12:15], v[52:55]\n\tds_read_b64 v[164:165], v26 offset:1024\n\tds_read_b64 v[166:167], v26 offset:1032\n\t" \
  "v_mfma_f32_16x16x32_f16 v[56:59], v[112:115], v[12:15], v[56:59]\n\tds_read_b64 v[76:77], v31 offset:72\n\tds_read_b64 v[78:79], v31 offset:176\n\t" \
  "ds_read_b128 v[12:15], v28 offset:576\n\tv_mfma_f32_16x16x32_f16 v[52:55], v[148:151], v[16:19], v[52:55]\n\tds_read_b64 v[80:81], v31 offset:40\n\t" \
  "ds_read_b64 v[82:83], v31 offset:144\n\tv_mfma_f32_16x16x32_f16 v[56:59], v[116:119], v[16:19], v[56:59]\n\tds_read_b64 v[84:85], v31 offset:8\n\t" \
  "ds_read_b64 v[86:87], v31 offset:112\n\tds_read_b128 v[16:19], v28 offset:640\n\ts_waitcnt lgkmcnt(15)\n\t" \
  "v_mfma_f32_16x16x32_f16 v[52:55], v[152:155], v[20:23], v[52:55]\n\tds_read_b64 v[88:89], v31 offset:280\n\tds_read_b64 v[90:91], v31 offset:384\n\t" \
  "v_mfma_f32_16x16x32_f16 v[56:59], v[120:123], v[20:23], v[56:59]\n\tds_read_b64 v[92:93], v31 offset:248\n\tds_read_b64 v[94:95], v31 offset:352\n\t" \
  "ds_read_b128 v[20:23], v28 offset:704\n\tv_mfma_f32_16x16x32_f16 v[52:55], v[156:159], v[0:3], v[52:55]\n\tds_read_b64 v[96:97], v31 offset:216\n\t" \
  "ds_read_b64 v[98:99], v31 offset:320\n\tv_mfma_f32_16x16x32_f16 v[56:59], v[124:127], v[0:3], v[56:59]\n\tds_read_b128 v[0:3], v28 offset:768\n\t" \
  "s_waitcnt lgkmcnt(15)\n\tv_mfma_f32_16x16x32_f16 v[52:55], v[160:163], v[4:7], v[52:55]\n\tv_mfma_f32_16x16x32_f16 v[56:59], v[128:131], v[4:7], v[56:59]\n\t" \
  "ds_read_b128 v[4:7], v28 offset:832\n\tv_mfma_f32_16x16x32_f16 v[52:55], v[164:167], v[8:11], v[52:55]\n\tv_mfma_f32_16x16x32_f16 v[56:59], v[132:135], v[8:11], v[56:59]\n\t" \
  "v_mfma_f32_16x16x32_f16 v[60:63], v[100:103], v[8:11], 0\n\tds_read_b128 v[8:11], v28 offset:896\n\ts_waitcnt lgkmcnt(15)\n\t" \
  "v_mfma_f32_16x16x32_f16 v[56:59], v[136:139], v[12:15], v[56:59]\n\tds_read_b32 v34, v30\n\tv_mfma_f32_16x16x32_f16 v[60:63], v[104:107], v[12:15], v[60:63]\n\t" \
  "ds_read_b128 v[12:15], v28 offset:960\n\tv_cvt_pk_bf16_f32 v52, v52, v53\n\tv_cvt_pk_bf16_f32 v53, v54, v55\n\t" \
  TS(64, 65) "s_waitcnt lgkmcnt(12)\n\tv_mfma_f32_16x16x32_f16 v[56:59], v[140:143], v[16:19], v[56:59]\n\t" \
  "ds_write_b64 v29, v[52:53] offset:0\n\tds_write_b32 v25, v25 offset:0\n\tv_mfma_f32_16x16x32_f16 v[60:63], v[108:111], v[16:19], v[60:63]\n\t" \
  "s_waitcnt lgkmcnt(3)\n\tv_cmp_gt_u32 vcc, %[n1], v34\n\ts_cbranch_vccz 2f\n\t" \
  "s_mov_b32 s6, 0x10000\n\t1:\n\ts_sleep 1\n\t" \
  "ds_read_b32 v34, v30\n\ts_sub_u32 s6, s6, 1\n\ts_waitcnt lgkmcnt(0)\n\t" \
  "v_cmp_gt_u32 vcc, %[n1], v34\n\ts_cbranch_scc1 2f\n\ts_cbranch_vccnz 1b\n\t" \
  "2:\n\tds_read_b128 v[16:19], v28 offset:1024\n\tv_mfma_f32_16x16x32_f16 v[56:59], v[144:147], v[20:23], v[56:59]\n\t" \
  "v_mfma_f32_16x16x32_f16 v[60:63], v[112:115], v[20:23], v[60:63]\n\tds_read_b128 v[68:71], v32\n\tds_read_b128 v[72:75], v32 offset:1024\n\t" \
  "ds_read_b128 v[20:23], v28 offset:1088\n\tv_mfma_f32_16x16x32_f16 v[56:59], v[148:151], v[0:3], v[56:59]\n\tv_mfma_f32_16x16x32_f16 v[60:63], v[116:119], v[0:3], v[60:63]\n\t" \
  "ds_read_b128 v[0:3], v28 offset:496\n\tv_mfma_f32_16x16x32_f16 v[56:59], v[152:155], v[4:7], v[56:59]\n\tv_mfma_f32_16x16x32_f16 v[60:63], v[120:123], v[4:7], v[60:63]\n\t" \
  "ds_read_b128 v[4:7], v28 offset:1152\n\tv_mfma_f32_16x16x32_f16 v[56:59], v[156:159], v[8:11], v[56:59]\n\tv_mfma_f32_16x16x32_f16 v[60:63], v[124:127], v[8:11], v[60:63]\n\t" \
  "ds_read_b128 v[8:11], v28 offset:1216\n\ts_waitcnt lgkmcnt(9)\n\tv_mfma_f32_16x16x32_f16 v[56:59], v[160:163], v[12:15], v[56:59]\n\t" \
  "v_mfma_f32_16x16x32_f16 v[60:63], v[128:131], v[12:15], v[60:63]\n\tds_read_b128 v[12:15], v28 offset:480\n\ts_waitcnt lgkmcnt(7)\n\t" \
  "v_mfma_f32_16x16x32_f16 v[56:59], v[164:167], v[16:19], v[56:59]\n\tv_mfma_f32_16x16x32_f16 v[60:63], v[132:135], v[16:19], v[60:63]\n\tv_mfma_f32_16x16x32_f16 v[64:67], v[100:103], v[16:19], 0\n\t" \
  "ds_read_b128 v[16:19], v28 offset:1280\n\ts_waitcnt lgkmcnt(5)\n\tv_mfma_f32_16x16x32_f16 v[60:63], v[136:139], v[20:23], v[60:63]\n\t" \
  "v_mfma_f32_16x16x32_f16 v[64:67], v[104:107], v[20:23], v[64:67]\n\tds_read_b128 v[20:23], v28 offset:1344\n\ts_waitcnt lgkmcnt(5)\n\t" \
  "v_mfma_f32_16x16x32_f16 v[36:39], v[0:3], v[68:71], 0\n\tv_cvt_pk_bf16_f32 v56, v56, v57\n\tv_cvt_pk_bf16_f32 v57, v58, v59\n\t" \
  "v_mfma_f32_16x16x32_f16 v[40:43], v[0:3], v[72:75], 0\n\tds_write_b64 v29, v[56:57] offset:512\n\tds_write_b32 v25, v25 offset:64\n\t" \
  "ds_read_b128 v[0:3], v28 offset:1408\n\ts_waitcnt lgkmcnt(7)\n\tv_mfma_f32_16x16x32_f16 v[60:63], v[140:143], v[4:7], v[60:63]\n\t" \
  "v_cvt_pk_f16_f32 v36, v36, v37\n\tv_cvt_pk_f16_f32 v37, v38, v39\n\tv_mfma_f32_16x16x32_f16 v[64:67], v[108:111], v[4:7], v[64:67]\n\t" \
  "ds_read_b128 v[4:7], v28 offset:1472\n\tv_cvt_pk_f16_f32 v38, v40, v41\n\tv_cvt_pk_f16_f32 v39, v42, v43\n\t" \
  "s_waitcnt lgkmcnt(7)\n\tv_mfma_f32_16x16x32_f16 v[60:63], v[144:147], v[8:11], v[60:63]\n\tv_mfma_f32_16x16x32_f16 v[64:67], v[112:115], v[8:11], v[64:67]\n\t" \
  "ds_read_b128 v[8:11], v28 offset:1536\n\tv_mfma_f32_16x16x32_f16 v[60:63], v[36:39], v[76:79], v[60:63]\n\tv_mfma_f32_16x16x32_f16 v[64:67], v[36:39], v[80:83], v[64:67]\n\t" \
  "v_mfma_f32_16x16x32_f16 v[52:55], v[36:39], v[84:87], 0\n\ts_waitcnt lgkmcnt(7)\n\tv_mfma_f32_16x16x32_f16 v[44:47], v[12:15], v[68:71], 0\n\t" \
  "v_mfma_f32_16x16x32_f16 v[40:43], v[12:15], v[72:75], 0\n\tds_read_b128 v[12:15], v28 offset:1600\n\ts_waitcnt lgkmcnt(7)\n\t" \
  "v_mfma_f32_16x16x32_f16 v[60:63], v[148:151], v[16:19], v[60:63]\n\tv_mfma_f32_16x16x32_f16 v[64:67], v[116:119], v[16:19], v[64:67]\n\tds_read_b128 v[16:19], v28 offset:1008\n\t" \
  "s_waitcnt lgkmcnt(7)\n\tv_mfma_f32_16x16x32_f16 v[60:63], v[152:155], v[20:23], v[60:63]\n\tv_cvt_pk_f16_f32 v44, v44, v45\n\t" \
  "v_cvt_pk_f16_f32 v45, v46, v47\n\tv_cvt_pk_f16_f32 v46, v40, v41\n\tv_cvt_pk_f16_f32 v47, v42, v43\n\t" \
  "v_mfma_f32_16x16x32_f16 v[64:67], v[120:123], v[20:23], v[64:67]\n\tds_read_b128 v[20:23], v28 offset:1664\n\tv_mfma_f32_16x16x32_f16 v[60:63], v[44:47], v[88:91], v[60:63]\n\t" \
  "v_mfma_f32_16x16x32_f16 v[64:67], v[44:47], v[92:95], v[64:67]\n\tv_mfma_f32_16x16x32_f16 v[52:55], v[44:47], v[96:99], v[52:55]\n\ts_waitcnt lgkmcnt(5)\n\t" \
  "v_mfma_f32_16x16x32_f16 v[60:63], v[156:159], v[0:3], v[60:63]\n\tv_mfma_f32_16x16x32_f16 v[64:67], v[124:127], v[0:3], v[64:67]\n\tds_read_b128 v[0:3], v28 offset:1728\n\t" \
  "s_waitcnt lgkmcnt(5)\n\tv_mfma_f32_16x16x32_f16 v[60:63], v[160:163], v[4:7], v[60:63]\n\tv_mfma_f32_16x16x32_f16 v[64:67], v[128:131], v[4:7], v[64:67]\n\t" \
  "ds_read_b128 v[4:7], v28 offset:992\n\ts_waitcnt lgkmcnt(5)\n\tv_mfma_f32_16x16x32_f16 v[60:63], v[164:167], v[8:11], v[60:63]\n\t" \
  "v_mfma_f32_16x16x32_f16 v[64:67], v[132:135], v[8:11], v[64:67]\n\tv_mfma_f32_16x16x32_f16 v[52:55], v[100:103], v[8:11], v[52:55]\n\tds_read_b128 v[8:11], v28 offset:1792\n\t" \
  "s_waitcnt lgkmcnt(5)\n\tv_mfma_f32_16x16x32_f16 v[64:67], v[136:139], v[12:15], v[64:67]\n\tv_mfma_f32_16x16x32_f16 v[52:55], v[104:107], v[12:15], v[52:55]\n\t" \
  "ds_read_b128 v[12:15], v28 offset:1856\n\ts_waitcnt lgkmcnt(5)\n\tv_mfma_f32_16x16x32_f16 v[36:39], v[16:19], v[68:71], 0\n\t" \
  "v_cvt_pk_bf16_f32 v60, v60, v61\n\tv_cvt_pk_bf16_f32 v61, v62, v63\n\tv_mfma_f32_16x16x32_f16 v[40:43], v[16:19], v[72:75], 0\n\t" \
  "ds_write_b64 v29, v[60:61] offset:1024\n\tds_write_b32 v25, v25 offset:128\n\tds_read_b128 v[16:19], v28 offset:1920\n\t" \
  "s_waitcnt lgkmcnt(7)\n\tv_mfma_f32_16x16x32_f16 v[64:67], v[140:143], v[20:23], v[64:67]\n\tds_read_b32 v34, v30\n\t" \
  "v_cvt_pk_f16_f32 v36, v36, v37\n\tv_cvt_pk_f16_f32 v37, v38, v39\n\tv_mfma_f32_16x16x32_f16 v[52:55], v[108:111], v[20:23], v[52:55]\n\t" \
  "ds_read_b128 v[20:23], v28 offset:1984\n\tv_cvt_pk_f16_f32 v38, v40, v41\n\tv_cvt_pk_f16_f32 v39, v42, v43\n\t" \
  "s_waitcnt lgkmcnt(8)\n\tv_mfma_f32_16x16x32_f16 v[64:67], v[144:147], v[0:3], v[64:67]\n\tv_mfma_f32_16x16x32_f16 v[52:55], v[112:115], v[0:3], v[52:55]\n\t" \
  "s_waitcnt lgkmcnt(1)\n\tv_cmp_gt_u32 vcc, %[n2], v34\n\ts_cbranch_vccz 2f\n\t" \
  "s_mov_b32 s6, 0x10000\n\t1:\n\ts_sleep 1\n\t" \
  "ds_read_b32 v34, v30\n\ts_sub_u32 s6, s6, 1\n\ts_waitcnt lgkmcnt(0)\n\t" \
  "v_cmp_gt_u32 vcc, %[n2], v34\n\ts_cbranch_scc1 2f\n\ts_cbranch_vccnz 1b\n\t" \
  "2:\n\tds_read_b128 v[0:3], v28 offset:2048\n\tv_mfma_f32_16x16x32_f16 v[64:67], v[36:39], v[76:79], v[64:67]\n\t" \
  "v_mfma_f32_16x16x32_f16 v[52:55], v[36:39], v[80:83], v[52:55]\n\tv_mfma_f32_16x16x32_f16 v[56:59], v[36:39], v[84:87], 0\n\tv_mfma_f32_16x16x32_f16 v[44:47], v[4:7], v[68:71], 0\n\t" \
  "v_mfma_f32_16x16x32_f16 v[40:43], v[4:7], v[72:75], 0\n\tds_read_b128 v[4:7], v28 offset:2112\n\tv_mfma_f32_16x16x32_f16 v[64:67], v[148:151], v[8:11], v[64:67]\n\t" \
  "v_mfma_f32_16x16x32_f16 v[52:55], v[116:119], v[8:11], v[52:55]\n\tds_read_b128 v[8:11], v28 offset:1520\n\tv_mfma_f32_16x16x32_f16 v[64:67], v[152:155], v[12:15], v[64:67]\n\t" \
  "v_mfma_f32_16x16x32_f16 v[52:55], v[120:123], v[12:15], v[52:55]\n\tds_read_b128 v[12:15], v28 offset:2176\n\tv_cvt_pk_f16_f32 v44, v44, v45\n\t" \
  "v_cvt_pk_f16_f32 v45, v46, v47\n\tv_cvt_pk_f16_f32 v46, v40, v41\n\tv_cvt_pk_f16_f32 v47, v42, v43\n\t" \
  "s_nop 1\n\tv_mfma_f32_16x16x32_f16 v[64:67], v[44:47], v[88:91], v[64:67]\n\tv_mfma_f32_16x16x32_f16 v[52:55], v[44:47], v[92:95], v[52:55]\n\t" \
  "v_mfma_f32_16x16x32_f16 v[56:59], v[44:47], v[96:99], v[56:59]\n\tv_mfma_f32_16x16x32_f16 v[64:67], v[156:159], v[16:19], v[64:67]\n\tv_mfma_f32_16x16x32_f16 v[52:55], v[124:127], v[16:19], v[52:55]\n\t" \
  "ds_read_b128 v[16:19], v28 offset:2240\n\ts_waitcnt lgkmcnt(5)\n\tv_mfma_f32_16x16x32_f16 v[64:67], v[160:163], v[20:23], v[64:67]\n\t" \
  "v_mfma_f32_16x16x32_f16 v[52:55], v[128:131], v[20:23], v[52:55]\n\tds_read_b128 v[20:23], v28 offset:1504\n\ts_waitcnt lgkmcnt(5)\n\t" \
  "v_mfma_f32_16x16x32_f16 v[64:67], v[164:167], v[0:3], v[64:67]\n\tv_mfma_f32_16x16x32_f16 v[52:55], v[132:135], v[0:3], v[52:55]\n\tv_mfma_f32_16x16x32_f16 v[56:59], v[100:103], v[0:3], v[56:59]\n\t" \
  "ds_read_b128 v[0:3], v28 offset:2304\n\ts_waitcnt lgkmcnt(5)\n\tv_mfma_f32_16x16x32_f16 v[52:55], v[136:139], v[4:7], v[52:55]\n\t" \
  "v_mfma_f32_16x16x32_f16 v[56:59], v[104:107], v[4:7], v[56:59]\n\tds_read_b128 v[4:7], v28 offset:2368\n\ts_waitcnt lgkmcnt(5)\n\t" \
  "v_mfma_f32_16x16x32_f16 v[36:39], v[8:11], v[68:71], 0\n\tv_cvt_pk_bf16_f32 v64, v64, v65\n\tv_cvt_pk_bf16_f32 v65, v66, v67\n\t" \
  TS(66, 67) "s_cmp_lt_u32 %[w], 4\n\ts_cbranch_scc1 1f\n\t" \
  "s_setprio 1\n\t1:\n\tv_mfma_f32_16x16x32_f16 v[40:43], v[8:11], v[72:75], 0\n\t" \
  "ds_write_b64 v29, v[64:65] offset:1536\n\tds_write_b32 v25, v25 offset:192\n\tds_read_b128 v[8:11], v28 offset:480\n\t" \
  "s_waitcnt lgkmcnt(7)\n\tv_mfma_f32_16x16x32_f16 v[52:55], v[140:143], v[12:15], v[52:55]\n\tv_cvt_pk_f16_f32 v36, v36, v37\n\t" \
  "v_cvt_pk_f16_f32 v37, v38, v39\n\tv_mfma_f32_16x16x32_f16 v[56:59], v[108:111], v[12:15], v[56:59]\n\tds_read_b128 v[12:15], v32 offset:3072\n\t" \
  "v_cvt_pk_f16_f32 v38, v40, v41\n\tv_cvt_pk_f16_f32 v39, v42, v43\n\ts_waitcnt lgkmcnt(7)\n\t" \
  "v_mfma_f32_16x16x32_f16 v[52:55], v[144:147], v[16:19], v[52:55]\n\tv_mfma_f32_16x16x32_f16 v[56:59], v[112:115], v[16:19], v[56:59]\n\tds_read_b128 v[16:19], v28 offset:496\n\t" \
  "v_mfma_f32_16x16x32_f16 v[52:55], v[36:39], v[76:79], v[52:55]\n\tv_mfma_f32_16x16x32_f16 v[56:59], v[36:39], v[80:83], v[56:59]\n\tv_mfma_f32_16x16x32_f16 v[60:63], v[36:39], v[84:87], 0\n\t" \
  "s_waitcnt lgkmcnt(7)\n\tv_mfma_f32_16x16x32_f16 v[44:47], v[20:23], v[68:71], 0\n\tv_mfma_f32_16x16x32_f16 v[40:43], v[20:23], v[72:75], 0\n\t" \
  "ds_read_b128 v[20:23], v32 offset:2048\n\ts_waitcnt lgkmcnt(7)\n\tv_mfma_f32_16x16x32_f16 v[52:55], v[148:151], v[0:3], v[52:55]\n\t" \
  "v_mfma_f32_16x16x32_f16 v[56:59], v[116:119], v[0:3], v[56:59]\n\tds_read_b128 v[0:3], v28 offset:2432\n\ts_waitcnt lgkmcnt(7)\n\t" \
  "v_mfma_f32_16x16x32_f16 v[52:55], v[152:155], v[4:7], v[52:55]\n\tv_cvt_pk_f16_f32 v44, v44, v45\n\tv_cvt_pk_f16_f32 v45, v46, v47\n\t" \
  "v_cvt_pk_f16_f32 v46, v40, v41\n\tv_cvt_pk_f16_f32 v47, v42, v43\n\tv_mfma_f32_16x16x32_f16 v[56:59], v[120:123], v[4:7], v[56:59]\n\t" \
  "ds_read_b128 v[4:7], v28 offset:2496\n\tv_mfma_f32_16x16x32_f16 v[52:55], v[44:47], v[88:91], v[52:55]\n\tv_mfma_f32_16x16x32_f16 v[56:59], v[44:47], v[92:95], v[56:59]\n\t" \
  "v_mfma_f32_16x16x32_f16 v[60:63], v[44:47], v[96:99], v[60:63]\n\ts_waitcnt lgkmcnt(4)\n\tv_mfma_f32_16x16x32_f16 v[40:43], v[8:11], v[12:15], 0\n\t" \
  "ds_read_b64 v[8:9], v31 offset:544\n\tds_read_b64 v[10:11], v31 offset:712\n\tds_read_b64 v[12:13], v31 offset:512\n\t" \
  "ds_read_b64 v[14:15], v31 offset:680\n\ts_waitcnt lgkmcnt(7)\n\tv_mfma_f32_16x16x32_f16 v[52:55], v[48:51], v[16:19], v[52:55]\n\t" \
  "s_waitcnt lgkmcnt(6)\n\tv_mfma_f32_16x16x32_f16 v[36:39], v[16:19], v[20:23], 0\n\tds_read_b64 v[16:17], v31 offset:480\n\t" \
  "ds_read_b64 v[18:19], v31 offset:648\n\tds_read_b64 v[20:21], v31 offset:448\n\tds_read_b64 v[22:23], v31 offset:616\n\t" \
  "s_waitcnt lgkmcnt(9)\n\tv_mfma_f32_16x16x32_f16 v[52:55], v[156:159], v[0:3], v[52:55]\n\tv_mfma_f32_16x16x32_f16 v[56:59], v[124:127], v[0:3], v[56:59]\n\t" \
  "ds_read_b128 v[0:3], v28 offset:2560\n\tv_cvt_pk_f16_f32 v36, v36, v37\n\tv_cvt_pk_f16_f32 v37, v38, v39\n\t" \
  "s_waitcnt lgkmcnt(9)\n\tv_mfma_f32_16x16x32_f16 v[52:55], v[160:163], v[4:7], v[52:55]\n\tv_cvt_pk_f16_f32 v38, v40, v41\n\t" \
  "v_cvt_pk_f16_f32 v39, v42, v43\n\tv_mfma_f32_16x16x32_f16 v[56:59], v[128:131], v[4:7], v[56:59]\n\tds_read_b128 v[4:7], v28 offset:2624\n\t" \
  "s_waitcnt lgkmcnt(8)\n\tv_mfma_f32_16x16x32_f16 v[52:55], v[36:39], v[8:11], v[52:55]\n\tds_read_b128 v[8:11], v28 offset:2032\n\t" \
  "s_waitcnt lgkmcnt(7)\n\tv_mfma_f32_16x16x32_f16 v[56:59], v[36:39], v[12:15], v[56:59]\n\tds_read_b128 v[12:15], v28 offset:2688\n\t" \
  "s_waitcnt lgkmcnt(6)\n\tv_mfma_f32_16x16x32_f16 v[60:63], v[36:39], v[16:19], v[60:63]\n\tds_read_b128 v[16:19], v28 offset:2752\n\t" \
  "s_waitcnt lgkmcnt(5)\n\tv_mfma_f32_16x16x32_f16 v[64:67], v[36:39], v[20:23], 0\n\tds_read_b128 v[20:23], v28 offset:2016\n\t" \
  "s_waitcnt lgkmcnt(5)\n\tv_mfma_f32_16x16x32_f16 v[52:55], v[164:167], v[0:3], v[52:55]\n\tv_mfma_f32_16x16x32_f16 v[56:59], v[132:135], v[0:3], v[56:59]\n\t" \
  "v_mfma_f32_16x16x32_f16 v[60:63], v[100:103], v[0:3], v[60:63]\n\tds_read_b128 v[0:3], v28 offset:2816\n\ts_waitcnt lgkmcnt(5)\n\t" \
  "v_mfma_f32_16x16x32_f16 v[56:59], v[136:139], v[4:7], v[56:59]\n\tv_mfma_f32_16x16x32_f16 v[60:63], v[104:107], v[4:7], v[60:63]\n\tds_read_b128 v[4:7], v28 offset:2880\n\t" \
  "s_waitcnt lgkmcnt(5)\n\tv_mfma_f32_16x16x32_f16 v[44:47], v[8:11], v[68:71], 0\n\tv_cvt_pk_bf16_f32 v52, v52, v53\n\t" \
  "v_cvt_pk_bf16_f32 v53, v54, v55\n\ts_cmp_lt_u32 %[w], 4\n\ts_cbranch_scc0 1f\n\t" \
  "s_setprio 0\n\t1:\n\tv_mfma_f32_16x16x32_f16 v[40:43], v[8:11], v[72:75], 0\n\t" \
  "ds_write_b64 v29, v[52:53] offset:2048\n\tds_write_b32 v25, v25 offset:256\n\tds_read_b128 v[8:11], v28 offset:992\n\t" \
  "s_waitcnt lgkmcnt(7)\n\tv_mfma_f32_16x16x32_f16 v[56:59], v[140:143], v[12:15], v[56:59]\n\tv_cvt_pk_f16_f32 v44, v44, v45\n\t" \
  "v_cvt_pk_f16_f32 v45, v46, v47\n\tv_mfma_f32_16x16x32_f16 v[60:63], v[108:111], v[12:15], v[60:63]\n\tds_read_b128 v[12:15], v32 offset:3072\n\t" \
  "v_cvt_pk_f16_f32 v46, v40, v41\n\tv_cvt_pk_f16_f32 v47, v42, v43\n\ts_waitcnt lgkmcnt(7)\n\t" \
  "v_mfma_f32_16x16x32_f16 v[56:59], v[144:147], v[16:19], v[56:59]\n\tv_mfma_f32_16x16x32_f16 v[60:63], v[112:115], v[16:19], v[60:63]\n\tds_read_b128 v[16:19], v28 offset:1008\n\t" \
  "v_mfma_f32_16x16x32_f16 v[56:59], v[44:47], v[76:79], v[56:59]\n\tv_mfma_f32_16x16x32_f16 v[60:63], v[44:47], v[80:83], v[60:63]\n\tv_mfma_f32_16x16x32_f16 v[64:67], v[44:47], v[84:87], v[64:67]\n\t" \
  "s_waitcnt lgkmcnt(7)\n\tv_mfma_f32_16x16x32_f16 v[36:39], v[20:23], v[68:71], 0\n\tv_mfma_f32_16x16x32_f16 v[40:43], v[20:23], v[72:75], 0\n\t" \
  "ds_read_b128 v[20:23], v32 offset:2048\n\ts_waitcnt lgkmcnt(7)\n\tv_mfma_f32_16x16x32_f16 v[56:59], v[148:151], v[0:3], v[56:59]\n\t" \
  "v_mfma_f32_16x16x32_f16 v[60:63], v[116:119], v[0:3], v[60:63]\n\tds_read_b128 v[0:3], v28 offset:2944\n\ts_waitcnt lgkmcnt(7)\n\t" \
  "v_mfma_f32_16x16x32_f16 v[56:59], v[152:155], v[4:7], v[56:59]\n\tv_cvt_pk_f16_f32 v36, v36, v37\n\tv_cvt_pk_f16_f32 v37, v38, v39\n\t" \
  "v_cvt_pk_f16_f32 v38, v40, v41\n\tv_cvt_pk_f16_f32 v39, v42, v43\n\tv_mfma_f32_16x16x32_f16 v[60:63], v[120:123], v[4:7], v[60:63]\n\t" \
  "ds_read_b128 v[4:7], v28 offset:3008\n\tv_mfma_f32_16x16x32_f16 v[56:59], v[36:39], v[88:91], v[56:59]\n\tv_mfma_f32_16x16x32_f16 v[60:63], v[36:39], v[92:95], v[60:63]\n\t" \
  "v_mfma_f32_16x16x32_f16 v[64:67], v[36:39], v[96:99], v[64:67]\n\tds_read_b32 v34, v30\n\ts_waitcnt lgkmcnt(5)\n\t" \
  "v_mfma_f32_16x16x32_f16 v[40:43], v[8:11], v[12:15], 0\n\tds_read_b64 v[8:9], v31 offset:544\n\tds_read_b64 v[10:11], v31 offset:712\n\t" \
  "ds_read_b64 v[12:13], v31 offset:512\n\tds_read_b64 v[14:15], v31 offset:680\n\ts_waitcnt lgkmcnt(8)\n\t" \
  "v_mfma_f32_16x16x32_f16 v[56:59], v[48:51], v[16:19], v[56:59]\n\ts_waitcnt lgkmcnt(7)\n\tv_mfma_f32_16x16x32_f16 v[44:47], v[16:19], v[20:23], 0\n\t" \
  "ds_read_b64 v[16:17], v31 offset:480\n\tds_read_b64 v[18:19], v31 offset:648\n\ts_waitcnt lgkmcnt(6)\n\t" \
  "v_cmp_gt_u32 vcc, %[n3], v34\n\ts_cbranch_vccz 2f\n\ts_mov_b32 s6, 0x10000\n\t" \
  "1:\n\ts_sleep 1\n\tds_read_b32 v34, v30\n\t" \
  "s_sub_u32 s6, s6, 1\n\ts_waitcnt lgkmcnt(0)\n\tv_cmp_gt_u32 vcc, %[n3], v34\n\t" \
  "s_cbranch_scc1 2f\n\ts_cbranch_vccnz 1b\n\t2:\n\t" \
  "ds_read_b128 v[20:23], v28 offset:3072\n\tv_mfma_f32_16x16x32_f16 v[56:59], v[156:159], v[0:3], v[56:59]\n\tv_mfma_f32_16x16x32_f16 v[60:63], v[124:127], v[0:3], v[60:63]\n\t" \
  "ds_read_b128 v[0:3], v28 offset:3136\n\tv_cvt_pk_f16_f32 v44, v44, v45\n\tv_cvt_pk_f16_f32 v45, v46, v47\n\t" \
  "v_mfma_f32_16x16x32_f16 v[56:59], v[160:163], v[4:7], v[56:59]\n\tv_cvt_pk_f16_f32 v46, v40, v41\n\tv_cvt_pk_f16_f32 v47, v42, v43\n\t" \
  "v_mfma_f32_16x16x32_f16 v[60:63], v[128:131], v[4:7], v[60:63]\n\tds_read_b128 v[4:7], v28 offset:2544\n\ts_waitcnt lgkmcnt(7)\n\t" \
  "v_mfma_f32_16x16x32_f16 v[56:59], v[44:47], v[8:11], v[56:59]\n\tds_read_b128 v[8:11], v28 offset:3200\n\ts_waitcnt lgkmcnt(6)\n\t" \
  "v_mfma_f32_16x16x32_f16 v[60:63], v[44:47], v[12:15], v[60:63]\n\tds_read_b128 v[12:15], v28 offset:3264\n\ts_waitcnt lgkmcnt(5)\n\t" \
  "v_mfma_f32_16x16x32_f16 v[64:67], v[44:47], v[16:19], v[64:67]\n\tds_read_b128 v[16:19], v28 offset:2528\n\ts_waitcnt lgkmcnt(5)\n\t" \
  "v_mfma_f32_16x16x32_f16 v[56:59], v[164:167], v[20:23], v[56:59]\n\tv_mfma_f32_16x16x32_f16 v[60:63], v[132:135], v[20:23], v[60:63]\n\tv_mfma_f32_16x16x32_f16 v[64:67], v[100:103], v[20:23], v[64:67]\n\t" \
  "ds_read_b128 v[20:23], v28 offset:3328\n\ts_waitcnt lgkmcnt(5)\n\tv_mfma_f32_16x16x32_f16 v[60:63], v[136:139], v[0:3], v[60:63]\n\t" \
  "v_mfma_f32_16x16x32_f16 v[64:67], v[104:107], v[0:3], v[64:67]\n\tds_read_b128 v[0:3], v28 offset:3392\n\ts_waitcnt lgkmcnt(5)\n\t" \
  "v_mfma_f32_16x16x32_f16 v[36:39], v[4:7], v[68:71], 0\n\tv_cvt_pk_bf16_f32 v56, v56, v57\n\tv_cvt_pk_bf16_f32 v57, v58, v59\n\t" \
  "s_cmp_lt_u32 %[w], 4\n\ts_cbranch_scc1 1f\n\ts_setprio 0\n\t" \
  "1:\n\tv_mfma_f32_16x16x32_f16 v[40:43], v[4:7], v[72:75], 0\n\tds_write_b64 v29, v[56:57] offset:2560\n\t" \
  "ds_write_b32 v25, v25 offset:320\n\tds_read_b128 v[4:7], v28 offset:1504\n\ts_waitcnt lgkmcnt(7)\n\t" \
  "v_mfma_f32_16x16x32_f16 v[60:63], v[140:143], v[8:11], v[60:63]\n\tv_cvt_pk_f16_f32 v36, v36, v37\n\tv_cvt_pk_f16_f32 v37, v38, v39\n\t" \
  "v_mfma_f32_16x16x32_f16 v[64:67], v[108:111], v[8:11], v[64:67]\n\tds_read_b128 v[8:11], v32 offset:3072\n\tv_cvt_pk_f16_f32 v38, v40, v41\n\t" \
  "v_cvt_pk_f16_f32 v39, v42, v43\n\ts_waitcnt lgkmcnt(7)\n\tv_mfma_f32_16x16x32_f16 v[60:63], v[144:147], v[12:15], v[60:63]\n\t" \
  "v_mfma_f32_16x16x32_f16 v[64:67], v[112:115], v[12:15], v[64:67]\n\tds_read_b128 v[12:15], v28 offset:1520\n\tv_mfma_f32_16x16x32_f16 v[60:63], v[36:39], v[76:79], v[60:63]\n\t" \
  "v_mfma_f32_16x16x32_f16 v[64:67], v[36:39], v[80:83], v[64:67]\n\ts_waitcnt lgkmcnt(7)\n\tv_mfma_f32_16x16x32_f16 v[44:47], v[16:19], v[68:71], 0\n\t" \
  "v_mfma_f32_16x16x32_f16 v[40:43], v[16:19], v[72:75], 0\n\tds_read_b128 v[16:19], v32 offset:2048\n\ts_waitcnt lgkmcnt(7)\n\t" \
  "v_mfma_f32_16x16x32_f16 v[60:63], v[148:151], v[20:23], v[60:63]\n\tv_mfma_f32_16x16x32_f16 v[64:67], v[116:119], v[20:23], v[64:67]\n\tds_read_b128 v[20:23], v28 offset:3456\n\t" \
  "s_waitcnt lgkmcnt(7)\n\tv_mfma_f32_16x16x32_f16 v[60:63], v[152:155], v[0:3], v[60:63]\n\tv_cvt_pk_f16_f32 v44, v44, v45\n\t" \
  "v_cvt_pk_f16_f32 v45, v46, v47\n\tv_cvt_pk_f16_f32 v46, v40, v41\n\tv_cvt_pk_f16_f32 v47, v42, v43\n\t" \
  "v_mfma_f32_16x16x32_f16 v[64:67], v[120:123], v[0:3], v[64:67]\n\tds_read_b128 v[0:3], v28 offset:3520\n\tv_mfma_f32_16x16x32_f16 v[60:63], v[44:47], v[88:91], v[60:63]\n\t" \
  "v_mfma_f32_16x16x32_f16 v[64:67], v[44:47], v[92:95], v[64:67]\n\ts_waitcnt lgkmcnt(4)\n\tv_mfma_f32_16x16x32_f16 v[40:43], v[4:7], v[8:11], 0\n\t" \
  "ds_read_b64 v[4:5], v31 offset:544\n\tds_read_b64 v[6:7], v31 offset:712\n\tds_read_b64 v[8:9], v31 offset:512\n\t" \
  "ds_read_b64 v[10:11], v31 offset:680\n\ts_waitcnt lgkmcnt(7)\n\tv_mfma_f32_16x16x32_f16 v[60:63], v[48:51], v[12:15], v[60:63]\n\t" \
  "s_waitcnt lgkmcnt(6)\n\tv_mfma_f32_16x16x32_f16 v[36:39], v[12:15], v[16:19], 0\n\tds_read_b128 v[12:15], v28 offset:3584\n\t" \
  "ds_read_b128 v[16:19], v28 offset:3648\n\ts_waitcnt lgkmcnt(7)\n\tv_mfma_f32_16x16x32_f16 v[60:63], v[156:159], v[20:23], v[60:63]\n\t" \
  "v_mfma_f32_16x16x32_f16 v[64:67], v[124:127], v[20:23], v[64:67]\n\tds_read_b128 v[20:23], v28 offset:3056\n\ts_waitcnt lgkmcnt(7)\n\t" \
  "v_mfma_f32_16x16x32_f16 v[60:63], v[160:163], v[0:3], v[60:63]\n\tv_cvt_pk_f16_f32 v36, v36, v37\n\tv_cvt_pk_f16_f32 v37, v38, v39\n\t" \
  "v_cvt_pk_f16_f32 v38, v40, v41\n\tv_cvt_pk_f16_f32 v39, v42, v43\n\tv_mfma_f32_16x16x32_f16 v[64:67], v[128:131], v[0:3], v[64:67]\n\t" \
  "ds_read_b128 v[0:3], v28 offset:3712\n\ts_waitcnt lgkmcnt(6)\n\tv_mfma_f32_16x16x32_f16 v[60:63], v[36:39], v[4:7], v[60:63]\n\t" \
  "ds_read_b128 v[4:7], v28 offset:3776\n\ts_waitcnt lgkmcnt(5)\n\tv_mfma_f32_16x16x32_f16 v[64:67], v[36:39], v[8:11], v[64:67]\n\t" \
  "ds_read_b128 v[8:11], v28 offset:3040\n\ts_waitcnt lgkmcnt(5)\n\tv_mfma_f32_16x16x32_f16 v[60:63], v[164:167], v[12:15], v[60:63]\n\t" \
  "v_mfma_f32_16x16x32_f16 v[64:67], v[132:135], v[12:15], v[64:67]\n\tds_read_b128 v[12:15], v28 offset:3840\n\ts_waitcnt lgkmcnt(5)\n\t" \
  "v_mfma_f32_16x16x32_f16 v[64:67], v[136:139], v[16:19], v[64:67]\n\tds_read_b128 v[16:19], v28 offset:3904\n\ts_waitcnt lgkmcnt(5)\n\t" \
  "v_mfma_f32_16x16x32_f16 v[44:47], v[20:23], v[68:71], 0\n\tv_mfma_f32_16x16x32_f16 v[40:43], v[20:23], v[72:75], 0\n\tds_read_b128 v[20:23], v28 offset:2016\n\t" \
  "v_cvt_pk_bf16_f32 v60, v60, v61\n\tv_cvt_pk_bf16_f32 v61, v62, v63\n\ts_waitcnt lgkmcnt(5)\n\t" \
  "v_mfma_f32_16x16x32_f16 v[64:67], v[140:143], v[0:3], v[64:67]\n\tds_write_b64 v29, v[60:61] offset:3072\n\tds_write_b32 v25, v25 offset:384\n\t" \
  "ds_read_b128 v[0:3], v32 offset:3072\n\tv_cvt_pk_f16_f32 v44, v44, v45\n\tv_cvt_pk_f16_f32 v45, v46, v47\n\t" \
  "v_cvt_pk_f16_f32 v46, v40, v41\n\tv_cvt_pk_f16_f32 v47, v42, v43\n\ts_waitcnt lgkmcnt(7)\n\t" \
  "v_mfma_f32_16x16x32_f16 v[64:67], v[144:147], v[4:7], v[64:67]\n\tds_read_b128 v[4:7], v28 offset:2032\n\tv_mfma_f32_16x16x32_f16 v[64:67], v[44:47], v[76:79], v[64:67]\n\t" \
  "s_waitcnt lgkmcnt(7)\n\tv_mfma_f32_16x16x32_f16 v[36:39], v[8:11], v[68:71], 0\n\tv_mfma_f32_16x16x32_f16 v[40:43], v[8:11], v[72:75], 0\n\t" \
  "ds_read_b128 v[8:11], v32 offset:2048\n\ts_waitcnt lgkmcnt(7)\n\tv_mfma_f32_16x16x32_f16 v[64:67], v[148:151], v[12:15], v[64:67]\n\t" \
  "ds_read_b128 v[12:15], v28 offset:3968\n\ts_waitcnt lgkmcnt(7)\n\tv_mfma_f32_16x16x32_f16 v[64:67], v[152:155], v[16:19], v[64:67]\n\t" \
  "ds_read_b128 v[16:19], v28 offset:4032\n\tv_cvt_pk_f16_f32 v36, v36, v37\n\tv_cvt_pk_f16_f32 v37, v38, v39\n\t" \
  "v_cvt_pk_f16_f32 v38, v40, v41\n\tv_cvt_pk_f16_f32 v39, v42, v43\n\ts_nop 1\n\t" \
  "v_mfma_f32_16x16x32_f16 v[64:67], v[36:39], v[88:91], v[64:67]\n\ts_waitcnt lgkmcnt(4)\n\tv_mfma_f32_16x16x32_f16 v[40:43], v[20:23], v[0:3], 0\n\t" \
  "ds_read_b64 v[0:1], v31 offset:544\n\tds_read_b64 v[2:3], v31 offset:712\n\tds_read_b128 v[20:23], v28 offset:4096\n\t" \
  "s_waitcnt lgkmcnt(6)\n\tv_mfma_f32_16x16x32_f16 v[64:67], v[48:51], v[4:7], v[64:67]\n\ts_waitcnt lgkmcnt(5)\n\t" \
  "v_mfma_f32_16x16x32_f16 v[44:47], v[4:7], v[8:11], 0\n\ts_waitcnt lgkmcnt(4)\n\tv_mfma_f32_16x16x32_f16 v[64:67], v[156:159], v[12:15], v[64:67]\n\t" \
  "s_waitcnt lgkmcnt(3)\n\tv_mfma_f32_16x16x32_f16 v[64:67], v[160:163], v[16:19], v[64:67]\n\ts_nop 3\n\t" \
  "v_cvt_pk_f16_f32 v44, v44, v45\n\tv_cvt_pk_f16_f32 v45, v46, v47\n\tv_cvt_pk_f16_f32 v46, v40, v41\n\t" \
  "v_cvt_pk_f16_f32 v47, v42, v43\n\ts_waitcnt lgkmcnt(1)\n\ts_nop 0\n\t" \
  "v_mfma_f32_16x16x32_f16 v[64:67], v[44:47], v[0:3], v[64:67]\n\ts_waitcnt lgkmcnt(0)\n\tv_mfma_f32_16x16x32_f16 v[64:67], v[164:167], v[20:23], v[64:67]\n\t" \
  "s_nop 7\n\tv_cvt_pk_bf16_f32 v64, v64, v65\n\tv_cvt_pk_bf16_f32 v65, v66, v67\n\t" \
  "ds_write_b64 v29, v[64:65] offset:3584\n\tds_write_b32 v25, v25 offset:448\n\t"
#define CONV_CLOBBER "v0", "v1", "v2", "v3", "v4", "v5", "v6", "v7", "v8", "v9", "v10", "v11", "v12", "v13", "v14", "v15", "v16", "v17", "v18", "v19", "v20", "v21", "v22", "v23", "v36", "v37", "v38", "v39", "v40", "v41", "v42", "v43", "v44", "v45", "v46", "v47", "v52", "v53", "v54", "v55", "v56", "v57", "v58", "v59", "v60", "v61", "v62", "v63", "v64", "v65", "v66", "v67", "v68", "v69", "v70", "v71", "v72", "v73", "v74", "v75", "v76", "v77", "v78", "v79", "v80", "v81", "v82", "v83", "v84", "v85", "v86", "v87", "v88", "v89", "v90", "v91", "v92", "v93", "v94", "v95", "v96", "v97", "v98", "v99", "v100", "v101", "v102", "v103", "v104", "v105", "v106", "v107", "v108", "v109", "v110", "v111", "v112", "v113", "v114", "v115", "v116", "v117", "v118", "v119", "v120", "v121", "v122", "v123", "v124", "v125", "v126", "v127", "v128", "v129", "v130", "v131", "v132", "v133", "v134", "v135", "v136", "v137", "v138", "v139", "v140", "v141", "v142", "v143", "v144", "v145", "v146", "v147", "v148", "v149", "v150", "v151", "v152", "v153", "v154", "v155", "v156", "v157", "v158", "v159", "v160", "v161", "v162", "v163", "v164", "v165", "v166", "v167"
#define G_CLOBBER "v8", "v9", "v10", "v11", "v12", "v13", "v14", "v15", "v16", "v17", "v18", "v19", "v20", "v21", "v22", "v23", "v24", "v25", "v26", "v27", "v28", "v29", "v30", "v32", "v33", "v34", "v35", "v36", "v37", "v38", "v39", "v40", "v41", "v42", "v43", "v44", "v45", "v46", "v47", "v48", "v49", "v50", "v51", "v52", "v53", "v54", "v55", "v56", "v57", "v58", "v59", "v60", "v61", "v62", "v63", "v64", "v65", "v66", "v67", "v68", "v69", "v70", "v71", "v72", "v73", "v74", "v75", "v76", "v77", "v78", "v79", "v80", "v81", "v82", "v83", "v84", "v85", "v86", "v87", "v88", "v89", "v90", "v91", "v92", "v93", "v94", "v95", "v96", "v97", "v98", "v99", "v100", "v101", "v102", "v103", "v104", "v105", "v106", "v107", "v108", "v109", "v110", "v111", "v112", "v113", "v114", "v115", "v116", "v117", "v118", "v119", "v120", "v121", "v122", "v123", "v124", "v125", "v126", "v127"
#define D_CLOBBER "v8", "v9", "v10", "v11", "v12", "v13", "v14", "v15", "v16", "v17", "v18", "v19", "v20", "v21", "v22", "v23", "v24", "v25", "v26", "v27", "v28", "v29", "v30", "v31"
__device__ __forceinline__ float fmav(float a, float b, float c) {
  float r;
  asm("v_fma_f32 %0, %1, %2, %3" : "=v"(r) : "v"(a), "v"(b), "v"(c));
  return r;
}
__device__ __forceinline__ float fmas(float a, u32 s, float c) {
  asm("v_fmac_f32 %0, %2, %1" : "+v"(c) : "v"(a), "s"(s));
  return c;
}
#define RANK8(d0, d1, d2, d3, c0, c1, i0, f0, f1) "v_perm_b32 " d0 ", " d0 ", " d1 ", %[sg]\n\tv_perm_b32 " d2 ", " d2 ", " d3 ", %[sg]\n\t" \
  "v_bcnt_u32_b32 " d0 ", " d0 ", " i0 "\n\tv_bcnt_u32_b32 " c0 ", " d2 ", " d0 "\n\tv_bcnt_u32_b32 " c1 ", " d2 ", " d0 "\n\t" f0 "\n\t" \
  "v_permlane16_swap_b32 " c0 ", " c1 "\n\tv_add_u32 " d0 ", " c0 ", " c1 "\n\tv_add_u32 " d1 ", " c0 ", " c1 "\n\t" f1 "\n\t" \
  "v_permlane32_swap_b32 " d0 ", " d1 "\n\tv_add_u32 " c0 ", " d0 ", " d1 "\n\t"
__device__ __forceinline__ void ln_rows(u32x4 (&xv)[4], u32x8 wv, rsrc_t rs, u32& tag, int b, int w, int l, u32 st, u32 sg TLARG) {
  float s[2][3] = {}, c[2], sw = 0.f;
  u32 os = l < 16 ? O_SG + 128 * (b - 128) + 8 * l : 0x80000000u;
  u32 ot = l < 16 ? O_TOP + 8 * (b & 15) + TL * ((b >> 4) & 7) : l < 32 ? O_RG - 128 + 128 * (b - 128) + 8 * l : 0x80000000u, om = l < 16 ? TOPS / 8 : 0u, fm = l < 16 ? 0xff0u : 0u;
  u32 aw = (l & 16) ? st + 8 * w + (l >> 5) * 4 : st + 136, ac = st + 128, ar = st + 4 * (l & 15), aq = st + (l & 48), one = 1u;
  u32 kl = (2047 - (16 * (b - 128) + 2 * w + (l >> 5))) << 1;
  asm volatile("" : "+v"(os), "+v"(aw), "+v"(ac), "+v"(ar), "+v"(aq), "+v"(one), "+v"(kl), "+v"(ot), "+v"(om), "+v"(fm));
  SB();
  asm volatile("s_waitcnt vmcnt(4) lgkmcnt(0)" : "+{s44}"(tag)::"memory");
  asm volatile("" : "+{v[116:123]}"(wv));
  tag += 1u;
  *(volatile LDS u32*)(size_t)(st - 104) = tag;
  STAMP(12);
#pragma unroll
  for (int i = 0; i < 8; i++) sw = dot2b(wv[i], 0x3f803f80u, sw);
  sw += dppf<0x121, 0xf>(sw); sw += dppf<0x122, 0xf>(sw); sw += dppf<0x124, 0xf>(sw); sw += dppf<0x128, 0xf>(sw);
  sw = (rdl(sw, 0) + rdl(sw, 16)) + (rdl(sw, 32) + rdl(sw, 48));
  asm volatile("" : "+v"(sw));
  SB();
  float cv = 0.f;
#pragma unroll
  for (int r = 0; r < 2; r++) {
#pragma unroll
    for (int i = 0; i < 8; i++) {
      if (r == 0 && i == 0) { asm volatile("s_waitcnt vmcnt(3)" : "+{v[100:103]}"(xv[0])); STAMP(13); }
      if (r == 0 && i == 4) asm volatile("s_waitcnt vmcnt(2)" : "+{v[104:107]}"(xv[1]));
      if (r == 1 && i == 0) asm volatile("s_waitcnt vmcnt(1)" : "+{v[108:111]}"(xv[2]));
      if (r == 1 && i == 4) asm volatile("s_waitcnt vmcnt(0)" : "+{v[112:115]}"(xv[3]));
      const u32 a = xv[2 * r + (i >> 2)][i & 3];
      s[r][0] = dot2b(a, 0x3f803f80u, s[r][0]); s[r][1] = dot2b(a, a, s[r][1]); s[r][2] = dot2b(a, wv[i], s[r][2]);
      if (i == 3) {
        c[r] = rdl(s[r][0] * 0.125f, 0);
        if (r) { cv = l < 32 ? c[0] : c[1]; asm volatile("" : "+v"(cv)); }
        SB();
      }
    }
    const float ml = s[r][0] * 0.0625f, dl = __builtin_fmaf(s[r][0], 0.25f, -4.f * c[r]);
    s[r][1] = __builtin_fmaf(dl, dl, __builtin_fmaf(-s[r][0], ml, s[r][1]));
    SB();
  }
#pragma unroll
  for (int k = 0; k < 3; k++) swap32(s[0][k ? 3 - k : 0], s[1][k ? 3 - k : 0]);
  float q[3];
#pragma unroll
  for (int k = 0; k < 3; k++) q[k] = s[0][k] + s[1][k];
#define RED3(m) "v_add_f32_dpp %0, %0, %0 " m " bank_mask:0xf\n\tv_add_f32_dpp %1, %1, %1 " m " bank_mask:0xf\n\tv_add_f32_dpp %2, %2, %2 " m " bank_mask:0xf\n\t"
  asm volatile("s_nop 1\n\t" RED3("row_ror:1 row_mask:0xf") RED3("row_ror:2 row_mask:0xf") RED3("row_ror:4 row_mask:0xf") RED3("row_ror:8 row_mask:0xf")
               RED3("row_bcast:15 row_mask:0xa") : "+v"(q[0]), "+v"(q[1]), "+v"(q[2]));
  const float mu = q[0] * (1.f / D), dv = mu - cv;
  const float var = fmaxf(__builtin_fmaf(-dv, dv, q[1] * (1.f / D)), 0.f);
  const float m = rb(mu);
  float sd = __builtin_amdgcn_sqrtf(rb(rb(var) + 1e-5f)), num = __builtin_fmaf(-m, sw, q[2]);
  asm volatile("s_nop 0\n\tv_cvt_pk_bf16_f32 %0, 0, %0" : "+v"(sd), "+v"(num));
  u32 rv = __float_as_uint(rb(num * __builtin_amdgcn_rcpf(sd)));
  rv ^= (u32)((int)rv >> 15) | 0x80000000u;
  rv = __builtin_amdgcn_perm(rv, kl, 0x07060100u) >> 1;
  asm volatile("ds_write2_b32 %[aw], %[sv], %[rv] offset1:16\n\ts_mov_b64 exec, 1\n\tds_add_rtn_u32 v104, %[ac], %[one]\n\ts_mov_b64 exec, -1\n\t"
               "ds_read_b32 v106, %[ar]\n\tds_read_b32 v105, %[ar] offset:64\n\tds_read_b128 v[100:103], %[aq] offset:64\n\tv_mov_b32 v107, %[t]\n\tv_mov_b32 v109, %[t]\n\t"
               "s_waitcnt lgkmcnt(3)\n\tv_cmp_eq_u32 vcc, 7, v104\n\ts_bitcmp1_b32 vcc_lo, 0\n\ts_cbranch_scc0 1f\n\t"
               "s_waitcnt lgkmcnt(2)\n\tbuffer_store_dwordx2 v[106:107], %[os], %[rs], 0 offen sc1\n\ts_waitcnt lgkmcnt(0)\n\t"
               "v_sub_u32 v100, v105, v100\n\tv_sub_u32 v101, v105, v101\n\tv_sub_u32 v102, v105, v102\n\tv_sub_u32 v103, v105, v103\n\t"
               RANK8("v100", "v101", "v102", "v103", "v104", "v103", "0", "s_nop 1", "s_nop 1") "v_cmp_eq_u32 vcc, 16, v104\n\t"
               "v_sub_u32 v108, 19, v104\n\ts_ff1_i32_b32 s6, vcc_lo\n\tv_readlane_b32 s6, v105, s6\n\t"
               "v_mad_u32_u24 v104, v104, %[om], %[ot]\n\ts_nop 0\n\tv_lshrrev_b32 v108, v108, s6\n\tv_bfi_b32 v108, %[fm], v108, v105\n\t"
               "buffer_store_dwordx2 v[108:109], v104, %[rs], 0 offen sc1\n1:\n\ts_waitcnt lgkmcnt(0)"
               :: [aw] "v"(aw), [sv] "v"(__builtin_amdgcn_perm(__float_as_uint(sd), __float_as_uint(m), 0x07060302u)), [rv] "v"(rv), [ac] "v"(ac), [one] "v"(one),
                  [ar] "v"(ar), [aq] "v"(aq), [os] "v"(os), [om] "v"(om), [ot] "v"(ot), [fm] "v"(fm), [sg] "s"(sg), [t] "s"(tag), [rs] "s"(rs)
               : "vcc", "scc", "s6", "v100", "v101", "v102", "v103", "v104", "v105", "v106", "v107", "v108", "v109", "memory");
  SB();
}

#define GL __attribute__((address_space(1)))
typedef const GL u16* gp;

__global__ __launch_bounds__(768, 3) void fused(const u16*, const u16*, const u16*, const u16*, const u16*, const u16*, const u16*, const u16*,
    const u16*, const u16*, const u16*, u16*, char*) {
  __shared__ __attribute__((aligned(256))) char smem[SMEM];
  const u32 lds0 = (u32)(size_t)(LDS char*)smem;
  int w0;
#define GLD(r0, r1, r2, r3, o_) "global_load_dwordx4 v[" #r0 ":" #r1 "], " #o_ ", s[16:17]\n\tglobal_load_dwordx4 v[" #r2 ":" #r3 "], " #o_ ", s[16:17] offset:2048\n\t"
  asm volatile(
      "s_load_dwordx16 s[16:31], s[0:1], 0\n\ts_load_dwordx8 s[32:39], s[0:1], 64\n\ts_load_dwordx2 s[40:41], s[0:1], 96\n\t"
      "s_memrealtime s[46:47]\n\t"
      "s_getpc_b64 s[4:5]\n2:\n\ts_lshr_b32 s7, s2, 3\n\ts_and_b32 s10, s4, 0xffffff00\n\ts_mov_b32 s11, s5\n\ts_mul_i32 s8, s7, 0x600\n\t"
      "v_lshl_add_u32 v3, v0, 1, s8\n\ts_cmpk_lt_u32 s2, %[pn]\n\ts_cbranch_scc0 1f\n\t"
      "global_load_ushort v163, v3, s[10:11] offset:%[pf]\n"
      "1:\n\ts_cmpk_lt_u32 s2, 0x80\n\tv_readfirstlane_b32 s3, v0\n\tv_and_b32 v1, 63, v0\n\tv_mov_b64 v[4:5], 0\n\tv_mov_b64 v[6:7], 0\n\tv_lshlrev_b32 v2, 4, v1\n\tv_lshlrev_b32 v8, 2, v0\n\t"
      "s_mov_b32 s42, %[we]\n\ts_mov_b32 s43, 0x20000\n\ts_cbranch_scc0 3f\n\t"
      "s_lshr_b32 s3, s3, 6\n\ts_mul_i32 s9, s3, %[xr]\n\ts_min_u32 s9, s9, %[zm]\n\tv_add_u32 v3, s9, v2\n\tv_add_u32 v3, %[zo], v3\n\t"
      "s_and_b32 s7, s7, 7\n\ts_and_b32 s6, s2, 7\n\ts_and_b32 s8, s2, 64\n\ts_lshl3_add_u32 s8, s6, s8\n\ts_add_u32 s45, s8, s7\n\t"
      "s_add_u32 s4, s4, lane_tab@rel32@lo+(6f-2b-4)\n6:\n\ts_addc_u32 s5, s5, lane_tab@rel32@hi+(6b-2b+4)\n\t"
      "ds_write_b128 v3, v[4:7]\n\ts_cmp_lt_u32 s3, 8\n\ts_cbranch_scc1 5f\n\t"
      "s_lshl_b32 s8, s45, 4\n\ts_lshl_b32 s9, s3, 18\n\ts_add_u32 s9, s9, s8\n\ts_bitset0_b32 s9, 21\n\tv_lshl_add_u32 v148, v1, 12, s9\n\t"
      "s_lshl3_add_u32 s9, s3, s7\n\ts_lshl_b32 s9, s9, 17\n\ts_add_u32 s9, s9, s8\n\ts_bitset0_b32 s9, 23\n\tv_lshl_add_u32 v9, v1, 11, s9\n\t"
      "v_add_u32 v11, 0x100000, v148\n\tv_add_u32 v12, 0x200000, v148\n\t"
      "v_lshlrev_b32 v149, 4, v0\n\ts_lshl_b32 s9, s3, 10\n\ts_add_u32 m0, s9, %[ob]\n\tv_add_u32 v10, 0xffffe800, v149\n\tglobal_load_lds_dwordx4 v10, s[4:5]\n\t"
      "v_add_u32 v149, %[sg], v149\n\t"
      "s_waitcnt lgkmcnt(0)\n\ts_load_dword s44, s[40:41], 0\n\t"
      "s_barrier\n\t"
      "global_load_dword v160, v9, s[16:17]" PRENT "\n\t"
      GLD(100, 103, 104, 107, v148)
      GLD(108, 111, 112, 115, v11)
      "s_cmp_eq_u32 s6, 3\n\ts_cbranch_scc1 .Lrole_p\n\t"
      GLD(116, 119, 120, 123, v12)
      "s_branch .Lrole_p\n"
      "3:\n\ts_lshr_b32 s3, s3, 6\n\ts_cmp_lt_u32 s3, 8\n\ts_cbranch_scc1 4f\n\ts_barrier\n\ts_branch .Lrole_d\n4:\n\t"
      "v_add_u32 v3, %[fc], v2\n\ts_add_u32 s8, s3, s2\n\ts_and_b32 s45, s8, 7\n\ts_lshl3_add_u32 s8, s2, s45\n\t"
      "s_lshl_b32 s8, s8, 12\n\ts_bitset0_b32 s8, 22\n\tds_write_b128 v3, v[4:7]\n\tv_add_u32 v3, s8, v2\n\t"
      "s_waitcnt lgkmcnt(0)\n\tglobal_load_dwordx4 v[116:119], v2, s[30:31]\n\tglobal_load_dwordx4 v[120:123], v2, s[30:31] offset:1024\n\t"
      "s_load_dword s44, s[40:41], 0\n\ts_barrier\n\t"
      "global_load_dwordx4 v[100:103], v3, s[16:17]" LNNT "\n\tglobal_load_dwordx4 v[104:107], v3, s[16:17] offset:1024" LNNT "\n\t"
      "global_load_dwordx4 v[108:111], v3, s[16:17] offset:2048" LNNT "\n\tglobal_load_dwordx4 v[112:115], v3, s[16:17] offset:3072" LNNT "\n\t"
      "s_branch .Lrole_l\n"
      "5:\n\t"
      "global_load_dwordx4 v[16:19], v2, s[4:5]\n\tglobal_load_dwordx4 v[20:23], v2, s[4:5] offset:1024\n\t"
      "s_lshl3_add_u32 s8, s45, s3\n\ts_lshl_b32 s9, s8, 1\n\tv_mov_b32 v9, s9\n\ts_lshl_b32 s9, s45, 11\n\tv_add_u32 v3, s9, v8\n\t"
      "s_waitcnt lgkmcnt(0)\n\tglobal_load_short_d16_hi v24, v9, s[26:27]\n\tglobal_load_ushort v160, v9, s[28:29]\n\t"
      "global_load_dword v96, v3, s[18:19]" PNT "\n\tglobal_load_dword v97, v3, s[20:21]" PNT "\n\t"
      "global_load_dword v68, v3, s[22:23]" PNT "\n\tglobal_load_dword v69, v3, s[24:25]" PNT "\n\ts_barrier\n\ts_branch .Lrole_c"
      : "={s3}"(w0)
      : "{s[0:1]}"(__builtin_amdgcn_kernarg_segment_ptr()), "{s2}"(blockIdx.x), "{v0}"(threadIdx.x), [pf] "n"(PFO), [pn] "n"(8 * PFN), [we] "n"(WS_END), [xr] "n"(XREG),
        [zm] "n"(8 * XREG), [zo] "n"(OX - 16), [sg] "n"(O_SG - 8192), [fc] "n"(FC), [ob] "n"(OB - 8192)
      : "memory");
#define TLREGS()
#define ROLE(name, text, ...) \
  gp x, wg, wu, wd; GL u16* out; rsrc_t rs; int b, w, t, l, g; u32 tag, st0; \
  asm volatile(".Lrole_" #name ":" : "={v0}"(t), "={v1}"(l) __VA_ARGS__ :: "memory"); \
  asm volatile(text : "={s2}"(b), "={s3}"(w), "={s[16:17]}"(x), "={s[32:33]}"(wg), "={s[34:35]}"(wu), "={s[36:37]}"(wd), \
               "={s[38:39]}"(out), "={s[40:43]}"(rs), "={s44}"(tag), "={s45}"(g), "={s46}"(st0) :: "memory"); \
  SB(); \
  __builtin_assume((u32)l < 64u); __builtin_assume((u32)t < 768u); __builtin_assume((u32)w < 12u); __builtin_assume((u32)b < 256u); __builtin_assume((u32)g < 128u); \
  TLREGS(); \
  const int xk = b & 7, j = (b >> 3) & 15, n16 = l & 15, kg = l >> 4
#define FLAG(o, v) do { asm volatile("" ::: "memory"); SB(); *(volatile LDS u32*)(LDS char*)(smem + (o)) = (v); SB(); } while (0)
#define CWAIT(o, n, nap) do { _Pragma("unroll 1") for (int spin = 0; spin < SPIN; spin++) { \
      if (__builtin_amdgcn_readfirstlane((int)*(const volatile LDS u32*)(LDS char*)(smem + (o))) >= (int)(n)) break; \
      asm volatile(nap ::: "memory"); } SB(); asm volatile("" ::: "memory"); } while (0)
#define AWAIT(o, n, nap) do { _Pragma("unroll 1") for (int spin = 0; spin < SPIN; spin++) { \
      if (__all(*(const volatile LDS u32*)(LDS char*)(smem + (o)) >= (u32)(n))) break; \
      asm volatile(nap ::: "memory"); } SB(); asm volatile("" ::: "memory"); } while (0)
#define POLL8(res_, off_) do { u32 n_ = SPIN; \
    asm volatile("1:\n\tbuffer_load_dwordx2 v[4:5], %2, %3, 0 offen sc1\n\ts_sub_u32 %1, %1, 1\n\ts_waitcnt vmcnt(0)\n\t" \
                 "v_cmp_ne_u32 vcc, %4, v5\n\ts_cbranch_scc1 2f\n\ts_cbranch_vccnz 1b\n2:" \
                 : "=&{v[4:5]}"(res_), "+s"(n_) : "v"(off_), "s"(rs), "s"(tag) : "vcc", "scc", "memory"); } while (0)
#define UNTIL(t_) "2:\n\ts_sleep 1\n\ts_memrealtime s[8:9]\n\ts_waitcnt lgkmcnt(0)\n\ts_sub_u32 s8, s8, %[st]\n\ts_cmp_lt_u32 s8, " t_ "\n\ts_cbranch_scc1 2b\n\t"
#define IXO(l_) (O_IDX + (2 * xk + (j & 1)) * 128 + ((l_) & 7) * 8)
#define IDS() const int id = 16 * xk + j, sb = id * 57 >> 10, mat = id & 1, u = (id - 18 * sb) >> 1; \
    const bool slow = u >= 6; \
    const int part = slow ? u - 6 : u & 1, cb = 4 * sb + (slow ? 1 : (u >> 1) + (u > 1)), r0 = part * (slow ? 320 : 512), ns = slow ? 5 + (u >> 3) : 8; \
    const int kp = 4 * sb + ((u >> 1) & 3), ct = 2 * (u & 1) + mat
#define TS(a, b_)
#define TSC
#define DONE "s_endpgm\n"
#define TSD
#define TSGET(i0, i1, i2, i3)
  if (blockIdx.x >= 128) {
    if (w0 >= 8) {
      ROLE(d, "s_waitcnt lgkmcnt(0)");
      IDS();
      const int dw = w - 8;
      const u32 sw = (((n16 >> 1) ^ (kg & 1)) << 5) + ((l & 1) << 4);
      gp base = (mat ? wu : wg) + (size_t)(r0 + 4 * dw) * F + cb * 128;
      u32 v = kg * (2 * F) + sw, fa = lds0 + FC + 4 * dw, vp = 96 + dw, t_;
      asm volatile("" : "+v"(v), "+v"(fa), "+v"(vp));
      asm volatile("s_mov_b32 m0, %0" ::"s"(lds0 + 1024 * dw));
#define VMC "s_getreg_b32 s6, hwreg(HW_REG_IB_STS, 0, 4)\n\t"
#define RUN(k, q, lim, v_, b_, st_) asm volatile("1:\n\ts_lshr_b32 s6, m0, 4\n\ts_and_b32 s6, s6, 0xc0\n\tv_xor_b32 %[t], s6, %[v]\n\ts_waitcnt vmcnt(%[c])\n\t" VMC \
        "global_load_lds_dwordx4 %[t], %[b] nt\n\ts_add_u32 m0, m0, %[m]\n\tv_add_u32 %[v], %[d], %[v]\n\t.if %[m] == 3072\n\tv_mad_i32_i24 %[t], s6, -3, %[p]\n\tv_add_u32 %[p], 3, %[p]\n\t" \
        "s_mov_b64 exec, 1\n\tds_max_u32 %[a], %[t]\n\ts_mov_b64 exec, -1\n\t.endif\n\ts_cmp_lt_u32 m0, %[l]\n\ts_cbranch_scc1 1b" \
        : [v] "+v"(v_), [p] "+v"(vp), [t] "=&v"(t_) : [b] "s"(b_), [c] "n"((q) - 1), [m] "n"(1024 * (k)), [d] "n"((k) * (st_)), \
          [a] "v"(fa), [l] "s"(lim) : "scc", "s6", "memory")
#define MARK(c, val) "s_waitcnt vmcnt(" #c ")\n\t" val "\n\ts_mov_b64 exec, 1\n\tds_max_u32 %[a], %[t]\n\ts_mov_b64 exec, -1\n\t"
      __builtin_amdgcn_s_setprio(DPRIO);
      STAMP(8);
      if (id < 126) { if (slow) RUN(4, QAS, 32768, v, base, 8 * F); else RUN(4, 8, 32768, v, base, 8 * F); }
      if (dw < 3) {
        if (id >= 126) __builtin_amdgcn_endpgm();
        gp bd = wd + (size_t)(128 * kp + 2 * dw) * D + ct * 256;
        u32 vd = (l >> 5) * (2 * D + 32) ^ 16 * (l & 31);
        asm volatile("" : "+v"(vd));
        if (slow) RUN(3, QS, 16384 * ns, v, base, 8 * F);
        else { RUN(3, 8, 32768 + 3072 * RAMP, v, base, 8 * F); RUN(3, QD, 131072, v, base, 8 * F); }
        STAMP(1);
        {
          u32 n_ = SPIN;
          asm volatile("1:\n\t" VMC MARK(63, "v_mad_i32_i24 %[t], s6, -3, %[p]") "ds_read_b32 %[t], %[f]\n\ts_sub_u32 %[n], %[n], 1\n\ts_waitcnt lgkmcnt(0)\n\t"
                       "v_cmp_lt_u32 vcc, %[k], %[t]\n\ts_cbranch_scc1 2f\n\ts_cbranch_vccz 1b\n2:\n\ts_mov_b32 m0, %[m]"
                       : [t] "=&v"(t_), [n] "+s"(n_) : [a] "v"(fa), [p] "v"(vp), [f] "v"(u == 8 ? fa : lds0 + FC + 20), [k] "s"(u == 8 ? 159 : 16 * HOLD - 105), [m] "s"(lds0 + 1024 * dw)
                       : "vcc", "scc", "s6", "memory");
        }
        STAMP(3);
        if (u < 8) {
          RUN(3, QE, 65536, vd, bd, 4 * D);
          STAMP(6);
          asm volatile(MARK(0, "v_mov_b32 %[t], -1") : [t] "=&v"(t_) : [a] "v"(fa) : "memory");
          STAMP(4);
        }
        FLUSH();
        __builtin_amdgcn_endpgm();
      }
      asm volatile(MARK(0, "v_mov_b32 %[t], -1") : [t] "=&v"(t_) : [a] "v"(fa) : "memory");
      u32 km, rk;
      u32 hi, lo, k2, k3, gd, thr, sg = 0x0c0c0b09u;
      u32 ka = lds0 + FC + 384 + 8 * (l >> 3), kc = 2047 * 2048 + 2 * r0 + 16 * ((l & 7) ^ ((l >> 2) & 4)), kq = lds0 + FC + 512 + (l & 48), kr = lds0 + FC + 512 + 4 * n16;
      u32 c1 = 896, c2 = 1792, c3 = 2688, cm = (u32)-2048, cs = O_SG + 8 * 2047, ca = lds0 + FC + 512;
      rsrc_t rx = __builtin_amdgcn_make_buffer_rsrc((void*)(x - 1344), 0, 2 * L * D + 4096, 0x00020000);
      u32 co = O_CLR + 8 * (18 * sb + (0x84c0 >> 4 * (kp & 3) & 15) + min(l, (kp & 3) == 1 ? 5 : 3));
      asm volatile("" : "+v"(ka), "+v"(kc), "+v"(kq), "+v"(kr), "+v"(co));
      asm volatile("" : "+s"(c1), "+s"(c2), "+s"(c3), "+s"(cm), "+s"(cs), "+s"(ca), "+s"(sg), "+s"(rx));
      *(u32x4*)(smem + FA + 6144 + 16 * l) = u32x4{0u, 0u, 0u, 0u};
      *(u32x4*)(smem + FA + 7168 + 16 * l) = u32x4{0u, 0u, 0u, 0u};
      CWAIT(FC + 256, 1, "s_sleep 4");
      tag = (u32)__builtin_amdgcn_readfirstlane((int)*(const volatile LDS u32*)(LDS char*)(smem + FC + 24));
      {
        u32 n_ = SPIN, t0, t1, t2, t3, se;
#define TLX(a, b_, c, d, sc) "buffer_load_dwordx4 v[" #a ":" #b_ "], %[o], %[r], 0 offen" sc "\n\tbuffer_load_dwordx4 v[" #c ":" #d "], %[o], %[r], %[ts] offen" sc "\n\t"
#define TLD(a, b_, c, d) TLX(a, b_, c, d, " sc1")
#define TCK(r0, r1, r2, r3, r4, r5, r6, r7) "s_waitcnt vmcnt(4)\n\tv_xor_b32 %[t0], %[t], v" #r1 "\n\tv_bitop3_b32 %[t0], %[t0], %[t], v" #r3 " bitop3:0xf6\n\t" \
          "v_bitop3_b32 %[t0], %[t0], %[t], v" #r5 " bitop3:0xf6\n\tv_bitop3_b32 %[t0], %[t0], %[t], v" #r7 " bitop3:0xf6\n\t" \
          "v_cmp_ne_u32 vcc, 0, %[t0]\n\tv_mov_b32 %[hi], v" #r0 "\n\tv_mov_b32 %[lo], v" #r2 "\n\tv_mov_b32 %[k2], v" #r4 "\n\tv_mov_b32 %[k3], v" #r6 "\n\t" \
          "s_cbranch_vccz 5f\n\t" TLD(r0, r3, r4, r7)
        asm volatile("2:\n\ts_sleep 1\n\ts_memrealtime s[8:9]\n\ts_waitcnt lgkmcnt(0)\n\ts_sub_u32 s8, s8, %[st]\n\ts_cmp_lt_u32 s8, %[tw]\n\ts_cbranch_scc1 2b\n\t"
                     TLX(128, 131, 132, 135, SCT) "s_sleep " STR(RSL) "\n\t" TLD(136, 139, 140, 143) "s_sleep " STR(RSL) "\n\t" TLX(144, 147, 148, 151, SCU)
                     "1:\n\t" TCK(128, 129, 130, 131, 132, 133, 134, 135) TCK(136, 137, 138, 139, 140, 141, 142, 143) TCK(144, 145, 146, 147, 148, 149, 150, 151)
                     "s_sub_u32 %[n], %[n], 1\n\ts_cbranch_scc0 1b\n5:\n\t" TS(60, 61)
                     "v_and_or_b32 %[t0], %[hi], %[mk], %[pa]\n\tv_and_or_b32 %[t1], %[lo], %[mk], %[pb]\n\t"
                     "v_max_u32 %[t2], %[t0], %[t1]\n\tv_max_u32 %[t3], %[t0], %[t1]\n\tv_perm_b32 %[gd], %[hi], %[lo], %[p0]\n\tv_perm_b32 %[se], %[k2], %[k3], %[p0]\n\t"
                     "v_permlane16_swap_b32 %[t2], %[t3]\n\tv_max_u32 %[km], %[t2], %[t3]\n\tv_max_u32 %[rk], %[t2], %[t3]\n\t"
                     "v_max_u32 %[hi], %[t0], %[t1]\n\tv_min_u32 %[lo], %[t0], %[t1]\n\t"
                     "v_permlane32_swap_b32 %[km], %[rk]\n\tv_max_u32 %[km], %[km], %[rk]\n\tv_not_b32 %[t2], %[km]\n\t"
                     "v_and_or_b32 %[k2], %[k2], %[mk], %[pa]\n\t"
                     "v_not_b32_dpp %[t2], %[km] row_ror:4 row_mask:0x2 bank_mask:0xf\n\tv_not_b32_dpp %[t2], %[km] row_ror:8 row_mask:0x4 bank_mask:0xf\n\t"
                     "v_not_b32_dpp %[t2], %[km] row_ror:12 row_mask:0x8 bank_mask:0xf\n\t"
                     "v_pk_lshlrev_b16 %[gd], 4, %[gd] op_sel_hi:[0,1]\n\tv_add_u32 %[t0], %[km], %[t2]\n\t"
                     "v_add_u32_dpp %[t1], %[t2], %[km] row_ror:1 row_mask:0xf bank_mask:0xf\n\tv_add_u32_dpp %[t3], %[t2], %[km] row_ror:2 row_mask:0xf bank_mask:0xf\n\t"
                     "v_add_u32_dpp %[t2], %[t2], %[km] row_ror:3 row_mask:0xf bank_mask:0xf\n\t"
                     RANK8("%[t0]", "%[t1]", "%[t2]", "%[t3]", "%[rk]", "%[t3]", "-2", "v_lshrrev_b32 %[se], 4, %[se]\n\tv_perm_b32 %[gd], %[gd], %[se], %[p1]",
                           "v_pk_max_u16 %[gd], %[gd], %[gd] op_sel:[0,1] op_sel_hi:[1,0]\n\tv_and_or_b32 %[k3], %[k3], %[mk], %[pb]")
                     "v_cmp_eq_u32 vcc, 56, %[rk]\n\tv_max3_u32 %[se], %[lo], %[k2], %[k3]\n\tv_lshrrev_b32 %[gd], 1, %[gd]\n\ts_ff1_i32_b32 s6, vcc_lo\n\t"
                     "v_readlane_b32 s6, %[km], s6\n\tv_or_b32 %[gd], 0x7fff, %[gd]\n\tv_med3_u32 %[t2], %[lo], %[k2], %[k3]\n\t"
                     "v_cmp_le_u32 s[8:9], s6, %[hi]\n\tv_cmp_le_u32 s[10:11], s6, %[se]\n\tv_max_u32 %[t2], %[t2], %[gd]\n\t"
                     "v_mbcnt_lo_u32_b32 %[t0], s8, 0\n\tv_mbcnt_lo_u32_b32 %[t1], s10, -15\n\tv_mbcnt_hi_u32_b32 %[t0], s9, %[t0]\n\tv_mbcnt_hi_u32_b32 %[t1], s11, %[t1]\n\t"
                     "v_lshl_add_u32 %[t0], %[t0], 2, %[ca]\n\tv_mad_i32_i24 %[t1], %[t1], -4, %[ca]\n\tv_cmp_le_u32 vcc, s6, %[t2]\n\t"
                     "s_mov_b64 exec, s[8:9]\n\tds_write_b32 %[t0], %[hi]\n\ts_mov_b64 exec, s[10:11]\n\tds_write_b32 %[t1], %[se]\n\ts_mov_b64 exec, -1\n\t"
                     "ds_read_b32 %[se], %[kr]\n\tds_read_b128 v[152:155], %[kq]\n\t"
                     "s_bcnt1_i32_b64 s8, s[8:9]\n\ts_bcnt1_i32_b64 s10, s[10:11]\n\ts_add_u32 s8, s8, s10\n\ts_cmp_gt_u32 s8, 16\n\ts_cselect_b64 s[8:9], -1, vcc\n\t"
                     "s_cmp_lg_u64 s[8:9], 0\n\ts_waitcnt lgkmcnt(0)\n\ts_cbranch_scc1 6f\n\t"
                     "v_sub_u32 %[t0], %[se], v152\n\tv_sub_u32 %[t1], %[se], v153\n\tv_sub_u32 %[t2], %[se], v154\n\tv_sub_u32 %[t3], %[se], v155\n\t"
                     RANK8("%[t0]", "%[t1]", "%[t2]", "%[t3]", "%[rk]", "%[t3]", "0", "v_mov_b32 %[km], %[se]\n\ts_nop 0", "s_nop 1") "6:\n\t" TS(62, 63)
                     : [hi] "=&v"(hi), [lo] "=&v"(lo), [k2] "=&v"(k2), [k3] "=&v"(k3), [gd] "=&v"(gd), [se] "=&v"(se), [km] "=&v"(km), [rk] "=&v"(rk),
                       [t0] "=&v"(t0), [t1] "=&v"(t1), [t2] "=&v"(t2), [t3] "=&v"(t3), [n] "+s"(n_)
                     : [o] "v"(O_TOP + 16 * (l & 7) + TL * (l >> 3)), [r] "s"(rs), [t] "s"(tag), [ts] "s"(TOPS), [st] "s"(st0), [tw] "n"(TW), [mk] "s"(0x7fff800fu),
                       [pa] "v"((127 - 2 * l) << 4), [pb] "v"((126 - 2 * l) << 4), [sg] "s"(sg), [p0] "s"(0x05040100u), [p1] "s"(0x07020500u), [ca] "s"(ca),
                       [kr] "v"(kr), [kq] "v"(kq)
                     : "vcc", "scc", "s6", "s8", "s9", "s10", "s11", "memory" TSC);
      }
      unsigned long long more;
      asm volatile("" : "={s6}"(thr), "={s[8:9]}"(more));
      if (__builtin_expect(more != 0, 0)) {
        const u32 m0 = km;
        unsigned long long all = __ballot(gd >= thr);
        if (gd >= thr) hi = lo = k2 = k3 = 0u;
#pragma unroll 1
        for (int i = 0; i < 4;) {
          u32 k, m = m0;
          if (all) {
            const int a = __builtin_ctzll(all);
            all &= all - 1;
            u32x2 r;
            const u32 o = O_RG + 256 * a + 8 * (l & 31);
            POLL8(r, o);
            k = l < 32 ? r.x : 0u;
            m = (u32)__builtin_amdgcn_readlane((int)m0, a);
          } else { k = hi; hi = lo; lo = k2; k2 = k3; i++; }
          asm volatile("v_cmp_le_u32 s[8:9], %[t], %[k]\n\tv_cmp_ne_u32 vcc, %[k], %[m0]\n\ts_and_b64 s[8:9], s[8:9], vcc\n\ts_cbranch_scc0 2f\n"
                       "1:\n\ts_ff1_i32_b64 s7, s[8:9]\n\tv_readlane_b32 s12, %[k], s7\n\ts_lshl_b64 s[10:11], 1, s7\n\ts_nop 0\n\t"
                       "v_cmpx_gt_u32 vcc, s12, %[km]\n\tv_add_u32 %[rk], 8, %[rk]\n\ts_bcnt0_i32_b32 s7, vcc_lo\n\tv_cmpx_eq_u32 vcc, 0x80, %[rk]\n\t"
                       "v_mov_b32 %[km], s12\n\tv_lshlrev_b32 %[rk], 2, s7\n\ts_mov_b64 exec, -1\n\ts_andn2_b64 s[8:9], s[8:9], s[10:11]\n\ts_cbranch_scc1 1b\n2:"
                       : [km] "+v"(km), [rk] "+v"(rk) : [k] "v"(k), [m0] "v"(m), [t] "s"(thr) : "vcc", "scc", "s7", "s8", "s9", "s10", "s11", "s12");
        }
      }
      if (id < 126) {
        u32 a_, v_, n_ = SPIN;
#define RLD(o, s_) "buffer_load_dwordx4 %[v], %[rx], " s_ " offen offset:" #o " lds\n\t"
        asm volatile("v_add_u32 %[a], %[kb], %[rk]\n\tv_and_b32 %[v], 0x7ff, %[km]\n\ts_mov_b32 m0, %[fa]\n\tds_write_b32 %[a], %[v]\n\tds_read_b32 %[a], %[ka]\n\t"
                     "s_cmp_eq_u32 %[ns], 8\n\ts_waitcnt lgkmcnt(0)\n\tv_mad_i32_i24 %[v], %[a], %[cm], %[kc]\n\t"
                     RLD(0, "%[c3]") RLD(1024, "%[c2]") RLD(2048, "%[c1]") RLD(3072, "0")
                     "s_mov_b32 m0, %[fb]\n\tv_add_u32 %[v], 0x200, %[v]\n\tv_mad_i32_i24 %[a], %[a], -8, %[cs]\n\t"
                     "s_cbranch_scc0 3f\n\t" RLD(2048, "%[c1]") RLD(3072, "0")
                     "3:\n\tbuffer_load_dwordx2 v[160:161], %[a], %[rs], 0 offen sc1\n\t" RLD(0, "%[c3]") RLD(1024, "%[c2]") "buffer_load_dwordx2 v[158:159], %[co], %[rs], 0 offen sc1\n\t"
                     "v_mov_b32 %[v], 16 * " STR(HOLD) " - 112\n\tv_subrev_u32 %[ka], %[kd], %[ka]\n\ts_mov_b64 exec, 1\n\tds_add_u32 %[fc], %[v] offset:8\n\ts_mov_b64 exec, -1\n\tv_mov_b32 %[v], -1\n\t" TS(64, 65)
                     "s_sleep " STR(CSL) "\n\tbuffer_load_dwordx2 v[154:155], %[co], %[rs], 0 offen sc1\n\t"
                     "s_waitcnt vmcnt(4)\n1:\n\tv_cmp_ne_u32 vcc, %[t], v161\n\tv_and_b32 v156, 0xffff0000, v160\n\tv_lshlrev_b32 v157, 16, v160\n\tv_rcp_f32 v156, v156\n\t"
                     "s_sub_u32 %[n], %[n], 1\n\ts_cbranch_vccz 2f\n\ts_cbranch_scc1 2f\n\t"
                     "buffer_load_dwordx2 v[160:161], %[a], %[rs], 0 offen sc1\n\ts_waitcnt vmcnt(0)\n\ts_branch 1b\n2:\n\tv_mul_f32 v157, -v157, v156\n\t"
                     "ds_write_b64 %[ka], v[156:157]\n\ts_waitcnt vmcnt(2)\n\tds_write_b32 %[fc], %[v] offset:4\n\t" TS(66, 67)
                     "s_mov_b32 %[n], %[spin]\n\ts_waitcnt vmcnt(1)\n\tv_cmp_ne_u32 vcc, %[t], v159\n\ts_cbranch_vccz 5f\n"
                     "4:\n\ts_waitcnt vmcnt(0)\n\tv_cmp_ne_u32 vcc, %[t], v155\n\ts_sub_u32 %[n], %[n], 1\n\ts_cbranch_vccz 5f\n\t"
                     "buffer_load_dwordx2 v[154:155], %[co], %[rs], 0 offen sc1\n\ts_cbranch_scc0 4b\n5:"
                     : [a] "=&v"(a_), [v] "=&v"(v_), [ka] "+v"(ka), [n] "+s"(n_)
                     : [co] "v"(co), [rk] "v"(rk), [km] "v"(km), [kc] "v"(kc), [kb] "n"(FC + 384), [kd] "n"(384 - 32), [fa] "n"(FA), [fb] "n"(FA + 4096), [rx] "s"(rx), [rs] "s"(rs),
                       [c1] "s"(c1), [c2] "s"(c2), [c3] "s"(c3), [cm] "s"(cm), [cs] "s"(cs), [ns] "s"(ns), [fc] "v"(fa), [t] "s"(tag), [spin] "n"(SPIN) : "vcc", "scc", "memory" TSC);
        TSGET(12, 13, 14, 15);
        FLAG(FC + 28, 1u);
        STAMP(10);
      } else {
        const u32 cd = km << 1;
        const u32x2 r = {(2047u - (km & 0x7ffu)) | ((cd ^ ((u32)((int)~cd >> 15) | 0x80000000u)) & 0xffff0000u), tag};
        const u32 o = rk < 64u ? O_IDX + 1024 * mat + 128 * kg + rk : 0x80000000u;
        st8(rs, o, r);
        st8(rs, o + 512, r);
        TSGET(12, 13, 14, 15);
        STAMP(14);
        STAMP(15);
        if (mat) {
#pragma unroll 1
          for (int spin = 0; spin < SPIN; spin++) {
            const u32x4 a = ld16(rs, O_ARR + 16 * l);
            if (__all(a.y == tag && a.w == tag)) break;
            asm volatile("s_sleep 16" ::: "memory");
          }
          if (l == 0) __builtin_amdgcn_raw_buffer_store_b32(tag, rs, 0, 0, 16);
        }
      }
      FLUSH();
      __builtin_amdgcn_endpgm();
    }
    u32x4 xv[4];
    u32x8 wv;
    ROLE(l, "", , "={v[100:103]}"(xv[0]), "={v[104:107]}"(xv[1]), "={v[108:111]}"(xv[2]), "={v[112:115]}"(xv[3]), "={v[116:123]}"(wv));
    STAMP(11);
    u32 sg = 0x0c0c0b09u;
    asm volatile("" : "+s"(sg));
    ln_rows(xv, wv, rs, tag, b, g, l, lds0 + FC + 128, sg TLVAL);
    STAMP(10);
    IDS();
    if (id >= 126) {
      FLUSH();
      __builtin_amdgcn_endpgm();
    }
    const int wr = 16 * (kg >> 1) + 4 * (kg & 1) + (n16 & 8) + ((n16 >> 2) & 1);
    u32 wa = lds0 + 256 * wr + ((w ^ ((wr & 1) | ((wr >> 1) & 6))) << 5) + 8 * (l & 3), sel = (l & 8) ? 0xeeeeu : 0x4444u;
    u32 so = 4 * t;
    asm volatile("" : "+v"(wa), "+v"(sel), "+v"(so));
    asm volatile(UNTIL(STR(CLD)) :: [st] "s"(st0) : "s8", "s9", "scc");
    __builtin_amdgcn_raw_buffer_store_b32(~0u, rs, so, O_HG + (6 * cb + 3 * mat + part) * 2048, 16);
    st8(rs, u < 8 ? O_PG + (4 * kp + ct) * 4096 + 2 * so : 0x80000000u, u32x2{~0u, ~0u});
#define SWP(a0, a1, a2, a3) "v_permlane32_swap_b32 " a0 ", " a2 "\n\tv_permlane32_swap_b32 " a1 ", " a3 "\n\t"
#define OUT(a0, a1, o) "v_cvt_pk_bf16_f32 " a0 ", " a0 ", " a1 "\n\tv_min_u32 " a0 ", -2, " a0 "\n\tbuffer_store_dword " a0 ", %[so], %[rs], " o " sc1\n\t"
#define TR4(a, b_, c, d, e, f, g_, h, adr, o) "ds_read_b64_tr_b16 v[" #a ":" #b_ "], " adr " offset:" #o "\n\tds_read_b64_tr_b16 v[" #c ":" #d "], " adr " offset:" #o "+512\n\t" \
      "ds_read_b64_tr_b16 v[" #e ":" #f "], " adr " offset:" #o "+8192\n\tds_read_b64_tr_b16 v[" #g_ ":" #h "], " adr " offset:" #o "+8704\n\t"
#define SM(d, a, b_) "v_smfmac_f32_16x16x64_bf16 v[" d "], v[" a "], v[" b_ "], %[sel]\n\t"
#define USE(c, xa, wf) "s_add_u32 s6, s6, 16\n\ts_waitcnt lgkmcnt(" #c ")\n\t" SM("16:19", "20:23", wf) SM("8:11", xa, wf)
#define XR(xa, o) "ds_read_b128 v[" xa "], %[xa] offset:" #o "\n\t"
#define XU(c, xa, wf) "s_waitcnt lgkmcnt(" #c ")\n\t" SM("8:11", xa, wf)
#define ZERO(r) "v_mfma_f32_32x32x16_bf16 v[" r "], v[8:11], v[8:11], 0\n\t"
#define POLL(c, rd) "7:\n\tds_read_b32 v30, %[fl]\n\ts_waitcnt lgkmcnt(0)\n\ts_call_b64 s[10:11], 1f\n\ts_cbranch_scc1 7b\n\t" rd
#define CLR "s_waitcnt vmcnt(0)\n\ts_mov_b64 exec, 1\n\tds_add_rtn_u32 v14, %[fl], v28 offset:96\n\ts_waitcnt lgkmcnt(0)\n\tv_cmpx_eq_u32 vcc, 7, v14\n\t" \
      "buffer_store_dwordx2 v[12:13], off, %[rs], %[co] sc1\n\ts_mov_b64 exec, -1\n\t"
#define XE(k) "s_cmpk_eq_u32 s6, " #k "\n\ts_cbranch_scc1 9f\n\t"
    asm volatile("v_mov_b64 v[8:9], 0\n\tv_mov_b64 v[10:11], 0\n\tv_mov_b64 v[16:17], 0\n\tv_mov_b64 v[18:19], 0\n\t"
                 ZERO("96:111") ZERO("112:127")
                 "v_mov_b32 v20, 0x3f803f80\n\tv_mov_b32 v21, v20\n\tv_mov_b64 v[22:23], v[20:21]\n\tv_mov_b32 v13, %[t]\n\tv_mov_b32 v28, 1\n\tv_mov_b32 v30, 0\n\t"
                 "v_add_u32 v29, 0x10000, %[wa]\n\ts_movk_i32 s6, 96\n\ts_mov_b32 s7, 1\n\ts_mov_b32 s8, 15\n\ts_mov_b32 s9, %[spin]\n\ts_nop 7\n\t"
                 POLL(8, TR4(32, 33, 34, 35, 36, 37, 38, 39, "%[wa]", 0) TR4(40, 41, 42, 43, 44, 45, 46, 47, "%[wa]", 16384))
                 USE(4, "96:99", "32:39") "s_waitcnt lgkmcnt(0)\n\t" SM("16:19", "20:23", "40:47") SM("8:11", "100:103", "40:47")
                 CLR
                 POLL(4, TR4(48, 49, 50, 51, 52, 53, 54, 55, "%[wa]", 32768)) USE(0, "104:107", "48:55")
                 POLL(4, TR4(56, 57, 58, 59, 60, 61, 62, 63, "%[wa]", 49152)) "s_mov_b64 exec, 1\n\tds_add_u32 %[fl], v28 offset:20\n\ts_mov_b64 exec, -1\n\t" TS(60, 61)
                 USE(0, "108:111", "56:63")
                 POLL(4, TR4(64, 65, 66, 67, 68, 69, 70, 71, "v29", 0)) USE(0, "112:115", "64:71") "s_cmp_eq_u32 %[ns], 5\n\ts_cbranch_scc1 5f\n\t"
                 POLL(4, TR4(72, 73, 74, 75, 76, 77, 78, 79, "v29", 16384)) USE(0, "116:119", "72:79") "s_cmp_eq_u32 %[ns], 6\n\ts_cbranch_scc1 5f\n\t"
                 POLL(4, TR4(80, 81, 82, 83, 84, 85, 86, 87, "v29", 32768)) USE(0, "120:123", "80:87")
                 POLL(4, TR4(88, 89, 90, 91, 92, 93, 94, 95, "v29", 49152)) USE(0, "124:127", "88:95")
                 "5:\n\t" TS(62, 63) "s_cmp_eq_u32 s7, 0\n\ts_cbranch_scc1 4f\n\ts_mov_b32 s8, 16\n\t"
                 "s_getpc_b64 s[10:11]\n\tds_read_b32 v30, %[fl]\n\ts_waitcnt lgkmcnt(0)\n"

                 "1:\n\tv_cmp_le_u32 vcc, s6, v30\n\ts_and_b32 s12, vcc_hi, s7\n\ts_cbranch_scc1 3f\n"
                 "2:\n\ts_andn2_b32 s12, s8, vcc_lo\n\ts_cbranch_scc1 6f\n\ts_setpc_b64 s[10:11]\n"
                 "6:\n\ts_sub_u32 s9, s9, 1\n\ts_cbranch_scc1 4f\n\t" GNAP "\n\ts_cmp_eq_u32 s6, s6\n\ts_setpc_b64 s[10:11]\n"
                 "3:\n\t"
                 XR("96:99", 0) XR("100:103", 1024) XR("104:107", 2048) XR("108:111", 3072) XR("112:115", 4096) XR("116:119", 5120) XR("120:123", 6144) XR("124:127", 7168)
                 "ds_read_b128 v[24:27], %[pa]\n\ts_mov_b32 s7, 0\n\t"
                 TS(64, 65)
                 XE(96) XU(8, "96:99", "32:39") XU(7, "100:103", "40:47") XE(112) XU(6, "104:107", "48:55") XE(128) XU(5, "108:111", "56:63") XE(144)
                 XU(4, "112:115", "64:71") XE(160) XU(3, "116:119", "72:79") XE(176) XU(2, "120:123", "80:87") XE(192) XU(1, "124:127", "88:95")
                 "9:\n\ts_cmp_eq_u32 s8, 16\n\ts_waitcnt lgkmcnt(0)\n\ts_cbranch_scc0 2b\n"
                 "4:\n\ts_nop 4\n\tv_permlane32_swap_b32 v16, v17\n\tv_add_f32 v16, v16, v17\n\tv_mul_f32 v25, v25, v16\n\tv_mul_f32 v27, v27, v16\n\t"
                 SWP("v8", "v9", "v10", "v11") "v_fmac_f32 v25, v10, v24\n\tv_fmac_f32 v27, v11, v26\n\tv_fma_f32 v8, v8, v24, v25\n\tv_fma_f32 v9, v9, v26, v27\n\t"
                 OUT("v8", "v9", "%[ho] offen") TS(66, 67)
                 :: [wa] "v"(wa), [sel] "v"(sel), [so] "v"(so), [fl] "v"(lds0 + FC + 4 * min(l, 4)), [xa] "v"(lds0 + FA + 128 * (l & 7) + 16 * ((2 * kg + (n16 >> 3)) ^ (2 * l & 4))),
                    [pa] "v"(lds0 + FC + 32 + 32 * (kg & 1) + 16 * (kg >> 1)), [ns] "s"(ns), [t] "s"(tag), [co] "s"(O_CLR + 8 * id), [rs] "s"(rs), [ho] "s"(O_HG + (6 * cb + 3 * mat + part) * 2048), [spin] "n"(SPIN)
                 : "vcc", "scc", "s6", "s7", "s8", "s9", "s10", "s11", "s12", "s13", "memory", G_CLOBBER TSC);
    TSGET(1, 4, 3, 5);
    if (u < 8) {
      const int tp = 2 * (kg & 1) + (kg >> 1), r7 = 4 * (kg & 1) + (n16 >> 2);
      u32 o3 = (kp & 3) == 1 ? so + 4096 : 0x80000000u, h0 = O_HG + kp * 12288, h1 = h0 + 6144;
      u32 hw = lds0 + FH + 2 * tp * HP + 32 * w + 8 * (l & 2) + (l & 4) * 2 + ((l & 8) >> 1) + 2 * (l & 1);
      u32 hr = lds0 + FH + (l & 7) * HP + 32 * kg + 2 * (l & 8);
      u32 da = lds0 + 512 * (16 * (kg >> 1) + r7) + ((2 * w ^ r7) << 5) + 8 * (l & 3), db = da ^ 32, fl = lds0 + FC + 4 * (l & 3);
      u32 po = 512 * w + 16 * (l & 8) + 32 * tp + 4 * (l & 7);
      u32 e0 = 0x3f80u, e1 = 0x3f800000u;
      asm volatile("" : "+v"(hw), "+v"(hr), "+v"(da), "+v"(db), "+v"(fl), "+v"(po), "+v"(o3));
      asm volatile("" : "+s"(e0), "+s"(e1), "+s"(h0), "+s"(h1));
#define TRD(a, b_, c, d, e, f, g_, h, adr, o) "ds_read_b64_tr_b16 v[" #a ":" #b_ "], " adr " offset:" #o "\n\tds_read_b64_tr_b16 v[" #c ":" #d "], " adr " offset:" #o "+4096\n\t" \
      "ds_read_b64_tr_b16 v[" #e ":" #f "], " adr " offset:" #o "+16384\n\tds_read_b64_tr_b16 v[" #g_ ":" #h "], " adr " offset:" #o "+20480\n\t"
#define HLD(X, r) X(r "0", "%[s1]", "%[h0] offen") X(r "3", "%[s1]", "%[h1] offen") X(r "1", "%[s1]", "%[h0] offen offset:2048") X(r "4", "%[s1]", "%[h1] offen offset:2048") \
      X(r "2", "%[o3]", "%[h0] offen") X(r "5", "%[o3]", "%[h1] offen")
#define HL0(a, o, s_) "buffer_load_dword " a ", " o ", %[rs], " s_ " sc1\n\t"
#define HLF(a, o, s_) "buffer_load_dword " a ", " o ", %[rs], " s_ SCH "\n\t"
#define HL1(a, o, s_) "v_cmpx_eq_u32 vcc, -1, " a "\n\t" HL0(a, o, s_) "s_mov_b64 exec, -1\n\t"
#define DOT(d, e, a) "v_dot2c_f32_bf16 " d ", %[" e "], " a "\n\t"
#define HCK(c, r) "s_cmp_eq_u32 s7, 0\n\ts_cbranch_scc1 5f\n\ts_call_b64 s[10:11], 4f\n5:\n\ts_waitcnt vmcnt(" #c ")\n\tv_max3_u32 v28, " r "0, " r "1, " r "2\n\tv_lshlrev_b32 v24, 16, " r "0\n\tv_max3_u32 v28, v28, " r "3, " r "4\n\t" \
      "v_and_b32 v25, %[hm], " r "0\n\tv_max_u32 v28, v28, " r "5\n\t" DOT("v24", "e0", r "1") "v_cmp_eq_u32 vcc, -1, v28\n\t" DOT("v25", "e1", r "1") DOT("v24", "e0", r "2") DOT("v25", "e1", r "2") \
      "v_lshlrev_b32 v26, 16, " r "3\n\tv_and_b32 v27, %[hm], " r "3\n\t" DOT("v26", "e0", r "4") DOT("v27", "e1", r "4") DOT("v26", "e0", r "5") DOT("v27", "e1", r "5")
#define RB2(a, b_) "v_cvt_pk_bf16_f32 " a ", 0, " a "\n\tv_cvt_pk_bf16_f32 " b_ ", 0, " b_ "\n\t"
      asm volatile("s_mov_b32 s7, 1\n\ts_mov_b32 s12, %[spin]\n\tv_mov_b64 v[8:9], 0\n\tv_mov_b64 v[10:11], 0\n\tv_mov_b64 v[12:13], 0\n\tv_mov_b64 v[14:15], 0\n"
                   "2:\n\ts_sleep 1\n\tds_read_b32 v28, %[fl] offset:16\n\ts_memrealtime s[8:9]\n\ts_waitcnt lgkmcnt(0)\n\tv_cmp_eq_u32 vcc, 0, v28\n\ts_sub_u32 s8, s8, %[st]\n\t"
                   "s_cmp_lt_u32 s8, %[th]\n\ts_cbranch_scc1 2b\n\ts_bitcmp1_b32 vcc_lo, 3\n\ts_cbranch_scc1 2b\n\t" TS(64, 65)
                   HLD(HLF, "v7") HCK(0, "v7") "s_cbranch_vccnz 9f\n"
                   "3:\n\t" TS(66, 67) RB2("v24", "v25") "v_mul_f32 v28, %[le], v24\n\tv_mul_f32 v29, %[le], v25\n\tv_exp_f32 v28, v28\n\tv_exp_f32 v29, v29\n\t" RB2("v26", "v27")
                   "v_add_f32 v28, 1.0, v28\n\tv_add_f32 v29, 1.0, v29\n\tv_rcp_f32 v28, v28\n\tv_rcp_f32 v29, v29\n\ts_cmp_eq_u32 s7, 0\n\tv_mul_f32 v24, v24, v28\n\tv_mul_f32 v25, v25, v29\n\t"
                   RB2("v24", "v25") "v_mul_f32 v24, v24, v26\n\tv_mul_f32 v25, v25, v27\n\tv_cvt_pk_bf16_f32 v24, v24, v25\n\ts_mov_b32 s12, %[spin]\n\t"
                   "ds_write_b16 %[hw], v24\n\tds_write_b16_d16_hi %[hw], v24 offset:%[hp]\n\ts_cbranch_scc1 8f\n"
                   "7:\n\ts_call_b64 s[10:11], 4f\n\ts_mul_i32 s12, s12, s7\n\ts_sub_u32 s12, s12, 1\n\ts_cbranch_scc0 7b\n"
                   "8:\n\ts_waitcnt lgkmcnt(0)\n\ts_barrier\n\t" TS(60, 61)
                   XR("16:19", 0) XR("20:23", 128)
                   XU(1, "16:19", "32:39") SM("12:15", "16:19", "40:47") XU(0, "20:23", "48:55") SM("12:15", "20:23", "56:63") "s_nop 6\n\t"
                   SWP("v8", "v9", "v10", "v11") "v_add_f32 v8, v8, v10\n\tv_add_f32 v9, v9, v11\n\t" SWP("v12", "v13", "v14", "v15") OUT("v8", "v9", "%[po] offen")
                   "v_add_f32 v12, v12, v14\n\tv_add_f32 v13, v13, v15\n\t" OUT("v12", "v13", "%[po] offen offset:256") TS(62, 63) DONE
                   "9:\n\tv_mov_b64 v[80:81], v[70:71]\n\tv_mov_b64 v[82:83], v[72:73]\n\tv_mov_b64 v[84:85], v[74:75]\n\t" HLD(HL1, "v7") "s_sleep " STR(HNAP) "\n"
                   "1:\n\t" HLD(HL1, "v8") HCK(6, "v7") "s_cbranch_vccz 3b\n\t" HLD(HL1, "v7") HCK(6, "v8") "s_sub_u32 s12, s12, 1\n\ts_cbranch_vccz 3b\n\ts_cbranch_scc0 1b\n\ts_branch 3b\n"
                   "4:\n\tds_read_b32 v28, %[fl]\n\ts_waitcnt lgkmcnt(0)\n\tv_cmp_ne_u32 vcc, -1, v28\n\ts_cbranch_vccnz 6f\n\t"
                   TRD(32, 33, 34, 35, 36, 37, 38, 39, "%[da]", 0) TRD(40, 41, 42, 43, 44, 45, 46, 47, "%[db]", 0)
                   TRD(48, 49, 50, 51, 52, 53, 54, 55, "%[da]", 32768) TRD(56, 57, 58, 59, 60, 61, 62, 63, "%[db]", 32768) "s_mov_b32 s7, 0\n"
                   "6:\n\ts_setpc_b64 s[10:11]\n0:"
                   :: [s1] "v"(so), [o3] "v"(o3), [fl] "v"(fl), [da] "v"(da), [db] "v"(db), [hw] "v"(hw), [xa] "v"(hr), [sel] "v"(sel), [so] "v"(po), [rs] "s"(rs), [h0] "s"(h0), [h1] "s"(h1),
                      [st] "s"(st0), [th] "n"(TH), [spin] "n"(SPIN), [hp] "n"(HP), [po] "s"(O_PG + (4 * kp + ct) * 4096), [e0] "s"(e0), [e1] "s"(e1), [hm] "s"(0xffff0000u),
                      [le] "s"(-1.4426950408889634f)
                   : "vcc", "scc", "s7", "s8", "s9", "s10", "s11", "s12", "memory", G_CLOBBER TSC);
      TSGET(8, 9, 6, 7);
    }
    FLUSH();
    __builtin_amdgcn_endpgm();
  }

  if (w0 >= 8) {
    u32x8 xa0, xa1, xa2, xa3, sa1;
    u32x4 junk;
    u32 op, so;
    ROLE(p, "", , "={v[100:107]}"(xa0), "={v[108:115]}"(xa1), "={v[116:123]}"(xa2),
         "={v148}"(op), "={v149}"(so), "={v[160:163]}"(junk));
    const int p = w - 8;
    __builtin_amdgcn_s_setprio(3);
    asm volatile("s_waitcnt lgkmcnt(0)" : "+{s44}"(tag)::"memory");
    tag += 1u;
    STAMP(9);
    f32x2 xq[4][8];
    u32x8 pa;
    {
#define SCK(r1, r3, r5, r7) "v_xor_b32 %[d], %[t], v" #r1 "\n\tv_bitop3_b32 %[d], %[d], %[t], v" #r3 " bitop3:0xf6\n\t" \
      "v_bitop3_b32 %[d], %[d], %[t], v" #r5 " bitop3:0xf6\n\tv_bitop3_b32 %[d], %[d], %[t], v" #r7 " bitop3:0xf6\n\t"
#define SLX(r0, r3, r4, r7, k, k1, sc) "buffer_load_dwordx4 v[" #r0 ":" #r3 "], %[so], %[rs], " k " offen" sc "\n\tbuffer_load_dwordx4 v[" #r4 ":" #r7 "], %[so], %[rs], " k1 " offen" sc "\n\t"
#define SLD(r0, r3, r4, r7, k, k1) SLX(r0, r3, r4, r7, k, k1, " sc1")
#define SLOOP(r0, r1, r3, r4, r5, r7, k, k1) "1:\n\ts_waitcnt vmcnt(0)\n4:\n\t" SCK(r1, r3, r5, r7) "s_sub_u32 %[n], %[n], 1\n\tv_cmp_ne_u32 vcc, 0, %[d]\n\ts_cbranch_scc1 2f\n\ts_cbranch_vccz 2f\n\t" \
      SLD(r0, r3, r4, r7, k, k1) "s_branch 1b\n2:\n\t"
      u32 d_, n_ = SPIN, k1 = 4096, k2 = 8192, k3 = 12288, s0[4];
      asm volatile("s_mov_b32 m0, %[m]\n\ts_cmp_eq_u32 %[k], 3\n\ts_waitcnt vmcnt(2)\n\ts_cbranch_scc0 1f\n\ts_waitcnt vmcnt(0)\n1:"
                   : "+{v[100:107]}"(xa0), "+{v[108:115]}"(xa1), "+{v[160:163]}"(junk) : [m] "s"(lds0 + OX + 2 * XO + 256 * p), [k] "s"(xk) : "scc", "memory");
      SB();
#define UNPACK(i, xs) _Pragma("unroll") for (int c = 0; c < 8; c++) { \
        xq[i][c] = (c & 1) ? f32x2{hi2f(xs[c >> 1]), hi2f(xs[4 + (c >> 1)])} : f32x2{lo2f(xs[c >> 1]), lo2f(xs[4 + (c >> 1)])}; asm volatile("" : "+v"(xq[i][c].x), "+v"(xq[i][c].y)); }
      UNPACK(0, xa0);
      UNPACK(1, xa1);
      u32 fa = lds0 + OXF + 4 * p, fv = 1;
      asm volatile("" : "+v"(fa), "+v"(fv));
      asm volatile("" : "+s"(k1), "+s"(k2), "+s"(k3), "+s"(n_));
      SB();
      STAMP(1);
#define SCP(r0, r1, r2, r3, r4, r5, r6, r7) "s_waitcnt vmcnt(2)\n\t" SCK(r1, r3, r5, r7) "v_cmp_ne_u32 vcc, 0, %[d]\n\tv_mov_b32 %[a0], v" #r0 "\n\tv_mov_b32 %[a1], v" #r2 "\n\t" \
      "v_mov_b32 %[a2], v" #r4 "\n\tv_mov_b32 %[a3], v" #r6 "\n\ts_cbranch_vccz 3f\n\t" SLD(r0, r3, r4, r7, "0", "%[k1]")
      asm volatile(UNTIL(STR(TSG)) SLD(132, 135, 136, 139, "0", "%[k1]") "s_sleep " STR(SSL) "\n\t" SLD(140, 143, 144, 147, "0", "%[k1]") "s_waitcnt vmcnt(4)"
                   : "=&{v[132:139]}"(pa), "=&{v[140:147]}"(sa1), "+{v[116:123]}"(xa2)
                   : [st] "s"(st0), [so] "v"(so), [rs] "s"(rs), [k1] "s"(k1) : "scc", "s8", "s9", "memory");
      UNPACK(2, xa2);
      SB();
      asm volatile("1:\n\t" SCP(132, 133, 134, 135, 136, 137, 138, 139) SCP(140, 141, 142, 143, 144, 145, 146, 147) "s_sub_u32 %[n], %[n], 1\n\ts_cbranch_scc0 1b\n"
                   "3:\n\t"
                   : [d] "=&v"(d_), [n] "+s"(n_), [a0] "=&v"(s0[0]), [a1] "=&v"(s0[1]), [a2] "=&v"(s0[2]), [a3] "=&v"(s0[3]), "+{v[132:139]}"(pa), "+{v[140:147]}"(sa1)
                   : [t] "s"(tag), [so] "v"(so), [rs] "s"(rs), [k1] "s"(k1), [k2] "s"(k2), [k3] "s"(k3) : "vcc", "scc", "memory");
#pragma unroll
      for (int i = 0; i < 5; i++) {
#define XLD(r0, r1, r2, r3, o_) "v_add_u32 %[d], " #o_ ", %[o]\n\tglobal_load_dwordx4 v[" #r0 ":" #r1 "], %[d], %[x]\n\tglobal_load_dwordx4 v[" #r2 ":" #r3 "], %[d], %[x] offset:2048\n"
        if (i == XI) asm volatile("s_cmp_eq_u32 %[k], 3\n\ts_cbranch_scc0 1f\n\t" XLD(116, 119, 120, 123, 0x200000) "1:\n\t" XLD(124, 127, 128, 131, 0x300000)
                                 : [d] "=&v"(d_), "+{v[116:123]}"(xa2), "=&{v[124:131]}"(xa3) : [k] "s"(xk), [o] "v"(op), [x] "s"(x) : "scc", "memory");
        if (i == 2) {
          asm volatile("s_waitcnt vmcnt(2)\n\ts_branch 4f\n" SLOOP(140, 141, 143, 144, 145, 147, "%[k2]", "%[k3]") : [d] "=&v"(d_), [n] "+s"(n_), "+{v[140:147]}"(sa1), "+{v[116:123]}"(xa2), "+{v[132:139]}"(pa)
                       : [t] "s"(tag), [so] "v"(so), [rs] "s"(rs), [k2] "s"(k2), [k3] "s"(k3) : "vcc", "scc", "memory");
          if (xk == 3) { UNPACK(2, xa2); }
        }
        if (i == 3) asm volatile("s_waitcnt vmcnt(%1)" : "+{v[124:131]}"(xa3) : "n"(DPI == 2 ? DPF : 0));
        if (i == DPI && DPF) {
          u32 lx = l;
          asm volatile("" : "+v"(lx));
          const u32 ti = min(16 * xk + 4 * p + (lx >> 4), 125u), tsb = __umul24(ti, 57) >> 10, tu = min(ti - __umul24(18, tsb), 15u);
          const u32 o = (128 * (4 * tsb + (tu >> 2)) + DPO / 2 + 4 * j + ((lx >> 2) & 3)) * (2 * D) + 512 * (tu & 3) + 128 * (lx & 3);
          if (DPI == 4) AWAIT(OXF + 4 * (l & 3), 3, "s_sleep 1");
          if (DPF == 1) asm volatile("global_load_dword v160, %1, %2" : "=&{v[160:163]}"(junk) : "v"(o), "s"(wd) : "memory");
          else asm volatile("global_load_dword v160, %1, %2\n\tglobal_load_dword v161, %1, %3" : "=&{v[160:163]}"(junk) : "v"(o), "s"(wd), "s"(wd + 64 * D) : "memory");
        }
        if (i == 4) break;
        if (i == 3) UNPACK(3, xa3);
        const u32 sa = i < 2 ? s0[2 * i] : sa1[4 * i - 8], sb = i < 2 ? s0[2 * i + 1] : sa1[4 * i - 6];
        if (i == 0) STAMP(3);
        if (i == 2) STAMP(10);
        const f32x2 r2 = {__builtin_amdgcn_rcpf(hi2f(sa)), __builtin_amdgcn_rcpf(hi2f(sb))};
        const f32x2 m2 = {-lo2f(sa) * r2.x, -lo2f(sb) * r2.y};
        f32x2 n2[8];
        u32 o[8];
        SB();
#pragma unroll
        for (int h = 0; h < (i >= HC ? 1 : 2); h++) {
          const int c0 = i >= HC ? 0 : 4 * h, c1 = i >= HC ? 8 : 4 * h + 4;
#pragma unroll
          for (int c = c0; c < c1; c++) n2[c] = f32x2{fmav(xq[i][c].x, r2.x, m2.x), fmav(xq[i][c].y, r2.y, m2.y)};
          SB();
#pragma unroll
          for (int c = c0; c < c1; c++) o[c] = packh(n2[c].x, n2[c].y);
          SB();
#pragma unroll
          for (int c = c0; c < c1; c++) asm volatile("ds_write_addtid_b32 %0 offset:%1" ::"v"(o[c]), "n"(c * XREG + 1024 * i) : "memory");
          asm volatile("ds_write_b32 %1, %0\n\tv_add_u32 %0, 1, %0" : "+v"(fv) : "v"(fa) : "memory");
          if (S2L && i == 0 && h == 0)
            asm volatile(SLD(140, 143, 144, 147, "%[k2]", "%[k3]") : "+{v[140:147]}"(sa1) : [so] "v"(so), [rs] "s"(rs), [k2] "s"(k2), [k3] "s"(k3) : "memory");
          SB();
        }
        if (i == PDROP) __builtin_amdgcn_s_setprio(PPRIO);
        SB();
      }
    }
    STAMP(4);
    const rsrc_t ro = __builtin_amdgcn_make_buffer_rsrc((void*)out, 0, 2 * L * D, 0x00020000);
    u32 ys[8], y[2][8] = {};
    u32x2 ix;
    u32x4 cl;
    u32x8 pb;
    float yc, rsel;
    const u32 ixo = IXO((l & 6) + (n16 >> 3));
    if (p == 0) asm volatile("buffer_load_dwordx2 v[4:5], %1, %2, 0 offen sc1" : "=&{v[4:5]}"(ix) : "v"(ixo), "s"(rs) : "memory");
    const int rc = 4 * (l & 1) + kg, u = 64 * (p & 1) + l;
    u32x4 pick;
    u32 po = O_PG + 128 * g + 16 * (l & 7) + 16384 * (l >> 3), p3 = l < 32 ? po + 3 * 131072 : 0x80000000u, yf = lds0 + OYP + 4 * min(n16, 12) + 64 * (p >> 1);
    u32 ya = lds0 + OY + CHR * rc, oo = 16 * g + 2 * rc, clo = O_CLR + 16 * min(l, 62);
    u32 yp = lds0 + OY + 512 * (p >> 1) + 4 * (u ^ ((u >> 4) & 6)), yq = yp + 4 * CHR, bp = lds0 + OMISC + 16 * p + 4 * kg, bs = 30 - 2 * n16;
#pragma unroll
    for (int q = 0; q < 4; q++) pick[q] = (n16 >> 2) == q ? 0x3f80u << 16 * (l & 1) : 0u;
    asm volatile("" : "+v"(pick), "+v"(ya), "+v"(oo), "+v"(po), "+v"(p3), "+v"(yf), "+v"(clo), "+v"(yp), "+v"(yq), "+v"(bp), "+v"(bs));
#pragma unroll
    for (int c = 0; c < 8; c++) asm volatile("" : "+v"(y[0][c]), "+v"(y[1][c]));
    if (TE) asm volatile(UNTIL(STR(TE)) :: [st] "s"(st0) : "s8", "s9", "scc");
#pragma unroll
    for (int i = 0; i < 4; i++) {
      if (i == IXI && p == 0) {
        u32 n_ = SPIN;
        asm volatile("s_waitcnt vmcnt(%5)\n1:\n\tv_cmp_ne_u32 vcc, %4, v5\n\ts_sub_u32 %1, %1, 1\n\ts_cbranch_vccz 2f\n\ts_cbranch_scc1 2f\n\t"
                     "buffer_load_dwordx2 v[4:5], %2, %3, 0 offen sc1\n\ts_waitcnt vmcnt(0)\n\ts_branch 1b\n2:"
                     : "+{v[4:5]}"(ix), "+s"(n_) : "v"(ixo), "s"(rs), "s"(tag), "n"(2 * IXI + 1) : "vcc", "scc", "memory");
        const u32 prow = ix.x & 2047u, v = prow >> 1;
        if (l < 16) atomicOr((u32*)(smem + OMISC) + (prow >> 5), 1u << (ix.x & 31u));
        FLAG(OYP + 256 + 32 + 64 * (l & 3), 8u);
        oo += prow * (2 * D);
        asm volatile("v_mov_b32 v162, 0\n\tglobal_load_short_d16_hi v162, %1, %2" : "+{v[160:163]}"(junk) : "v"(oo), "s"(x) : "memory");
        yc = *(const float*)(smem + OSC + 4 * rc); rsel = rbt(sigm(hi2f(ix.x)));
        ya += 4 * (v ^ ((v >> 4) & 6)) + 2 * (prow & 1);
        asm volatile("" : "+v"(yc), "+v"(rsel), "+v"(ya));
        STAMP(11);
      }
      if (i == 2 && p == 0) cl = ld16(rs, clo);
      if (i == 3) {
        asm volatile("s_waitcnt vmcnt(2)" ::: "memory");
        FLAG(OYP + 448 + 36 + 4 * p, 8u);
        if (p == 0) {
#pragma unroll 1
          for (int spin = 0; spin < SPIN; spin++) {
            if (__all(cl.y == tag && cl.w == tag)) break;
            cl = ld16(rs, clo);
          }
          STAMP(12);
        }
        __builtin_amdgcn_s_setprio(LPRIO);
      }
      {
        u32 n_ = SPIN, f_;
        asm volatile("1:\n\tds_read_b32 %0, %2 offset:%3\n\ts_sub_u32 %1, %1, 1\n\ts_waitcnt lgkmcnt(0)\n\tv_cmp_eq_u32 vcc, 0, %0\n\ts_cbranch_scc1 2f\n\ts_and_b32 vcc_lo, vcc_lo, %4\n\t"
                     "s_cbranch_scc0 2f\n\ts_sleep %5\n\ts_branch 1b\n2:" : "=&v"(f_), "+s"(n_) : "v"(yf), "n"(128 * i), "n"(i < 2 ? 255 : 511), "n"(i < 3 ? YN0 : YN3) : "vcc", "scc", "memory");
      }
      if (i == 0) STAMP(5); else if (i == 1) STAMP(6); else if (i == 2) STAMP(7); else STAMP(8);
      u32 bm = 0;
      if (i >= 2) asm volatile("ds_read_b32 %0, %1 offset:%2" : "=v"(bm) : "v"(bp), "n"(64 * i) : "memory");
#pragma unroll
      for (int r = 0; r < 2; r++)
#pragma unroll
        for (int c = 0; c < 8; c++) asm volatile("ds_read_u16_d16_hi %0, %1 offset:%2" : "+v"(y[r][c]) : "v"(c < 4 ? yp : yq), "n"((c & 3) * CHR + 1024 * i + 2 * r) : "memory");
      if (i == 3 && p == 0) {
#define PLD(X) X(132, 133, 134, 135, "%[po]", "0") X(136, 137, 138, 139, "%[po]", "%[k1]") X(140, 141, 142, 143, "%[po]", "%[k2]") X(144, 145, 146, 147, "%[p3]", "0")
#define PL0(a, b_, c, d, o, s_) "buffer_load_dwordx4 v[" #a ":" #d "], " o ", %[rs], " s_ " offen sc1\n\t"
        asm volatile(PLD(PL0) : "=&{v[132:139]}"(pa), "=&{v[140:147]}"(pb) : [po] "v"(po), [p3] "v"(p3), [rs] "s"(rs), [k1] "s"(131072), [k2] "s"(262144) : "memory");
      }
      if (i == 0) {
        st8(rs, t == 512 ? O_ARR + b * 8 : 0x80000000u, u32x2{0u, tag});
        const u32x4 a = *(const u32x4*)(smem + OSC), c = *(const u32x4*)(smem + OSC + 16);
#pragma unroll
        for (int k = 0; k < 4; k++) { ys[k] = (u32)__builtin_amdgcn_readfirstlane((int)a[k]); ys[4 + k] = (u32)__builtin_amdgcn_readfirstlane((int)c[k]); }
      }
      SB();
#pragma unroll
      for (int r = 0; r < 2; r++) {
        asm volatile("s_waitcnt lgkmcnt(%9)" : "+v"(y[r][0]), "+v"(y[r][1]), "+v"(y[r][2]), "+v"(y[r][3]), "+v"(y[r][4]), "+v"(y[r][5]), "+v"(y[r][6]), "+v"(y[r][7]), "+v"(bm) : "n"(8 - 8 * r));
        u32x4 v;
#pragma unroll
        for (int k = 0; k < 4; k++)
          v[k] = pack2(fmas(__uint_as_float(y[r][2 * k]), ys[2 * k], xq[i][2 * k][r]), fmas(__uint_as_float(y[r][2 * k + 1]), ys[2 * k + 1], xq[i][2 * k + 1][r]));
        if (i >= 2 && r == 0) bm <<= bs;
        __builtin_amdgcn_raw_buffer_store_b128(v, ro, i < 2 ? op : op | (bm & 0x40000000u << r), (512 * i + r) * (2 * D), OAUX);
        SB();
        asm volatile("s_nop 1");
        SB();
      }
    }
    asm volatile("" ::"{v[160:163]}"(junk));
    STAMP(13);
    if (p == 0) {
      u32 n_ = SPIN;
#define PL1(a, b_, c, d, o, s_) "v_max3_u32 v150, v" #a ", v" #b_ ", v" #c "\n\tv_max_u32 v150, v150, v" #d "\n\tv_cmpx_eq_u32 vcc, -1, v150\n\t" PL0(a, b_, c, d, o, s_) "s_mov_b64 exec, -1\n\t"
#define PLE(X) X(112, 113, 114, 115, "%[po]", "0") X(116, 117, 118, 119, "%[po]", "%[k1]") X(120, 121, 122, 123, "%[po]", "%[k2]") X(124, 125, 126, 127, "%[p3]", "0")
#define PMX(a, b_) "v_max3_u32 v150, v150, v" a ", v" b_ "\n\t"
#define PMM(d, a, c) "v_mfma_f32_16x16x32_bf16 v[" d "], %[pk], v[" a "], " c "\n\t"
#define PCK(c, p, q) "s_waitcnt vmcnt(" #c ")\n\t" PMM("152:155", p "2:" p "5", "0") "v_max_u32 v150, v" p "2, v" p "3\n\t" PMX(p "4", p "5") PMM("156:159", p "6:" p "9", "0") PMX(p "6", p "7") PMX(p "8", p "9") \
      PMM("152:155", q "0:" q "3", "v[152:155]") PMX(q "0", q "1") PMX(q "2", q "3") PMM("156:159", q "4:" q "7", "v[156:159]") PMX(q "4", q "5") PMX(q "6", q "7") \
      "v_cmp_eq_u32 vcc, -1, v150\n\ts_sub_u32 %[n], %[n], 1\n\t"
#define PCP(a, b_, c, d) "v_mov_b64 v[" #a ":" #b_ "], v[" #c ":" #d "]\n\t"
      asm volatile(PCK(2, "13", "14") "s_cbranch_vccnz 9f\n"
                   "2:\n\t" TS(60, 61) "ds_read_b32 v150, %[yf] offset:448\n\tds_read_u16_d16_hi v151, %[ya]\n\ts_mov_b32 %[n], %[spin]\n\ts_nop 1\n\tv_add_f32 v152, v152, v156\n\tv_add_f32 v153, v153, v157\n\ts_nop 0\n\t"
                   "v_add_f32_dpp v152, v152, v152 row_ror:8 row_mask:0xf bank_mask:0x3\n\tv_add_f32_dpp v152, v153, v153 row_ror:8 row_mask:0xf bank_mask:0xc\n\t"
                   "v_cvt_pk_bf16_f32 v152, 0, v152\n\tv_mul_f32 v152, v152, %[rl]\n\tv_cvt_pk_bf16_f32 v152, 0, v152\n\tv_add_f32 v153, v162, v152\n"
                   "3:\n\ts_waitcnt lgkmcnt(0)\n\tv_cmp_eq_u32 vcc, 0, v150\n\tv_and_b32 v152, 0xffff0000, v151\n\ts_sub_u32 %[n], %[n], 1\n\ts_cbranch_vccz 4f\n\ts_cbranch_scc1 4f\n\t"
                   "ds_read_b32 v150, %[yf] offset:448\n\tds_read_u16_d16_hi v151, %[ya]\n\ts_branch 3b\n"
                   "4:\n\tv_fmac_f32 v153, v152, %[yc]\n\tv_cvt_pk_bf16_f32 v153, v153, v153\n\tbuffer_store_short v153, %[oo], %[ro], 0 offen\n\t" DONE
                   "9:\n\t" PCP(112, 113, 132, 133) PCP(114, 115, 134, 135) PCP(116, 117, 136, 137) PCP(118, 119, 138, 139) PCP(120, 121, 140, 141) PCP(122, 123, 142, 143)
                   PCP(124, 125, 144, 145) PCP(126, 127, 146, 147) PLD(PL1) "s_sleep " STR(PNAP) "\n1:\n\t" PLE(PL1) PCK(4, "13", "14") "s_cbranch_vccz 2b\n\t" PLD(PL1) PCK(4, "11", "12")
                   "s_cbranch_vccz 2b\n\ts_cbranch_scc0 1b\n\ts_branch 2b\n0:"
                   : [n] "+s"(n_), "+{v[132:139]}"(pa), "+{v[140:147]}"(pb)
                   : [po] "v"(po), [p3] "v"(p3), [rs] "s"(rs), [k1] "s"(131072), [k2] "s"(262144), [pk] "v"(pick), [yf] "v"(yf), [ya] "v"(ya), "{v[160:163]}"(junk), [rl] "v"(rsel), [yc] "v"(yc),
                     [oo] "v"(oo), [ro] "s"(ro), [spin] "n"(SPIN)
                   : "vcc", "scc", "memory", "v112", "v113", "v114", "v115", "v116", "v117", "v118", "v119", "v120", "v121", "v122", "v123", "v124", "v125", "v126", "v127", "v150", "v151", "v152", "v153", "v154", "v155", "v156", "v157", "v158", "v159" TSD);
      TSGET(14, 2, 2, 2);
      STAMP(15);
    }
    FLUSH();
    __builtin_amdgcn_endpgm();
  }

  u32 pl, pa, pr, pi, ldtv;
  u32x4 tv, fx, junk;
  ROLE(c, "", , "={v[16:19]}"(tv), "={v[20:23]}"(fx), "={v24}"(ldtv), "={v96}"(pl), "={v97}"(pa), "={v68}"(pr), "={v69}"(pi), "={v[160:163]}"(junk));
  char* W = smem + w * CHR;
  __builtin_amdgcn_s_setprio(2);
  STAMP(3);
  const u32x4 zero4 = {0u, 0u, 0u, 0u};
#pragma unroll
  for (int i = 0; i < 4; i++) *(u32x4*)(W + (i < 3 ? TB + 1024 * i : CHR - 1024) + 16 * l) = zero4;
  SB();
  const u32 wb0 = lds0 + w * CHR, xb0 = lds0 + OX + w * XREG;
  asm volatile("s_mov_b32 m0, %0\n\ts_nop 0\n\tds_write_addtid_b32 %1\n\tds_write_addtid_b32 %1 offset:%2" ::"s"(wb0), "v"(0x3c00u), "n"(PB) : "memory");
  u32 ape = wb0 + (n16 == 12 ? 12 : (n16 ^ 3) - 7 * (n16 >> 3)) * RS + 16 * kg, apo = wb0 + (0x765412b0fdec2301ull >> 4 * n16 & 15) * RS + 16 * kg;
  u32x2 kb = {wb0 + ((15 - n16) & 3) * CPE + 2 * ((15 - n16) & 12) + 16 * kg, wb0 + (((n16 >> 2) & 1) + (n16 & 8) + 2 * (3 - (n16 & 3))) * RS + 16 * kg};
  u32x8 ka = {xb0 + 32 * n16 + 16 * kg, wb0 + OY + 32 * n16 + 8 * (kg ^ ((l >> 2) & 3)),
              lds0 + OXF + 4 * (l & 3), wb0 + TB + ((15 - n16) & 3) * CPO + 2 * ((15 - n16) & 12) + 8 * kg, lds0 + OB + 16 * l,
              l < 32 ? 0x01000100u : 0x03020100u, l < 32 ? 0x03020100u : 0x03020302u, (u32)(l >> 2) & 8u};
  u32 fad = lds0 + OYP + 4 * w, za = wb0 + 16 * kg;
  asm volatile("" : "+{v70}"(ape), "+{v71}"(apo), "+{v[28:35]}"(ka), "+{v[26:27]}"(kb), "+{v25}"(fad), "+{v12}"(za));
  STAMP(4);
  asm volatile("s_waitcnt vmcnt(4)" : "+{v24}"(ldtv), "+{v[160:163]}"(junk));
  float dt = rbt(fexp(hi2f(ldtv)));
  asm volatile("" : "+v"(dt));
  const f32x2 cdt = {rdl(dt * -1.4426950408889634f, 0), rdl(dt * 0.15915494309189535f, 0)};
  const u32 dk = (u32)__builtin_amdgcn_readfirstlane((int)junk.x) << 16, db = max(__float_as_uint(rdl(dt, 0)), 27u << 23);
  u32 em = max(dk >> 23 & 255u, 127u) + 119 - (db >> 23);
  asm("s_max_i32 %0, %0, 8\n\ts_min_u32 %0, %0, 0xfe" : "+s"(em) :: "scc");
  SB();
  STAMP(10);
  STAMPC(11);
  {
    u32 e, o0, o1, of, os;
    asm volatile(GEN_ASM
                 : [e] "=&s"(e), "=&{v93}"(o0), "=&{v46}"(o1), "=&{v9}"(of), "=&{v8}"(os), "+{v25}"(fad), "+{v12}"(za), "+{v68}"(pr), "+{v69}"(pi), "+{v70}"(ape),
                   "+{v71}"(apo), "+{v96}"(pl), "+{v97}"(pa), "+{v[28:35]}"(ka), "+{v[26:27]}"(kb), "+{v[16:19]}"(tv), "+{v[20:23]}"(fx)
                 : [w] "s"(wb0), [c] "s"(cdt), [k] "s"(f32x2{1.4426950408889634f, 128.f}), [em] "s"(em)
                 : "memory", "scc", "s6", "s7", GEN_CLOBBER);
    STAMP(1);
    STAMPC(12);
    const float ys = __uint_as_float((e - 5) << 23) * __uint_as_float(db);
    asm volatile("v_rcp_f32 %1, %2\n\ts_nop 0\n\tv_fma_f32 %1, %3, %1, %1\n\tv_fma_mixhi_f16 %0, %1, 1.0, %0 op_sel:[0,0,1] op_sel_hi:[0,0,1]\n\ts_nop 0"
                 : "+v"(o0), "=&v"(dt) : "v"(ys), "s"(dk));
    const u32 k0 = (u32)__builtin_amdgcn_readlane((int)o0, 3), k256 = (u32)__builtin_amdgcn_readlane((int)o1, 60);
    const u32 k512 = (u32)__builtin_amdgcn_readlane((int)os, 28), k1024 = (u32)__builtin_amdgcn_readlane((int)of, 28) >> 16;
    *(LDS u16*)(size_t)(wb0 + tv.z) = (u16)__builtin_amdgcn_perm((k512 & 0xffffu) | (k1024 << 16), (k0 >> 16) | (k256 << 16), tv.w);
    *(float*)(smem + OSC + 4 * w) = ys;
    fx &= (k1024 | (k1024 << 16)) ^ 0x80008000u;
  }
  asm volatile(CONV_ASM : "+{v[28:35]}"(ka) : "{v[26:27]}"(kb), "{v25}"(fad), "{v[48:51]}"(fx), [w] "s"(w), [n0] "s"(1 + (w >> 2)), [n1] "s"(3 + (w >> 2)), [n2] "s"(5 + (w >> 2)), [n3] "s"(7 + (w >> 2)) : "vcc", "scc", "s6", "memory", CONV_CLOBBER TSC);
  TSGET(2, 5, 6, 7);
  STAMP(13);
  STAMPC(15);
  FLUSH();
  __builtin_amdgcn_endpgm();
}

extern "C" void kernel_launch(void* const* d_in, const int* in_sizes, int n_in, void* d_out, int out_size,
                              void* d_ws, size_t ws_size, hipStream_t stream) {
  const u16* const* in = (const u16* const*)d_in;
  fused<<<256, 768, 0, stream>>>(in[0], in[1], in[2], in[3], in[4], in[5], in[6], in[8], in[9], in[10], in[11],
                                 (u16*)d_out, (char*)d_ws);
}
```
